```python
import torch

def _softmax(x, axis=-1):
    return torch.softmax(x, dim=axis)
def _gelu(x):
    return torch.nn.functional.gelu(x, approximate="tanh")
def _silu(x):
    return torch.nn.functional.silu(x)
def _sigmoid(x):
    return torch.sigmoid(x)
def _layernorm(x, axis=-1, eps=1e-5):
    m = x.mean(dim=axis, keepdim=True)
    v = x.var(dim=axis, keepdim=True, unbiased=False)
    return (x - m) / torch.sqrt(v + eps)
def _scatter_add(tgt, idx, src, axis=0):
    return tgt.clone().index_add_(axis, idx, src)
def _segment_sum(src, seg, n):
    out = torch.zeros((n,) + src.shape[1:], dtype=src.dtype)
    return out.index_add_(0, seg, src)
def _pad(x, pw):
    flat = tuple(v for pair in reversed(pw) for v in pair)
    return torch.nn.functional.pad(x, flat)

kernel_name = 'standalone__edge_featured_mpnn_gatedgcn__graph'

def setup_inputs():

    _w = (lambda a: (lambda t: t.to(torch.bfloat16) if t.is_floating_point() else t)(torch.as_tensor(a)))
    N, deg, D, De = 50000, 16, 64, max(16, 1)
    E = N * deg
    return {
        "x": _w((lambda *s: torch.randn(*s, dtype=torch.bfloat16) * 0.4)(N, D)),
        "e_attr": _w((lambda *s: torch.randn(*s, dtype=torch.bfloat16) * 0.8)(E, De)),
        "src": _w((lambda lo, hi, *s: torch.randint(lo, hi, s))(0, N, E).to(torch.int64)),
        "dst": _w((lambda lo, hi, *s: torch.randint(lo, hi, s))(0, N, E).to(torch.int64)),
        "w_a": _w((lambda *s: torch.randn(*s, dtype=torch.bfloat16) * 0.8)(D, D)),
        "w_b": _w((lambda *s: torch.randn(*s, dtype=torch.bfloat16) * 0.4)(D, D)),
        "w_c": _w((lambda *s: torch.randn(*s, dtype=torch.bfloat16) * 0.8)(De, D)),
        "w_u": _w((lambda *s: torch.randn(*s, dtype=torch.bfloat16) * 0.05)(D, D)),
        "w_v": _w((lambda *s: torch.randn(*s, dtype=torch.bfloat16) * 0.05)(D, D)),
    }

def reference(x, e_attr, src, dst, w_a, w_b, w_c, w_u, w_v):
    N, D = x.shape
    hv = x @ w_v
    g = x[dst] @ w_a + x[src] @ w_b + e_attr @ w_c
    eta = _sigmoid(g)
    denom = _segment_sum(eta, dst, N) + 1e-6
    eta_n = eta / denom[dst]
    agg = _segment_sum(eta_n * hv[src], dst, N)
    z = x @ w_u + agg
    relu_z = torch.where(z > 0, z, 0 * z)
    return x + relu_z
```

```cpp
#include <hip/hip_runtime.h>
#include <hip/hip_bf16.h>
#include <stdint.h>

using bf16x8 = __attribute__((ext_vector_type(8))) short;
using s16x4 = __attribute__((ext_vector_type(4))) short;
using f32x16 = __attribute__((ext_vector_type(16))) float;
using u32x4 = __attribute__((ext_vector_type(4))) unsigned;
using u32x2 = __attribute__((ext_vector_type(2))) unsigned;
using i32x4 = __attribute__((ext_vector_type(4))) int;
using u16x2 = __attribute__((ext_vector_type(2))) unsigned short;

#define MFMA32(a, b, c) __builtin_amdgcn_mfma_f32_32x32x16_bf16((a), (b), (c), 0, 0, 0)
#define LAS __attribute__((address_space(3)))
#define SB() __builtin_amdgcn_sched_barrier(0)

constexpr int NCH = 256;
constexpr int NBK = 256;
constexpr int NBIN = NBK * 8;
constexpr int NFRAG = 34;
constexpr int CHMAX = 3200;
constexpr float NLOG2E = -1.44269504088896340736f;
constexpr unsigned TPITCH = 80;

__device__ __forceinline__ unsigned pack_bf16(float lo, float hi) {
  typedef __attribute__((ext_vector_type(2))) float f2;
  typedef __attribute__((ext_vector_type(2))) __bf16 b2;
  f2 v = {lo, hi};
  b2 r = __builtin_convertvector(v, b2);
  return __builtin_bit_cast(unsigned, r);
}

__device__ __forceinline__ int wave_scan(int v) {
  v += __builtin_amdgcn_update_dpp(0, v, 0x111, 0xf, 0xf, false);
  v += __builtin_amdgcn_update_dpp(0, v, 0x112, 0xf, 0xf, false);
  v += __builtin_amdgcn_update_dpp(0, v, 0x114, 0xf, 0xf, false);
  v += __builtin_amdgcn_update_dpp(0, v, 0x118, 0xf, 0xf, false);
  v += __builtin_amdgcn_update_dpp(0, v, 0x142, 0xa, 0xf, false);
  v += __builtin_amdgcn_update_dpp(0, v, 0x143, 0xc, 0xf, false);
  return v;
}

__device__ __forceinline__ void glds16(const void* sbase, unsigned voff, unsigned lds_dst) {
  asm volatile(
      "s_mov_b32 m0, %2\n\t"
      "s_nop 0\n\t"
      "global_load_lds_dwordx4 %0, %1"
      :: "v"(voff), "s"(sbase), "s"(lds_dst) : "memory", "m0");
}
__device__ __forceinline__ void glds16nt(const void* sbase, unsigned voff, unsigned lds_dst) {
  asm volatile(
      "s_mov_b32 m0, %2\n\t"
      "s_nop 0\n\t"
      "global_load_lds_dwordx4 %0, %1 nt"
      :: "v"(voff), "s"(sbase), "s"(lds_dst) : "memory", "m0");
}
__device__ __forceinline__ void glds16x(const void* sbase, unsigned a, unsigned c, unsigned lds_dst) {
  unsigned tmp;
  asm volatile(
      "s_mov_b32 m0, %4\n\t"
      "v_mad_u32_u16 %0, %1, %5, %2\n\t"
      "global_load_lds_dwordx4 %0, %3"
      : "=&v"(tmp) : "v"(a), "v"(c), "s"(sbase), "s"(lds_dst), "s"(128u) : "memory", "m0");
}
__device__ __forceinline__ void glds16e(const void* sbase, unsigned a, unsigned c, unsigned lds_dst) {
  unsigned tmp;
  asm volatile(
      "s_mov_b32 m0, %4\n\t"
      "v_add_u32 %0, %1, %2\n\t"
      "global_load_lds_dwordx4 %0, %3"
      : "=&v"(tmp) : "v"(a), "v"(c), "s"(sbase), "s"(lds_dst) : "memory", "m0");
}
__device__ __forceinline__ int gload32(const void* sbase, unsigned voff) {
  int r;
  asm volatile("global_load_dword %0, %1, %2" : "=v"(r) : "v"(voff), "s"(sbase) : "memory");
  return r;
}
__device__ __forceinline__ u32x4 gload128(const void* sbase, unsigned voff) {
  u32x4 r;
  asm volatile("global_load_dwordx4 %0, %1, %2" : "=v"(r) : "v"(voff), "s"(sbase) : "memory");
  return r;
}

constexpr int KP_E = 0;
constexpr int KP_ENT = CHMAX * 32;
constexpr int KP_HIST = KP_ENT + NBK * 128;
constexpr int KP_INV = KP_HIST + NBIN * 4;
constexpr int KP_WS = KP_INV + CHMAX * 2;
constexpr int KP_LDS = KP_WS + 64;
static_assert(KP_LDS <= 160 * 1024, "LDS");

__global__ __launch_bounds__(1024) void k_part(const int* __restrict__ src, const int* __restrict__ dst,
                                               const u32x4* __restrict__ eattr, int E, int chunk,
                                               u32x4* __restrict__ ENT, u32x4* __restrict__ ES,
                                               unsigned* __restrict__ TS,
                                               const unsigned short* __restrict__ wa,
                                               const unsigned short* __restrict__ wb,
                                               const unsigned short* __restrict__ wc,
                                               const unsigned short* __restrict__ wu,
                                               const unsigned short* __restrict__ wv,
                                               unsigned short* __restrict__ wpack) {
  const int tid = threadIdx.x;
  if (blockIdx.x < NCH) {
    __shared__ __attribute__((aligned(16))) char sm[KP_LDS];
    LAS unsigned* hist = (LAS unsigned*)(sm + KP_HIST);
    LAS unsigned short* inv = (LAS unsigned short*)(sm + KP_INV);
    LAS int* wsum = (LAS int*)(sm + KP_WS);
    const unsigned lds0 = (unsigned)(size_t)(LAS char*)sm;
    const int lane = tid & 63;
    const int w = __builtin_amdgcn_readfirstlane(tid >> 6);
    const int c = blockIdx.x;
    const int base = c * chunk;
    const int cnt = max(min(chunk, E - base), 0);
    const int last = max(min(base + cnt, E) - 1, 0);
    int d[4], sv[4];
#pragma unroll
    for (int k = 0; k < 4; ++k) d[k] = gload32(dst, (unsigned)min(base + tid + 1024 * k, last) * 4u);
#pragma unroll
    for (int k = 0; k < 4; ++k) sv[k] = gload32(src, (unsigned)min(base + tid + 1024 * k, last) * 4u);
    auto eload = [&](int k) {
      glds16nt(eattr, (unsigned)min(2 * base + tid + 1024 * k, 2 * last + 1) * 16u,
               lds0 + KP_E + (w * 64 + 1024 * k) * 16);
    };
    eload(0);
    eload(1);
    *(LAS u32x2*)(hist + tid * 2) = u32x2{0u, 0u};
    asm volatile("s_waitcnt lgkmcnt(0)\n\ts_barrier\n\ts_waitcnt vmcnt(6)"
                 : "+v"(d[0]), "+v"(d[1]), "+v"(d[2]), "+v"(d[3]) :: "memory");
    int bin[4], rk[4], slot[4];
#pragma unroll
    for (int k = 0; k < 4; ++k) {
      const int g2 = d[k] / 49;
      const int rem = d[k] - 49 * g2;
      const int hi = rem >= 25;
      bin[k] = (g2 >> 2) * 8 + (g2 & 3) + 4 * hi;
      slot[k] = rem - 25 * hi;
      rk[k] = 0;
      if (tid + 1024 * k < cnt) rk[k] = __hip_atomic_fetch_add(hist + bin[k], 1u, __ATOMIC_RELAXED, __HIP_MEMORY_SCOPE_WORKGROUP);
    }
    eload(2);
    eload(3);
    asm volatile("s_waitcnt lgkmcnt(0)\n\ts_barrier" ::: "memory");
    const u32x2 tt = *(const LAS u32x2*)(hist + tid * 2);
    const int t0 = tt[0], t1 = tt[1];
    const int s = t0 + t1;
    const int inc = wave_scan(s);
    if (lane == 63) wsum[w] = inc;
    eload(4);
    asm volatile("s_waitcnt lgkmcnt(0)\n\ts_barrier" ::: "memory");
    int wbs = 0;
    {
      const i32x4 a = *(const LAS i32x4*)(wsum), b = *(const LAS i32x4*)(wsum + 4);
      const i32x4 e = *(const LAS i32x4*)(wsum + 8), f = *(const LAS i32x4*)(wsum + 12);
#pragma unroll
      for (int i = 0; i < 4; ++i) {
        wbs += i < w ? a[i] : 0;
        wbs += i + 4 < w ? b[i] : 0;
        wbs += i + 8 < w ? e[i] : 0;
        wbs += i + 12 < w ? f[i] : 0;
      }
    }
    const int run = inc - s + wbs;
    *(LAS u32x2*)(hist + tid * 2) =
        u32x2{(unsigned)run | ((unsigned)t0 << 16), (unsigned)(run + t0) | ((unsigned)t1 << 16)};
    {
      LAS char* cell = (LAS char*)sm + KP_ENT + (tid >> 2) * 128;
      const int w0 = (tid & 3) * 2;
      const unsigned f0 = (unsigned)t0 | (((unsigned)run | ((unsigned)min(t0, 6) << 12)) << 16);
      const unsigned f1 = (unsigned)t1 | (((unsigned)(run + t0) | ((unsigned)min(t1, 6) << 12)) << 16);
      *(LAS u32x2*)(cell + ((w0 ^ (c & 7)) * 16) + 8) = u32x2{f0, 0u};
      *(LAS u32x2*)(cell + (((w0 + 1) ^ (c & 7)) * 16) + 8) = u32x2{f1, 0u};
    }
    eload(5);
    asm volatile("s_waitcnt vmcnt(6) lgkmcnt(0)\n\ts_barrier"
                 : "+v"(sv[0]), "+v"(sv[1]), "+v"(sv[2]), "+v"(sv[3]) :: "memory");
    unsigned hp[4];
#pragma unroll
    for (int k = 0; k < 4; ++k) hp[k] = hist[bin[k]];
#pragma unroll
    for (int k = 0; k < 4; ++k) {
      if (tid + 1024 * k < cnt) {
        const int p = (hp[k] & 0xffffu) + rk[k];
        const int cn = hp[k] >> 16;
        inv[p] = (unsigned short)(tid + 1024 * k);
        LAS char* piece = (LAS char*)sm + KP_ENT + (bin[k] >> 3) * 128 + (((bin[k] ^ c) & 7) * 16);
        if (rk[k] < 4 || cn == 5) {
          *(LAS unsigned short*)(piece + rk[k] * 2) = (unsigned short)sv[k];
          __hip_atomic_fetch_or((LAS unsigned*)(piece + 12), (unsigned)slot[k] << (5 * rk[k]), __ATOMIC_RELAXED,
                                __HIP_MEMORY_SCOPE_WORKGROUP);
        } else {
          TS[(size_t)base + p] = (unsigned)sv[k] | ((unsigned)slot[k] * (TPITCH << 16));
        }
      }
    }
    if (w < 4) eload(6);
    asm volatile("s_waitcnt lgkmcnt(0)\n\ts_barrier" ::: "memory");
#pragma unroll
    for (int j = 0; j < 2; ++j) {
      const int i = tid + 1024 * j;
      ENT[((size_t)(i >> 3) * NCH + c) * 8 + (i & 7)] = *(const LAS u32x4*)(sm + KP_ENT + i * 16);
    }
    int iv[7];
#pragma unroll
    for (int k = 0; k < 7; ++k) iv[k] = inv[min((tid + 1024 * k) >> 1, CHMAX - 1)];
    asm volatile("s_waitcnt vmcnt(2)\n\ts_barrier" ::: "memory");
    u32x4* eo = ES + (size_t)base * 2;
#pragma unroll
    for (int k = 0; k < 7; ++k) {
      const int q = tid + 1024 * k;
      if (q < 2 * cnt) {
        const u32x4 v = *(const LAS u32x4*)(sm + KP_E + iv[k] * 32 + (q & 1) * 16);
        __builtin_nontemporal_store(v, eo + q);
      }
    }
  } else if (tid < 512) {
    const int f = blockIdx.x - NCH;
    const int lane = tid >> 3, j = tid & 7;
    const int n = lane & 31, h = lane >> 5;
    const int ct = f & 1;
    const int col = 32 * ((n >> 2) & 1) + 16 * ct + 4 * (n >> 3) + (n & 3);
    unsigned short v;
    if (f == 16 || f == 17) {
      v = wc[(8 * h + j) * 64 + col];
    } else {
      const int g = f < 16 ? f : f - 18;
      const int t = (g >> 1) & 3;
      const unsigned short* W = f < 8 ? wb : f < 16 ? wv : f < 26 ? wu : wa;
      v = W[(32 * h + 8 * t + j) * 64 + col];
    }
    if (f < 8 || f == 16 || f == 17)
      v = (unsigned short)(pack_bf16(__uint_as_float((unsigned)v << 16) * NLOG2E, 0.f) & 0xffffu);
    wpack[f * 512 + tid] = v;
  }
}

constexpr int RING_SLOT = 5 * 1024;
constexpr int LCAP = 512;
constexpr int LARR = 2560;
constexpr int LIST_BYTES = 2 * LARR;
constexpr int NWL = 26;
constexpr int OFF_W = 0;
constexpr int OFF_T1 = NWL * 1024;
constexpr int OFF_T2 = OFF_T1 + 32 * TPITCH;
constexpr int OFF_R0 = OFF_T2 + 32 * TPITCH;
constexpr int OFF_R1 = OFF_R0 + 8 * RING_SLOT;
constexpr int OFF_L = OFF_R1 + 8 * RING_SLOT;
constexpr int MAIN_LDS = OFF_L + 8 * LIST_BYTES;
static_assert(MAIN_LDS <= 160 * 1024, "LDS");

__device__ __forceinline__ float add1(float t) { return 1.0f + t; }
__device__ __forceinline__ float mulf(int i, float a, float b) { return (i & 1) ? __builtin_fmaf(a, b, 0.0f) : a * b; }

__global__ __launch_bounds__(512, 2) void k_main(const unsigned short* __restrict__ x,
                                                 const u32x4* __restrict__ ENT,
                                                 const u32x4* __restrict__ ES,
                                                 const unsigned* __restrict__ TS, int chunk,
                                                 const bf16x8* __restrict__ wpack,
                                                 unsigned short* __restrict__ out, int N
                                                 ) {
  __shared__ __attribute__((aligned(16))) char smem[MAIN_LDS];
  const int tid = threadIdx.x, lane = tid & 63;
  const int wave = __builtin_amdgcn_readfirstlane(tid >> 6);
  const int r = lane & 31, h = lane >> 5;
  const int bk = blockIdx.x;
  const int nb0 = bk * 196 + 49 * (wave & 3) + 25 * (wave >> 2);
  const int nodes = max(min(wave < 4 ? 25 : 24, N - nb0), 0);
  const int n0 = min(nb0, N - 1);
  const unsigned lds0 = (unsigned)(size_t)(LAS char*)smem;

  u32x4 xg[4], wa[8];
  {
    const unsigned vo = lane * 16;
    const char* eb = reinterpret_cast<const char*>(ENT) + (size_t)bk * (NCH * 128) + wave * 4096;
#pragma unroll
    for (int i = 0; i < 4; ++i) glds16(eb + i * 1024, vo, lds0 + OFF_R1 + wave * 4096 + i * 1024);
    const char* wp = reinterpret_cast<const char*>(wpack);
#pragma unroll
    for (int i = 0; i < 4; ++i) {
      const int f = min(wave + 8 * i, NWL - 1);
      glds16(wp + f * 1024, vo, lds0 + OFF_W + f * 1024);
    }
    const unsigned xo = (unsigned)(n0 + max(min(r, nodes - 1), 0)) * 128u + 64u * h;
#pragma unroll
    for (int t = 0; t < 4; ++t) xg[t] = gload128(x, xo + 16 * t);
#pragma unroll
    for (int k = 0; k < 8; ++k) wa[k] = gload128(wp + (NWL + k) * 1024, vo);
  }
  {
    const int sl = tid >> 4, dw = tid & 15;
    const unsigned v1 = (sl == 2 * dw ? 0x3F80u : 0u) | (sl == 2 * dw + 1 ? 0x3F800000u : 0u);
    *(LAS unsigned*)(smem + OFF_T1 + sl * TPITCH + dw * 4) = v1;
    const int hh = dw >> 3, ks = (dw >> 2) & 1, jp = dw & 3;
    const int nd0 = 16 * ks + 8 * (jp >> 1) + 4 * hh + 2 * (jp & 1);
    const unsigned v2 = (sl == nd0 ? 0x3F80u : 0u) | (sl == nd0 + 1 ? 0x3F800000u : 0u);
    *(LAS unsigned*)(smem + OFF_T2 + sl * TPITCH + dw * 4) = v2;
  }
  asm volatile("s_waitcnt vmcnt(12) lgkmcnt(0)\n\ts_barrier" ::: "memory");
  u32x4 cl[4];
#pragma unroll
  for (int j = 0; j < 4; ++j)
    cl[j] = *(const LAS u32x4*)(smem + OFF_R1 + (lane + 64 * j) * 128 + ((wave ^ (lane & 7)) * 16));
  asm volatile("s_waitcnt lgkmcnt(0)\n\ts_barrier" : "+v"(cl[0]), "+v"(cl[1]), "+v"(cl[2]), "+v"(cl[3]) :: "memory");

  LAS char* lbase = (LAS char*)smem + OFF_L + wave * LIST_BYTES;
  const unsigned la = (unsigned)(size_t)lbase;
  auto put = [&](int p, unsigned a, unsigned b) {
    *(LAS unsigned*)(lbase + p * 4) = a;
    *(LAS unsigned*)(lbase + LARR + p * 4) = b;
  };
  const unsigned aLX = la + (lane >> 3) * 16;
  const unsigned aLE = la + LARR + ((((lane >> 1) & 7) << 2) | (lane >> 4)) * 4;
  const unsigned aLS4 = la + (4 * h + ((lane >> 2) & 3)) * 16;
  const unsigned aLS1 = la + (((r & 7) << 2) | (r >> 3)) * 4 + 2;
  const unsigned cx0 = (unsigned)(((lane & 7) ^ (lane >> 4)) * 16);
  const unsigned cx1 = cx0 ^ 64u;
  const unsigned ce = (unsigned)((((lane & 1) ^ ((lane >> 4) & 1))) * 16);
  const unsigned ring0 = lds0 + OFF_R0 + wave * RING_SLOT;
  const unsigned ringd = 8 * RING_SLOT;
  auto ld128 = [&](unsigned a) { return *(const LAS u32x4*)(size_t)a; };
  auto issue_gather = [&](int tile, int rs, unsigned mask) {
    const u32x4 sx = ld128(aLX + tile * 128) & mask;
    const unsigned re = *(const LAS unsigned*)(size_t)(aLE + tile * 128) & mask;
    const unsigned dstb = ring0 + rs * ringd;
    glds16x(x, sx[0], cx0, dstb);
    glds16x(x, sx[1], cx1, dstb + 1024);
    glds16x(x, sx[2], cx0, dstb + 2048);
    glds16x(x, sx[3], cx1, dstb + 3072);
    glds16e(ES, re, ce, dstb + 4096);
  };
  auto pad_list = [&](int cnt) {
#pragma unroll
    for (int k = 0; k < 2; ++k) {
      const int p = cnt + lane + 64 * k;
      const int row = ((p >> 2) & 7) + 8 * (p & 3);
      put(p, (unsigned)(n0 + max(min(row, nodes - 1), 0)) | (31u * TPITCH << 16), 0u);
    }
  };
  const int gbin = bk * 8 + wave;
  auto fill_slow = [&](int wb0) {
    int nn[4], tot = 0;
    u32x4 ce4[4];
#pragma unroll
    for (int j = 0; j < 4; ++j) {
      ce4[j] = ENT[((size_t)bk * NCH + lane + 64 * j) * 8 + (wave ^ (lane & 7))];
      const int code = ce4[j][2] >> 28;
      nn[j] = code == 6 ? (int)(ce4[j][2] & 0xffffu) : code;
      tot += nn[j];
    }
    int pp = wave_scan(tot) - tot - wb0;
#pragma unroll
    for (int j = 0; j < 4; ++j) {
      const int rec0 = (lane + 64 * j) * chunk + (int)((ce4[j][2] >> 16) & 0xfffu);
      const int inl = nn[j] > 5 ? 4 : 5;
      const int te = min(nn[j], LCAP - pp);
#pragma unroll 1
      for (int t = max(0, -pp); t < te; ++t) {
        unsigned a;
        if (t < inl) {
          const unsigned wd = t < 2 ? ce4[j][0] : t < 4 ? ce4[j][1] : ce4[j][2];
          a = ((t & 1) ? wd >> 16 : wd & 0xffffu) | (((ce4[j][3] >> (5 * t)) & 31u) * (TPITCH << 16));
        } else {
          a = TS[rec0 + t];
        }
        put(pp + t, a, (unsigned)(rec0 + t) * 32u);
      }
      pp += nn[j];
    }
  };

  int ni[4], rec0[4], ex[4], orc[4];
  int ox = 0;
#pragma unroll
  for (int j = 0; j < 4; ++j) {
    const int code = cl[j][2] >> 28;
    rec0[j] = (lane + 64 * j) * chunk + (int)((cl[j][2] >> 16) & 0xfffu);
    const bool ov = code == 6;
    ni[j] = ov ? 4 : code;
    ex[j] = ov ? (int)(cl[j][2] & 0xffffu) - 4 : 0;
    orc[j] = rec0[j] + 4;
    ox += ex[j];
  }
  int tv[4], sr[4], sn[4];
  auto ov_issue = [&]() {
    int o_n = 0, o_rec = 0, o_j = 0;
#pragma unroll
    for (int j = 3; j >= 0; --j) {
      const bool on = ex[j] > 0;
      o_n = on ? min(ex[j], 64) : o_n;
      o_rec = on ? orc[j] : o_rec;
      o_j = on ? j : o_j;
    }
    unsigned long long om = __ballot(o_n > 0);
    const int rank = __builtin_amdgcn_mbcnt_hi((unsigned)(om >> 32), __builtin_amdgcn_mbcnt_lo((unsigned)om, 0u));
#pragma unroll
    for (int k = 0; k < 4; ++k) {
      const int l = om ? __builtin_ctzll(om) : 0;
      sr[k] = __builtin_amdgcn_readlane(o_rec, l);
      sn[k] = om ? __builtin_amdgcn_readlane(o_n, l) : 0;
      om &= om - 1;
      tv[k] = gload32(TS, (unsigned)(sr[k] + max(min(lane, sn[k] - 1), 0)) * 4u);
    }
    const int tk = rank < 4 ? o_n : 0;
#pragma unroll
    for (int j = 0; j < 4; ++j) {
      ex[j] -= o_j == j ? tk : 0;
      orc[j] += o_j == j ? tk : 0;
    }
  };
  ov_issue();
  const int s01 = wave_scan(ni[0] | (ni[1] << 16));
  const int s23 = wave_scan(ni[2] | (ni[3] << 16));
  const int sox = wave_scan(ox);
  const int e01 = __builtin_amdgcn_readlane(s01, 63), e23 = __builtin_amdgcn_readlane(s23, 63);
  const int tot0 = e01 & 0xffff, tot1 = tot0 + (e01 >> 16), tot2 = tot1 + (e23 & 0xffff), tot3 = tot2 + (e23 >> 16);
  const int oxt = __builtin_amdgcn_readlane(sox, 63);
  const int cnt_all = tot3 + oxt;
  const bool fast = cnt_all <= LCAP;
  int cnt = min(cnt_all, LCAP);
  pad_list(cnt);
  auto fill = [&](int j, int p) {
    LAS unsigned* pa = (LAS unsigned*)(lbase + p * 4);
    const unsigned b0 = (unsigned)rec0[j] * 32u;
    const unsigned K = TPITCH << 16;
    if (ni[j] > 0) {
      pa[0] = (cl[j][0] & 0xffffu) + (cl[j][3] & 31u) * K;
      pa[LARR / 4] = b0;
      if (ni[j] > 1) {
        pa[1] = (cl[j][0] >> 16) + ((cl[j][3] >> 5) & 31u) * K;
        pa[LARR / 4 + 1] = b0 + 32;
        if (ni[j] > 2) {
          pa[2] = (cl[j][1] & 0xffffu) + ((cl[j][3] >> 10) & 31u) * K;
          pa[LARR / 4 + 2] = b0 + 64;
          if (ni[j] > 3) {
            pa[3] = (cl[j][1] >> 16) + ((cl[j][3] >> 15) & 31u) * K;
            pa[LARR / 4 + 3] = b0 + 96;
            if (ni[j] > 4) {
              pa[4] = (cl[j][2] & 0xffffu) + ((cl[j][3] >> 20) & 31u) * K;
              pa[LARR / 4 + 4] = b0 + 128;
            }
          }
        }
      }
    }
  };
  if (fast) fill(0, (s01 & 0xffff) - ni[0]);
  const bool early = fast && tot0 >= min(64, cnt_all);
  issue_gather(0, 0, early ? ~0u : 0u);
  issue_gather(1, 1, early ? ~0u : 0u);
  if (fast) {
    fill(1, tot0 + (s01 >> 16) - ni[1]);
    fill(2, tot1 + (s23 & 0xffff) - ni[2]);
    fill(3, tot2 + (s23 >> 16) - ni[3]);
  }
  asm volatile("s_waitcnt vmcnt(10)"
               : "+v"(tv[0]), "+v"(tv[1]), "+v"(tv[2]), "+v"(tv[3]), "+v"(xg[0]), "+v"(xg[1]), "+v"(xg[2]), "+v"(xg[3]),
                 "+v"(wa[0]), "+v"(wa[1]), "+v"(wa[2]), "+v"(wa[3]), "+v"(wa[4]), "+v"(wa[5]), "+v"(wa[6]), "+v"(wa[7])
               :: "memory");
  if (fast) {
    int tp = tot3;
    while (true) {
#pragma unroll
      for (int k = 0; k < 4; ++k) {
        if (lane < sn[k]) put(tp + lane, (unsigned)tv[k], (unsigned)(sr[k] + lane) * 32u);
        tp += sn[k];
      }
      if (tp >= cnt_all) break;
      ov_issue();
      asm volatile("s_waitcnt vmcnt(0)" : "+v"(tv[0]), "+v"(tv[1]), "+v"(tv[2]), "+v"(tv[3]) :: "memory");
    }
  }
  if (!early) {
    asm volatile("s_waitcnt vmcnt(0)" ::: "memory");
    if (!fast) fill_slow(0);
    issue_gather(0, 0, ~0u);
    issue_gather(1, 1, ~0u);
  }

  const unsigned cS = lds0 + OFF_T1 + (((lane >> 4) & 1) * 16 + (lane & 3) * 4) * 2;
  const unsigned cSt = lds0 + OFF_T2 + h * 32;
  unsigned fo[4];
#pragma unroll
  for (int t = 0; t < 4; ++t) fo[t] = ring0 + (unsigned)(r * 128 + (((4 * h + t) ^ ((r >> 1) & 7)) * 16));
  const unsigned foe = ring0 + (unsigned)(4096 + r * 32 + ((h ^ ((r >> 3) & 1)) * 16));

  bf16x8 XAf[2][2];
#pragma unroll
  for (int ct = 0; ct < 2; ++ct) {
    f32x16 A;
#pragma unroll
    for (int i = 0; i < 16; ++i) A[i] = 0.f;
#pragma unroll
    for (int k = 0; k < 4; ++k)
      A = MFMA32(__builtin_bit_cast(bf16x8, xg[k]), __builtin_bit_cast(bf16x8, wa[k * 2 + ct]), A);
#pragma unroll
    for (int s = 0; s < 2; ++s) {
      u32x4 p;
#pragma unroll
      for (int j = 0; j < 4; ++j) p[j] = pack_bf16(A[8 * s + 2 * j] * NLOG2E, A[8 * s + 2 * j + 1] * NLOG2E);
      XAf[s][ct] = __builtin_bit_cast(bf16x8, p);
    }
  }

  f32x16 Y[4];
#pragma unroll
  for (int q = 0; q < 4; ++q)
#pragma unroll
    for (int i = 0; i < 16; ++i) Y[q][i] = 0.f;
  const f32x16 zero16 = {0.f, 0.f, 0.f, 0.f, 0.f, 0.f, 0.f, 0.f, 0.f, 0.f, 0.f, 0.f, 0.f, 0.f, 0.f, 0.f};
  const LAS bf16x8* wl = (const LAS bf16x8*)(smem + OFF_W) + lane;

  f32x16 G0, V0, G1, V1;
  bf16x8 S[2], St[2], as[4], ae, b[3];
  u32x4 pd[2], pn[2];
  float tq[16], rq[16], pq[16];

  auto rd128 = [&](unsigned a) { return *(const LAS bf16x8*)(size_t)a; };
  auto load_S = [&](const u32x4 ss) {
    const s16x4 a0 = __builtin_amdgcn_ds_read_tr16_b64_v4i16((LAS s16x4*)(size_t)((ss[0] >> 16) + cS));
    const s16x4 a1 = __builtin_amdgcn_ds_read_tr16_b64_v4i16((LAS s16x4*)(size_t)((ss[1] >> 16) + cS));
    const s16x4 a2 = __builtin_amdgcn_ds_read_tr16_b64_v4i16((LAS s16x4*)(size_t)((ss[2] >> 16) + cS));
    const s16x4 a3 = __builtin_amdgcn_ds_read_tr16_b64_v4i16((LAS s16x4*)(size_t)((ss[3] >> 16) + cS));
    S[0] = __builtin_shufflevector(a0, a1, 0, 1, 2, 3, 4, 5, 6, 7);
    S[1] = __builtin_shufflevector(a2, a3, 0, 1, 2, 3, 4, 5, 6, 7);
  };
  auto widx = [&](int m, int ct) { return m == 6 ? 16 + ct : m < 11 ? (m - 7) * 2 + ct : 8 + (m - 11) * 2 + ct; };

  auto ew = [&](int m, const f32x16& Gc, const f32x16& Vc, const f32x16& Gn, const f32x16& Vp) {
    if (m < 2) {
      const int i = 14 + m;
      pq[i] = mulf(i, Vp[i], rq[i]);
      if (m == 1) pn[1][3] = pack_bf16(pq[14], pq[15]);
    } else if (m == 2) {
      pq[0] = mulf(0, Vc[0], rq[0]);
      pq[1] = mulf(1, Vc[1], rq[1]);
      pn[0][0] = pack_bf16(pq[0], pq[1]);
    } else {
      const int i = m - 1;
      pq[i] = mulf(i, Vc[i], rq[i]);
      if (i & 1) pn[i >> 3][(i >> 1) & 3] = pack_bf16(pq[i - 1], pq[i]);
    }
    if (m == 0) pd[0][0] = pack_bf16(rq[0], rq[1]);
    else if ((m & 1) == 0) { const int i = m + 1; pd[i >> 3][(i >> 1) & 3] = pack_bf16(rq[i - 1], rq[i]); }
    if (m < 14) {
      const int i = m + 2;
      rq[i] = __builtin_amdgcn_rcpf(add1(tq[i]));
    } else {
      rq[0] = __builtin_amdgcn_rcpf(add1(tq[0]));
      rq[1] = __builtin_amdgcn_rcpf(add1(tq[1]));
    }
    if (m < 13) tq[m + 3] = __builtin_amdgcn_exp2f(Gc[m + 3]);
    else if (m == 13) { tq[0] = __builtin_amdgcn_exp2f(Gn[0]); tq[1] = __builtin_amdgcn_exp2f(Gn[1]); }
    else tq[2] = __builtin_amdgcn_exp2f(Gn[2]);
  };

  int wb0 = 0;
  while (true) {
    const int ntiles = (cnt + 31) >> 5;
    asm volatile("s_waitcnt vmcnt(5)" ::: "memory");
    {
      const unsigned sl = *(const LAS unsigned short*)(size_t)aLS1;
#pragma unroll
      for (int k = 0; k < 4; ++k) as[k] = rd128(fo[k]);
      ae = rd128(foe);
      St[0] = rd128(sl + cSt);
      St[1] = rd128(sl + cSt + 16);
      G0 = MFMA32(St[0], XAf[0][0], zero16);
      G0 = MFMA32(St[1], XAf[1][0], G0);
      G0 = MFMA32(ae, wl[widx(6, 0) * 64], G0);
#pragma unroll
      for (int k = 0; k < 4; ++k) G0 = MFMA32(as[k], wl[widx(7 + k, 0) * 64], G0);
      V0 = MFMA32(as[0], wl[widx(11, 0) * 64], zero16);
#pragma unroll
      for (int k = 1; k < 4; ++k) V0 = MFMA32(as[k], wl[widx(11 + k, 0) * 64], V0);
      asm volatile("s_waitcnt lgkmcnt(0)" ::: "memory");
      issue_gather(2, 0, ~0u);
#pragma unroll
      for (int j = 0; j < 8; ++j) { S[0][j] = 0; S[1][j] = 0; }
#pragma unroll
      for (int j = 0; j < 4; ++j) { pd[0][j] = 0; pd[1][j] = 0; pn[0][j] = 0; pn[1][j] = 0; }
#pragma unroll
      for (int i = 0; i < 16; ++i) V1[i] = 0.f;
      rq[14] = 0.f; rq[15] = 0.f;
      tq[0] = __builtin_amdgcn_exp2f(G0[0]);
      tq[1] = __builtin_amdgcn_exp2f(G0[1]);
      tq[2] = __builtin_amdgcn_exp2f(G0[2]);
      rq[0] = __builtin_amdgcn_rcpf(add1(tq[0]));
      rq[1] = __builtin_amdgcn_rcpf(add1(tq[1]));
      SB();
    }
    unsigned lS4 = aLS4, lS1 = aLS1, lX = aLX, lE = aLE;
    auto body = [&](const int par) {
      const unsigned lo = par * 128;
      const unsigned ro = (1 - par) * ringd;
      {
        u32x4 ss;
        unsigned sl;
#pragma unroll
        for (int m = 0; m < 15; ++m) {
          if (m == 0) Y[1] = MFMA32(__builtin_bit_cast(bf16x8, pd[0]), S[0], Y[1]);
          else if (m == 1) Y[3] = MFMA32(__builtin_bit_cast(bf16x8, pn[0]), S[0], Y[3]);
          else if (m == 2) Y[1] = MFMA32(__builtin_bit_cast(bf16x8, pd[1]), S[1], Y[1]);
          else if (m == 3) Y[3] = MFMA32(__builtin_bit_cast(bf16x8, pn[1]), S[1], Y[3]);
          else if (m == 4) G1 = MFMA32(St[0], XAf[0][1], zero16);
          else if (m == 5) G1 = MFMA32(St[1], XAf[1][1], G1);
          else if (m == 6) G1 = MFMA32(ae, b[0], G1);
          else if (m < 11) G1 = MFMA32(as[m - 7], b[(m - 6) % 3], G1);
          else if (m == 11) V1 = MFMA32(as[0], b[(m - 6) % 3], zero16);
          else V1 = MFMA32(as[m - 11], b[(m - 6) % 3], V1);
          SB();
          if (m == 0) { ss = ld128(lS4 + lo); sl = *(const LAS unsigned short*)(size_t)(lS1 + lo + 128); }
          if (m >= 3 && m < 6) b[m - 3] = wl[widx(m + 3, 1) * 64];
          if (m >= 6 && m < 12) b[(m - 6) % 3] = wl[widx(m + 3, 1) * 64];
          if (m == 4) load_S(ss);
          if (m == 5) { St[0] = rd128(sl + cSt); St[1] = rd128(sl + cSt + 16); }
          if (m == 6) { asm volatile("s_waitcnt vmcnt(5)" ::: "memory"); ae = rd128(foe + ro); }
          if (m >= 11) as[m - 11] = rd128(fo[m - 11] + ro);
          ew(m, G0, V0, G1, V1);
          SB();
        }
      }
      {
        u32x4 sx;
        unsigned re;
#pragma unroll
        for (int m = 0; m < 15; ++m) {
          if (m == 0) Y[0] = MFMA32(__builtin_bit_cast(bf16x8, pd[0]), S[0], Y[0]);
          else if (m == 1) Y[2] = MFMA32(__builtin_bit_cast(bf16x8, pn[0]), S[0], Y[2]);
          else if (m == 2) Y[0] = MFMA32(__builtin_bit_cast(bf16x8, pd[1]), S[1], Y[0]);
          else if (m == 3) Y[2] = MFMA32(__builtin_bit_cast(bf16x8, pn[1]), S[1], Y[2]);
          else if (m == 4) G0 = MFMA32(St[0], XAf[0][0], zero16);
          else if (m == 5) G0 = MFMA32(St[1], XAf[1][0], G0);
          else if (m == 6) G0 = MFMA32(ae, b[0], G0);
          else if (m < 11) G0 = MFMA32(as[m - 7], b[(m - 6) % 3], G0);
          else if (m == 11) V0 = MFMA32(as[0], b[(m - 6) % 3], zero16);
          else V0 = MFMA32(as[m - 11], b[(m - 6) % 3], V0);
          SB();
          if (m == 4) { sx = ld128(lX + lo + 3 * 128); re = *(const LAS unsigned*)(size_t)(lE + lo + 3 * 128); }
          if (m >= 3 && m < 6) b[m - 3] = wl[widx(m + 3, 0) * 64];
          if (m >= 6 && m < 12) b[(m - 6) % 3] = wl[widx(m + 3, 0) * 64];
          if (m == 7) glds16x(x, sx[0], cx0, ring0 + ro);
          if (m == 8) glds16x(x, sx[1], cx1, ring0 + ro + 1024);
          if (m == 9) glds16x(x, sx[2], cx0, ring0 + ro + 2048);
          if (m == 10) glds16x(x, sx[3], cx1, ring0 + ro + 3072);
          if (m == 11) glds16e(ES, re, ce, ring0 + ro + 4096);
          ew(m, G1, V1, G0, V0);
          SB();
        }
      }
    };
    for (int t = 0; t < ntiles; t += 2) {
      body(0);
      if (t + 1 >= ntiles) break;
      body(1);
      lS4 += 256; lS1 += 256; lX += 256; lE += 256;
    }
    pq[14] = mulf(0, V1[14], rq[14]);
    pq[15] = mulf(1, V1[15], rq[15]);
    pn[1][3] = pack_bf16(pq[14], pq[15]);
    Y[1] = MFMA32(__builtin_bit_cast(bf16x8, pd[0]), S[0], Y[1]);
    Y[3] = MFMA32(__builtin_bit_cast(bf16x8, pn[0]), S[0], Y[3]);
    Y[1] = MFMA32(__builtin_bit_cast(bf16x8, pd[1]), S[1], Y[1]);
    Y[3] = MFMA32(__builtin_bit_cast(bf16x8, pn[1]), S[1], Y[3]);
    wb0 += LCAP;
    if (wb0 >= cnt_all) break;
    asm volatile("s_waitcnt vmcnt(0)" ::: "memory");
    cnt = min(cnt_all - wb0, LCAP);
    pad_list(cnt);
    fill_slow(wb0);
    issue_gather(0, 0, ~0u);
    issue_gather(1, 1, ~0u);
  }

  if (nodes > 0) {
    u32x4* orow = reinterpret_cast<u32x4*>(out + (size_t)(n0 + r) * 64 + 32 * h);
#pragma unroll
    for (int ct = 0; ct < 2; ++ct) {
      f32x16 U = zero16;
#pragma unroll
      for (int k = 0; k < 4; ++k) U = MFMA32(wl[(18 + k * 2 + ct) * 64], as[k], U);
#pragma unroll
      for (int q = 0; q < 2; ++q) {
        const u32x4 xv = __builtin_bit_cast(u32x4, as[2 * ct + q]);
        u32x4 ov;
#pragma unroll
        for (int j = 0; j < 4; ++j) {
          const int i = 8 * q + 2 * j;
          const float z0 = U[i] + Y[2 + ct][i] * __builtin_amdgcn_rcpf(Y[ct][i] + 1e-6f);
          const float z1 = U[i + 1] + Y[2 + ct][i + 1] * __builtin_amdgcn_rcpf(Y[ct][i + 1] + 1e-6f);
          const float o0 = __uint_as_float(xv[j] << 16) + (z0 > 0.f ? z0 : 0.f);
          const float o1 = __uint_as_float(xv[j] & 0xffff0000u) + (z1 > 0.f ? z1 : 0.f);
          ov[j] = pack_bf16(o0, o1);
        }
        if (r < nodes) orow[2 * ct + q] = ov;
      }
    }
  }
  asm volatile("s_waitcnt vmcnt(0)" ::: "memory");
}

extern "C" void kernel_launch(void* const* d_in, const int* in_sizes, int n_in,
                              void* d_out, int out_size, void* d_ws, size_t ws_size,
                              hipStream_t stream) {
  const unsigned short* x = (const unsigned short*)d_in[0];
  const u32x4* eattr = (const u32x4*)d_in[1];
  const int* src = (const int*)d_in[2];
  const int* dst = (const int*)d_in[3];
  const unsigned short* wa = (const unsigned short*)d_in[4];
  const unsigned short* wb = (const unsigned short*)d_in[5];
  const unsigned short* wc = (const unsigned short*)d_in[6];
  const unsigned short* wu = (const unsigned short*)d_in[7];
  const unsigned short* wv = (const unsigned short*)d_in[8];
  unsigned short* out = (unsigned short*)d_out;
  const int N = in_sizes[0] / 64;
  const int E = in_sizes[2];
  const int chunk = (((E + NCH - 1) / NCH) + 3) & ~3;
  const int nbk = (N + 195) / 196;

  char* ws = (char*)d_ws;
  size_t off = 0;
  unsigned short* wpack = (unsigned short*)(ws + off);   off += 64 * 1024;
  u32x4* ENT = (u32x4*)(ws + off);                       off += (size_t)NBK * NCH * 128;
  u32x4* ES = (u32x4*)(ws + off);                        off += ((size_t)NCH * chunk * 32 + 1023) & ~(size_t)1023;
  unsigned* TS = (unsigned*)(ws + off);

  k_part<<<NCH + NFRAG, 1024, 0, stream>>>(src, dst, eattr, E, chunk, ENT, ES, TS, wa, wb, wc, wu, wv, wpack);
  k_main<<<nbk, 512, 0, stream>>>(x, ENT, ES, TS, chunk, (const bf16x8*)wpack, out, N
     );
}
```

```cpp
#include <hip/hip_runtime.h>
#include <hip/hip_bf16.h>
#include <stdint.h>

using bf16x8 = __attribute__((ext_vector_type(8))) short;
using s16x4 = __attribute__((ext_vector_type(4))) short;
using f32x16 = __attribute__((ext_vector_type(16))) float;
using u32x4 = __attribute__((ext_vector_type(4))) unsigned;
using u32x2 = __attribute__((ext_vector_type(2))) unsigned;
using i32x4 = __attribute__((ext_vector_type(4))) int;
using u16x2 = __attribute__((ext_vector_type(2))) unsigned short;

#define MFMA32(a, b, c) __builtin_amdgcn_mfma_f32_32x32x16_bf16((a), (b), (c), 0, 0, 0)
#define LAS __attribute__((address_space(3)))
#define SB() __builtin_amdgcn_sched_barrier(0)

constexpr int NCH = 256;
constexpr int NBK = 256;
constexpr int NBIN = NBK * 8;
constexpr int NFRAG = 39;
constexpr int CHMAX = 3200;
constexpr float NLOG2E = -1.44269504088896340736f;
constexpr unsigned TPITCH = 80;

__device__ __forceinline__ unsigned pack_bf16(float lo, float hi) {
  typedef __attribute__((ext_vector_type(2))) float f2;
  typedef __attribute__((ext_vector_type(2))) __bf16 b2;
  f2 v = {lo, hi};
  b2 r = __builtin_convertvector(v, b2);
  return __builtin_bit_cast(unsigned, r);
}

__device__ __forceinline__ int wave_scan(int v) {
  v += __builtin_amdgcn_update_dpp(0, v, 0x111, 0xf, 0xf, false);
  v += __builtin_amdgcn_update_dpp(0, v, 0x112, 0xf, 0xf, false);
  v += __builtin_amdgcn_update_dpp(0, v, 0x114, 0xf, 0xf, false);
  v += __builtin_amdgcn_update_dpp(0, v, 0x118, 0xf, 0xf, false);
  v += __builtin_amdgcn_update_dpp(0, v, 0x142, 0xa, 0xf, false);
  v += __builtin_amdgcn_update_dpp(0, v, 0x143, 0xc, 0xf, false);
  return v;
}

__device__ __forceinline__ void glds16(const void* sbase, unsigned voff, unsigned lds_dst) {
  asm volatile(
      "s_mov_b32 m0, %2\n\t"
      "s_nop 0\n\t"
      "global_load_lds_dwordx4 %0, %1"
      :: "v"(voff), "s"(sbase), "s"(lds_dst) : "memory", "m0");
}
__device__ __forceinline__ void glds16nt(const void* sbase, unsigned voff, unsigned lds_dst) {
  asm volatile(
      "s_mov_b32 m0, %2\n\t"
      "s_nop 0\n\t"
      "global_load_lds_dwordx4 %0, %1 nt"
      :: "v"(voff), "s"(sbase), "s"(lds_dst) : "memory", "m0");
}
__device__ __forceinline__ void glds16x(const void* sbase, unsigned a, unsigned c, unsigned lds_dst) {
  unsigned tmp;
  asm volatile(
      "s_mov_b32 m0, %4\n\t"
      "v_mad_u32_u16 %0, %1, %5, %2\n\t"
      "global_load_lds_dwordx4 %0, %3"
      : "=&v"(tmp) : "v"(a), "v"(c), "s"(sbase), "s"(lds_dst), "s"(128u) : "memory", "m0");
}
__device__ __forceinline__ void glds16e(const void* sbase, unsigned a, unsigned c, unsigned lds_dst) {
  unsigned tmp;
  asm volatile(
      "s_mov_b32 m0, %4\n\t"
      "v_add_u32 %0, %1, %2\n\t"
      "global_load_lds_dwordx4 %0, %3"
      : "=&v"(tmp) : "v"(a), "v"(c), "s"(sbase), "s"(lds_dst) : "memory", "m0");
}
__device__ __forceinline__ int gload32(const void* sbase, unsigned voff) {
  int r;
  asm volatile("global_load_dword %0, %1, %2" : "=v"(r) : "v"(voff), "s"(sbase) : "memory");
  return r;
}
__device__ __forceinline__ u32x4 gload128(const void* sbase, unsigned voff) {
  u32x4 r;
  asm volatile("global_load_dwordx4 %0, %1, %2" : "=v"(r) : "v"(voff), "s"(sbase) : "memory");
  return r;
}

constexpr int CELL = 160;
constexpr unsigned DMARK = 0xF0000000u;
constexpr int KP_ENT = 0;
constexpr int KP_HIST = KP_ENT + NBK * CELL;
constexpr int KP_OVC = KP_HIST + NBIN * 4;
constexpr int KP_LDS = KP_OVC + 16;
constexpr int NWL = 26;
constexpr int NBLOB = 31;

__global__ __launch_bounds__(1024) void k_part(const int* __restrict__ src, const int* __restrict__ dst,
                                               int E, int chunk, u32x4* __restrict__ ENT,
                                               unsigned* __restrict__ TS,
                                               const unsigned short* __restrict__ wa,
                                               const unsigned short* __restrict__ wb,
                                               const unsigned short* __restrict__ wc,
                                               const unsigned short* __restrict__ wu,
                                               const unsigned short* __restrict__ wv,
                                               unsigned short* __restrict__ wpack) {
  const int tid = threadIdx.x;
  if (blockIdx.x < NCH) {
    __shared__ __attribute__((aligned(16))) char sm[KP_LDS];
    LAS unsigned* hist = (LAS unsigned*)(sm + KP_HIST);
    LAS unsigned* ovc = (LAS unsigned*)(sm + KP_OVC);
    const int c = blockIdx.x;
    const int base = c * chunk;
    const int cnt = max(min(chunk, E - base), 0);
    const int last = max(min(base + cnt, E) - 1, 0);
    int d[4], sv[4];
#pragma unroll
    for (int k = 0; k < 4; ++k) d[k] = gload32(dst, (unsigned)min(base + tid + 1024 * k, last) * 4u);
#pragma unroll
    for (int k = 0; k < 4; ++k) sv[k] = gload32(src, (unsigned)min(base + tid + 1024 * k, last) * 4u);
    *(LAS u32x2*)(hist + tid * 2) = u32x2{0u, 0u};
    if (tid == 0) *ovc = 0u;
    asm volatile("s_waitcnt lgkmcnt(0)\n\ts_barrier\n\ts_waitcnt vmcnt(4)"
                 : "+v"(d[0]), "+v"(d[1]), "+v"(d[2]), "+v"(d[3]) :: "memory");
    int bin[4], rk[4];
    unsigned tup[4];
#pragma unroll
    for (int k = 0; k < 4; ++k) {
      const int g2 = d[k] / 49;
      const int rem = d[k] - 49 * g2;
      const int hi = rem >= 25;
      bin[k] = (g2 >> 2) * 8 + (g2 & 3) + 4 * hi;
      tup[k] = (unsigned)(rem - 25 * hi) * 3200u + (unsigned)(tid + 1024 * k);
      rk[k] = -1;
      if (tid + 1024 * k < cnt)
        rk[k] = __hip_atomic_fetch_add(hist + bin[k], 1u, __ATOMIC_RELAXED, __HIP_MEMORY_SCOPE_WORKGROUP);
    }
    asm volatile("s_waitcnt vmcnt(0)" : "+v"(sv[0]), "+v"(sv[1]), "+v"(sv[2]), "+v"(sv[3]) :: "memory");
#pragma unroll
    for (int k = 0; k < 4; ++k) {
      tup[k] += (unsigned)sv[k] * 80000u;
      if ((unsigned)rk[k] < 4u)
        *(LAS unsigned*)(sm + KP_ENT + (bin[k] >> 3) * CELL + (bin[k] & 7) * 16 + rk[k] * 4) = tup[k];
    }
    asm volatile("s_waitcnt lgkmcnt(0)\n\ts_barrier" ::: "memory");
    {
      const u32x2 tt = *(const LAS u32x2*)(hist + tid * 2);
      u32x2 ds;
#pragma unroll
      for (int i = 0; i < 2; ++i) {
        unsigned pos = 0;
        if (tt[i] > 5u) pos = __hip_atomic_fetch_add(ovc, tt[i] - 4u, __ATOMIC_RELAXED, __HIP_MEMORY_SCOPE_WORKGROUP);
        ds[i] = DMARK | (tt[i] << 12) | pos;
        if (tt[i] != 5u)
          *(LAS unsigned*)(sm + KP_ENT + (tid >> 2) * CELL + 128 + ((((tid & 3) * 2 + i + (c >> 3)) & 7) * 4)) = ds[i];
      }
      *(LAS u32x2*)(hist + tid * 2) = ds;
    }
    asm volatile("s_waitcnt lgkmcnt(0)\n\ts_barrier" ::: "memory");
#pragma unroll
    for (int k = 0; k < 4; ++k) {
      if (rk[k] >= 4) {
        const unsigned ds = hist[bin[k]];
        if (((ds >> 12) & 0xfffu) == 5u)
          *(LAS unsigned*)(sm + KP_ENT + (bin[k] >> 3) * CELL + 128 + (((bin[k] + (c >> 3)) & 7) * 4)) = tup[k];
        else
          TS[(size_t)base + (ds & 0xfffu) + rk[k] - 4] = tup[k];
      }
    }
    asm volatile("s_waitcnt lgkmcnt(0)\n\ts_barrier" ::: "memory");
#pragma unroll
    for (int j = 0; j < 3; ++j) {
      const int i = tid + 1024 * j;
      if (i < NBK * (CELL / 16)) {
        const int bk = (i * 6554) >> 16;
        ENT[((size_t)bk * NCH + c) * (CELL / 16) + (i - bk * 10)] = *(const LAS u32x4*)(sm + KP_ENT + i * 16);
      }
    }
  } else if (tid < 512) {
    const int f = blockIdx.x - NCH;
    unsigned short v;
    if (f >= NWL && f < NBLOB) {
      const int e = (f - NWL) * 512 + tid;
      const int tb = e >= 32 * (TPITCH / 2);
      const int e2 = e - tb * 32 * (TPITCH / 2);
      const int sl = e2 / (TPITCH / 2), col = e2 - sl * (TPITCH / 2);
      const int nd = tb ? 16 * ((col >> 3) & 1) + 8 * ((col >> 2) & 1) + 4 * (col >> 4) + (col & 3) : col;
      v = (col < 32 && nd == sl) ? 0x3F80 : 0;
    } else {
      const int lane = tid >> 3, j = tid & 7;
      const int n = lane & 31, h = lane >> 5;
      const int g = f < 16 ? f : f < NWL ? f - 18 : f - NBLOB;
      const int ct = g & 1;
      const int col = 32 * ((n >> 2) & 1) + 16 * ct + 4 * (n >> 3) + (n & 3);
      if (f == 16 || f == 17) {
        v = wc[(8 * h + j) * 64 + col];
      } else {
        const int t = (g >> 1) & 3;
        const unsigned short* W = f < 8 ? wb : f < 16 ? wv : f < NWL ? wu : wa;
        v = W[(32 * h + 8 * t + j) * 64 + col];
      }
      if (f < 8 || f == 16 || f == 17 || f >= NBLOB)
        v = (unsigned short)(pack_bf16(__uint_as_float((unsigned)v << 16) * NLOG2E, 0.f) & 0xffffu);
    }
    wpack[f * 512 + tid] = v;
  }
}

constexpr int RING_SLOT = 5 * 1024;
constexpr int LCAP = 512;
constexpr int LARR = 2816;
constexpr int LIST_BYTES = 2 * LARR;
constexpr int OFF_W = 0;
constexpr int OFF_T1 = NWL * 1024;
constexpr int OFF_T2 = OFF_T1 + 32 * TPITCH;
constexpr int OFF_R0 = OFF_T2 + 32 * TPITCH;
constexpr int OFF_R1 = OFF_R0 + 8 * RING_SLOT;
constexpr int OFF_L = OFF_R1 + 8 * RING_SLOT;
constexpr int MAIN_LDS = OFF_L + 8 * LIST_BYTES;
static_assert(MAIN_LDS <= 160 * 1024, "LDS");

__device__ __forceinline__ float add1(float t) { return 1.0f + t; }
__device__ __forceinline__ float mulf(int i, float a, float b) { return (i & 1) ? __builtin_fmaf(a, b, 0.0f) : a * b; }

__global__ __launch_bounds__(512, 2) void k_main(const unsigned short* __restrict__ x,
                                                 const u32x4* __restrict__ ENT,
                                                 const u32x4* __restrict__ ES,
                                                 const unsigned* __restrict__ TS, int chunk,
                                                 const bf16x8* __restrict__ wpack,
                                                 unsigned short* __restrict__ out, int N
                                                 ) {
  __shared__ __attribute__((aligned(16))) char smem[MAIN_LDS];
  const int tid = threadIdx.x, lane = tid & 63;
  const int wave = __builtin_amdgcn_readfirstlane(tid >> 6);
  const int r = lane & 31, h = lane >> 5;
  const int bk = blockIdx.x;
  const int nb0 = bk * 196 + 49 * (wave & 3) + 25 * (wave >> 2);
  const int nodes = max(min(wave < 4 ? 25 : 24, N - nb0), 0);
  const int n0 = min(nb0, N - 1);
  const unsigned lds0 = (unsigned)(size_t)(LAS char*)smem;

  u32x4 xg[4], wa[8];
  {
    const unsigned vo = lane * 16;
    const char* eb = reinterpret_cast<const char*>(ENT) + (size_t)bk * (NCH * CELL) + wave * 5120;
#pragma unroll
    for (int i = 0; i < 5; ++i) glds16(eb + i * 1024, vo, lds0 + OFF_R1 + wave * 5120 + i * 1024);
    const char* wp = reinterpret_cast<const char*>(wpack);
#pragma unroll
    for (int i = 0; i < 4; ++i) {
      const int f = min(wave + 8 * i, NBLOB - 1);
      glds16(wp + f * 1024, vo, lds0 + OFF_W + f * 1024);
    }
    const unsigned xo = (unsigned)(n0 + max(min(r, nodes - 1), 0)) * 128u + 64u * h;
#pragma unroll
    for (int t = 0; t < 4; ++t) xg[t] = gload128(x, xo + 16 * t);
#pragma unroll
    for (int k = 0; k < 8; ++k) wa[k] = gload128(wp + (NBLOB + k) * 1024, vo);
  }
  LAS char* lbase = (LAS char*)smem + OFF_L + wave * LIST_BYTES;
  const unsigned la = (unsigned)(size_t)lbase;
  auto put = [&](int p, unsigned a, unsigned b) {
    *(LAS unsigned*)(lbase + p * 4) = a;
    *(LAS unsigned*)(lbase + LARR + p * 4) = b;
  };
  const unsigned aLX = la + (lane >> 3) * 16;
  const unsigned aLE = la + LARR + ((((lane >> 1) & 7) << 2) | (lane >> 4)) * 4;
  const unsigned aLS4 = la + (4 * h + ((lane >> 2) & 3)) * 16;
  const unsigned aLS1 = la + (((r & 7) << 2) | (r >> 3)) * 4 + 2;
  const unsigned cx0 = (unsigned)(((lane & 7) ^ (lane >> 4)) * 16);
  const unsigned cx1 = cx0 ^ 64u;
  const unsigned ce = (unsigned)((((lane & 1) ^ ((lane >> 4) & 1))) * 16);
  const unsigned ring0 = lds0 + OFF_R0 + wave * RING_SLOT;
  const unsigned ringd = 8 * RING_SLOT;
  const unsigned padA = (unsigned)(n0 + max(min(((lane >> 2) & 7) + 8 * (lane & 3), nodes - 1), 0)) | (31u * TPITCH << 16);
  asm volatile("s_waitcnt vmcnt(12)\n\ts_barrier" ::: "memory");
  u32x4 cl[4];
  unsigned cx[4];
#pragma unroll
  for (int j = 0; j < 4; ++j) {
    cl[j] = *(const LAS u32x4*)(smem + OFF_R1 + (lane + 64 * j) * CELL + wave * 16);
    cx[j] = *(const LAS unsigned*)(smem + OFF_R1 + (lane + 64 * j) * CELL + 128 + (((lane >> 3) + wave) & 7) * 4);
  }
  asm volatile("s_waitcnt lgkmcnt(0)\n\ts_barrier"
               : "+v"(cl[0]), "+v"(cl[1]), "+v"(cl[2]), "+v"(cl[3]), "+v"(cx[0]), "+v"(cx[1]), "+v"(cx[2]), "+v"(cx[3])
               :: "memory");

  auto ld128 = [&](unsigned a) { return *(const LAS u32x4*)(size_t)a; };
  auto issue_gather = [&](int tile, int rs, unsigned mask) {
    const u32x4 sx = ld128(aLX + tile * 128) & mask;
    const unsigned re = *(const LAS unsigned*)(size_t)(aLE + tile * 128) & mask;
    const unsigned dstb = ring0 + rs * ringd;
    glds16x(x, sx[0], cx0, dstb);
    glds16x(x, sx[1], cx1, dstb + 1024);
    glds16x(x, sx[2], cx0, dstb + 2048);
    glds16x(x, sx[3], cx1, dstb + 3072);
    glds16e(ES, re, ce, dstb + 4096);
  };
  auto dense = [&](int k, int cnt) {
    LAS unsigned* pa = (LAS unsigned*)(lbase + (k * 64 + lane) * 4);
    const unsigned v = pa[0], cb = pa[LARR / 4];
    const unsigned sv = v / 80000u;
    const unsigned rem = v - __umul24(sv, 80000u);
    const unsigned sl = __umul24(rem >> 7, 41u) >> 10;
    const unsigned ix = rem - __umul24(sl, 3200u);
    const bool ok = k * 64 + lane < cnt;
    pa[0] = ok ? __umul24(sl, TPITCH << 16) + sv : padA;
    pa[LARR / 4] = ok ? (cb + ix) * 32u : 0u;
  };
  const int gbin = bk * 8 + wave;
  auto fill_slow = [&](int wb0) {
    int nn[4], tot = 0;
    u32x4 ce4[4];
    unsigned cx4[4];
#pragma unroll
    for (int j = 0; j < 4; ++j) {
      const char* cp = reinterpret_cast<const char*>(ENT) + ((size_t)bk * NCH + lane + 64 * j) * CELL;
      ce4[j] = *reinterpret_cast<const u32x4*>(cp + wave * 16);
      cx4[j] = *reinterpret_cast<const unsigned*>(cp + 128 + (((lane >> 3) + wave) & 7) * 4);
      nn[j] = cx4[j] >= DMARK ? (int)((cx4[j] >> 12) & 0xfffu) : 5;
      tot += nn[j];
    }
    int pp = wave_scan(tot) - tot - wb0;
#pragma unroll
    for (int j = 0; j < 4; ++j) {
      const int cb = (lane + 64 * j) * chunk;
      const int te = min(nn[j], LCAP - pp);
#pragma unroll 1
      for (int t = max(0, -pp); t < te; ++t) {
        unsigned a;
        if (t < 4) a = t == 0 ? ce4[j][0] : t == 1 ? ce4[j][1] : t == 2 ? ce4[j][2] : ce4[j][3];
        else if (nn[j] == 5) a = cx4[j];
        else a = TS[cb + (int)(cx4[j] & 0xfffu) + t - 4];
        put(pp + t, a, (unsigned)cb);
      }
      pp += nn[j];
    }
    __builtin_amdgcn_s_waitcnt(0x0F70);
  };

  int ni[4], ex[4], orc[4];
  int ox = 0;
#pragma unroll
  for (int j = 0; j < 4; ++j) {
    const int n = cx[j] >= DMARK ? (int)((cx[j] >> 12) & 0xfffu) : 5;
    const bool ov = n > 5;
    ni[j] = ov ? 4 : n;
    ex[j] = ov ? n - 4 : 0;
    orc[j] = (int)(cx[j] & 0xfffu);
    ox += ex[j];
  }
  int tv[4], sb[4], sn[4];
  auto ov_issue = [&]() {
    int o_n = 0, o_rec = 0, o_j = 0;
#pragma unroll
    for (int j = 3; j >= 0; --j) {
      const bool on = ex[j] > 0;
      o_n = on ? min(ex[j], 64) : o_n;
      o_rec = on ? orc[j] : o_rec;
      o_j = on ? j : o_j;
    }
    unsigned long long om = __ballot(o_n > 0);
    const int rank = __builtin_amdgcn_mbcnt_hi((unsigned)(om >> 32), __builtin_amdgcn_mbcnt_lo((unsigned)om, 0u));
    const int o_pk = o_rec | (o_n << 12) | (o_j << 20);
#pragma unroll
    for (int k = 0; k < 4; ++k) {
      const int l = om ? __builtin_ctzll(om) : 0;
      const int pk = om ? __builtin_amdgcn_readlane(o_pk, l) : 0;
      om &= om - 1;
      sb[k] = (l + 64 * (pk >> 20)) * chunk;
      sn[k] = (pk >> 12) & 0xff;
      tv[k] = gload32(TS, (unsigned)(sb[k] + (pk & 0xfff) + max(min(lane, sn[k] - 1), 0)) * 4u);
    }
    const int tk = rank < 4 ? o_n : 0;
#pragma unroll
    for (int j = 0; j < 4; ++j) {
      ex[j] -= o_j == j ? tk : 0;
      orc[j] += o_j == j ? tk : 0;
    }
  };
  ov_issue();
  const int s01 = wave_scan(ni[0] | (ni[1] << 16));
  const int s23 = wave_scan(ni[2] | (ni[3] << 16));
  const int sox = wave_scan(ox);
  const int e01 = __builtin_amdgcn_readlane(s01, 63), e23 = __builtin_amdgcn_readlane(s23, 63);
  const int tot0 = e01 & 0xffff, tot1 = tot0 + (e01 >> 16), tot2 = tot1 + (e23 & 0xffff), tot3 = tot2 + (e23 >> 16);
  const int oxt = __builtin_amdgcn_readlane(sox, 63);
  const int cnt_all = tot3 + oxt;
  const bool fast = cnt_all <= LCAP;
  int cnt = min(cnt_all, LCAP);
  auto fill = [&](int j, int p) {
    LAS unsigned* pa = (LAS unsigned*)(lbase + p * 4);
    const unsigned cb = (unsigned)((lane + 64 * j) * chunk);
    if (ni[j] > 0) {
      pa[0] = cl[j][0];
      pa[LARR / 4] = cb;
      if (ni[j] > 1) {
        pa[1] = cl[j][1];
        pa[LARR / 4 + 1] = cb;
        if (ni[j] > 2) {
          pa[2] = cl[j][2];
          pa[LARR / 4 + 2] = cb;
          if (ni[j] > 3) {
            pa[3] = cl[j][3];
            pa[LARR / 4 + 3] = cb;
            if (ni[j] > 4) {
              pa[4] = cx[j];
              pa[LARR / 4 + 4] = cb;
            }
          }
        }
      }
    }
  };
  const bool early = fast && tot0 >= min(64, cnt_all);
  if (early) {
    fill(0, (s01 & 0xffff) - ni[0]);
    dense(0, cnt);
  }
  issue_gather(0, 0, early ? ~0u : 0u);
  issue_gather(1, 1, early ? ~0u : 0u);
  int kd = early ? 1 : 0;
  if (fast) {
    if (!early) fill(0, (s01 & 0xffff) - ni[0]);
    fill(1, tot0 + (s01 >> 16) - ni[1]);
    fill(2, tot1 + (s23 & 0xffff) - ni[2]);
    fill(3, tot2 + (s23 >> 16) - ni[3]);
#pragma unroll 1
    for (; kd * 64 + 64 <= tot3; ++kd) dense(kd, cnt);
  }
  asm volatile("s_waitcnt vmcnt(10)"
               : "+v"(tv[0]), "+v"(tv[1]), "+v"(tv[2]), "+v"(tv[3]), "+v"(xg[0]), "+v"(xg[1]), "+v"(xg[2]), "+v"(xg[3]),
                 "+v"(wa[0]), "+v"(wa[1]), "+v"(wa[2]), "+v"(wa[3]), "+v"(wa[4]), "+v"(wa[5]), "+v"(wa[6]), "+v"(wa[7])
               :: "memory");
  if (fast) {
    int tp = tot3;
    while (true) {
#pragma unroll
      for (int k = 0; k < 4; ++k) {
        if (lane < sn[k]) put(tp + lane, (unsigned)tv[k], (unsigned)sb[k]);
        tp += sn[k];
      }
      if (tp >= cnt_all) break;
      ov_issue();
      asm volatile("s_waitcnt vmcnt(0)" : "+v"(tv[0]), "+v"(tv[1]), "+v"(tv[2]), "+v"(tv[3]) :: "memory");
    }
  } else {
    asm volatile("s_waitcnt vmcnt(0)" ::: "memory");
    fill_slow(0);
  }
#pragma unroll 1
  for (; kd * 64 < cnt + 128; ++kd) dense(kd, cnt);
  if (!early) {
    asm volatile("s_waitcnt vmcnt(0)" ::: "memory");
    issue_gather(0, 0, ~0u);
    issue_gather(1, 1, ~0u);
  }

  const unsigned cS = lds0 + OFF_T1 + (((lane >> 4) & 1) * 16 + (lane & 3) * 4) * 2;
  const unsigned cSt = lds0 + OFF_T2 + h * 32;
  unsigned fo[4];
#pragma unroll
  for (int t = 0; t < 4; ++t) fo[t] = ring0 + (unsigned)(r * 128 + (((4 * h + t) ^ ((r >> 1) & 7)) * 16));
  const unsigned foe = ring0 + (unsigned)(4096 + r * 32 + ((h ^ ((r >> 3) & 1)) * 16));

  bf16x8 XAf[2][2];
#pragma unroll
  for (int ct = 0; ct < 2; ++ct) {
    f32x16 A;
#pragma unroll
    for (int i = 0; i < 16; ++i) A[i] = 0.f;
#pragma unroll
    for (int k = 0; k < 4; ++k)
      A = MFMA32(__builtin_bit_cast(bf16x8, xg[k]), __builtin_bit_cast(bf16x8, wa[k * 2 + ct]), A);
#pragma unroll
    for (int s = 0; s < 2; ++s) {
      u32x4 p;
#pragma unroll
      for (int j = 0; j < 4; ++j) p[j] = pack_bf16(A[8 * s + 2 * j], A[8 * s + 2 * j + 1]);
      XAf[s][ct] = __builtin_bit_cast(bf16x8, p);
    }
  }

  f32x16 Y[4];
#pragma unroll
  for (int q = 0; q < 4; ++q)
#pragma unroll
    for (int i = 0; i < 16; ++i) Y[q][i] = 0.f;
  const f32x16 zero16 = {0.f, 0.f, 0.f, 0.f, 0.f, 0.f, 0.f, 0.f, 0.f, 0.f, 0.f, 0.f, 0.f, 0.f, 0.f, 0.f};
  const LAS bf16x8* wl = (const LAS bf16x8*)(smem + OFF_W) + lane;

  f32x16 G0, V0, G1, V1;
  bf16x8 S[2], St[2], as[4], ae, b[3];
  u32x4 pd[2], pn[2];
  float tq[16], rq[16], pq[16];

  auto rd128 = [&](unsigned a) { return *(const LAS bf16x8*)(size_t)a; };
  auto load_S = [&](const u32x4 ss) {
    const s16x4 a0 = __builtin_amdgcn_ds_read_tr16_b64_v4i16((LAS s16x4*)(size_t)((ss[0] >> 16) + cS));
    const s16x4 a1 = __builtin_amdgcn_ds_read_tr16_b64_v4i16((LAS s16x4*)(size_t)((ss[1] >> 16) + cS));
    const s16x4 a2 = __builtin_amdgcn_ds_read_tr16_b64_v4i16((LAS s16x4*)(size_t)((ss[2] >> 16) + cS));
    const s16x4 a3 = __builtin_amdgcn_ds_read_tr16_b64_v4i16((LAS s16x4*)(size_t)((ss[3] >> 16) + cS));
    S[0] = __builtin_shufflevector(a0, a1, 0, 1, 2, 3, 4, 5, 6, 7);
    S[1] = __builtin_shufflevector(a2, a3, 0, 1, 2, 3, 4, 5, 6, 7);
  };
  auto widx = [&](int m, int ct) { return m == 6 ? 16 + ct : m < 11 ? (m - 7) * 2 + ct : 8 + (m - 11) * 2 + ct; };

  auto ew = [&](int m, const f32x16& Gc, const f32x16& Vc, const f32x16& Gn, const f32x16& Vp) {
    if (m < 2) {
      const int i = 14 + m;
      pq[i] = mulf(i, Vp[i], rq[i]);
      if (m == 1) pn[1][3] = pack_bf16(pq[14], pq[15]);
    } else if (m == 2) {
      pq[0] = mulf(0, Vc[0], rq[0]);
      pq[1] = mulf(1, Vc[1], rq[1]);
      pn[0][0] = pack_bf16(pq[0], pq[1]);
    } else {
      const int i = m - 1;
      pq[i] = mulf(i, Vc[i], rq[i]);
      if (i & 1) pn[i >> 3][(i >> 1) & 3] = pack_bf16(pq[i - 1], pq[i]);
    }
    if (m == 0) pd[0][0] = pack_bf16(rq[0], rq[1]);
    else if ((m & 1) == 0) { const int i = m + 1; pd[i >> 3][(i >> 1) & 3] = pack_bf16(rq[i - 1], rq[i]); }
    if (m < 14) {
      const int i = m + 2;
      rq[i] = __builtin_amdgcn_rcpf(add1(tq[i]));
    } else {
      rq[0] = __builtin_amdgcn_rcpf(add1(tq[0]));
      rq[1] = __builtin_amdgcn_rcpf(add1(tq[1]));
    }
    if (m < 13) tq[m + 3] = __builtin_amdgcn_exp2f(Gc[m + 3]);
    else if (m == 13) { tq[0] = __builtin_amdgcn_exp2f(Gn[0]); tq[1] = __builtin_amdgcn_exp2f(Gn[1]); }
    else tq[2] = __builtin_amdgcn_exp2f(Gn[2]);
  };

  int wb0 = 0;
  while (true) {
    const int ntiles = (cnt + 31) >> 5;
    asm volatile("s_waitcnt vmcnt(5)" ::: "memory");
    {
      const unsigned sl = *(const LAS unsigned short*)(size_t)aLS1;
#pragma unroll
      for (int k = 0; k < 4; ++k) as[k] = rd128(fo[k]);
      ae = rd128(foe);
      St[0] = rd128(sl + cSt);
      St[1] = rd128(sl + cSt + 16);
      G0 = MFMA32(St[0], XAf[0][0], zero16);
      G0 = MFMA32(St[1], XAf[1][0], G0);
      G0 = MFMA32(ae, wl[widx(6, 0) * 64], G0);
#pragma unroll
      for (int k = 0; k < 4; ++k) G0 = MFMA32(as[k], wl[widx(7 + k, 0) * 64], G0);
      V0 = MFMA32(as[0], wl[widx(11, 0) * 64], zero16);
#pragma unroll
      for (int k = 1; k < 4; ++k) V0 = MFMA32(as[k], wl[widx(11 + k, 0) * 64], V0);
      asm volatile("s_waitcnt lgkmcnt(0)" ::: "memory");
      issue_gather(2, 0, ~0u);
#pragma unroll
      for (int j = 0; j < 8; ++j) { S[0][j] = 0; S[1][j] = 0; }
#pragma unroll
      for (int j = 0; j < 4; ++j) { pd[0][j] = 0; pd[1][j] = 0; pn[0][j] = 0; pn[1][j] = 0; }
#pragma unroll
      for (int i = 0; i < 16; ++i) V1[i] = 0.f;
      rq[14] = 0.f; rq[15] = 0.f;
      tq[0] = __builtin_amdgcn_exp2f(G0[0]);
      tq[1] = __builtin_amdgcn_exp2f(G0[1]);
      tq[2] = __builtin_amdgcn_exp2f(G0[2]);
      rq[0] = __builtin_amdgcn_rcpf(add1(tq[0]));
      rq[1] = __builtin_amdgcn_rcpf(add1(tq[1]));
      SB();
    }
    unsigned lS4 = aLS4, lS1 = aLS1, lX = aLX, lE = aLE;
    auto body = [&](const int par) {
      const unsigned lo = par * 128;
      const unsigned ro = (1 - par) * ringd;
      {
        u32x4 ss;
        unsigned sl;
#pragma unroll
        for (int m = 0; m < 15; ++m) {
          if (m == 0) Y[1] = MFMA32(__builtin_bit_cast(bf16x8, pd[0]), S[0], Y[1]);
          else if (m == 1) Y[3] = MFMA32(__builtin_bit_cast(bf16x8, pn[0]), S[0], Y[3]);
          else if (m == 2) Y[1] = MFMA32(__builtin_bit_cast(bf16x8, pd[1]), S[1], Y[1]);
          else if (m == 3) Y[3] = MFMA32(__builtin_bit_cast(bf16x8, pn[1]), S[1], Y[3]);
          else if (m == 4) G1 = MFMA32(St[0], XAf[0][1], zero16);
          else if (m == 5) G1 = MFMA32(St[1], XAf[1][1], G1);
          else if (m == 6) G1 = MFMA32(ae, b[0], G1);
          else if (m < 11) G1 = MFMA32(as[m - 7], b[(m - 6) % 3], G1);
          else if (m == 11) V1 = MFMA32(as[0], b[(m - 6) % 3], zero16);
          else V1 = MFMA32(as[m - 11], b[(m - 6) % 3], V1);
          SB();
          if (m == 0) { ss = ld128(lS4 + lo); sl = *(const LAS unsigned short*)(size_t)(lS1 + lo + 128); }
          if (m >= 3 && m < 6) b[m - 3] = wl[widx(m + 3, 1) * 64];
          if (m >= 6 && m < 12) b[(m - 6) % 3] = wl[widx(m + 3, 1) * 64];
          if (m == 4) load_S(ss);
          if (m == 5) { St[0] = rd128(sl + cSt); St[1] = rd128(sl + cSt + 16); }
          if (m == 6) { asm volatile("s_waitcnt vmcnt(5)" ::: "memory"); ae = rd128(foe + ro); }
          if (m >= 11) as[m - 11] = rd128(fo[m - 11] + ro);
          ew(m, G0, V0, G1, V1);
          SB();
        }
      }
      {
        u32x4 sx;
        unsigned re;
#pragma unroll
        for (int m = 0; m < 15; ++m) {
          if (m == 0) Y[0] = MFMA32(__builtin_bit_cast(bf16x8, pd[0]), S[0], Y[0]);
          else if (m == 1) Y[2] = MFMA32(__builtin_bit_cast(bf16x8, pn[0]), S[0], Y[2]);
          else if (m == 2) Y[0] = MFMA32(__builtin_bit_cast(bf16x8, pd[1]), S[1], Y[0]);
          else if (m == 3) Y[2] = MFMA32(__builtin_bit_cast(bf16x8, pn[1]), S[1], Y[2]);
          else if (m == 4) G0 = MFMA32(St[0], XAf[0][0], zero16);
          else if (m == 5) G0 = MFMA32(St[1], XAf[1][0], G0);
          else if (m == 6) G0 = MFMA32(ae, b[0], G0);
          else if (m < 11) G0 = MFMA32(as[m - 7], b[(m - 6) % 3], G0);
          else if (m == 11) V0 = MFMA32(as[0], b[(m - 6) % 3], zero16);
          else V0 = MFMA32(as[m - 11], b[(m - 6) % 3], V0);
          SB();
          if (m == 4) { sx = ld128(lX + lo + 3 * 128); re = *(const LAS unsigned*)(size_t)(lE + lo + 3 * 128); }
          if (m >= 3 && m < 6) b[m - 3] = wl[widx(m + 3, 0) * 64];
          if (m >= 6 && m < 12) b[(m - 6) % 3] = wl[widx(m + 3, 0) * 64];
          if (m == 7) glds16x(x, sx[0], cx0, ring0 + ro);
          if (m == 8) glds16x(x, sx[1], cx1, ring0 + ro + 1024);
          if (m == 9) glds16x(x, sx[2], cx0, ring0 + ro + 2048);
          if (m == 10) glds16x(x, sx[3], cx1, ring0 + ro + 3072);
          if (m == 11) glds16e(ES, re, ce, ring0 + ro + 4096);
          ew(m, G1, V1, G0, V0);
          SB();
        }
      }
    };
    for (int t = 0; t < ntiles; t += 2) {
      body(0);
      if (t + 1 >= ntiles) break;
      body(1);
      lS4 += 256; lS1 += 256; lX += 256; lE += 256;
    }
    pq[14] = mulf(0, V1[14], rq[14]);
    pq[15] = mulf(1, V1[15], rq[15]);
    pn[1][3] = pack_bf16(pq[14], pq[15]);
    Y[1] = MFMA32(__builtin_bit_cast(bf16x8, pd[0]), S[0], Y[1]);
    Y[3] = MFMA32(__builtin_bit_cast(bf16x8, pn[0]), S[0], Y[3]);
    Y[1] = MFMA32(__builtin_bit_cast(bf16x8, pd[1]), S[1], Y[1]);
    Y[3] = MFMA32(__builtin_bit_cast(bf16x8, pn[1]), S[1], Y[3]);
    wb0 += LCAP;
    if (wb0 >= cnt_all) break;
    asm volatile("s_waitcnt vmcnt(0)" ::: "memory");
    cnt = min(cnt_all - wb0, LCAP);
    fill_slow(wb0);
#pragma unroll 1
    for (int k = 0; k * 64 < cnt + 128; ++k) dense(k, cnt);
    issue_gather(0, 0, ~0u);
    issue_gather(1, 1, ~0u);
  }

  if (nodes > 0) {
    u32x4* orow = reinterpret_cast<u32x4*>(out + (size_t)(n0 + r) * 64 + 32 * h);
#pragma unroll
    for (int ct = 0; ct < 2; ++ct) {
      f32x16 U = zero16;
#pragma unroll
      for (int k = 0; k < 4; ++k) U = MFMA32(wl[(18 + k * 2 + ct) * 64], as[k], U);
#pragma unroll
      for (int q = 0; q < 2; ++q) {
        const u32x4 xv = __builtin_bit_cast(u32x4, as[2 * ct + q]);
        u32x4 ov;
#pragma unroll
        for (int j = 0; j < 4; ++j) {
          const int i = 8 * q + 2 * j;
          const float z0 = U[i] + Y[2 + ct][i] * __builtin_amdgcn_rcpf(Y[ct][i] + 1e-6f);
          const float z1 = U[i + 1] + Y[2 + ct][i + 1] * __builtin_amdgcn_rcpf(Y[ct][i + 1] + 1e-6f);
          const float o0 = __uint_as_float(xv[j] << 16) + (z0 > 0.f ? z0 : 0.f);
          const float o1 = __uint_as_float(xv[j] & 0xffff0000u) + (z1 > 0.f ? z1 : 0.f);
          ov[j] = pack_bf16(o0, o1);
        }
        if (r < nodes) orow[2 * ct + q] = ov;
      }
    }
  }
  asm volatile("s_waitcnt vmcnt(0)" ::: "memory");
}

extern "C" void kernel_launch(void* const* d_in, const int* in_sizes, int n_in,
                              void* d_out, int out_size, void* d_ws, size_t ws_size,
                              hipStream_t stream) {
  const unsigned short* x = (const unsigned short*)d_in[0];
  const u32x4* eattr = (const u32x4*)d_in[1];
  const int* src = (const int*)d_in[2];
  const int* dst = (const int*)d_in[3];
  const unsigned short* wa = (const unsigned short*)d_in[4];
  const unsigned short* wb = (const unsigned short*)d_in[5];
  const unsigned short* wc = (const unsigned short*)d_in[6];
  const unsigned short* wu = (const unsigned short*)d_in[7];
  const unsigned short* wv = (const unsigned short*)d_in[8];
  unsigned short* out = (unsigned short*)d_out;
  const int N = in_sizes[0] / 64;
  const int E = in_sizes[2];
  const int chunk = (((E + NCH - 1) / NCH) + 3) & ~3;
  const int nbk = (N + 195) / 196;

  char* ws = (char*)d_ws;
  size_t off = 0;
  unsigned short* wpack = (unsigned short*)(ws + off);   off += 64 * 1024;
  u32x4* ENT = (u32x4*)(ws + off);                       off += (size_t)NBK * NCH * CELL;
  unsigned* TS = (unsigned*)(ws + off);

  k_part<<<NCH + NFRAG, 1024, 0, stream>>>(src, dst, E, chunk, ENT, TS, wa, wb, wc, wu, wv, wpack);
  k_main<<<nbk, 512, 0, stream>>>(x, ENT, eattr, TS, chunk, (const bf16x8*)wpack, out, N
     );
}
```

```cpp
#include <hip/hip_runtime.h>
#include <hip/hip_bf16.h>
#include <stdint.h>

using bf16x8 = __attribute__((ext_vector_type(8))) short;
using s16x4 = __attribute__((ext_vector_type(4))) short;
using f32x16 = __attribute__((ext_vector_type(16))) float;
using u32x4 = __attribute__((ext_vector_type(4))) unsigned;
using u32x2 = __attribute__((ext_vector_type(2))) unsigned;
using i32x4 = __attribute__((ext_vector_type(4))) int;
using u16x2 = __attribute__((ext_vector_type(2))) unsigned short;

#define MFMA32(a, b, c) __builtin_amdgcn_mfma_f32_32x32x16_bf16((a), (b), (c), 0, 0, 0)
#define LAS __attribute__((address_space(3)))
#define SB() __builtin_amdgcn_sched_barrier(0)

constexpr int NCH = 256;
constexpr int NBK = 256;
constexpr int NBIN = NBK * 8;
constexpr int NFRAG = 39;
constexpr int CHMAX = 3200;
constexpr float NLOG2E = -1.44269504088896340736f;
constexpr unsigned TPITCH = 80;

__device__ __forceinline__ unsigned pack_bf16(float lo, float hi) {
  typedef __attribute__((ext_vector_type(2))) float f2;
  typedef __attribute__((ext_vector_type(2))) __bf16 b2;
  f2 v = {lo, hi};
  b2 r = __builtin_convertvector(v, b2);
  return __builtin_bit_cast(unsigned, r);
}

__device__ __forceinline__ int wave_scan(int v) {
  v += __builtin_amdgcn_update_dpp(0, v, 0x111, 0xf, 0xf, false);
  v += __builtin_amdgcn_update_dpp(0, v, 0x112, 0xf, 0xf, false);
  v += __builtin_amdgcn_update_dpp(0, v, 0x114, 0xf, 0xf, false);
  v += __builtin_amdgcn_update_dpp(0, v, 0x118, 0xf, 0xf, false);
  v += __builtin_amdgcn_update_dpp(0, v, 0x142, 0xa, 0xf, false);
  v += __builtin_amdgcn_update_dpp(0, v, 0x143, 0xc, 0xf, false);
  return v;
}

__device__ __forceinline__ void glds16(const void* sbase, unsigned voff, unsigned lds_dst) {
  asm volatile(
      "s_mov_b32 m0, %2\n\t"
      "s_nop 0\n\t"
      "global_load_lds_dwordx4 %0, %1"
      :: "v"(voff), "s"(sbase), "s"(lds_dst) : "memory", "m0");
}
__device__ __forceinline__ void glds16nt(const void* sbase, unsigned voff, unsigned lds_dst) {
  asm volatile(
      "s_mov_b32 m0, %2\n\t"
      "s_nop 0\n\t"
      "global_load_lds_dwordx4 %0, %1 nt"
      :: "v"(voff), "s"(sbase), "s"(lds_dst) : "memory", "m0");
}
__device__ __forceinline__ void glds16x(const void* sbase, unsigned a, unsigned c, unsigned lds_dst) {
  unsigned tmp;
  asm volatile(
      "s_mov_b32 m0, %4\n\t"
      "v_mad_u32_u16 %0, %1, %5, %2\n\t"
      "global_load_lds_dwordx4 %0, %3"
      : "=&v"(tmp) : "v"(a), "v"(c), "s"(sbase), "s"(lds_dst), "s"(128u) : "memory", "m0");
}
__device__ __forceinline__ void glds16e(const void* sbase, unsigned a, unsigned c, unsigned lds_dst) {
  unsigned tmp;
  asm volatile(
      "s_mov_b32 m0, %4\n\t"
      "v_add_u32 %0, %1, %2\n\t"
      "global_load_lds_dwordx4 %0, %3"
      : "=&v"(tmp) : "v"(a), "v"(c), "s"(sbase), "s"(lds_dst) : "memory", "m0");
}
__device__ __forceinline__ int gload32(const void* sbase, unsigned voff) {
  int r;
  asm volatile("global_load_dword %0, %1, %2" : "=v"(r) : "v"(voff), "s"(sbase) : "memory");
  return r;
}
__device__ __forceinline__ u32x4 gload128(const void* sbase, unsigned voff) {
  u32x4 r;
  asm volatile("global_load_dwordx4 %0, %1, %2" : "=v"(r) : "v"(voff), "s"(sbase) : "memory");
  return r;
}

__device__ __forceinline__ void gstore128wt(void* sbase, unsigned voff, u32x4 v) {
  asm volatile("global_store_dwordx4 %0, %1, %2 sc1\n\ts_nop 1" :: "v"(voff), "v"(v), "s"(sbase) : "memory");
}

constexpr int CELL = 128;
constexpr int CIN = 30;
constexpr int CCAP = 62;
constexpr int CPIT = 272;
constexpr unsigned E2OFF = NBK * NCH * CELL;
constexpr int KP_ENT = 0;
constexpr int KP_HIST = KP_ENT + NBK * CPIT;
constexpr int KP_OVC = KP_HIST + NBIN * 4;
constexpr int KP_LDS = KP_OVC + 16;
constexpr int NWL = 26;
constexpr int NBLOB = 31;

__device__ __forceinline__ unsigned mad24(unsigned a, int b, unsigned c) {
  unsigned r;
  asm("v_mad_i32_i24 %0, %1, %2, %3" : "=v"(r) : "v"(a), "s"(b), "v"(c));
  return r;
}
__device__ __forceinline__ unsigned dot4(unsigned a, unsigned b, unsigned c) {
  return __builtin_amdgcn_udot4(a, b, c, false);
}

__global__ __launch_bounds__(1024) void k_part(const int* __restrict__ src, const int* __restrict__ dst,
                                               int E, int chunk, u32x4* __restrict__ ENT,
                                               unsigned* __restrict__ TS,
                                               const unsigned short* __restrict__ wa,
                                               const unsigned short* __restrict__ wb,
                                               const unsigned short* __restrict__ wc,
                                               const unsigned short* __restrict__ wu,
                                               const unsigned short* __restrict__ wv,
                                               unsigned short* __restrict__ wpack) {
  const int tid = threadIdx.x;
#define PT(i)
  PT(0);
  if (blockIdx.x < NCH) {
    __shared__ __attribute__((aligned(16))) char sm[KP_LDS];
    LAS unsigned* hist = (LAS unsigned*)(sm + KP_HIST);
    LAS unsigned* ovc = (LAS unsigned*)(sm + KP_OVC);
    const int c = blockIdx.x;
    const int base = c * chunk;
    const int cnt = max(min(chunk, E - base), 0);
    const int i0 = max(min(base + 4 * tid, min(base + cnt, E) - 4), 0);
    const int lo = base + 4 * tid - i0, hi4 = base + cnt - i0;
    u32x4 d = gload128(dst, (unsigned)i0 * 4u);
    u32x4 sv = gload128(src, (unsigned)i0 * 4u);
    if (tid < NBK) *(LAS u32x2*)(sm + KP_ENT + tid * CPIT) = u32x2{0u, 0u};
    if (tid == 0) *(LAS u32x2*)ovc = u32x2{0u, 0u};
    asm volatile("s_waitcnt lgkmcnt(0)\n\ts_barrier\n\ts_waitcnt vmcnt(1)" : "+v"(d) :: "memory");
    PT(1);
    unsigned ca[4], sh[4], rk[4], tup[4], m0[4], m1[4];
    bool hi[4], on[4];
#pragma unroll
    for (int k = 0; k < 4; ++k) {
      const unsigned g2 = (unsigned)__umul24(d[k], 85599u) >> 22;
      const unsigned rem = mad24(g2, -49, d[k]);
      hi[k] = rem >= 25u;
      on[k] = k >= lo && k < hi4;
      ca[k] = KP_ENT + __umul24(g2 >> 2, CPIT);
      sh[k] = (g2 << 3) & 24u;
      const unsigned oh = 1u << sh[k];
      rk[k] = 0;
      if (on[k])
        rk[k] = __hip_atomic_fetch_add((LAS unsigned*)sm + (ca[k] >> 2) + (hi[k] ? 1 : 0), oh, __ATOMIC_RELAXED,
                                       __HIP_MEMORY_SCOPE_WORKGROUP);
      tup[k] = __umul24(rem, 3200u) + (unsigned)(i0 + k - base);
      const unsigned m = (oh - 1u) & 0x01010101u;
      m0[k] = hi[k] ? 0x01010101u : m;
      m1[k] = hi[k] ? m : 0u;
    }
    asm volatile("s_waitcnt lgkmcnt(0)\n\ts_barrier\n\ts_waitcnt vmcnt(0)"
                 : "+v"(sv), "+v"(rk[0]), "+v"(rk[1]), "+v"(rk[2]), "+v"(rk[3]), "+v"(m0[0]), "+v"(m0[1]), "+v"(m0[2]),
                   "+v"(m0[3]), "+v"(m1[0]), "+v"(m1[1]), "+v"(m1[2]), "+v"(m1[3]), "+v"(tup[0]), "+v"(tup[1]),
                   "+v"(tup[2]), "+v"(tup[3])
                 :: "memory");
    PT(2);
    u32x2 hd[4];
#pragma unroll
    for (int k = 0; k < 4; ++k) hd[k] = *(const LAS u32x2*)(sm + ca[k]);
    bool ovf = false;
#pragma unroll
    for (int k = 0; k < 4; ++k) {
      tup[k] = mad24(sv[k] - (hi[k] ? 1u : 0u), 80000, tup[k]);
      const unsigned pos = dot4(hd[k][0], m0[k], dot4(hd[k][1], m1[k], (rk[k] >> sh[k]) & 0xffu));
      if (on[k] && pos < (unsigned)CCAP) *(LAS unsigned*)(sm + ca[k] + 8 + pos * 4) = tup[k];
      ovf |= on[k] && pos >= (unsigned)CCAP;
    }
    if (ovf) ovc[1] = 1u;
    asm volatile("s_waitcnt lgkmcnt(0)\n\ts_barrier" ::: "memory");
    PT(3); PT(4);
    if (__builtin_amdgcn_readfirstlane(ovc[1]) != 0u) {
      *(LAS u32x2*)(hist + tid * 2) = u32x2{0u, 0u};
      __syncthreads();
#pragma unroll
      for (int k = 0; k < 4; ++k) {
        const int bin = (d[k] / 196u) * 8 + (sh[k] >> 3) + (hi[k] ? 4 : 0);
        if (on[k]) rk[k] = __hip_atomic_fetch_add(hist + bin, 1u, __ATOMIC_RELAXED, __HIP_MEMORY_SCOPE_WORKGROUP);
      }
      __syncthreads();
      if (tid < NBK) {
        unsigned tot = 0;
        for (int i = 0; i < 8; ++i) tot += hist[tid * 8 + i];
        LAS unsigned* cp = (LAS unsigned*)(sm + KP_ENT + tid * CPIT);
        unsigned p = ~0u;
        if (tot > (unsigned)CCAP) {
          p = (unsigned)base + __hip_atomic_fetch_add(ovc, tot, __ATOMIC_RELAXED, __HIP_MEMORY_SCOPE_WORKGROUP);
          cp[0] = ~0u;
          cp[1] = ~0u;
        }
        for (int i = 0; i < 8; ++i) {
          const unsigned n = hist[tid * 8 + i];
          hist[tid * 8 + i] = p;
          if (p != ~0u) {
            cp[2 + i] = p;
            cp[10 + i] = n;
            p += n;
          }
        }
      }
      __syncthreads();
#pragma unroll
      for (int k = 0; k < 4; ++k) {
        const unsigned p = hist[(d[k] / 196u) * 8 + (sh[k] >> 3) + (hi[k] ? 4 : 0)];
        if (on[k] && p != ~0u) TS[p + rk[k]] = tup[k];
      }
    }
    {
      const int bk = tid >> 2, p0 = (tid & 3) * 16;
      const LAS char* cp = (const LAS char*)sm + KP_ENT + bk * CPIT;
      const u32x2 hc = *(const LAS u32x2*)cp;
      const u32x4 v0 = *(const LAS u32x4*)(cp + p0);
      const u32x4 v1 = *(const LAS u32x4*)(cp + p0 + 64);
      const int used = 8 + 4 * (int)dot4(hc[0], 0x01010101u, dot4(hc[1], 0x01010101u, 0u));
      const unsigned go = (unsigned)((bk * NCH + c) * CELL + p0);
      gstore128wt(ENT, go, v0);
      if (64 < used) gstore128wt(ENT, go + 64, v1);
      if (128 < used) {
        gstore128wt(ENT, go + E2OFF, *(const LAS u32x4*)(cp + p0 + 128));
        gstore128wt(ENT, go + E2OFF + 64, *(const LAS u32x4*)(cp + p0 + 192));
      }
    }
  } else if (tid < 512) {
    const int f = blockIdx.x - NCH;
    unsigned short v;
    if (f >= NWL && f < NBLOB) {
      const int e = (f - NWL) * 512 + tid;
      const int tb = e >= 32 * (TPITCH / 2);
      const int e2 = e - tb * 32 * (TPITCH / 2);
      const int sl = e2 / (TPITCH / 2), col = e2 - sl * (TPITCH / 2);
      const int nd = tb ? 16 * ((col >> 3) & 1) + 8 * ((col >> 2) & 1) + 4 * (col >> 4) + (col & 3) : col;
      v = (col < 32 && nd == sl) ? 0x3F80 : 0;
    } else {
      const int lane = tid >> 3, j = tid & 7;
      const int n = lane & 31, h = lane >> 5;
      const int g = f < 16 ? f : f < NWL ? f - 18 : f - NBLOB;
      const int ct = g & 1;
      const int col = 32 * ((n >> 2) & 1) + 16 * ct + 4 * (n >> 3) + (n & 3);
      if (f == 16 || f == 17) {
        v = wc[(8 * h + j) * 64 + col];
      } else {
        const int t = (g >> 1) & 3;
        const unsigned short* W = f < 8 ? wb : f < 16 ? wv : f < NWL ? wu : wa;
        v = W[(32 * h + 8 * t + j) * 64 + col];
      }
      if (f < 8 || f == 16 || f == 17 || f >= NBLOB)
        v = (unsigned short)(pack_bf16(__uint_as_float((unsigned)v << 16) * NLOG2E, 0.f) & 0xffffu);
    }
    wpack[f * 512 + tid] = v;
  }
}

constexpr int RING_SLOT = 5 * 1024;
constexpr int SPIT = 1040;
constexpr int LCAP = 512;
constexpr int LARR = 2816;
constexpr int LIST_BYTES = 2 * LARR;
constexpr int OFF_W = 0;
constexpr int OFF_T1 = NWL * 1024;
constexpr int OFF_T2 = OFF_T1 + 32 * TPITCH;
constexpr int OFF_R0 = OFF_T2 + 32 * TPITCH;
constexpr int OFF_R1 = OFF_R0 + 8 * RING_SLOT;
constexpr int OFF_L = OFF_R1 + 8 * RING_SLOT;
constexpr int MAIN_LDS = OFF_L + 8 * LIST_BYTES;
static_assert(MAIN_LDS <= 160 * 1024, "LDS");

__device__ __forceinline__ float add1(float t) { return 1.0f + t; }
__device__ __forceinline__ float mulf(int i, float a, float b) { return (i & 1) ? __builtin_fmaf(a, b, 0.0f) : a * b; }

__global__ __launch_bounds__(512, 2) void k_main(const unsigned short* __restrict__ x,
                                                 const u32x4* __restrict__ ENT,
                                                 const u32x4* __restrict__ ES,
                                                 const unsigned* __restrict__ TS, int chunk,
                                                 const bf16x8* __restrict__ wpack,
                                                 unsigned short* __restrict__ out, int N
                                                 ) {
  __shared__ __attribute__((aligned(16))) char smem[MAIN_LDS];
  const int tid = threadIdx.x, lane = tid & 63;
  const int wave = __builtin_amdgcn_readfirstlane(tid >> 6);
  const int r = lane & 31, h = lane >> 5;
  if (wave >= 4) __builtin_amdgcn_s_setprio(1);
  const int bk = blockIdx.x;
  const int nb0 = bk * 196 + 49 * (wave & 3) + 25 * (wave >> 2);
  const int nodes = max(min(wave < 4 ? 25 : 24, N - nb0), 0);
  const int n0 = min(nb0, N - 1);
  const unsigned lds0 = (unsigned)(size_t)(LAS char*)smem;

  u32x4 xg[4], wa[8];
  {
    const unsigned vo = lane * 16;
    const char* eb = reinterpret_cast<const char*>(ENT) + (size_t)bk * (NCH * CELL);
#pragma unroll
    for (int i = 0; i < 4; ++i) glds16(eb + (wave * 4 + i) * 1024, vo, lds0 + OFF_R1 + (wave * 4 + i) * SPIT);
    const char* wp = reinterpret_cast<const char*>(wpack);
#pragma unroll
    for (int i = 0; i < 4; ++i) {
      const int f = min(wave + 8 * i, NBLOB - 1);
      glds16(wp + f * 1024, vo, lds0 + OFF_W + f * 1024);
    }
    const unsigned xo = (unsigned)(n0 + max(min(r, nodes - 1), 0)) * 128u + 64u * h;
#pragma unroll
    for (int t = 0; t < 4; ++t) xg[t] = gload128(x, xo + 16 * t);
#pragma unroll
    for (int k = 0; k < 8; ++k) wa[k] = gload128(wp + (NBLOB + k) * 1024, vo);
  }
  LAS char* lbase = (LAS char*)smem + OFF_L + wave * LIST_BYTES;
  const unsigned la = (unsigned)(size_t)lbase;
  auto put = [&](int p, unsigned a, unsigned b) {
    *(LAS unsigned*)(lbase + p * 4) = a;
    *(LAS unsigned*)(lbase + LARR + p * 4) = b;
  };
  const unsigned aLX = la + (lane >> 3) * 16;
  const unsigned aLE = la + LARR + ((((lane >> 1) & 7) << 2) | (lane >> 4)) * 4;
  const unsigned aLS4 = la + (4 * h + ((lane >> 2) & 3)) * 16;
  const unsigned aLS1 = la + (((r & 7) << 2) | (r >> 3)) * 4 + 2;
  const unsigned cx0 = (unsigned)(((lane & 7) ^ (lane >> 4)) * 16);
  const unsigned cx1 = cx0 ^ 64u;
  const unsigned ce = (unsigned)((((lane & 1) ^ ((lane >> 4) & 1))) * 16);
  const unsigned ring0 = lds0 + OFF_R0 + wave * RING_SLOT;
  const unsigned ringd = 8 * RING_SLOT;
  const unsigned padA = (unsigned)(n0 + max(min(((lane >> 2) & 7) + 8 * (lane & 3), nodes - 1), 0)) | (31u * TPITCH << 16);
  asm volatile("s_waitcnt vmcnt(12)\n\ts_barrier" ::: "memory");
  const unsigned mlo = wave >= 4 ? 0x01010101u : 0x01010101u & ((1u << (8 * wave)) - 1u);
  const unsigned mhi = wave <= 4 ? 0u : 0x01010101u & ((1u << (8 * (wave - 4))) - 1u);
  const int ch0 = 8 * r + h;
  unsigned cl[4][8];
  int ni[4], ex[4];
  {
    const unsigned sa = lds0 + OFF_R1 + r * SPIT + h * CELL;
    u32x2 hd[4];
#pragma unroll
    for (int j = 0; j < 4; ++j) hd[j] = *(const LAS u32x2*)(size_t)(sa + 2 * j * CELL);
#pragma unroll
    for (int j = 0; j < 4; ++j) {
      const int n = (int)(((wave >= 4 ? hd[j][1] : hd[j][0]) >> (8 * (wave & 3))) & 0xffu);
      const int st = (int)dot4(hd[j][0], mlo, dot4(hd[j][1], mhi, 0u));
      const unsigned ra = sa + min(st, CIN) * 4;
#pragma unroll
      for (int t = 0; t < 8; ++t) cl[j][t] = *(const LAS unsigned*)(size_t)(ra + 2 * j * CELL + 8 + 4 * t);
      ni[j] = max(min(min(n, 8), CIN - st), 0);
      ex[j] = (n - ni[j]) | ((st + ni[j]) << 8);
    }
  }
  asm volatile("s_waitcnt lgkmcnt(0)\n\ts_barrier"
               : "+v"(cl[0][0]), "+v"(cl[0][1]), "+v"(cl[0][2]), "+v"(cl[0][3]), "+v"(cl[0][4]), "+v"(cl[0][5]),
                 "+v"(cl[0][6]), "+v"(cl[0][7]), "+v"(cl[1][0]), "+v"(cl[1][1]), "+v"(cl[1][2]), "+v"(cl[1][3]),
                 "+v"(cl[1][4]), "+v"(cl[1][5]), "+v"(cl[1][6]), "+v"(cl[1][7])
               :: "memory");
  asm volatile(""
               : "+v"(cl[2][0]), "+v"(cl[2][1]), "+v"(cl[2][2]), "+v"(cl[2][3]), "+v"(cl[2][4]), "+v"(cl[2][5]),
                 "+v"(cl[2][6]), "+v"(cl[2][7]), "+v"(cl[3][0]), "+v"(cl[3][1]), "+v"(cl[3][2]), "+v"(cl[3][3]),
                 "+v"(cl[3][4]), "+v"(cl[3][5]), "+v"(cl[3][6]), "+v"(cl[3][7])
               :: "memory");
  const int s01 = wave_scan(ni[0] | (ni[1] << 16));
  const int s23 = wave_scan(ni[2] | (ni[3] << 16));
  const int e01 = __builtin_amdgcn_readlane(s01, 63), e23 = __builtin_amdgcn_readlane(s23, 63);
  const int tot0 = e01 & 0xffff, tot1 = tot0 + (e01 >> 16), tot2 = tot1 + (e23 & 0xffff);
  int cnt_all = tot2 + (e23 >> 16);
  if (__ballot(((ex[0] | ex[1] | ex[2] | ex[3]) & 0xff) != 0) != 0ull) {
#pragma unroll
    for (int j = 0; j < 4; ++j) {
      unsigned long long om = __ballot((ex[j] & 0xff) != 0);
      while (om) {
        const int l = __builtin_ctzll(om);
        om &= om - 1;
        const int q = __builtin_amdgcn_readlane(ex[j], l);
        const int n = q & 0xff, p = (q >> 8) + lane;
        const int cc = 8 * (l & 31) + (l >> 5) + 2 * j;
        if (n > CCAP) cnt_all = LCAP;
        if (cnt_all + n <= LCAP && lane < n) {
          const unsigned* cp = reinterpret_cast<const unsigned*>(reinterpret_cast<const char*>(ENT) +
                                                                 ((size_t)bk * NCH + cc) * CELL);
          put(cnt_all + lane, p < CIN ? cp[2 + p] : cp[E2OFF / 4 + p - CIN], (unsigned)(cc * chunk));
        }
        cnt_all += n;
      }
    }
    __builtin_amdgcn_s_waitcnt(0x0F70);
  }
  const bool fast = cnt_all <= LCAP;

  auto ld128 = [&](unsigned a) { return *(const LAS u32x4*)(size_t)a; };
  auto issue_gather = [&](int tile, int rs, unsigned mask) {
    const u32x4 sx = ld128(aLX + tile * 128) & mask;
    const unsigned re = *(const LAS unsigned*)(size_t)(aLE + tile * 128) & mask;
    const unsigned dstb = ring0 + rs * ringd;
    glds16x(x, sx[0], cx0, dstb);
    glds16x(x, sx[1], cx1, dstb + 1024);
    glds16x(x, sx[2], cx0, dstb + 2048);
    glds16x(x, sx[3], cx1, dstb + 3072);
    glds16e(ES, re, ce, dstb + 4096);
  };
  auto dense = [&](int k, int cnt) {
    LAS unsigned* pa = (LAS unsigned*)(lbase + (k * 64 + lane) * 4);
    const unsigned v = pa[0], cb = pa[LARR / 4];
    const unsigned sv = v / 80000u;
    const unsigned rem = v - __umul24(sv, 80000u);
    const unsigned sl = __umul24(rem >> 7, 41u) >> 10;
    const bool ok = k * 64 + lane < cnt;
    pa[0] = ok ? __umul24(sl, TPITCH << 16) + sv : padA;
    pa[LARR / 4] = ok ? mad24(sl, -102400, (cb + rem) << 5) : 0u;
  };
  const int gbin = bk * 8 + wave;
  auto fill_slow = [&](int wb0) {
    int nn[4], st[4], tot = 0;
    const unsigned* rp[4];
#pragma unroll
    for (int j = 0; j < 4; ++j) {
      const unsigned* cp = reinterpret_cast<const unsigned*>(reinterpret_cast<const char*>(ENT) +
                                                             ((size_t)bk * NCH + ch0 + 2 * j) * CELL);
      const unsigned h0 = cp[0], h1 = cp[1];
      nn[j] = (int)(((wave >= 4 ? h1 : h0) >> (8 * (wave & 3))) & 0xffu);
      st[j] = (int)dot4(h0, mlo, dot4(h1, mhi, 0u));
      rp[j] = cp + 2 + st[j];
      st[j] = CIN - st[j];
      if (nn[j] == 0xff) {
        rp[j] = TS + cp[2 + wave];
        nn[j] = (int)cp[10 + wave];
        st[j] = nn[j];
      }
      tot += nn[j];
    }
    const int sc = wave_scan(tot);
    int pp = sc - tot - wb0;
#pragma unroll
    for (int j = 0; j < 4; ++j) {
      const int cb = (ch0 + 2 * j) * chunk;
      const int te = min(nn[j], LCAP - pp);
#pragma unroll 1
      for (int t = max(0, -pp); t < te; ++t) put(pp + t, rp[j][t < st[j] ? t : t + (int)(E2OFF / 4) - 32], (unsigned)cb);
      pp += nn[j];
    }
    __builtin_amdgcn_s_waitcnt(0x0F70);
    return __builtin_amdgcn_readlane(sc, 63);
  };

  int cnt = min(cnt_all, LCAP);
  auto fill = [&](int j, int p) {
    LAS unsigned* pa = (LAS unsigned*)(lbase + p * 4);
    const unsigned cb = (unsigned)((ch0 + 2 * j) * chunk);
    if (ni[j] > 0) {
      pa[0] = cl[j][0];
      pa[LARR / 4] = cb;
      if (ni[j] > 1) {
        pa[1] = cl[j][1];
        pa[LARR / 4 + 1] = cb;
        if (ni[j] > 2) {
          pa[2] = cl[j][2];
          pa[LARR / 4 + 2] = cb;
          if (ni[j] > 3) {
            pa[3] = cl[j][3];
            pa[LARR / 4 + 3] = cb;
            if (ni[j] > 4) {
              pa[4] = cl[j][4];
              pa[LARR / 4 + 4] = cb;
              if (ni[j] > 5) {
                pa[5] = cl[j][5];
                pa[LARR / 4 + 5] = cb;
                if (ni[j] > 6) {
                  pa[6] = cl[j][6];
                  pa[LARR / 4 + 6] = cb;
                  if (ni[j] > 7) {
                    pa[7] = cl[j][7];
                    pa[LARR / 4 + 7] = cb;
                  }
                }
              }
            }
          }
        }
      }
    }
  };
  const bool early = fast && tot0 >= min(64, cnt_all);
  if (early) {
    fill(0, (s01 & 0xffff) - ni[0]);
    dense(0, cnt);
  }
  issue_gather(0, 0, early ? ~0u : 0u);
  issue_gather(1, 1, early ? ~0u : 0u);
  if (fast) {
    if (!early) fill(0, (s01 & 0xffff) - ni[0]);
    fill(1, tot0 + (s01 >> 16) - ni[1]);
    fill(2, tot1 + (s23 & 0xffff) - ni[2]);
    fill(3, tot2 + (s23 >> 16) - ni[3]);
  } else {
    asm volatile("s_waitcnt vmcnt(0)" ::: "memory");
    cnt_all = fill_slow(0);
  }
  cnt_all = __builtin_amdgcn_readfirstlane(cnt_all);
  cnt = min(cnt_all, LCAP);
  if (!early) dense(0, cnt);
  {
    LAS unsigned* pa = (LAS unsigned*)(lbase + lane * 4);
    unsigned v[9], cb[9];
#pragma unroll
    for (int k = 1; k < 10; ++k) { v[k - 1] = pa[k * 64]; cb[k - 1] = pa[k * 64 + LARR / 4]; }
#pragma unroll
    for (int k = 1; k < 10; ++k) {
      unsigned a = padA, b = 0u;
      if (k < 8 && k * 64 < cnt) {
        const unsigned sv = v[k - 1] / 80000u;
        const unsigned rem = v[k - 1] - __umul24(sv, 80000u);
        const unsigned sl = __umul24(rem >> 7, 41u) >> 10;
        const bool ok = k * 64 + lane < cnt;
        a = ok ? __umul24(sl, TPITCH << 16) + sv : padA;
        b = ok ? mad24(sl, -102400, (cb[k - 1] + rem) << 5) : 0u;
      }
      pa[k * 64] = a;
      pa[k * 64 + LARR / 4] = b;
    }
  }
  asm volatile("s_waitcnt vmcnt(10)"
               : "+v"(xg[0]), "+v"(xg[1]), "+v"(xg[2]), "+v"(xg[3]),
                 "+v"(wa[0]), "+v"(wa[1]), "+v"(wa[2]), "+v"(wa[3]), "+v"(wa[4]), "+v"(wa[5]), "+v"(wa[6]), "+v"(wa[7])
               :: "memory");
  if (!early) {
    asm volatile("s_waitcnt vmcnt(0)" ::: "memory");
    issue_gather(0, 0, ~0u);
    issue_gather(1, 1, ~0u);
  }

  const unsigned cS = lds0 + OFF_T1 + (((lane >> 4) & 1) * 16 + (lane & 3) * 4) * 2;
  const unsigned cSt = lds0 + OFF_T2 + h * 32;
  unsigned fo[4];
#pragma unroll
  for (int t = 0; t < 4; ++t) fo[t] = ring0 + (unsigned)(r * 128 + (((4 * h + t) ^ ((r >> 1) & 7)) * 16));
  const unsigned foe = ring0 + (unsigned)(4096 + r * 32 + ((h ^ ((r >> 3) & 1)) * 16));

  bf16x8 XAf[2][2];
#pragma unroll
  for (int ct = 0; ct < 2; ++ct) {
    f32x16 A;
#pragma unroll
    for (int i = 0; i < 16; ++i) A[i] = 0.f;
#pragma unroll
    for (int k = 0; k < 4; ++k)
      A = MFMA32(__builtin_bit_cast(bf16x8, xg[k]), __builtin_bit_cast(bf16x8, wa[k * 2 + ct]), A);
#pragma unroll
    for (int s = 0; s < 2; ++s) {
      u32x4 p;
#pragma unroll
      for (int j = 0; j < 4; ++j) p[j] = pack_bf16(A[8 * s + 2 * j], A[8 * s + 2 * j + 1]);
      XAf[s][ct] = __builtin_bit_cast(bf16x8, p);
    }
  }

  f32x16 Y[4];
#pragma unroll
  for (int q = 0; q < 4; ++q)
#pragma unroll
    for (int i = 0; i < 16; ++i) Y[q][i] = 0.f;
  const f32x16 zero16 = {0.f, 0.f, 0.f, 0.f, 0.f, 0.f, 0.f, 0.f, 0.f, 0.f, 0.f, 0.f, 0.f, 0.f, 0.f, 0.f};
  const LAS bf16x8* wl = (const LAS bf16x8*)(smem + OFF_W) + lane;

  f32x16 G0, V0, G1, V1;
  bf16x8 S[2], St[2], as[4], ae, b[3];
  u32x4 pd[2], pn[2];
  float tq[16], rq[16], pq[16];

  auto rd128 = [&](unsigned a) { return *(const LAS bf16x8*)(size_t)a; };
  auto load_S = [&](const u32x4 ss) {
    const s16x4 a0 = __builtin_amdgcn_ds_read_tr16_b64_v4i16((LAS s16x4*)(size_t)((ss[0] >> 16) + cS));
    const s16x4 a1 = __builtin_amdgcn_ds_read_tr16_b64_v4i16((LAS s16x4*)(size_t)((ss[1] >> 16) + cS));
    const s16x4 a2 = __builtin_amdgcn_ds_read_tr16_b64_v4i16((LAS s16x4*)(size_t)((ss[2] >> 16) + cS));
    const s16x4 a3 = __builtin_amdgcn_ds_read_tr16_b64_v4i16((LAS s16x4*)(size_t)((ss[3] >> 16) + cS));
    S[0] = __builtin_shufflevector(a0, a1, 0, 1, 2, 3, 4, 5, 6, 7);
    S[1] = __builtin_shufflevector(a2, a3, 0, 1, 2, 3, 4, 5, 6, 7);
  };
  auto widx = [&](int m, int ct) { return m == 6 ? 16 + ct : m < 11 ? (m - 7) * 2 + ct : 8 + (m - 11) * 2 + ct; };

  auto ew = [&](int m, const f32x16& Gc, const f32x16& Vc, const f32x16& Gn, const f32x16& Vp) {
    if (m < 2) {
      const int i = 14 + m;
      pq[i] = mulf(i, Vp[i], rq[i]);
      if (m == 1) pn[1][3] = pack_bf16(pq[14], pq[15]);
    } else if (m == 2) {
      pq[0] = mulf(0, Vc[0], rq[0]);
      pq[1] = mulf(1, Vc[1], rq[1]);
      pn[0][0] = pack_bf16(pq[0], pq[1]);
    } else {
      const int i = m - 1;
      pq[i] = mulf(i, Vc[i], rq[i]);
      if (i & 1) pn[i >> 3][(i >> 1) & 3] = pack_bf16(pq[i - 1], pq[i]);
    }
    if (m == 0) pd[0][0] = pack_bf16(rq[0], rq[1]);
    else if ((m & 1) == 0) { const int i = m + 1; pd[i >> 3][(i >> 1) & 3] = pack_bf16(rq[i - 1], rq[i]); }
    if (m < 14) {
      const int i = m + 2;
      rq[i] = __builtin_amdgcn_rcpf(add1(tq[i]));
    } else {
      rq[0] = __builtin_amdgcn_rcpf(add1(tq[0]));
      rq[1] = __builtin_amdgcn_rcpf(add1(tq[1]));
    }
    if (m < 13) tq[m + 3] = __builtin_amdgcn_exp2f(Gc[m + 3]);
    else if (m == 13) { tq[0] = __builtin_amdgcn_exp2f(Gn[0]); tq[1] = __builtin_amdgcn_exp2f(Gn[1]); }
    else tq[2] = __builtin_amdgcn_exp2f(Gn[2]);
  };

  int wb0 = 0;
  while (true) {
    const int ntiles = (cnt + 31) >> 5;
    asm volatile("s_waitcnt vmcnt(5)" ::: "memory");
    {
      const unsigned sl = *(const LAS unsigned short*)(size_t)aLS1;
#pragma unroll
      for (int k = 0; k < 4; ++k) as[k] = rd128(fo[k]);
      ae = rd128(foe);
      St[0] = rd128(sl + cSt);
      St[1] = rd128(sl + cSt + 16);
      G0 = MFMA32(St[0], XAf[0][0], zero16);
      G0 = MFMA32(St[1], XAf[1][0], G0);
      G0 = MFMA32(ae, wl[widx(6, 0) * 64], G0);
#pragma unroll
      for (int k = 0; k < 4; ++k) G0 = MFMA32(as[k], wl[widx(7 + k, 0) * 64], G0);
      V0 = MFMA32(as[0], wl[widx(11, 0) * 64], zero16);
#pragma unroll
      for (int k = 1; k < 4; ++k) V0 = MFMA32(as[k], wl[widx(11 + k, 0) * 64], V0);
      asm volatile("s_waitcnt lgkmcnt(0)" ::: "memory");
      issue_gather(2, 0, ~0u);
#pragma unroll
      for (int j = 0; j < 8; ++j) { S[0][j] = 0; S[1][j] = 0; }
#pragma unroll
      for (int j = 0; j < 4; ++j) { pd[0][j] = 0; pd[1][j] = 0; pn[0][j] = 0; pn[1][j] = 0; }
#pragma unroll
      for (int i = 0; i < 16; ++i) V1[i] = 0.f;
      rq[14] = 0.f; rq[15] = 0.f;
      tq[0] = __builtin_amdgcn_exp2f(G0[0]);
      tq[1] = __builtin_amdgcn_exp2f(G0[1]);
      tq[2] = __builtin_amdgcn_exp2f(G0[2]);
      rq[0] = __builtin_amdgcn_rcpf(add1(tq[0]));
      rq[1] = __builtin_amdgcn_rcpf(add1(tq[1]));
      SB();
    }
    unsigned lS4 = aLS4, lS1 = aLS1, lX = aLX, lE = aLE;
    auto body = [&](const int par) {
      const unsigned lo = par * 128;
      const unsigned ro = (1 - par) * ringd;
      {
        u32x4 ss;
        unsigned sl;
#pragma unroll
        for (int m = 0; m < 15; ++m) {
          if (m == 0) Y[1] = MFMA32(__builtin_bit_cast(bf16x8, pd[0]), S[0], Y[1]);
          else if (m == 1) Y[3] = MFMA32(__builtin_bit_cast(bf16x8, pn[0]), S[0], Y[3]);
          else if (m == 2) Y[1] = MFMA32(__builtin_bit_cast(bf16x8, pd[1]), S[1], Y[1]);
          else if (m == 3) Y[3] = MFMA32(__builtin_bit_cast(bf16x8, pn[1]), S[1], Y[3]);
          else if (m == 4) G1 = MFMA32(St[0], XAf[0][1], zero16);
          else if (m == 5) G1 = MFMA32(St[1], XAf[1][1], G1);
          else if (m == 6) G1 = MFMA32(ae, b[0], G1);
          else if (m < 11) G1 = MFMA32(as[m - 7], b[(m - 6) % 3], G1);
          else if (m == 11) V1 = MFMA32(as[0], b[(m - 6) % 3], zero16);
          else V1 = MFMA32(as[m - 11], b[(m - 6) % 3], V1);
          SB();
          if (m == 0) { ss = ld128(lS4 + lo); sl = *(const LAS unsigned short*)(size_t)(lS1 + lo + 128); }
          if (m >= 3 && m < 6) b[m - 3] = wl[widx(m + 3, 1) * 64];
          if (m >= 6 && m < 12) b[(m - 6) % 3] = wl[widx(m + 3, 1) * 64];
          if (m == 4) load_S(ss);
          if (m == 5) { St[0] = rd128(sl + cSt); St[1] = rd128(sl + cSt + 16); }
          if (m == 6) { asm volatile("s_waitcnt vmcnt(5)" ::: "memory"); ae = rd128(foe + ro); }
          if (m >= 11) as[m - 11] = rd128(fo[m - 11] + ro);
          ew(m, G0, V0, G1, V1);
          SB();
        }
      }
      {
        u32x4 sx;
        unsigned re;
#pragma unroll
        for (int m = 0; m < 15; ++m) {
          if (m == 0) Y[0] = MFMA32(__builtin_bit_cast(bf16x8, pd[0]), S[0], Y[0]);
          else if (m == 1) Y[2] = MFMA32(__builtin_bit_cast(bf16x8, pn[0]), S[0], Y[2]);
          else if (m == 2) Y[0] = MFMA32(__builtin_bit_cast(bf16x8, pd[1]), S[1], Y[0]);
          else if (m == 3) Y[2] = MFMA32(__builtin_bit_cast(bf16x8, pn[1]), S[1], Y[2]);
          else if (m == 4) G0 = MFMA32(St[0], XAf[0][0], zero16);
          else if (m == 5) G0 = MFMA32(St[1], XAf[1][0], G0);
          else if (m == 6) G0 = MFMA32(ae, b[0], G0);
          else if (m < 11) G0 = MFMA32(as[m - 7], b[(m - 6) % 3], G0);
          else if (m == 11) V0 = MFMA32(as[0], b[(m - 6) % 3], zero16);
          else V0 = MFMA32(as[m - 11], b[(m - 6) % 3], V0);
          SB();
          if (m == 4) { sx = ld128(lX + lo + 3 * 128); re = *(const LAS unsigned*)(size_t)(lE + lo + 3 * 128); }
          if (m >= 3 && m < 6) b[m - 3] = wl[widx(m + 3, 0) * 64];
          if (m >= 6 && m < 12) b[(m - 6) % 3] = wl[widx(m + 3, 0) * 64];
          if (m == 7) glds16x(x, sx[0], cx0, ring0 + ro);
          if (m == 8) glds16x(x, sx[1], cx1, ring0 + ro + 1024);
          if (m == 9) glds16x(x, sx[2], cx0, ring0 + ro + 2048);
          if (m == 10) glds16x(x, sx[3], cx1, ring0 + ro + 3072);
          if (m == 11) glds16e(ES, re, ce, ring0 + ro + 4096);
          ew(m, G1, V1, G0, V0);
          SB();
        }
      }
    };
    for (int t = 0; t < ntiles; t += 2) {
      if (wave >= 4) __builtin_amdgcn_s_setprio(1);
      body(0);
      if (t + 1 >= ntiles) break;
      if (wave >= 4) __builtin_amdgcn_s_setprio(0);
      body(1);
      lS4 += 256; lS1 += 256; lX += 256; lE += 256;
    }
    pq[14] = mulf(0, V1[14], rq[14]);
    pq[15] = mulf(1, V1[15], rq[15]);
    pn[1][3] = pack_bf16(pq[14], pq[15]);
    Y[1] = MFMA32(__builtin_bit_cast(bf16x8, pd[0]), S[0], Y[1]);
    Y[3] = MFMA32(__builtin_bit_cast(bf16x8, pn[0]), S[0], Y[3]);
    Y[1] = MFMA32(__builtin_bit_cast(bf16x8, pd[1]), S[1], Y[1]);
    Y[3] = MFMA32(__builtin_bit_cast(bf16x8, pn[1]), S[1], Y[3]);
    wb0 += LCAP;
    if (wb0 >= cnt_all) break;
    asm volatile("s_waitcnt vmcnt(0)" ::: "memory");
    cnt = min(cnt_all - wb0, LCAP);
    fill_slow(wb0);
#pragma unroll 1
    for (int k = 0; k * 64 < cnt + 128; ++k) dense(k, cnt);
    issue_gather(0, 0, ~0u);
    issue_gather(1, 1, ~0u);
  }

  if (nodes > 0) {
    u32x4* orow = reinterpret_cast<u32x4*>(out + (size_t)(n0 + r) * 64 + 32 * h);
#pragma unroll
    for (int ct = 0; ct < 2; ++ct) {
      f32x16 U = zero16;
#pragma unroll
      for (int k = 0; k < 4; ++k) U = MFMA32(wl[(18 + k * 2 + ct) * 64], as[k], U);
#pragma unroll
      for (int q = 0; q < 2; ++q) {
        const u32x4 xv = __builtin_bit_cast(u32x4, as[2 * ct + q]);
        u32x4 ov;
#pragma unroll
        for (int j = 0; j < 4; ++j) {
          const int i = 8 * q + 2 * j;
          const float z0 = U[i] + Y[2 + ct][i] * __builtin_amdgcn_rcpf(Y[ct][i] + 1e-6f);
          const float z1 = U[i + 1] + Y[2 + ct][i + 1] * __builtin_amdgcn_rcpf(Y[ct][i + 1] + 1e-6f);
          const float o0 = __uint_as_float(xv[j] << 16) + (z0 > 0.f ? z0 : 0.f);
          const float o1 = __uint_as_float(xv[j] & 0xffff0000u) + (z1 > 0.f ? z1 : 0.f);
          ov[j] = pack_bf16(o0, o1);
        }
        if (r < nodes) orow[2 * ct + q] = ov;
      }
    }
  }
  asm volatile("s_waitcnt vmcnt(0)" ::: "memory");
}

extern "C" void kernel_launch(void* const* d_in, const int* in_sizes, int n_in,
                              void* d_out, int out_size, void* d_ws, size_t ws_size,
                              hipStream_t stream) {
  const unsigned short* x = (const unsigned short*)d_in[0];
  const u32x4* eattr = (const u32x4*)d_in[1];
  const int* src = (const int*)d_in[2];
  const int* dst = (const int*)d_in[3];
  const unsigned short* wa = (const unsigned short*)d_in[4];
  const unsigned short* wb = (const unsigned short*)d_in[5];
  const unsigned short* wc = (const unsigned short*)d_in[6];
  const unsigned short* wu = (const unsigned short*)d_in[7];
  const unsigned short* wv = (const unsigned short*)d_in[8];
  unsigned short* out = (unsigned short*)d_out;
  const int N = in_sizes[0] / 64;
  const int E = in_sizes[2];
  const int chunk = (((E + NCH - 1) / NCH) + 3) & ~3;
  const int nbk = (N + 195) / 196;

  char* ws = (char*)d_ws;
  size_t off = 0;
  unsigned short* wpack = (unsigned short*)(ws + off);   off += 64 * 1024;
  u32x4* ENT = (u32x4*)(ws + off);                       off += (size_t)2 * NBK * NCH * CELL;
  unsigned* TS = (unsigned*)(ws + off);

  k_part<<<NCH + NFRAG, 1024, 0, stream>>>(src, dst, E, chunk, ENT, TS, wa, wb, wc, wu, wv, wpack);
  k_main<<<nbk, 512, 0, stream>>>(x, ENT, eattr, TS, chunk, (const bf16x8*)wpack, out, N
     );
}
```

```cpp
#include <hip/hip_runtime.h>
#include <hip/hip_bf16.h>
#include <stdint.h>

using bf16x8 = __attribute__((ext_vector_type(8))) short;
using s16x4 = __attribute__((ext_vector_type(4))) short;
using f32x16 = __attribute__((ext_vector_type(16))) float;
using u32x4 = __attribute__((ext_vector_type(4))) unsigned;
using u32x2 = __attribute__((ext_vector_type(2))) unsigned;
using i32x4 = __attribute__((ext_vector_type(4))) int;
using u16x2 = __attribute__((ext_vector_type(2))) unsigned short;

#define MFMA32(a, b, c) __builtin_amdgcn_mfma_f32_32x32x16_bf16((a), (b), (c), 0, 0, 0)
#define LAS __attribute__((address_space(3)))
#define SB() __builtin_amdgcn_sched_barrier(0)

constexpr int NCH = 256;
constexpr int NBK = 256;
constexpr int NBIN = NBK * 8;
constexpr int NFRAG = 39;
constexpr int CHMAX = 3200;
constexpr float NLOG2E = -1.44269504088896340736f;
constexpr unsigned TPITCH = 80;

__device__ __forceinline__ unsigned pack_bf16(float lo, float hi) {
  typedef __attribute__((ext_vector_type(2))) float f2;
  typedef __attribute__((ext_vector_type(2))) __bf16 b2;
  f2 v = {lo, hi};
  b2 r = __builtin_convertvector(v, b2);
  return __builtin_bit_cast(unsigned, r);
}

__device__ __forceinline__ int wave_scan(int v) {
  v += __builtin_amdgcn_update_dpp(0, v, 0x111, 0xf, 0xf, false);
  v += __builtin_amdgcn_update_dpp(0, v, 0x112, 0xf, 0xf, false);
  v += __builtin_amdgcn_update_dpp(0, v, 0x114, 0xf, 0xf, false);
  v += __builtin_amdgcn_update_dpp(0, v, 0x118, 0xf, 0xf, false);
  v += __builtin_amdgcn_update_dpp(0, v, 0x142, 0xa, 0xf, false);
  v += __builtin_amdgcn_update_dpp(0, v, 0x143, 0xc, 0xf, false);
  return v;
}

__device__ __forceinline__ void glds16(const void* sbase, unsigned voff, unsigned lds_dst) {
  asm volatile(
      "s_mov_b32 m0, %2\n\t"
      "s_nop 0\n\t"
      "global_load_lds_dwordx4 %0, %1"
      :: "v"(voff), "s"(sbase), "s"(lds_dst) : "memory", "m0");
}
__device__ __forceinline__ void glds16nt(const void* sbase, unsigned voff, unsigned lds_dst) {
  asm volatile(
      "s_mov_b32 m0, %2\n\t"
      "s_nop 0\n\t"
      "global_load_lds_dwordx4 %0, %1 nt"
      :: "v"(voff), "s"(sbase), "s"(lds_dst) : "memory", "m0");
}
__device__ __forceinline__ void glds16x(const void* sbase, unsigned a, unsigned c, unsigned lds_dst) {
  unsigned tmp;
  asm volatile(
      "s_mov_b32 m0, %4\n\t"
      "v_mad_u32_u16 %0, %1, %5, %2\n\t"
      "global_load_lds_dwordx4 %0, %3"
      : "=&v"(tmp) : "v"(a), "v"(c), "s"(sbase), "s"(lds_dst), "s"(128u) : "memory", "m0");
}
__device__ __forceinline__ void glds16e(const void* sbase, unsigned a, unsigned c, unsigned lds_dst) {
  unsigned tmp;
  asm volatile(
      "s_mov_b32 m0, %4\n\t"
      "v_add_u32 %0, %1, %2\n\t"
      "global_load_lds_dwordx4 %0, %3"
      : "=&v"(tmp) : "v"(a), "v"(c), "s"(sbase), "s"(lds_dst) : "memory", "m0");
}
__device__ __forceinline__ int gload32(const void* sbase, unsigned voff) {
  int r;
  asm volatile("global_load_dword %0, %1, %2" : "=v"(r) : "v"(voff), "s"(sbase) : "memory");
  return r;
}
__device__ __forceinline__ u32x4 gload128(const void* sbase, unsigned voff) {
  u32x4 r;
  asm volatile("global_load_dwordx4 %0, %1, %2" : "=v"(r) : "v"(voff), "s"(sbase) : "memory");
  return r;
}

__device__ __forceinline__ u32x4 gload128nt(const void* sbase, unsigned voff) {
  u32x4 r;
  asm volatile("global_load_dwordx4 %0, %1, %2 nt" : "=v"(r) : "v"(voff), "s"(sbase) : "memory");
  return r;
}
__device__ __forceinline__ void gstore128wt(void* sbase, unsigned voff, u32x4 v) {
  asm volatile("global_store_dwordx4 %0, %1, %2 sc1\n\ts_nop 1" :: "v"(voff), "v"(v), "s"(sbase) : "memory");
}

constexpr int CELL = 128;
constexpr int CIN = 30;
constexpr int CCAP = 62;
constexpr int CPIT = 272;
constexpr unsigned E2OFF = NBK * NCH * CELL;
constexpr int KP_ENT = 0;
constexpr int KP_HIST = KP_ENT + NBK * CPIT;
constexpr int KP_HDR = KP_HIST + NBIN * 4;
constexpr int KP_OVC = KP_HDR + NBK * 8;
constexpr int KP_LDS = KP_OVC + 16;
constexpr int NWL = 26;
constexpr int NBLOB = 31;

__device__ __forceinline__ unsigned mad24(unsigned a, int b, unsigned c) {
  unsigned r;
  asm("v_mad_i32_i24 %0, %1, %2, %3" : "=v"(r) : "v"(a), "s"(b), "v"(c));
  return r;
}
__device__ __forceinline__ unsigned dot4(unsigned a, unsigned b, unsigned c) {
  return __builtin_amdgcn_udot4(a, b, c, false);
}

__global__ __launch_bounds__(1024) void k_part(const int* __restrict__ src, const int* __restrict__ dst,
                                               int E, int chunk, u32x4* __restrict__ ENT,
                                               unsigned* __restrict__ TS,
                                               const unsigned short* __restrict__ wa,
                                               const unsigned short* __restrict__ wb,
                                               const unsigned short* __restrict__ wc,
                                               const unsigned short* __restrict__ wu,
                                               const unsigned short* __restrict__ wv,
                                               unsigned short* __restrict__ wpack) {
  const int tid = threadIdx.x;
#define PT(i)
  PT(0);
  if (blockIdx.x < NCH) {
    __shared__ __attribute__((aligned(16))) char sm[KP_LDS];
    LAS unsigned* hist = (LAS unsigned*)(sm + KP_HIST);
    LAS unsigned* ovc = (LAS unsigned*)(sm + KP_OVC);
    const int c = blockIdx.x;
    const int base = c * chunk;
    const int cnt = max(min(chunk, E - base), 0);
    const int i0 = max(min(base + 4 * tid, min(base + cnt, E) - 4), 0);
    const int lo = base + 4 * tid - i0, hi4 = base + cnt - i0;
    u32x4 d = gload128nt(dst, (unsigned)i0 * 4u);
    u32x4 sv = gload128nt(src, (unsigned)i0 * 4u);
    if (tid < NBK) *(LAS u32x2*)(sm + KP_HDR + tid * 8) = u32x2{0u, 0u};
    if (tid == 0) *(LAS u32x2*)ovc = u32x2{0u, 0u};
    asm volatile("s_waitcnt lgkmcnt(0)\n\ts_barrier\n\ts_waitcnt vmcnt(1)" : "+v"(d) :: "memory");
    PT(1);
    unsigned ca[4], sh[4], rk[4], tup[4], m0[4], m1[4];
    bool hi[4], on[4];
#pragma unroll
    for (int k = 0; k < 4; ++k) {
      const unsigned g2 = (unsigned)__umul24(d[k], 85599u) >> 22;
      const unsigned rem = mad24(g2, -49, d[k]);
      hi[k] = rem >= 25u;
      on[k] = k >= lo && k < hi4;
      ca[k] = g2 >> 2;
      sh[k] = (g2 << 3) & 24u;
      const unsigned oh = 1u << sh[k];
      rk[k] = 0;
      if (on[k])
        rk[k] = __hip_atomic_fetch_add((LAS unsigned*)(sm + KP_HDR) + ca[k] * 2 + (hi[k] ? 1 : 0), oh, __ATOMIC_RELAXED,
                                       __HIP_MEMORY_SCOPE_WORKGROUP);
      tup[k] = __umul24(rem, 3200u) + (unsigned)(i0 + k - base);
      const unsigned m = (oh - 1u) & 0x01010101u;
      m0[k] = hi[k] ? 0x01010101u : m;
      m1[k] = hi[k] ? m : 0u;
    }
    asm volatile("s_waitcnt lgkmcnt(0)\n\ts_barrier\n\ts_waitcnt vmcnt(0)"
                 : "+v"(sv), "+v"(rk[0]), "+v"(rk[1]), "+v"(rk[2]), "+v"(rk[3]), "+v"(m0[0]), "+v"(m0[1]), "+v"(m0[2]),
                   "+v"(m0[3]), "+v"(m1[0]), "+v"(m1[1]), "+v"(m1[2]), "+v"(m1[3]), "+v"(tup[0]), "+v"(tup[1]),
                   "+v"(tup[2]), "+v"(tup[3])
                 :: "memory");
    PT(2);
    u32x2 hd[4];
#pragma unroll
    for (int k = 0; k < 4; ++k) hd[k] = *(const LAS u32x2*)(sm + KP_HDR + ca[k] * 8);
    bool ovf = false;
#pragma unroll
    for (int k = 0; k < 4; ++k) {
      tup[k] = mad24(sv[k] - (hi[k] ? 1u : 0u), 80000, tup[k]);
      const unsigned pos = dot4(hd[k][0], m0[k], dot4(hd[k][1], m1[k], (rk[k] >> sh[k]) & 0xffu));
      if (on[k] && pos < (unsigned)CCAP) *(LAS unsigned*)(sm + KP_ENT + __umul24(ca[k], CPIT) + 8 + pos * 4) = tup[k];
      ovf |= on[k] && pos >= (unsigned)CCAP;
    }
    if (ovf) ovc[1] = 1u;
    asm volatile("s_waitcnt lgkmcnt(0)\n\ts_barrier" ::: "memory");
    PT(3); PT(4);
    if (__builtin_expect(__builtin_amdgcn_readfirstlane(ovc[1]) != 0u, 0)) {
      *(LAS u32x2*)(hist + tid * 2) = u32x2{0u, 0u};
      __syncthreads();
#pragma unroll
      for (int k = 0; k < 4; ++k) {
        const int bin = (d[k] / 196u) * 8 + (sh[k] >> 3) + (hi[k] ? 4 : 0);
        if (on[k]) rk[k] = __hip_atomic_fetch_add(hist + bin, 1u, __ATOMIC_RELAXED, __HIP_MEMORY_SCOPE_WORKGROUP);
      }
      __syncthreads();
      if (tid < NBK) {
        unsigned tot = 0;
        for (int i = 0; i < 8; ++i) tot += hist[tid * 8 + i];
        LAS unsigned* cp = (LAS unsigned*)(sm + KP_ENT + tid * CPIT);
        unsigned p = ~0u;
        if (tot > (unsigned)CCAP) {
          p = (unsigned)base + __hip_atomic_fetch_add(ovc, tot, __ATOMIC_RELAXED, __HIP_MEMORY_SCOPE_WORKGROUP);
          *(LAS u32x2*)(sm + KP_HDR + tid * 8) = u32x2{~0u, ~0u};
        }
        for (int i = 0; i < 8; ++i) {
          const unsigned n = hist[tid * 8 + i];
          hist[tid * 8 + i] = p;
          if (p != ~0u) {
            cp[2 + i] = p;
            cp[10 + i] = n;
            p += n;
          }
        }
      }
      __syncthreads();
#pragma unroll
      for (int k = 0; k < 4; ++k) {
        const unsigned p = hist[(d[k] / 196u) * 8 + (sh[k] >> 3) + (hi[k] ? 4 : 0)];
        if (on[k] && p != ~0u) TS[p + rk[k]] = tup[k];
      }
    }
    {
      const int bk = tid >> 2, p0 = (tid & 3) * 16;
      const LAS char* cp = (const LAS char*)sm + KP_ENT + bk * CPIT;
      const u32x2 hc = *(const LAS u32x2*)(sm + KP_HDR + bk * 8);
      u32x4 v0 = *(const LAS u32x4*)(cp + p0);
      if (p0 == 0) { v0[0] = hc[0]; v0[1] = hc[1]; }
      const u32x4 v1 = *(const LAS u32x4*)(cp + p0 + 64);
      const int used = 8 + 4 * (int)dot4(hc[0], 0x01010101u, dot4(hc[1], 0x01010101u, 0u));
      const unsigned go = (unsigned)((bk * NCH + c) * CELL + p0);
      gstore128wt(ENT, go, v0);
      if (64 < used) gstore128wt(ENT, go + 64, v1);
      if (__builtin_expect(128 < used, 0)) {
        gstore128wt(ENT, go + E2OFF, *(const LAS u32x4*)(cp + p0 + 128));
        gstore128wt(ENT, go + E2OFF + 64, *(const LAS u32x4*)(cp + p0 + 192));
      }
    }
  } else if (tid < 512) {
    const int f = blockIdx.x - NCH;
    unsigned short v;
    if (f >= NWL && f < NBLOB) {
      const int e = (f - NWL) * 512 + tid;
      const int tb = e >= 32 * (TPITCH / 2);
      const int e2 = e - tb * 32 * (TPITCH / 2);
      const int sl = e2 / (TPITCH / 2), col = e2 - sl * (TPITCH / 2);
      const int nd = tb ? 16 * ((col >> 3) & 1) + 8 * ((col >> 2) & 1) + 4 * (col >> 4) + (col & 3) : col;
      v = (col < 32 && nd == sl) ? 0x3F80 : 0;
    } else {
      const int lane = tid >> 3, j = tid & 7;
      const int n = lane & 31, h = lane >> 5;
      const int g = f < 16 ? f : f < NWL ? f - 18 : f - NBLOB;
      const int ct = g & 1;
      const int col = 32 * ((n >> 2) & 1) + 16 * ct + 4 * (n >> 3) + (n & 3);
      if (f == 16 || f == 17) {
        v = wc[(8 * h + j) * 64 + col];
      } else {
        const int t = (g >> 1) & 3;
        const unsigned short* W = f < 8 ? wb : f < 16 ? wv : f < NWL ? wu : wa;
        v = W[(32 * h + 8 * t + j) * 64 + col];
      }
      if (f < 8 || f == 16 || f == 17 || f >= NBLOB)
        v = (unsigned short)(pack_bf16(__uint_as_float((unsigned)v << 16) * NLOG2E, 0.f) & 0xffffu);
    }
    wpack[f * 512 + tid] = v;
  }
}

constexpr int RING_SLOT = 5 * 1024;
constexpr int SPIT = 1040;
constexpr int LCAP = 512;
constexpr int LARR = 2816;
constexpr int LIST_BYTES = 2 * LARR;
constexpr int OFF_W = 0;
constexpr int OFF_T1 = NWL * 1024;
constexpr int OFF_T2 = OFF_T1 + 32 * TPITCH;
constexpr int OFF_R0 = OFF_T2 + 32 * TPITCH;
constexpr int OFF_R1 = OFF_R0 + 8 * RING_SLOT;
constexpr int OFF_L = OFF_R1 + 8 * RING_SLOT;
constexpr int MAIN_LDS = OFF_L + 8 * LIST_BYTES;
static_assert(MAIN_LDS <= 160 * 1024, "LDS");

__device__ __forceinline__ float add1(float t) { return 1.0f + t; }
__device__ __forceinline__ float mulf(int i, float a, float b) { return (i & 1) ? __builtin_fmaf(a, b, 0.0f) : a * b; }

__global__ __launch_bounds__(512, 2) void k_main(const unsigned short* __restrict__ x,
                                                 const u32x4* __restrict__ ENT,
                                                 const u32x4* __restrict__ ES,
                                                 const unsigned* __restrict__ TS, int chunk,
                                                 const bf16x8* __restrict__ wpack,
                                                 unsigned short* __restrict__ out, int N
                                                 ) {
  __shared__ __attribute__((aligned(16))) char smem[MAIN_LDS];
  const int tid = threadIdx.x, lane = tid & 63;
  const int wave = __builtin_amdgcn_readfirstlane(tid >> 6);
  const int r = lane & 31, h = lane >> 5;
  if (wave >= 4) __builtin_amdgcn_s_setprio(1);
  const int bk = blockIdx.x;
  const int nb0 = bk * 196 + 49 * (wave & 3) + 25 * (wave >> 2);
  const int nodes = max(min(wave < 4 ? 25 : 24, N - nb0), 0);
  const int n0 = min(nb0, N - 1);
  const unsigned lds0 = (unsigned)(size_t)(LAS char*)smem;

  u32x4 xg[4], wa[8];
  {
    const unsigned vo = lane * 16;
    const char* eb = reinterpret_cast<const char*>(ENT) + (size_t)bk * (NCH * CELL);
#pragma unroll
    for (int i = 0; i < 4; ++i) glds16(eb + (wave * 4 + i) * 1024, vo, lds0 + OFF_R1 + (wave * 4 + i) * SPIT);
    const char* wp = reinterpret_cast<const char*>(wpack);
#pragma unroll
    for (int i = 0; i < 4; ++i) {
      const int f = min(wave + 8 * i, NBLOB - 1);
      glds16(wp + f * 1024, vo, lds0 + OFF_W + f * 1024);
    }
    const unsigned xo = (unsigned)(n0 + max(min(r, nodes - 1), 0)) * 128u + 64u * h;
#pragma unroll
    for (int t = 0; t < 4; ++t) xg[t] = gload128(x, xo + 16 * t);
#pragma unroll
    for (int k = 0; k < 8; ++k) wa[k] = gload128(wp + (NBLOB + k) * 1024, vo);
  }
  LAS char* lbase = (LAS char*)smem + OFF_L + wave * LIST_BYTES;
  const unsigned la = (unsigned)(size_t)lbase;
  auto put = [&](int p, unsigned a, unsigned b) {
    *(LAS unsigned*)(lbase + p * 4) = a;
    *(LAS unsigned*)(lbase + LARR + p * 4) = b;
  };
  const unsigned aLX = la + (lane >> 3) * 16;
  const unsigned aLE = la + LARR + ((((lane >> 1) & 7) << 2) | (lane >> 4)) * 4;
  const unsigned aLS4 = la + (4 * h + ((lane >> 2) & 3)) * 16;
  const unsigned aLS1 = la + (((r & 7) << 2) | (r >> 3)) * 4 + 2;
  const unsigned cx0 = (unsigned)(((lane & 7) ^ (lane >> 4)) * 16);
  const unsigned cx1 = cx0 ^ 64u;
  const unsigned ce = (unsigned)((((lane & 1) ^ ((lane >> 4) & 1))) * 16);
  const unsigned ring0 = lds0 + OFF_R0 + wave * RING_SLOT;
  const unsigned ringd = 8 * RING_SLOT;
  const unsigned padA = (unsigned)(n0 + max(min(((lane >> 2) & 7) + 8 * (lane & 3), nodes - 1), 0)) | (31u * TPITCH << 16);
  asm volatile("s_waitcnt vmcnt(12)\n\ts_barrier" ::: "memory");
  const unsigned mlo = wave >= 4 ? 0x01010101u : 0x01010101u & ((1u << (8 * wave)) - 1u);
  const unsigned mhi = wave <= 4 ? 0u : 0x01010101u & ((1u << (8 * (wave - 4))) - 1u);
  const int ch0 = 8 * r + h;
  u32x2 cl[4][4];
  int ni[4], ex[4];
  {
    const unsigned sa = lds0 + OFF_R1 + r * SPIT + h * CELL;
    u32x2 hd[4];
#pragma unroll
    for (int j = 0; j < 4; ++j) hd[j] = *(const LAS u32x2*)(size_t)(sa + 2 * j * CELL);
    asm volatile("" : "+v"(hd[0]), "+v"(hd[1]), "+v"(hd[2]), "+v"(hd[3]));
#pragma unroll
    for (int j = 0; j < 4; ++j) {
      const int n = (int)(((wave >= 4 ? hd[j][1] : hd[j][0]) >> (8 * (wave & 3))) & 0xffu);
      const int st = (int)dot4(hd[j][0], mlo, dot4(hd[j][1], mhi, 0u));
      const unsigned ra = sa + min(st, CIN) * 4;
      unsigned long long sx;
      asm volatile(
          "ds_read2_b32 %0, %5 offset0:%7 offset1:%7+1\n\t"
          "v_cmp_lt_u32 vcc, 2, %6\n\t"
          "s_and_saveexec_b64 %4, vcc\n\t"
          "ds_read2_b32 %1, %5 offset0:%7+2 offset1:%7+3\n\t"
          "v_cmp_lt_u32 vcc, 4, %6\n\t"
          "s_and_b64 exec, exec, vcc\n\t"
          "ds_read2_b32 %2, %5 offset0:%7+4 offset1:%7+5\n\t"
          "v_cmp_lt_u32 vcc, 6, %6\n\t"
          "s_and_b64 exec, exec, vcc\n\t"
          "ds_read2_b32 %3, %5 offset0:%7+6 offset1:%7+7\n\t"
          "s_mov_b64 exec, %4"
          : "=&v"(cl[j][0]), "=&v"(cl[j][1]), "=&v"(cl[j][2]), "=&v"(cl[j][3]), "=&s"(sx)
          : "v"(ra), "v"(n), "n"(j * 64 + 2)
          : "vcc", "scc", "memory");
      ni[j] = max(min(min(n, 8), CIN - st), 0);
      ex[j] = (n - ni[j]) | ((st + ni[j]) << 8);
    }
  }
  asm volatile("s_waitcnt lgkmcnt(0)\n\ts_barrier"
               : "+v"(cl[0][0]), "+v"(cl[0][1]), "+v"(cl[0][2]), "+v"(cl[0][3]), "+v"(cl[1][0]), "+v"(cl[1][1]),
                 "+v"(cl[1][2]), "+v"(cl[1][3]), "+v"(cl[2][0]), "+v"(cl[2][1]), "+v"(cl[2][2]), "+v"(cl[2][3]),
                 "+v"(cl[3][0]), "+v"(cl[3][1]), "+v"(cl[3][2]), "+v"(cl[3][3])
               :: "memory");
  const int s01 = wave_scan(ni[0] | (ni[1] << 16));
  const int s23 = wave_scan(ni[2] | (ni[3] << 16));
  const int e01 = __builtin_amdgcn_readlane(s01, 63), e23 = __builtin_amdgcn_readlane(s23, 63);
  const int tot0 = e01 & 0xffff, tot1 = tot0 + (e01 >> 16), tot2 = tot1 + (e23 & 0xffff);
  int cnt_all = tot2 + (e23 >> 16);
  if (__builtin_expect(__ballot(((ex[0] | ex[1] | ex[2] | ex[3]) & 0xff) != 0) != 0ull, 0)) {
#pragma unroll
    for (int j = 0; j < 4; ++j) {
      unsigned long long om = __ballot((ex[j] & 0xff) != 0);
      while (om) {
        const int l = __builtin_ctzll(om);
        om &= om - 1;
        const int q = __builtin_amdgcn_readlane(ex[j], l);
        const int n = q & 0xff, p = (q >> 8) + lane;
        const int cc = 8 * (l & 31) + (l >> 5) + 2 * j;
        if (n > CCAP) cnt_all = LCAP;
        if (cnt_all + n <= LCAP && lane < n) {
          const unsigned* cp = reinterpret_cast<const unsigned*>(reinterpret_cast<const char*>(ENT) +
                                                                 ((size_t)bk * NCH + cc) * CELL);
          put(cnt_all + lane, p < CIN ? cp[2 + p] : cp[E2OFF / 4 + p - CIN], (unsigned)(cc * chunk));
        }
        cnt_all += n;
      }
    }
    __builtin_amdgcn_s_waitcnt(0x0F70);
  }
  const bool fast = cnt_all <= LCAP;

  auto ld128 = [&](unsigned a) { return *(const LAS u32x4*)(size_t)a; };
  auto issue_gather = [&](int tile, int rs, unsigned mask) {
    const u32x4 sx = ld128(aLX + tile * 128) & mask;
    const unsigned re = *(const LAS unsigned*)(size_t)(aLE + tile * 128) & mask;
    const unsigned dstb = ring0 + rs * ringd;
    glds16x(x, sx[0], cx0, dstb);
    glds16x(x, sx[1], cx1, dstb + 1024);
    glds16x(x, sx[2], cx0, dstb + 2048);
    glds16x(x, sx[3], cx1, dstb + 3072);
    glds16e(ES, re, ce, dstb + 4096);
  };
  auto dense = [&](int k, int cnt) {
    LAS unsigned* pa = (LAS unsigned*)(lbase + (k * 64 + lane) * 4);
    const unsigned v = pa[0], cb = pa[LARR / 4];
    const unsigned sv = v / 80000u;
    const unsigned rem = v - __umul24(sv, 80000u);
    const unsigned sl = __umul24(rem >> 7, 41u) >> 10;
    const bool ok = k * 64 + lane < cnt;
    pa[0] = ok ? __umul24(sl, TPITCH << 16) + sv : padA;
    pa[LARR / 4] = ok ? mad24(sl, -102400, (cb + rem) << 5) : 0u;
  };
  const int gbin = bk * 8 + wave;
  auto fill_slow = [&](int wb0) {
    int nn[4], st[4], tot = 0;
    const unsigned* rp[4];
#pragma unroll
    for (int j = 0; j < 4; ++j) {
      const unsigned* cp = reinterpret_cast<const unsigned*>(reinterpret_cast<const char*>(ENT) +
                                                             ((size_t)bk * NCH + ch0 + 2 * j) * CELL);
      const unsigned h0 = cp[0], h1 = cp[1];
      nn[j] = (int)(((wave >= 4 ? h1 : h0) >> (8 * (wave & 3))) & 0xffu);
      st[j] = (int)dot4(h0, mlo, dot4(h1, mhi, 0u));
      rp[j] = cp + 2 + st[j];
      st[j] = CIN - st[j];
      if (nn[j] == 0xff) {
        rp[j] = TS + cp[2 + wave];
        nn[j] = (int)cp[10 + wave];
        st[j] = nn[j];
      }
      tot += nn[j];
    }
    const int sc = wave_scan(tot);
    int pp = sc - tot - wb0;
#pragma unroll
    for (int j = 0; j < 4; ++j) {
      const int cb = (ch0 + 2 * j) * chunk;
      const int te = min(nn[j], LCAP - pp);
#pragma unroll 1
      for (int t = max(0, -pp); t < te; ++t) put(pp + t, rp[j][t < st[j] ? t : t + (int)(E2OFF / 4) - 32], (unsigned)cb);
      pp += nn[j];
    }
    __builtin_amdgcn_s_waitcnt(0x0F70);
    return __builtin_amdgcn_readlane(sc, 63);
  };

  int cnt = min(cnt_all, LCAP);
  auto fill = [&](int j, int p) {
    LAS unsigned* pa = (LAS unsigned*)(lbase + p * 4);
    const unsigned cb = (unsigned)((ch0 + 2 * j) * chunk);
    if (ni[j] > 0) {
      pa[0] = cl[j][0][0];
      pa[LARR / 4] = cb;
      if (ni[j] > 1) {
        pa[1] = cl[j][0][1];
        pa[LARR / 4 + 1] = cb;
        if (ni[j] > 2) {
          pa[2] = cl[j][1][0];
          pa[LARR / 4 + 2] = cb;
          if (ni[j] > 3) {
            pa[3] = cl[j][1][1];
            pa[LARR / 4 + 3] = cb;
            if (ni[j] > 4) {
              pa[4] = cl[j][2][0];
              pa[LARR / 4 + 4] = cb;
              if (ni[j] > 5) {
                pa[5] = cl[j][2][1];
                pa[LARR / 4 + 5] = cb;
                if (ni[j] > 6) {
                  pa[6] = cl[j][3][0];
                  pa[LARR / 4 + 6] = cb;
                  if (ni[j] > 7) {
                    pa[7] = cl[j][3][1];
                    pa[LARR / 4 + 7] = cb;
                  }
                }
              }
            }
          }
        }
      }
    }
  };
  const bool early = fast && tot0 >= min(64, cnt_all);
  if (__builtin_expect(early, 1)) {
    fill(0, (s01 & 0xffff) - ni[0]);
    dense(0, cnt);
  }
  issue_gather(0, 0, early ? ~0u : 0u);
  if (__builtin_expect(fast, 1)) {
    if (__builtin_expect(!early, 0)) fill(0, (s01 & 0xffff) - ni[0]);
    fill(1, tot0 + (s01 >> 16) - ni[1]);
    fill(2, tot1 + (s23 & 0xffff) - ni[2]);
    fill(3, tot2 + (s23 >> 16) - ni[3]);
  }
  if (__builtin_expect(!fast, 0)) {
    asm volatile("s_waitcnt vmcnt(0)" ::: "memory");
    cnt_all = fill_slow(0);
  }
  cnt_all = __builtin_amdgcn_readfirstlane(cnt_all);
  cnt = min(cnt_all, LCAP);
  if (__builtin_expect(!early, 0)) dense(0, cnt);
  {
    LAS unsigned* pa = (LAS unsigned*)(lbase + lane * 4);
    unsigned v[9], cb[9];
#pragma unroll
    for (int k = 1; k < 10; ++k) { v[k - 1] = pa[k * 64]; cb[k - 1] = pa[k * 64 + LARR / 4]; }
#pragma unroll
    for (int k = 1; k < 10; ++k) {
      unsigned a = padA, b = 0u;
      if (k < 8 && k * 64 < cnt) {
        const unsigned sv = v[k - 1] / 80000u;
        const unsigned rem = v[k - 1] - __umul24(sv, 80000u);
        const unsigned sl = __umul24(rem >> 7, 41u) >> 10;
        const bool ok = k * 64 + lane < cnt;
        a = ok ? __umul24(sl, TPITCH << 16) + sv : padA;
        b = ok ? mad24(sl, -102400, (cb[k - 1] + rem) << 5) : 0u;
      }
      pa[k * 64] = a;
      pa[k * 64 + LARR / 4] = b;
    }
  }
  issue_gather(1, 1, ~0u);
  asm volatile("s_waitcnt vmcnt(10)"
               : "+v"(xg[0]), "+v"(xg[1]), "+v"(xg[2]), "+v"(xg[3]),
                 "+v"(wa[0]), "+v"(wa[1]), "+v"(wa[2]), "+v"(wa[3]), "+v"(wa[4]), "+v"(wa[5]), "+v"(wa[6]), "+v"(wa[7])
               :: "memory");
  if (__builtin_expect(!early, 0)) {
    asm volatile("s_waitcnt vmcnt(0)" ::: "memory");
    issue_gather(0, 0, ~0u);
    issue_gather(1, 1, ~0u);
  }

  const unsigned cS = lds0 + OFF_T1 + (((lane >> 4) & 1) * 16 + (lane & 3) * 4) * 2;
  const unsigned cSt = lds0 + OFF_T2 + h * 32;
  unsigned fo[4];
#pragma unroll
  for (int t = 0; t < 4; ++t) fo[t] = ring0 + (unsigned)(r * 128 + (((4 * h + t) ^ ((r >> 1) & 7)) * 16));
  const unsigned foe = ring0 + (unsigned)(4096 + r * 32 + ((h ^ ((r >> 3) & 1)) * 16));

  bf16x8 XAf[2][2];
#pragma unroll
  for (int ct = 0; ct < 2; ++ct) {
    f32x16 A;
#pragma unroll
    for (int i = 0; i < 16; ++i) A[i] = 0.f;
#pragma unroll
    for (int k = 0; k < 4; ++k)
      A = MFMA32(__builtin_bit_cast(bf16x8, xg[k]), __builtin_bit_cast(bf16x8, wa[k * 2 + ct]), A);
#pragma unroll
    for (int s = 0; s < 2; ++s) {
      u32x4 p;
#pragma unroll
      for (int j = 0; j < 4; ++j) p[j] = pack_bf16(A[8 * s + 2 * j], A[8 * s + 2 * j + 1]);
      XAf[s][ct] = __builtin_bit_cast(bf16x8, p);
    }
  }

  f32x16 Y[4];
#pragma unroll
  for (int q = 0; q < 4; ++q)
#pragma unroll
    for (int i = 0; i < 16; ++i) Y[q][i] = 0.f;
  const f32x16 zero16 = {0.f, 0.f, 0.f, 0.f, 0.f, 0.f, 0.f, 0.f, 0.f, 0.f, 0.f, 0.f, 0.f, 0.f, 0.f, 0.f};
  const LAS bf16x8* wl = (const LAS bf16x8*)(smem + OFF_W) + lane;

  f32x16 G0, V0, G1, V1;
  bf16x8 S[2], St[2], as[4], ae, b[3];
  u32x4 pd[2], pn[2];
  float tq[16], rq[16], pq[16];

  auto rd128 = [&](unsigned a) { return *(const LAS bf16x8*)(size_t)a; };
  auto load_S = [&](const u32x4 ss) {
    const s16x4 a0 = __builtin_amdgcn_ds_read_tr16_b64_v4i16((LAS s16x4*)(size_t)((ss[0] >> 16) + cS));
    const s16x4 a1 = __builtin_amdgcn_ds_read_tr16_b64_v4i16((LAS s16x4*)(size_t)((ss[1] >> 16) + cS));
    const s16x4 a2 = __builtin_amdgcn_ds_read_tr16_b64_v4i16((LAS s16x4*)(size_t)((ss[2] >> 16) + cS));
    const s16x4 a3 = __builtin_amdgcn_ds_read_tr16_b64_v4i16((LAS s16x4*)(size_t)((ss[3] >> 16) + cS));
    S[0] = __builtin_shufflevector(a0, a1, 0, 1, 2, 3, 4, 5, 6, 7);
    S[1] = __builtin_shufflevector(a2, a3, 0, 1, 2, 3, 4, 5, 6, 7);
  };
  auto widx = [&](int m, int ct) { return m == 6 ? 16 + ct : m < 11 ? (m - 7) * 2 + ct : 8 + (m - 11) * 2 + ct; };

  auto ew = [&](int m, const f32x16& Gc, const f32x16& Vc, const f32x16& Gn, const f32x16& Vp) {
    if (m < 2) {
      const int i = 14 + m;
      pq[i] = mulf(i, Vp[i], rq[i]);
      if (m == 1) pn[1][3] = pack_bf16(pq[14], pq[15]);
    } else if (m == 2) {
      pq[0] = mulf(0, Vc[0], rq[0]);
      pq[1] = mulf(1, Vc[1], rq[1]);
      pn[0][0] = pack_bf16(pq[0], pq[1]);
    } else {
      const int i = m - 1;
      pq[i] = mulf(i, Vc[i], rq[i]);
      if (i & 1) pn[i >> 3][(i >> 1) & 3] = pack_bf16(pq[i - 1], pq[i]);
    }
    if (m == 0) pd[0][0] = pack_bf16(rq[0], rq[1]);
    else if ((m & 1) == 0) { const int i = m + 1; pd[i >> 3][(i >> 1) & 3] = pack_bf16(rq[i - 1], rq[i]); }
    if (m < 14) {
      const int i = m + 2;
      rq[i] = __builtin_amdgcn_rcpf(add1(tq[i]));
    } else {
      rq[0] = __builtin_amdgcn_rcpf(add1(tq[0]));
      rq[1] = __builtin_amdgcn_rcpf(add1(tq[1]));
    }
    if (m < 13) tq[m + 3] = __builtin_amdgcn_exp2f(Gc[m + 3]);
    else if (m == 13) { tq[0] = __builtin_amdgcn_exp2f(Gn[0]); tq[1] = __builtin_amdgcn_exp2f(Gn[1]); }
    else tq[2] = __builtin_amdgcn_exp2f(Gn[2]);
  };

  int wb0 = 0;
  while (true) {
    const int ntiles = (cnt + 31) >> 5;
    asm volatile("s_waitcnt vmcnt(5)" ::: "memory");
    {
      const unsigned sl = *(const LAS unsigned short*)(size_t)aLS1;
#pragma unroll
      for (int k = 0; k < 4; ++k) as[k] = rd128(fo[k]);
      ae = rd128(foe);
      St[0] = rd128(sl + cSt);
      St[1] = rd128(sl + cSt + 16);
      G0 = MFMA32(St[0], XAf[0][0], zero16);
      G0 = MFMA32(St[1], XAf[1][0], G0);
      G0 = MFMA32(ae, wl[widx(6, 0) * 64], G0);
#pragma unroll
      for (int k = 0; k < 4; ++k) G0 = MFMA32(as[k], wl[widx(7 + k, 0) * 64], G0);
      V0 = MFMA32(as[0], wl[widx(11, 0) * 64], zero16);
#pragma unroll
      for (int k = 1; k < 4; ++k) V0 = MFMA32(as[k], wl[widx(11 + k, 0) * 64], V0);
      asm volatile("s_waitcnt lgkmcnt(0)" ::: "memory");
      issue_gather(2, 0, ~0u);
#pragma unroll
      for (int j = 0; j < 8; ++j) { S[0][j] = 0; S[1][j] = 0; }
#pragma unroll
      for (int j = 0; j < 4; ++j) { pd[0][j] = 0; pd[1][j] = 0; pn[0][j] = 0; pn[1][j] = 0; }
#pragma unroll
      for (int i = 0; i < 16; ++i) V1[i] = 0.f;
      rq[14] = 0.f; rq[15] = 0.f;
      tq[0] = __builtin_amdgcn_exp2f(G0[0]);
      tq[1] = __builtin_amdgcn_exp2f(G0[1]);
      tq[2] = __builtin_amdgcn_exp2f(G0[2]);
      rq[0] = __builtin_amdgcn_rcpf(add1(tq[0]));
      rq[1] = __builtin_amdgcn_rcpf(add1(tq[1]));
      SB();
    }
    unsigned lS4 = aLS4, lS1 = aLS1, lX = aLX, lE = aLE;
    auto body = [&](const int par) {
      const unsigned lo = par * 128;
      const unsigned ro = (1 - par) * ringd;
      {
        u32x4 ss;
        unsigned sl;
#pragma unroll
        for (int m = 0; m < 15; ++m) {
          if (m == 0) Y[1] = MFMA32(__builtin_bit_cast(bf16x8, pd[0]), S[0], Y[1]);
          else if (m == 1) Y[3] = MFMA32(__builtin_bit_cast(bf16x8, pn[0]), S[0], Y[3]);
          else if (m == 2) Y[1] = MFMA32(__builtin_bit_cast(bf16x8, pd[1]), S[1], Y[1]);
          else if (m == 3) Y[3] = MFMA32(__builtin_bit_cast(bf16x8, pn[1]), S[1], Y[3]);
          else if (m == 4) G1 = MFMA32(St[0], XAf[0][1], zero16);
          else if (m == 5) G1 = MFMA32(St[1], XAf[1][1], G1);
          else if (m == 6) G1 = MFMA32(ae, b[0], G1);
          else if (m < 11) G1 = MFMA32(as[m - 7], b[(m - 6) % 3], G1);
          else if (m == 11) V1 = MFMA32(as[0], b[(m - 6) % 3], zero16);
          else V1 = MFMA32(as[m - 11], b[(m - 6) % 3], V1);
          SB();
          if (m == 0) { ss = ld128(lS4 + lo); sl = *(const LAS unsigned short*)(size_t)(lS1 + lo + 128); }
          if (m >= 3 && m < 6) b[m - 3] = wl[widx(m + 3, 1) * 64];
          if (m >= 6 && m < 12) b[(m - 6) % 3] = wl[widx(m + 3, 1) * 64];
          if (m == 4) load_S(ss);
          if (m == 5) { St[0] = rd128(sl + cSt); St[1] = rd128(sl + cSt + 16); }
          if (m == 6) { asm volatile("s_waitcnt vmcnt(5)" ::: "memory"); ae = rd128(foe + ro); }
          if (m >= 11) as[m - 11] = rd128(fo[m - 11] + ro);
          ew(m, G0, V0, G1, V1);
          SB();
        }
      }
      {
        u32x4 sx;
        unsigned re;
#pragma unroll
        for (int m = 0; m < 15; ++m) {
          if (m == 0) Y[0] = MFMA32(__builtin_bit_cast(bf16x8, pd[0]), S[0], Y[0]);
          else if (m == 1) Y[2] = MFMA32(__builtin_bit_cast(bf16x8, pn[0]), S[0], Y[2]);
          else if (m == 2) Y[0] = MFMA32(__builtin_bit_cast(bf16x8, pd[1]), S[1], Y[0]);
          else if (m == 3) Y[2] = MFMA32(__builtin_bit_cast(bf16x8, pn[1]), S[1], Y[2]);
          else if (m == 4) G0 = MFMA32(St[0], XAf[0][0], zero16);
          else if (m == 5) G0 = MFMA32(St[1], XAf[1][0], G0);
          else if (m == 6) G0 = MFMA32(ae, b[0], G0);
          else if (m < 11) G0 = MFMA32(as[m - 7], b[(m - 6) % 3], G0);
          else if (m == 11) V0 = MFMA32(as[0], b[(m - 6) % 3], zero16);
          else V0 = MFMA32(as[m - 11], b[(m - 6) % 3], V0);
          SB();
          if (m == 4) { sx = ld128(lX + lo + 3 * 128); re = *(const LAS unsigned*)(size_t)(lE + lo + 3 * 128); }
          if (m >= 3 && m < 6) b[m - 3] = wl[widx(m + 3, 0) * 64];
          if (m >= 6 && m < 12) b[(m - 6) % 3] = wl[widx(m + 3, 0) * 64];
          if (m == 7) glds16x(x, sx[0], cx0, ring0 + ro);
          if (m == 8) glds16x(x, sx[1], cx1, ring0 + ro + 1024);
          if (m == 9) glds16x(x, sx[2], cx0, ring0 + ro + 2048);
          if (m == 10) glds16x(x, sx[3], cx1, ring0 + ro + 3072);
          if (m == 11) glds16e(ES, re, ce, ring0 + ro + 4096);
          ew(m, G1, V1, G0, V0);
          SB();
        }
      }
    };
    for (int t = 0; t < ntiles; t += 2) {
      if (wave >= 4) __builtin_amdgcn_s_setprio(1);
      body(0);
      if (t + 1 >= ntiles) break;
      if (wave >= 4) __builtin_amdgcn_s_setprio(0);
      body(1);
      lS4 += 256; lS1 += 256; lX += 256; lE += 256;
    }
    pq[14] = mulf(0, V1[14], rq[14]);
    pq[15] = mulf(1, V1[15], rq[15]);
    pn[1][3] = pack_bf16(pq[14], pq[15]);
    Y[1] = MFMA32(__builtin_bit_cast(bf16x8, pd[0]), S[0], Y[1]);
    Y[3] = MFMA32(__builtin_bit_cast(bf16x8, pn[0]), S[0], Y[3]);
    Y[1] = MFMA32(__builtin_bit_cast(bf16x8, pd[1]), S[1], Y[1]);
    Y[3] = MFMA32(__builtin_bit_cast(bf16x8, pn[1]), S[1], Y[3]);
    wb0 += LCAP;
    if (__builtin_expect(wb0 >= cnt_all, 1)) break;
    asm volatile("s_waitcnt vmcnt(0)" ::: "memory");
    cnt = min(cnt_all - wb0, LCAP);
    fill_slow(wb0);
#pragma unroll 1
    for (int k = 0; k * 64 < cnt + 128; ++k) dense(k, cnt);
    issue_gather(0, 0, ~0u);
    issue_gather(1, 1, ~0u);
  }

  if (nodes > 0) {
#pragma unroll
    for (int ct = 0; ct < 2; ++ct) {
      f32x16 U = zero16;
#pragma unroll
      for (int k = 0; k < 4; ++k) U = MFMA32(wl[(18 + k * 2 + ct) * 64], as[k], U);
#pragma unroll
      for (int q = 0; q < 2; ++q) {
        const u32x4 xv = __builtin_bit_cast(u32x4, as[2 * ct + q]);
        u32x4 ov;
#pragma unroll
        for (int j = 0; j < 4; ++j) {
          const int i = 8 * q + 2 * j;
          const float z0 = U[i] + Y[2 + ct][i] * __builtin_amdgcn_rcpf(Y[ct][i] + 1e-6f);
          const float z1 = U[i + 1] + Y[2 + ct][i + 1] * __builtin_amdgcn_rcpf(Y[ct][i + 1] + 1e-6f);
          const float o0 = __uint_as_float(xv[j] << 16) + (z0 > 0.f ? z0 : 0.f);
          const float o1 = __uint_as_float(xv[j] & 0xffff0000u) + (z1 > 0.f ? z1 : 0.f);
          ov[j] = pack_bf16(o0, o1);
        }
        *(LAS u32x4*)(size_t)(la + r * 128 + (((4 * h + 2 * ct + q) ^ (r & 7)) * 16)) = ov;
      }
    }
#pragma unroll
    for (int q = 0; q < 4; ++q) {
      const int row = 8 * q + (lane >> 3);
      const u32x4 ov = *(const LAS u32x4*)(size_t)(la + row * 128 + (((lane & 7) ^ (row & 7)) * 16));
      if (row < nodes) gstore128wt(out, (unsigned)n0 * 128u + q * 1024u + lane * 16u, ov);
    }
  }
  asm volatile("s_waitcnt vmcnt(0)" ::: "memory");
}

extern "C" void kernel_launch(void* const* d_in, const int* in_sizes, int n_in,
                              void* d_out, int out_size, void* d_ws, size_t ws_size,
                              hipStream_t stream) {
  const unsigned short* x = (const unsigned short*)d_in[0];
  const u32x4* eattr = (const u32x4*)d_in[1];
  const int* src = (const int*)d_in[2];
  const int* dst = (const int*)d_in[3];
  const unsigned short* wa = (const unsigned short*)d_in[4];
  const unsigned short* wb = (const unsigned short*)d_in[5];
  const unsigned short* wc = (const unsigned short*)d_in[6];
  const unsigned short* wu = (const unsigned short*)d_in[7];
  const unsigned short* wv = (const unsigned short*)d_in[8];
  unsigned short* out = (unsigned short*)d_out;
  const int N = in_sizes[0] / 64;
  const int E = in_sizes[2];
  const int chunk = (((E + NCH - 1) / NCH) + 3) & ~3;
  const int nbk = (N + 195) / 196;

  char* ws = (char*)d_ws;
  size_t off = 0;
  unsigned short* wpack = (unsigned short*)(ws + off);   off += 64 * 1024;
  u32x4* ENT = (u32x4*)(ws + off);                       off += (size_t)2 * NBK * NCH * CELL;
  unsigned* TS = (unsigned*)(ws + off);

  k_part<<<NCH + NFRAG, 1024, 0, stream>>>(src, dst, E, chunk, ENT, TS, wa, wb, wc, wu, wv, wpack);
  k_main<<<nbk, 512, 0, stream>>>(x, ENT, eattr, TS, chunk, (const bf16x8*)wpack, out, N
     );
}
```

```cpp
#include <hip/hip_runtime.h>
#include <hip/hip_bf16.h>
#include <stdint.h>

using bf16x8 = __attribute__((ext_vector_type(8))) short;
using s16x4 = __attribute__((ext_vector_type(4))) short;
using f32x16 = __attribute__((ext_vector_type(16))) float;
using u32x4 = __attribute__((ext_vector_type(4))) unsigned;
using u32x2 = __attribute__((ext_vector_type(2))) unsigned;
using i32x4 = __attribute__((ext_vector_type(4))) int;
using u16x2 = __attribute__((ext_vector_type(2))) unsigned short;

#define MFMA32(a, b, c) __builtin_amdgcn_mfma_f32_32x32x16_bf16((a), (b), (c), 0, 0, 0)
#define LAS __attribute__((address_space(3)))
#define SB() __builtin_amdgcn_sched_barrier(0)

constexpr int NCH = 256;
constexpr int NBK = 256;
constexpr int NBIN = NBK * 8;
constexpr int NFRAG = 39;
constexpr int CHMAX = 3200;
constexpr float NLOG2E = -1.44269504088896340736f;
constexpr unsigned TPITCH = 80;

__device__ __forceinline__ unsigned pack_bf16(float lo, float hi) {
  typedef __attribute__((ext_vector_type(2))) float f2;
  typedef __attribute__((ext_vector_type(2))) __bf16 b2;
  f2 v = {lo, hi};
  b2 r = __builtin_convertvector(v, b2);
  return __builtin_bit_cast(unsigned, r);
}

__device__ __forceinline__ int wave_scan(int v) {
  v += __builtin_amdgcn_update_dpp(0, v, 0x111, 0xf, 0xf, false);
  v += __builtin_amdgcn_update_dpp(0, v, 0x112, 0xf, 0xf, false);
  v += __builtin_amdgcn_update_dpp(0, v, 0x114, 0xf, 0xf, false);
  v += __builtin_amdgcn_update_dpp(0, v, 0x118, 0xf, 0xf, false);
  v += __builtin_amdgcn_update_dpp(0, v, 0x142, 0xa, 0xf, false);
  v += __builtin_amdgcn_update_dpp(0, v, 0x143, 0xc, 0xf, false);
  return v;
}

__device__ __forceinline__ void glds16(const void* sbase, unsigned voff, unsigned lds_dst) {
  asm volatile(
      "s_mov_b32 m0, %2\n\t"
      "s_nop 0\n\t"
      "global_load_lds_dwordx4 %0, %1"
      :: "v"(voff), "s"(sbase), "s"(lds_dst) : "memory", "m0");
}
__device__ __forceinline__ void glds16nt(const void* sbase, unsigned voff, unsigned lds_dst) {
  asm volatile(
      "s_mov_b32 m0, %2\n\t"
      "s_nop 0\n\t"
      "global_load_lds_dwordx4 %0, %1 nt"
      :: "v"(voff), "s"(sbase), "s"(lds_dst) : "memory", "m0");
}
__device__ __forceinline__ void glds16x(const void* sbase, unsigned a, unsigned c, unsigned lds_dst) {
  unsigned tmp;
  asm volatile(
      "s_mov_b32 m0, %4\n\t"
      "v_mad_u32_u16 %0, %1, %5, %2\n\t"
      "global_load_lds_dwordx4 %0, %3"
      : "=&v"(tmp) : "v"(a), "v"(c), "s"(sbase), "s"(lds_dst), "s"(128u) : "memory", "m0");
}
__device__ __forceinline__ void glds16e(const void* sbase, unsigned a, unsigned c, unsigned lds_dst) {
  unsigned tmp;
  asm volatile(
      "s_mov_b32 m0, %4\n\t"
      "v_add_u32 %0, %1, %2\n\t"
      "global_load_lds_dwordx4 %0, %3"
      : "=&v"(tmp) : "v"(a), "v"(c), "s"(sbase), "s"(lds_dst) : "memory", "m0");
}
__device__ __forceinline__ int gload32(const void* sbase, unsigned voff) {
  int r;
  asm volatile("global_load_dword %0, %1, %2" : "=v"(r) : "v"(voff), "s"(sbase) : "memory");
  return r;
}
__device__ __forceinline__ u32x4 gload128(const void* sbase, unsigned voff) {
  u32x4 r;
  asm volatile("global_load_dwordx4 %0, %1, %2" : "=v"(r) : "v"(voff), "s"(sbase) : "memory");
  return r;
}

__device__ __forceinline__ u32x4 gload128nt(const void* sbase, unsigned voff) {
  u32x4 r;
  asm volatile("global_load_dwordx4 %0, %1, %2 nt" : "=v"(r) : "v"(voff), "s"(sbase) : "memory");
  return r;
}
__device__ __forceinline__ void gstore128wt(void* sbase, unsigned voff, u32x4 v) {
  asm volatile("global_store_dwordx4 %0, %1, %2 sc1\n\ts_nop 1" :: "v"(voff), "v"(v), "s"(sbase) : "memory");
}

constexpr int CELL = 128;
constexpr int CIN = 30;
constexpr int CCAP = 62;
constexpr int CPIT = 272;
constexpr unsigned E2OFF = NBK * NCH * CELL;
constexpr int KP_HDR = 0;
constexpr int KP_OVC = KP_HDR + NBK * 8;
constexpr int KP_ENT = KP_OVC + 16;
constexpr int KP_HIST = KP_ENT + NBK * CPIT;
constexpr int KP_LDS = KP_HIST + NBIN * 4;
constexpr int NWL = 26;
constexpr int NBLOB = 31;

__device__ __forceinline__ unsigned mad24(unsigned a, int b, unsigned c) {
  unsigned r;
  asm("v_mad_i32_i24 %0, %1, %2, %3" : "=v"(r) : "v"(a), "s"(b), "v"(c));
  return r;
}
__device__ __forceinline__ unsigned mul24(unsigned a, unsigned b) {
  unsigned r;
  asm("v_mul_u32_u24 %0, %1, %2" : "=v"(r) : "n"(b), "v"(a));
  return r;
}
__device__ __forceinline__ unsigned mulhi24(unsigned a, unsigned b) {
  return (unsigned)(((unsigned long long)(a & 0xffffffu) * (unsigned long long)b) >> 32);
}
__device__ __forceinline__ unsigned dot4(unsigned a, unsigned b, unsigned c) {
  return __builtin_amdgcn_udot4(a, b, c, false);
}

__global__ __launch_bounds__(1024) void k_part(const int* __restrict__ src, const int* __restrict__ dst,
                                               int E, int chunk, u32x4* __restrict__ ENT,
                                               unsigned* __restrict__ TS,
                                               const unsigned short* __restrict__ wa,
                                               const unsigned short* __restrict__ wb,
                                               const unsigned short* __restrict__ wc,
                                               const unsigned short* __restrict__ wu,
                                               const unsigned short* __restrict__ wv,
                                               unsigned short* __restrict__ wpack) {
  const int tid = threadIdx.x;
#define PT(i)
  PT(0);
  if (blockIdx.x < NCH) {
    __shared__ __attribute__((aligned(16))) char sm[KP_LDS];
    LAS unsigned* hist = (LAS unsigned*)(sm + KP_HIST);
    LAS unsigned* ovc = (LAS unsigned*)(sm + KP_OVC);
    const int c = blockIdx.x;
    const int base = c * chunk;
    const int cnt = max(min(chunk, E - base), 0);
    const int i0 = max(min(base + 4 * tid, min(base + cnt, E) - 4), 0);
    const int lo = base + 4 * tid - i0, hi4 = base + cnt - i0;
    u32x4 d = gload128nt(dst, (unsigned)i0 * 4u);
    u32x4 sv = gload128nt(src, (unsigned)i0 * 4u);
    if (tid < NBK) *(LAS u32x2*)(sm + KP_HDR + tid * 8) = u32x2{0u, 0u};
    if (tid == 0) *(LAS u32x2*)ovc = u32x2{0u, 0u};
    const unsigned sm0 = (unsigned)(size_t)(LAS char*)sm;
    const bool act = __builtin_amdgcn_readfirstlane(tid) * 4 < cnt;
    unsigned long long on[4];
    unsigned tup[4];
#pragma unroll
    for (int k = 0; k < 4; ++k) {
      on[k] = __ballot(k >= lo && k < hi4);
      tup[k] = (unsigned)(i0 + k - base);
    }
    const int bk = tid >> 2, p0 = (tid & 3) * 16;
    unsigned ha = sm0 + KP_HDR + bk * 8, ia = sm0 + KP_ENT + bk * CPIT + p0;
    unsigned go = (unsigned)((bk * NCH + c) * CELL + p0);
    asm volatile("s_waitcnt lgkmcnt(0)\n\ts_barrier\n\ts_waitcnt vmcnt(1)"
                 : "+v"(d), "+v"(tup[0]), "+v"(tup[1]), "+v"(tup[2]), "+v"(tup[3]), "+v"(ha), "+v"(ia), "+v"(go),
                   "+s"(on[0]), "+s"(on[1]), "+s"(on[2]), "+s"(on[3])
                 :: "memory");
    PT(1);
    unsigned gi[4], rk[4], m0[4], m1[4], cb[4];
    if (act) {
      unsigned oh[4], sh[4];
#pragma unroll
      for (int k = 0; k < 4; ++k) {
        gi[k] = mulhi24(d[k] << 8, 684785u);
        sh[k] = gi[k] << 3;
        oh[k] = 1u << (sh[k] & 31u);
        rk[k] = 0u;
      }
      asm volatile(
          "s_mov_b64 exec, %12\n\t"
          "ds_add_rtn_u32 %0, %4, %8\n\t"
          "s_mov_b64 exec, %13\n\t"
          "ds_add_rtn_u32 %1, %5, %9\n\t"
          "s_mov_b64 exec, %14\n\t"
          "ds_add_rtn_u32 %2, %6, %10\n\t"
          "s_mov_b64 exec, %15\n\t"
          "ds_add_rtn_u32 %3, %7, %11\n\t"
          "s_mov_b64 exec, -1"
          : "+&v"(rk[0]), "+&v"(rk[1]), "+&v"(rk[2]), "+&v"(rk[3])
          : "v"((gi[0] & ~3u) + sm0), "v"((gi[1] & ~3u) + sm0), "v"((gi[2] & ~3u) + sm0), "v"((gi[3] & ~3u) + sm0),
            "v"(oh[0]), "v"(oh[1]), "v"(oh[2]), "v"(oh[3]), "s"(on[0]), "s"(on[1]), "s"(on[2]), "s"(on[3])
          : "memory");
#pragma unroll
      for (int k = 0; k < 4; ++k) {
        const unsigned slot = mad24(gi[k], -49 * 128, d[k] << 8) >> 8;
        tup[k] = mad24(slot, 3200, tup[k]);
        const unsigned hm = (unsigned)((int)(gi[k] << 29) >> 31);
        m0[k] = ((oh[k] - 1u) | hm) & 0x04040404u;
        m1[k] = (oh[k] - 1u) & hm & 0x04040404u;
        gi[k] &= ~7u;
        cb[k] = mul24(gi[k], CPIT / 8) + sm0;
        gi[k] += sm0;
      }
      SB();
      asm volatile("s_waitcnt lgkmcnt(0)" : "+v"(rk[0]), "+v"(rk[1]), "+v"(rk[2]), "+v"(rk[3]) :: "memory");
#pragma unroll
      for (int k = 0; k < 4; ++k) asm("v_bfe_u32 %0, %0, %1, 8" : "+v"(rk[k]) : "v"(sh[k]));
      if (max(max(rk[0], rk[1]), max(rk[2], rk[3])) >= (unsigned)CCAP) ovc[1] = 1u;
#pragma unroll
      for (int k = 0; k < 4; ++k) {
        rk[k] = cb[k] + rk[k] * 4;
        cb[k] += CCAP * 4;
      }
    }
    asm volatile("s_waitcnt vmcnt(0)" : "+v"(sv) :: "memory");
#pragma unroll
    for (int k = 0; k < 4; ++k) tup[k] = mad24(sv[k], 81920, tup[k]);
    asm volatile("s_barrier"
                 : "+v"(rk[0]), "+v"(rk[1]), "+v"(rk[2]), "+v"(rk[3]), "+v"(m0[0]), "+v"(m0[1]), "+v"(m0[2]),
                   "+v"(m0[3]), "+v"(m1[0]), "+v"(m1[1]), "+v"(m1[2]), "+v"(m1[3]), "+v"(tup[0]), "+v"(tup[1]),
                   "+v"(tup[2]), "+v"(tup[3]), "+v"(cb[0]), "+v"(cb[1]), "+v"(cb[2]), "+v"(cb[3]), "+v"(gi[0]),
                   "+v"(gi[1]), "+v"(gi[2]), "+v"(gi[3])
                 :: "memory");
    PT(2);
    u32x2 hc = *(const LAS u32x2*)(size_t)ha;
    if (act) {
      u32x2 hd[4];
      unsigned pa[4];
#pragma unroll
      for (int k = 0; k < 4; ++k) hd[k] = *(const LAS u32x2*)(size_t)gi[k];
#pragma unroll
      for (int k = 0; k < 4; ++k) pa[k] = min(dot4(hd[k][0], m0[k], dot4(hd[k][1], m1[k], rk[k])), cb[k]);
      asm volatile(
          "s_mov_b64 exec, %8\n\t"
          "ds_write_b32 %0, %4 offset:%12\n\t"
          "s_mov_b64 exec, %9\n\t"
          "ds_write_b32 %1, %5 offset:%12\n\t"
          "s_mov_b64 exec, %10\n\t"
          "ds_write_b32 %2, %6 offset:%12\n\t"
          "s_mov_b64 exec, %11\n\t"
          "ds_write_b32 %3, %7 offset:%12\n\t"
          "s_mov_b64 exec, -1"
          :: "v"(pa[0]), "v"(pa[1]), "v"(pa[2]), "v"(pa[3]), "v"(tup[0]), "v"(tup[1]), "v"(tup[2]), "v"(tup[3]),
             "s"(on[0]), "s"(on[1]), "s"(on[2]), "s"(on[3]), "n"(KP_ENT + 8)
          : "memory");
    }
    unsigned used = dot4(hc[0], 0x01010101u, dot4(hc[1], 0x01010101u, 0u));
    if (used > (unsigned)CCAP) ovc[1] = 1u;
    asm volatile("s_waitcnt lgkmcnt(0)\n\ts_barrier" ::: "memory");
    PT(3); PT(4);
    const unsigned ovf = ovc[1];
    u32x4 v0 = *(const LAS u32x4*)(size_t)ia;
    u32x4 v1 = *(const LAS u32x4*)(size_t)(ia + 64);
    asm volatile("" : "+v"(v0), "+v"(v1));
    if (__builtin_expect(__builtin_amdgcn_readfirstlane(ovf) != 0u, 0)) {
      *(LAS u32x2*)(hist + tid * 2) = u32x2{0u, 0u};
      __syncthreads();
#pragma unroll
      for (int k = 0; k < 4; ++k) {
        gi[k] = mulhi24(d[k] << 8, 684785u);
        if (k >= lo && k < hi4)
          rk[k] = __hip_atomic_fetch_add(hist + gi[k], 1u, __ATOMIC_RELAXED, __HIP_MEMORY_SCOPE_WORKGROUP);
      }
      __syncthreads();
      if (tid < NBK) {
        unsigned tot = 0;
        for (int i = 0; i < 8; ++i) tot += hist[tid * 8 + i];
        LAS unsigned* cp = (LAS unsigned*)(sm + KP_ENT + tid * CPIT);
        unsigned p = ~0u;
        if (tot > (unsigned)CCAP) {
          p = (unsigned)base + __hip_atomic_fetch_add(ovc, tot, __ATOMIC_RELAXED, __HIP_MEMORY_SCOPE_WORKGROUP);
          *(LAS u32x2*)(sm + KP_HDR + tid * 8) = u32x2{~0u, ~0u};
        }
        for (int i = 0; i < 8; ++i) {
          const unsigned n = hist[tid * 8 + i];
          hist[tid * 8 + i] = p;
          if (p != ~0u) {
            cp[2 + i] = 0u;
            cp[10 + i] = p;
            cp[18 + i] = n;
            p += n;
          }
        }
      }
      __syncthreads();
#pragma unroll
      for (int k = 0; k < 4; ++k) {
        if (k >= lo && k < hi4) {
          const unsigned p = hist[gi[k]];
          if (p != ~0u) TS[p + rk[k]] = tup[k];
        }
      }
      hc = *(const LAS u32x2*)(size_t)ha;
      used = dot4(hc[0], 0x01010101u, dot4(hc[1], 0x01010101u, 0u));
      v0 = *(const LAS u32x4*)(size_t)ia;
      v1 = *(const LAS u32x4*)(size_t)(ia + 64);
    }
    if (p0 == 0) { v0[0] = hc[0]; v0[1] = hc[1]; }
    gstore128wt(ENT, go, v0);
    if (used > 14u) gstore128wt(ENT, go + 64, v1);
    if (__builtin_expect(used > (unsigned)CIN, 0)) {
      gstore128wt(ENT, go + E2OFF, *(const LAS u32x4*)(size_t)(ia + 128));
      gstore128wt(ENT, go + E2OFF + 64, *(const LAS u32x4*)(size_t)(ia + 192));
    }
  } else if (tid < 512) {
    const int f = blockIdx.x - NCH;
    unsigned short v;
    if (f >= NWL && f < NBLOB) {
      const int e = (f - NWL) * 512 + tid;
      const int tb = e >= 32 * (TPITCH / 2);
      const int e2 = e - tb * 32 * (TPITCH / 2);
      const int sl = e2 / (TPITCH / 2), col = e2 - sl * (TPITCH / 2);
      const int nd = tb ? 16 * ((col >> 3) & 1) + 8 * ((col >> 2) & 1) + 4 * (col >> 4) + (col & 3) : col;
      v = (col < 32 && nd == sl) ? 0x3F80 : 0;
    } else {
      const int lane = tid >> 3, j = tid & 7;
      const int n = lane & 31, h = lane >> 5;
      const int g = f < 16 ? f : f < NWL ? f - 18 : f - NBLOB;
      const int ct = g & 1;
      const int col = 32 * ((n >> 2) & 1) + 16 * ct + 4 * (n >> 3) + (n & 3);
      if (f == 16 || f == 17) {
        v = wc[(8 * h + j) * 64 + col];
      } else {
        const int t = (g >> 1) & 3;
        const unsigned short* W = f < 8 ? wb : f < 16 ? wv : f < NWL ? wu : wa;
        v = W[(32 * h + 8 * t + j) * 64 + col];
      }
      if (f < 8 || f == 16 || f == 17 || f >= NBLOB)
        v = (unsigned short)(pack_bf16(__uint_as_float((unsigned)v << 16) * NLOG2E, 0.f) & 0xffffu);
    }
    wpack[f * 512 + tid] = v;
  }
}

constexpr int RING_SLOT = 5 * 1024;
constexpr int SPIT = 1040;
constexpr int LCAP = 512;
constexpr int LARR = 2560;
constexpr int LIST_BYTES = 2 * LARR;
constexpr int LRAW = LIST_BYTES - (LCAP + 8) * 8;
constexpr int OFF_W = 0;
constexpr int OFF_T1 = NWL * 1024;
constexpr int OFF_T2 = OFF_T1 + 32 * TPITCH;
constexpr int OFF_WA = NBLOB * 1024;
constexpr int OFF_R0 = NFRAG * 1024;
constexpr int OFF_R1 = OFF_R0 + 8 * RING_SLOT;
constexpr int OFF_L = OFF_R1 + 8 * RING_SLOT;
constexpr int MAIN_LDS = OFF_L + 8 * LIST_BYTES;
static_assert(MAIN_LDS <= 160 * 1024, "LDS");

__device__ __forceinline__ float add1(float t) { return 1.0f + t; }
__device__ __forceinline__ float mulf(int i, float a, float b) { return (i & 1) ? __builtin_fmaf(a, b, 0.0f) : a * b; }

__global__ __launch_bounds__(512, 2) void k_main(const unsigned short* __restrict__ x,
                                                 const u32x4* __restrict__ ENT,
                                                 const u32x4* __restrict__ ES,
                                                 const unsigned* __restrict__ TS, int chunk,
                                                 const bf16x8* __restrict__ wpack,
                                                 unsigned short* __restrict__ out, int N
                                                 ) {
  __shared__ __attribute__((aligned(16))) char smem[MAIN_LDS];
  const int tid = threadIdx.x, lane = tid & 63;
  const int wave = __builtin_amdgcn_readfirstlane(tid >> 6);
  const int r = lane & 31, h = lane >> 5;
  const int bk = blockIdx.x;
  const int gb = 2 * (wave & 3) + (wave >> 2);
  const int nb0 = bk * 196 + 49 * (wave & 3) + 25 * (wave >> 2);
  const int nodes = max(min(wave < 4 ? 25 : 24, N - nb0), 0);
  const int n0 = min(nb0, N - 1);
  const unsigned lds0 = (unsigned)(size_t)(LAS char*)smem;

  u32x4 xg[4];
  {
    const unsigned vo = lane * 16;
    const char* eb = reinterpret_cast<const char*>(ENT) + (size_t)bk * (NCH * CELL);
#pragma unroll
    for (int i = 0; i < 4; ++i) glds16(eb + (wave * 4 + i) * 1024, vo, lds0 + OFF_R1 + (wave * 4 + i) * SPIT);
    const char* wp = reinterpret_cast<const char*>(wpack);
#pragma unroll
    for (int i = 0; i < 5; ++i) {
      const int f = min(wave + 8 * i, NFRAG - 1);
      glds16(wp + f * 1024, vo, lds0 + OFF_W + f * 1024);
    }
    const unsigned xo = (unsigned)(n0 + max(min(r, nodes - 1), 0)) * 128u + 64u * h;
#pragma unroll
    for (int t = 0; t < 4; ++t) xg[t] = gload128(x, xo + 16 * t);
    if (wave >= 4) __builtin_amdgcn_s_setprio(1);
  }
  LAS char* lbase = (LAS char*)smem + OFF_L + wave * LIST_BYTES;
  const unsigned la = (unsigned)(size_t)lbase;
  auto put = [&](int p, unsigned a, unsigned b) { *(LAS u32x2*)(lbase + LRAW + p * 8) = u32x2{a, b}; };
  unsigned aLX = la + (lane >> 3) * 16;
  unsigned aLE = la + LARR + ((((lane >> 1) & 7) << 2) | (lane >> 4)) * 4;
  unsigned aLS4 = la + (4 * h + ((lane >> 2) & 3)) * 16;
  unsigned aLS1 = la + (((r & 7) << 2) | (r >> 3)) * 4 + 2;
  unsigned cx0 = (unsigned)(((lane & 7) ^ (lane >> 4)) * 16);
  unsigned cx1 = cx0 ^ 64u;
  unsigned ce = (unsigned)((((lane & 1) ^ ((lane >> 4) & 1))) * 16);
  const unsigned ring0 = lds0 + OFF_R0 + wave * RING_SLOT;
  const unsigned ringd = 8 * RING_SLOT;
  unsigned cS = lds0 + OFF_T1 + (((lane >> 4) & 1) * 16 + (lane & 3) * 4) * 2;
  unsigned cSt = lds0 + OFF_T2 + h * 32;
  unsigned fo[4];
#pragma unroll
  for (int t = 0; t < 4; ++t) fo[t] = ring0 + (unsigned)(r * 128 + (((4 * h + t) ^ ((r >> 1) & 7)) * 16));
  unsigned foe = ring0 + (unsigned)(4096 + r * 32 + ((h ^ ((r >> 3) & 1)) * 16));
  unsigned sa = lds0 + OFF_R1 + r * SPIT + h * CELL;
  unsigned cb0 = (unsigned)((8 * r + h) * chunk);
  unsigned aR = la + LRAW + lane * 8, aF = la + lane * 4;
  unsigned aRX = la + LRAW + (lane >> 3) * 32, aRE = la + LRAW + ((((lane >> 1) & 7) << 2) | (lane >> 4)) * 8;
  f32x16 Y[4];
#pragma unroll
  for (int q = 0; q < 4; ++q)
#pragma unroll
    for (int i = 0; i < 16; ++i) Y[q][i] = 0.f;
  f32x16 G0, V0, G1, V1;
  bf16x8 S[2], St[2], as[4], ae, b[3];
  u32x4 pd[2], pn[2];
  float tq[16], rq[16], pq[16];
  auto pipe_zero = [&]() {
#pragma unroll
    for (int j = 0; j < 8; ++j) { S[0][j] = 0; S[1][j] = 0; }
#pragma unroll
    for (int j = 0; j < 4; ++j) { pd[0][j] = 0; pd[1][j] = 0; pn[0][j] = 0; pn[1][j] = 0; }
#pragma unroll
    for (int i = 0; i < 16; ++i) V1[i] = 0.f;
    rq[14] = 0.f; rq[15] = 0.f;
  };
  asm volatile("" : "+v"(aLX), "+v"(aLE), "+v"(aLS4), "+v"(aLS1), "+v"(cx0), "+v"(cx1), "+v"(ce), "+v"(cS), "+v"(cSt),
                    "+v"(fo[0]), "+v"(fo[1]), "+v"(fo[2]), "+v"(fo[3]), "+v"(foe), "+v"(sa), "+v"(cb0), "+v"(aR),
                    "+v"(aF), "+v"(aRX), "+v"(aRE));
  asm volatile("" : "+v"(Y[0]), "+v"(Y[1]), "+v"(Y[2]), "+v"(Y[3]));
  const unsigned mlo = gb >= 4 ? 0x01010101u : 0x01010101u & ((1u << (8 * gb)) - 1u);
  const unsigned mhi = gb <= 4 ? 0u : 0x01010101u & ((1u << (8 * (gb - 4))) - 1u);
  asm volatile("s_waitcnt vmcnt(9)\n\ts_barrier" ::: "memory");
  u32x2 cl[4][4], hd[4];
  int nr[4], st[4], ni[4];
  unsigned long long mk[4][4];
#pragma unroll
  for (int j = 0; j < 4; ++j) hd[j] = *(const LAS u32x2*)(size_t)(sa + 2 * j * CELL);
  auto head = [&](int j) {
    nr[j] = (int)(((gb >= 4 ? hd[j][1] : hd[j][0]) >> (8 * (gb & 3))) & 0xffu);
    st[j] = (int)dot4(hd[j][0], mlo, dot4(hd[j][1], mhi, 0u));
    const int n = min(min(nr[j], 8), CIN - st[j]);
#pragma unroll
    for (int q = 0; q < 4; ++q) mk[j][q] = __ballot(n > 2 * q);
    ni[j] = max(n, 0);
  };
  auto fetch = [&](int j) {
    asm volatile(
        "s_mov_b64 exec, %5\n\t"
        "ds_read2_b32 %0, %4 offset0:%9 offset1:%9+1\n\t"
        "s_mov_b64 exec, %6\n\t"
        "ds_read2_b32 %1, %4 offset0:%9+2 offset1:%9+3\n\t"
        "s_mov_b64 exec, %7\n\t"
        "ds_read2_b32 %2, %4 offset0:%9+4 offset1:%9+5\n\t"
        "s_mov_b64 exec, %8\n\t"
        "ds_read2_b32 %3, %4 offset0:%9+6 offset1:%9+7\n\t"
        "s_mov_b64 exec, -1"
        : "=&v"(cl[j][0]), "=&v"(cl[j][1]), "=&v"(cl[j][2]), "=&v"(cl[j][3])
        : "v"(sa + st[j] * 4), "s"(mk[j][0]), "s"(mk[j][1]), "s"(mk[j][2]), "s"(mk[j][3]), "n"(j * 64 + 2)
        : "memory");
  };
  auto fill = [&](int j, int p) {
    asm volatile(
        "s_mov_b64 exec, %13\n\t"
        "ds_write2_b32 %0, %9, %1 offset0:14 offset1:15\n\t"
        "ds_write2_b32 %0, %8, %1 offset0:12 offset1:13\n\t"
        "s_mov_b64 exec, %12\n\t"
        "ds_write2_b32 %0, %7, %1 offset0:10 offset1:11\n\t"
        "ds_write2_b32 %0, %6, %1 offset0:8 offset1:9\n\t"
        "s_mov_b64 exec, %11\n\t"
        "ds_write2_b32 %0, %5, %1 offset0:6 offset1:7\n\t"
        "ds_write2_b32 %0, %4, %1 offset0:4 offset1:5\n\t"
        "s_mov_b64 exec, %10\n\t"
        "ds_write2_b32 %0, %3, %1 offset0:2 offset1:3\n\t"
        "ds_write2_b32 %0, %2, %1 offset0:0 offset1:1\n\t"
        "s_mov_b64 exec, -1"
        :: "v"(la + LRAW + p * 8), "v"(cb0 + 2 * j * chunk), "v"(cl[j][0][0]), "v"(cl[j][0][1]), "v"(cl[j][1][0]),
           "v"(cl[j][1][1]), "v"(cl[j][2][0]), "v"(cl[j][2][1]), "v"(cl[j][3][0]), "v"(cl[j][3][1]), "s"(mk[j][0]),
           "s"(mk[j][1]), "s"(mk[j][2]), "s"(mk[j][3])
        : "memory");
  };
  auto src_of = [&](unsigned v) { return mulhi24(v >> 8, 13421773u); };
  auto decode = [&](unsigned v, unsigned cb, unsigned& a, unsigned& e) {
    unsigned sl;
    asm("v_lshrrev_b32 %0, 8, %3\n\t"
        "v_mul_hi_u32_u24 %0, 0xcccccd, %0\n\t"
        "v_mad_i32_i24 %1, %0, %5, %3\n\t"
        "v_mul_hi_u32_u24 %2, 0x147ae2, %1\n\t"
        "v_add_lshl_u32 %1, %1, %4, 5\n\t"
        "v_mad_i32_i24 %0, %2, %6, %0\n\t"
        "v_mad_i32_i24 %1, %2, %7, %1"
        : "=&v"(a), "=&v"(e), "=&v"(sl)
        : "v"(v), "v"(cb), "s"(-81920), "s"((int)(TPITCH << 16)), "s"(-102400));
  };
  auto decode2 = [&](const u32x2 p, const u32x2 q, unsigned& a, unsigned& e, unsigned& c, unsigned& f) {
    unsigned sl, sm;
    asm("v_lshrrev_b32 %0, 8, %6\n\t"
        "v_lshrrev_b32 %3, 8, %8\n\t"
        "v_mul_hi_u32_u24 %0, 0xcccccd, %0\n\t"
        "v_mul_hi_u32_u24 %3, 0xcccccd, %3\n\t"
        "v_mad_i32_i24 %1, %0, %10, %6\n\t"
        "v_mad_i32_i24 %4, %3, %10, %8\n\t"
        "v_mul_hi_u32_u24 %2, 0x147ae2, %1\n\t"
        "v_mul_hi_u32_u24 %5, 0x147ae2, %4\n\t"
        "v_add_lshl_u32 %1, %1, %7, 5\n\t"
        "v_add_lshl_u32 %4, %4, %9, 5\n\t"
        "v_mad_i32_i24 %0, %2, %11, %0\n\t"
        "v_mad_i32_i24 %3, %5, %11, %3\n\t"
        "v_mad_i32_i24 %1, %2, %12, %1\n\t"
        "v_mad_i32_i24 %4, %5, %12, %4"
        : "=&v"(a), "=&v"(e), "=&v"(sl), "=&v"(c), "=&v"(f), "=&v"(sm)
        : "v"(p[0]), "v"(p[1]), "v"(q[0]), "v"(q[1]), "s"(-81920), "s"((int)(TPITCH << 16)), "s"(-102400));
  };
  auto ld128 = [&](unsigned a) { return *(const LAS u32x4*)(size_t)a; };
  auto gather5 = [&](const u32x4 sx, unsigned re, int rs) {
    const unsigned dstb = ring0 + rs * ringd;
    glds16x(x, sx[0], cx0, dstb);
    glds16x(x, sx[1], cx1, dstb + 1024);
    glds16x(x, sx[2], cx0, dstb + 2048);
    glds16x(x, sx[3], cx1, dstb + 3072);
    glds16e(ES, re, ce, dstb + 4096);
  };
  auto issue_gather = [&](int tile, int rs) {
    gather5(ld128(aLX + tile * 128), *(const LAS unsigned*)(size_t)(aLE + tile * 128), rs);
  };
  head(0);
  fetch(0);
  SB();
  const int s0 = wave_scan(ni[0]);
  head(1);
  head(2);
  head(3);
  const int tot0 = __builtin_amdgcn_readlane(s0, 63);
  const int p0 = s0 - ni[0];
  SB();
  asm volatile("s_waitcnt lgkmcnt(0)" : "+v"(cl[0][0]), "+v"(cl[0][1]), "+v"(cl[0][2]), "+v"(cl[0][3]) :: "memory");
  fill(0, p0);
  bool early = tot0 >= 32;
  int s123;
  {
    unsigned v0 = *(const LAS unsigned*)(size_t)aRX, v1 = *(const LAS unsigned*)(size_t)(aRX + 8);
    unsigned v2 = *(const LAS unsigned*)(size_t)(aRX + 16), v3 = *(const LAS unsigned*)(size_t)(aRX + 24);
    u32x2 ve = *(const LAS u32x2*)(size_t)aRE;
    SB();
    s123 = wave_scan(ni[1] | (ni[2] << 10) | (ni[3] << 20));
    SB();
    asm volatile("" : "+v"(v0), "+v"(v1), "+v"(v2), "+v"(v3), "+v"(ve), "+v"(s123));
    if (__builtin_expect(early, 1)) {
      unsigned a, e;
      decode(ve[0], ve[1], a, e);
      gather5(u32x4{src_of(v0), src_of(v1), src_of(v2), src_of(v3)}, e, 0);
    } else {
      gather5(u32x4{0u, 0u, 0u, 0u}, 0u, 0);
    }
  }
  fetch(1);
  fetch(2);
  fetch(3);
  const int e123 = __builtin_amdgcn_readlane(s123, 63);
  const int tot1 = tot0 + (e123 & 0x3ff), tot2 = tot1 + ((e123 >> 10) & 0x3ff);
  int cnt_all = tot2 + (e123 >> 20);
  asm volatile("s_waitcnt vmcnt(5) lgkmcnt(0)\n\ts_barrier"
               : "+v"(xg[0]), "+v"(xg[1]), "+v"(xg[2]), "+v"(xg[3]),
                 "+v"(cl[1][0]), "+v"(cl[1][1]), "+v"(cl[1][2]), "+v"(cl[1][3]), "+v"(cl[2][0]), "+v"(cl[2][1]),
                 "+v"(cl[2][2]), "+v"(cl[2][3]), "+v"(cl[3][0]), "+v"(cl[3][1]), "+v"(cl[3][2]), "+v"(cl[3][3])
               :: "memory");
  const LAS bf16x8* wl = (const LAS bf16x8*)(smem + OFF_W) + lane;
  const f32x16 zero16 = {0.f, 0.f, 0.f, 0.f, 0.f, 0.f, 0.f, 0.f, 0.f, 0.f, 0.f, 0.f, 0.f, 0.f, 0.f, 0.f};
  f32x16 XA[2] = {zero16, zero16};
  bf16x8 wa[8];
#pragma unroll
  for (int k = 0; k < 8; ++k) wa[k] = wl[(NBLOB + k) * 64];
  SB();
  auto xa_step = [&](int k) {
#pragma unroll
    for (int ct = 0; ct < 2; ++ct) XA[ct] = MFMA32(__builtin_bit_cast(bf16x8, xg[k]), wa[k * 2 + ct], XA[ct]);
    SB();
  };
  if (__builtin_expect(cnt_all > LCAP, 0)) {
#pragma unroll
    for (int j = 1; j < 4; ++j)
#pragma unroll
      for (int q = 0; q < 4; ++q) mk[j][q] = 0ull;
  }
  fill(1, tot0 + (s123 & 0x3ff) - ni[1]);
  SB();
  xa_step(0);
  fill(2, tot1 + ((s123 >> 10) & 0x3ff) - ni[2]);
  SB();
  xa_step(1);
  fill(3, tot2 + (s123 >> 20) - ni[3]);
  SB();
  xa_step(2);
  xa_step(3);
  if (__builtin_expect(__ballot(nr[0] + nr[1] + nr[2] + nr[3] != ni[0] + ni[1] + ni[2] + ni[3]) != 0ull, 0)) {
#pragma unroll
    for (int j = 0; j < 4; ++j) {
      unsigned long long om = __ballot(nr[j] != ni[j]);
      while (om) {
        const int l = __builtin_ctzll(om);
        om &= om - 1;
        const int n = __builtin_amdgcn_readlane(nr[j] - ni[j], l);
        const int p = __builtin_amdgcn_readlane(st[j] + ni[j], l) + lane;
        const int cc = 8 * (l & 31) + (l >> 5) + 2 * j;
        if (n > CCAP) cnt_all = LCAP;
        if (cnt_all + n <= LCAP && lane < n) {
          const unsigned* cp = reinterpret_cast<const unsigned*>(reinterpret_cast<const char*>(ENT) +
                                                                 ((size_t)bk * NCH + cc) * CELL);
          put(cnt_all + lane, p < CIN ? cp[2 + p] : cp[E2OFF / 4 + p - CIN], (unsigned)(cc * chunk));
        }
        cnt_all += n;
      }
    }
    __builtin_amdgcn_s_waitcnt(0x0F70);
  }
  const int gbin = bk * 8 + wave;
  auto fill_slow = [&](int wb0) __attribute__((always_inline)) {
    int nn[4], sp[4], tot = 0;
    const unsigned* rp[4];
    int l2 = lane;
    asm volatile("" : "+v"(l2));
    const int ch0 = 8 * (l2 & 31) + (l2 >> 5);
#pragma unroll
    for (int j = 0; j < 4; ++j) {
      const unsigned* cp = reinterpret_cast<const unsigned*>(reinterpret_cast<const char*>(ENT) +
                                                             ((size_t)bk * NCH + ch0 + 2 * j) * CELL);
      const unsigned h0 = cp[0], h1 = cp[1];
      nn[j] = (int)(((gb >= 4 ? h1 : h0) >> (8 * (gb & 3))) & 0xffu);
      sp[j] = (int)dot4(h0, mlo, dot4(h1, mhi, 0u));
      rp[j] = cp + 2 + sp[j];
      sp[j] = CIN - sp[j];
      if (nn[j] == 0xff) {
        rp[j] = TS + cp[10 + gb];
        nn[j] = (int)cp[18 + gb];
        sp[j] = nn[j];
      }
      tot += nn[j];
    }
    const int sc = wave_scan(tot);
    int pp = sc - tot - wb0;
#pragma unroll
    for (int j = 0; j < 4; ++j) {
      const int cb = (ch0 + 2 * j) * chunk;
      const int te = min(nn[j], LCAP - pp);
#pragma unroll 1
      for (int t = max(0, -pp); t < te; ++t) put(pp + t, rp[j][t < sp[j] ? t : t + (int)(E2OFF / 4) - 32], (unsigned)cb);
      pp += nn[j];
    }
    __builtin_amdgcn_s_waitcnt(0x0F70);
    return __builtin_amdgcn_readlane(sc, 63);
  };
  if (__builtin_expect(cnt_all > LCAP, 0)) {
    asm volatile("s_waitcnt vmcnt(0)" ::: "memory");
    cnt_all = fill_slow(0);
    early = false;
  }
  cnt_all = __builtin_amdgcn_readfirstlane(cnt_all);
  int cnt = min(cnt_all, LCAP);
  auto convert = [&](int cnt, unsigned aR, unsigned aF, int lane) __attribute__((always_inline)) {
    u32x2 v[8];
#pragma unroll
    for (int k = 0; k < 8; ++k) v[k] = *(const LAS u32x2*)(size_t)(aR + k * 512);
    asm volatile("" ::: "memory");
#pragma unroll
    for (int k = 0; k < 6; k += 2) {
      unsigned a, e, c, f;
      decode2(v[k], v[k + 1], a, e, c, f);
      *(LAS unsigned*)(size_t)(aF + k * 256) = a;
      *(LAS unsigned*)(size_t)(aF + k * 256 + 256) = c;
      *(LAS unsigned*)(size_t)(aF + k * 256 + LARR) = e;
      *(LAS unsigned*)(size_t)(aF + k * 256 + LARR + 256) = f;
    }
#pragma unroll
    for (int k = 6; k < 8; ++k) {
      if (cnt > k * 64) {
        unsigned a, e;
        decode(v[k][0], v[k][1], a, e);
        *(LAS unsigned*)(size_t)(aF + k * 256) = a;
        *(LAS unsigned*)(size_t)(aF + k * 256 + LARR) = e;
      }
    }
    const int u = cnt + lane;
    const unsigned padA = (unsigned)(n0 + max(min(((u >> 2) & 7) + 8 * (u & 3), nodes - 1), 0)) | (31u * TPITCH << 16);
    const unsigned pa = aF + cnt * 4;
    *(LAS unsigned*)(size_t)pa = padA;
    *(LAS unsigned*)(size_t)(pa + 256) = padA;
    *(LAS unsigned*)(size_t)(pa + LARR) = 0u;
    *(LAS unsigned*)(size_t)(pa + LARR + 256) = 0u;
  };
  convert(cnt, aR, aF, lane);
  issue_gather(1, 1);
  if (__builtin_expect(!early, 0)) {
    asm volatile("s_waitcnt vmcnt(0)" ::: "memory");
    issue_gather(0, 0);
    issue_gather(1, 1);
  }
  bf16x8 XAf[2][2];
#pragma unroll
  for (int ct = 0; ct < 2; ++ct)
#pragma unroll
    for (int s = 0; s < 2; ++s) {
      u32x4 p;
#pragma unroll
      for (int j = 0; j < 4; ++j) p[j] = pack_bf16(XA[ct][8 * s + 2 * j], XA[ct][8 * s + 2 * j + 1]);
      XAf[s][ct] = __builtin_bit_cast(bf16x8, p);
    }

  auto rd128 = [&](unsigned a) { return *(const LAS bf16x8*)(size_t)a; };
  auto load_S = [&](const u32x4 ss) {
    const s16x4 a0 = __builtin_amdgcn_ds_read_tr16_b64_v4i16((LAS s16x4*)(size_t)((ss[0] >> 16) + cS));
    const s16x4 a1 = __builtin_amdgcn_ds_read_tr16_b64_v4i16((LAS s16x4*)(size_t)((ss[1] >> 16) + cS));
    const s16x4 a2 = __builtin_amdgcn_ds_read_tr16_b64_v4i16((LAS s16x4*)(size_t)((ss[2] >> 16) + cS));
    const s16x4 a3 = __builtin_amdgcn_ds_read_tr16_b64_v4i16((LAS s16x4*)(size_t)((ss[3] >> 16) + cS));
    S[0] = __builtin_shufflevector(a0, a1, 0, 1, 2, 3, 4, 5, 6, 7);
    S[1] = __builtin_shufflevector(a2, a3, 0, 1, 2, 3, 4, 5, 6, 7);
  };
  auto widx = [&](int m, int ct) { return m == 6 ? 16 + ct : m < 11 ? (m - 7) * 2 + ct : 8 + (m - 11) * 2 + ct; };

  auto ew = [&](int m, const f32x16& Gc, const f32x16& Vc, const f32x16& Gn, const f32x16& Vp) {
    if (m < 2) {
      const int i = 14 + m;
      pq[i] = mulf(i, Vp[i], rq[i]);
      if (m == 1) pn[1][3] = pack_bf16(pq[14], pq[15]);
    } else if (m == 2) {
      pq[0] = mulf(0, Vc[0], rq[0]);
      pq[1] = mulf(1, Vc[1], rq[1]);
      pn[0][0] = pack_bf16(pq[0], pq[1]);
    } else {
      const int i = m - 1;
      pq[i] = mulf(i, Vc[i], rq[i]);
      if (i & 1) pn[i >> 3][(i >> 1) & 3] = pack_bf16(pq[i - 1], pq[i]);
    }
    if (m == 0) pd[0][0] = pack_bf16(rq[0], rq[1]);
    else if ((m & 1) == 0) { const int i = m + 1; pd[i >> 3][(i >> 1) & 3] = pack_bf16(rq[i - 1], rq[i]); }
    if (m < 14) {
      const int i = m + 2;
      rq[i] = __builtin_amdgcn_rcpf(add1(tq[i]));
    } else {
      rq[0] = __builtin_amdgcn_rcpf(add1(tq[0]));
      rq[1] = __builtin_amdgcn_rcpf(add1(tq[1]));
    }
    if (m < 13) tq[m + 3] = __builtin_amdgcn_exp2f(Gc[m + 3]);
    else if (m == 13) { tq[0] = __builtin_amdgcn_exp2f(Gn[0]); tq[1] = __builtin_amdgcn_exp2f(Gn[1]); }
    else tq[2] = __builtin_amdgcn_exp2f(Gn[2]);
  };

  int wb0 = 0;
  while (true) {
    const int ntiles = (cnt + 31) >> 5;
    asm volatile("s_waitcnt vmcnt(5)" ::: "memory");
    {
      const unsigned sl = *(const LAS unsigned short*)(size_t)aLS1;
#pragma unroll
      for (int k = 0; k < 4; ++k) as[k] = rd128(fo[k]);
      ae = rd128(foe);
      St[0] = rd128(sl + cSt);
      St[1] = rd128(sl + cSt + 16);
      G0 = MFMA32(St[0], XAf[0][0], zero16);
      G0 = MFMA32(St[1], XAf[1][0], G0);
      G0 = MFMA32(ae, wl[widx(6, 0) * 64], G0);
#pragma unroll
      for (int k = 0; k < 4; ++k) G0 = MFMA32(as[k], wl[widx(7 + k, 0) * 64], G0);
      V0 = MFMA32(as[0], wl[widx(11, 0) * 64], zero16);
#pragma unroll
      for (int k = 1; k < 4; ++k) V0 = MFMA32(as[k], wl[widx(11 + k, 0) * 64], V0);
      asm volatile("s_waitcnt lgkmcnt(0)" ::: "memory");
      issue_gather(2, 0);
      pipe_zero();
      tq[0] = __builtin_amdgcn_exp2f(G0[0]);
      tq[1] = __builtin_amdgcn_exp2f(G0[1]);
      tq[2] = __builtin_amdgcn_exp2f(G0[2]);
      rq[0] = __builtin_amdgcn_rcpf(add1(tq[0]));
      rq[1] = __builtin_amdgcn_rcpf(add1(tq[1]));
      SB();
    }
    unsigned lS4 = aLS4, lS1 = aLS1, lX = aLX, lE = aLE;
    auto body = [&](const int par) {
      const unsigned lo = par * 128;
      const unsigned ro = (1 - par) * ringd;
      {
        u32x4 ss;
        unsigned sl;
#pragma unroll
        for (int m = 0; m < 15; ++m) {
          if (m == 0) Y[1] = MFMA32(__builtin_bit_cast(bf16x8, pd[0]), S[0], Y[1]);
          else if (m == 1) Y[3] = MFMA32(__builtin_bit_cast(bf16x8, pn[0]), S[0], Y[3]);
          else if (m == 2) Y[1] = MFMA32(__builtin_bit_cast(bf16x8, pd[1]), S[1], Y[1]);
          else if (m == 3) Y[3] = MFMA32(__builtin_bit_cast(bf16x8, pn[1]), S[1], Y[3]);
          else if (m == 4) G1 = MFMA32(St[0], XAf[0][1], zero16);
          else if (m == 5) G1 = MFMA32(St[1], XAf[1][1], G1);
          else if (m == 6) G1 = MFMA32(ae, b[0], G1);
          else if (m < 11) G1 = MFMA32(as[m - 7], b[(m - 6) % 3], G1);
          else if (m == 11) V1 = MFMA32(as[0], b[(m - 6) % 3], zero16);
          else V1 = MFMA32(as[m - 11], b[(m - 6) % 3], V1);
          SB();
          if (m == 0) { ss = ld128(lS4 + lo); sl = *(const LAS unsigned short*)(size_t)(lS1 + lo + 128); }
          if (m >= 3 && m < 6) b[m - 3] = wl[widx(m + 3, 1) * 64];
          if (m >= 6 && m < 12) b[(m - 6) % 3] = wl[widx(m + 3, 1) * 64];
          if (m == 4) load_S(ss);
          if (m == 5) { St[0] = rd128(sl + cSt); St[1] = rd128(sl + cSt + 16); }
          if (m == 6) { asm volatile("s_waitcnt vmcnt(5)" ::: "memory"); ae = rd128(foe + ro); }
          if (m >= 11) as[m - 11] = rd128(fo[m - 11] + ro);
          ew(m, G0, V0, G1, V1);
          SB();
        }
      }
      {
        u32x4 sx;
        unsigned re;
#pragma unroll
        for (int m = 0; m < 15; ++m) {
          if (m == 0) Y[0] = MFMA32(__builtin_bit_cast(bf16x8, pd[0]), S[0], Y[0]);
          else if (m == 1) Y[2] = MFMA32(__builtin_bit_cast(bf16x8, pn[0]), S[0], Y[2]);
          else if (m == 2) Y[0] = MFMA32(__builtin_bit_cast(bf16x8, pd[1]), S[1], Y[0]);
          else if (m == 3) Y[2] = MFMA32(__builtin_bit_cast(bf16x8, pn[1]), S[1], Y[2]);
          else if (m == 4) G0 = MFMA32(St[0], XAf[0][0], zero16);
          else if (m == 5) G0 = MFMA32(St[1], XAf[1][0], G0);
          else if (m == 6) G0 = MFMA32(ae, b[0], G0);
          else if (m < 11) G0 = MFMA32(as[m - 7], b[(m - 6) % 3], G0);
          else if (m == 11) V0 = MFMA32(as[0], b[(m - 6) % 3], zero16);
          else V0 = MFMA32(as[m - 11], b[(m - 6) % 3], V0);
          SB();
          if (m == 4) { sx = ld128(lX + lo + 3 * 128); re = *(const LAS unsigned*)(size_t)(lE + lo + 3 * 128); }
          if (m >= 3 && m < 6) b[m - 3] = wl[widx(m + 3, 0) * 64];
          if (m >= 6 && m < 12) b[(m - 6) % 3] = wl[widx(m + 3, 0) * 64];
          if (m == 7) glds16x(x, sx[0], cx0, ring0 + ro);
          if (m == 8) glds16x(x, sx[1], cx1, ring0 + ro + 1024);
          if (m == 9) glds16x(x, sx[2], cx0, ring0 + ro + 2048);
          if (m == 10) glds16x(x, sx[3], cx1, ring0 + ro + 3072);
          if (m == 11) glds16e(ES, re, ce, ring0 + ro + 4096);
          ew(m, G1, V1, G0, V0);
          SB();
        }
      }
    };
    for (int t = 0; t < ntiles; t += 2) {
      if (wave >= 4) __builtin_amdgcn_s_setprio(1);
      body(0);
      if (t + 1 >= ntiles) break;
      if (wave >= 4) __builtin_amdgcn_s_setprio(0);
      body(1);
      lS4 += 256; lS1 += 256; lX += 256; lE += 256;
    }
    pq[14] = mulf(0, V1[14], rq[14]);
    pq[15] = mulf(1, V1[15], rq[15]);
    pn[1][3] = pack_bf16(pq[14], pq[15]);
    Y[1] = MFMA32(__builtin_bit_cast(bf16x8, pd[0]), S[0], Y[1]);
    Y[3] = MFMA32(__builtin_bit_cast(bf16x8, pn[0]), S[0], Y[3]);
    Y[1] = MFMA32(__builtin_bit_cast(bf16x8, pd[1]), S[1], Y[1]);
    Y[3] = MFMA32(__builtin_bit_cast(bf16x8, pn[1]), S[1], Y[3]);
    wb0 += LCAP;
    if (__builtin_expect(wb0 >= cnt_all, 1)) break;
    asm volatile("s_waitcnt vmcnt(0)" ::: "memory");
    cnt = min(cnt_all - wb0, LCAP);
    fill_slow(wb0);
    {
      int l2 = lane;
      asm volatile("" : "+v"(l2));
      convert(cnt, la + LRAW + l2 * 8, la + l2 * 4, l2);
    }
    issue_gather(0, 0);
    issue_gather(1, 1);
  }

  if (nodes > 0) {
#pragma unroll
    for (int ct = 0; ct < 2; ++ct) {
      f32x16 U = zero16;
#pragma unroll
      for (int k = 0; k < 4; ++k) U = MFMA32(wl[(18 + k * 2 + ct) * 64], as[k], U);
#pragma unroll
      for (int q = 0; q < 2; ++q) {
        const u32x4 xv = __builtin_bit_cast(u32x4, as[2 * ct + q]);
        u32x4 ov;
#pragma unroll
        for (int j = 0; j < 4; ++j) {
          const int i = 8 * q + 2 * j;
          const float z0 = U[i] + Y[2 + ct][i] * __builtin_amdgcn_rcpf(Y[ct][i] + 1e-6f);
          const float z1 = U[i + 1] + Y[2 + ct][i + 1] * __builtin_amdgcn_rcpf(Y[ct][i + 1] + 1e-6f);
          const float o0 = __uint_as_float(xv[j] << 16) + (z0 > 0.f ? z0 : 0.f);
          const float o1 = __uint_as_float(xv[j] & 0xffff0000u) + (z1 > 0.f ? z1 : 0.f);
          ov[j] = pack_bf16(o0, o1);
        }
        *(LAS u32x4*)(size_t)(la + r * 128 + (((4 * h + 2 * ct + q) ^ (r & 7)) * 16)) = ov;
      }
    }
#pragma unroll
    for (int q = 0; q < 4; ++q) {
      const int row = 8 * q + (lane >> 3);
      const u32x4 ov = *(const LAS u32x4*)(size_t)(la + row * 128 + (((lane & 7) ^ (row & 7)) * 16));
      if (row < nodes) gstore128wt(out, (unsigned)n0 * 128u + q * 1024u + lane * 16u, ov);
    }
  }
  asm volatile("s_waitcnt vmcnt(0)" ::: "memory");
}

extern "C" void kernel_launch(void* const* d_in, const int* in_sizes, int n_in,
                              void* d_out, int out_size, void* d_ws, size_t ws_size,
                              hipStream_t stream) {
  const unsigned short* x = (const unsigned short*)d_in[0];
  const u32x4* eattr = (const u32x4*)d_in[1];
  const int* src = (const int*)d_in[2];
  const int* dst = (const int*)d_in[3];
  const unsigned short* wa = (const unsigned short*)d_in[4];
  const unsigned short* wb = (const unsigned short*)d_in[5];
  const unsigned short* wc = (const unsigned short*)d_in[6];
  const unsigned short* wu = (const unsigned short*)d_in[7];
  const unsigned short* wv = (const unsigned short*)d_in[8];
  unsigned short* out = (unsigned short*)d_out;
  const int N = in_sizes[0] / 64;
  const int E = in_sizes[2];
  const int chunk = (((E + NCH - 1) / NCH) + 3) & ~3;
  const int nbk = (N + 195) / 196;

  char* ws = (char*)d_ws;
  size_t off = 0;
  unsigned short* wpack = (unsigned short*)(ws + off);   off += 64 * 1024;
  u32x4* ENT = (u32x4*)(ws + off);                       off += (size_t)2 * NBK * NCH * CELL;
  unsigned* TS = (unsigned*)(ws + off);

  k_part<<<NCH + NFRAG, 1024, 0, stream>>>(src, dst, E, chunk, ENT, TS, wa, wb, wc, wu, wv, wpack);
  k_main<<<nbk, 512, 0, stream>>>(x, ENT, eattr, TS, chunk, (const bf16x8*)wpack, out, N
     );
}
```

```cpp
#include <hip/hip_runtime.h>
#include <hip/hip_bf16.h>
#include <stdint.h>

using bf16x8 = __attribute__((ext_vector_type(8))) short;
using s16x4 = __attribute__((ext_vector_type(4))) short;
using f32x16 = __attribute__((ext_vector_type(16))) float;
using u32x4 = __attribute__((ext_vector_type(4))) unsigned;
using u32x2 = __attribute__((ext_vector_type(2))) unsigned;
using i32x4 = __attribute__((ext_vector_type(4))) int;
using u16x2 = __attribute__((ext_vector_type(2))) unsigned short;

#define MFMA32(a, b, c) __builtin_amdgcn_mfma_f32_32x32x16_bf16((a), (b), (c), 0, 0, 0)
#define LAS __attribute__((address_space(3)))
#define SB() __builtin_amdgcn_sched_barrier(0)

constexpr int NCH = 256;
constexpr int NBK = 256;
constexpr int NBIN = NBK * 8;
constexpr int NFRAG = 39;
constexpr int CHMAX = 3200;
constexpr float NLOG2E = -1.44269504088896340736f;
constexpr unsigned TPITCH = 80;

__device__ __forceinline__ unsigned pack_bf16(float lo, float hi) {
  typedef __attribute__((ext_vector_type(2))) float f2;
  typedef __attribute__((ext_vector_type(2))) __bf16 b2;
  f2 v = {lo, hi};
  b2 r = __builtin_convertvector(v, b2);
  return __builtin_bit_cast(unsigned, r);
}

__device__ __forceinline__ int wave_scan(int v) {
  v += __builtin_amdgcn_update_dpp(0, v, 0x111, 0xf, 0xf, false);
  v += __builtin_amdgcn_update_dpp(0, v, 0x112, 0xf, 0xf, false);
  v += __builtin_amdgcn_update_dpp(0, v, 0x114, 0xf, 0xf, false);
  v += __builtin_amdgcn_update_dpp(0, v, 0x118, 0xf, 0xf, false);
  v += __builtin_amdgcn_update_dpp(0, v, 0x142, 0xa, 0xf, false);
  v += __builtin_amdgcn_update_dpp(0, v, 0x143, 0xc, 0xf, false);
  return v;
}

__device__ __forceinline__ void glds16(const void* sbase, unsigned voff, unsigned lds_dst) {
  asm volatile(
      "s_mov_b32 m0, %2\n\t"
      "s_nop 0\n\t"
      "global_load_lds_dwordx4 %0, %1"
      :: "v"(voff), "s"(sbase), "s"(lds_dst) : "memory", "m0");
}
__device__ __forceinline__ void glds16nt(const void* sbase, unsigned voff, unsigned lds_dst) {
  asm volatile(
      "s_mov_b32 m0, %2\n\t"
      "s_nop 0\n\t"
      "global_load_lds_dwordx4 %0, %1 nt"
      :: "v"(voff), "s"(sbase), "s"(lds_dst) : "memory", "m0");
}
__device__ __forceinline__ void glds16x(const void* sbase, unsigned a, unsigned c, unsigned lds_dst) {
  unsigned tmp;
  asm volatile(
      "s_mov_b32 m0, %4\n\t"
      "v_mad_u32_u16 %0, %1, %5, %2\n\t"
      "global_load_lds_dwordx4 %0, %3"
      : "=&v"(tmp) : "v"(a), "v"(c), "s"(sbase), "s"(lds_dst), "s"(128u) : "memory", "m0");
}
__device__ __forceinline__ void glds16e(const void* sbase, unsigned a, unsigned c, unsigned lds_dst) {
  unsigned tmp;
  asm volatile(
      "s_mov_b32 m0, %4\n\t"
      "v_add_u32 %0, %1, %2\n\t"
      "global_load_lds_dwordx4 %0, %3"
      : "=&v"(tmp) : "v"(a), "v"(c), "s"(sbase), "s"(lds_dst) : "memory", "m0");
}
__device__ __forceinline__ int gload32(const void* sbase, unsigned voff) {
  int r;
  asm volatile("global_load_dword %0, %1, %2" : "=v"(r) : "v"(voff), "s"(sbase) : "memory");
  return r;
}
__device__ __forceinline__ u32x4 gload128(const void* sbase, unsigned voff) {
  u32x4 r;
  asm volatile("global_load_dwordx4 %0, %1, %2" : "=v"(r) : "v"(voff), "s"(sbase) : "memory");
  return r;
}

__device__ __forceinline__ u32x4 gload128nt(const void* sbase, unsigned voff) {
  u32x4 r;
  asm volatile("global_load_dwordx4 %0, %1, %2 nt" : "=v"(r) : "v"(voff), "s"(sbase) : "memory");
  return r;
}
__device__ __forceinline__ void gstore128wt(void* sbase, unsigned voff, u32x4 v) {
  asm volatile("global_store_dwordx4 %0, %1, %2 sc1\n\ts_nop 1" :: "v"(voff), "v"(v), "s"(sbase) : "memory");
}

constexpr int CELL = 128;
constexpr int CIN = 30;
constexpr int CCAP = 62;
constexpr int CPIT = 272;
constexpr unsigned E2OFF = NBK * NCH * CELL;
constexpr int KP_HDR = 0;
constexpr int KP_OVC = KP_HDR + NBK * 8;
constexpr int KP_ENT = KP_OVC + 16;
constexpr int KP_HIST = KP_ENT + NBK * CPIT;
constexpr int KP_LDS = KP_HIST + NBIN * 4;
constexpr int NWL = 26;
constexpr int NBLOB = 31;

__device__ __forceinline__ unsigned mad24(unsigned a, int b, unsigned c) {
  unsigned r;
  asm("v_mad_i32_i24 %0, %1, %2, %3" : "=v"(r) : "v"(a), "s"(b), "v"(c));
  return r;
}
__device__ __forceinline__ unsigned mul24(unsigned a, unsigned b) {
  unsigned r;
  asm("v_mul_u32_u24 %0, %1, %2" : "=v"(r) : "n"(b), "v"(a));
  return r;
}
__device__ __forceinline__ unsigned mulhi24(unsigned a, unsigned b) {
  return (unsigned)(((unsigned long long)(a & 0xffffffu) * (unsigned long long)b) >> 32);
}
__device__ __forceinline__ unsigned dot4(unsigned a, unsigned b, unsigned c) {
  return __builtin_amdgcn_udot4(a, b, c, false);
}

__global__ __launch_bounds__(1024) void k_part(const int* __restrict__ src, const int* __restrict__ dst,
                                               int E, int chunk, u32x4* __restrict__ ENT,
                                               unsigned* __restrict__ TS,
                                               const unsigned short* __restrict__ wa,
                                               const unsigned short* __restrict__ wb,
                                               const unsigned short* __restrict__ wc,
                                               const unsigned short* __restrict__ wu,
                                               const unsigned short* __restrict__ wv,
                                               unsigned short* __restrict__ wpack) {
  const int tid = threadIdx.x;
#define PT(i)
  PT(0);
  if (blockIdx.x < NCH) {
    __shared__ __attribute__((aligned(16))) char sm[KP_LDS];
    LAS unsigned* hist = (LAS unsigned*)(sm + KP_HIST);
    LAS unsigned* ovc = (LAS unsigned*)(sm + KP_OVC);
    const int c = blockIdx.x;
    const int base = c * chunk;
    const int cnt = max(min(chunk, E - base), 0);
    const int i0 = max(min(base + 4 * tid, min(base + cnt, E) - 4), 0);
    const int lo = base + 4 * tid - i0, hi4 = base + cnt - i0;
    u32x4 d = gload128nt(dst, (unsigned)i0 * 4u);
    u32x4 sv = gload128nt(src, (unsigned)i0 * 4u);
    if (tid < NBK) *(LAS u32x2*)(sm + KP_HDR + tid * 8) = u32x2{0u, 0u};
    if (tid == 0) *(LAS u32x2*)ovc = u32x2{0u, 0u};
    const unsigned sm0 = (unsigned)(size_t)(LAS char*)sm;
    const bool act = __builtin_amdgcn_readfirstlane(tid) * 4 < cnt;
    unsigned long long on[4];
    unsigned tup[4];
#pragma unroll
    for (int k = 0; k < 4; ++k) {
      on[k] = __ballot(k >= lo && k < hi4);
      tup[k] = (unsigned)(i0 + k - base);
    }
    const int bk = tid >> 2, p0 = (tid & 3) * 16;
    unsigned ha = sm0 + KP_HDR + bk * 8, ia = sm0 + KP_ENT + bk * CPIT + p0;
    unsigned go = (unsigned)((bk * NCH + c) * CELL + p0);
    asm volatile("s_waitcnt lgkmcnt(0)\n\ts_barrier\n\ts_waitcnt vmcnt(1)"
                 : "+v"(d), "+v"(tup[0]), "+v"(tup[1]), "+v"(tup[2]), "+v"(tup[3]), "+v"(ha), "+v"(ia), "+v"(go),
                   "+s"(on[0]), "+s"(on[1]), "+s"(on[2]), "+s"(on[3])
                 :: "memory");
    PT(1);
    unsigned gi[4], rk[4], m0[4], m1[4], cb[4];
    if (act) {
      unsigned oh[4], sh[4];
#pragma unroll
      for (int k = 0; k < 4; ++k) {
        gi[k] = mulhi24(d[k] << 8, 684785u);
        sh[k] = gi[k] << 3;
        oh[k] = 1u << (sh[k] & 31u);
        rk[k] = 0u;
      }
      asm volatile(
          "s_mov_b64 exec, %12\n\t"
          "ds_add_rtn_u32 %0, %4, %8\n\t"
          "s_mov_b64 exec, %13\n\t"
          "ds_add_rtn_u32 %1, %5, %9\n\t"
          "s_mov_b64 exec, %14\n\t"
          "ds_add_rtn_u32 %2, %6, %10\n\t"
          "s_mov_b64 exec, %15\n\t"
          "ds_add_rtn_u32 %3, %7, %11\n\t"
          "s_mov_b64 exec, -1"
          : "+&v"(rk[0]), "+&v"(rk[1]), "+&v"(rk[2]), "+&v"(rk[3])
          : "v"((gi[0] & ~3u) + sm0), "v"((gi[1] & ~3u) + sm0), "v"((gi[2] & ~3u) + sm0), "v"((gi[3] & ~3u) + sm0),
            "v"(oh[0]), "v"(oh[1]), "v"(oh[2]), "v"(oh[3]), "s"(on[0]), "s"(on[1]), "s"(on[2]), "s"(on[3])
          : "memory");
#pragma unroll
      for (int k = 0; k < 4; ++k) {
        const unsigned slot = mad24(gi[k], -49 * 128, d[k] << 8) >> 8;
        tup[k] = mad24(slot, 3200, tup[k]);
        const unsigned hm = (unsigned)((int)(gi[k] << 29) >> 31);
        m0[k] = ((oh[k] - 1u) | hm) & 0x04040404u;
        m1[k] = (oh[k] - 1u) & hm & 0x04040404u;
        gi[k] &= ~7u;
        cb[k] = mul24(gi[k], CPIT / 8) + sm0;
        gi[k] += sm0;
      }
      SB();
      asm volatile("s_waitcnt lgkmcnt(0)" : "+v"(rk[0]), "+v"(rk[1]), "+v"(rk[2]), "+v"(rk[3]) :: "memory");
#pragma unroll
      for (int k = 0; k < 4; ++k) asm("v_bfe_u32 %0, %0, %1, 8" : "+v"(rk[k]) : "v"(sh[k]));
      if (max(max(rk[0], rk[1]), max(rk[2], rk[3])) >= (unsigned)CCAP) ovc[1] = 1u;
#pragma unroll
      for (int k = 0; k < 4; ++k) {
        rk[k] = cb[k] + rk[k] * 4;
        cb[k] += CCAP * 4;
      }
    }
    asm volatile("s_waitcnt vmcnt(0)" : "+v"(sv) :: "memory");
#pragma unroll
    for (int k = 0; k < 4; ++k) tup[k] = mad24(sv[k], 81920, tup[k]);
    asm volatile("s_barrier"
                 : "+v"(rk[0]), "+v"(rk[1]), "+v"(rk[2]), "+v"(rk[3]), "+v"(m0[0]), "+v"(m0[1]), "+v"(m0[2]),
                   "+v"(m0[3]), "+v"(m1[0]), "+v"(m1[1]), "+v"(m1[2]), "+v"(m1[3]), "+v"(tup[0]), "+v"(tup[1]),
                   "+v"(tup[2]), "+v"(tup[3]), "+v"(cb[0]), "+v"(cb[1]), "+v"(cb[2]), "+v"(cb[3]), "+v"(gi[0]),
                   "+v"(gi[1]), "+v"(gi[2]), "+v"(gi[3])
                 :: "memory");
    PT(2);
    u32x2 hc = *(const LAS u32x2*)(size_t)ha;
    if (act) {
      u32x2 hd[4];
      unsigned pa[4];
#pragma unroll
      for (int k = 0; k < 4; ++k) hd[k] = *(const LAS u32x2*)(size_t)gi[k];
#pragma unroll
      for (int k = 0; k < 4; ++k) pa[k] = min(dot4(hd[k][0], m0[k], dot4(hd[k][1], m1[k], rk[k])), cb[k]);
      asm volatile(
          "s_mov_b64 exec, %8\n\t"
          "ds_write_b32 %0, %4 offset:%12\n\t"
          "s_mov_b64 exec, %9\n\t"
          "ds_write_b32 %1, %5 offset:%12\n\t"
          "s_mov_b64 exec, %10\n\t"
          "ds_write_b32 %2, %6 offset:%12\n\t"
          "s_mov_b64 exec, %11\n\t"
          "ds_write_b32 %3, %7 offset:%12\n\t"
          "s_mov_b64 exec, -1"
          :: "v"(pa[0]), "v"(pa[1]), "v"(pa[2]), "v"(pa[3]), "v"(tup[0]), "v"(tup[1]), "v"(tup[2]), "v"(tup[3]),
             "s"(on[0]), "s"(on[1]), "s"(on[2]), "s"(on[3]), "n"(KP_ENT + 8)
          : "memory");
    }
    unsigned used = dot4(hc[0], 0x01010101u, dot4(hc[1], 0x01010101u, 0u));
    if (used > (unsigned)CCAP) ovc[1] = 1u;
    asm volatile("s_waitcnt lgkmcnt(0)\n\ts_barrier" ::: "memory");
    PT(3); PT(4);
    const unsigned ovf = ovc[1];
    u32x4 v0 = *(const LAS u32x4*)(size_t)ia;
    u32x4 v1 = *(const LAS u32x4*)(size_t)(ia + 64);
    asm volatile("" : "+v"(v0), "+v"(v1));
    if (__builtin_expect(__builtin_amdgcn_readfirstlane(ovf) != 0u, 0)) {
      *(LAS u32x2*)(hist + tid * 2) = u32x2{0u, 0u};
      __syncthreads();
#pragma unroll
      for (int k = 0; k < 4; ++k) {
        gi[k] = mulhi24(d[k] << 8, 684785u);
        if (k >= lo && k < hi4)
          rk[k] = __hip_atomic_fetch_add(hist + gi[k], 1u, __ATOMIC_RELAXED, __HIP_MEMORY_SCOPE_WORKGROUP);
      }
      __syncthreads();
      if (tid < NBK) {
        unsigned tot = 0;
        for (int i = 0; i < 8; ++i) tot += hist[tid * 8 + i];
        LAS unsigned* cp = (LAS unsigned*)(sm + KP_ENT + tid * CPIT);
        unsigned p = ~0u;
        if (tot > (unsigned)CCAP) {
          p = (unsigned)base + __hip_atomic_fetch_add(ovc, tot, __ATOMIC_RELAXED, __HIP_MEMORY_SCOPE_WORKGROUP);
          *(LAS u32x2*)(sm + KP_HDR + tid * 8) = u32x2{~0u, ~0u};
        }
        for (int i = 0; i < 8; ++i) {
          const unsigned n = hist[tid * 8 + i];
          hist[tid * 8 + i] = p;
          if (p != ~0u) {
            cp[2 + i] = 0u;
            cp[10 + i] = p;
            cp[18 + i] = n;
            p += n;
          }
        }
      }
      __syncthreads();
#pragma unroll
      for (int k = 0; k < 4; ++k) {
        if (k >= lo && k < hi4) {
          const unsigned p = hist[gi[k]];
          if (p != ~0u) TS[p + rk[k]] = tup[k];
        }
      }
      hc = *(const LAS u32x2*)(size_t)ha;
      used = dot4(hc[0], 0x01010101u, dot4(hc[1], 0x01010101u, 0u));
      v0 = *(const LAS u32x4*)(size_t)ia;
      v1 = *(const LAS u32x4*)(size_t)(ia + 64);
    }
    if (p0 == 0) { v0[0] = hc[0]; v0[1] = hc[1]; }
    gstore128wt(ENT, go, v0);
    if (used > 14u) gstore128wt(ENT, go + 64, v1);
    if (__builtin_expect(used > (unsigned)CIN, 0)) {
      gstore128wt(ENT, go + E2OFF, *(const LAS u32x4*)(size_t)(ia + 128));
      gstore128wt(ENT, go + E2OFF + 64, *(const LAS u32x4*)(size_t)(ia + 192));
    }
  } else if (tid < 512) {
    const int f = blockIdx.x - NCH;
    unsigned short v;
    if (f >= NWL && f < NBLOB) {
      const int e = (f - NWL) * 512 + tid;
      const int tb = e >= 32 * (TPITCH / 2);
      const int e2 = e - tb * 32 * (TPITCH / 2);
      const int sl = e2 / (TPITCH / 2), col = e2 - sl * (TPITCH / 2);
      const int nd = tb ? 16 * ((col >> 3) & 1) + 8 * ((col >> 2) & 1) + 4 * (col >> 4) + (col & 3) : col;
      v = (col < 32 && nd == sl) ? 0x3F80 : 0;
    } else {
      const int lane = tid >> 3, j = tid & 7;
      const int n = lane & 31, h = lane >> 5;
      const int g = f < 16 ? f : f < NWL ? f - 18 : f - NBLOB;
      const int ct = g & 1;
      const int col = 32 * ((n >> 2) & 1) + 16 * ct + 4 * (n >> 3) + (n & 3);
      if (f == 16 || f == 17) {
        v = wc[(8 * h + j) * 64 + col];
      } else {
        const int t = (g >> 1) & 3;
        const unsigned short* W = f < 8 ? wb : f < 16 ? wv : f < NWL ? wu : wa;
        v = W[(32 * h + 8 * t + j) * 64 + col];
      }
      if (f < 8 || f == 16 || f == 17 || f >= NBLOB)
        v = (unsigned short)(pack_bf16(__uint_as_float((unsigned)v << 16) * NLOG2E, 0.f) & 0xffffu);
    }
    wpack[f * 512 + tid] = v;
  }
}

constexpr int RING_SLOT = 5 * 1024;
constexpr int SPIT = 1040;
constexpr int LCAP = 512;
constexpr int LARR = 2560;
constexpr int LIST_BYTES = 2 * LARR;
constexpr int LRAW = LIST_BYTES - (LCAP + 8) * 8;
constexpr int OFF_W = 0;
constexpr int OFF_T1 = NWL * 1024;
constexpr int OFF_T2 = OFF_T1 + 32 * TPITCH;
constexpr int OFF_WA = NBLOB * 1024;
constexpr int OFF_R0 = NFRAG * 1024;
constexpr int OFF_R1 = OFF_R0 + 8 * RING_SLOT;
constexpr int OFF_L = OFF_R1 + 8 * RING_SLOT;
constexpr int MAIN_LDS = OFF_L + 8 * LIST_BYTES;
static_assert(MAIN_LDS <= 160 * 1024, "LDS");

__device__ __forceinline__ float add1(float t) { return 1.0f + t; }
__device__ __forceinline__ float mulf(int i, float a, float b) { return (i & 1) ? __builtin_fmaf(a, b, 0.0f) : a * b; }

__global__ __launch_bounds__(512, 2) void k_main(const unsigned short* __restrict__ x,
                                                 const u32x4* __restrict__ ENT,
                                                 const u32x4* __restrict__ ES,
                                                 const unsigned* __restrict__ TS, int chunk,
                                                 const bf16x8* __restrict__ wpack,
                                                 unsigned short* __restrict__ out, int N
                                                 ) {
  __shared__ __attribute__((aligned(16))) char smem[MAIN_LDS];
  const int tid = threadIdx.x, lane = tid & 63;
  const int wave = __builtin_amdgcn_readfirstlane(tid >> 6);
  const int r = lane & 31, h = lane >> 5;
  const int bk = blockIdx.x;
  const int gb = 2 * (wave & 3) + (wave >> 2);
  const int nb0 = bk * 196 + 49 * (wave & 3) + 25 * (wave >> 2);
  const int nodes = max(min(wave < 4 ? 25 : 24, N - nb0), 0);
  const int n0 = min(nb0, N - 1);
  const unsigned lds0 = (unsigned)(size_t)(LAS char*)smem;

  u32x4 xg[4];
  {
    const unsigned vo = lane * 16;
    const char* eb = reinterpret_cast<const char*>(ENT) + (size_t)bk * (NCH * CELL);
#pragma unroll
    for (int i = 0; i < 4; ++i) glds16(eb + (wave * 4 + i) * 1024, vo, lds0 + OFF_R1 + (wave * 4 + i) * SPIT);
    const char* wp = reinterpret_cast<const char*>(wpack);
#pragma unroll
    for (int i = 0; i < 5; ++i) {
      const int f = min(wave + 8 * i, NFRAG - 1);
      glds16(wp + f * 1024, vo, lds0 + OFF_W + f * 1024);
    }
    const unsigned xo = (unsigned)(n0 + max(min(r, nodes - 1), 0)) * 128u + 64u * h;
#pragma unroll
    for (int t = 0; t < 4; ++t) xg[t] = gload128(x, xo + 16 * t);
    if (wave >= 4) __builtin_amdgcn_s_setprio(1);
  }
  LAS char* lbase = (LAS char*)smem + OFF_L + wave * LIST_BYTES;
  const unsigned la = (unsigned)(size_t)lbase;
  auto put = [&](int p, unsigned a, unsigned b) { *(LAS u32x2*)(lbase + LRAW + p * 8) = u32x2{a, b}; };
  unsigned aLX = la + (lane >> 3) * 16;
  unsigned aLE = la + LARR + ((((lane >> 1) & 7) << 2) | (lane >> 4)) * 4;
  unsigned aLS4 = la + (4 * h + ((lane >> 2) & 3)) * 16;
  unsigned aLS1 = la + (((r & 7) << 2) | (r >> 3)) * 4 + 2;
  unsigned cx0 = (unsigned)(((lane & 7) ^ (lane >> 4)) * 16);
  unsigned cx1 = cx0 ^ 64u;
  unsigned ce = (unsigned)((((lane & 1) ^ ((lane >> 4) & 1))) * 16);
  const unsigned ring0 = lds0 + OFF_R0 + wave * RING_SLOT;
  const unsigned ringd = 8 * RING_SLOT;
  unsigned cS = lds0 + OFF_T1 + (((lane >> 4) & 1) * 16 + (lane & 3) * 4) * 2;
  unsigned cSt = lds0 + OFF_T2 + h * 32;
  unsigned fo[4];
#pragma unroll
  for (int t = 0; t < 4; ++t) fo[t] = ring0 + (unsigned)(r * 128 + (((4 * h + t) ^ ((r >> 1) & 7)) * 16));
  unsigned foe = ring0 + (unsigned)(4096 + r * 32 + ((h ^ ((r >> 3) & 1)) * 16));
  unsigned sa = lds0 + OFF_R1 + r * SPIT + h * CELL;
  unsigned cb0 = (unsigned)((8 * r + h) * chunk);
  unsigned aR = la + LRAW + lane * 8, aF = la + lane * 4;
  unsigned aRX = la + LRAW + (lane >> 3) * 32, aRE = la + LRAW + ((((lane >> 1) & 7) << 2) | (lane >> 4)) * 8;
  f32x16 Y[4];
#pragma unroll
  for (int q = 0; q < 4; ++q)
#pragma unroll
    for (int i = 0; i < 16; ++i) Y[q][i] = 0.f;
  f32x16 G0, V0, G1, V1;
  bf16x8 S[2], St[2], as[4], ae, b[3];
  u32x4 pd[2], pn[2];
  float tq[16], rq[16], pq[16];
  auto pipe_zero = [&]() {
#pragma unroll
    for (int j = 0; j < 8; ++j) { S[0][j] = 0; S[1][j] = 0; }
#pragma unroll
    for (int j = 0; j < 4; ++j) { pd[0][j] = 0; pd[1][j] = 0; pn[0][j] = 0; pn[1][j] = 0; }
#pragma unroll
    for (int i = 0; i < 16; ++i) V1[i] = 0.f;
    rq[14] = 0.f; rq[15] = 0.f;
  };
  asm volatile("" : "+v"(aLX), "+v"(aLE), "+v"(aLS4), "+v"(aLS1), "+v"(cx0), "+v"(cx1), "+v"(ce), "+v"(cS), "+v"(cSt),
                    "+v"(fo[0]), "+v"(fo[1]), "+v"(fo[2]), "+v"(fo[3]), "+v"(foe), "+v"(sa), "+v"(cb0), "+v"(aR),
                    "+v"(aF), "+v"(aRX), "+v"(aRE));
  asm volatile("" : "+v"(Y[0]), "+v"(Y[1]), "+v"(Y[2]), "+v"(Y[3]));
  const unsigned mlo = gb >= 4 ? 0x01010101u : 0x01010101u & ((1u << (8 * gb)) - 1u);
  const unsigned mhi = gb <= 4 ? 0u : 0x01010101u & ((1u << (8 * (gb - 4))) - 1u);
  asm volatile("s_waitcnt vmcnt(9)\n\ts_barrier" ::: "memory");
  u32x2 cl[4][4], hd[4];
  int nr[4], st[4], ni[4];
  unsigned long long mk[4][4];
#pragma unroll
  for (int j = 0; j < 4; ++j) hd[j] = *(const LAS u32x2*)(size_t)(sa + 2 * j * CELL);
  auto head = [&](int j) {
    nr[j] = (int)(((gb >= 4 ? hd[j][1] : hd[j][0]) >> (8 * (gb & 3))) & 0xffu);
    st[j] = (int)dot4(hd[j][0], mlo, dot4(hd[j][1], mhi, 0u));
    const int n = min(min(nr[j], 8), CIN - st[j]);
#pragma unroll
    for (int q = 0; q < 4; ++q) mk[j][q] = __ballot(n > 2 * q);
    ni[j] = max(n, 0);
  };
  auto fetch = [&](int j) {
    asm volatile(
        "s_mov_b64 exec, %5\n\t"
        "ds_read2_b32 %0, %4 offset0:%9 offset1:%9+1\n\t"
        "s_mov_b64 exec, %6\n\t"
        "ds_read2_b32 %1, %4 offset0:%9+2 offset1:%9+3\n\t"
        "s_mov_b64 exec, %7\n\t"
        "ds_read2_b32 %2, %4 offset0:%9+4 offset1:%9+5\n\t"
        "s_mov_b64 exec, %8\n\t"
        "ds_read2_b32 %3, %4 offset0:%9+6 offset1:%9+7\n\t"
        "s_mov_b64 exec, -1"
        : "=&v"(cl[j][0]), "=&v"(cl[j][1]), "=&v"(cl[j][2]), "=&v"(cl[j][3])
        : "v"(sa + st[j] * 4), "s"(mk[j][0]), "s"(mk[j][1]), "s"(mk[j][2]), "s"(mk[j][3]), "n"(j * 64 + 2)
        : "memory");
  };
  auto fill = [&](int j, int p) {
    asm volatile(
        "s_mov_b64 exec, %13\n\t"
        "ds_write2_b32 %0, %9, %1 offset0:14 offset1:15\n\t"
        "ds_write2_b32 %0, %8, %1 offset0:12 offset1:13\n\t"
        "s_mov_b64 exec, %12\n\t"
        "ds_write2_b32 %0, %7, %1 offset0:10 offset1:11\n\t"
        "ds_write2_b32 %0, %6, %1 offset0:8 offset1:9\n\t"
        "s_mov_b64 exec, %11\n\t"
        "ds_write2_b32 %0, %5, %1 offset0:6 offset1:7\n\t"
        "ds_write2_b32 %0, %4, %1 offset0:4 offset1:5\n\t"
        "s_mov_b64 exec, %10\n\t"
        "ds_write2_b32 %0, %3, %1 offset0:2 offset1:3\n\t"
        "ds_write2_b32 %0, %2, %1 offset0:0 offset1:1\n\t"
        "s_mov_b64 exec, -1"
        :: "v"(la + LRAW + p * 8), "v"(cb0 + 2 * j * chunk), "v"(cl[j][0][0]), "v"(cl[j][0][1]), "v"(cl[j][1][0]),
           "v"(cl[j][1][1]), "v"(cl[j][2][0]), "v"(cl[j][2][1]), "v"(cl[j][3][0]), "v"(cl[j][3][1]), "s"(mk[j][0]),
           "s"(mk[j][1]), "s"(mk[j][2]), "s"(mk[j][3])
        : "memory");
  };
  auto src_of = [&](unsigned v) { return mulhi24(v >> 8, 13421773u); };
  auto decode = [&](unsigned v, unsigned cb, unsigned& a, unsigned& e) {
    unsigned sl;
    asm("v_lshrrev_b32 %0, 8, %3\n\t"
        "v_mul_hi_u32_u24 %0, 0xcccccd, %0\n\t"
        "v_mad_i32_i24 %1, %0, %5, %3\n\t"
        "v_mul_hi_u32_u24 %2, 0x147ae2, %1\n\t"
        "v_add_lshl_u32 %1, %1, %4, 5\n\t"
        "v_mad_i32_i24 %0, %2, %6, %0\n\t"
        "v_mad_i32_i24 %1, %2, %7, %1"
        : "=&v"(a), "=&v"(e), "=&v"(sl)
        : "v"(v), "v"(cb), "s"(-81920), "s"((int)(TPITCH << 16)), "s"(-102400));
  };
  auto decode2 = [&](const u32x2 p, const u32x2 q, unsigned& a, unsigned& e, unsigned& c, unsigned& f) {
    unsigned sl, sm;
    asm("v_lshrrev_b32 %0, 8, %6\n\t"
        "v_lshrrev_b32 %3, 8, %8\n\t"
        "v_mul_hi_u32_u24 %0, 0xcccccd, %0\n\t"
        "v_mul_hi_u32_u24 %3, 0xcccccd, %3\n\t"
        "v_mad_i32_i24 %1, %0, %10, %6\n\t"
        "v_mad_i32_i24 %4, %3, %10, %8\n\t"
        "v_mul_hi_u32_u24 %2, 0x147ae2, %1\n\t"
        "v_mul_hi_u32_u24 %5, 0x147ae2, %4\n\t"
        "v_add_lshl_u32 %1, %1, %7, 5\n\t"
        "v_add_lshl_u32 %4, %4, %9, 5\n\t"
        "v_mad_i32_i24 %0, %2, %11, %0\n\t"
        "v_mad_i32_i24 %3, %5, %11, %3\n\t"
        "v_mad_i32_i24 %1, %2, %12, %1\n\t"
        "v_mad_i32_i24 %4, %5, %12, %4"
        : "=&v"(a), "=&v"(e), "=&v"(sl), "=&v"(c), "=&v"(f), "=&v"(sm)
        : "v"(p[0]), "v"(p[1]), "v"(q[0]), "v"(q[1]), "s"(-81920), "s"((int)(TPITCH << 16)), "s"(-102400));
  };
  auto ld128 = [&](unsigned a) { return *(const LAS u32x4*)(size_t)a; };
  auto gather5 = [&](const u32x4 sx, unsigned re, int rs) {
    const unsigned dstb = ring0 + rs * ringd;
    glds16x(x, sx[0], cx0, dstb);
    glds16x(x, sx[1], cx1, dstb + 1024);
    glds16x(x, sx[2], cx0, dstb + 2048);
    glds16x(x, sx[3], cx1, dstb + 3072);
    glds16e(ES, re, ce, dstb + 4096);
  };
  auto issue_gather = [&](int tile, int rs) {
    gather5(ld128(aLX + tile * 128), *(const LAS unsigned*)(size_t)(aLE + tile * 128), rs);
  };
  head(0);
  fetch(0);
  SB();
  const int s0 = wave_scan(ni[0]);
  head(1);
  head(2);
  head(3);
  const int tot0 = __builtin_amdgcn_readlane(s0, 63);
  const int p0 = s0 - ni[0];
  SB();
  asm volatile("s_waitcnt lgkmcnt(0)" : "+v"(cl[0][0]), "+v"(cl[0][1]), "+v"(cl[0][2]), "+v"(cl[0][3]) :: "memory");
  fill(0, p0);
  bool early = tot0 >= 32;
  int s123;
  {
    unsigned v0 = *(const LAS unsigned*)(size_t)aRX, v1 = *(const LAS unsigned*)(size_t)(aRX + 8);
    unsigned v2 = *(const LAS unsigned*)(size_t)(aRX + 16), v3 = *(const LAS unsigned*)(size_t)(aRX + 24);
    u32x2 ve = *(const LAS u32x2*)(size_t)aRE;
    SB();
    s123 = wave_scan(ni[1] | (ni[2] << 10) | (ni[3] << 20));
    SB();
    asm volatile("" : "+v"(v0), "+v"(v1), "+v"(v2), "+v"(v3), "+v"(ve), "+v"(s123));
    if (__builtin_expect(early, 1)) {
      unsigned a, e;
      decode(ve[0], ve[1], a, e);
      gather5(u32x4{src_of(v0), src_of(v1), src_of(v2), src_of(v3)}, e, 0);
    } else {
      gather5(u32x4{0u, 0u, 0u, 0u}, 0u, 0);
    }
  }
  fetch(1);
  fetch(2);
  fetch(3);
  const int e123 = __builtin_amdgcn_readlane(s123, 63);
  const int tot1 = tot0 + (e123 & 0x3ff), tot2 = tot1 + ((e123 >> 10) & 0x3ff);
  int cnt_all = tot2 + (e123 >> 20);
  asm volatile("s_waitcnt vmcnt(5) lgkmcnt(0)\n\ts_barrier"
               : "+v"(xg[0]), "+v"(xg[1]), "+v"(xg[2]), "+v"(xg[3]),
                 "+v"(cl[1][0]), "+v"(cl[1][1]), "+v"(cl[1][2]), "+v"(cl[1][3]), "+v"(cl[2][0]), "+v"(cl[2][1]),
                 "+v"(cl[2][2]), "+v"(cl[2][3]), "+v"(cl[3][0]), "+v"(cl[3][1]), "+v"(cl[3][2]), "+v"(cl[3][3])
               :: "memory");
  const LAS bf16x8* wl = (const LAS bf16x8*)(smem + OFF_W) + lane;
  const f32x16 zero16 = {0.f, 0.f, 0.f, 0.f, 0.f, 0.f, 0.f, 0.f, 0.f, 0.f, 0.f, 0.f, 0.f, 0.f, 0.f, 0.f};
  f32x16 XA[2] = {zero16, zero16};
  bf16x8 wa[8];
#pragma unroll
  for (int k = 0; k < 8; ++k) wa[k] = wl[(NBLOB + k) * 64];
  SB();
  auto xa_step = [&](int k) {
#pragma unroll
    for (int ct = 0; ct < 2; ++ct) XA[ct] = MFMA32(__builtin_bit_cast(bf16x8, xg[k]), wa[k * 2 + ct], XA[ct]);
    SB();
  };
  if (__builtin_expect(cnt_all > LCAP, 0)) {
#pragma unroll
    for (int j = 1; j < 4; ++j)
#pragma unroll
      for (int q = 0; q < 4; ++q) mk[j][q] = 0ull;
  }
  fill(1, tot0 + (s123 & 0x3ff) - ni[1]);
  SB();
  xa_step(0);
  fill(2, tot1 + ((s123 >> 10) & 0x3ff) - ni[2]);
  SB();
  xa_step(1);
  fill(3, tot2 + (s123 >> 20) - ni[3]);
  SB();
  xa_step(2);
  xa_step(3);
  if (__builtin_expect(__ballot(nr[0] + nr[1] + nr[2] + nr[3] != ni[0] + ni[1] + ni[2] + ni[3]) != 0ull, 0)) {
#pragma unroll
    for (int j = 0; j < 4; ++j) {
      unsigned long long om = __ballot(nr[j] != ni[j]);
      while (om) {
        const int l = __builtin_ctzll(om);
        om &= om - 1;
        const int n = __builtin_amdgcn_readlane(nr[j] - ni[j], l);
        const int p = __builtin_amdgcn_readlane(st[j] + ni[j], l) + lane;
        const int cc = 8 * (l & 31) + (l >> 5) + 2 * j;
        if (n > CCAP) cnt_all = LCAP;
        if (cnt_all + n <= LCAP && lane < n) {
          const unsigned* cp = reinterpret_cast<const unsigned*>(reinterpret_cast<const char*>(ENT) +
                                                                 ((size_t)bk * NCH + cc) * CELL);
          put(cnt_all + lane, p < CIN ? cp[2 + p] : cp[E2OFF / 4 + p - CIN], (unsigned)(cc * chunk));
        }
        cnt_all += n;
      }
    }
    __builtin_amdgcn_s_waitcnt(0x0F70);
  }
  const int gbin = bk * 8 + wave;
  auto fill_slow = [&](int wb0) __attribute__((always_inline)) {
    int nn[4], sp[4], tot = 0;
    const unsigned* rp[4];
    int l2;
    asm volatile("v_mbcnt_lo_u32_b32 %0, -1, 0\n\tv_mbcnt_hi_u32_b32 %0, -1, %0" : "=v"(l2));
    const int ch0 = 8 * (l2 & 31) + (l2 >> 5);
#pragma unroll
    for (int j = 0; j < 4; ++j) {
      const unsigned* cp = reinterpret_cast<const unsigned*>(reinterpret_cast<const char*>(ENT) +
                                                             ((size_t)bk * NCH + ch0 + 2 * j) * CELL);
      const unsigned h0 = cp[0], h1 = cp[1];
      nn[j] = (int)(((gb >= 4 ? h1 : h0) >> (8 * (gb & 3))) & 0xffu);
      sp[j] = (int)dot4(h0, mlo, dot4(h1, mhi, 0u));
      rp[j] = cp + 2 + sp[j];
      sp[j] = CIN - sp[j];
      if (nn[j] == 0xff) {
        rp[j] = TS + cp[10 + gb];
        nn[j] = (int)cp[18 + gb];
        sp[j] = nn[j];
      }
      tot += nn[j];
    }
    const int sc = wave_scan(tot);
    int pp = sc - tot - wb0;
#pragma unroll
    for (int j = 0; j < 4; ++j) {
      const int cb = (ch0 + 2 * j) * chunk;
      const int te = min(nn[j], LCAP - pp);
#pragma unroll 1
      for (int t = max(0, -pp); t < te; ++t) put(pp + t, rp[j][t < sp[j] ? t : t + (int)(E2OFF / 4) - 32], (unsigned)cb);
      pp += nn[j];
    }
    __builtin_amdgcn_s_waitcnt(0x0F70);
    return __builtin_amdgcn_readlane(sc, 63);
  };
  if (__builtin_expect(cnt_all > LCAP, 0)) {
    asm volatile("s_waitcnt vmcnt(0)" ::: "memory");
    cnt_all = fill_slow(0);
    early = false;
  }
  cnt_all = __builtin_amdgcn_readfirstlane(cnt_all);
  int cnt = min(cnt_all, LCAP);
  auto convert = [&](int cnt, unsigned aR, unsigned aF, int lane) __attribute__((always_inline)) {
    u32x2 v[8];
#pragma unroll
    for (int k = 0; k < 8; ++k) v[k] = *(const LAS u32x2*)(size_t)(aR + k * 512);
    asm volatile("" ::: "memory");
#pragma unroll
    for (int k = 0; k < 6; k += 2) {
      unsigned a, e, c, f;
      decode2(v[k], v[k + 1], a, e, c, f);
      *(LAS unsigned*)(size_t)(aF + k * 256) = a;
      *(LAS unsigned*)(size_t)(aF + k * 256 + 256) = c;
      *(LAS unsigned*)(size_t)(aF + k * 256 + LARR) = e;
      *(LAS unsigned*)(size_t)(aF + k * 256 + LARR + 256) = f;
    }
#pragma unroll
    for (int k = 6; k < 8; ++k) {
      if (cnt > k * 64) {
        unsigned a, e;
        decode(v[k][0], v[k][1], a, e);
        *(LAS unsigned*)(size_t)(aF + k * 256) = a;
        *(LAS unsigned*)(size_t)(aF + k * 256 + LARR) = e;
      }
    }
    const int u = cnt + lane;
    const unsigned padA = (unsigned)(n0 + max(min(((u >> 2) & 7) + 8 * (u & 3), nodes - 1), 0)) | (31u * TPITCH << 16);
    const unsigned pa = aF + cnt * 4;
    *(LAS unsigned*)(size_t)pa = padA;
    *(LAS unsigned*)(size_t)(pa + 256) = padA;
    *(LAS unsigned*)(size_t)(pa + LARR) = 0u;
    *(LAS unsigned*)(size_t)(pa + LARR + 256) = 0u;
  };
  convert(cnt, aR, aF, lane);
  issue_gather(1, 1);
  if (__builtin_expect(!early, 0)) {
    asm volatile("s_waitcnt vmcnt(0)" ::: "memory");
    issue_gather(0, 0);
    issue_gather(1, 1);
  }
  bf16x8 XAf[2][2];
#pragma unroll
  for (int ct = 0; ct < 2; ++ct)
#pragma unroll
    for (int s = 0; s < 2; ++s) {
      u32x4 p;
#pragma unroll
      for (int j = 0; j < 4; ++j) p[j] = pack_bf16(XA[ct][8 * s + 2 * j], XA[ct][8 * s + 2 * j + 1]);
      XAf[s][ct] = __builtin_bit_cast(bf16x8, p);
    }

  auto rd128 = [&](unsigned a) { return *(const LAS bf16x8*)(size_t)a; };
  auto load_S = [&](const u32x4 ss) {
    const s16x4 a0 = __builtin_amdgcn_ds_read_tr16_b64_v4i16((LAS s16x4*)(size_t)((ss[0] >> 16) + cS));
    const s16x4 a1 = __builtin_amdgcn_ds_read_tr16_b64_v4i16((LAS s16x4*)(size_t)((ss[1] >> 16) + cS));
    const s16x4 a2 = __builtin_amdgcn_ds_read_tr16_b64_v4i16((LAS s16x4*)(size_t)((ss[2] >> 16) + cS));
    const s16x4 a3 = __builtin_amdgcn_ds_read_tr16_b64_v4i16((LAS s16x4*)(size_t)((ss[3] >> 16) + cS));
    S[0] = __builtin_shufflevector(a0, a1, 0, 1, 2, 3, 4, 5, 6, 7);
    S[1] = __builtin_shufflevector(a2, a3, 0, 1, 2, 3, 4, 5, 6, 7);
  };
  auto widx = [&](int m, int ct) { return m == 6 ? 16 + ct : m < 11 ? (m - 7) * 2 + ct : 8 + (m - 11) * 2 + ct; };

#define HELD ((1 << 6) | (1 << 8) | (1 << 10) | (1 << 12) | (1 << 14))
  bf16x8 wr[2][15];
#pragma unroll
  for (int ct = 0; ct < 2; ++ct)
#pragma unroll
    for (int m = 6; m < 15; ++m)
      if ((HELD >> m) & 1) {
        wr[ct][m] = wl[widx(m, ct) * 64];
        asm volatile("" : "+v"(wr[ct][m]));
      }
  auto wf = [&](int m, int ct) { return ((HELD >> m) & 1) ? wr[ct][m] : b[(m - 6) % 3]; };

  auto ew = [&](int m, const f32x16& Gc, const f32x16& Vc, const f32x16& Gn, const f32x16& Vp) {
    if (m < 2) {
      const int i = 14 + m;
      pq[i] = mulf(i, Vp[i], rq[i]);
      if (m == 1) pn[1][3] = pack_bf16(pq[14], pq[15]);
    } else if (m == 2) {
      pq[0] = mulf(0, Vc[0], rq[0]);
      pq[1] = mulf(1, Vc[1], rq[1]);
      pn[0][0] = pack_bf16(pq[0], pq[1]);
    } else {
      const int i = m - 1;
      pq[i] = mulf(i, Vc[i], rq[i]);
      if (i & 1) pn[i >> 3][(i >> 1) & 3] = pack_bf16(pq[i - 1], pq[i]);
    }
    if (m == 0) pd[0][0] = pack_bf16(rq[0], rq[1]);
    else if ((m & 1) == 0) { const int i = m + 1; pd[i >> 3][(i >> 1) & 3] = pack_bf16(rq[i - 1], rq[i]); }
    if (m < 14) {
      const int i = m + 2;
      rq[i] = __builtin_amdgcn_rcpf(add1(tq[i]));
    } else {
      rq[0] = __builtin_amdgcn_rcpf(add1(tq[0]));
      rq[1] = __builtin_amdgcn_rcpf(add1(tq[1]));
    }
    if (m < 13) tq[m + 3] = __builtin_amdgcn_exp2f(Gc[m + 3]);
    else if (m == 13) { tq[0] = __builtin_amdgcn_exp2f(Gn[0]); tq[1] = __builtin_amdgcn_exp2f(Gn[1]); }
    else tq[2] = __builtin_amdgcn_exp2f(Gn[2]);
  };

  int wb0 = 0;
  while (true) {
    const int ntiles = (cnt + 31) >> 5;
    asm volatile("s_waitcnt vmcnt(5)" ::: "memory");
    {
      const unsigned sl = *(const LAS unsigned short*)(size_t)aLS1;
#pragma unroll
      for (int k = 0; k < 4; ++k) as[k] = rd128(fo[k]);
      ae = rd128(foe);
      St[0] = rd128(sl + cSt);
      St[1] = rd128(sl + cSt + 16);
      G0 = MFMA32(St[0], XAf[0][0], zero16);
      G0 = MFMA32(St[1], XAf[1][0], G0);
      G0 = MFMA32(ae, wl[widx(6, 0) * 64], G0);
#pragma unroll
      for (int k = 0; k < 4; ++k) G0 = MFMA32(as[k], wl[widx(7 + k, 0) * 64], G0);
      V0 = MFMA32(as[0], wl[widx(11, 0) * 64], zero16);
#pragma unroll
      for (int k = 1; k < 4; ++k) V0 = MFMA32(as[k], wl[widx(11 + k, 0) * 64], V0);
      asm volatile("s_waitcnt lgkmcnt(0)" ::: "memory");
      issue_gather(2, 0);
      pipe_zero();
      tq[0] = __builtin_amdgcn_exp2f(G0[0]);
      tq[1] = __builtin_amdgcn_exp2f(G0[1]);
      tq[2] = __builtin_amdgcn_exp2f(G0[2]);
      rq[0] = __builtin_amdgcn_rcpf(add1(tq[0]));
      rq[1] = __builtin_amdgcn_rcpf(add1(tq[1]));
      SB();
    }
    unsigned &lS4 = aLS4, &lS1 = aLS1, &lX = aLX, &lE = aLE;
    int adv = 0;
    auto body = [&](const int par) {
      const unsigned lo = par * 128;
      const unsigned ro = (1 - par) * ringd;
      {
        u32x4 ss;
        unsigned sl;
#pragma unroll
        for (int m = 0; m < 15; ++m) {
          if (m == 0) Y[1] = MFMA32(__builtin_bit_cast(bf16x8, pd[0]), S[0], Y[1]);
          else if (m == 1) Y[3] = MFMA32(__builtin_bit_cast(bf16x8, pn[0]), S[0], Y[3]);
          else if (m == 2) Y[1] = MFMA32(__builtin_bit_cast(bf16x8, pd[1]), S[1], Y[1]);
          else if (m == 3) Y[3] = MFMA32(__builtin_bit_cast(bf16x8, pn[1]), S[1], Y[3]);
          else if (m == 4) G1 = MFMA32(St[0], XAf[0][1], zero16);
          else if (m == 5) G1 = MFMA32(St[1], XAf[1][1], G1);
          else if (m == 6) G1 = MFMA32(ae, wf(m, 1), G1);
          else if (m < 11) G1 = MFMA32(as[m - 7], wf(m, 1), G1);
          else if (m == 11) V1 = MFMA32(as[0], wf(m, 1), zero16);
          else V1 = MFMA32(as[m - 11], wf(m, 1), V1);
          SB();
          if (m == 0) { ss = ld128(lS4 + lo); sl = *(const LAS unsigned short*)(size_t)(lS1 + lo + 128); }
          if (m >= 3 && m < 12 && !((HELD >> (m + 3)) & 1)) b[(m - 3) % 3] = wl[widx(m + 3, 1) * 64];
          if (m == 4) load_S(ss);
          if (m == 5) { St[0] = rd128(sl + cSt); St[1] = rd128(sl + cSt + 16); }
          if (m == 6) { asm volatile("s_waitcnt vmcnt(5)" ::: "memory"); ae = rd128(foe + ro); }
          if (m >= 11) as[m - 11] = rd128(fo[m - 11] + ro);
          ew(m, G0, V0, G1, V1);
          SB();
        }
      }
      {
        u32x4 sx;
        unsigned re;
#pragma unroll
        for (int m = 0; m < 15; ++m) {
          if (m == 0) Y[0] = MFMA32(__builtin_bit_cast(bf16x8, pd[0]), S[0], Y[0]);
          else if (m == 1) Y[2] = MFMA32(__builtin_bit_cast(bf16x8, pn[0]), S[0], Y[2]);
          else if (m == 2) Y[0] = MFMA32(__builtin_bit_cast(bf16x8, pd[1]), S[1], Y[0]);
          else if (m == 3) Y[2] = MFMA32(__builtin_bit_cast(bf16x8, pn[1]), S[1], Y[2]);
          else if (m == 4) G0 = MFMA32(St[0], XAf[0][0], zero16);
          else if (m == 5) G0 = MFMA32(St[1], XAf[1][0], G0);
          else if (m == 6) G0 = MFMA32(ae, wf(m, 0), G0);
          else if (m < 11) G0 = MFMA32(as[m - 7], wf(m, 0), G0);
          else if (m == 11) V0 = MFMA32(as[0], wf(m, 0), zero16);
          else V0 = MFMA32(as[m - 11], wf(m, 0), V0);
          SB();
          if (m == 4) { sx = ld128(lX + lo + 3 * 128); re = *(const LAS unsigned*)(size_t)(lE + lo + 3 * 128); }
          if (m >= 3 && m < 12 && !((HELD >> (m + 3)) & 1)) b[(m - 3) % 3] = wl[widx(m + 3, 0) * 64];
          if (m == 7) glds16x(x, sx[0], cx0, ring0 + ro);
          if (m == 8) glds16x(x, sx[1], cx1, ring0 + ro + 1024);
          if (m == 9) glds16x(x, sx[2], cx0, ring0 + ro + 2048);
          if (m == 10) glds16x(x, sx[3], cx1, ring0 + ro + 3072);
          if (m == 11) glds16e(ES, re, ce, ring0 + ro + 4096);
          ew(m, G1, V1, G0, V0);
          SB();
        }
      }
    };
    for (int t = 0; t < ntiles; t += 2) {
      asm volatile("s_cmp_lt_u32 %0, 4\n\ts_cbranch_scc1 1f\n\ts_setprio 1\n1:" :: "s"(wave) : "scc");
      body(0);
      if (t + 1 >= ntiles) break;
      __builtin_amdgcn_s_setprio(0);
      body(1);
      lS4 += 256; lS1 += 256; lX += 256; lE += 256;
      asm volatile("" : "+v"(lS4), "+v"(lS1), "+v"(lX), "+v"(lE));
      adv += 256;
    }
    pq[14] = mulf(0, V1[14], rq[14]);
    pq[15] = mulf(1, V1[15], rq[15]);
    pn[1][3] = pack_bf16(pq[14], pq[15]);
    Y[1] = MFMA32(__builtin_bit_cast(bf16x8, pd[0]), S[0], Y[1]);
    Y[3] = MFMA32(__builtin_bit_cast(bf16x8, pn[0]), S[0], Y[3]);
    Y[1] = MFMA32(__builtin_bit_cast(bf16x8, pd[1]), S[1], Y[1]);
    Y[3] = MFMA32(__builtin_bit_cast(bf16x8, pn[1]), S[1], Y[3]);
    wb0 += LCAP;
    if (__builtin_expect(wb0 >= cnt_all, 1)) break;
    asm volatile("s_waitcnt vmcnt(0)" ::: "memory");
    cnt = min(cnt_all - wb0, LCAP);
    lS4 -= adv; lS1 -= adv; lX -= adv; lE -= adv;
    fill_slow(wb0);
    {
      int l2;
      asm volatile("v_mbcnt_lo_u32_b32 %0, -1, 0\n\tv_mbcnt_hi_u32_b32 %0, -1, %0" : "=v"(l2));
      convert(cnt, la + LRAW + l2 * 8, la + l2 * 4, l2);
    }
    issue_gather(0, 0);
    issue_gather(1, 1);
  }

  if (nodes > 0) {
    int lane;
    asm volatile("v_mbcnt_lo_u32_b32 %0, -1, 0\n\tv_mbcnt_hi_u32_b32 %0, -1, %0" : "=v"(lane));
    const int r = lane & 31, h = lane >> 5;
#pragma unroll
    for (int ct = 0; ct < 2; ++ct) {
      f32x16 U = zero16;
#pragma unroll
      for (int k = 0; k < 4; ++k) U = MFMA32(wl[(18 + k * 2 + ct) * 64], as[k], U);
#pragma unroll
      for (int q = 0; q < 2; ++q) {
        const u32x4 xv = __builtin_bit_cast(u32x4, as[2 * ct + q]);
        u32x4 ov;
#pragma unroll
        for (int j = 0; j < 4; ++j) {
          const int i = 8 * q + 2 * j;
          const float z0 = U[i] + Y[2 + ct][i] * __builtin_amdgcn_rcpf(Y[ct][i] + 1e-6f);
          const float z1 = U[i + 1] + Y[2 + ct][i + 1] * __builtin_amdgcn_rcpf(Y[ct][i + 1] + 1e-6f);
          const float o0 = __uint_as_float(xv[j] << 16) + (z0 > 0.f ? z0 : 0.f);
          const float o1 = __uint_as_float(xv[j] & 0xffff0000u) + (z1 > 0.f ? z1 : 0.f);
          ov[j] = pack_bf16(o0, o1);
        }
        *(LAS u32x4*)(size_t)(la + r * 128 + (((4 * h + 2 * ct + q) ^ (r & 7)) * 16)) = ov;
      }
    }
#pragma unroll
    for (int q = 0; q < 4; ++q) {
      const int row = 8 * q + (lane >> 3);
      const u32x4 ov = *(const LAS u32x4*)(size_t)(la + row * 128 + (((lane & 7) ^ (row & 7)) * 16));
      if (row < nodes) gstore128wt(out, (unsigned)n0 * 128u + q * 1024u + lane * 16u, ov);
    }
  }
  asm volatile("s_waitcnt vmcnt(0)" ::: "memory");
}

extern "C" void kernel_launch(void* const* d_in, const int* in_sizes, int n_in,
                              void* d_out, int out_size, void* d_ws, size_t ws_size,
                              hipStream_t stream) {
  const unsigned short* x = (const unsigned short*)d_in[0];
  const u32x4* eattr = (const u32x4*)d_in[1];
  const int* src = (const int*)d_in[2];
  const int* dst = (const int*)d_in[3];
  const unsigned short* wa = (const unsigned short*)d_in[4];
  const unsigned short* wb = (const unsigned short*)d_in[5];
  const unsigned short* wc = (const unsigned short*)d_in[6];
  const unsigned short* wu = (const unsigned short*)d_in[7];
  const unsigned short* wv = (const unsigned short*)d_in[8];
  unsigned short* out = (unsigned short*)d_out;
  const int N = in_sizes[0] / 64;
  const int E = in_sizes[2];
  const int chunk = (((E + NCH - 1) / NCH) + 3) & ~3;
  const int nbk = (N + 195) / 196;

  char* ws = (char*)d_ws;
  size_t off = 0;
  unsigned short* wpack = (unsigned short*)(ws + off);   off += 64 * 1024;
  u32x4* ENT = (u32x4*)(ws + off);                       off += (size_t)2 * NBK * NCH * CELL;
  unsigned* TS = (unsigned*)(ws + off);

  k_part<<<NCH + NFRAG, 1024, 0, stream>>>(src, dst, E, chunk, ENT, TS, wa, wb, wc, wu, wv, wpack);
  k_main<<<nbk, 512, 0, stream>>>(x, ENT, eattr, TS, chunk, (const bf16x8*)wpack, out, N
     );
}
```

```cpp
#include <hip/hip_runtime.h>
#include <hip/hip_bf16.h>
#include <stdint.h>

using bf16x8 = __attribute__((ext_vector_type(8))) short;
using s16x4 = __attribute__((ext_vector_type(4))) short;
using f32x16 = __attribute__((ext_vector_type(16))) float;
using u32x4 = __attribute__((ext_vector_type(4))) unsigned;
using u32x2 = __attribute__((ext_vector_type(2))) unsigned;
using i32x4 = __attribute__((ext_vector_type(4))) int;
using u16x2 = __attribute__((ext_vector_type(2))) unsigned short;

#define MFMA32(a, b, c) __builtin_amdgcn_mfma_f32_32x32x16_bf16((a), (b), (c), 0, 0, 0)
#define LAS __attribute__((address_space(3)))
#define RANKOF(w) ((w) < 4 ? 7 - (w) : (w) - 4)
#define SB() __builtin_amdgcn_sched_barrier(0)

constexpr int NCH = 256;
constexpr int NBK = 256;
constexpr int NBIN = NBK * 8;
constexpr int NFRAG = 39;
constexpr int CHMAX = 3200;
constexpr float NLOG2E = -1.44269504088896340736f;
constexpr unsigned TPITCH = 80;

__device__ __forceinline__ unsigned pack_bf16(float lo, float hi) {
  typedef __attribute__((ext_vector_type(2))) float f2;
  typedef __attribute__((ext_vector_type(2))) __bf16 b2;
  f2 v = {lo, hi};
  b2 r = __builtin_convertvector(v, b2);
  return __builtin_bit_cast(unsigned, r);
}

__device__ __forceinline__ int wave_scan(int v) {
  v += __builtin_amdgcn_update_dpp(0, v, 0x111, 0xf, 0xf, false);
  v += __builtin_amdgcn_update_dpp(0, v, 0x112, 0xf, 0xf, false);
  v += __builtin_amdgcn_update_dpp(0, v, 0x114, 0xf, 0xf, false);
  v += __builtin_amdgcn_update_dpp(0, v, 0x118, 0xf, 0xf, false);
  v += __builtin_amdgcn_update_dpp(0, v, 0x142, 0xa, 0xf, false);
  v += __builtin_amdgcn_update_dpp(0, v, 0x143, 0xc, 0xf, false);
  return v;
}

__device__ __forceinline__ void glds16(const void* sbase, unsigned voff, unsigned lds_dst) {
  asm volatile(
      "s_mov_b32 m0, %2\n\t"
      "s_nop 0\n\t"
      "global_load_lds_dwordx4 %0, %1"
      :: "v"(voff), "s"(sbase), "s"(lds_dst) : "memory", "m0");
}
__device__ __forceinline__ void glds16nt(const void* sbase, unsigned voff, unsigned lds_dst) {
  asm volatile(
      "s_mov_b32 m0, %2\n\t"
      "s_nop 0\n\t"
      "global_load_lds_dwordx4 %0, %1 nt"
      :: "v"(voff), "s"(sbase), "s"(lds_dst) : "memory", "m0");
}
__device__ __forceinline__ void glds16x(const void* sbase, unsigned a, unsigned c, unsigned lds_dst) {
  unsigned tmp;
  asm volatile(
      "s_mov_b32 m0, %4\n\t"
      "v_mad_u32_u16 %0, %1, %5, %2\n\t"
      "global_load_lds_dwordx4 %0, %3"
      : "=&v"(tmp) : "v"(a), "v"(c), "s"(sbase), "s"(lds_dst), "s"(128u) : "memory", "m0");
}
__device__ __forceinline__ void glds16e(const void* sbase, unsigned a, unsigned c, unsigned lds_dst) {
  unsigned tmp;
  asm volatile(
      "s_mov_b32 m0, %4\n\t"
      "v_add_u32 %0, %1, %2\n\t"
      "global_load_lds_dwordx4 %0, %3"
      : "=&v"(tmp) : "v"(a), "v"(c), "s"(sbase), "s"(lds_dst) : "memory", "m0");
}
__device__ __forceinline__ int gload32(const void* sbase, unsigned voff) {
  int r;
  asm volatile("global_load_dword %0, %1, %2" : "=v"(r) : "v"(voff), "s"(sbase) : "memory");
  return r;
}
__device__ __forceinline__ u32x4 gload128(const void* sbase, unsigned voff) {
  u32x4 r;
  asm volatile("global_load_dwordx4 %0, %1, %2" : "=v"(r) : "v"(voff), "s"(sbase) : "memory");
  return r;
}

__device__ __forceinline__ u32x4 gload128nt(const void* sbase, unsigned voff) {
  u32x4 r;
  asm volatile("global_load_dwordx4 %0, %1, %2 nt" : "=v"(r) : "v"(voff), "s"(sbase) : "memory");
  return r;
}
__device__ __forceinline__ void gstore128wt(void* sbase, unsigned voff, u32x4 v) {
  asm volatile("global_store_dwordx4 %0, %1, %2 sc1\n\ts_nop 1" :: "v"(voff), "v"(v), "s"(sbase) : "memory");
}

constexpr int CELL = 128;
constexpr int CIN = 30;
constexpr int CCAP = 62;
constexpr int CPIT = 272;
constexpr unsigned E2OFF = NBK * NCH * CELL;
constexpr int KP_HDR = 0;
constexpr int KP_OVC = KP_HDR + NBK * 8;
constexpr int KP_ENT = KP_OVC + 16;
constexpr int KP_HIST = KP_ENT + NBK * CPIT;
constexpr int KP_LDS = KP_HIST + NBIN * 4;
constexpr int NWL = 26;
constexpr int NBLOB = 31;

__device__ __forceinline__ unsigned mad24(unsigned a, int b, unsigned c) {
  unsigned r;
  asm("v_mad_i32_i24 %0, %1, %2, %3" : "=v"(r) : "v"(a), "s"(b), "v"(c));
  return r;
}
__device__ __forceinline__ unsigned mul24(unsigned a, unsigned b) {
  unsigned r;
  asm("v_mul_u32_u24 %0, %1, %2" : "=v"(r) : "n"(b), "v"(a));
  return r;
}
__device__ __forceinline__ unsigned mulhi24(unsigned a, unsigned b) {
  return (unsigned)(((unsigned long long)(a & 0xffffffu) * (unsigned long long)b) >> 32);
}
__device__ __forceinline__ unsigned dot4(unsigned a, unsigned b, unsigned c) {
  return __builtin_amdgcn_udot4(a, b, c, false);
}

__global__ __launch_bounds__(1024) void k_part(const int* __restrict__ src, const int* __restrict__ dst,
                                               int E, int chunk, u32x4* __restrict__ ENT,
                                               unsigned* __restrict__ TS,
                                               const unsigned short* __restrict__ wa,
                                               const unsigned short* __restrict__ wb,
                                               const unsigned short* __restrict__ wc,
                                               const unsigned short* __restrict__ wu,
                                               const unsigned short* __restrict__ wv,
                                               unsigned short* __restrict__ wpack) {
  const int tid = threadIdx.x;
#define PT(i)
  PT(0);
  if (blockIdx.x < NCH) {
    __shared__ __attribute__((aligned(16))) char sm[KP_LDS];
    LAS unsigned* hist = (LAS unsigned*)(sm + KP_HIST);
    LAS unsigned* ovc = (LAS unsigned*)(sm + KP_OVC);
    const int c = blockIdx.x;
    const int base = c * chunk;
    const int cnt = max(min(chunk, E - base), 0);
    const int i0 = max(min(base + 4 * tid, min(base + cnt, E) - 4), 0);
    const int lo = base + 4 * tid - i0, hi4 = base + cnt - i0;
    u32x4 d = gload128nt(dst, (unsigned)i0 * 4u);
    u32x4 sv = gload128nt(src, (unsigned)i0 * 4u);
    if (tid < NBK) *(LAS u32x2*)(sm + KP_HDR + tid * 8) = u32x2{0u, 0u};
    if (tid == 0) *(LAS u32x2*)ovc = u32x2{0u, 0u};
    const unsigned sm0 = (unsigned)(size_t)(LAS char*)sm;
    const bool act = __builtin_amdgcn_readfirstlane(tid) * 4 < cnt;
    unsigned long long on[4];
    unsigned tup[4];
#pragma unroll
    for (int k = 0; k < 4; ++k) {
      on[k] = __ballot(k >= lo && k < hi4);
      tup[k] = (unsigned)(i0 + k - base);
    }
    const int bk = tid >> 2, p0 = (tid & 3) * 16;
    unsigned ha = sm0 + KP_HDR + bk * 8, ia = sm0 + KP_ENT + bk * CPIT + p0;
    unsigned go = (unsigned)((bk * NCH + c) * CELL + p0);
    asm volatile("s_waitcnt lgkmcnt(0)\n\ts_barrier\n\ts_waitcnt vmcnt(1)"
                 : "+v"(d), "+v"(tup[0]), "+v"(tup[1]), "+v"(tup[2]), "+v"(tup[3]), "+v"(ha), "+v"(ia), "+v"(go),
                   "+s"(on[0]), "+s"(on[1]), "+s"(on[2]), "+s"(on[3])
                 :: "memory");
    PT(1);
    unsigned gi[4], rk[4], m0[4], m1[4], cb[4];
    if (act) {
      unsigned oh[4], sh[4];
#pragma unroll
      for (int k = 0; k < 4; ++k) {
        gi[k] = mulhi24(d[k] << 8, 684785u);
        sh[k] = gi[k] << 3;
        oh[k] = 1u << (sh[k] & 31u);
        rk[k] = 0u;
      }
      asm volatile(
          "s_mov_b64 exec, %12\n\t"
          "ds_add_rtn_u32 %0, %4, %8\n\t"
          "s_mov_b64 exec, %13\n\t"
          "ds_add_rtn_u32 %1, %5, %9\n\t"
          "s_mov_b64 exec, %14\n\t"
          "ds_add_rtn_u32 %2, %6, %10\n\t"
          "s_mov_b64 exec, %15\n\t"
          "ds_add_rtn_u32 %3, %7, %11\n\t"
          "s_mov_b64 exec, -1"
          : "+&v"(rk[0]), "+&v"(rk[1]), "+&v"(rk[2]), "+&v"(rk[3])
          : "v"((gi[0] & ~3u) + sm0), "v"((gi[1] & ~3u) + sm0), "v"((gi[2] & ~3u) + sm0), "v"((gi[3] & ~3u) + sm0),
            "v"(oh[0]), "v"(oh[1]), "v"(oh[2]), "v"(oh[3]), "s"(on[0]), "s"(on[1]), "s"(on[2]), "s"(on[3])
          : "memory");
#pragma unroll
      for (int k = 0; k < 4; ++k) {
        const unsigned slot = mad24(gi[k], -49 * 128, d[k] << 8) >> 8;
        tup[k] = mad24(slot, 3200, tup[k]);
        const unsigned hm = (unsigned)((int)(gi[k] << 29) >> 31);
        m0[k] = ((oh[k] - 1u) | hm) & 0x04040404u;
        m1[k] = (oh[k] - 1u) & hm & 0x04040404u;
        gi[k] &= ~7u;
        cb[k] = mul24(gi[k], CPIT / 8) + sm0;
        gi[k] += sm0;
      }
      SB();
      asm volatile("s_waitcnt lgkmcnt(0)" : "+v"(rk[0]), "+v"(rk[1]), "+v"(rk[2]), "+v"(rk[3]) :: "memory");
#pragma unroll
      for (int k = 0; k < 4; ++k) asm("v_bfe_u32 %0, %0, %1, 8" : "+v"(rk[k]) : "v"(sh[k]));
      if (max(max(rk[0], rk[1]), max(rk[2], rk[3])) >= (unsigned)CCAP) ovc[1] = 1u;
#pragma unroll
      for (int k = 0; k < 4; ++k) {
        rk[k] = cb[k] + rk[k] * 4;
        cb[k] += CCAP * 4;
      }
    }
    asm volatile("s_waitcnt vmcnt(0)" : "+v"(sv) :: "memory");
#pragma unroll
    for (int k = 0; k < 4; ++k) tup[k] = mad24(sv[k], 81920, tup[k]);
    asm volatile("s_barrier"
                 : "+v"(rk[0]), "+v"(rk[1]), "+v"(rk[2]), "+v"(rk[3]), "+v"(m0[0]), "+v"(m0[1]), "+v"(m0[2]),
                   "+v"(m0[3]), "+v"(m1[0]), "+v"(m1[1]), "+v"(m1[2]), "+v"(m1[3]), "+v"(tup[0]), "+v"(tup[1]),
                   "+v"(tup[2]), "+v"(tup[3]), "+v"(cb[0]), "+v"(cb[1]), "+v"(cb[2]), "+v"(cb[3]), "+v"(gi[0]),
                   "+v"(gi[1]), "+v"(gi[2]), "+v"(gi[3])
                 :: "memory");
    PT(2);
    u32x2 hc = *(const LAS u32x2*)(size_t)ha;
    if (act) {
      u32x2 hd[4];
      unsigned pa[4];
#pragma unroll
      for (int k = 0; k < 4; ++k) hd[k] = *(const LAS u32x2*)(size_t)gi[k];
#pragma unroll
      for (int k = 0; k < 4; ++k) pa[k] = min(dot4(hd[k][0], m0[k], dot4(hd[k][1], m1[k], rk[k])), cb[k]);
      asm volatile(
          "s_mov_b64 exec, %8\n\t"
          "ds_write_b32 %0, %4 offset:%12\n\t"
          "s_mov_b64 exec, %9\n\t"
          "ds_write_b32 %1, %5 offset:%12\n\t"
          "s_mov_b64 exec, %10\n\t"
          "ds_write_b32 %2, %6 offset:%12\n\t"
          "s_mov_b64 exec, %11\n\t"
          "ds_write_b32 %3, %7 offset:%12\n\t"
          "s_mov_b64 exec, -1"
          :: "v"(pa[0]), "v"(pa[1]), "v"(pa[2]), "v"(pa[3]), "v"(tup[0]), "v"(tup[1]), "v"(tup[2]), "v"(tup[3]),
             "s"(on[0]), "s"(on[1]), "s"(on[2]), "s"(on[3]), "n"(KP_ENT + 8)
          : "memory");
    }
    unsigned used = dot4(hc[0], 0x01010101u, dot4(hc[1], 0x01010101u, 0u));
    if (used > (unsigned)CCAP) ovc[1] = 1u;
    asm volatile("s_waitcnt lgkmcnt(0)\n\ts_barrier" ::: "memory");
    PT(3); PT(4);
    const unsigned ovf = ovc[1];
    u32x4 v0 = *(const LAS u32x4*)(size_t)ia;
    u32x4 v1 = *(const LAS u32x4*)(size_t)(ia + 64);
    asm volatile("" : "+v"(v0), "+v"(v1));
    if (__builtin_expect(__builtin_amdgcn_readfirstlane(ovf) != 0u, 0)) {
      *(LAS u32x2*)(hist + tid * 2) = u32x2{0u, 0u};
      __syncthreads();
#pragma unroll
      for (int k = 0; k < 4; ++k) {
        gi[k] = mulhi24(d[k] << 8, 684785u);
        if (k >= lo && k < hi4)
          rk[k] = __hip_atomic_fetch_add(hist + gi[k], 1u, __ATOMIC_RELAXED, __HIP_MEMORY_SCOPE_WORKGROUP);
      }
      __syncthreads();
      if (tid < NBK) {
        unsigned tot = 0;
        for (int i = 0; i < 8; ++i) tot += hist[tid * 8 + i];
        LAS unsigned* cp = (LAS unsigned*)(sm + KP_ENT + tid * CPIT);
        unsigned p = ~0u;
        if (tot > (unsigned)CCAP) {
          p = (unsigned)base + __hip_atomic_fetch_add(ovc, tot, __ATOMIC_RELAXED, __HIP_MEMORY_SCOPE_WORKGROUP);
          *(LAS u32x2*)(sm + KP_HDR + tid * 8) = u32x2{~0u, ~0u};
        }
        for (int i = 0; i < 8; ++i) {
          const unsigned n = hist[tid * 8 + i];
          hist[tid * 8 + i] = p;
          if (p != ~0u) {
            cp[2 + i] = 0u;
            cp[10 + i] = p;
            cp[18 + i] = n;
            p += n;
          }
        }
      }
      __syncthreads();
#pragma unroll
      for (int k = 0; k < 4; ++k) {
        if (k >= lo && k < hi4) {
          const unsigned p = hist[gi[k]];
          if (p != ~0u) TS[p + rk[k]] = tup[k];
        }
      }
      hc = *(const LAS u32x2*)(size_t)ha;
      used = dot4(hc[0], 0x01010101u, dot4(hc[1], 0x01010101u, 0u));
      v0 = *(const LAS u32x4*)(size_t)ia;
      v1 = *(const LAS u32x4*)(size_t)(ia + 64);
    }
    if (p0 == 0) { v0[0] = hc[0]; v0[1] = hc[1]; }
    gstore128wt(ENT, go, v0);
    if (used > 14u) gstore128wt(ENT, go + 64, v1);
    if (__builtin_expect(used > (unsigned)CIN, 0)) {
      gstore128wt(ENT, go + E2OFF, *(const LAS u32x4*)(size_t)(ia + 128));
      gstore128wt(ENT, go + E2OFF + 64, *(const LAS u32x4*)(size_t)(ia + 192));
    }
  } else if (tid < 512) {
    const int f = blockIdx.x - NCH;
    unsigned short v;
    if (f >= NWL && f < NBLOB) {
      const int e = (f - NWL) * 512 + tid;
      const int tb = e >= 32 * (TPITCH / 2);
      const int e2 = e - tb * 32 * (TPITCH / 2);
      const int sl = e2 / (TPITCH / 2), col = e2 - sl * (TPITCH / 2);
      const int nd = tb ? 16 * ((col >> 3) & 1) + 8 * ((col >> 2) & 1) + 4 * (col >> 4) + (col & 3) : col;
      v = (col < 32 && nd == sl) ? 0x3F80 : 0;
    } else {
      const int lane = tid >> 3, j = tid & 7;
      const int n = lane & 31, h = lane >> 5;
      const int g = f < 16 ? f : f < NWL ? f - 18 : f - NBLOB;
      const int ct = g & 1;
      const int col = 32 * ((n >> 2) & 1) + 16 * ct + 4 * (n >> 3) + (n & 3);
      if (f == 16 || f == 17) {
        v = wc[(8 * h + j) * 64 + col];
      } else {
        const int t = (g >> 1) & 3;
        const unsigned short* W = f < 8 ? wb : f < 16 ? wv : f < NWL ? wu : wa;
        v = W[(32 * h + 8 * t + j) * 64 + col];
      }
      if (f < 8 || f == 16 || f == 17 || f >= NBLOB)
        v = (unsigned short)(pack_bf16(__uint_as_float((unsigned)v << 16) * NLOG2E, 0.f) & 0xffffu);
    }
    wpack[f * 512 + tid] = v;
  }
}

constexpr int RING_SLOT = 5 * 1024;
constexpr int SPIT = 1040;
constexpr int LCAP = 512;
constexpr int LARR = 2560;
constexpr int LIST_BYTES = 2 * LARR;
constexpr int LRAW = LIST_BYTES - (LCAP + 8) * 8;
constexpr int OFF_W = 0;
constexpr int OFF_T1 = NWL * 1024;
constexpr int OFF_T2 = OFF_T1 + 32 * TPITCH;
constexpr int OFF_WA = NBLOB * 1024;
constexpr int OFF_R0 = NFRAG * 1024;
constexpr int OFF_R1 = OFF_R0 + 8 * RING_SLOT;
constexpr int OFF_L = OFF_R1 + 8 * RING_SLOT;
constexpr int MAIN_LDS = OFF_L + 8 * LIST_BYTES;
static_assert(MAIN_LDS <= 160 * 1024, "LDS");

__device__ __forceinline__ float add1(float t) { return 1.0f + t; }
__device__ __forceinline__ float mulf(int i, float a, float b) { return (i & 1) ? __builtin_fmaf(a, b, 0.0f) : a * b; }

__global__ __launch_bounds__(512, 2) void k_main(const unsigned short* __restrict__ x,
                                                 const u32x4* __restrict__ ENT,
                                                 const u32x4* __restrict__ ES,
                                                 const unsigned* __restrict__ TS, int chunk,
                                                 const bf16x8* __restrict__ wpack,
                                                 unsigned short* __restrict__ out, int N
                                                 ) {
  __shared__ __attribute__((aligned(16))) char smem[MAIN_LDS];
  const int tid = threadIdx.x, lane = tid & 63;
  const int wave = __builtin_amdgcn_readfirstlane(tid >> 6);
  const int r = lane & 31, h = lane >> 5;
  const int bk = blockIdx.x;
  const unsigned lds0 = (unsigned)(size_t)(LAS char*)smem;

  int warm;
  {
    const unsigned vo = lane * 16;
    const char* eb = reinterpret_cast<const char*>(ENT) + (size_t)bk * (NCH * CELL);
#pragma unroll
    for (int i = 0; i < 4; ++i) glds16(eb + (wave * 4 + i) * 1024, vo, lds0 + OFF_R1 + (wave * 4 + i) * SPIT);
    const char* wp = reinterpret_cast<const char*>(wpack);
#pragma unroll
    for (int i = 0; i < 5; ++i) {
      const int f = min(wave + 8 * i, NFRAG - 1);
      glds16(wp + f * 1024, vo, lds0 + OFF_W + f * 1024);
    }
    warm = gload32(x, (unsigned)min(bk * 196 + 25 * wave + min(lane, 24), N - 1) * 128u);
    if (wave >= 4) __builtin_amdgcn_s_setprio(1);
  }
  LAS char* lbase = (LAS char*)smem + OFF_L + wave * LIST_BYTES;
  const unsigned la = (unsigned)(size_t)lbase;
  auto put = [&](int p, unsigned a, unsigned b) { *(LAS u32x2*)(lbase + LRAW + p * 8) = u32x2{a, b}; };
  unsigned aLX = la + (lane >> 3) * 16;
  unsigned aLE = la + LARR + ((((lane >> 1) & 7) << 2) | (lane >> 4)) * 4;
  unsigned aLS4 = la + (4 * h + ((lane >> 2) & 3)) * 16;
  unsigned aLS1 = la + (((r & 7) << 2) | (r >> 3)) * 4 + 2;
  unsigned cx0 = (unsigned)(((lane & 7) ^ (lane >> 4)) * 16);
  unsigned cx1 = cx0 ^ 64u;
  unsigned ce = (unsigned)((((lane & 1) ^ ((lane >> 4) & 1))) * 16);
  const unsigned ring0 = lds0 + OFF_R0 + wave * RING_SLOT;
  const unsigned ringd = 8 * RING_SLOT;
  unsigned cS = lds0 + OFF_T1 + (((lane >> 4) & 1) * 16 + (lane & 3) * 4) * 2;
  unsigned cSt = lds0 + OFF_T2 + h * 32;
  unsigned fo[4];
#pragma unroll
  for (int t = 0; t < 4; ++t) fo[t] = ring0 + (unsigned)(r * 128 + (((4 * h + t) ^ ((r >> 1) & 7)) * 16));
  unsigned foe = ring0 + (unsigned)(4096 + r * 32 + ((h ^ ((r >> 3) & 1)) * 16));
  unsigned sa = lds0 + OFF_R1 + r * SPIT + h * CELL;
  unsigned cb0 = (unsigned)((8 * r + h) * chunk);
  unsigned aR = la + LRAW + lane * 8, aF = la + lane * 4;
  unsigned aRX = la + LRAW + (lane >> 3) * 32, aRE = la + LRAW + ((((lane >> 1) & 7) << 2) | (lane >> 4)) * 8;
  f32x16 Y[4];
#pragma unroll
  for (int q = 0; q < 4; ++q)
#pragma unroll
    for (int i = 0; i < 16; ++i) Y[q][i] = 0.f;
  f32x16 G0, V0, G1, V1;
  bf16x8 S[2], St[2], as[4], ae, b[3];
  u32x4 pd[2], pn[2];
  float tq[16], rq[16], pq[16];
  auto pipe_zero = [&]() {
#pragma unroll
    for (int j = 0; j < 8; ++j) { S[0][j] = 0; S[1][j] = 0; }
#pragma unroll
    for (int j = 0; j < 4; ++j) { pd[0][j] = 0; pd[1][j] = 0; pn[0][j] = 0; pn[1][j] = 0; }
#pragma unroll
    for (int i = 0; i < 16; ++i) V1[i] = 0.f;
    rq[14] = 0.f; rq[15] = 0.f;
  };
  asm volatile("" : "+v"(aLX), "+v"(aLE), "+v"(aLS4), "+v"(aLS1), "+v"(cx0), "+v"(cx1), "+v"(ce), "+v"(cS), "+v"(cSt),
                    "+v"(fo[0]), "+v"(fo[1]), "+v"(fo[2]), "+v"(fo[3]), "+v"(foe), "+v"(sa), "+v"(cb0), "+v"(aR),
                    "+v"(aF), "+v"(aRX), "+v"(aRE));
  asm volatile("" : "+v"(Y[0]), "+v"(Y[1]), "+v"(Y[2]), "+v"(Y[3]));
  unsigned rsh = (lane & 3) * 8, rid = lane & 7;
  unsigned long long rhi = 0xf0f0f0f0f0f0f0f0ull;
  asm volatile("" : "+v"(rsh), "+v"(rid), "+s"(rhi));
  asm volatile("s_waitcnt vmcnt(6)\n\ts_barrier" ::: "memory");
  u32x2 cl[4][4], hd[4];
  int nr[4], st[4], ni[4];
  unsigned long long mk[4][4];
#pragma unroll
  for (int j = 0; j < 4; ++j) hd[j] = *(const LAS u32x2*)(size_t)(sa + 2 * j * CELL);
  int gb;
  {
    int lo = (int)(hd[0][0] + hd[1][0] + hd[2][0] + hd[3][0]), hi = (int)(hd[0][1] + hd[1][1] + hd[2][1] + hd[3][1]);
    lo += __builtin_amdgcn_update_dpp(0, lo, 0xb1, 0xf, 0xf, false);
    hi += __builtin_amdgcn_update_dpp(0, hi, 0xb1, 0xf, 0xf, false);
    lo += __builtin_amdgcn_update_dpp(0, lo, 0x4e, 0xf, 0xf, false);
    hi += __builtin_amdgcn_update_dpp(0, hi, 0x4e, 0xf, 0xf, false);
    lo += __builtin_amdgcn_update_dpp(0, lo, 0x141, 0xf, 0xf, false);
    hi += __builtin_amdgcn_update_dpp(0, hi, 0x141, 0xf, 0xf, false);
    lo += __builtin_amdgcn_update_dpp(0, lo, 0x140, 0xf, 0xf, false);
    hi += __builtin_amdgcn_update_dpp(0, hi, 0x140, 0xf, 0xf, false);
    unsigned c;
    asm("v_cndmask_b32 %0, %1, %2, %3\n\tv_bfe_u32 %0, %0, %4, 8" : "=&v"(c) : "v"(lo), "v"(hi), "s"(rhi), "v"(rsh));
    auto p16 = __builtin_amdgcn_permlane16_swap(c, c, false, false);
    c = p16[0] + p16[1];
    auto p32 = __builtin_amdgcn_permlane32_swap(c, c, false, false);
    const int key = (int)(((p32[0] + p32[1]) << 3) | rid);
    unsigned acc = 0u;
#define RK(k) acc = __builtin_amdgcn_alignbit(acc, (unsigned)(__builtin_amdgcn_update_dpp(0, key, 0x120 + k, 0xf, 0xf, false) - key), 31)
    RK(1); RK(2); RK(3); RK(4); RK(5); RK(6); RK(7);
#undef RK
    const int want = RANKOF(wave);
    gb = __builtin_ctzll(__ballot(__builtin_popcount(acc) == want)) & 7;
  }
  const int nb0 = bk * 196 + 49 * (gb >> 1) + 25 * (gb & 1);
  const int nodes = max(min(25 - (gb & 1), N - nb0), 0);
  const int n0 = min(nb0, N - 1);
  const unsigned mlo = gb >= 4 ? 0x01010101u : 0x01010101u & ((1u << (8 * gb)) - 1u);
  const unsigned mhi = gb <= 4 ? 0u : 0x01010101u & ((1u << (8 * (gb - 4))) - 1u);
  u32x4 xg[4];
  {
    const unsigned xo = (unsigned)(n0 + max(min(r, nodes - 1), 0)) * 128u + 64u * h;
#pragma unroll
    for (int t = 0; t < 4; ++t) xg[t] = gload128(x, xo + 16 * t);
  }
  auto head = [&](int j) {
    nr[j] = (int)(((gb >= 4 ? hd[j][1] : hd[j][0]) >> (8 * (gb & 3))) & 0xffu);
    st[j] = (int)dot4(hd[j][0], mlo, dot4(hd[j][1], mhi, 0u));
    const int n = min(min(nr[j], 8), CIN - st[j]);
#pragma unroll
    for (int q = 0; q < 4; ++q) mk[j][q] = __ballot(n > 2 * q);
    ni[j] = max(n, 0);
  };
  auto fetch = [&](int j) {
    asm volatile(
        "s_mov_b64 exec, %5\n\t"
        "ds_read2_b32 %0, %4 offset0:%9 offset1:%9+1\n\t"
        "s_mov_b64 exec, %6\n\t"
        "ds_read2_b32 %1, %4 offset0:%9+2 offset1:%9+3\n\t"
        "s_mov_b64 exec, %7\n\t"
        "ds_read2_b32 %2, %4 offset0:%9+4 offset1:%9+5\n\t"
        "s_mov_b64 exec, %8\n\t"
        "ds_read2_b32 %3, %4 offset0:%9+6 offset1:%9+7\n\t"
        "s_mov_b64 exec, -1"
        : "=&v"(cl[j][0]), "=&v"(cl[j][1]), "=&v"(cl[j][2]), "=&v"(cl[j][3])
        : "v"(sa + st[j] * 4), "s"(mk[j][0]), "s"(mk[j][1]), "s"(mk[j][2]), "s"(mk[j][3]), "n"(j * 64 + 2)
        : "memory");
  };
  auto fill = [&](int j, int p) {
    asm volatile(
        "s_mov_b64 exec, %13\n\t"
        "ds_write2_b32 %0, %9, %1 offset0:14 offset1:15\n\t"
        "ds_write2_b32 %0, %8, %1 offset0:12 offset1:13\n\t"
        "s_mov_b64 exec, %12\n\t"
        "ds_write2_b32 %0, %7, %1 offset0:10 offset1:11\n\t"
        "ds_write2_b32 %0, %6, %1 offset0:8 offset1:9\n\t"
        "s_mov_b64 exec, %11\n\t"
        "ds_write2_b32 %0, %5, %1 offset0:6 offset1:7\n\t"
        "ds_write2_b32 %0, %4, %1 offset0:4 offset1:5\n\t"
        "s_mov_b64 exec, %10\n\t"
        "ds_write2_b32 %0, %3, %1 offset0:2 offset1:3\n\t"
        "ds_write2_b32 %0, %2, %1 offset0:0 offset1:1\n\t"
        "s_mov_b64 exec, -1"
        :: "v"(la + LRAW + p * 8), "v"(cb0 + 2 * j * chunk), "v"(cl[j][0][0]), "v"(cl[j][0][1]), "v"(cl[j][1][0]),
           "v"(cl[j][1][1]), "v"(cl[j][2][0]), "v"(cl[j][2][1]), "v"(cl[j][3][0]), "v"(cl[j][3][1]), "s"(mk[j][0]),
           "s"(mk[j][1]), "s"(mk[j][2]), "s"(mk[j][3])
        : "memory");
  };
  auto src_of = [&](unsigned v) { return mulhi24(v >> 8, 13421773u); };
  auto decode = [&](unsigned v, unsigned cb, unsigned& a, unsigned& e) {
    unsigned sl;
    asm("v_lshrrev_b32 %0, 8, %3\n\t"
        "v_mul_hi_u32_u24 %0, 0xcccccd, %0\n\t"
        "v_mad_i32_i24 %1, %0, %5, %3\n\t"
        "v_mul_hi_u32_u24 %2, 0x147ae2, %1\n\t"
        "v_add_lshl_u32 %1, %1, %4, 5\n\t"
        "v_mad_i32_i24 %0, %2, %6, %0\n\t"
        "v_mad_i32_i24 %1, %2, %7, %1"
        : "=&v"(a), "=&v"(e), "=&v"(sl)
        : "v"(v), "v"(cb), "s"(-81920), "s"((int)(TPITCH << 16)), "s"(-102400));
  };
  auto decode2 = [&](const u32x2 p, const u32x2 q, unsigned& a, unsigned& e, unsigned& c, unsigned& f) {
    unsigned sl, sm;
    asm("v_lshrrev_b32 %0, 8, %6\n\t"
        "v_lshrrev_b32 %3, 8, %8\n\t"
        "v_mul_hi_u32_u24 %0, 0xcccccd, %0\n\t"
        "v_mul_hi_u32_u24 %3, 0xcccccd, %3\n\t"
        "v_mad_i32_i24 %1, %0, %10, %6\n\t"
        "v_mad_i32_i24 %4, %3, %10, %8\n\t"
        "v_mul_hi_u32_u24 %2, 0x147ae2, %1\n\t"
        "v_mul_hi_u32_u24 %5, 0x147ae2, %4\n\t"
        "v_add_lshl_u32 %1, %1, %7, 5\n\t"
        "v_add_lshl_u32 %4, %4, %9, 5\n\t"
        "v_mad_i32_i24 %0, %2, %11, %0\n\t"
        "v_mad_i32_i24 %3, %5, %11, %3\n\t"
        "v_mad_i32_i24 %1, %2, %12, %1\n\t"
        "v_mad_i32_i24 %4, %5, %12, %4"
        : "=&v"(a), "=&v"(e), "=&v"(sl), "=&v"(c), "=&v"(f), "=&v"(sm)
        : "v"(p[0]), "v"(p[1]), "v"(q[0]), "v"(q[1]), "s"(-81920), "s"((int)(TPITCH << 16)), "s"(-102400));
  };
  auto ld128 = [&](unsigned a) { return *(const LAS u32x4*)(size_t)a; };
  auto gather5 = [&](const u32x4 sx, unsigned re, int rs) {
    const unsigned dstb = ring0 + rs * ringd;
    glds16x(x, sx[0], cx0, dstb);
    glds16x(x, sx[1], cx1, dstb + 1024);
    glds16x(x, sx[2], cx0, dstb + 2048);
    glds16x(x, sx[3], cx1, dstb + 3072);
    glds16e(ES, re, ce, dstb + 4096);
  };
  auto issue_gather = [&](int tile, int rs) {
    gather5(ld128(aLX + tile * 128), *(const LAS unsigned*)(size_t)(aLE + tile * 128), rs);
  };
  head(0);
  fetch(0);
  SB();
  const int s0 = wave_scan(ni[0]);
  head(1);
  head(2);
  head(3);
  const int tot0 = __builtin_amdgcn_readlane(s0, 63);
  const int p0 = s0 - ni[0];
  SB();
  asm volatile("s_waitcnt lgkmcnt(0)" : "+v"(cl[0][0]), "+v"(cl[0][1]), "+v"(cl[0][2]), "+v"(cl[0][3]) :: "memory");
  fill(0, p0);
  bool early = tot0 >= 32;
  int s123;
  {
    unsigned v0 = *(const LAS unsigned*)(size_t)aRX, v1 = *(const LAS unsigned*)(size_t)(aRX + 8);
    unsigned v2 = *(const LAS unsigned*)(size_t)(aRX + 16), v3 = *(const LAS unsigned*)(size_t)(aRX + 24);
    u32x2 ve = *(const LAS u32x2*)(size_t)aRE;
    SB();
    s123 = wave_scan(ni[1] | (ni[2] << 10) | (ni[3] << 20));
    SB();
    asm volatile("" : "+v"(v0), "+v"(v1), "+v"(v2), "+v"(v3), "+v"(ve), "+v"(s123));
    if (__builtin_expect(early, 1)) {
      unsigned a, e;
      decode(ve[0], ve[1], a, e);
      gather5(u32x4{src_of(v0), src_of(v1), src_of(v2), src_of(v3)}, e, 0);
    } else {
      gather5(u32x4{0u, 0u, 0u, 0u}, 0u, 0);
    }
  }
  fetch(1);
  fetch(2);
  fetch(3);
  const int e123 = __builtin_amdgcn_readlane(s123, 63);
  const int tot1 = tot0 + (e123 & 0x3ff), tot2 = tot1 + ((e123 >> 10) & 0x3ff);
  int cnt_all = tot2 + (e123 >> 20);
  asm volatile("s_waitcnt vmcnt(5) lgkmcnt(0)\n\ts_barrier"
               : "+v"(xg[0]), "+v"(xg[1]), "+v"(xg[2]), "+v"(xg[3]), "+v"(warm),
                 "+v"(cl[1][0]), "+v"(cl[1][1]), "+v"(cl[1][2]), "+v"(cl[1][3]), "+v"(cl[2][0]), "+v"(cl[2][1]),
                 "+v"(cl[2][2]), "+v"(cl[2][3]), "+v"(cl[3][0]), "+v"(cl[3][1]), "+v"(cl[3][2]), "+v"(cl[3][3])
               :: "memory");
  const LAS bf16x8* wl = (const LAS bf16x8*)(smem + OFF_W) + lane;
  const f32x16 zero16 = {0.f, 0.f, 0.f, 0.f, 0.f, 0.f, 0.f, 0.f, 0.f, 0.f, 0.f, 0.f, 0.f, 0.f, 0.f, 0.f};
  f32x16 XA[2] = {zero16, zero16};
  bf16x8 wa[8];
#pragma unroll
  for (int k = 0; k < 8; ++k) wa[k] = wl[(NBLOB + k) * 64];
  SB();
  auto xa_step = [&](int k) {
#pragma unroll
    for (int ct = 0; ct < 2; ++ct) XA[ct] = MFMA32(__builtin_bit_cast(bf16x8, xg[k]), wa[k * 2 + ct], XA[ct]);
    SB();
  };
  if (__builtin_expect(cnt_all > LCAP, 0)) {
#pragma unroll
    for (int j = 1; j < 4; ++j)
#pragma unroll
      for (int q = 0; q < 4; ++q) mk[j][q] = 0ull;
  }
  fill(1, tot0 + (s123 & 0x3ff) - ni[1]);
  SB();
  xa_step(0);
  fill(2, tot1 + ((s123 >> 10) & 0x3ff) - ni[2]);
  SB();
  xa_step(1);
  fill(3, tot2 + (s123 >> 20) - ni[3]);
  SB();
  xa_step(2);
  xa_step(3);
  if (__builtin_expect(__ballot(nr[0] + nr[1] + nr[2] + nr[3] != ni[0] + ni[1] + ni[2] + ni[3]) != 0ull, 0)) {
#pragma unroll
    for (int j = 0; j < 4; ++j) {
      unsigned long long om = __ballot(nr[j] != ni[j]);
      while (om) {
        const int l = __builtin_ctzll(om);
        om &= om - 1;
        const int n = __builtin_amdgcn_readlane(nr[j] - ni[j], l);
        const int p = __builtin_amdgcn_readlane(st[j] + ni[j], l) + lane;
        const int cc = 8 * (l & 31) + (l >> 5) + 2 * j;
        if (n > CCAP) cnt_all = LCAP;
        if (cnt_all + n <= LCAP && lane < n) {
          const unsigned* cp = reinterpret_cast<const unsigned*>(reinterpret_cast<const char*>(ENT) +
                                                                 ((size_t)bk * NCH + cc) * CELL);
          put(cnt_all + lane, p < CIN ? cp[2 + p] : cp[E2OFF / 4 + p - CIN], (unsigned)(cc * chunk));
        }
        cnt_all += n;
      }
    }
    __builtin_amdgcn_s_waitcnt(0x0F70);
  }
  const int gbin = bk * 8 + wave;
  auto fill_slow = [&](int wb0) __attribute__((always_inline)) {
    int nn[4], sp[4], tot = 0;
    const unsigned* rp[4];
    int l2;
    asm volatile("v_mbcnt_lo_u32_b32 %0, -1, 0\n\tv_mbcnt_hi_u32_b32 %0, -1, %0" : "=v"(l2));
    const int ch0 = 8 * (l2 & 31) + (l2 >> 5);
#pragma unroll
    for (int j = 0; j < 4; ++j) {
      const unsigned* cp = reinterpret_cast<const unsigned*>(reinterpret_cast<const char*>(ENT) +
                                                             ((size_t)bk * NCH + ch0 + 2 * j) * CELL);
      const unsigned h0 = cp[0], h1 = cp[1];
      nn[j] = (int)(((gb >= 4 ? h1 : h0) >> (8 * (gb & 3))) & 0xffu);
      sp[j] = (int)dot4(h0, mlo, dot4(h1, mhi, 0u));
      rp[j] = cp + 2 + sp[j];
      sp[j] = CIN - sp[j];
      if (nn[j] == 0xff) {
        rp[j] = TS + cp[10 + gb];
        nn[j] = (int)cp[18 + gb];
        sp[j] = nn[j];
      }
      tot += nn[j];
    }
    const int sc = wave_scan(tot);
    int pp = sc - tot - wb0;
#pragma unroll
    for (int j = 0; j < 4; ++j) {
      const int cb = (ch0 + 2 * j) * chunk;
      const int te = min(nn[j], LCAP - pp);
#pragma unroll 1
      for (int t = max(0, -pp); t < te; ++t) put(pp + t, rp[j][t < sp[j] ? t : t + (int)(E2OFF / 4) - 32], (unsigned)cb);
      pp += nn[j];
    }
    __builtin_amdgcn_s_waitcnt(0x0F70);
    return __builtin_amdgcn_readlane(sc, 63);
  };
  if (__builtin_expect(cnt_all > LCAP, 0)) {
    asm volatile("s_waitcnt vmcnt(0)" ::: "memory");
    cnt_all = fill_slow(0);
    early = false;
  }
  cnt_all = __builtin_amdgcn_readfirstlane(cnt_all);
  int cnt = min(cnt_all, LCAP);
  auto convert = [&](int cnt, unsigned aR, unsigned aF, int lane) __attribute__((always_inline)) {
    u32x2 v[8];
#pragma unroll
    for (int k = 0; k < 8; ++k) v[k] = *(const LAS u32x2*)(size_t)(aR + k * 512);
    asm volatile("" ::: "memory");
#pragma unroll
    for (int k = 0; k < 6; k += 2) {
      unsigned a, e, c, f;
      decode2(v[k], v[k + 1], a, e, c, f);
      *(LAS unsigned*)(size_t)(aF + k * 256) = a;
      *(LAS unsigned*)(size_t)(aF + k * 256 + 256) = c;
      *(LAS unsigned*)(size_t)(aF + k * 256 + LARR) = e;
      *(LAS unsigned*)(size_t)(aF + k * 256 + LARR + 256) = f;
    }
#pragma unroll
    for (int k = 6; k < 8; ++k) {
      if (cnt > k * 64) {
        unsigned a, e;
        decode(v[k][0], v[k][1], a, e);
        *(LAS unsigned*)(size_t)(aF + k * 256) = a;
        *(LAS unsigned*)(size_t)(aF + k * 256 + LARR) = e;
      }
    }
    const int u = cnt + lane;
    const unsigned padA = (unsigned)(n0 + max(min(((u >> 2) & 7) + 8 * (u & 3), nodes - 1), 0)) | (31u * TPITCH << 16);
    const unsigned pa = aF + cnt * 4;
    *(LAS unsigned*)(size_t)pa = padA;
    *(LAS unsigned*)(size_t)(pa + 256) = padA;
    *(LAS unsigned*)(size_t)(pa + LARR) = 0u;
    *(LAS unsigned*)(size_t)(pa + LARR + 256) = 0u;
  };
  convert(cnt, aR, aF, lane);
  issue_gather(1, 1);
  if (__builtin_expect(!early, 0)) {
    asm volatile("s_waitcnt vmcnt(0)" ::: "memory");
    issue_gather(0, 0);
    issue_gather(1, 1);
  }
  bf16x8 XAf[2][2];
#pragma unroll
  for (int ct = 0; ct < 2; ++ct)
#pragma unroll
    for (int s = 0; s < 2; ++s) {
      u32x4 p;
#pragma unroll
      for (int j = 0; j < 4; ++j) p[j] = pack_bf16(XA[ct][8 * s + 2 * j], XA[ct][8 * s + 2 * j + 1]);
      XAf[s][ct] = __builtin_bit_cast(bf16x8, p);
    }

  auto rd128 = [&](unsigned a) { return *(const LAS bf16x8*)(size_t)a; };
  auto load_S = [&](const u32x4 ss) {
    const s16x4 a0 = __builtin_amdgcn_ds_read_tr16_b64_v4i16((LAS s16x4*)(size_t)((ss[0] >> 16) + cS));
    const s16x4 a1 = __builtin_amdgcn_ds_read_tr16_b64_v4i16((LAS s16x4*)(size_t)((ss[1] >> 16) + cS));
    const s16x4 a2 = __builtin_amdgcn_ds_read_tr16_b64_v4i16((LAS s16x4*)(size_t)((ss[2] >> 16) + cS));
    const s16x4 a3 = __builtin_amdgcn_ds_read_tr16_b64_v4i16((LAS s16x4*)(size_t)((ss[3] >> 16) + cS));
    S[0] = __builtin_shufflevector(a0, a1, 0, 1, 2, 3, 4, 5, 6, 7);
    S[1] = __builtin_shufflevector(a2, a3, 0, 1, 2, 3, 4, 5, 6, 7);
  };
  auto widx = [&](int m, int ct) { return m == 6 ? 16 + ct : m < 11 ? (m - 7) * 2 + ct : 8 + (m - 11) * 2 + ct; };

#define HELD ((1 << 6) | (1 << 8) | (1 << 10) | (1 << 12) | (1 << 14))
  bf16x8 wr[2][15];
#pragma unroll
  for (int ct = 0; ct < 2; ++ct)
#pragma unroll
    for (int m = 6; m < 15; ++m)
      if ((HELD >> m) & 1) {
        wr[ct][m] = wl[widx(m, ct) * 64];
        asm volatile("" : "+v"(wr[ct][m]));
      }
  auto wf = [&](int m, int ct) { return ((HELD >> m) & 1) ? wr[ct][m] : b[(m - 6) % 3]; };

  auto ew = [&](int m, const f32x16& Gc, const f32x16& Vc, const f32x16& Gn, const f32x16& Vp) {
    if (m < 2) {
      const int i = 14 + m;
      pq[i] = mulf(i, Vp[i], rq[i]);
      if (m == 1) pn[1][3] = pack_bf16(pq[14], pq[15]);
    } else if (m == 2) {
      pq[0] = mulf(0, Vc[0], rq[0]);
      pq[1] = mulf(1, Vc[1], rq[1]);
      pn[0][0] = pack_bf16(pq[0], pq[1]);
    } else {
      const int i = m - 1;
      pq[i] = mulf(i, Vc[i], rq[i]);
      if (i & 1) pn[i >> 3][(i >> 1) & 3] = pack_bf16(pq[i - 1], pq[i]);
    }
    if (m == 0) pd[0][0] = pack_bf16(rq[0], rq[1]);
    else if ((m & 1) == 0) { const int i = m + 1; pd[i >> 3][(i >> 1) & 3] = pack_bf16(rq[i - 1], rq[i]); }
    if (m < 14) {
      const int i = m + 2;
      rq[i] = __builtin_amdgcn_rcpf(add1(tq[i]));
    } else {
      rq[0] = __builtin_amdgcn_rcpf(add1(tq[0]));
      rq[1] = __builtin_amdgcn_rcpf(add1(tq[1]));
    }
    if (m < 13) tq[m + 3] = __builtin_amdgcn_exp2f(Gc[m + 3]);
    else if (m == 13) { tq[0] = __builtin_amdgcn_exp2f(Gn[0]); tq[1] = __builtin_amdgcn_exp2f(Gn[1]); }
    else tq[2] = __builtin_amdgcn_exp2f(Gn[2]);
  };

  int wb0 = 0;
  while (true) {
    const int ntiles = (cnt + 31) >> 5;
    asm volatile("s_waitcnt vmcnt(5)" ::: "memory");
    {
      const unsigned sl = *(const LAS unsigned short*)(size_t)aLS1;
#pragma unroll
      for (int k = 0; k < 4; ++k) as[k] = rd128(fo[k]);
      ae = rd128(foe);
      St[0] = rd128(sl + cSt);
      St[1] = rd128(sl + cSt + 16);
      G0 = MFMA32(St[0], XAf[0][0], zero16);
      G0 = MFMA32(St[1], XAf[1][0], G0);
      G0 = MFMA32(ae, wl[widx(6, 0) * 64], G0);
#pragma unroll
      for (int k = 0; k < 4; ++k) G0 = MFMA32(as[k], wl[widx(7 + k, 0) * 64], G0);
      V0 = MFMA32(as[0], wl[widx(11, 0) * 64], zero16);
#pragma unroll
      for (int k = 1; k < 4; ++k) V0 = MFMA32(as[k], wl[widx(11 + k, 0) * 64], V0);
      asm volatile("s_waitcnt lgkmcnt(0)" ::: "memory");
      issue_gather(2, 0);
      pipe_zero();
      tq[0] = __builtin_amdgcn_exp2f(G0[0]);
      tq[1] = __builtin_amdgcn_exp2f(G0[1]);
      tq[2] = __builtin_amdgcn_exp2f(G0[2]);
      rq[0] = __builtin_amdgcn_rcpf(add1(tq[0]));
      rq[1] = __builtin_amdgcn_rcpf(add1(tq[1]));
      SB();
    }
    unsigned &lS4 = aLS4, &lS1 = aLS1, &lX = aLX, &lE = aLE;
    int adv = 0;
    auto body = [&](const int par) {
      const unsigned lo = par * 128;
      const unsigned ro = (1 - par) * ringd;
      {
        u32x4 ss;
        unsigned sl;
#pragma unroll
        for (int m = 0; m < 15; ++m) {
          if (m == 0) Y[1] = MFMA32(__builtin_bit_cast(bf16x8, pd[0]), S[0], Y[1]);
          else if (m == 1) Y[3] = MFMA32(__builtin_bit_cast(bf16x8, pn[0]), S[0], Y[3]);
          else if (m == 2) Y[1] = MFMA32(__builtin_bit_cast(bf16x8, pd[1]), S[1], Y[1]);
          else if (m == 3) Y[3] = MFMA32(__builtin_bit_cast(bf16x8, pn[1]), S[1], Y[3]);
          else if (m == 4) G1 = MFMA32(St[0], XAf[0][1], zero16);
          else if (m == 5) G1 = MFMA32(St[1], XAf[1][1], G1);
          else if (m == 6) G1 = MFMA32(ae, wf(m, 1), G1);
          else if (m < 11) G1 = MFMA32(as[m - 7], wf(m, 1), G1);
          else if (m == 11) V1 = MFMA32(as[0], wf(m, 1), zero16);
          else V1 = MFMA32(as[m - 11], wf(m, 1), V1);
          SB();
          if (m == 0) { ss = ld128(lS4 + lo); sl = *(const LAS unsigned short*)(size_t)(lS1 + lo + 128); }
          if (m >= 3 && m < 12 && !((HELD >> (m + 3)) & 1)) b[(m - 3) % 3] = wl[widx(m + 3, 1) * 64];
          if (m == 4) load_S(ss);
          if (m == 5) { St[0] = rd128(sl + cSt); St[1] = rd128(sl + cSt + 16); }
          if (m == 6) { asm volatile("s_waitcnt vmcnt(5)" ::: "memory"); ae = rd128(foe + ro); }
          if (m >= 11) as[m - 11] = rd128(fo[m - 11] + ro);
          ew(m, G0, V0, G1, V1);
          SB();
        }
      }
      {
        u32x4 sx;
        unsigned re;
#pragma unroll
        for (int m = 0; m < 15; ++m) {
          if (m == 0) Y[0] = MFMA32(__builtin_bit_cast(bf16x8, pd[0]), S[0], Y[0]);
          else if (m == 1) Y[2] = MFMA32(__builtin_bit_cast(bf16x8, pn[0]), S[0], Y[2]);
          else if (m == 2) Y[0] = MFMA32(__builtin_bit_cast(bf16x8, pd[1]), S[1], Y[0]);
          else if (m == 3) Y[2] = MFMA32(__builtin_bit_cast(bf16x8, pn[1]), S[1], Y[2]);
          else if (m == 4) G0 = MFMA32(St[0], XAf[0][0], zero16);
          else if (m == 5) G0 = MFMA32(St[1], XAf[1][0], G0);
          else if (m == 6) G0 = MFMA32(ae, wf(m, 0), G0);
          else if (m < 11) G0 = MFMA32(as[m - 7], wf(m, 0), G0);
          else if (m == 11) V0 = MFMA32(as[0], wf(m, 0), zero16);
          else V0 = MFMA32(as[m - 11], wf(m, 0), V0);
          SB();
          if (m == 4) { sx = ld128(lX + lo + 3 * 128); re = *(const LAS unsigned*)(size_t)(lE + lo + 3 * 128); }
          if (m >= 3 && m < 12 && !((HELD >> (m + 3)) & 1)) b[(m - 3) % 3] = wl[widx(m + 3, 0) * 64];
          if (m == 7) glds16x(x, sx[0], cx0, ring0 + ro);
          if (m == 8) glds16x(x, sx[1], cx1, ring0 + ro + 1024);
          if (m == 9) glds16x(x, sx[2], cx0, ring0 + ro + 2048);
          if (m == 10) glds16x(x, sx[3], cx1, ring0 + ro + 3072);
          if (m == 11) glds16e(ES, re, ce, ring0 + ro + 4096);
          ew(m, G1, V1, G0, V0);
          SB();
        }
      }
    };
    for (int t = 0; t < ntiles; t += 2) {
      asm volatile("s_cmp_lt_u32 %0, 4\n\ts_cbranch_scc1 1f\n\ts_setprio 1\n1:" :: "s"(wave) : "scc");
      body(0);
      if (t + 1 >= ntiles) break;
      __builtin_amdgcn_s_setprio(0);
      body(1);
      lS4 += 256; lS1 += 256; lX += 256; lE += 256;
      asm volatile("" : "+v"(lS4), "+v"(lS1), "+v"(lX), "+v"(lE));
      adv += 256;
    }
    pq[14] = mulf(0, V1[14], rq[14]);
    pq[15] = mulf(1, V1[15], rq[15]);
    pn[1][3] = pack_bf16(pq[14], pq[15]);
    Y[1] = MFMA32(__builtin_bit_cast(bf16x8, pd[0]), S[0], Y[1]);
    Y[3] = MFMA32(__builtin_bit_cast(bf16x8, pn[0]), S[0], Y[3]);
    Y[1] = MFMA32(__builtin_bit_cast(bf16x8, pd[1]), S[1], Y[1]);
    Y[3] = MFMA32(__builtin_bit_cast(bf16x8, pn[1]), S[1], Y[3]);
    wb0 += LCAP;
    if (__builtin_expect(wb0 >= cnt_all, 1)) break;
    asm volatile("s_waitcnt vmcnt(0)" ::: "memory");
    cnt = min(cnt_all - wb0, LCAP);
    lS4 -= adv; lS1 -= adv; lX -= adv; lE -= adv;
    fill_slow(wb0);
    {
      int l2;
      asm volatile("v_mbcnt_lo_u32_b32 %0, -1, 0\n\tv_mbcnt_hi_u32_b32 %0, -1, %0" : "=v"(l2));
      convert(cnt, la + LRAW + l2 * 8, la + l2 * 4, l2);
    }
    issue_gather(0, 0);
    issue_gather(1, 1);
  }

  if (nodes > 0) {
    int lane;
    asm volatile("v_mbcnt_lo_u32_b32 %0, -1, 0\n\tv_mbcnt_hi_u32_b32 %0, -1, %0" : "=v"(lane));
    const int r = lane & 31, h = lane >> 5;
#pragma unroll
    for (int ct = 0; ct < 2; ++ct) {
      f32x16 U = zero16;
#pragma unroll
      for (int k = 0; k < 4; ++k) U = MFMA32(wl[(18 + k * 2 + ct) * 64], as[k], U);
#pragma unroll
      for (int q = 0; q < 2; ++q) {
        const u32x4 xv = __builtin_bit_cast(u32x4, as[2 * ct + q]);
        u32x4 ov;
#pragma unroll
        for (int j = 0; j < 4; ++j) {
          const int i = 8 * q + 2 * j;
          const float z0 = U[i] + Y[2 + ct][i] * __builtin_amdgcn_rcpf(Y[ct][i] + 1e-6f);
          const float z1 = U[i + 1] + Y[2 + ct][i + 1] * __builtin_amdgcn_rcpf(Y[ct][i + 1] + 1e-6f);
          const float o0 = __uint_as_float(xv[j] << 16) + (z0 > 0.f ? z0 : 0.f);
          const float o1 = __uint_as_float(xv[j] & 0xffff0000u) + (z1 > 0.f ? z1 : 0.f);
          ov[j] = pack_bf16(o0, o1);
        }
        *(LAS u32x4*)(size_t)(la + r * 128 + (((4 * h + 2 * ct + q) ^ (r & 7)) * 16)) = ov;
      }
    }
#pragma unroll
    for (int q = 0; q < 4; ++q) {
      const int row = 8 * q + (lane >> 3);
      const u32x4 ov = *(const LAS u32x4*)(size_t)(la + row * 128 + (((lane & 7) ^ (row & 7)) * 16));
      if (row < nodes) gstore128wt(out, (unsigned)n0 * 128u + q * 1024u + lane * 16u, ov);
    }
  }
  asm volatile("s_waitcnt vmcnt(0)" ::: "memory");
}

extern "C" void kernel_launch(void* const* d_in, const int* in_sizes, int n_in,
                              void* d_out, int out_size, void* d_ws, size_t ws_size,
                              hipStream_t stream) {
  const unsigned short* x = (const unsigned short*)d_in[0];
  const u32x4* eattr = (const u32x4*)d_in[1];
  const int* src = (const int*)d_in[2];
  const int* dst = (const int*)d_in[3];
  const unsigned short* wa = (const unsigned short*)d_in[4];
  const unsigned short* wb = (const unsigned short*)d_in[5];
  const unsigned short* wc = (const unsigned short*)d_in[6];
  const unsigned short* wu = (const unsigned short*)d_in[7];
  const unsigned short* wv = (const unsigned short*)d_in[8];
  unsigned short* out = (unsigned short*)d_out;
  const int N = in_sizes[0] / 64;
  const int E = in_sizes[2];
  const int chunk = (((E + NCH - 1) / NCH) + 3) & ~3;
  const int nbk = (N + 195) / 196;

  char* ws = (char*)d_ws;
  size_t off = 0;
  unsigned short* wpack = (unsigned short*)(ws + off);   off += 64 * 1024;
  u32x4* ENT = (u32x4*)(ws + off);                       off += (size_t)2 * NBK * NCH * CELL;
  unsigned* TS = (unsigned*)(ws + off);

  k_part<<<NCH + NFRAG, 1024, 0, stream>>>(src, dst, E, chunk, ENT, TS, wa, wb, wc, wu, wv, wpack);
  k_main<<<nbk, 512, 0, stream>>>(x, ENT, eattr, TS, chunk, (const bf16x8*)wpack, out, N
     );
}
```

```cpp
#include <hip/hip_runtime.h>
#include <hip/hip_bf16.h>
#include <stdint.h>
#include <stdio.h>
#define DBGDUMP 1
#define HELD 0x5500

using bf16x8 = __attribute__((ext_vector_type(8))) short;
using s16x4 = __attribute__((ext_vector_type(4))) short;
using f32x16 = __attribute__((ext_vector_type(16))) float;
using u32x4 = __attribute__((ext_vector_type(4))) unsigned;
using u32x2 = __attribute__((ext_vector_type(2))) unsigned;
using i32x4 = __attribute__((ext_vector_type(4))) int;
using u16x2 = __attribute__((ext_vector_type(2))) unsigned short;

#define MFMA32(a, b, c) __builtin_amdgcn_mfma_f32_32x32x16_bf16((a), (b), (c), 0, 0, 0)
#define LAS __attribute__((address_space(3)))
#define RANKOF(w) ((w) < 4 ? 7 - (w) : (w) - 4)
#define SB() __builtin_amdgcn_sched_barrier(0)

constexpr int NCH = 256;
constexpr int NBK = 256;
constexpr int NBIN = NBK * 8;
constexpr int NFRAG = 39;
constexpr int CHMAX = 3200;
constexpr float NLOG2E = -1.44269504088896340736f;
constexpr unsigned TPITCH = 80;

__device__ __forceinline__ unsigned pack_bf16(float lo, float hi) {
  typedef __attribute__((ext_vector_type(2))) float f2;
  typedef __attribute__((ext_vector_type(2))) __bf16 b2;
  f2 v = {lo, hi};
  b2 r = __builtin_convertvector(v, b2);
  return __builtin_bit_cast(unsigned, r);
}

__device__ __forceinline__ int wave_scan(int v) {
  v += __builtin_amdgcn_update_dpp(0, v, 0x111, 0xf, 0xf, false);
  v += __builtin_amdgcn_update_dpp(0, v, 0x112, 0xf, 0xf, false);
  v += __builtin_amdgcn_update_dpp(0, v, 0x114, 0xf, 0xf, false);
  v += __builtin_amdgcn_update_dpp(0, v, 0x118, 0xf, 0xf, false);
  v += __builtin_amdgcn_update_dpp(0, v, 0x142, 0xa, 0xf, false);
  v += __builtin_amdgcn_update_dpp(0, v, 0x143, 0xc, 0xf, false);
  return v;
}

__device__ __forceinline__ void glds16(const void* sbase, unsigned voff, unsigned lds_dst) {
  asm volatile(
      "s_mov_b32 m0, %2\n\t"
      "s_nop 0\n\t"
      "global_load_lds_dwordx4 %0, %1"
      :: "v"(voff), "s"(sbase), "s"(lds_dst) : "memory", "m0");
}
__device__ __forceinline__ void glds16nt(const void* sbase, unsigned voff, unsigned lds_dst) {
  asm volatile(
      "s_mov_b32 m0, %2\n\t"
      "s_nop 0\n\t"
      "global_load_lds_dwordx4 %0, %1 nt"
      :: "v"(voff), "s"(sbase), "s"(lds_dst) : "memory", "m0");
}
__device__ __forceinline__ void glds16x(const void* sbase, unsigned a, unsigned c, unsigned lds_dst) {
  unsigned tmp;
  asm volatile(
      "s_mov_b32 m0, %4\n\t"
      "v_mad_u32_u16 %0, %1, %5, %2\n\t"
      "global_load_lds_dwordx4 %0, %3"
      : "=&v"(tmp) : "v"(a), "v"(c), "s"(sbase), "s"(lds_dst), "s"(128u) : "memory", "m0");
}
__device__ __forceinline__ void glds16e(const void* sbase, unsigned a, unsigned c, unsigned lds_dst) {
  unsigned tmp;
  asm volatile(
      "s_mov_b32 m0, %4\n\t"
      "v_add_u32 %0, %1, %2\n\t"
      "global_load_lds_dwordx4 %0, %3"
      : "=&v"(tmp) : "v"(a), "v"(c), "s"(sbase), "s"(lds_dst) : "memory", "m0");
}
__device__ __forceinline__ int gload32(const void* sbase, unsigned voff) {
  int r;
  asm volatile("global_load_dword %0, %1, %2" : "=v"(r) : "v"(voff), "s"(sbase) : "memory");
  return r;
}
__device__ __forceinline__ u32x4 gload128(const void* sbase, unsigned voff) {
  u32x4 r;
  asm volatile("global_load_dwordx4 %0, %1, %2" : "=v"(r) : "v"(voff), "s"(sbase) : "memory");
  return r;
}

__device__ __forceinline__ u32x4 gload128nt(const void* sbase, unsigned voff) {
  u32x4 r;
  asm volatile("global_load_dwordx4 %0, %1, %2 nt" : "=v"(r) : "v"(voff), "s"(sbase) : "memory");
  return r;
}
__device__ __forceinline__ void gstore128wt(void* sbase, unsigned voff, u32x4 v) {
  asm volatile("global_store_dwordx4 %0, %1, %2 sc1\n\ts_nop 1" :: "v"(voff), "v"(v), "s"(sbase) : "memory");
}

constexpr int CELL = 128;
constexpr int CIN = 30;
constexpr int CCAP = 62;
constexpr int CPIT = 272;
constexpr unsigned E2OFF = NBK * NCH * CELL;
constexpr int KP_HDR = 0;
constexpr int KP_OVC = KP_HDR + NBK * 8;
constexpr int KP_ENT = KP_OVC + 16;
constexpr int KP_HIST = KP_ENT + NBK * CPIT;
constexpr int KP_LDS = KP_HIST + NBIN * 4;
constexpr int NWL = 26;
constexpr int NBLOB = 31;

__device__ __forceinline__ unsigned mad24(unsigned a, int b, unsigned c) {
  unsigned r;
  asm("v_mad_i32_i24 %0, %1, %2, %3" : "=v"(r) : "v"(a), "s"(b), "v"(c));
  return r;
}
__device__ __forceinline__ unsigned mul24(unsigned a, unsigned b) {
  unsigned r;
  asm("v_mul_u32_u24 %0, %1, %2" : "=v"(r) : "n"(b), "v"(a));
  return r;
}
__device__ __forceinline__ unsigned mulhi24(unsigned a, unsigned b) {
  return (unsigned)(((unsigned long long)(a & 0xffffffu) * (unsigned long long)b) >> 32);
}
__device__ __forceinline__ unsigned dot4(unsigned a, unsigned b, unsigned c) {
  return __builtin_amdgcn_udot4(a, b, c, false);
}

__global__ __launch_bounds__(1024) void k_part(const int* __restrict__ src, const int* __restrict__ dst,
                                               int E, int chunk, u32x4* __restrict__ ENT,
                                               unsigned* __restrict__ TS,
                                               const unsigned short* __restrict__ wa,
                                               const unsigned short* __restrict__ wb,
                                               const unsigned short* __restrict__ wc,
                                               const unsigned short* __restrict__ wu,
                                               const unsigned short* __restrict__ wv,
                                               unsigned short* __restrict__ wpack) {
  const int tid = threadIdx.x;
#define PT(i)
  PT(0);
  if (tid == 0 && blockIdx.x < NCH) ((unsigned long long*)(wpack + 20000))[blockIdx.x] = __builtin_amdgcn_s_memrealtime();
  if (blockIdx.x < NCH) {
    __shared__ __attribute__((aligned(16))) char sm[KP_LDS];
    LAS unsigned* hist = (LAS unsigned*)(sm + KP_HIST);
    LAS unsigned* ovc = (LAS unsigned*)(sm + KP_OVC);
    const int c = blockIdx.x;
    const int base = c * chunk;
    const int cnt = max(min(chunk, E - base), 0);
    const int i0 = max(min(base + 4 * tid, min(base + cnt, E) - 4), 0);
    const int lo = base + 4 * tid - i0, hi4 = base + cnt - i0;
    u32x4 d = gload128nt(dst, (unsigned)i0 * 4u);
    u32x4 sv = gload128nt(src, (unsigned)i0 * 4u);
    if (tid < NBK) *(LAS u32x2*)(sm + KP_HDR + tid * 8) = u32x2{0u, 0u};
    if (tid == 0) *(LAS u32x2*)ovc = u32x2{0u, 0u};
    const unsigned sm0 = (unsigned)(size_t)(LAS char*)sm;
    const bool act = __builtin_amdgcn_readfirstlane(tid) * 4 < cnt;
    unsigned long long on[4];
    unsigned tup[4];
#pragma unroll
    for (int k = 0; k < 4; ++k) {
      on[k] = __ballot(k >= lo && k < hi4);
      tup[k] = (unsigned)(i0 + k - base);
    }
    const int bk = tid >> 2, p0 = (tid & 3) * 16;
    unsigned ha = sm0 + KP_HDR + bk * 8, ia = sm0 + KP_ENT + bk * CPIT + p0;
    unsigned go = (unsigned)((bk * NCH + c) * CELL + p0);
    asm volatile("s_waitcnt lgkmcnt(0)\n\ts_barrier\n\ts_waitcnt vmcnt(1)"
                 : "+v"(d), "+v"(tup[0]), "+v"(tup[1]), "+v"(tup[2]), "+v"(tup[3]), "+v"(ha), "+v"(ia), "+v"(go),
                   "+s"(on[0]), "+s"(on[1]), "+s"(on[2]), "+s"(on[3])
                 :: "memory");
    PT(1);
    unsigned gi[4], rk[4], m0[4], m1[4], cb[4];
    if (act) {
      unsigned oh[4], sh[4];
#pragma unroll
      for (int k = 0; k < 4; ++k) {
        gi[k] = mulhi24(d[k] << 8, 684785u);
        sh[k] = gi[k] << 3;
        oh[k] = 1u << (sh[k] & 31u);
        rk[k] = 0u;
      }
      asm volatile(
          "s_mov_b64 exec, %12\n\t"
          "ds_add_rtn_u32 %0, %4, %8\n\t"
          "s_mov_b64 exec, %13\n\t"
          "ds_add_rtn_u32 %1, %5, %9\n\t"
          "s_mov_b64 exec, %14\n\t"
          "ds_add_rtn_u32 %2, %6, %10\n\t"
          "s_mov_b64 exec, %15\n\t"
          "ds_add_rtn_u32 %3, %7, %11\n\t"
          "s_mov_b64 exec, -1"
          : "+&v"(rk[0]), "+&v"(rk[1]), "+&v"(rk[2]), "+&v"(rk[3])
          : "v"((gi[0] & ~3u) + sm0), "v"((gi[1] & ~3u) + sm0), "v"((gi[2] & ~3u) + sm0), "v"((gi[3] & ~3u) + sm0),
            "v"(oh[0]), "v"(oh[1]), "v"(oh[2]), "v"(oh[3]), "s"(on[0]), "s"(on[1]), "s"(on[2]), "s"(on[3])
          : "memory");
#pragma unroll
      for (int k = 0; k < 4; ++k) {
        const unsigned slot = mad24(gi[k], -49 * 128, d[k] << 8) >> 8;
        tup[k] = mad24(slot, 3200, tup[k]);
        const unsigned hm = (unsigned)((int)(gi[k] << 29) >> 31);
        m0[k] = ((oh[k] - 1u) | hm) & 0x04040404u;
        m1[k] = (oh[k] - 1u) & hm & 0x04040404u;
        gi[k] &= ~7u;
        cb[k] = mul24(gi[k], CPIT / 8) + sm0;
        gi[k] += sm0;
      }
      SB();
      asm volatile("s_waitcnt lgkmcnt(0)" : "+v"(rk[0]), "+v"(rk[1]), "+v"(rk[2]), "+v"(rk[3]) :: "memory");
#pragma unroll
      for (int k = 0; k < 4; ++k) asm("v_bfe_u32 %0, %0, %1, 8" : "+v"(rk[k]) : "v"(sh[k]));
      if (max(max(rk[0], rk[1]), max(rk[2], rk[3])) >= (unsigned)CCAP) ovc[1] = 1u;
#pragma unroll
      for (int k = 0; k < 4; ++k) {
        rk[k] = cb[k] + rk[k] * 4;
        cb[k] += CCAP * 4;
      }
    }
    asm volatile("s_waitcnt vmcnt(0)" : "+v"(sv) :: "memory");
#pragma unroll
    for (int k = 0; k < 4; ++k) tup[k] = mad24(sv[k], 81920, tup[k]);
    asm volatile("s_barrier"
                 : "+v"(rk[0]), "+v"(rk[1]), "+v"(rk[2]), "+v"(rk[3]), "+v"(m0[0]), "+v"(m0[1]), "+v"(m0[2]),
                   "+v"(m0[3]), "+v"(m1[0]), "+v"(m1[1]), "+v"(m1[2]), "+v"(m1[3]), "+v"(tup[0]), "+v"(tup[1]),
                   "+v"(tup[2]), "+v"(tup[3]), "+v"(cb[0]), "+v"(cb[1]), "+v"(cb[2]), "+v"(cb[3]), "+v"(gi[0]),
                   "+v"(gi[1]), "+v"(gi[2]), "+v"(gi[3])
                 :: "memory");
    PT(2);
    u32x2 hc = *(const LAS u32x2*)(size_t)ha;
    if (act) {
      u32x2 hd[4];
      unsigned pa[4];
#pragma unroll
      for (int k = 0; k < 4; ++k) hd[k] = *(const LAS u32x2*)(size_t)gi[k];
#pragma unroll
      for (int k = 0; k < 4; ++k) pa[k] = min(dot4(hd[k][0], m0[k], dot4(hd[k][1], m1[k], rk[k])), cb[k]);
      asm volatile(
          "s_mov_b64 exec, %8\n\t"
          "ds_write_b32 %0, %4 offset:%12\n\t"
          "s_mov_b64 exec, %9\n\t"
          "ds_write_b32 %1, %5 offset:%12\n\t"
          "s_mov_b64 exec, %10\n\t"
          "ds_write_b32 %2, %6 offset:%12\n\t"
          "s_mov_b64 exec, %11\n\t"
          "ds_write_b32 %3, %7 offset:%12\n\t"
          "s_mov_b64 exec, -1"
          :: "v"(pa[0]), "v"(pa[1]), "v"(pa[2]), "v"(pa[3]), "v"(tup[0]), "v"(tup[1]), "v"(tup[2]), "v"(tup[3]),
             "s"(on[0]), "s"(on[1]), "s"(on[2]), "s"(on[3]), "n"(KP_ENT + 8)
          : "memory");
    }
    unsigned used = dot4(hc[0], 0x01010101u, dot4(hc[1], 0x01010101u, 0u));
    if (used > (unsigned)CCAP) ovc[1] = 1u;
    asm volatile("s_waitcnt lgkmcnt(0)\n\ts_barrier" ::: "memory");
    PT(3); PT(4);
    const unsigned ovf = ovc[1];
    u32x4 v0 = *(const LAS u32x4*)(size_t)ia;
    u32x4 v1 = *(const LAS u32x4*)(size_t)(ia + 64);
    asm volatile("" : "+v"(v0), "+v"(v1));
    if (__builtin_expect(__builtin_amdgcn_readfirstlane(ovf) != 0u, 0)) {
      *(LAS u32x2*)(hist + tid * 2) = u32x2{0u, 0u};
      __syncthreads();
#pragma unroll
      for (int k = 0; k < 4; ++k) {
        gi[k] = mulhi24(d[k] << 8, 684785u);
        if (k >= lo && k < hi4)
          rk[k] = __hip_atomic_fetch_add(hist + gi[k], 1u, __ATOMIC_RELAXED, __HIP_MEMORY_SCOPE_WORKGROUP);
      }
      __syncthreads();
      if (tid < NBK) {
        unsigned tot = 0;
        for (int i = 0; i < 8; ++i) tot += hist[tid * 8 + i];
        LAS unsigned* cp = (LAS unsigned*)(sm + KP_ENT + tid * CPIT);
        unsigned p = ~0u;
        if (tot > (unsigned)CCAP) {
          p = (unsigned)base + __hip_atomic_fetch_add(ovc, tot, __ATOMIC_RELAXED, __HIP_MEMORY_SCOPE_WORKGROUP);
          *(LAS u32x2*)(sm + KP_HDR + tid * 8) = u32x2{~0u, ~0u};
        }
        for (int i = 0; i < 8; ++i) {
          const unsigned n = hist[tid * 8 + i];
          hist[tid * 8 + i] = p;
          if (p != ~0u) {
            cp[2 + i] = 0u;
            cp[10 + i] = p;
            cp[18 + i] = n;
            p += n;
          }
        }
      }
      __syncthreads();
#pragma unroll
      for (int k = 0; k < 4; ++k) {
        if (k >= lo && k < hi4) {
          const unsigned p = hist[gi[k]];
          if (p != ~0u) TS[p + rk[k]] = tup[k];
        }
      }
      hc = *(const LAS u32x2*)(size_t)ha;
      used = dot4(hc[0], 0x01010101u, dot4(hc[1], 0x01010101u, 0u));
      v0 = *(const LAS u32x4*)(size_t)ia;
      v1 = *(const LAS u32x4*)(size_t)(ia + 64);
    }
    if (p0 == 0) { v0[0] = hc[0]; v0[1] = hc[1]; }
    gstore128wt(ENT, go, v0);
    if (used > 14u) gstore128wt(ENT, go + 64, v1);
    if (__builtin_expect(used > (unsigned)CIN, 0)) {
      gstore128wt(ENT, go + E2OFF, *(const LAS u32x4*)(size_t)(ia + 128));
      gstore128wt(ENT, go + E2OFF + 64, *(const LAS u32x4*)(size_t)(ia + 192));
    }
  } else if (tid < 512) {
    const int f = blockIdx.x - NCH;
    unsigned short v;
    if (f >= NWL && f < NBLOB) {
      const int e = (f - NWL) * 512 + tid;
      const int tb = e >= 32 * (TPITCH / 2);
      const int e2 = e - tb * 32 * (TPITCH / 2);
      const int sl = e2 / (TPITCH / 2), col = e2 - sl * (TPITCH / 2);
      const int nd = tb ? 16 * ((col >> 3) & 1) + 8 * ((col >> 2) & 1) + 4 * (col >> 4) + (col & 3) : col;
      v = (col < 32 && nd == sl) ? 0x3F80 : 0;
    } else {
      const int lane = tid >> 3, j = tid & 7;
      const int n = lane & 31, h = lane >> 5;
      const int g = f < 16 ? f : f < NWL ? f - 18 : f - NBLOB;
      const int ct = g & 1;
      const int col = 32 * ((n >> 2) & 1) + 16 * ct + 4 * (n >> 3) + (n & 3);
      if (f == 16 || f == 17) {
        v = wc[(8 * h + j) * 64 + col];
      } else {
        const int t = (g >> 1) & 3;
        const unsigned short* W = f < 8 ? wb : f < 16 ? wv : f < NWL ? wu : wa;
        v = W[(32 * h + 8 * t + j) * 64 + col];
      }
      if (f < 8 || f == 16 || f == 17 || f >= NBLOB)
        v = (unsigned short)(pack_bf16(__uint_as_float((unsigned)v << 16) * NLOG2E, 0.f) & 0xffffu);
    }
    wpack[f * 512 + tid] = v;
  }
}

constexpr int RING_SLOT = 5 * 1024;
constexpr int SPIT = 1040;
constexpr int LCAP = 512;
constexpr int LARR = 2560;
constexpr int LIST_BYTES = 2 * LARR;
constexpr int LRAW = LIST_BYTES - (LCAP + 8) * 8;
constexpr int OFF_W = 0;
constexpr int OFF_T1 = NWL * 1024;
constexpr int OFF_T2 = OFF_T1 + 32 * TPITCH;
constexpr int OFF_WA = NBLOB * 1024;
constexpr int OFF_R0 = NFRAG * 1024;
constexpr int OFF_R1 = OFF_R0 + 8 * RING_SLOT;
constexpr int OFF_L = OFF_R1 + 8 * RING_SLOT;
constexpr int MAIN_LDS = OFF_L + 8 * LIST_BYTES;
static_assert(MAIN_LDS <= 160 * 1024, "LDS");

__device__ __forceinline__ float add1(float t) { return 1.0f + t; }
__device__ __forceinline__ float mulf(int i, float a, float b) { return (i & 1) ? __builtin_fmaf(a, b, 0.0f) : a * b; }

__global__ __launch_bounds__(512, 2) void k_main(const unsigned short* __restrict__ x,
                                                 const u32x4* __restrict__ ENT,
                                                 const u32x4* __restrict__ ES,
                                                 const unsigned* __restrict__ TS, int chunk,
                                                 const bf16x8* __restrict__ wpack,
                                                 unsigned short* __restrict__ out, int N
                                                 , long long* dbg
                                                 ) {
  const long long tm0 = __builtin_amdgcn_s_memrealtime();
  __shared__ __attribute__((aligned(16))) char smem[MAIN_LDS];
  const int tid = threadIdx.x, lane = tid & 63;
  const int wave = __builtin_amdgcn_readfirstlane(tid >> 6);
  const int r = lane & 31, h = lane >> 5;
  const int bk = blockIdx.x;
  const unsigned lds0 = (unsigned)(size_t)(LAS char*)smem;

  int warm;
  {
    const unsigned vo = lane * 16;
    const char* eb = reinterpret_cast<const char*>(ENT) + (size_t)bk * (NCH * CELL);
#pragma unroll
    for (int i = 0; i < 4; ++i) glds16(eb + (wave * 4 + i) * 1024, vo, lds0 + OFF_R1 + (wave * 4 + i) * SPIT);
    const char* wp = reinterpret_cast<const char*>(wpack);
#pragma unroll
    for (int i = 0; i < 5; ++i) {
      const int f = min(wave + 8 * i, NFRAG - 1);
      glds16(wp + f * 1024, vo, lds0 + OFF_W + f * 1024);
    }
    warm = gload32(x, (unsigned)min(bk * 196 + 25 * wave + min(lane, 24), N - 1) * 128u);
    if (wave >= 4) __builtin_amdgcn_s_setprio(1);
  }
  LAS char* lbase = (LAS char*)smem + OFF_L + wave * LIST_BYTES;
  const unsigned la = (unsigned)(size_t)lbase;
  auto put = [&](int p, unsigned a, unsigned b) { *(LAS u32x2*)(lbase + LRAW + p * 8) = u32x2{a, b}; };
  unsigned aLX = la + (lane >> 3) * 16;
  unsigned aLE = la + LARR + ((((lane >> 1) & 7) << 2) | (lane >> 4)) * 4;
  unsigned aLS4 = la + (4 * h + ((lane >> 2) & 3)) * 16;
  unsigned aLS1 = la + (((r & 7) << 2) | (r >> 3)) * 4 + 2;
  unsigned cx0 = (unsigned)(((lane & 7) ^ (lane >> 4)) * 16);
  unsigned cx1 = cx0 ^ 64u;
  unsigned ce = (unsigned)((((lane & 1) ^ ((lane >> 4) & 1))) * 16);
  const unsigned ring0 = lds0 + OFF_R0 + wave * RING_SLOT;
  const unsigned ringd = 8 * RING_SLOT;
  unsigned cS = lds0 + OFF_T1 + (((lane >> 4) & 1) * 16 + (lane & 3) * 4) * 2;
  unsigned cSt = lds0 + OFF_T2 + h * 32;
  unsigned fo[4];
#pragma unroll
  for (int t = 0; t < 4; ++t) fo[t] = ring0 + (unsigned)(r * 128 + (((4 * h + t) ^ ((r >> 1) & 7)) * 16));
  unsigned foe = ring0 + (unsigned)(4096 + r * 32 + ((h ^ ((r >> 3) & 1)) * 16));
  unsigned sa = lds0 + OFF_R1 + r * SPIT + h * CELL;
  unsigned cb0 = (unsigned)((8 * r + h) * chunk);
  unsigned aR = la + LRAW + lane * 8, aF = la + lane * 4;
  unsigned aRX = la + LRAW + (lane >> 3) * 32, aRE = la + LRAW + ((((lane >> 1) & 7) << 2) | (lane >> 4)) * 8;
  f32x16 Y[4];
#pragma unroll
  for (int q = 0; q < 4; ++q)
#pragma unroll
    for (int i = 0; i < 16; ++i) Y[q][i] = 0.f;
  f32x16 G0, V0, G1, V1;
  bf16x8 S[2], St[2], as[4], ae, b[3];
  u32x4 pd[2], pn[2];
  float tq[16], rq[16], pq[16];
  auto pipe_zero = [&]() {
#pragma unroll
    for (int j = 0; j < 8; ++j) { S[0][j] = 0; S[1][j] = 0; }
#pragma unroll
    for (int j = 0; j < 4; ++j) { pd[0][j] = 0; pd[1][j] = 0; pn[0][j] = 0; pn[1][j] = 0; }
#pragma unroll
    for (int i = 0; i < 16; ++i) V1[i] = 0.f;
    rq[14] = 0.f; rq[15] = 0.f;
  };
  asm volatile("" : "+v"(aLX), "+v"(aLE), "+v"(aLS4), "+v"(aLS1), "+v"(cx0), "+v"(cx1), "+v"(ce), "+v"(cS), "+v"(cSt),
                    "+v"(fo[0]), "+v"(fo[1]), "+v"(fo[2]), "+v"(fo[3]), "+v"(foe), "+v"(sa), "+v"(cb0), "+v"(aR),
                    "+v"(aF), "+v"(aRX), "+v"(aRE));
  asm volatile("" : "+v"(Y[0]), "+v"(Y[1]), "+v"(Y[2]), "+v"(Y[3]));
  unsigned rsh = (lane & 3) * 8, rid = lane & 7;
  unsigned long long rhi = 0xf0f0f0f0f0f0f0f0ull;
  asm volatile("" : "+v"(rsh), "+v"(rid), "+s"(rhi));
  asm volatile("s_waitcnt vmcnt(6)\n\ts_barrier" ::: "memory");
  const long long tmA = __builtin_amdgcn_s_memrealtime();
  u32x2 cl[4][4], hd[4];
  int nr[4], st[4], ni[4];
  unsigned long long mk[4][4];
#pragma unroll
  for (int j = 0; j < 4; ++j) hd[j] = *(const LAS u32x2*)(size_t)(sa + 2 * j * CELL);
  int gb;
  {
    int lo = (int)(hd[0][0] + hd[1][0] + hd[2][0] + hd[3][0]), hi = (int)(hd[0][1] + hd[1][1] + hd[2][1] + hd[3][1]);
    lo += __builtin_amdgcn_update_dpp(0, lo, 0xb1, 0xf, 0xf, false);
    hi += __builtin_amdgcn_update_dpp(0, hi, 0xb1, 0xf, 0xf, false);
    lo += __builtin_amdgcn_update_dpp(0, lo, 0x4e, 0xf, 0xf, false);
    hi += __builtin_amdgcn_update_dpp(0, hi, 0x4e, 0xf, 0xf, false);
    lo += __builtin_amdgcn_update_dpp(0, lo, 0x141, 0xf, 0xf, false);
    hi += __builtin_amdgcn_update_dpp(0, hi, 0x141, 0xf, 0xf, false);
    lo += __builtin_amdgcn_update_dpp(0, lo, 0x140, 0xf, 0xf, false);
    hi += __builtin_amdgcn_update_dpp(0, hi, 0x140, 0xf, 0xf, false);
    unsigned c;
    asm("v_cndmask_b32 %0, %1, %2, %3\n\tv_bfe_u32 %0, %0, %4, 8" : "=&v"(c) : "v"(lo), "v"(hi), "s"(rhi), "v"(rsh));
    auto p16 = __builtin_amdgcn_permlane16_swap(c, c, false, false);
    c = p16[0] + p16[1];
    auto p32 = __builtin_amdgcn_permlane32_swap(c, c, false, false);
    const int key = (int)(((p32[0] + p32[1]) << 3) | rid);
    unsigned acc = 0u;
#define RK(k) acc = __builtin_amdgcn_alignbit(acc, (unsigned)(__builtin_amdgcn_update_dpp(0, key, 0x120 + k, 0xf, 0xf, false) - key), 31)
    RK(1); RK(2); RK(3); RK(4); RK(5); RK(6); RK(7);
#undef RK
    const int want = RANKOF(wave);
    gb = __builtin_ctzll(__ballot(__builtin_popcount(acc) == want)) & 7;
  }
  const int nb0 = bk * 196 + 49 * (gb >> 1) + 25 * (gb & 1);
  const int nodes = max(min(25 - (gb & 1), N - nb0), 0);
  const int n0 = min(nb0, N - 1);
  const unsigned mlo = gb >= 4 ? 0x01010101u : 0x01010101u & ((1u << (8 * gb)) - 1u);
  const unsigned mhi = gb <= 4 ? 0u : 0x01010101u & ((1u << (8 * (gb - 4))) - 1u);
  u32x4 xg[4];
  {
    const unsigned xo = (unsigned)(n0 + max(min(r, nodes - 1), 0)) * 128u + 64u * h;
#pragma unroll
    for (int t = 0; t < 4; ++t) xg[t] = gload128(x, xo + 16 * t);
  }
  auto head = [&](int j) {
    nr[j] = (int)(((gb >= 4 ? hd[j][1] : hd[j][0]) >> (8 * (gb & 3))) & 0xffu);
    st[j] = (int)dot4(hd[j][0], mlo, dot4(hd[j][1], mhi, 0u));
    const int n = min(min(nr[j], 8), CIN - st[j]);
#pragma unroll
    for (int q = 0; q < 4; ++q) mk[j][q] = __ballot(n > 2 * q);
    ni[j] = max(n, 0);
  };
  auto fetch = [&](int j) {
    asm volatile(
        "s_mov_b64 exec, %5\n\t"
        "ds_read2_b32 %0, %4 offset0:%9 offset1:%9+1\n\t"
        "s_mov_b64 exec, %6\n\t"
        "ds_read2_b32 %1, %4 offset0:%9+2 offset1:%9+3\n\t"
        "s_mov_b64 exec, %7\n\t"
        "ds_read2_b32 %2, %4 offset0:%9+4 offset1:%9+5\n\t"
        "s_mov_b64 exec, %8\n\t"
        "ds_read2_b32 %3, %4 offset0:%9+6 offset1:%9+7\n\t"
        "s_mov_b64 exec, -1"
        : "=&v"(cl[j][0]), "=&v"(cl[j][1]), "=&v"(cl[j][2]), "=&v"(cl[j][3])
        : "v"(sa + st[j] * 4), "s"(mk[j][0]), "s"(mk[j][1]), "s"(mk[j][2]), "s"(mk[j][3]), "n"(j * 64 + 2)
        : "memory");
  };
  auto fill = [&](int j, int p) {
    asm volatile(
        "s_mov_b64 exec, %13\n\t"
        "ds_write2_b32 %0, %9, %1 offset0:14 offset1:15\n\t"
        "ds_write2_b32 %0, %8, %1 offset0:12 offset1:13\n\t"
        "s_mov_b64 exec, %12\n\t"
        "ds_write2_b32 %0, %7, %1 offset0:10 offset1:11\n\t"
        "ds_write2_b32 %0, %6, %1 offset0:8 offset1:9\n\t"
        "s_mov_b64 exec, %11\n\t"
        "ds_write2_b32 %0, %5, %1 offset0:6 offset1:7\n\t"
        "ds_write2_b32 %0, %4, %1 offset0:4 offset1:5\n\t"
        "s_mov_b64 exec, %10\n\t"
        "ds_write2_b32 %0, %3, %1 offset0:2 offset1:3\n\t"
        "ds_write2_b32 %0, %2, %1 offset0:0 offset1:1\n\t"
        "s_mov_b64 exec, -1"
        :: "v"(la + LRAW + p * 8), "v"(cb0 + 2 * j * chunk), "v"(cl[j][0][0]), "v"(cl[j][0][1]), "v"(cl[j][1][0]),
           "v"(cl[j][1][1]), "v"(cl[j][2][0]), "v"(cl[j][2][1]), "v"(cl[j][3][0]), "v"(cl[j][3][1]), "s"(mk[j][0]),
           "s"(mk[j][1]), "s"(mk[j][2]), "s"(mk[j][3])
        : "memory");
  };
  auto src_of = [&](unsigned v) { return mulhi24(v >> 8, 13421773u); };
  auto decode = [&](unsigned v, unsigned cb, unsigned& a, unsigned& e) {
    unsigned sl;
    asm("v_lshrrev_b32 %0, 8, %3\n\t"
        "v_mul_hi_u32_u24 %0, 0xcccccd, %0\n\t"
        "v_mad_i32_i24 %1, %0, %5, %3\n\t"
        "v_mul_hi_u32_u24 %2, 0x147ae2, %1\n\t"
        "v_add_lshl_u32 %1, %1, %4, 5\n\t"
        "v_mad_i32_i24 %0, %2, %6, %0\n\t"
        "v_mad_i32_i24 %1, %2, %7, %1"
        : "=&v"(a), "=&v"(e), "=&v"(sl)
        : "v"(v), "v"(cb), "s"(-81920), "s"((int)(TPITCH << 16)), "s"(-102400));
  };
  auto decode2 = [&](const u32x2 p, const u32x2 q, unsigned& a, unsigned& e, unsigned& c, unsigned& f) {
    unsigned sl, sm;
    asm("v_lshrrev_b32 %0, 8, %6\n\t"
        "v_lshrrev_b32 %3, 8, %8\n\t"
        "v_mul_hi_u32_u24 %0, 0xcccccd, %0\n\t"
        "v_mul_hi_u32_u24 %3, 0xcccccd, %3\n\t"
        "v_mad_i32_i24 %1, %0, %10, %6\n\t"
        "v_mad_i32_i24 %4, %3, %10, %8\n\t"
        "v_mul_hi_u32_u24 %2, 0x147ae2, %1\n\t"
        "v_mul_hi_u32_u24 %5, 0x147ae2, %4\n\t"
        "v_add_lshl_u32 %1, %1, %7, 5\n\t"
        "v_add_lshl_u32 %4, %4, %9, 5\n\t"
        "v_mad_i32_i24 %0, %2, %11, %0\n\t"
        "v_mad_i32_i24 %3, %5, %11, %3\n\t"
        "v_mad_i32_i24 %1, %2, %12, %1\n\t"
        "v_mad_i32_i24 %4, %5, %12, %4"
        : "=&v"(a), "=&v"(e), "=&v"(sl), "=&v"(c), "=&v"(f), "=&v"(sm)
        : "v"(p[0]), "v"(p[1]), "v"(q[0]), "v"(q[1]), "s"(-81920), "s"((int)(TPITCH << 16)), "s"(-102400));
  };
  auto ld128 = [&](unsigned a) { return *(const LAS u32x4*)(size_t)a; };
  auto gather5 = [&](const u32x4 sx, unsigned re, int rs) {
    const unsigned dstb = ring0 + rs * ringd;
    glds16x(x, sx[0], cx0, dstb);
    glds16x(x, sx[1], cx1, dstb + 1024);
    glds16x(x, sx[2], cx0, dstb + 2048);
    glds16x(x, sx[3], cx1, dstb + 3072);
    glds16e(ES, re, ce, dstb + 4096);
  };
  auto issue_gather = [&](int tile, int rs) {
    gather5(ld128(aLX + tile * 128), *(const LAS unsigned*)(size_t)(aLE + tile * 128), rs);
  };
  head(0);
  fetch(0);
  SB();
  const int s0 = wave_scan(ni[0]);
  head(1);
  head(2);
  head(3);
  const int tot0 = __builtin_amdgcn_readlane(s0, 63);
  const int p0 = s0 - ni[0];
  SB();
  asm volatile("s_waitcnt lgkmcnt(0)" : "+v"(cl[0][0]), "+v"(cl[0][1]), "+v"(cl[0][2]), "+v"(cl[0][3]) :: "memory");
  fill(0, p0);
  bool early = tot0 >= 32;
  int s123;
  {
    unsigned v0 = *(const LAS unsigned*)(size_t)aRX, v1 = *(const LAS unsigned*)(size_t)(aRX + 8);
    unsigned v2 = *(const LAS unsigned*)(size_t)(aRX + 16), v3 = *(const LAS unsigned*)(size_t)(aRX + 24);
    u32x2 ve = *(const LAS u32x2*)(size_t)aRE;
    SB();
    s123 = wave_scan(ni[1] | (ni[2] << 10) | (ni[3] << 20));
    SB();
    asm volatile("" : "+v"(v0), "+v"(v1), "+v"(v2), "+v"(v3), "+v"(ve), "+v"(s123));
    if (__builtin_expect(early, 1)) {
      unsigned a, e;
      decode(ve[0], ve[1], a, e);
      gather5(u32x4{src_of(v0), src_of(v1), src_of(v2), src_of(v3)}, e, 0);
    } else {
      gather5(u32x4{0u, 0u, 0u, 0u}, 0u, 0);
    }
  }
  fetch(1);
  fetch(2);
  fetch(3);
  const int e123 = __builtin_amdgcn_readlane(s123, 63);
  const int tot1 = tot0 + (e123 & 0x3ff), tot2 = tot1 + ((e123 >> 10) & 0x3ff);
  int cnt_all = tot2 + (e123 >> 20);
  asm volatile("s_waitcnt vmcnt(5) lgkmcnt(0)\n\ts_barrier"
               : "+v"(xg[0]), "+v"(xg[1]), "+v"(xg[2]), "+v"(xg[3]), "+v"(warm),
                 "+v"(cl[1][0]), "+v"(cl[1][1]), "+v"(cl[1][2]), "+v"(cl[1][3]), "+v"(cl[2][0]), "+v"(cl[2][1]),
                 "+v"(cl[2][2]), "+v"(cl[2][3]), "+v"(cl[3][0]), "+v"(cl[3][1]), "+v"(cl[3][2]), "+v"(cl[3][3])
               :: "memory");
  const LAS bf16x8* wl = (const LAS bf16x8*)(smem + OFF_W) + lane;
  const f32x16 zero16 = {0.f, 0.f, 0.f, 0.f, 0.f, 0.f, 0.f, 0.f, 0.f, 0.f, 0.f, 0.f, 0.f, 0.f, 0.f, 0.f};
  f32x16 XA[2] = {zero16, zero16};
  bf16x8 wa[8];
#pragma unroll
  for (int k = 0; k < 8; ++k) wa[k] = wl[(NBLOB + k) * 64];
  SB();
  auto xa_step = [&](int k) {
#pragma unroll
    for (int ct = 0; ct < 2; ++ct) XA[ct] = MFMA32(__builtin_bit_cast(bf16x8, xg[k]), wa[k * 2 + ct], XA[ct]);
    SB();
  };
  if (__builtin_expect(cnt_all > LCAP, 0)) {
#pragma unroll
    for (int j = 1; j < 4; ++j)
#pragma unroll
      for (int q = 0; q < 4; ++q) mk[j][q] = 0ull;
  }
  fill(1, tot0 + (s123 & 0x3ff) - ni[1]);
  SB();
  xa_step(0);
  fill(2, tot1 + ((s123 >> 10) & 0x3ff) - ni[2]);
  SB();
  xa_step(1);
  fill(3, tot2 + (s123 >> 20) - ni[3]);
  SB();
  xa_step(2);
  xa_step(3);
  if (__builtin_expect(__ballot(nr[0] + nr[1] + nr[2] + nr[3] != ni[0] + ni[1] + ni[2] + ni[3]) != 0ull, 0)) {
#pragma unroll
    for (int j = 0; j < 4; ++j) {
      unsigned long long om = __ballot(nr[j] != ni[j]);
      while (om) {
        const int l = __builtin_ctzll(om);
        om &= om - 1;
        const int n = __builtin_amdgcn_readlane(nr[j] - ni[j], l);
        const int p = __builtin_amdgcn_readlane(st[j] + ni[j], l) + lane;
        const int cc = 8 * (l & 31) + (l >> 5) + 2 * j;
        if (n > CCAP) cnt_all = LCAP;
        if (cnt_all + n <= LCAP && lane < n) {
          const unsigned* cp = reinterpret_cast<const unsigned*>(reinterpret_cast<const char*>(ENT) +
                                                                 ((size_t)bk * NCH + cc) * CELL);
          put(cnt_all + lane, p < CIN ? cp[2 + p] : cp[E2OFF / 4 + p - CIN], (unsigned)(cc * chunk));
        }
        cnt_all += n;
      }
    }
    __builtin_amdgcn_s_waitcnt(0x0F70);
  }
  const int gbin = bk * 8 + wave;
  auto fill_slow = [&](int wb0) __attribute__((always_inline)) {
    int nn[4], sp[4], tot = 0;
    const unsigned* rp[4];
    int l2;
    asm volatile("v_mbcnt_lo_u32_b32 %0, -1, 0\n\tv_mbcnt_hi_u32_b32 %0, -1, %0" : "=v"(l2));
    const int ch0 = 8 * (l2 & 31) + (l2 >> 5);
#pragma unroll
    for (int j = 0; j < 4; ++j) {
      const unsigned* cp = reinterpret_cast<const unsigned*>(reinterpret_cast<const char*>(ENT) +
                                                             ((size_t)bk * NCH + ch0 + 2 * j) * CELL);
      const unsigned h0 = cp[0], h1 = cp[1];
      nn[j] = (int)(((gb >= 4 ? h1 : h0) >> (8 * (gb & 3))) & 0xffu);
      sp[j] = (int)dot4(h0, mlo, dot4(h1, mhi, 0u));
      rp[j] = cp + 2 + sp[j];
      sp[j] = CIN - sp[j];
      if (nn[j] == 0xff) {
        rp[j] = TS + cp[10 + gb];
        nn[j] = (int)cp[18 + gb];
        sp[j] = nn[j];
      }
      tot += nn[j];
    }
    const int sc = wave_scan(tot);
    int pp = sc - tot - wb0;
#pragma unroll
    for (int j = 0; j < 4; ++j) {
      const int cb = (ch0 + 2 * j) * chunk;
      const int te = min(nn[j], LCAP - pp);
#pragma unroll 1
      for (int t = max(0, -pp); t < te; ++t) put(pp + t, rp[j][t < sp[j] ? t : t + (int)(E2OFF / 4) - 32], (unsigned)cb);
      pp += nn[j];
    }
    __builtin_amdgcn_s_waitcnt(0x0F70);
    return __builtin_amdgcn_readlane(sc, 63);
  };
  if (__builtin_expect(cnt_all > LCAP, 0)) {
    asm volatile("s_waitcnt vmcnt(0)" ::: "memory");
    cnt_all = fill_slow(0);
    early = false;
  }
  cnt_all = __builtin_amdgcn_readfirstlane(cnt_all);
  int cnt = min(cnt_all, LCAP);
  auto convert = [&](int cnt, unsigned aR, unsigned aF, int lane) __attribute__((always_inline)) {
    u32x2 v[8];
#pragma unroll
    for (int k = 0; k < 8; ++k) v[k] = *(const LAS u32x2*)(size_t)(aR + k * 512);
    asm volatile("" ::: "memory");
#pragma unroll
    for (int k = 0; k < 6; k += 2) {
      unsigned a, e, c, f;
      decode2(v[k], v[k + 1], a, e, c, f);
      *(LAS unsigned*)(size_t)(aF + k * 256) = a;
      *(LAS unsigned*)(size_t)(aF + k * 256 + 256) = c;
      *(LAS unsigned*)(size_t)(aF + k * 256 + LARR) = e;
      *(LAS unsigned*)(size_t)(aF + k * 256 + LARR + 256) = f;
    }
#pragma unroll
    for (int k = 6; k < 8; ++k) {
      if (cnt > k * 64) {
        unsigned a, e;
        decode(v[k][0], v[k][1], a, e);
        *(LAS unsigned*)(size_t)(aF + k * 256) = a;
        *(LAS unsigned*)(size_t)(aF + k * 256 + LARR) = e;
      }
    }
    const int u = cnt + lane;
    const unsigned padA = (unsigned)(n0 + max(min(((u >> 2) & 7) + 8 * (u & 3), nodes - 1), 0)) | (31u * TPITCH << 16);
    const unsigned pa = aF + cnt * 4;
    *(LAS unsigned*)(size_t)pa = padA;
    *(LAS unsigned*)(size_t)(pa + 256) = padA;
    *(LAS unsigned*)(size_t)(pa + LARR) = 0u;
    *(LAS unsigned*)(size_t)(pa + LARR + 256) = 0u;
  };
  convert(cnt, aR, aF, lane);
  issue_gather(1, 1);
  if (__builtin_expect(!early, 0)) {
    asm volatile("s_waitcnt vmcnt(0)" ::: "memory");
    issue_gather(0, 0);
    issue_gather(1, 1);
  }
  bf16x8 XAf[2][2];
#pragma unroll
  for (int ct = 0; ct < 2; ++ct)
#pragma unroll
    for (int s = 0; s < 2; ++s) {
      u32x4 p;
#pragma unroll
      for (int j = 0; j < 4; ++j) p[j] = pack_bf16(XA[ct][8 * s + 2 * j], XA[ct][8 * s + 2 * j + 1]);
      XAf[s][ct] = __builtin_bit_cast(bf16x8, p);
    }

  auto rd128 = [&](unsigned a) { return *(const LAS bf16x8*)(size_t)a; };
  auto load_S = [&](const u32x4 ss) {
    const s16x4 a0 = __builtin_amdgcn_ds_read_tr16_b64_v4i16((LAS s16x4*)(size_t)((ss[0] >> 16) + cS));
    const s16x4 a1 = __builtin_amdgcn_ds_read_tr16_b64_v4i16((LAS s16x4*)(size_t)((ss[1] >> 16) + cS));
    const s16x4 a2 = __builtin_amdgcn_ds_read_tr16_b64_v4i16((LAS s16x4*)(size_t)((ss[2] >> 16) + cS));
    const s16x4 a3 = __builtin_amdgcn_ds_read_tr16_b64_v4i16((LAS s16x4*)(size_t)((ss[3] >> 16) + cS));
    S[0] = __builtin_shufflevector(a0, a1, 0, 1, 2, 3, 4, 5, 6, 7);
    S[1] = __builtin_shufflevector(a2, a3, 0, 1, 2, 3, 4, 5, 6, 7);
  };
  auto widx = [&](int m, int ct) { return m == 6 ? 16 + ct : m < 11 ? (m - 7) * 2 + ct : 8 + (m - 11) * 2 + ct; };

  bf16x8 wr[2][15];
#pragma unroll
  for (int ct = 0; ct < 2; ++ct)
#pragma unroll
    for (int m = 6; m < 15; ++m)
      if ((HELD >> m) & 1) {
        wr[ct][m] = wl[widx(m, ct) * 64];
        asm volatile("" : "+v"(wr[ct][m]));
      }
  auto wf = [&](int m, int ct) { return ((HELD >> m) & 1) ? wr[ct][m] : b[(m - 6) % 3]; };

  auto ew = [&](int m, const f32x16& Gc, const f32x16& Vc, const f32x16& Gn, const f32x16& Vp) {
    if (m < 2) {
      const int i = 14 + m;
      pq[i] = mulf(i, Vp[i], rq[i]);
      if (m == 1) pn[1][3] = pack_bf16(pq[14], pq[15]);
    } else if (m == 2) {
      pq[0] = mulf(0, Vc[0], rq[0]);
      pq[1] = mulf(1, Vc[1], rq[1]);
      pn[0][0] = pack_bf16(pq[0], pq[1]);
    } else {
      const int i = m - 1;
      pq[i] = mulf(i, Vc[i], rq[i]);
      if (i & 1) pn[i >> 3][(i >> 1) & 3] = pack_bf16(pq[i - 1], pq[i]);
    }
    if (m == 0) pd[0][0] = pack_bf16(rq[0], rq[1]);
    else if ((m & 1) == 0) { const int i = m + 1; pd[i >> 3][(i >> 1) & 3] = pack_bf16(rq[i - 1], rq[i]); }
    if (m < 14) {
      const int i = m + 2;
      rq[i] = __builtin_amdgcn_rcpf(add1(tq[i]));
    } else {
      rq[0] = __builtin_amdgcn_rcpf(add1(tq[0]));
      rq[1] = __builtin_amdgcn_rcpf(add1(tq[1]));
    }
    if (m < 13) tq[m + 3] = __builtin_amdgcn_exp2f(Gc[m + 3]);
    else if (m == 13) { tq[0] = __builtin_amdgcn_exp2f(Gn[0]); tq[1] = __builtin_amdgcn_exp2f(Gn[1]); }
    else tq[2] = __builtin_amdgcn_exp2f(Gn[2]);
  };

  long long tm1 = 0, c1 = 0;
  const long long tmB = __builtin_amdgcn_s_memrealtime();
  int wb0 = 0;
  while (true) {
    const int ntiles = (cnt + 31) >> 5;
    asm volatile("s_waitcnt vmcnt(5)" ::: "memory");
    {
      const unsigned sl = *(const LAS unsigned short*)(size_t)aLS1;
#pragma unroll
      for (int k = 0; k < 4; ++k) as[k] = rd128(fo[k]);
      ae = rd128(foe);
      St[0] = rd128(sl + cSt);
      St[1] = rd128(sl + cSt + 16);
      G0 = MFMA32(St[0], XAf[0][0], zero16);
      G0 = MFMA32(St[1], XAf[1][0], G0);
      G0 = MFMA32(ae, wl[widx(6, 0) * 64], G0);
#pragma unroll
      for (int k = 0; k < 4; ++k) G0 = MFMA32(as[k], wl[widx(7 + k, 0) * 64], G0);
      V0 = MFMA32(as[0], wl[widx(11, 0) * 64], zero16);
#pragma unroll
      for (int k = 1; k < 4; ++k) V0 = MFMA32(as[k], wl[widx(11 + k, 0) * 64], V0);
      asm volatile("s_waitcnt lgkmcnt(0)" ::: "memory");
      issue_gather(2, 0);
      pipe_zero();
      tq[0] = __builtin_amdgcn_exp2f(G0[0]);
      tq[1] = __builtin_amdgcn_exp2f(G0[1]);
      tq[2] = __builtin_amdgcn_exp2f(G0[2]);
      rq[0] = __builtin_amdgcn_rcpf(add1(tq[0]));
      rq[1] = __builtin_amdgcn_rcpf(add1(tq[1]));
      SB();
    }
    tm1 = __builtin_amdgcn_s_memrealtime(); c1 = __builtin_amdgcn_s_memtime(); __builtin_amdgcn_s_waitcnt(0xC07F);
    unsigned &lS4 = aLS4, &lS1 = aLS1, &lX = aLX, &lE = aLE;
    int adv = 0;
    auto body = [&](const int par) {
      const unsigned lo = par * 128;
      const unsigned ro = (1 - par) * ringd;
      {
        u32x4 ss;
        unsigned sl;
#pragma unroll
        for (int m = 0; m < 15; ++m) {
          if (m == 0) Y[1] = MFMA32(__builtin_bit_cast(bf16x8, pd[0]), S[0], Y[1]);
          else if (m == 1) Y[3] = MFMA32(__builtin_bit_cast(bf16x8, pn[0]), S[0], Y[3]);
          else if (m == 2) Y[1] = MFMA32(__builtin_bit_cast(bf16x8, pd[1]), S[1], Y[1]);
          else if (m == 3) Y[3] = MFMA32(__builtin_bit_cast(bf16x8, pn[1]), S[1], Y[3]);
          else if (m == 4) G1 = MFMA32(St[0], XAf[0][1], zero16);
          else if (m == 5) G1 = MFMA32(St[1], XAf[1][1], G1);
          else if (m == 6) G1 = MFMA32(ae, wf(m, 1), G1);
          else if (m < 11) G1 = MFMA32(as[m - 7], wf(m, 1), G1);
          else if (m == 11) V1 = MFMA32(as[0], wf(m, 1), zero16);
          else V1 = MFMA32(as[m - 11], wf(m, 1), V1);
          SB();
          if (m == 0) { ss = ld128(lS4 + lo); sl = *(const LAS unsigned short*)(size_t)(lS1 + lo + 128); }
          if (m >= 3 && m < 12 && !((HELD >> (m + 3)) & 1)) b[(m - 3) % 3] = wl[widx(m + 3, 1) * 64];
          if (m == 4) load_S(ss);
          if (m == 5) { St[0] = rd128(sl + cSt); St[1] = rd128(sl + cSt + 16); }
          if (m == 6) { asm volatile("s_waitcnt vmcnt(5)" ::: "memory"); ae = rd128(foe + ro); }
          if (m >= 11) as[m - 11] = rd128(fo[m - 11] + ro);
          ew(m, G0, V0, G1, V1);
          SB();
        }
      }
      {
        u32x4 sx;
        unsigned re;
#pragma unroll
        for (int m = 0; m < 15; ++m) {
          if (m == 0) Y[0] = MFMA32(__builtin_bit_cast(bf16x8, pd[0]), S[0], Y[0]);
          else if (m == 1) Y[2] = MFMA32(__builtin_bit_cast(bf16x8, pn[0]), S[0], Y[2]);
          else if (m == 2) Y[0] = MFMA32(__builtin_bit_cast(bf16x8, pd[1]), S[1], Y[0]);
          else if (m == 3) Y[2] = MFMA32(__builtin_bit_cast(bf16x8, pn[1]), S[1], Y[2]);
          else if (m == 4) G0 = MFMA32(St[0], XAf[0][0], zero16);
          else if (m == 5) G0 = MFMA32(St[1], XAf[1][0], G0);
          else if (m == 6) G0 = MFMA32(ae, wf(m, 0), G0);
          else if (m < 11) G0 = MFMA32(as[m - 7], wf(m, 0), G0);
          else if (m == 11) V0 = MFMA32(as[0], wf(m, 0), zero16);
          else V0 = MFMA32(as[m - 11], wf(m, 0), V0);
          SB();
          if (m == 4) { sx = ld128(lX + lo + 3 * 128); re = *(const LAS unsigned*)(size_t)(lE + lo + 3 * 128); }
          if (m >= 3 && m < 12 && !((HELD >> (m + 3)) & 1)) b[(m - 3) % 3] = wl[widx(m + 3, 0) * 64];
          if (m == 7) glds16x(x, sx[0], cx0, ring0 + ro);
          if (m == 8) glds16x(x, sx[1], cx1, ring0 + ro + 1024);
          if (m == 9) glds16x(x, sx[2], cx0, ring0 + ro + 2048);
          if (m == 10) glds16x(x, sx[3], cx1, ring0 + ro + 3072);
          if (m == 11) glds16e(ES, re, ce, ring0 + ro + 4096);
          ew(m, G1, V1, G0, V0);
          SB();
        }
      }
    };
    for (int t = 0; t < ntiles; t += 2) {
      asm volatile("s_cmp_lt_u32 %0, 4\n\ts_cbranch_scc1 1f\n\ts_setprio 1\n1:" :: "s"(wave) : "scc");
      body(0);
      if (t + 1 >= ntiles) break;
      __builtin_amdgcn_s_setprio(0);
      body(1);
      lS4 += 256; lS1 += 256; lX += 256; lE += 256;
      asm volatile("" : "+v"(lS4), "+v"(lS1), "+v"(lX), "+v"(lE));
      adv += 256;
    }
    pq[14] = mulf(0, V1[14], rq[14]);
    pq[15] = mulf(1, V1[15], rq[15]);
    pn[1][3] = pack_bf16(pq[14], pq[15]);
    Y[1] = MFMA32(__builtin_bit_cast(bf16x8, pd[0]), S[0], Y[1]);
    Y[3] = MFMA32(__builtin_bit_cast(bf16x8, pn[0]), S[0], Y[3]);
    Y[1] = MFMA32(__builtin_bit_cast(bf16x8, pd[1]), S[1], Y[1]);
    Y[3] = MFMA32(__builtin_bit_cast(bf16x8, pn[1]), S[1], Y[3]);
    wb0 += LCAP;
    if (__builtin_expect(wb0 >= cnt_all, 1)) break;
    asm volatile("s_waitcnt vmcnt(0)" ::: "memory");
    cnt = min(cnt_all - wb0, LCAP);
    lS4 -= adv; lS1 -= adv; lX -= adv; lE -= adv;
    fill_slow(wb0);
    {
      int l2;
      asm volatile("v_mbcnt_lo_u32_b32 %0, -1, 0\n\tv_mbcnt_hi_u32_b32 %0, -1, %0" : "=v"(l2));
      convert(cnt, la + LRAW + l2 * 8, la + l2 * 4, l2);
    }
    issue_gather(0, 0);
    issue_gather(1, 1);
  }
  const long long tm2 = __builtin_amdgcn_s_memrealtime(); const long long c2 = __builtin_amdgcn_s_memtime();

  if (nodes > 0) {
    int lane;
    asm volatile("v_mbcnt_lo_u32_b32 %0, -1, 0\n\tv_mbcnt_hi_u32_b32 %0, -1, %0" : "=v"(lane));
    const int r = lane & 31, h = lane >> 5;
#pragma unroll
    for (int ct = 0; ct < 2; ++ct) {
      f32x16 U = zero16;
#pragma unroll
      for (int k = 0; k < 4; ++k) U = MFMA32(wl[(18 + k * 2 + ct) * 64], as[k], U);
#pragma unroll
      for (int q = 0; q < 2; ++q) {
        const u32x4 xv = __builtin_bit_cast(u32x4, as[2 * ct + q]);
        u32x4 ov;
#pragma unroll
        for (int j = 0; j < 4; ++j) {
          const int i = 8 * q + 2 * j;
          const float z0 = U[i] + Y[2 + ct][i] * __builtin_amdgcn_rcpf(Y[ct][i] + 1e-6f);
          const float z1 = U[i + 1] + Y[2 + ct][i + 1] * __builtin_amdgcn_rcpf(Y[ct][i + 1] + 1e-6f);
          const float o0 = __uint_as_float(xv[j] << 16) + (z0 > 0.f ? z0 : 0.f);
          const float o1 = __uint_as_float(xv[j] & 0xffff0000u) + (z1 > 0.f ? z1 : 0.f);
          ov[j] = pack_bf16(o0, o1);
        }
        *(LAS u32x4*)(size_t)(la + r * 128 + (((4 * h + 2 * ct + q) ^ (r & 7)) * 16)) = ov;
      }
    }
#pragma unroll
    for (int q = 0; q < 4; ++q) {
      const int row = 8 * q + (lane >> 3);
      const u32x4 ov = *(const LAS u32x4*)(size_t)(la + row * 128 + (((lane & 7) ^ (row & 7)) * 16));
      if (row < nodes) gstore128wt(out, (unsigned)n0 * 128u + q * 1024u + lane * 16u, ov);
    }
  }
  asm volatile("s_waitcnt vmcnt(0)" ::: "memory");
  const long long tm3 = __builtin_amdgcn_s_memrealtime();
  if (__builtin_amdgcn_mbcnt_hi(-1, __builtin_amdgcn_mbcnt_lo(-1, 0)) == 0) { const int g = gbin; dbg[g*8+0]=tm0; dbg[g*8+1]=tm1; dbg[g*8+2]=tm2; dbg[g*8+3]=tm3; dbg[g*8+4]=(cnt_all+31)>>5; dbg[g*8+5]=c2-c1; dbg[g*8+6]=tmA-tm0; dbg[g*8+7]=tmB-tm0;
  }
}

__global__ void k_dbg(const long long* dbg, const unsigned long long* p0, int ng) {
  __shared__ long long sh[8][256];
  const int t = threadIdx.x;
  long long pmin = (long long)p0[t], pmax = pmin;
  long long s0 = 1ll << 62, s0m = 0, l1 = 0, e3 = 0, cyc = 0, rt = 0, l1max = 0, e3s = 0;
  for (int g = t; g < ng; g += 256) {
    const long long* d = dbg + g * 8;
    s0 = min(s0, d[0]); s0m = max(s0m, d[0]); l1 += d[1] - d[0]; l1max = max(l1max, d[1]); e3 = max(e3, d[3]); cyc += d[5]; rt += d[2] - d[1]; e3s += d[3] - d[0];
  }
  sh[0][t] = pmin; sh[1][t] = s0; sh[2][t] = l1; sh[3][t] = e3; sh[4][t] = cyc; sh[5][t] = rt; sh[6][t] = e3s; sh[7][t] = s0m;
  __syncthreads();
  if (t == 0) {
    for (int i = 1; i < 256; ++i) {
      pmin = min(pmin, sh[0][i]); pmax = max(pmax, sh[0][i]); s0 = min(s0, sh[1][i]); l1 += sh[2][i]; e3 = max(e3, sh[3][i]); cyc += sh[4][i]; rt += sh[5][i]; e3s += sh[6][i]; s0m = max(s0m, sh[7][i]);
    }
    const long long now = __builtin_amdgcn_s_memrealtime();
    printf("DBG pskew %d mstart %d mskew %d pro %d avgend %d end %d clk %d dbg %d\n", (int)(pmax - pmin), (int)(s0 - pmin), (int)(s0m - s0), (int)(l1 / ng), (int)(e3s / ng), (int)(e3 - pmin), (int)(cyc * 100 / rt), (int)(now - pmin));
  }
}

extern "C" void kernel_launch(void* const* d_in, const int* in_sizes, int n_in,
                              void* d_out, int out_size, void* d_ws, size_t ws_size,
                              hipStream_t stream) {
  const unsigned short* x = (const unsigned short*)d_in[0];
  const u32x4* eattr = (const u32x4*)d_in[1];
  const int* src = (const int*)d_in[2];
  const int* dst = (const int*)d_in[3];
  const unsigned short* wa = (const unsigned short*)d_in[4];
  const unsigned short* wb = (const unsigned short*)d_in[5];
  const unsigned short* wc = (const unsigned short*)d_in[6];
  const unsigned short* wu = (const unsigned short*)d_in[7];
  const unsigned short* wv = (const unsigned short*)d_in[8];
  unsigned short* out = (unsigned short*)d_out;
  const int N = in_sizes[0] / 64;
  const int E = in_sizes[2];
  const int chunk = (((E + NCH - 1) / NCH) + 3) & ~3;
  const int nbk = (N + 195) / 196;

  char* ws = (char*)d_ws;
  size_t off = 0;
  unsigned short* wpack = (unsigned short*)(ws + off);   off += 64 * 1024;
  u32x4* ENT = (u32x4*)(ws + off);                       off += (size_t)2 * NBK * NCH * CELL;
  unsigned* TS = (unsigned*)(ws + off);

  k_part<<<NCH + NFRAG, 1024, 0, stream>>>(src, dst, E, chunk, ENT, TS, wa, wb, wc, wu, wv, wpack);
  k_main<<<nbk, 512, 0, stream>>>(x, ENT, eattr, TS, chunk, (const bf16x8*)wpack, out, N
     , (long long*)(ws + (128u<<20))
     );
  k_dbg<<<1, 256, 0, stream>>>((const long long*)(ws + (128u<<20)), (const unsigned long long*)(wpack + 20000), 2040);
}
```

```cpp
#include <hip/hip_runtime.h>
#include <hip/hip_bf16.h>
#include <stdint.h>

using bf16x8 = __attribute__((ext_vector_type(8))) short;
using s16x4 = __attribute__((ext_vector_type(4))) short;
using f32x16 = __attribute__((ext_vector_type(16))) float;
using u32x4 = __attribute__((ext_vector_type(4))) unsigned;
using u32x2 = __attribute__((ext_vector_type(2))) unsigned;
using i32x4 = __attribute__((ext_vector_type(4))) int;
using u16x2 = __attribute__((ext_vector_type(2))) unsigned short;

#define MFMA32(a, b, c) __builtin_amdgcn_mfma_f32_32x32x16_bf16((a), (b), (c), 0, 0, 0)
#define LAS __attribute__((address_space(3)))
#define RANKOF(w) ((w) < 4 ? 7 - (w) : (w) - 4)
#define SLEEP "s_sleep 2\n\t"
#define NREP 8
#define REPOF(b) ((b) % NREP)
#define SB() __builtin_amdgcn_sched_barrier(0)

constexpr int NCH = 256;
constexpr int NBK = 256;
constexpr int NBIN = NBK * 8;
constexpr int NFRAG = 39;
constexpr int CHMAX = 3200;
constexpr float NLOG2E = -1.44269504088896340736f;
constexpr unsigned TPITCH = 80;

__device__ __forceinline__ unsigned pack_bf16(float lo, float hi) {
  typedef __attribute__((ext_vector_type(2))) float f2;
  typedef __attribute__((ext_vector_type(2))) __bf16 b2;
  f2 v = {lo, hi};
  b2 r = __builtin_convertvector(v, b2);
  return __builtin_bit_cast(unsigned, r);
}

__device__ __forceinline__ int wave_scan(int v) {
  v += __builtin_amdgcn_update_dpp(0, v, 0x111, 0xf, 0xf, false);
  v += __builtin_amdgcn_update_dpp(0, v, 0x112, 0xf, 0xf, false);
  v += __builtin_amdgcn_update_dpp(0, v, 0x114, 0xf, 0xf, false);
  v += __builtin_amdgcn_update_dpp(0, v, 0x118, 0xf, 0xf, false);
  v += __builtin_amdgcn_update_dpp(0, v, 0x142, 0xa, 0xf, false);
  v += __builtin_amdgcn_update_dpp(0, v, 0x143, 0xc, 0xf, false);
  return v;
}

__device__ __forceinline__ void glds16(const void* sbase, unsigned voff, unsigned lds_dst) {
  asm volatile(
      "s_mov_b32 m0, %2\n\t"
      "s_nop 0\n\t"
      "global_load_lds_dwordx4 %0, %1"
      :: "v"(voff), "s"(sbase), "s"(lds_dst) : "memory", "m0");
}
__device__ __forceinline__ void glds16nt(const void* sbase, unsigned voff, unsigned lds_dst) {
  asm volatile(
      "s_mov_b32 m0, %2\n\t"
      "s_nop 0\n\t"
      "global_load_lds_dwordx4 %0, %1 nt"
      :: "v"(voff), "s"(sbase), "s"(lds_dst) : "memory", "m0");
}
__device__ __forceinline__ void glds16x(const void* sbase, unsigned a, unsigned c, unsigned lds_dst) {
  unsigned tmp;
  asm volatile(
      "s_mov_b32 m0, %4\n\t"
      "v_mad_u32_u16 %0, %1, %5, %2\n\t"
      "global_load_lds_dwordx4 %0, %3"
      : "=&v"(tmp) : "v"(a), "v"(c), "s"(sbase), "s"(lds_dst), "s"(128u) : "memory", "m0");
}
__device__ __forceinline__ void glds16e(const void* sbase, unsigned a, unsigned c, unsigned lds_dst) {
  unsigned tmp;
  asm volatile(
      "s_mov_b32 m0, %4\n\t"
      "v_add_u32 %0, %1, %2\n\t"
      "global_load_lds_dwordx4 %0, %3"
      : "=&v"(tmp) : "v"(a), "v"(c), "s"(sbase), "s"(lds_dst) : "memory", "m0");
}
__device__ __forceinline__ int gload32(const void* sbase, unsigned voff) {
  int r;
  asm volatile("global_load_dword %0, %1, %2" : "=v"(r) : "v"(voff), "s"(sbase) : "memory");
  return r;
}
__device__ __forceinline__ u32x4 gload128(const void* sbase, unsigned voff) {
  u32x4 r;
  asm volatile("global_load_dwordx4 %0, %1, %2" : "=v"(r) : "v"(voff), "s"(sbase) : "memory");
  return r;
}

__device__ __forceinline__ u32x4 gload128nt(const void* sbase, unsigned voff) {
  u32x4 r;
  asm volatile("global_load_dwordx4 %0, %1, %2 nt" : "=v"(r) : "v"(voff), "s"(sbase) : "memory");
  return r;
}
__device__ __forceinline__ void gstore128wt(void* sbase, unsigned voff, u32x4 v) {
  asm volatile("global_store_dwordx4 %0, %1, %2 sc1\n\ts_nop 1" :: "v"(voff), "v"(v), "s"(sbase) : "memory");
}

constexpr int CELL = 128;
constexpr int CIN = 30;
constexpr int CCAP = 62;
constexpr int CPIT = 272;
constexpr unsigned E2OFF = NBK * NCH * CELL;
constexpr int KP_HDR = 0;
constexpr int KP_OVC = KP_HDR + NBK * 8;
constexpr int KP_ENT = KP_OVC + 16;
constexpr int KP_HIST = KP_ENT + NBK * CPIT;
constexpr int KP_LDS = KP_HIST + NBIN * 4;
constexpr int NWL = 26;
constexpr int NBLOB = 31;

__device__ __forceinline__ unsigned mad24(unsigned a, int b, unsigned c) {
  unsigned r;
  asm("v_mad_i32_i24 %0, %1, %2, %3" : "=v"(r) : "v"(a), "s"(b), "v"(c));
  return r;
}
__device__ __forceinline__ unsigned mul24(unsigned a, unsigned b) {
  unsigned r;
  asm("v_mul_u32_u24 %0, %1, %2" : "=v"(r) : "n"(b), "v"(a));
  return r;
}
__device__ __forceinline__ unsigned mulhi24(unsigned a, unsigned b) {
  return (unsigned)(((unsigned long long)(a & 0xffffffu) * (unsigned long long)b) >> 32);
}
__device__ __forceinline__ unsigned dot4(unsigned a, unsigned b, unsigned c) {
  return __builtin_amdgcn_udot4(a, b, c, false);
}

constexpr int RING_SLOT = 5 * 1024;
constexpr int SPIT = 1040;
constexpr int LCAP = 512;
constexpr int LARR = 2560;
constexpr int LIST_BYTES = 2 * LARR;
constexpr int LRAW = LIST_BYTES - (LCAP + 8) * 8;
constexpr int OFF_W = 0;
constexpr int OFF_T1 = NWL * 1024;
constexpr int OFF_T2 = OFF_T1 + 32 * TPITCH;
constexpr int OFF_WA = NBLOB * 1024;
constexpr int OFF_R0 = NFRAG * 1024;
constexpr int OFF_R1 = OFF_R0 + 8 * RING_SLOT;
constexpr int OFF_L = OFF_R1 + 8 * RING_SLOT;
constexpr int OFF_RDY = OFF_L + 8 * LIST_BYTES;
constexpr int MAIN_LDS = OFF_RDY + 16;
static_assert(MAIN_LDS <= 160 * 1024, "LDS");
static_assert(KP_LDS <= 16 * RING_SLOT, "phase 1 lives in the ring");

__device__ __forceinline__ unsigned ldd(const unsigned* p) {
  return __hip_atomic_load(p, __ATOMIC_RELAXED, __HIP_MEMORY_SCOPE_AGENT);
}
__device__ __forceinline__ float add1(float t) { return 1.0f + t; }
__device__ __forceinline__ float mulf(int i, float a, float b) { return (i & 1) ? __builtin_fmaf(a, b, 0.0f) : a * b; }

__global__ __launch_bounds__(512, 2) void k_main(const unsigned short* __restrict__ x,
                                                 const u32x4* __restrict__ ES,
                                                 const int* __restrict__ src, const int* __restrict__ dst,
                                                 const unsigned short* __restrict__ wa_g,
                                                 const unsigned short* __restrict__ wb_g,
                                                 const unsigned short* __restrict__ wc_g,
                                                 const unsigned short* __restrict__ wu_g,
                                                 const unsigned short* __restrict__ wv_g,
                                                 u32x4* ENT, unsigned* TS, unsigned long long* FL,
                                                 unsigned short* __restrict__ out, int N, int E, int chunk
                                                 ) {
#define PT(i)
  PT(0);
  __shared__ __attribute__((aligned(16))) char smem[MAIN_LDS];
  const int tid0 = threadIdx.x;
  const int wave = __builtin_amdgcn_readfirstlane(tid0 >> 6);
  const int bk = blockIdx.x;
  const unsigned lds0 = (unsigned)(size_t)(LAS char*)smem;

  LAS char* sm = (LAS char*)smem + OFF_R0;
  LAS unsigned* hist = (LAS unsigned*)(sm + KP_HIST);
  LAS unsigned* ovc = (LAS unsigned*)(sm + KP_OVC);
  const int base = bk * chunk;
  const int ecnt = max(min(chunk, E - base), 0);
  int i0[2];
  u32x4 d[2], sv[2];
  u32x2 tag;
#pragma unroll
  for (int s = 0; s < 2; ++s) {
    i0[s] = max(min(base + 4 * (tid0 + 512 * s), min(base + ecnt, E) - 4), 0);
    d[s] = gload128nt(dst, (unsigned)i0[s] * 4u);
  }
  if (tid0 <= NBK) *(LAS u32x2*)(sm + KP_HDR + tid0 * 8) = u32x2{0u, 0u};
#pragma unroll
  for (int s = 0; s < 2; ++s) sv[s] = gload128nt(src, (unsigned)i0[s] * 4u);
  if (tid0 == 511) *(LAS u32x2*)((LAS char*)smem + OFF_RDY) = u32x2{0u, 0u};
  int tid = tid0;
  asm volatile("s_waitcnt lgkmcnt(0)\n\ts_barrier" : "+v"(tid) :: "memory");
  const int lane = tid & 63, r = lane & 31, h = lane >> 5;
  asm volatile("global_load_dwordx2 %0, %1, %2 sc1" : "=v"(tag) : "v"(NCH * 8u), "s"(FL) : "memory");
  const unsigned f1 = lds0 + OFF_W + ((wave < 4 ? 0 : NBLOB) + (wave & 3) * 2) * 1024;
  const unsigned f2 = lds0 + OFF_W + ((wave < 4 ? 8 : 18) + (wave & 3) * 2) * 1024;
  const unsigned f3 = lds0 + OFF_W + 16 * 1024;
  {
    const unsigned short* w1 = wave < 4 ? wb_g : wa_g;
    const unsigned short* w2 = wave < 4 ? wv_g : wu_g;
    const unsigned vo = lane * 16 + (wave & 3) * 1024;
    glds16(w1, vo, f1);
    glds16(w1 + 2048, vo, f1 + 1024);
    glds16(w2, vo, f2);
    glds16(w2 + 2048, vo, f2 + 1024);
    if (wave == 7) {
      glds16(wc_g, lane * 16, f3);
      glds16(wc_g + 512, lane * 16, f3 + 1024);
    }
  }
  u32x4 xg[4];
  unsigned xo = (unsigned)min(bk * 196 + 49 * (wave >> 1) + 25 * (wave & 1) + min(r, 24 - (wave & 1)), N - 1) * 128u + 64u * h;
  asm volatile("" : "+v"(xo));
  int warm = gload32(x, xo);
  unsigned tup[8];
  unsigned long long on[8];
  const unsigned sm0 = lds0 + OFF_R0;
#pragma unroll
  for (int s = 0; s < 2; ++s) {
    const int lo = base + 4 * (tid + 512 * s) - i0[s], hi4 = base + ecnt - i0[s];
#pragma unroll
    for (int k = 0; k < 4; ++k) {
      on[4 * s + k] = __ballot(k >= lo && k < hi4);
      tup[4 * s + k] = (unsigned)(i0[s] + k - base);
    }
  }
  const int bq = tid >> 2, p0 = (tid & 3) * 16;
  unsigned ha = sm0 + KP_HDR + bq * 8, ia = sm0 + KP_ENT + bq * CPIT + p0;
  unsigned go = (unsigned)((bq * NCH + bk) * CELL + p0);
  asm volatile("" : "+v"(tup[0]), "+v"(tup[1]), "+v"(tup[2]), "+v"(tup[3]), "+v"(tup[4]), "+v"(tup[5]), "+v"(tup[6]),
                    "+v"(tup[7]), "+v"(ha), "+v"(ia), "+v"(go), "+s"(on[0]), "+s"(on[1]), "+s"(on[2]), "+s"(on[3]),
                    "+s"(on[4]), "+s"(on[5]), "+s"(on[6]), "+s"(on[7]));
  {
    asm volatile("s_waitcnt vmcnt(8)" : "+v"(d[0]), "+v"(d[1]) :: "memory");
    PT(1);
    unsigned gi[8], rk[8], m0[8], m1[8], cb[8], sh[8], oh[8];
    auto count = [&](int s) {
      unsigned *g = gi + 4 * s, *rr = rk + 4 * s, *o = oh + 4 * s;
#pragma unroll
      for (int k = 0; k < 4; ++k) {
        g[k] = mulhi24(d[s][k] << 8, 684785u);
        sh[4 * s + k] = g[k] << 3;
        o[k] = 1u << (sh[4 * s + k] & 31u);
        rr[k] = 0u;
      }
      asm volatile(
          "s_mov_b64 exec, %12\n\t"
          "ds_add_rtn_u32 %0, %4, %8\n\t"
          "s_mov_b64 exec, %13\n\t"
          "ds_add_rtn_u32 %1, %5, %9\n\t"
          "s_mov_b64 exec, %14\n\t"
          "ds_add_rtn_u32 %2, %6, %10\n\t"
          "s_mov_b64 exec, %15\n\t"
          "ds_add_rtn_u32 %3, %7, %11\n\t"
          "s_mov_b64 exec, -1"
          : "+&v"(rr[0]), "+&v"(rr[1]), "+&v"(rr[2]), "+&v"(rr[3])
          : "v"((g[0] & ~3u) + sm0), "v"((g[1] & ~3u) + sm0), "v"((g[2] & ~3u) + sm0), "v"((g[3] & ~3u) + sm0),
            "v"(o[0]), "v"(o[1]), "v"(o[2]), "v"(o[3]), "s"(on[4 * s]), "s"(on[4 * s + 1]), "s"(on[4 * s + 2]),
            "s"(on[4 * s + 3])
          : "memory");
    };
    auto masks = [&](int s) {
#pragma unroll
      for (int k = 0; k < 4; ++k) {
        const int i = 4 * s + k;
        const unsigned slot = mad24(gi[i], -49 * 128, d[s][k] << 8) >> 8;
        tup[i] = mad24(slot, 3200, tup[i]);
        const unsigned hm = (unsigned)((int)(gi[i] << 29) >> 31);
        m0[i] = ((oh[i] - 1u) | hm) & 0x04040404u;
        m1[i] = (oh[i] - 1u) & hm & 0x04040404u;
        gi[i] &= ~7u;
        cb[i] = mul24(gi[i], CPIT / 8) + sm0;
        gi[i] += sm0;
      }
      const int i = 4 * s;
      asm volatile("" : "+v"(m0[i]), "+v"(m0[i + 1]), "+v"(m0[i + 2]), "+v"(m0[i + 3]), "+v"(m1[i]), "+v"(m1[i + 1]),
                        "+v"(m1[i + 2]), "+v"(m1[i + 3]), "+v"(cb[i]), "+v"(cb[i + 1]), "+v"(cb[i + 2]), "+v"(cb[i + 3]),
                        "+v"(gi[i]), "+v"(gi[i + 1]), "+v"(gi[i + 2]), "+v"(gi[i + 3]), "+v"(tup[i]), "+v"(tup[i + 1]),
                        "+v"(tup[i + 2]), "+v"(tup[i + 3]));
    };
    auto ranks = [&](int s) {
      unsigned* rr = rk + 4 * s;
#pragma unroll
      for (int k = 0; k < 4; ++k) asm("v_bfe_u32 %0, %0, %1, 8" : "+v"(rr[k]) : "v"(sh[4 * s + k]));
      const unsigned mx = max(max(rr[0], rr[1]), max(rr[2], rr[3]));
#pragma unroll
      for (int k = 0; k < 4; ++k) {
        rr[k] = cb[4 * s + k] + rr[k] * 4;
        cb[4 * s + k] += CCAP * 4;
      }
      return mx;
    };
    auto place = [&](int s, const u32x2* hd) {
      unsigned pa[4];
#pragma unroll
      for (int k = 0; k < 4; ++k)
        pa[k] = min(dot4(hd[k][0], m0[4 * s + k], dot4(hd[k][1], m1[4 * s + k], rk[4 * s + k])), cb[4 * s + k]);
      asm volatile(
          "s_mov_b64 exec, %8\n\t"
          "ds_write_b32 %0, %4 offset:%12\n\t"
          "s_mov_b64 exec, %9\n\t"
          "ds_write_b32 %1, %5 offset:%12\n\t"
          "s_mov_b64 exec, %10\n\t"
          "ds_write_b32 %2, %6 offset:%12\n\t"
          "s_mov_b64 exec, %11\n\t"
          "ds_write_b32 %3, %7 offset:%12\n\t"
          "s_mov_b64 exec, -1"
          :: "v"(pa[0]), "v"(pa[1]), "v"(pa[2]), "v"(pa[3]), "v"(tup[4 * s]), "v"(tup[4 * s + 1]),
             "v"(tup[4 * s + 2]), "v"(tup[4 * s + 3]), "s"(on[4 * s]), "s"(on[4 * s + 1]), "s"(on[4 * s + 2]),
             "s"(on[4 * s + 3]), "n"(KP_ENT + 8)
          : "memory");
    };
    auto addsrc = [&](int s) {
      asm volatile(
          "s_waitcnt vmcnt(1)\n\t"
          "v_mad_i32_i24 %0, %4, %8, %0\n\t"
          "v_mad_i32_i24 %1, %5, %8, %1\n\t"
          "v_mad_i32_i24 %2, %6, %8, %2\n\t"
          "v_mad_i32_i24 %3, %7, %8, %3"
          : "+v"(tup[4 * s]), "+v"(tup[4 * s + 1]), "+v"(tup[4 * s + 2]), "+v"(tup[4 * s + 3])
          : "v"(sv[s][0]), "v"(sv[s][1]), "v"(sv[s][2]), "v"(sv[s][3]), "s"(81920)
          : "memory");
    };
    u32x2 hc[2];
    if ((wave * 64 + 512) * 4 < ecnt) {
      count(0);
      count(1);
      masks(0);
      masks(1);
      SB();
      asm volatile("s_waitcnt lgkmcnt(0)" : "+v"(rk[0]), "+v"(rk[1]), "+v"(rk[2]), "+v"(rk[3]), "+v"(rk[4]),
                   "+v"(rk[5]), "+v"(rk[6]), "+v"(rk[7]) :: "memory");
      if (max(ranks(0), ranks(1)) >= (unsigned)CCAP) ovc[1] = 1u;
      addsrc(0);
      addsrc(1);
      SB();
      asm volatile("s_barrier" ::: "memory");
      PT(2);
      u32x2 hd[8];
#pragma unroll
      for (int i = 0; i < 8; ++i) hd[i] = *(const LAS u32x2*)(size_t)gi[i];
#pragma unroll
      for (int s = 0; s < 2; ++s) hc[s] = *(const LAS u32x2*)(size_t)(ha + s * 1024);
      place(0, hd);
      place(1, hd + 4);
    } else {
      count(0);
      masks(0);
      SB();
      asm volatile("s_waitcnt lgkmcnt(0)" : "+v"(rk[0]), "+v"(rk[1]), "+v"(rk[2]), "+v"(rk[3]) :: "memory");
      if (ranks(0) >= (unsigned)CCAP) ovc[1] = 1u;
      addsrc(0);
      asm volatile("" :: "v"(sv[1]));
      SB();
      asm volatile("s_barrier" ::: "memory");
      PT(2);
      u32x2 hd[4];
#pragma unroll
      for (int i = 0; i < 4; ++i) hd[i] = *(const LAS u32x2*)(size_t)gi[i];
#pragma unroll
      for (int s = 0; s < 2; ++s) hc[s] = *(const LAS u32x2*)(size_t)(ha + s * 1024);
      place(0, hd);
    }
    unsigned used[2];
#pragma unroll
    for (int s = 0; s < 2; ++s) used[s] = dot4(hc[s][0], 0x01010101u, dot4(hc[s][1], 0x01010101u, 0u));
    if (max(used[0], used[1]) > (unsigned)CCAP) ovc[1] = 1u;
    asm volatile("s_waitcnt lgkmcnt(0)\n\ts_barrier" : "+v"(tag) :: "memory");
    PT(3); PT(4);
    const unsigned ovf = ovc[1];
    u32x4 v0[2], v1[2];
#pragma unroll
    for (int s = 0; s < 2; ++s) {
      v0[s] = *(const LAS u32x4*)(size_t)(ia + s * 128 * CPIT);
      v1[s] = *(const LAS u32x4*)(size_t)(ia + s * 128 * CPIT + 64);
    }
    asm volatile("" : "+v"(v0[0]), "+v"(v1[0]), "+v"(v0[1]), "+v"(v1[1]));
    if (__builtin_expect(__builtin_amdgcn_readfirstlane(ovf) != 0u, 0)) {
      *(LAS u32x4*)(hist + tid * 4) = u32x4{0u, 0u, 0u, 0u};
      asm volatile("s_waitcnt lgkmcnt(0)\n\ts_barrier" ::: "memory");
#pragma unroll
      for (int i = 0; i < 8; ++i) {
        gi[i] = mulhi24(d[i >> 2][i & 3] << 8, 684785u);
        if ((on[i] >> lane) & 1ull)
          rk[i] = __hip_atomic_fetch_add(hist + gi[i], 1u, __ATOMIC_RELAXED, __HIP_MEMORY_SCOPE_WORKGROUP);
      }
      asm volatile("s_waitcnt lgkmcnt(0)\n\ts_barrier" ::: "memory");
      if (tid < NBK) {
        unsigned tot = 0;
        for (int i = 0; i < 8; ++i) tot += hist[tid * 8 + i];
        LAS unsigned* cp = (LAS unsigned*)(sm + KP_ENT + tid * CPIT);
        unsigned p = ~0u;
        if (tot > (unsigned)CCAP) {
          p = (unsigned)base + __hip_atomic_fetch_add(ovc, tot, __ATOMIC_RELAXED, __HIP_MEMORY_SCOPE_WORKGROUP);
          *(LAS u32x2*)(sm + KP_HDR + tid * 8) = u32x2{~0u, ~0u};
        }
        for (int i = 0; i < 8; ++i) {
          const unsigned n = hist[tid * 8 + i];
          hist[tid * 8 + i] = p;
          if (p != ~0u) {
            cp[2 + i] = 0u;
            cp[10 + i] = p;
            cp[18 + i] = n;
            p += n;
          }
        }
      }
      asm volatile("s_waitcnt lgkmcnt(0)\n\ts_barrier" ::: "memory");
#pragma unroll
      for (int i = 0; i < 8; ++i) {
        if ((on[i] >> lane) & 1ull) {
          const unsigned p = hist[gi[i]];
          if (p != ~0u) __hip_atomic_store(TS + p + rk[i], tup[i], __ATOMIC_RELAXED, __HIP_MEMORY_SCOPE_AGENT);
        }
      }
#pragma unroll
      for (int s = 0; s < 2; ++s) {
        hc[s] = *(const LAS u32x2*)(size_t)(ha + s * 1024);
        used[s] = dot4(hc[s][0], 0x01010101u, dot4(hc[s][1], 0x01010101u, 0u));
        v0[s] = *(const LAS u32x4*)(size_t)(ia + s * 128 * CPIT);
        v1[s] = *(const LAS u32x4*)(size_t)(ia + s * 128 * CPIT + 64);
      }
    }
#pragma unroll
    for (int s = 0; s < 2; ++s) {
      if (p0 == 0) { v0[s][0] = hc[s][0]; v0[s][1] = hc[s][1]; }
      gstore128wt(ENT, go + s * (128 * NCH * CELL), v0[s]);
    }
#pragma unroll
    for (int s = 0; s < 2; ++s) {
      const unsigned g2 = go + s * (128 * NCH * CELL);
      if (used[s] > 14u) gstore128wt(ENT, g2 + 64, v1[s]);
      if (__builtin_expect(used[s] > (unsigned)CIN, 0)) {
        gstore128wt(ENT, g2 + E2OFF, *(const LAS u32x4*)(size_t)(ia + s * 128 * CPIT + 128));
        gstore128wt(ENT, g2 + E2OFF + 64, *(const LAS u32x4*)(size_t)(ia + s * 128 * CPIT + 192));
      }
    }
    PT(5);
    const unsigned to = (unsigned)(h * 1024 + ((lane >> 2) & 3) * 128 + (lane & 1) * 64 + ((lane >> 4) & 1) * 16 +
                                   ((lane >> 1) & 1) * 8);
    auto prep = [&](unsigned f, bool scale) {
      u32x4 w[2];
#pragma unroll
      for (int ct = 0; ct < 2; ++ct) {
        const s16x4 a0 = __builtin_amdgcn_ds_read_tr16_b64_v4i16((LAS s16x4*)(size_t)(f + to + ct * 32));
        const s16x4 a1 = __builtin_amdgcn_ds_read_tr16_b64_v4i16((LAS s16x4*)(size_t)(f + to + ct * 32 + 512));
        w[ct] = __builtin_bit_cast(u32x4, __builtin_shufflevector(a0, a1, 0, 1, 2, 3, 4, 5, 6, 7));
      }
      asm volatile("s_waitcnt lgkmcnt(0)" : "+v"(w[0]), "+v"(w[1]) :: "memory");
#pragma unroll
      for (int ct = 0; ct < 2; ++ct) {
        if (scale) {
#pragma unroll
          for (int j = 0; j < 4; ++j)
            w[ct][j] = pack_bf16(__uint_as_float(w[ct][j] << 16) * NLOG2E, __uint_as_float(w[ct][j] & 0xffff0000u) * NLOG2E);
        }
        *(LAS u32x4*)(size_t)(f + ct * 1024 + lane * 16) = w[ct];
      }
    };
    {
      const unsigned long long t64 = (((unsigned long long)tag[1] << 32) | tag[0]) + 0x9E3779B97F4A7C15ull;
      tag = u32x2{(unsigned)t64, (unsigned)(t64 >> 32)};
    }
    prep(f1, true);
    prep(f2, false);
    if (wave == 7) prep(f3, true);
    asm volatile("s_waitcnt vmcnt(0) lgkmcnt(0)\n\ts_barrier" : "+v"(tag), "+v"(warm) :: "memory");
    if (tid < NREP) asm volatile("global_store_dwordx2 %0, %1, %2 sc1\n\ts_nop 1" :: "v"(4096u + tid * 2048u + bk * 8u), "v"(tag), "s"(FL) : "memory");
    PT(6);
#pragma unroll
    for (int t = 0; t < 4; ++t) xg[t] = gload128(x, xo + 16 * t);
  }
  u32x4 cv[5];
  const int pc0 = wave < 4 ? 5 * wave : 4 * wave + 4;
  const unsigned pskip = wave < 4 ? 0u : wave < 7 ? 0x100u : 0x155u;
  unsigned pdone = wave < 7 ? pskip : 0u;
  int spin = 1 << 22;
  const char* eb = wave < 7 ? reinterpret_cast<const char*>(ENT) + (size_t)bk * (NCH * CELL) + pc0 * 1024 + 2048
                            : reinterpret_cast<const char*>(FL) + 4096 + REPOF(bk) * 2048;
  unsigned pvo = wave < 7 ? lane * 16u : lane * 32u;
  asm volatile("s_cmp_lg_u32 %4, 7\n\t"
               "s_cbranch_scc1 1f\n\t"
               "s_setprio 2\n\t"
               "global_load_dwordx4 %0, %2, %3 sc1\n\t"
               "global_load_dwordx4 %1, %2, %3 offset:16 sc1\n"
               "1:"
               : "=&v"(cv[0]), "=&v"(cv[1]) : "v"(pvo), "s"(eb), "s"(wave) : "memory", "scc");
  asm volatile("" : "=v"(cv[2]), "=v"(cv[3]), "=v"(cv[4]));
#define ROUND(AGAIN_P, AGAIN_C)                                                                                       \
  {                                                                                                                   \
    unsigned long long m1, m2;                                                                                        \
    unsigned t;                                                                                                       \
    u32x2 rd;                                                                                                         \
    asm volatile("s_cmp_eq_u32 %0, 0x155\n\t"                                                                         \
                 "s_cbranch_scc1 9f\n\t"                                                                              \
                 "s_cmp_lg_u32 %20, 7\n\t"                                                                            \
                 "s_cbranch_scc1 2f\n"                                                                                \
                 "1:\n\t"                                                                                             \
                 "s_sub_u32 %9, %9, 1\n\t"                                                                            \
                 "s_cselect_b64 %8, -1, 0\n\t"                                                                        \
                 "s_waitcnt vmcnt(0)\n\t"                                                                             \
                 "v_cmp_eq_u64 vcc, %16, %15\n\t"                                                                     \
                 "v_cmp_eq_u64 %7, %17, %15\n\t"                                                                      \
                 "s_and_b64 vcc, vcc, %7\n\t"                                                                         \
                 "v_cmp_eq_u64 %7, %18, %15\n\t"                                                                      \
                 "s_and_b64 vcc, vcc, %7\n\t"                                                                         \
                 "v_cmp_eq_u64 %7, %19, %15\n\t"                                                                      \
                 "s_and_b64 vcc, vcc, %7\n\t"                                                                         \
                 "s_or_b64 %7, vcc, %8\n\t"                                                                           \
                 "s_lshr_b64 vcc, %7, 1\n\t"                                                                          \
                 "s_and_b64 vcc, vcc, %7\n\t"                                                                         \
                 "s_mov_b64 exec, 1\n\t"                                                                              \
                 "v_mov_b64 %10, vcc\n\t"                                                                             \
                 "ds_write_b64 %13, %10\n\t"                                                                          \
                 "s_mov_b64 exec, -1\n\t"                                                                             \
                 "s_cmp_eq_u64 %7, -1\n\t"                                                                            \
                 "s_cselect_b32 %0, 0x155, 0\n\t"                                                                     \
                 "s_cbranch_scc1 9f\n\t"                                                                              \
                 "global_load_dwordx4 %2, %11, %12 sc1\n\t"                                                           \
                 "global_load_dwordx4 %3, %11, %12 offset:16 sc1\n\t"                                                 \
                 AGAIN_P                                                                                              \
                 "s_branch 9f\n"                                                                                      \
                 "2:\n\t"                                                                                             \
                 "ds_read_b64 v[240:241], %13\n\t"                                                                    \
                 "s_waitcnt lgkmcnt(0)\n\t"                                                                           \
                 "v_readfirstlane_b32 vcc_lo, v240\n\t"                                                               \
                 "v_readfirstlane_b32 vcc_hi, v241\n\t"                                                               \
                 "s_lshr_b64 vcc, vcc, %14\n\t"                                                                       \
                 "s_andn2_b32 %1, vcc_lo, %0\n\t"                                                                     \
                 "s_and_b32 %1, %1, 0x155\n\t"                                                                        \
                 "s_cbranch_scc0 8f\n\t"                                                                              \
                 "s_or_b32 %0, %0, %1\n\t"                                                                            \
                 "s_bitcmp1_b32 %1, 0\n\t"                                                                            \
                 "s_cbranch_scc0 3f\n\t"                                                                              \
                 "global_load_dwordx4 %2, %11, %12 offset:-2048 sc1\n"                                                \
                 "3:\n\t"                                                                                             \
                 "s_bitcmp1_b32 %1, 2\n\t"                                                                            \
                 "s_cbranch_scc0 4f\n\t"                                                                              \
                 "global_load_dwordx4 %3, %11, %12 offset:-1024 sc1\n"                                                \
                 "4:\n\t"                                                                                             \
                 "s_bitcmp1_b32 %1, 4\n\t"                                                                            \
                 "s_cbranch_scc0 5f\n\t"                                                                              \
                 "global_load_dwordx4 %4, %11, %12 sc1\n"                                                             \
                 "5:\n\t"                                                                                             \
                 "s_bitcmp1_b32 %1, 6\n\t"                                                                            \
                 "s_cbranch_scc0 6f\n\t"                                                                              \
                 "global_load_dwordx4 %5, %11, %12 offset:1024 sc1\n"                                                 \
                 "6:\n\t"                                                                                             \
                 "s_bitcmp1_b32 %1, 8\n\t"                                                                            \
                 "s_cbranch_scc0 8f\n\t"                                                                              \
                 "global_load_dwordx4 %6, %11, %12 offset:2048 sc1\n"                                                 \
                 "8:\n\t"                                                                                             \
                 AGAIN_C                                                                                              \
                 "9:"                                                                                                 \
                 : "+&s"(pdone), "=&s"(t), "+v"(cv[0]), "+v"(cv[1]), "+v"(cv[2]), "+v"(cv[3]), "+v"(cv[4]),           \
                   "=&s"(m1), "=&s"(m2), "+&s"(spin), "=&v"(rd)                                                       \
                 : "v"(pvo), "s"(eb), "v"(lds0 + OFF_RDY), "s"(2 * pc0), "v"(tag),                                    \
                   "v"(__builtin_shufflevector(cv[0], cv[0], 0, 1)), "v"(__builtin_shufflevector(cv[0], cv[0], 2, 3)), \
                   "v"(__builtin_shufflevector(cv[1], cv[1], 0, 1)), "v"(__builtin_shufflevector(cv[1], cv[1], 2, 3)), \
                   "s"(wave)                                                                                          \
                 : "memory", "vcc", "scc", "v240", "v241");                                                           \
  }
  const LAS bf16x8* wl = (const LAS bf16x8*)(smem + OFF_W) + lane;
  const f32x16 zero16 = {0.f, 0.f, 0.f, 0.f, 0.f, 0.f, 0.f, 0.f, 0.f, 0.f, 0.f, 0.f, 0.f, 0.f, 0.f, 0.f};
  auto widx = [&](int m, int ct) { return m == 6 ? 16 + ct : m < 11 ? (m - 7) * 2 + ct : 8 + (m - 11) * 2 + ct; };
#define HELD ((1 << 6) | (1 << 8) | (1 << 10) | (1 << 12) | (1 << 14))
  bf16x8 wr[2][15];
  {
  if (tid >= 192) {
    const unsigned p = tid - 192, tb = p >= 160u, p2 = p - tb * 160u;
    const unsigned row = (p2 * 205u) >> 10, seg = p2 - row * 5u;
    const unsigned col = tb ? ((row >> 1) & 12u) | ((row << 2) & 16u) | (row & 3u) : row;
    const unsigned one = (col >> 3) == seg ? 0x3F80u << ((col & 1u) * 16u) : 0u;
    const unsigned dw = (col >> 1) & 3u;
    *(LAS u32x4*)((LAS char*)smem + OFF_T1 + p * 16) =
        u32x4{dw == 0u ? one : 0u, dw == 1u ? one : 0u, dw == 2u ? one : 0u, dw == 3u ? one : 0u};
  }
#pragma unroll
    for (int ct = 0; ct < 2; ++ct)
#pragma unroll
      for (int m = 6; m < 15; ++m)
        if ((HELD >> m) & 1) {
          wr[ct][m] = wl[widx(m, ct) * 64];
          asm volatile("" : "+v"(wr[ct][m]));
        }
    asm volatile("s_waitcnt vmcnt(0)" : "+v"(xg[0]), "+v"(xg[1]), "+v"(xg[2]), "+v"(xg[3]) :: "memory");
    ROUND("", "")
    f32x16 XA[2] = {zero16, zero16};
#pragma unroll
    for (int k = 0; k < 4; ++k)
#pragma unroll
      for (int ct = 0; ct < 2; ++ct)
        XA[ct] = MFMA32(__builtin_bit_cast(bf16x8, xg[k]), wl[(NBLOB + k * 2 + ct) * 64], XA[ct]);
#pragma unroll
    for (int ct = 0; ct < 2; ++ct)
#pragma unroll
      for (int s = 0; s < 2; ++s) {
        u32x4 p;
#pragma unroll
        for (int j = 0; j < 4; ++j) p[j] = pack_bf16(XA[ct][8 * s + 2 * j], XA[ct][8 * s + 2 * j + 1]);
        *(LAS u32x4*)((LAS char*)smem + OFF_R0 + wave * RING_SLOT + (s * 2 + ct) * 1024 + lane * 16) = p;
      }
  }
  ROUND("", "")
  LAS char* lbase = (LAS char*)smem + OFF_L + wave * LIST_BYTES;
  const unsigned la = (unsigned)(size_t)lbase;
  auto put = [&](int p, unsigned a, unsigned b) { *(LAS u32x2*)(lbase + LRAW + p * 8) = u32x2{a, b}; };
  unsigned aLX = la + (lane >> 3) * 16;
  unsigned aLE = la + LARR + ((((lane >> 1) & 7) << 2) | (lane >> 4)) * 4;
  unsigned aLS4 = la + (4 * h + ((lane >> 2) & 3)) * 16;
  unsigned aLS1 = la + (((r & 7) << 2) | (r >> 3)) * 4 + 2;
  unsigned cx0 = (unsigned)(((lane & 7) ^ (lane >> 4)) * 16);
  unsigned cx1 = cx0 ^ 64u;
  unsigned ce = (unsigned)((((lane & 1) ^ ((lane >> 4) & 1))) * 16);
  const unsigned ringd = 8 * RING_SLOT;
  unsigned cS = lds0 + OFF_T1 + (((lane >> 4) & 1) * 16 + (lane & 3) * 4) * 2;
  unsigned cSt = lds0 + OFF_T2 + h * 32;
  unsigned fo[4];
#pragma unroll
  for (int t = 0; t < 4; ++t) fo[t] = lds0 + OFF_R0 + (unsigned)(r * 128 + (((4 * h + t) ^ ((r >> 1) & 7)) * 16));
  unsigned foe = lds0 + OFF_R0 + (unsigned)(4096 + r * 32 + ((h ^ ((r >> 3) & 1)) * 16));
  unsigned sa = lds0 + OFF_R1 + r * SPIT + h * CELL;
  unsigned cb0 = (unsigned)((8 * r + h) * chunk);
  unsigned aR = la + LRAW + lane * 8, aF = la + lane * 4;
  unsigned aRX = la + LRAW + (lane >> 3) * 32, aRE = la + LRAW + ((((lane >> 1) & 7) << 2) | (lane >> 4)) * 8;
  f32x16 Y[4];
#pragma unroll
  for (int q = 0; q < 4; ++q)
#pragma unroll
    for (int i = 0; i < 16; ++i) Y[q][i] = 0.f;
  f32x16 G0, V0, G1, V1;
  bf16x8 S[2], St[2], as[4], ae, b[3];
  u32x4 pd[2], pn[2];
  float tq[16], rq[16], pq[16];
  auto pipe_zero = [&]() {
#pragma unroll
    for (int j = 0; j < 8; ++j) { S[0][j] = 0; S[1][j] = 0; }
#pragma unroll
    for (int j = 0; j < 4; ++j) { pd[0][j] = 0; pd[1][j] = 0; pn[0][j] = 0; pn[1][j] = 0; }
#pragma unroll
    for (int i = 0; i < 16; ++i) V1[i] = 0.f;
    rq[14] = 0.f; rq[15] = 0.f;
  };
  asm volatile("" : "+v"(aLX), "+v"(aLE), "+v"(aLS4), "+v"(aLS1), "+v"(cx0), "+v"(cx1), "+v"(ce), "+v"(cS), "+v"(cSt),
                    "+v"(fo[0]), "+v"(fo[1]), "+v"(fo[2]), "+v"(fo[3]), "+v"(foe), "+v"(sa), "+v"(cb0), "+v"(aR),
                    "+v"(aF), "+v"(aRX), "+v"(aRE));
  asm volatile("" : "+v"(Y[0]), "+v"(Y[1]), "+v"(Y[2]), "+v"(Y[3]));
  unsigned rsh = (lane & 3) * 8, rid = lane & 7;
  unsigned long long rhi = 0xf0f0f0f0f0f0f0f0ull;
  asm volatile("" : "+v"(rsh), "+v"(rid), "+s"(rhi));

  ROUND("s_branch 1b\n\t", "s_sleep 1\n\ts_cmp_lg_u32 %0, 0x155\n\ts_cbranch_scc1 2b\n")
  {
    asm volatile("s_cmp_eq_u32 %7, 0x155\n\t"
                 "s_cbranch_scc1 1f\n\t"
                 "s_waitcnt vmcnt(0)\n\t"
                 "ds_write_b128 %5, %0\n\t"
                 "ds_write_b128 %5, %1 offset:%6\n\t"
                 "ds_write_b128 %5, %2 offset:2*%6\n\t"
                 "ds_write_b128 %5, %3 offset:3*%6\n\t"
                 "s_cmp_lg_u32 %7, 0\n\t"
                 "s_cbranch_scc1 1f\n\t"
                 "ds_write_b128 %5, %4 offset:4*%6\n"
                 "1:\n\t"
                 "s_waitcnt lgkmcnt(0)\n\t"
                 "s_barrier"
                 :: "v"(cv[0]), "v"(cv[1]), "v"(cv[2]), "v"(cv[3]), "v"(cv[4]),
                    "v"(lds0 + OFF_R1 + pc0 * SPIT + lane * 16), "n"(SPIT), "s"(pskip)
                 : "memory", "scc");
    if (bk == 0 && tid == 0)
      asm volatile("global_store_dwordx2 %0, %1, %2 sc1\n\ts_nop 1" :: "v"(NCH * 8u), "v"(tag), "s"(FL) : "memory");
    if (wave >= 4) __builtin_amdgcn_s_setprio(1);
  }
  u32x2 cl[4][4], hd[4];
  int nr[4], st[4], ni[4];
  unsigned long long mk[4][4];
#pragma unroll
  for (int j = 0; j < 4; ++j) hd[j] = *(const LAS u32x2*)(size_t)(sa + 2 * j * CELL);
  int gb;
  {
    int lo = (int)(hd[0][0] + hd[1][0] + hd[2][0] + hd[3][0]), hi = (int)(hd[0][1] + hd[1][1] + hd[2][1] + hd[3][1]);
    lo += __builtin_amdgcn_update_dpp(0, lo, 0xb1, 0xf, 0xf, false);
    hi += __builtin_amdgcn_update_dpp(0, hi, 0xb1, 0xf, 0xf, false);
    lo += __builtin_amdgcn_update_dpp(0, lo, 0x4e, 0xf, 0xf, false);
    hi += __builtin_amdgcn_update_dpp(0, hi, 0x4e, 0xf, 0xf, false);
    lo += __builtin_amdgcn_update_dpp(0, lo, 0x141, 0xf, 0xf, false);
    hi += __builtin_amdgcn_update_dpp(0, hi, 0x141, 0xf, 0xf, false);
    lo += __builtin_amdgcn_update_dpp(0, lo, 0x140, 0xf, 0xf, false);
    hi += __builtin_amdgcn_update_dpp(0, hi, 0x140, 0xf, 0xf, false);
    unsigned c;
    asm("v_cndmask_b32 %0, %1, %2, %3\n\tv_bfe_u32 %0, %0, %4, 8" : "=&v"(c) : "v"(lo), "v"(hi), "s"(rhi), "v"(rsh));
    auto p16 = __builtin_amdgcn_permlane16_swap(c, c, false, false);
    c = p16[0] + p16[1];
    auto p32 = __builtin_amdgcn_permlane32_swap(c, c, false, false);
    const int key = (int)(((p32[0] + p32[1]) << 3) | rid);
    unsigned acc = 0u;
#define RK(k) acc = __builtin_amdgcn_alignbit(acc, (unsigned)(__builtin_amdgcn_update_dpp(0, key, 0x120 + k, 0xf, 0xf, false) - key), 31)
    RK(1); RK(2); RK(3); RK(4); RK(5); RK(6); RK(7);
#undef RK
    const int want = RANKOF(wave);
    gb = __builtin_ctzll(__ballot(__builtin_popcount(acc) == want)) & 7;
  }
  const int nb0 = bk * 196 + 49 * (gb >> 1) + 25 * (gb & 1);
  const int nodes = max(min(25 - (gb & 1), N - nb0), 0);
  const int n0 = min(nb0, N - 1);
  const unsigned mlo = gb >= 4 ? 0x01010101u : 0x01010101u & ((1u << (8 * gb)) - 1u);
  const unsigned mhi = gb <= 4 ? 0u : 0x01010101u & ((1u << (8 * (gb - 4))) - 1u);
  const unsigned ring0 = lds0 + OFF_R0 + gb * RING_SLOT;
  bf16x8 XAf[2][2];
#pragma unroll
  for (int q = 0; q < 4; ++q) XAf[q >> 1][q & 1] = *(const LAS bf16x8*)(size_t)(ring0 + q * 1024 + lane * 16);
#pragma unroll
  for (int t = 0; t < 4; ++t) fo[t] += gb * RING_SLOT;
  foe += gb * RING_SLOT;
  auto head = [&](int j) {
    nr[j] = (int)(((gb >= 4 ? hd[j][1] : hd[j][0]) >> (8 * (gb & 3))) & 0xffu);
    st[j] = (int)dot4(hd[j][0], mlo, dot4(hd[j][1], mhi, 0u));
    const int n = min(min(nr[j], 8), CIN - st[j]);
#pragma unroll
    for (int q = 0; q < 4; ++q) mk[j][q] = __ballot(n > 2 * q);
    ni[j] = max(n, 0);
  };
  auto fetch = [&](int j) {
    asm volatile(
        "s_mov_b64 exec, %5\n\t"
        "ds_read2_b32 %0, %4 offset0:%9 offset1:%9+1\n\t"
        "s_mov_b64 exec, %6\n\t"
        "ds_read2_b32 %1, %4 offset0:%9+2 offset1:%9+3\n\t"
        "s_mov_b64 exec, %7\n\t"
        "ds_read2_b32 %2, %4 offset0:%9+4 offset1:%9+5\n\t"
        "s_mov_b64 exec, %8\n\t"
        "ds_read2_b32 %3, %4 offset0:%9+6 offset1:%9+7\n\t"
        "s_mov_b64 exec, -1"
        : "=&v"(cl[j][0]), "=&v"(cl[j][1]), "=&v"(cl[j][2]), "=&v"(cl[j][3])
        : "v"(sa + st[j] * 4), "s"(mk[j][0]), "s"(mk[j][1]), "s"(mk[j][2]), "s"(mk[j][3]), "n"(j * 64 + 2)
        : "memory");
  };
  auto fill = [&](int j, int p) {
    asm volatile(
        "s_mov_b64 exec, %13\n\t"
        "ds_write2_b32 %0, %9, %1 offset0:14 offset1:15\n\t"
        "ds_write2_b32 %0, %8, %1 offset0:12 offset1:13\n\t"
        "s_mov_b64 exec, %12\n\t"
        "ds_write2_b32 %0, %7, %1 offset0:10 offset1:11\n\t"
        "ds_write2_b32 %0, %6, %1 offset0:8 offset1:9\n\t"
        "s_mov_b64 exec, %11\n\t"
        "ds_write2_b32 %0, %5, %1 offset0:6 offset1:7\n\t"
        "ds_write2_b32 %0, %4, %1 offset0:4 offset1:5\n\t"
        "s_mov_b64 exec, %10\n\t"
        "ds_write2_b32 %0, %3, %1 offset0:2 offset1:3\n\t"
        "ds_write2_b32 %0, %2, %1 offset0:0 offset1:1\n\t"
        "s_mov_b64 exec, -1"
        :: "v"(la + LRAW + p * 8), "v"(cb0 + 2 * j * chunk), "v"(cl[j][0][0]), "v"(cl[j][0][1]), "v"(cl[j][1][0]),
           "v"(cl[j][1][1]), "v"(cl[j][2][0]), "v"(cl[j][2][1]), "v"(cl[j][3][0]), "v"(cl[j][3][1]), "s"(mk[j][0]),
           "s"(mk[j][1]), "s"(mk[j][2]), "s"(mk[j][3])
        : "memory");
  };
  auto src_of = [&](unsigned v) { return mulhi24(v >> 8, 13421773u); };
  auto decode = [&](unsigned v, unsigned cb, unsigned& a, unsigned& e) {
    unsigned sl;
    asm("v_lshrrev_b32 %0, 8, %3\n\t"
        "v_mul_hi_u32_u24 %0, 0xcccccd, %0\n\t"
        "v_mad_i32_i24 %1, %0, %5, %3\n\t"
        "v_mul_hi_u32_u24 %2, 0x147ae2, %1\n\t"
        "v_add_lshl_u32 %1, %1, %4, 5\n\t"
        "v_mad_i32_i24 %0, %2, %6, %0\n\t"
        "v_mad_i32_i24 %1, %2, %7, %1"
        : "=&v"(a), "=&v"(e), "=&v"(sl)
        : "v"(v), "v"(cb), "s"(-81920), "s"((int)(TPITCH << 16)), "s"(-102400));
  };
  auto decode2 = [&](const u32x2 p, const u32x2 q, unsigned& a, unsigned& e, unsigned& c, unsigned& f) {
    unsigned sl, sm;
    asm("v_lshrrev_b32 %0, 8, %6\n\t"
        "v_lshrrev_b32 %3, 8, %8\n\t"
        "v_mul_hi_u32_u24 %0, 0xcccccd, %0\n\t"
        "v_mul_hi_u32_u24 %3, 0xcccccd, %3\n\t"
        "v_mad_i32_i24 %1, %0, %10, %6\n\t"
        "v_mad_i32_i24 %4, %3, %10, %8\n\t"
        "v_mul_hi_u32_u24 %2, 0x147ae2, %1\n\t"
        "v_mul_hi_u32_u24 %5, 0x147ae2, %4\n\t"
        "v_add_lshl_u32 %1, %1, %7, 5\n\t"
        "v_add_lshl_u32 %4, %4, %9, 5\n\t"
        "v_mad_i32_i24 %0, %2, %11, %0\n\t"
        "v_mad_i32_i24 %3, %5, %11, %3\n\t"
        "v_mad_i32_i24 %1, %2, %12, %1\n\t"
        "v_mad_i32_i24 %4, %5, %12, %4"
        : "=&v"(a), "=&v"(e), "=&v"(sl), "=&v"(c), "=&v"(f), "=&v"(sm)
        : "v"(p[0]), "v"(p[1]), "v"(q[0]), "v"(q[1]), "s"(-81920), "s"((int)(TPITCH << 16)), "s"(-102400));
  };
  auto ld128 = [&](unsigned a) { return *(const LAS u32x4*)(size_t)a; };
  auto gather5 = [&](const u32x4 sx, unsigned re, int rs) {
    const unsigned dstb = ring0 + rs * ringd;
    glds16x(x, sx[0], cx0, dstb);
    glds16x(x, sx[1], cx1, dstb + 1024);
    glds16x(x, sx[2], cx0, dstb + 2048);
    glds16x(x, sx[3], cx1, dstb + 3072);
    glds16e(ES, re, ce, dstb + 4096);
  };
  auto issue_gather = [&](int tile, int rs) {
    gather5(ld128(aLX + tile * 128), *(const LAS unsigned*)(size_t)(aLE + tile * 128), rs);
  };
  head(0);
  fetch(0);
  SB();
  const int s0 = wave_scan(ni[0]);
  head(1);
  head(2);
  head(3);
  const int tot0 = __builtin_amdgcn_readlane(s0, 63);
  const int pz = s0 - ni[0];
  SB();
  asm volatile("s_waitcnt lgkmcnt(0)" : "+v"(cl[0][0]), "+v"(cl[0][1]), "+v"(cl[0][2]), "+v"(cl[0][3]),
               "+v"(XAf[0][0]), "+v"(XAf[0][1]), "+v"(XAf[1][0]), "+v"(XAf[1][1]) :: "memory");
  fill(0, pz);
  bool early = tot0 >= 32;
  int s123;
  {
    unsigned v0 = *(const LAS unsigned*)(size_t)aRX, v1 = *(const LAS unsigned*)(size_t)(aRX + 8);
    unsigned v2 = *(const LAS unsigned*)(size_t)(aRX + 16), v3 = *(const LAS unsigned*)(size_t)(aRX + 24);
    u32x2 ve = *(const LAS u32x2*)(size_t)aRE;
    SB();
    s123 = wave_scan(ni[1] | (ni[2] << 10) | (ni[3] << 20));
    SB();
    asm volatile("" : "+v"(v0), "+v"(v1), "+v"(v2), "+v"(v3), "+v"(ve), "+v"(s123));
    if (__builtin_expect(early, 1)) {
      unsigned a, e;
      decode(ve[0], ve[1], a, e);
      gather5(u32x4{src_of(v0), src_of(v1), src_of(v2), src_of(v3)}, e, 0);
    } else {
      gather5(u32x4{0u, 0u, 0u, 0u}, 0u, 0);
    }
  }
  fetch(1);
  fetch(2);
  fetch(3);
  const int e123 = __builtin_amdgcn_readlane(s123, 63);
  const int tot1 = tot0 + (e123 & 0x3ff), tot2 = tot1 + ((e123 >> 10) & 0x3ff);
  int cnt_all = tot2 + (e123 >> 20);
  asm volatile("s_waitcnt lgkmcnt(0)\n\ts_barrier"
               : "+v"(cl[1][0]), "+v"(cl[1][1]), "+v"(cl[1][2]), "+v"(cl[1][3]), "+v"(cl[2][0]), "+v"(cl[2][1]),
                 "+v"(cl[2][2]), "+v"(cl[2][3]), "+v"(cl[3][0]), "+v"(cl[3][1]), "+v"(cl[3][2]), "+v"(cl[3][3])
               :: "memory");
  if (__builtin_expect(cnt_all > LCAP, 0)) {
#pragma unroll
    for (int j = 1; j < 4; ++j)
#pragma unroll
      for (int q = 0; q < 4; ++q) mk[j][q] = 0ull;
  }
  fill(1, tot0 + (s123 & 0x3ff) - ni[1]);
  fill(2, tot1 + ((s123 >> 10) & 0x3ff) - ni[2]);
  fill(3, tot2 + (s123 >> 20) - ni[3]);
  if (__builtin_expect(__ballot(nr[0] + nr[1] + nr[2] + nr[3] != ni[0] + ni[1] + ni[2] + ni[3]) != 0ull, 0)) {
#pragma unroll
    for (int j = 0; j < 4; ++j) {
      unsigned long long om = __ballot(nr[j] != ni[j]);
      while (om) {
        const int l = __builtin_ctzll(om);
        om &= om - 1;
        const int n = __builtin_amdgcn_readlane(nr[j] - ni[j], l);
        const int p = __builtin_amdgcn_readlane(st[j] + ni[j], l) + lane;
        const int cc = 8 * (l & 31) + (l >> 5) + 2 * j;
        if (n > CCAP) cnt_all = LCAP;
        if (cnt_all + n <= LCAP && lane < n) {
          const unsigned* cp = reinterpret_cast<const unsigned*>(reinterpret_cast<const char*>(ENT) +
                                                                 ((size_t)bk * NCH + cc) * CELL);
          put(cnt_all + lane, ldd(p < CIN ? cp + 2 + p : cp + E2OFF / 4 + p - CIN), (unsigned)(cc * chunk));
        }
        cnt_all += n;
      }
    }
    __builtin_amdgcn_s_waitcnt(0x0F70);
  }
  const int gbin = bk * 8 + wave;
  auto fill_slow = [&](int wb0) __attribute__((always_inline)) {
    int nn[4], sp[4], tot = 0;
    const unsigned* rp[4];
    int l2;
    asm volatile("v_mbcnt_lo_u32_b32 %0, -1, 0\n\tv_mbcnt_hi_u32_b32 %0, -1, %0" : "=v"(l2));
    const int ch0 = 8 * (l2 & 31) + (l2 >> 5);
#pragma unroll
    for (int j = 0; j < 4; ++j) {
      const unsigned* cp = reinterpret_cast<const unsigned*>(reinterpret_cast<const char*>(ENT) +
                                                             ((size_t)bk * NCH + ch0 + 2 * j) * CELL);
      const unsigned h0 = ldd(cp), h1 = ldd(cp + 1);
      nn[j] = (int)(((gb >= 4 ? h1 : h0) >> (8 * (gb & 3))) & 0xffu);
      sp[j] = (int)dot4(h0, mlo, dot4(h1, mhi, 0u));
      rp[j] = cp + 2 + sp[j];
      sp[j] = CIN - sp[j];
      if (nn[j] == 0xff) {
        rp[j] = TS + ldd(cp + 10 + gb);
        nn[j] = (int)ldd(cp + 18 + gb);
        sp[j] = nn[j];
      }
      tot += nn[j];
    }
    const int sc = wave_scan(tot);
    int pp = sc - tot - wb0;
#pragma unroll
    for (int j = 0; j < 4; ++j) {
      const int cb = (ch0 + 2 * j) * chunk;
      const int te = min(nn[j], LCAP - pp);
#pragma unroll 1
      for (int t = max(0, -pp); t < te; ++t) put(pp + t, ldd(rp[j] + (t < sp[j] ? t : t + (int)(E2OFF / 4) - 32)), (unsigned)cb);
      pp += nn[j];
    }
    __builtin_amdgcn_s_waitcnt(0x0F70);
    return __builtin_amdgcn_readlane(sc, 63);
  };
  if (__builtin_expect(cnt_all > LCAP, 0)) {
    asm volatile("s_waitcnt vmcnt(0)" ::: "memory");
    cnt_all = fill_slow(0);
    early = false;
  }
  cnt_all = __builtin_amdgcn_readfirstlane(cnt_all);
  int cnt = min(cnt_all, LCAP);
  auto convert = [&](int cnt, unsigned aR, unsigned aF, int lane) __attribute__((always_inline)) {
    u32x2 v[8];
#pragma unroll
    for (int k = 0; k < 8; ++k) v[k] = *(const LAS u32x2*)(size_t)(aR + k * 512);
    asm volatile("" ::: "memory");
#pragma unroll
    for (int k = 0; k < 6; k += 2) {
      unsigned a, e, c, f;
      decode2(v[k], v[k + 1], a, e, c, f);
      *(LAS unsigned*)(size_t)(aF + k * 256) = a;
      *(LAS unsigned*)(size_t)(aF + k * 256 + 256) = c;
      *(LAS unsigned*)(size_t)(aF + k * 256 + LARR) = e;
      *(LAS unsigned*)(size_t)(aF + k * 256 + LARR + 256) = f;
    }
#pragma unroll
    for (int k = 6; k < 8; ++k) {
      if (cnt > k * 64) {
        unsigned a, e;
        decode(v[k][0], v[k][1], a, e);
        *(LAS unsigned*)(size_t)(aF + k * 256) = a;
        *(LAS unsigned*)(size_t)(aF + k * 256 + LARR) = e;
      }
    }
    const int u = cnt + lane;
    const unsigned padA = (unsigned)(n0 + max(min(((u >> 2) & 7) + 8 * (u & 3), nodes - 1), 0)) | (31u * TPITCH << 16);
    const unsigned pa = aF + cnt * 4;
    *(LAS unsigned*)(size_t)pa = padA;
    *(LAS unsigned*)(size_t)(pa + 256) = padA;
    *(LAS unsigned*)(size_t)(pa + LARR) = 0u;
    *(LAS unsigned*)(size_t)(pa + LARR + 256) = 0u;
  };
  convert(cnt, aR, aF, lane);
  issue_gather(1, 1);
  if (__builtin_expect(!early, 0)) {
    asm volatile("s_waitcnt vmcnt(0)" ::: "memory");
    issue_gather(0, 0);
    issue_gather(1, 1);
  }
  auto rd128 = [&](unsigned a) { return *(const LAS bf16x8*)(size_t)a; };
  auto load_S = [&](const u32x4 ss) {
    const s16x4 a0 = __builtin_amdgcn_ds_read_tr16_b64_v4i16((LAS s16x4*)(size_t)((ss[0] >> 16) + cS));
    const s16x4 a1 = __builtin_amdgcn_ds_read_tr16_b64_v4i16((LAS s16x4*)(size_t)((ss[1] >> 16) + cS));
    const s16x4 a2 = __builtin_amdgcn_ds_read_tr16_b64_v4i16((LAS s16x4*)(size_t)((ss[2] >> 16) + cS));
    const s16x4 a3 = __builtin_amdgcn_ds_read_tr16_b64_v4i16((LAS s16x4*)(size_t)((ss[3] >> 16) + cS));
    S[0] = __builtin_shufflevector(a0, a1, 0, 1, 2, 3, 4, 5, 6, 7);
    S[1] = __builtin_shufflevector(a2, a3, 0, 1, 2, 3, 4, 5, 6, 7);
  };
  auto wf = [&](int m, int ct) { return ((HELD >> m) & 1) ? wr[ct][m] : b[(m - 6) % 3]; };

  auto ew = [&](int m, const f32x16& Gc, const f32x16& Vc, const f32x16& Gn, const f32x16& Vp) {
    if (m < 2) {
      const int i = 14 + m;
      pq[i] = mulf(i, Vp[i], rq[i]);
      if (m == 1) pn[1][3] = pack_bf16(pq[14], pq[15]);
    } else if (m == 2) {
      pq[0] = mulf(0, Vc[0], rq[0]);
      pq[1] = mulf(1, Vc[1], rq[1]);
      pn[0][0] = pack_bf16(pq[0], pq[1]);
    } else {
      const int i = m - 1;
      pq[i] = mulf(i, Vc[i], rq[i]);
      if (i & 1) pn[i >> 3][(i >> 1) & 3] = pack_bf16(pq[i - 1], pq[i]);
    }
    if (m == 0) pd[0][0] = pack_bf16(rq[0], rq[1]);
    else if ((m & 1) == 0) { const int i = m + 1; pd[i >> 3][(i >> 1) & 3] = pack_bf16(rq[i - 1], rq[i]); }
    if (m < 14) {
      const int i = m + 2;
      rq[i] = __builtin_amdgcn_rcpf(add1(tq[i]));
    } else {
      rq[0] = __builtin_amdgcn_rcpf(add1(tq[0]));
      rq[1] = __builtin_amdgcn_rcpf(add1(tq[1]));
    }
    if (m < 13) tq[m + 3] = __builtin_amdgcn_exp2f(Gc[m + 3]);
    else if (m == 13) { tq[0] = __builtin_amdgcn_exp2f(Gn[0]); tq[1] = __builtin_amdgcn_exp2f(Gn[1]); }
    else tq[2] = __builtin_amdgcn_exp2f(Gn[2]);
  };

  int wb0 = 0;
  while (true) {
    const int ntiles = (cnt + 31) >> 5;
    asm volatile("s_waitcnt vmcnt(5)" ::: "memory");
    {
      const unsigned sl = *(const LAS unsigned short*)(size_t)aLS1;
#pragma unroll
      for (int k = 0; k < 4; ++k) as[k] = rd128(fo[k]);
      ae = rd128(foe);
      St[0] = rd128(sl + cSt);
      St[1] = rd128(sl + cSt + 16);
      G0 = MFMA32(St[0], XAf[0][0], zero16);
      G0 = MFMA32(St[1], XAf[1][0], G0);
      G0 = MFMA32(ae, wl[widx(6, 0) * 64], G0);
#pragma unroll
      for (int k = 0; k < 4; ++k) G0 = MFMA32(as[k], wl[widx(7 + k, 0) * 64], G0);
      V0 = MFMA32(as[0], wl[widx(11, 0) * 64], zero16);
#pragma unroll
      for (int k = 1; k < 4; ++k) V0 = MFMA32(as[k], wl[widx(11 + k, 0) * 64], V0);
      asm volatile("s_waitcnt lgkmcnt(0)" ::: "memory");
      issue_gather(2, 0);
      pipe_zero();
      tq[0] = __builtin_amdgcn_exp2f(G0[0]);
      tq[1] = __builtin_amdgcn_exp2f(G0[1]);
      tq[2] = __builtin_amdgcn_exp2f(G0[2]);
      rq[0] = __builtin_amdgcn_rcpf(add1(tq[0]));
      rq[1] = __builtin_amdgcn_rcpf(add1(tq[1]));
      SB();
    }
    unsigned &lS4 = aLS4, &lS1 = aLS1, &lX = aLX, &lE = aLE;
    int adv = 0;
    auto body = [&](const int par) {
      const unsigned lo = par * 128;
      const unsigned ro = (1 - par) * ringd;
      {
        u32x4 ss;
        unsigned sl;
#pragma unroll
        for (int m = 0; m < 15; ++m) {
          if (m == 0) Y[1] = MFMA32(__builtin_bit_cast(bf16x8, pd[0]), S[0], Y[1]);
          else if (m == 1) Y[3] = MFMA32(__builtin_bit_cast(bf16x8, pn[0]), S[0], Y[3]);
          else if (m == 2) Y[1] = MFMA32(__builtin_bit_cast(bf16x8, pd[1]), S[1], Y[1]);
          else if (m == 3) Y[3] = MFMA32(__builtin_bit_cast(bf16x8, pn[1]), S[1], Y[3]);
          else if (m == 4) G1 = MFMA32(St[0], XAf[0][1], zero16);
          else if (m == 5) G1 = MFMA32(St[1], XAf[1][1], G1);
          else if (m == 6) G1 = MFMA32(ae, wf(m, 1), G1);
          else if (m < 11) G1 = MFMA32(as[m - 7], wf(m, 1), G1);
          else if (m == 11) V1 = MFMA32(as[0], wf(m, 1), zero16);
          else V1 = MFMA32(as[m - 11], wf(m, 1), V1);
          SB();
          if (m == 0) { ss = ld128(lS4 + lo); sl = *(const LAS unsigned short*)(size_t)(lS1 + lo + 128); }
          if (m >= 3 && m < 12 && !((HELD >> (m + 3)) & 1)) b[(m - 3) % 3] = wl[widx(m + 3, 1) * 64];
          if (m == 4) load_S(ss);
          if (m == 5) { St[0] = rd128(sl + cSt); St[1] = rd128(sl + cSt + 16); }
          if (m == 6) { asm volatile("s_waitcnt vmcnt(5)" ::: "memory"); ae = rd128(foe + ro); }
          if (m >= 11) as[m - 11] = rd128(fo[m - 11] + ro);
          ew(m, G0, V0, G1, V1);
          SB();
        }
      }
      {
        u32x4 sx;
        unsigned re;
#pragma unroll
        for (int m = 0; m < 15; ++m) {
          if (m == 0) Y[0] = MFMA32(__builtin_bit_cast(bf16x8, pd[0]), S[0], Y[0]);
          else if (m == 1) Y[2] = MFMA32(__builtin_bit_cast(bf16x8, pn[0]), S[0], Y[2]);
          else if (m == 2) Y[0] = MFMA32(__builtin_bit_cast(bf16x8, pd[1]), S[1], Y[0]);
          else if (m == 3) Y[2] = MFMA32(__builtin_bit_cast(bf16x8, pn[1]), S[1], Y[2]);
          else if (m == 4) G0 = MFMA32(St[0], XAf[0][0], zero16);
          else if (m == 5) G0 = MFMA32(St[1], XAf[1][0], G0);
          else if (m == 6) G0 = MFMA32(ae, wf(m, 0), G0);
          else if (m < 11) G0 = MFMA32(as[m - 7], wf(m, 0), G0);
          else if (m == 11) V0 = MFMA32(as[0], wf(m, 0), zero16);
          else V0 = MFMA32(as[m - 11], wf(m, 0), V0);
          SB();
          if (m == 4) { sx = ld128(lX + lo + 3 * 128); re = *(const LAS unsigned*)(size_t)(lE + lo + 3 * 128); }
          if (m >= 3 && m < 12 && !((HELD >> (m + 3)) & 1)) b[(m - 3) % 3] = wl[widx(m + 3, 0) * 64];
          if (m == 7) glds16x(x, sx[0], cx0, ring0 + ro);
          if (m == 8) glds16x(x, sx[1], cx1, ring0 + ro + 1024);
          if (m == 9) glds16x(x, sx[2], cx0, ring0 + ro + 2048);
          if (m == 10) glds16x(x, sx[3], cx1, ring0 + ro + 3072);
          if (m == 11) glds16e(ES, re, ce, ring0 + ro + 4096);
          ew(m, G1, V1, G0, V0);
          SB();
        }
      }
    };
    for (int t = 0; t < ntiles; t += 2) {
      asm volatile("s_cmp_lt_u32 %0, 4\n\ts_cbranch_scc1 1f\n\ts_setprio 1\n1:" :: "s"(wave) : "scc");
      body(0);
      if (t + 1 >= ntiles) break;
      __builtin_amdgcn_s_setprio(0);
      body(1);
      lS4 += 256; lS1 += 256; lX += 256; lE += 256;
      asm volatile("" : "+v"(lS4), "+v"(lS1), "+v"(lX), "+v"(lE));
      adv += 256;
    }
    pq[14] = mulf(0, V1[14], rq[14]);
    pq[15] = mulf(1, V1[15], rq[15]);
    pn[1][3] = pack_bf16(pq[14], pq[15]);
    Y[1] = MFMA32(__builtin_bit_cast(bf16x8, pd[0]), S[0], Y[1]);
    Y[3] = MFMA32(__builtin_bit_cast(bf16x8, pn[0]), S[0], Y[3]);
    Y[1] = MFMA32(__builtin_bit_cast(bf16x8, pd[1]), S[1], Y[1]);
    Y[3] = MFMA32(__builtin_bit_cast(bf16x8, pn[1]), S[1], Y[3]);
    wb0 += LCAP;
    if (__builtin_expect(wb0 >= cnt_all, 1)) break;
    asm volatile("s_waitcnt vmcnt(0)" ::: "memory");
    cnt = min(cnt_all - wb0, LCAP);
    lS4 -= adv; lS1 -= adv; lX -= adv; lE -= adv;
    fill_slow(wb0);
    {
      int l2;
      asm volatile("v_mbcnt_lo_u32_b32 %0, -1, 0\n\tv_mbcnt_hi_u32_b32 %0, -1, %0" : "=v"(l2));
      convert(cnt, la + LRAW + l2 * 8, la + l2 * 4, l2);
    }
    issue_gather(0, 0);
    issue_gather(1, 1);
  }

  if (nodes > 0) {
    int lane;
    asm volatile("v_mbcnt_lo_u32_b32 %0, -1, 0\n\tv_mbcnt_hi_u32_b32 %0, -1, %0" : "=v"(lane));
    const int r = lane & 31, h = lane >> 5;
#pragma unroll
    for (int ct = 0; ct < 2; ++ct) {
      f32x16 U = zero16;
#pragma unroll
      for (int k = 0; k < 4; ++k) U = MFMA32(wl[(18 + k * 2 + ct) * 64], as[k], U);
#pragma unroll
      for (int q = 0; q < 2; ++q) {
        const u32x4 xv = __builtin_bit_cast(u32x4, as[2 * ct + q]);
        u32x4 ov;
#pragma unroll
        for (int j = 0; j < 4; ++j) {
          const int i = 8 * q + 2 * j;
          const float z0 = U[i] + Y[2 + ct][i] * __builtin_amdgcn_rcpf(Y[ct][i] + 1e-6f);
          const float z1 = U[i + 1] + Y[2 + ct][i + 1] * __builtin_amdgcn_rcpf(Y[ct][i + 1] + 1e-6f);
          const float o0 = __uint_as_float(xv[j] << 16) + (z0 > 0.f ? z0 : 0.f);
          const float o1 = __uint_as_float(xv[j] & 0xffff0000u) + (z1 > 0.f ? z1 : 0.f);
          ov[j] = pack_bf16(o0, o1);
        }
        *(LAS u32x4*)(size_t)(la + r * 128 + (((4 * h + 2 * ct + q) ^ (r & 7)) * 16)) = ov;
      }
    }
#pragma unroll
    for (int q = 0; q < 4; ++q) {
      const int row = 8 * q + (lane >> 3);
      const u32x4 ov = *(const LAS u32x4*)(size_t)(la + row * 128 + (((lane & 7) ^ (row & 7)) * 16));
      if (row < nodes) gstore128wt(out, (unsigned)n0 * 128u + q * 1024u + lane * 16u, ov);
    }
  }
  asm volatile("s_waitcnt vmcnt(0)" ::: "memory");
}

extern "C" void kernel_launch(void* const* d_in, const int* in_sizes, int n_in,
                              void* d_out, int out_size, void* d_ws, size_t ws_size,
                              hipStream_t stream) {
  const unsigned short* x = (const unsigned short*)d_in[0];
  const u32x4* eattr = (const u32x4*)d_in[1];
  const int* src = (const int*)d_in[2];
  const int* dst = (const int*)d_in[3];
  const unsigned short* wa = (const unsigned short*)d_in[4];
  const unsigned short* wb = (const unsigned short*)d_in[5];
  const unsigned short* wc = (const unsigned short*)d_in[6];
  const unsigned short* wu = (const unsigned short*)d_in[7];
  const unsigned short* wv = (const unsigned short*)d_in[8];
  unsigned short* out = (unsigned short*)d_out;
  const int N = in_sizes[0] / 64;
  const int E = in_sizes[2];
  const int chunk = (((E + NCH - 1) / NCH) + 3) & ~3;

  char* ws = (char*)d_ws;
  size_t off = 0;
  off = 1 << 20;
  unsigned long long* FL = (unsigned long long*)(ws + off);   off += 1 << 20;
  u32x4* ENT = (u32x4*)(ws + off);                       off += (size_t)2 * NBK * NCH * CELL;
  unsigned* TS = (unsigned*)(ws + off);

  k_main<<<NBK, 512, 0, stream>>>(x, eattr, src, dst, wa, wb, wc, wu, wv, ENT, TS, FL, out, N, E, chunk
     );
}
```

```cpp
#define QSEL 0
#include <hip/hip_runtime.h>
#include <hip/hip_bf16.h>
#include <stdint.h>
#include <stdio.h>
#define DBGDUMP 1
#define HELD 0x5500

using bf16x8 = __attribute__((ext_vector_type(8))) short;
using s16x4 = __attribute__((ext_vector_type(4))) short;
using f32x16 = __attribute__((ext_vector_type(16))) float;
using u32x4 = __attribute__((ext_vector_type(4))) unsigned;
using u32x2 = __attribute__((ext_vector_type(2))) unsigned;
using i32x4 = __attribute__((ext_vector_type(4))) int;
using u16x2 = __attribute__((ext_vector_type(2))) unsigned short;

#define MFMA32(a, b, c) __builtin_amdgcn_mfma_f32_32x32x16_bf16((a), (b), (c), 0, 0, 0)
#define LAS __attribute__((address_space(3)))
#define RANKOF(w) ((w) < 4 ? 7 - (w) : (w) - 4)
#define SB() __builtin_amdgcn_sched_barrier(0)

constexpr int NCH = 256;
constexpr int NBK = 256;
constexpr int NBIN = NBK * 8;
constexpr int NFRAG = 39;
constexpr int CHMAX = 3200;
constexpr float NLOG2E = -1.44269504088896340736f;
constexpr unsigned TPITCH = 80;

__device__ __forceinline__ unsigned pack_bf16(float lo, float hi) {
  typedef __attribute__((ext_vector_type(2))) float f2;
  typedef __attribute__((ext_vector_type(2))) __bf16 b2;
  f2 v = {lo, hi};
  b2 r = __builtin_convertvector(v, b2);
  return __builtin_bit_cast(unsigned, r);
}

__device__ __forceinline__ int wave_scan(int v) {
  v += __builtin_amdgcn_update_dpp(0, v, 0x111, 0xf, 0xf, false);
  v += __builtin_amdgcn_update_dpp(0, v, 0x112, 0xf, 0xf, false);
  v += __builtin_amdgcn_update_dpp(0, v, 0x114, 0xf, 0xf, false);
  v += __builtin_amdgcn_update_dpp(0, v, 0x118, 0xf, 0xf, false);
  v += __builtin_amdgcn_update_dpp(0, v, 0x142, 0xa, 0xf, false);
  v += __builtin_amdgcn_update_dpp(0, v, 0x143, 0xc, 0xf, false);
  return v;
}

__device__ __forceinline__ void glds16(const void* sbase, unsigned voff, unsigned lds_dst) {
  asm volatile(
      "s_mov_b32 m0, %2\n\t"
      "s_nop 0\n\t"
      "global_load_lds_dwordx4 %0, %1"
      :: "v"(voff), "s"(sbase), "s"(lds_dst) : "memory", "m0");
}
__device__ __forceinline__ void glds16nt(const void* sbase, unsigned voff, unsigned lds_dst) {
  asm volatile(
      "s_mov_b32 m0, %2\n\t"
      "s_nop 0\n\t"
      "global_load_lds_dwordx4 %0, %1 nt"
      :: "v"(voff), "s"(sbase), "s"(lds_dst) : "memory", "m0");
}
__device__ __forceinline__ void glds16x(const void* sbase, unsigned a, unsigned c, unsigned lds_dst) {
  unsigned tmp;
  asm volatile(
      "s_mov_b32 m0, %4\n\t"
      "v_mad_u32_u16 %0, %1, %5, %2\n\t"
      "global_load_lds_dwordx4 %0, %3"
      : "=&v"(tmp) : "v"(a), "v"(c), "s"(sbase), "s"(lds_dst), "s"(128u) : "memory", "m0");
}
__device__ __forceinline__ void glds16e(const void* sbase, unsigned a, unsigned c, unsigned lds_dst) {
  unsigned tmp;
  asm volatile(
      "s_mov_b32 m0, %4\n\t"
      "v_add_u32 %0, %1, %2\n\t"
      "global_load_lds_dwordx4 %0, %3"
      : "=&v"(tmp) : "v"(a), "v"(c), "s"(sbase), "s"(lds_dst) : "memory", "m0");
}
__device__ __forceinline__ int gload32(const void* sbase, unsigned voff) {
  int r;
  asm volatile("global_load_dword %0, %1, %2" : "=v"(r) : "v"(voff), "s"(sbase) : "memory");
  return r;
}
__device__ __forceinline__ u32x4 gload128(const void* sbase, unsigned voff) {
  u32x4 r;
  asm volatile("global_load_dwordx4 %0, %1, %2" : "=v"(r) : "v"(voff), "s"(sbase) : "memory");
  return r;
}

__device__ __forceinline__ u32x4 gload128nt(const void* sbase, unsigned voff) {
  u32x4 r;
  asm volatile("global_load_dwordx4 %0, %1, %2 nt" : "=v"(r) : "v"(voff), "s"(sbase) : "memory");
  return r;
}
__device__ __forceinline__ void gstore128wt(void* sbase, unsigned voff, u32x4 v) {
  asm volatile("global_store_dwordx4 %0, %1, %2 sc1\n\ts_nop 1" :: "v"(voff), "v"(v), "s"(sbase) : "memory");
}

constexpr int CELL = 128;
constexpr int CIN = 30;
constexpr int CCAP = 62;
constexpr int CPIT = 272;
constexpr unsigned E2OFF = NBK * NCH * CELL;
constexpr int KP_HDR = 0;
constexpr int KP_OVC = KP_HDR + NBK * 8;
constexpr int KP_ENT = KP_OVC + 16;
constexpr int KP_HIST = KP_ENT + NBK * CPIT;
constexpr int KP_LDS = KP_HIST + NBIN * 4;
constexpr int NWL = 26;
constexpr int NBLOB = 31;

__device__ __forceinline__ unsigned mad24(unsigned a, int b, unsigned c) {
  unsigned r;
  asm("v_mad_i32_i24 %0, %1, %2, %3" : "=v"(r) : "v"(a), "s"(b), "v"(c));
  return r;
}
__device__ __forceinline__ unsigned mul24(unsigned a, unsigned b) {
  unsigned r;
  asm("v_mul_u32_u24 %0, %1, %2" : "=v"(r) : "n"(b), "v"(a));
  return r;
}
__device__ __forceinline__ unsigned mulhi24(unsigned a, unsigned b) {
  return (unsigned)(((unsigned long long)(a & 0xffffffu) * (unsigned long long)b) >> 32);
}
__device__ __forceinline__ unsigned dot4(unsigned a, unsigned b, unsigned c) {
  return __builtin_amdgcn_udot4(a, b, c, false);
}

__global__ __launch_bounds__(1024) void k_part(const int* __restrict__ src, const int* __restrict__ dst,
                                               int E, int chunk, u32x4* __restrict__ ENT,
                                               unsigned* __restrict__ TS,
                                               const unsigned short* __restrict__ wa,
                                               const unsigned short* __restrict__ wb,
                                               const unsigned short* __restrict__ wc,
                                               const unsigned short* __restrict__ wu,
                                               const unsigned short* __restrict__ wv,
                                               unsigned short* __restrict__ wpack) {
  const int tid = threadIdx.x;
#define PT(i)
  PT(0);
  if (tid == 0 && blockIdx.x < NCH) ((unsigned long long*)(wpack + 20000))[blockIdx.x] = __builtin_amdgcn_s_memrealtime();
  if (blockIdx.x < NCH) {
    __shared__ __attribute__((aligned(16))) char sm[KP_LDS];
    LAS unsigned* hist = (LAS unsigned*)(sm + KP_HIST);
    LAS unsigned* ovc = (LAS unsigned*)(sm + KP_OVC);
    const int c = blockIdx.x;
    const int base = c * chunk;
    const int cnt = max(min(chunk, E - base), 0);
    const int i0 = max(min(base + 4 * tid, min(base + cnt, E) - 4), 0);
    const int lo = base + 4 * tid - i0, hi4 = base + cnt - i0;
    u32x4 d = gload128nt(dst, (unsigned)i0 * 4u);
    u32x4 sv = gload128nt(src, (unsigned)i0 * 4u);
    if (tid < NBK) *(LAS u32x2*)(sm + KP_HDR + tid * 8) = u32x2{0u, 0u};
    if (tid == 0) *(LAS u32x2*)ovc = u32x2{0u, 0u};
    const unsigned sm0 = (unsigned)(size_t)(LAS char*)sm;
    const bool act = __builtin_amdgcn_readfirstlane(tid) * 4 < cnt;
    unsigned long long on[4];
    unsigned tup[4];
#pragma unroll
    for (int k = 0; k < 4; ++k) {
      on[k] = __ballot(k >= lo && k < hi4);
      tup[k] = (unsigned)(i0 + k - base);
    }
    const int bk = tid >> 2, p0 = (tid & 3) * 16;
    unsigned ha = sm0 + KP_HDR + bk * 8, ia = sm0 + KP_ENT + bk * CPIT + p0;
    unsigned go = (unsigned)((bk * NCH + c) * CELL + p0);
    asm volatile("s_waitcnt lgkmcnt(0)\n\ts_barrier\n\ts_waitcnt vmcnt(1)"
                 : "+v"(d), "+v"(tup[0]), "+v"(tup[1]), "+v"(tup[2]), "+v"(tup[3]), "+v"(ha), "+v"(ia), "+v"(go),
                   "+s"(on[0]), "+s"(on[1]), "+s"(on[2]), "+s"(on[3])
                 :: "memory");
    PT(1);
    unsigned gi[4], rk[4], m0[4], m1[4], cb[4];
    if (act) {
      unsigned oh[4], sh[4];
#pragma unroll
      for (int k = 0; k < 4; ++k) {
        gi[k] = mulhi24(d[k] << 8, 684785u);
        sh[k] = gi[k] << 3;
        oh[k] = 1u << (sh[k] & 31u);
        rk[k] = 0u;
      }
      asm volatile(
          "s_mov_b64 exec, %12\n\t"
          "ds_add_rtn_u32 %0, %4, %8\n\t"
          "s_mov_b64 exec, %13\n\t"
          "ds_add_rtn_u32 %1, %5, %9\n\t"
          "s_mov_b64 exec, %14\n\t"
          "ds_add_rtn_u32 %2, %6, %10\n\t"
          "s_mov_b64 exec, %15\n\t"
          "ds_add_rtn_u32 %3, %7, %11\n\t"
          "s_mov_b64 exec, -1"
          : "+&v"(rk[0]), "+&v"(rk[1]), "+&v"(rk[2]), "+&v"(rk[3])
          : "v"((gi[0] & ~3u) + sm0), "v"((gi[1] & ~3u) + sm0), "v"((gi[2] & ~3u) + sm0), "v"((gi[3] & ~3u) + sm0),
            "v"(oh[0]), "v"(oh[1]), "v"(oh[2]), "v"(oh[3]), "s"(on[0]), "s"(on[1]), "s"(on[2]), "s"(on[3])
          : "memory");
#pragma unroll
      for (int k = 0; k < 4; ++k) {
        const unsigned slot = mad24(gi[k], -49 * 128, d[k] << 8) >> 8;
        tup[k] = mad24(slot, 3200, tup[k]);
        const unsigned hm = (unsigned)((int)(gi[k] << 29) >> 31);
        m0[k] = ((oh[k] - 1u) | hm) & 0x04040404u;
        m1[k] = (oh[k] - 1u) & hm & 0x04040404u;
        gi[k] &= ~7u;
        cb[k] = mul24(gi[k], CPIT / 8) + sm0;
        gi[k] += sm0;
      }
      SB();
      asm volatile("s_waitcnt lgkmcnt(0)" : "+v"(rk[0]), "+v"(rk[1]), "+v"(rk[2]), "+v"(rk[3]) :: "memory");
#pragma unroll
      for (int k = 0; k < 4; ++k) asm("v_bfe_u32 %0, %0, %1, 8" : "+v"(rk[k]) : "v"(sh[k]));
      if (max(max(rk[0], rk[1]), max(rk[2], rk[3])) >= (unsigned)CCAP) ovc[1] = 1u;
#pragma unroll
      for (int k = 0; k < 4; ++k) {
        rk[k] = cb[k] + rk[k] * 4;
        cb[k] += CCAP * 4;
      }
    }
    asm volatile("s_waitcnt vmcnt(0)" : "+v"(sv) :: "memory");
#pragma unroll
    for (int k = 0; k < 4; ++k) tup[k] = mad24(sv[k], 81920, tup[k]);
    asm volatile("s_barrier"
                 : "+v"(rk[0]), "+v"(rk[1]), "+v"(rk[2]), "+v"(rk[3]), "+v"(m0[0]), "+v"(m0[1]), "+v"(m0[2]),
                   "+v"(m0[3]), "+v"(m1[0]), "+v"(m1[1]), "+v"(m1[2]), "+v"(m1[3]), "+v"(tup[0]), "+v"(tup[1]),
                   "+v"(tup[2]), "+v"(tup[3]), "+v"(cb[0]), "+v"(cb[1]), "+v"(cb[2]), "+v"(cb[3]), "+v"(gi[0]),
                   "+v"(gi[1]), "+v"(gi[2]), "+v"(gi[3])
                 :: "memory");
    PT(2);
    u32x2 hc = *(const LAS u32x2*)(size_t)ha;
    if (act) {
      u32x2 hd[4];
      unsigned pa[4];
#pragma unroll
      for (int k = 0; k < 4; ++k) hd[k] = *(const LAS u32x2*)(size_t)gi[k];
#pragma unroll
      for (int k = 0; k < 4; ++k) pa[k] = min(dot4(hd[k][0], m0[k], dot4(hd[k][1], m1[k], rk[k])), cb[k]);
      asm volatile(
          "s_mov_b64 exec, %8\n\t"
          "ds_write_b32 %0, %4 offset:%12\n\t"
          "s_mov_b64 exec, %9\n\t"
          "ds_write_b32 %1, %5 offset:%12\n\t"
          "s_mov_b64 exec, %10\n\t"
          "ds_write_b32 %2, %6 offset:%12\n\t"
          "s_mov_b64 exec, %11\n\t"
          "ds_write_b32 %3, %7 offset:%12\n\t"
          "s_mov_b64 exec, -1"
          :: "v"(pa[0]), "v"(pa[1]), "v"(pa[2]), "v"(pa[3]), "v"(tup[0]), "v"(tup[1]), "v"(tup[2]), "v"(tup[3]),
             "s"(on[0]), "s"(on[1]), "s"(on[2]), "s"(on[3]), "n"(KP_ENT + 8)
          : "memory");
    }
    unsigned used = dot4(hc[0], 0x01010101u, dot4(hc[1], 0x01010101u, 0u));
    if (used > (unsigned)CCAP) ovc[1] = 1u;
    asm volatile("s_waitcnt lgkmcnt(0)\n\ts_barrier" ::: "memory");
    PT(3); PT(4);
    const unsigned ovf = ovc[1];
    u32x4 v0 = *(const LAS u32x4*)(size_t)ia;
    u32x4 v1 = *(const LAS u32x4*)(size_t)(ia + 64);
    asm volatile("" : "+v"(v0), "+v"(v1));
    if (__builtin_expect(__builtin_amdgcn_readfirstlane(ovf) != 0u, 0)) {
      *(LAS u32x2*)(hist + tid * 2) = u32x2{0u, 0u};
      __syncthreads();
#pragma unroll
      for (int k = 0; k < 4; ++k) {
        gi[k] = mulhi24(d[k] << 8, 684785u);
        if (k >= lo && k < hi4)
          rk[k] = __hip_atomic_fetch_add(hist + gi[k], 1u, __ATOMIC_RELAXED, __HIP_MEMORY_SCOPE_WORKGROUP);
      }
      __syncthreads();
      if (tid < NBK) {
        unsigned tot = 0;
        for (int i = 0; i < 8; ++i) tot += hist[tid * 8 + i];
        LAS unsigned* cp = (LAS unsigned*)(sm + KP_ENT + tid * CPIT);
        unsigned p = ~0u;
        if (tot > (unsigned)CCAP) {
          p = (unsigned)base + __hip_atomic_fetch_add(ovc, tot, __ATOMIC_RELAXED, __HIP_MEMORY_SCOPE_WORKGROUP);
          *(LAS u32x2*)(sm + KP_HDR + tid * 8) = u32x2{~0u, ~0u};
        }
        for (int i = 0; i < 8; ++i) {
          const unsigned n = hist[tid * 8 + i];
          hist[tid * 8 + i] = p;
          if (p != ~0u) {
            cp[2 + i] = 0u;
            cp[10 + i] = p;
            cp[18 + i] = n;
            p += n;
          }
        }
      }
      __syncthreads();
#pragma unroll
      for (int k = 0; k < 4; ++k) {
        if (k >= lo && k < hi4) {
          const unsigned p = hist[gi[k]];
          if (p != ~0u) TS[p + rk[k]] = tup[k];
        }
      }
      hc = *(const LAS u32x2*)(size_t)ha;
      used = dot4(hc[0], 0x01010101u, dot4(hc[1], 0x01010101u, 0u));
      v0 = *(const LAS u32x4*)(size_t)ia;
      v1 = *(const LAS u32x4*)(size_t)(ia + 64);
    }
    if (p0 == 0) { v0[0] = hc[0]; v0[1] = hc[1]; }
    gstore128wt(ENT, go, v0);
    if (used > 14u) gstore128wt(ENT, go + 64, v1);
    if (__builtin_expect(used > (unsigned)CIN, 0)) {
      gstore128wt(ENT, go + E2OFF, *(const LAS u32x4*)(size_t)(ia + 128));
      gstore128wt(ENT, go + E2OFF + 64, *(const LAS u32x4*)(size_t)(ia + 192));
    }
  } else if (tid < 512) {
    const int f = blockIdx.x - NCH;
    unsigned short v;
    if (f >= NWL && f < NBLOB) {
      const int e = (f - NWL) * 512 + tid;
      const int tb = e >= 32 * (TPITCH / 2);
      const int e2 = e - tb * 32 * (TPITCH / 2);
      const int sl = e2 / (TPITCH / 2), col = e2 - sl * (TPITCH / 2);
      const int nd = tb ? 16 * ((col >> 3) & 1) + 8 * ((col >> 2) & 1) + 4 * (col >> 4) + (col & 3) : col;
      v = (col < 32 && nd == sl) ? 0x3F80 : 0;
    } else {
      const int lane = tid >> 3, j = tid & 7;
      const int n = lane & 31, h = lane >> 5;
      const int g = f < 16 ? f : f < NWL ? f - 18 : f - NBLOB;
      const int ct = g & 1;
      const int col = 32 * ((n >> 2) & 1) + 16 * ct + 4 * (n >> 3) + (n & 3);
      if (f == 16 || f == 17) {
        v = wc[(8 * h + j) * 64 + col];
      } else {
        const int t = (g >> 1) & 3;
        const unsigned short* W = f < 8 ? wb : f < 16 ? wv : f < NWL ? wu : wa;
        v = W[(32 * h + 8 * t + j) * 64 + col];
      }
      if (f < 8 || f == 16 || f == 17 || f >= NBLOB)
        v = (unsigned short)(pack_bf16(__uint_as_float((unsigned)v << 16) * NLOG2E, 0.f) & 0xffffu);
    }
    wpack[f * 512 + tid] = v;
  }
}

constexpr int RING_SLOT = 5 * 1024;
constexpr int SPIT = 1040;
constexpr int LCAP = 512;
constexpr int LARR = 2560;
constexpr int LIST_BYTES = 2 * LARR;
constexpr int LRAW = LIST_BYTES - (LCAP + 8) * 8;
constexpr int OFF_W = 0;
constexpr int OFF_T1 = NWL * 1024;
constexpr int OFF_T2 = OFF_T1 + 32 * TPITCH;
constexpr int OFF_WA = NBLOB * 1024;
constexpr int OFF_R0 = NFRAG * 1024;
constexpr int OFF_R1 = OFF_R0 + 8 * RING_SLOT;
constexpr int OFF_L = OFF_R1 + 8 * RING_SLOT;
constexpr int MAIN_LDS = OFF_L + 8 * LIST_BYTES;
static_assert(MAIN_LDS <= 160 * 1024, "LDS");

__device__ __forceinline__ float add1(float t) { return 1.0f + t; }
__device__ __forceinline__ float mulf(int i, float a, float b) { return (i & 1) ? __builtin_fmaf(a, b, 0.0f) : a * b; }

__global__ __launch_bounds__(512, 2) void k_main(const unsigned short* __restrict__ x,
                                                 const u32x4* __restrict__ ENT,
                                                 const u32x4* __restrict__ ES,
                                                 const unsigned* __restrict__ TS, int chunk,
                                                 const bf16x8* __restrict__ wpack,
                                                 unsigned short* __restrict__ out, int N
                                                 , long long* dbg
                                                 ) {
  const long long tm0 = __builtin_amdgcn_s_memrealtime();
  __shared__ __attribute__((aligned(16))) char smem[MAIN_LDS];
  const int tid = threadIdx.x, lane = tid & 63;
  const int wave = __builtin_amdgcn_readfirstlane(tid >> 6);
  const int r = lane & 31, h = lane >> 5;
  const int bk = blockIdx.x;
  const unsigned lds0 = (unsigned)(size_t)(LAS char*)smem;

  int warm;
  {
    const unsigned vo = lane * 16;
    const char* eb = reinterpret_cast<const char*>(ENT) + (size_t)bk * (NCH * CELL);
#pragma unroll
    for (int i = 0; i < 4; ++i) glds16(eb + (wave * 4 + i) * 1024, vo, lds0 + OFF_R1 + (wave * 4 + i) * SPIT);
    const char* wp = reinterpret_cast<const char*>(wpack);
#pragma unroll
    for (int i = 0; i < 5; ++i) {
      const int f = min(wave + 8 * i, NFRAG - 1);
      glds16(wp + f * 1024, vo, lds0 + OFF_W + f * 1024);
    }
    warm = gload32(x, (unsigned)min(bk * 196 + 25 * wave + min(lane, 24), N - 1) * 128u);
    if (wave >= 4) __builtin_amdgcn_s_setprio(1);
  }
  LAS char* lbase = (LAS char*)smem + OFF_L + wave * LIST_BYTES;
  const unsigned la = (unsigned)(size_t)lbase;
  auto put = [&](int p, unsigned a, unsigned b) { *(LAS u32x2*)(lbase + LRAW + p * 8) = u32x2{a, b}; };
  unsigned aLX = la + (lane >> 3) * 16;
  unsigned aLE = la + LARR + ((((lane >> 1) & 7) << 2) | (lane >> 4)) * 4;
  unsigned aLS4 = la + (4 * h + ((lane >> 2) & 3)) * 16;
  unsigned aLS1 = la + (((r & 7) << 2) | (r >> 3)) * 4 + 2;
  unsigned cx0 = (unsigned)(((lane & 7) ^ (lane >> 4)) * 16);
  unsigned cx1 = cx0 ^ 64u;
  unsigned ce = (unsigned)((((lane & 1) ^ ((lane >> 4) & 1))) * 16);
  const unsigned ring0 = lds0 + OFF_R0 + wave * RING_SLOT;
  const unsigned ringd = 8 * RING_SLOT;
  unsigned cS = lds0 + OFF_T1 + (((lane >> 4) & 1) * 16 + (lane & 3) * 4) * 2;
  unsigned cSt = lds0 + OFF_T2 + h * 32;
  unsigned fo[4];
#pragma unroll
  for (int t = 0; t < 4; ++t) fo[t] = ring0 + (unsigned)(r * 128 + (((4 * h + t) ^ ((r >> 1) & 7)) * 16));
  unsigned foe = ring0 + (unsigned)(4096 + r * 32 + ((h ^ ((r >> 3) & 1)) * 16));
  unsigned sa = lds0 + OFF_R1 + r * SPIT + h * CELL;
  unsigned cb0 = (unsigned)((8 * r + h) * chunk);
  unsigned aR = la + LRAW + lane * 8, aF = la + lane * 4;
  unsigned aRX = la + LRAW + (lane >> 3) * 32, aRE = la + LRAW + ((((lane >> 1) & 7) << 2) | (lane >> 4)) * 8;
  f32x16 Y[4];
#pragma unroll
  for (int q = 0; q < 4; ++q)
#pragma unroll
    for (int i = 0; i < 16; ++i) Y[q][i] = 0.f;
  f32x16 G0, V0, G1, V1;
  bf16x8 S[2], St[2], as[4], ae, b[3];
  u32x4 pd[2], pn[2];
  float tq[16], rq[16], pq[16];
  auto pipe_zero = [&]() {
#pragma unroll
    for (int j = 0; j < 8; ++j) { S[0][j] = 0; S[1][j] = 0; }
#pragma unroll
    for (int j = 0; j < 4; ++j) { pd[0][j] = 0; pd[1][j] = 0; pn[0][j] = 0; pn[1][j] = 0; }
#pragma unroll
    for (int i = 0; i < 16; ++i) V1[i] = 0.f;
    rq[14] = 0.f; rq[15] = 0.f;
  };
  asm volatile("" : "+v"(aLX), "+v"(aLE), "+v"(aLS4), "+v"(aLS1), "+v"(cx0), "+v"(cx1), "+v"(ce), "+v"(cS), "+v"(cSt),
                    "+v"(fo[0]), "+v"(fo[1]), "+v"(fo[2]), "+v"(fo[3]), "+v"(foe), "+v"(sa), "+v"(cb0), "+v"(aR),
                    "+v"(aF), "+v"(aRX), "+v"(aRE));
  asm volatile("" : "+v"(Y[0]), "+v"(Y[1]), "+v"(Y[2]), "+v"(Y[3]));
  unsigned rsh = (lane & 3) * 8, rid = lane & 7;
  unsigned long long rhi = 0xf0f0f0f0f0f0f0f0ull;
  asm volatile("" : "+v"(rsh), "+v"(rid), "+s"(rhi));
  asm volatile("s_waitcnt vmcnt(6)\n\ts_barrier" ::: "memory");
  const long long tmA = __builtin_amdgcn_s_memrealtime();
  u32x2 cl[4][4], hd[4];
  int nr[4], st[4], ni[4];
  unsigned long long mk[4][4];
#pragma unroll
  for (int j = 0; j < 4; ++j) hd[j] = *(const LAS u32x2*)(size_t)(sa + 2 * j * CELL);
  int gb;
  {
    int lo = (int)(hd[0][0] + hd[1][0] + hd[2][0] + hd[3][0]), hi = (int)(hd[0][1] + hd[1][1] + hd[2][1] + hd[3][1]);
    lo += __builtin_amdgcn_update_dpp(0, lo, 0xb1, 0xf, 0xf, false);
    hi += __builtin_amdgcn_update_dpp(0, hi, 0xb1, 0xf, 0xf, false);
    lo += __builtin_amdgcn_update_dpp(0, lo, 0x4e, 0xf, 0xf, false);
    hi += __builtin_amdgcn_update_dpp(0, hi, 0x4e, 0xf, 0xf, false);
    lo += __builtin_amdgcn_update_dpp(0, lo, 0x141, 0xf, 0xf, false);
    hi += __builtin_amdgcn_update_dpp(0, hi, 0x141, 0xf, 0xf, false);
    lo += __builtin_amdgcn_update_dpp(0, lo, 0x140, 0xf, 0xf, false);
    hi += __builtin_amdgcn_update_dpp(0, hi, 0x140, 0xf, 0xf, false);
    unsigned c;
    asm("v_cndmask_b32 %0, %1, %2, %3\n\tv_bfe_u32 %0, %0, %4, 8" : "=&v"(c) : "v"(lo), "v"(hi), "s"(rhi), "v"(rsh));
    auto p16 = __builtin_amdgcn_permlane16_swap(c, c, false, false);
    c = p16[0] + p16[1];
    auto p32 = __builtin_amdgcn_permlane32_swap(c, c, false, false);
    const int key = (int)(((p32[0] + p32[1]) << 3) | rid);
    unsigned acc = 0u;
#define RK(k) acc = __builtin_amdgcn_alignbit(acc, (unsigned)(__builtin_amdgcn_update_dpp(0, key, 0x120 + k, 0xf, 0xf, false) - key), 31)
    RK(1); RK(2); RK(3); RK(4); RK(5); RK(6); RK(7);
#undef RK
    const int want = RANKOF(wave);
    gb = __builtin_ctzll(__ballot(__builtin_popcount(acc) == want)) & 7;
  }
  const int nb0 = bk * 196 + 49 * (gb >> 1) + 25 * (gb & 1);
  const int nodes = max(min(25 - (gb & 1), N - nb0), 0);
  const int n0 = min(nb0, N - 1);
  const unsigned mlo = gb >= 4 ? 0x01010101u : 0x01010101u & ((1u << (8 * gb)) - 1u);
  const unsigned mhi = gb <= 4 ? 0u : 0x01010101u & ((1u << (8 * (gb - 4))) - 1u);
  u32x4 xg[4];
  {
    const unsigned xo = (unsigned)(n0 + max(min(r, nodes - 1), 0)) * 128u + 64u * h;
#pragma unroll
    for (int t = 0; t < 4; ++t) xg[t] = gload128(x, xo + 16 * t);
  }
  auto head = [&](int j) {
    nr[j] = (int)(((gb >= 4 ? hd[j][1] : hd[j][0]) >> (8 * (gb & 3))) & 0xffu);
    st[j] = (int)dot4(hd[j][0], mlo, dot4(hd[j][1], mhi, 0u));
    const int n = min(min(nr[j], 8), CIN - st[j]);
#pragma unroll
    for (int q = 0; q < 4; ++q) mk[j][q] = __ballot(n > 2 * q);
    ni[j] = max(n, 0);
  };
  auto fetch = [&](int j) {
    asm volatile(
        "s_mov_b64 exec, %5\n\t"
        "ds_read2_b32 %0, %4 offset0:%9 offset1:%9+1\n\t"
        "s_mov_b64 exec, %6\n\t"
        "ds_read2_b32 %1, %4 offset0:%9+2 offset1:%9+3\n\t"
        "s_mov_b64 exec, %7\n\t"
        "ds_read2_b32 %2, %4 offset0:%9+4 offset1:%9+5\n\t"
        "s_mov_b64 exec, %8\n\t"
        "ds_read2_b32 %3, %4 offset0:%9+6 offset1:%9+7\n\t"
        "s_mov_b64 exec, -1"
        : "=&v"(cl[j][0]), "=&v"(cl[j][1]), "=&v"(cl[j][2]), "=&v"(cl[j][3])
        : "v"(sa + st[j] * 4), "s"(mk[j][0]), "s"(mk[j][1]), "s"(mk[j][2]), "s"(mk[j][3]), "n"(j * 64 + 2)
        : "memory");
  };
  auto fill = [&](int j, int p) {
    asm volatile(
        "s_mov_b64 exec, %13\n\t"
        "ds_write2_b32 %0, %9, %1 offset0:14 offset1:15\n\t"
        "ds_write2_b32 %0, %8, %1 offset0:12 offset1:13\n\t"
        "s_mov_b64 exec, %12\n\t"
        "ds_write2_b32 %0, %7, %1 offset0:10 offset1:11\n\t"
        "ds_write2_b32 %0, %6, %1 offset0:8 offset1:9\n\t"
        "s_mov_b64 exec, %11\n\t"
        "ds_write2_b32 %0, %5, %1 offset0:6 offset1:7\n\t"
        "ds_write2_b32 %0, %4, %1 offset0:4 offset1:5\n\t"
        "s_mov_b64 exec, %10\n\t"
        "ds_write2_b32 %0, %3, %1 offset0:2 offset1:3\n\t"
        "ds_write2_b32 %0, %2, %1 offset0:0 offset1:1\n\t"
        "s_mov_b64 exec, -1"
        :: "v"(la + LRAW + p * 8), "v"(cb0 + 2 * j * chunk), "v"(cl[j][0][0]), "v"(cl[j][0][1]), "v"(cl[j][1][0]),
           "v"(cl[j][1][1]), "v"(cl[j][2][0]), "v"(cl[j][2][1]), "v"(cl[j][3][0]), "v"(cl[j][3][1]), "s"(mk[j][0]),
           "s"(mk[j][1]), "s"(mk[j][2]), "s"(mk[j][3])
        : "memory");
  };
  auto src_of = [&](unsigned v) { return mulhi24(v >> 8, 13421773u); };
  auto decode = [&](unsigned v, unsigned cb, unsigned& a, unsigned& e) {
    unsigned sl;
    asm("v_lshrrev_b32 %0, 8, %3\n\t"
        "v_mul_hi_u32_u24 %0, 0xcccccd, %0\n\t"
        "v_mad_i32_i24 %1, %0, %5, %3\n\t"
        "v_mul_hi_u32_u24 %2, 0x147ae2, %1\n\t"
        "v_add_lshl_u32 %1, %1, %4, 5\n\t"
        "v_mad_i32_i24 %0, %2, %6, %0\n\t"
        "v_mad_i32_i24 %1, %2, %7, %1"
        : "=&v"(a), "=&v"(e), "=&v"(sl)
        : "v"(v), "v"(cb), "s"(-81920), "s"((int)(TPITCH << 16)), "s"(-102400));
  };
  auto decode2 = [&](const u32x2 p, const u32x2 q, unsigned& a, unsigned& e, unsigned& c, unsigned& f) {
    unsigned sl, sm;
    asm("v_lshrrev_b32 %0, 8, %6\n\t"
        "v_lshrrev_b32 %3, 8, %8\n\t"
        "v_mul_hi_u32_u24 %0, 0xcccccd, %0\n\t"
        "v_mul_hi_u32_u24 %3, 0xcccccd, %3\n\t"
        "v_mad_i32_i24 %1, %0, %10, %6\n\t"
        "v_mad_i32_i24 %4, %3, %10, %8\n\t"
        "v_mul_hi_u32_u24 %2, 0x147ae2, %1\n\t"
        "v_mul_hi_u32_u24 %5, 0x147ae2, %4\n\t"
        "v_add_lshl_u32 %1, %1, %7, 5\n\t"
        "v_add_lshl_u32 %4, %4, %9, 5\n\t"
        "v_mad_i32_i24 %0, %2, %11, %0\n\t"
        "v_mad_i32_i24 %3, %5, %11, %3\n\t"
        "v_mad_i32_i24 %1, %2, %12, %1\n\t"
        "v_mad_i32_i24 %4, %5, %12, %4"
        : "=&v"(a), "=&v"(e), "=&v"(sl), "=&v"(c), "=&v"(f), "=&v"(sm)
        : "v"(p[0]), "v"(p[1]), "v"(q[0]), "v"(q[1]), "s"(-81920), "s"((int)(TPITCH << 16)), "s"(-102400));
  };
  auto ld128 = [&](unsigned a) { return *(const LAS u32x4*)(size_t)a; };
  auto gather5 = [&](const u32x4 sx, unsigned re, int rs) {
    const unsigned dstb = ring0 + rs * ringd;
    glds16x(x, sx[0], cx0, dstb);
    glds16x(x, sx[1], cx1, dstb + 1024);
    glds16x(x, sx[2], cx0, dstb + 2048);
    glds16x(x, sx[3], cx1, dstb + 3072);
    glds16e(ES, re, ce, dstb + 4096);
  };
  auto issue_gather = [&](int tile, int rs) {
    gather5(ld128(aLX + tile * 128), *(const LAS unsigned*)(size_t)(aLE + tile * 128), rs);
  };
  head(0);
  fetch(0);
  SB();
  const int s0 = wave_scan(ni[0]);
  head(1);
  head(2);
  head(3);
  const int tot0 = __builtin_amdgcn_readlane(s0, 63);
  const int p0 = s0 - ni[0];
  SB();
  asm volatile("s_waitcnt lgkmcnt(0)" : "+v"(cl[0][0]), "+v"(cl[0][1]), "+v"(cl[0][2]), "+v"(cl[0][3]) :: "memory");
  fill(0, p0);
  bool early = tot0 >= 32;
  int s123;
  {
    unsigned v0 = *(const LAS unsigned*)(size_t)aRX, v1 = *(const LAS unsigned*)(size_t)(aRX + 8);
    unsigned v2 = *(const LAS unsigned*)(size_t)(aRX + 16), v3 = *(const LAS unsigned*)(size_t)(aRX + 24);
    u32x2 ve = *(const LAS u32x2*)(size_t)aRE;
    SB();
    s123 = wave_scan(ni[1] | (ni[2] << 10) | (ni[3] << 20));
    SB();
    asm volatile("" : "+v"(v0), "+v"(v1), "+v"(v2), "+v"(v3), "+v"(ve), "+v"(s123));
    if (__builtin_expect(early, 1)) {
      unsigned a, e;
      decode(ve[0], ve[1], a, e);
      gather5(u32x4{src_of(v0), src_of(v1), src_of(v2), src_of(v3)}, e, 0);
    } else {
      gather5(u32x4{0u, 0u, 0u, 0u}, 0u, 0);
    }
  }
  fetch(1);
  fetch(2);
  fetch(3);
  const int e123 = __builtin_amdgcn_readlane(s123, 63);
  const int tot1 = tot0 + (e123 & 0x3ff), tot2 = tot1 + ((e123 >> 10) & 0x3ff);
  int cnt_all = tot2 + (e123 >> 20);
  asm volatile("s_waitcnt vmcnt(5) lgkmcnt(0)\n\ts_barrier"
               : "+v"(xg[0]), "+v"(xg[1]), "+v"(xg[2]), "+v"(xg[3]), "+v"(warm),
                 "+v"(cl[1][0]), "+v"(cl[1][1]), "+v"(cl[1][2]), "+v"(cl[1][3]), "+v"(cl[2][0]), "+v"(cl[2][1]),
                 "+v"(cl[2][2]), "+v"(cl[2][3]), "+v"(cl[3][0]), "+v"(cl[3][1]), "+v"(cl[3][2]), "+v"(cl[3][3])
               :: "memory");
  const LAS bf16x8* wl = (const LAS bf16x8*)(smem + OFF_W) + lane;
  const f32x16 zero16 = {0.f, 0.f, 0.f, 0.f, 0.f, 0.f, 0.f, 0.f, 0.f, 0.f, 0.f, 0.f, 0.f, 0.f, 0.f, 0.f};
  f32x16 XA[2] = {zero16, zero16};
  bf16x8 wa[8];
#pragma unroll
  for (int k = 0; k < 8; ++k) wa[k] = wl[(NBLOB + k) * 64];
  SB();
  auto xa_step = [&](int k) {
#pragma unroll
    for (int ct = 0; ct < 2; ++ct) XA[ct] = MFMA32(__builtin_bit_cast(bf16x8, xg[k]), wa[k * 2 + ct], XA[ct]);
    SB();
  };
  if (__builtin_expect(cnt_all > LCAP, 0)) {
#pragma unroll
    for (int j = 1; j < 4; ++j)
#pragma unroll
      for (int q = 0; q < 4; ++q) mk[j][q] = 0ull;
  }
  fill(1, tot0 + (s123 & 0x3ff) - ni[1]);
  SB();
  xa_step(0);
  fill(2, tot1 + ((s123 >> 10) & 0x3ff) - ni[2]);
  SB();
  xa_step(1);
  fill(3, tot2 + (s123 >> 20) - ni[3]);
  SB();
  xa_step(2);
  xa_step(3);
  if (__builtin_expect(__ballot(nr[0] + nr[1] + nr[2] + nr[3] != ni[0] + ni[1] + ni[2] + ni[3]) != 0ull, 0)) {
#pragma unroll
    for (int j = 0; j < 4; ++j) {
      unsigned long long om = __ballot(nr[j] != ni[j]);
      while (om) {
        const int l = __builtin_ctzll(om);
        om &= om - 1;
        const int n = __builtin_amdgcn_readlane(nr[j] - ni[j], l);
        const int p = __builtin_amdgcn_readlane(st[j] + ni[j], l) + lane;
        const int cc = 8 * (l & 31) + (l >> 5) + 2 * j;
        if (n > CCAP) cnt_all = LCAP;
        if (cnt_all + n <= LCAP && lane < n) {
          const unsigned* cp = reinterpret_cast<const unsigned*>(reinterpret_cast<const char*>(ENT) +
                                                                 ((size_t)bk * NCH + cc) * CELL);
          put(cnt_all + lane, p < CIN ? cp[2 + p] : cp[E2OFF / 4 + p - CIN], (unsigned)(cc * chunk));
        }
        cnt_all += n;
      }
    }
    __builtin_amdgcn_s_waitcnt(0x0F70);
  }
  const int gbin = bk * 8 + wave;
  auto fill_slow = [&](int wb0) __attribute__((always_inline)) {
    int nn[4], sp[4], tot = 0;
    const unsigned* rp[4];
    int l2;
    asm volatile("v_mbcnt_lo_u32_b32 %0, -1, 0\n\tv_mbcnt_hi_u32_b32 %0, -1, %0" : "=v"(l2));
    const int ch0 = 8 * (l2 & 31) + (l2 >> 5);
#pragma unroll
    for (int j = 0; j < 4; ++j) {
      const unsigned* cp = reinterpret_cast<const unsigned*>(reinterpret_cast<const char*>(ENT) +
                                                             ((size_t)bk * NCH + ch0 + 2 * j) * CELL);
      const unsigned h0 = cp[0], h1 = cp[1];
      nn[j] = (int)(((gb >= 4 ? h1 : h0) >> (8 * (gb & 3))) & 0xffu);
      sp[j] = (int)dot4(h0, mlo, dot4(h1, mhi, 0u));
      rp[j] = cp + 2 + sp[j];
      sp[j] = CIN - sp[j];
      if (nn[j] == 0xff) {
        rp[j] = TS + cp[10 + gb];
        nn[j] = (int)cp[18 + gb];
        sp[j] = nn[j];
      }
      tot += nn[j];
    }
    const int sc = wave_scan(tot);
    int pp = sc - tot - wb0;
#pragma unroll
    for (int j = 0; j < 4; ++j) {
      const int cb = (ch0 + 2 * j) * chunk;
      const int te = min(nn[j], LCAP - pp);
#pragma unroll 1
      for (int t = max(0, -pp); t < te; ++t) put(pp + t, rp[j][t < sp[j] ? t : t + (int)(E2OFF / 4) - 32], (unsigned)cb);
      pp += nn[j];
    }
    __builtin_amdgcn_s_waitcnt(0x0F70);
    return __builtin_amdgcn_readlane(sc, 63);
  };
  if (__builtin_expect(cnt_all > LCAP, 0)) {
    asm volatile("s_waitcnt vmcnt(0)" ::: "memory");
    cnt_all = fill_slow(0);
    early = false;
  }
  cnt_all = __builtin_amdgcn_readfirstlane(cnt_all);
  int cnt = min(cnt_all, LCAP);
  auto convert = [&](int cnt, unsigned aR, unsigned aF, int lane) __attribute__((always_inline)) {
    u32x2 v[8];
#pragma unroll
    for (int k = 0; k < 8; ++k) v[k] = *(const LAS u32x2*)(size_t)(aR + k * 512);
    asm volatile("" ::: "memory");
#pragma unroll
    for (int k = 0; k < 6; k += 2) {
      unsigned a, e, c, f;
      decode2(v[k], v[k + 1], a, e, c, f);
      *(LAS unsigned*)(size_t)(aF + k * 256) = a;
      *(LAS unsigned*)(size_t)(aF + k * 256 + 256) = c;
      *(LAS unsigned*)(size_t)(aF + k * 256 + LARR) = e;
      *(LAS unsigned*)(size_t)(aF + k * 256 + LARR + 256) = f;
    }
#pragma unroll
    for (int k = 6; k < 8; ++k) {
      if (cnt > k * 64) {
        unsigned a, e;
        decode(v[k][0], v[k][1], a, e);
        *(LAS unsigned*)(size_t)(aF + k * 256) = a;
        *(LAS unsigned*)(size_t)(aF + k * 256 + LARR) = e;
      }
    }
    const int u = cnt + lane;
    const unsigned padA = (unsigned)(n0 + max(min(((u >> 2) & 7) + 8 * (u & 3), nodes - 1), 0)) | (31u * TPITCH << 16);
    const unsigned pa = aF + cnt * 4;
    *(LAS unsigned*)(size_t)pa = padA;
    *(LAS unsigned*)(size_t)(pa + 256) = padA;
    *(LAS unsigned*)(size_t)(pa + LARR) = 0u;
    *(LAS unsigned*)(size_t)(pa + LARR + 256) = 0u;
  };
  convert(cnt, aR, aF, lane);
  issue_gather(1, 1);
  if (__builtin_expect(!early, 0)) {
    asm volatile("s_waitcnt vmcnt(0)" ::: "memory");
    issue_gather(0, 0);
    issue_gather(1, 1);
  }
  bf16x8 XAf[2][2];
#pragma unroll
  for (int ct = 0; ct < 2; ++ct)
#pragma unroll
    for (int s = 0; s < 2; ++s) {
      u32x4 p;
#pragma unroll
      for (int j = 0; j < 4; ++j) p[j] = pack_bf16(XA[ct][8 * s + 2 * j], XA[ct][8 * s + 2 * j + 1]);
      XAf[s][ct] = __builtin_bit_cast(bf16x8, p);
    }

  auto rd128 = [&](unsigned a) { return *(const LAS bf16x8*)(size_t)a; };
  auto load_S = [&](const u32x4 ss) {
    const s16x4 a0 = __builtin_amdgcn_ds_read_tr16_b64_v4i16((LAS s16x4*)(size_t)((ss[0] >> 16) + cS));
    const s16x4 a1 = __builtin_amdgcn_ds_read_tr16_b64_v4i16((LAS s16x4*)(size_t)((ss[1] >> 16) + cS));
    const s16x4 a2 = __builtin_amdgcn_ds_read_tr16_b64_v4i16((LAS s16x4*)(size_t)((ss[2] >> 16) + cS));
    const s16x4 a3 = __builtin_amdgcn_ds_read_tr16_b64_v4i16((LAS s16x4*)(size_t)((ss[3] >> 16) + cS));
    S[0] = __builtin_shufflevector(a0, a1, 0, 1, 2, 3, 4, 5, 6, 7);
    S[1] = __builtin_shufflevector(a2, a3, 0, 1, 2, 3, 4, 5, 6, 7);
  };
  auto widx = [&](int m, int ct) { return m == 6 ? 16 + ct : m < 11 ? (m - 7) * 2 + ct : 8 + (m - 11) * 2 + ct; };

  bf16x8 wr[2][15];
#pragma unroll
  for (int ct = 0; ct < 2; ++ct)
#pragma unroll
    for (int m = 6; m < 15; ++m)
      if ((HELD >> m) & 1) {
        wr[ct][m] = wl[widx(m, ct) * 64];
        asm volatile("" : "+v"(wr[ct][m]));
      }
  auto wf = [&](int m, int ct) { return ((HELD >> m) & 1) ? wr[ct][m] : b[(m - 6) % 3]; };

  auto ew = [&](int m, const f32x16& Gc, const f32x16& Vc, const f32x16& Gn, const f32x16& Vp) {
    if (m < 2) {
      const int i = 14 + m;
      pq[i] = mulf(i, Vp[i], rq[i]);
      if (m == 1) pn[1][3] = pack_bf16(pq[14], pq[15]);
    } else if (m == 2) {
      pq[0] = mulf(0, Vc[0], rq[0]);
      pq[1] = mulf(1, Vc[1], rq[1]);
      pn[0][0] = pack_bf16(pq[0], pq[1]);
    } else {
      const int i = m - 1;
      pq[i] = mulf(i, Vc[i], rq[i]);
      if (i & 1) pn[i >> 3][(i >> 1) & 3] = pack_bf16(pq[i - 1], pq[i]);
    }
    if (m == 0) pd[0][0] = pack_bf16(rq[0], rq[1]);
    else if ((m & 1) == 0) { const int i = m + 1; pd[i >> 3][(i >> 1) & 3] = pack_bf16(rq[i - 1], rq[i]); }
    if (m < 14) {
      const int i = m + 2;
      rq[i] = __builtin_amdgcn_rcpf(add1(tq[i]));
    } else {
      rq[0] = __builtin_amdgcn_rcpf(add1(tq[0]));
      rq[1] = __builtin_amdgcn_rcpf(add1(tq[1]));
    }
    if (m < 13) tq[m + 3] = __builtin_amdgcn_exp2f(Gc[m + 3]);
    else if (m == 13) { tq[0] = __builtin_amdgcn_exp2f(Gn[0]); tq[1] = __builtin_amdgcn_exp2f(Gn[1]); }
    else tq[2] = __builtin_amdgcn_exp2f(Gn[2]);
  };

  long long tm1 = 0, c1 = 0;
  const long long tmB = __builtin_amdgcn_s_memrealtime();
  int wb0 = 0;
  while (true) {
    const int ntiles = (cnt + 31) >> 5;
    asm volatile("s_waitcnt vmcnt(5)" ::: "memory");
    {
      const unsigned sl = *(const LAS unsigned short*)(size_t)aLS1;
#pragma unroll
      for (int k = 0; k < 4; ++k) as[k] = rd128(fo[k]);
      ae = rd128(foe);
      St[0] = rd128(sl + cSt);
      St[1] = rd128(sl + cSt + 16);
      G0 = MFMA32(St[0], XAf[0][0], zero16);
      G0 = MFMA32(St[1], XAf[1][0], G0);
      G0 = MFMA32(ae, wl[widx(6, 0) * 64], G0);
#pragma unroll
      for (int k = 0; k < 4; ++k) G0 = MFMA32(as[k], wl[widx(7 + k, 0) * 64], G0);
      V0 = MFMA32(as[0], wl[widx(11, 0) * 64], zero16);
#pragma unroll
      for (int k = 1; k < 4; ++k) V0 = MFMA32(as[k], wl[widx(11 + k, 0) * 64], V0);
      asm volatile("s_waitcnt lgkmcnt(0)" ::: "memory");
      issue_gather(2, 0);
      pipe_zero();
      tq[0] = __builtin_amdgcn_exp2f(G0[0]);
      tq[1] = __builtin_amdgcn_exp2f(G0[1]);
      tq[2] = __builtin_amdgcn_exp2f(G0[2]);
      rq[0] = __builtin_amdgcn_rcpf(add1(tq[0]));
      rq[1] = __builtin_amdgcn_rcpf(add1(tq[1]));
      SB();
    }
    tm1 = __builtin_amdgcn_s_memrealtime(); c1 = __builtin_amdgcn_s_memtime(); __builtin_amdgcn_s_waitcnt(0xC07F);
    unsigned &lS4 = aLS4, &lS1 = aLS1, &lX = aLX, &lE = aLE;
    int adv = 0;
    auto body = [&](const int par) {
      const unsigned lo = par * 128;
      const unsigned ro = (1 - par) * ringd;
      {
        u32x4 ss;
        unsigned sl;
#pragma unroll
        for (int m = 0; m < 15; ++m) {
          if (m == 0) Y[1] = MFMA32(__builtin_bit_cast(bf16x8, pd[0]), S[0], Y[1]);
          else if (m == 1) Y[3] = MFMA32(__builtin_bit_cast(bf16x8, pn[0]), S[0], Y[3]);
          else if (m == 2) Y[1] = MFMA32(__builtin_bit_cast(bf16x8, pd[1]), S[1], Y[1]);
          else if (m == 3) Y[3] = MFMA32(__builtin_bit_cast(bf16x8, pn[1]), S[1], Y[3]);
          else if (m == 4) G1 = MFMA32(St[0], XAf[0][1], zero16);
          else if (m == 5) G1 = MFMA32(St[1], XAf[1][1], G1);
          else if (m == 6) G1 = MFMA32(ae, wf(m, 1), G1);
          else if (m < 11) G1 = MFMA32(as[m - 7], wf(m, 1), G1);
          else if (m == 11) V1 = MFMA32(as[0], wf(m, 1), zero16);
          else V1 = MFMA32(as[m - 11], wf(m, 1), V1);
          SB();
          if (m == 0) { ss = ld128(lS4 + lo); sl = *(const LAS unsigned short*)(size_t)(lS1 + lo + 128); }
          if (m >= 3 && m < 12 && !((HELD >> (m + 3)) & 1)) b[(m - 3) % 3] = wl[widx(m + 3, 1) * 64];
          if (m == 4) load_S(ss);
          if (m == 5) { St[0] = rd128(sl + cSt); St[1] = rd128(sl + cSt + 16); }
          if (m == 6) { asm volatile("s_waitcnt vmcnt(5)" ::: "memory"); ae = rd128(foe + ro); }
          if (m >= 11) as[m - 11] = rd128(fo[m - 11] + ro);
          ew(m, G0, V0, G1, V1);
          SB();
        }
      }
      {
        u32x4 sx;
        unsigned re;
#pragma unroll
        for (int m = 0; m < 15; ++m) {
          if (m == 0) Y[0] = MFMA32(__builtin_bit_cast(bf16x8, pd[0]), S[0], Y[0]);
          else if (m == 1) Y[2] = MFMA32(__builtin_bit_cast(bf16x8, pn[0]), S[0], Y[2]);
          else if (m == 2) Y[0] = MFMA32(__builtin_bit_cast(bf16x8, pd[1]), S[1], Y[0]);
          else if (m == 3) Y[2] = MFMA32(__builtin_bit_cast(bf16x8, pn[1]), S[1], Y[2]);
          else if (m == 4) G0 = MFMA32(St[0], XAf[0][0], zero16);
          else if (m == 5) G0 = MFMA32(St[1], XAf[1][0], G0);
          else if (m == 6) G0 = MFMA32(ae, wf(m, 0), G0);
          else if (m < 11) G0 = MFMA32(as[m - 7], wf(m, 0), G0);
          else if (m == 11) V0 = MFMA32(as[0], wf(m, 0), zero16);
          else V0 = MFMA32(as[m - 11], wf(m, 0), V0);
          SB();
          if (m == 4) { sx = ld128(lX + lo + 3 * 128); re = *(const LAS unsigned*)(size_t)(lE + lo + 3 * 128); }
          if (m >= 3 && m < 12 && !((HELD >> (m + 3)) & 1)) b[(m - 3) % 3] = wl[widx(m + 3, 0) * 64];
          if (m == 7) glds16x(x, sx[0], cx0, ring0 + ro);
          if (m == 8) glds16x(x, sx[1], cx1, ring0 + ro + 1024);
          if (m == 9) glds16x(x, sx[2], cx0, ring0 + ro + 2048);
          if (m == 10) glds16x(x, sx[3], cx1, ring0 + ro + 3072);
          if (m == 11) glds16e(ES, re, ce, ring0 + ro + 4096);
          ew(m, G1, V1, G0, V0);
          SB();
        }
      }
    };
    for (int t = 0; t < ntiles; t += 2) {
      asm volatile("s_cmp_lt_u32 %0, 4\n\ts_cbranch_scc1 1f\n\ts_setprio 1\n1:" :: "s"(wave) : "scc");
      body(0);
      if (t + 1 >= ntiles) break;
      __builtin_amdgcn_s_setprio(0);
      body(1);
      lS4 += 256; lS1 += 256; lX += 256; lE += 256;
      asm volatile("" : "+v"(lS4), "+v"(lS1), "+v"(lX), "+v"(lE));
      adv += 256;
    }
    pq[14] = mulf(0, V1[14], rq[14]);
    pq[15] = mulf(1, V1[15], rq[15]);
    pn[1][3] = pack_bf16(pq[14], pq[15]);
    Y[1] = MFMA32(__builtin_bit_cast(bf16x8, pd[0]), S[0], Y[1]);
    Y[3] = MFMA32(__builtin_bit_cast(bf16x8, pn[0]), S[0], Y[3]);
    Y[1] = MFMA32(__builtin_bit_cast(bf16x8, pd[1]), S[1], Y[1]);
    Y[3] = MFMA32(__builtin_bit_cast(bf16x8, pn[1]), S[1], Y[3]);
    wb0 += LCAP;
    if (__builtin_expect(wb0 >= cnt_all, 1)) break;
    asm volatile("s_waitcnt vmcnt(0)" ::: "memory");
    cnt = min(cnt_all - wb0, LCAP);
    lS4 -= adv; lS1 -= adv; lX -= adv; lE -= adv;
    fill_slow(wb0);
    {
      int l2;
      asm volatile("v_mbcnt_lo_u32_b32 %0, -1, 0\n\tv_mbcnt_hi_u32_b32 %0, -1, %0" : "=v"(l2));
      convert(cnt, la + LRAW + l2 * 8, la + l2 * 4, l2);
    }
    issue_gather(0, 0);
    issue_gather(1, 1);
  }
  const long long tm2 = __builtin_amdgcn_s_memrealtime(); const long long c2 = __builtin_amdgcn_s_memtime();

  if (nodes > 0) {
    int lane;
    asm volatile("v_mbcnt_lo_u32_b32 %0, -1, 0\n\tv_mbcnt_hi_u32_b32 %0, -1, %0" : "=v"(lane));
    const int r = lane & 31, h = lane >> 5;
#pragma unroll
    for (int ct = 0; ct < 2; ++ct) {
      f32x16 U = zero16;
#pragma unroll
      for (int k = 0; k < 4; ++k) U = MFMA32(wl[(18 + k * 2 + ct) * 64], as[k], U);
#pragma unroll
      for (int q = 0; q < 2; ++q) {
        const u32x4 xv = __builtin_bit_cast(u32x4, as[2 * ct + q]);
        u32x4 ov;
#pragma unroll
        for (int j = 0; j < 4; ++j) {
          const int i = 8 * q + 2 * j;
          const float z0 = U[i] + Y[2 + ct][i] * __builtin_amdgcn_rcpf(Y[ct][i] + 1e-6f);
          const float z1 = U[i + 1] + Y[2 + ct][i + 1] * __builtin_amdgcn_rcpf(Y[ct][i + 1] + 1e-6f);
          const float o0 = __uint_as_float(xv[j] << 16) + (z0 > 0.f ? z0 : 0.f);
          const float o1 = __uint_as_float(xv[j] & 0xffff0000u) + (z1 > 0.f ? z1 : 0.f);
          ov[j] = pack_bf16(o0, o1);
        }
        *(LAS u32x4*)(size_t)(la + r * 128 + (((4 * h + 2 * ct + q) ^ (r & 7)) * 16)) = ov;
      }
    }
#pragma unroll
    for (int q = 0; q < 4; ++q) {
      const int row = 8 * q + (lane >> 3);
      const u32x4 ov = *(const LAS u32x4*)(size_t)(la + row * 128 + (((lane & 7) ^ (row & 7)) * 16));
      if (row < nodes) gstore128wt(out, (unsigned)n0 * 128u + q * 1024u + lane * 16u, ov);
    }
  }
  asm volatile("s_waitcnt vmcnt(0)" ::: "memory");
  const long long tm3 = __builtin_amdgcn_s_memrealtime();
  if (__builtin_amdgcn_mbcnt_hi(-1, __builtin_amdgcn_mbcnt_lo(-1, 0)) == 0) { const int g = gbin; dbg[g*8+0]=tm0; dbg[g*8+1]=tm1; dbg[g*8+2]=tm2; dbg[g*8+3]=tm3; dbg[g*8+4]=(cnt_all+31)>>5; dbg[g*8+5]=c2-c1; dbg[g*8+6]=tmA-tm0; dbg[g*8+7]=tmB-tm0;
  }
}

__global__ void k_dbg(const long long* dbg, const unsigned long long* p0, int ng) {
  __shared__ long long sh[8][256];
  const int t = threadIdx.x;
  long long pmin = (long long)p0[t], pmax = pmin;
  long long s0 = 1ll << 62, s0m = 0, l1 = 0, e3 = 0, cyc = 0, rt = 0, l1max = 0, e3s = 0;
  for (int g = t; g < ng; g += 256) {
    const long long* d = dbg + g * 8;
    s0 = min(s0, d[0]); s0m = max(s0m, d[0]); l1 += d[1] - d[0]; l1max = max(l1max, d[1]); e3 = max(e3, d[3]); cyc += d[5]; rt += d[2] - d[1]; e3s += d[3] - d[0];
  }
  sh[0][t] = pmin; sh[1][t] = s0; sh[2][t] = l1; sh[3][t] = e3; sh[4][t] = cyc; sh[5][t] = rt; sh[6][t] = e3s; sh[7][t] = s0m;
  __syncthreads();
  if (t == 0) {
    for (int i = 1; i < 256; ++i) {
      pmin = min(pmin, sh[0][i]); pmax = max(pmax, sh[0][i]); s0 = min(s0, sh[1][i]); l1 += sh[2][i]; e3 = max(e3, sh[3][i]); cyc += sh[4][i]; rt += sh[5][i]; e3s += sh[6][i]; s0m = max(s0m, sh[7][i]);
    }
    long long q = 0;
    if (QSEL == 1) q = s0 - pmin;
    if (QSEL == 2) q = l1 / ng;
    if (QSEL == 3) q = e3 - pmin;
    if (QSEL == 4) q = cyc * 100 / rt;
    if (QSEL == 5) q = rt / ng;
    if (QSEL == 6) q = e3s / ng;
    if (QSEL == 7) q = pmax - pmin;
    const long long tend = pmin + 10000 + 4 * q;
    for (int i = 0; i < 1000000; ++i) { if (__builtin_amdgcn_s_memrealtime() >= tend) break; __builtin_amdgcn_s_sleep(1); }
  }
}

extern "C" void kernel_launch(void* const* d_in, const int* in_sizes, int n_in,
                              void* d_out, int out_size, void* d_ws, size_t ws_size,
                              hipStream_t stream) {
  const unsigned short* x = (const unsigned short*)d_in[0];
  const u32x4* eattr = (const u32x4*)d_in[1];
  const int* src = (const int*)d_in[2];
  const int* dst = (const int*)d_in[3];
  const unsigned short* wa = (const unsigned short*)d_in[4];
  const unsigned short* wb = (const unsigned short*)d_in[5];
  const unsigned short* wc = (const unsigned short*)d_in[6];
  const unsigned short* wu = (const unsigned short*)d_in[7];
  const unsigned short* wv = (const unsigned short*)d_in[8];
  unsigned short* out = (unsigned short*)d_out;
  const int N = in_sizes[0] / 64;
  const int E = in_sizes[2];
  const int chunk = (((E + NCH - 1) / NCH) + 3) & ~3;
  const int nbk = (N + 195) / 196;

  char* ws = (char*)d_ws;
  size_t off = 0;
  unsigned short* wpack = (unsigned short*)(ws + off);   off += 64 * 1024;
  u32x4* ENT = (u32x4*)(ws + off);                       off += (size_t)2 * NBK * NCH * CELL;
  unsigned* TS = (unsigned*)(ws + off);

  k_part<<<NCH + NFRAG, 1024, 0, stream>>>(src, dst, E, chunk, ENT, TS, wa, wb, wc, wu, wv, wpack);
  k_main<<<nbk, 512, 0, stream>>>(x, ENT, eattr, TS, chunk, (const bf16x8*)wpack, out, N
     , (long long*)(ws + (128u<<20))
     );
  k_dbg<<<1, 256, 0, stream>>>((const long long*)(ws + (128u<<20)), (const unsigned long long*)(wpack + 20000), 2040);
}
```

```cpp
#include <hip/hip_runtime.h>
#include <hip/hip_bf16.h>
#include <stdint.h>

using bf16x8 = __attribute__((ext_vector_type(8))) short;
using s16x4 = __attribute__((ext_vector_type(4))) short;
using f32x16 = __attribute__((ext_vector_type(16))) float;
using u32x4 = __attribute__((ext_vector_type(4))) unsigned;
using u32x2 = __attribute__((ext_vector_type(2))) unsigned;
using i32x4 = __attribute__((ext_vector_type(4))) int;
using u16x2 = __attribute__((ext_vector_type(2))) unsigned short;

#define MFMA32(a, b, c) __builtin_amdgcn_mfma_f32_32x32x16_bf16((a), (b), (c), 0, 0, 0)
#define LAS __attribute__((address_space(3)))
#define RANKOF(w) ((w) < 4 ? 7 - (w) : (w) - 4)
#define SLEEP "s_sleep 2\n\t"
#define NREP 8
#define REPOF(b) ((b) % NREP)
#define SB() __builtin_amdgcn_sched_barrier(0)

constexpr int NCH = 256;
constexpr int NBK = 256;
constexpr int NBIN = NBK * 8;
constexpr int NFRAG = 39;
constexpr int CHMAX = 3200;
constexpr float NLOG2E = -1.44269504088896340736f;
constexpr unsigned TPITCH = 80;

__device__ __forceinline__ unsigned pack_bf16(float lo, float hi) {
  typedef __attribute__((ext_vector_type(2))) float f2;
  typedef __attribute__((ext_vector_type(2))) __bf16 b2;
  f2 v = {lo, hi};
  b2 r = __builtin_convertvector(v, b2);
  return __builtin_bit_cast(unsigned, r);
}

__device__ __forceinline__ int wave_scan(int v) {
  v += __builtin_amdgcn_update_dpp(0, v, 0x111, 0xf, 0xf, false);
  v += __builtin_amdgcn_update_dpp(0, v, 0x112, 0xf, 0xf, false);
  v += __builtin_amdgcn_update_dpp(0, v, 0x114, 0xf, 0xf, false);
  v += __builtin_amdgcn_update_dpp(0, v, 0x118, 0xf, 0xf, false);
  v += __builtin_amdgcn_update_dpp(0, v, 0x142, 0xa, 0xf, false);
  v += __builtin_amdgcn_update_dpp(0, v, 0x143, 0xc, 0xf, false);
  return v;
}

__device__ __forceinline__ void glds16(const void* sbase, unsigned voff, unsigned lds_dst) {
  asm volatile(
      "s_mov_b32 m0, %2\n\t"
      "s_nop 0\n\t"
      "global_load_lds_dwordx4 %0, %1"
      :: "v"(voff), "s"(sbase), "s"(lds_dst) : "memory", "m0");
}
__device__ __forceinline__ void glds16nt(const void* sbase, unsigned voff, unsigned lds_dst) {
  asm volatile(
      "s_mov_b32 m0, %2\n\t"
      "s_nop 0\n\t"
      "global_load_lds_dwordx4 %0, %1 nt"
      :: "v"(voff), "s"(sbase), "s"(lds_dst) : "memory", "m0");
}
__device__ __forceinline__ void glds16x(const void* sbase, unsigned a, unsigned c, unsigned lds_dst) {
  unsigned tmp;
  asm volatile(
      "s_mov_b32 m0, %4\n\t"
      "v_mad_u32_u16 %0, %1, %5, %2\n\t"
      "global_load_lds_dwordx4 %0, %3"
      : "=&v"(tmp) : "v"(a), "v"(c), "s"(sbase), "s"(lds_dst), "s"(128u) : "memory", "m0");
}
__device__ __forceinline__ void glds16e(const void* sbase, unsigned a, unsigned c, unsigned lds_dst) {
  unsigned tmp;
  asm volatile(
      "s_mov_b32 m0, %4\n\t"
      "v_add_u32 %0, %1, %2\n\t"
      "global_load_lds_dwordx4 %0, %3"
      : "=&v"(tmp) : "v"(a), "v"(c), "s"(sbase), "s"(lds_dst) : "memory", "m0");
}
__device__ __forceinline__ int gload32(const void* sbase, unsigned voff) {
  int r;
  asm volatile("global_load_dword %0, %1, %2" : "=v"(r) : "v"(voff), "s"(sbase) : "memory");
  return r;
}
__device__ __forceinline__ u32x4 gload128(const void* sbase, unsigned voff) {
  u32x4 r;
  asm volatile("global_load_dwordx4 %0, %1, %2" : "=v"(r) : "v"(voff), "s"(sbase) : "memory");
  return r;
}

__device__ __forceinline__ u32x4 gload128nt(const void* sbase, unsigned voff) {
  u32x4 r;
  asm volatile("global_load_dwordx4 %0, %1, %2 nt" : "=v"(r) : "v"(voff), "s"(sbase) : "memory");
  return r;
}
__device__ __forceinline__ void gstore128wt(void* sbase, unsigned voff, u32x4 v) {
  asm volatile("global_store_dwordx4 %0, %1, %2 sc1\n\ts_nop 1" :: "v"(voff), "v"(v), "s"(sbase) : "memory");
}

constexpr int CELL = 128;
constexpr int CIN = 30;
constexpr int CCAP = 62;
constexpr int CPIT = 272;
constexpr unsigned E2OFF = NBK * NCH * CELL;
constexpr int KP_HDR = 0;
constexpr int KP_OVC = KP_HDR + NBK * 8;
constexpr int KP_ENT = KP_OVC + 16;
constexpr int KP_HIST = KP_ENT + NBK * CPIT;
constexpr int KP_LDS = KP_HIST + NBIN * 4;
constexpr int NWL = 26;
constexpr int NBLOB = 31;

__device__ __forceinline__ unsigned mad24(unsigned a, int b, unsigned c) {
  unsigned r;
  asm("v_mad_i32_i24 %0, %1, %2, %3" : "=v"(r) : "v"(a), "s"(b), "v"(c));
  return r;
}
__device__ __forceinline__ unsigned mul24(unsigned a, unsigned b) {
  unsigned r;
  asm("v_mul_u32_u24 %0, %1, %2" : "=v"(r) : "n"(b), "v"(a));
  return r;
}
__device__ __forceinline__ unsigned mulhi24(unsigned a, unsigned b) {
  return (unsigned)(((unsigned long long)(a & 0xffffffu) * (unsigned long long)b) >> 32);
}
__device__ __forceinline__ unsigned dot4(unsigned a, unsigned b, unsigned c) {
  return __builtin_amdgcn_udot4(a, b, c, false);
}

constexpr int RING_SLOT = 5 * 1024;
constexpr int SPIT = 1040;
constexpr int LCAP = 512;
constexpr int LARR = 2560;
constexpr int LIST_BYTES = 2 * LARR;
constexpr int LRAW = LIST_BYTES - (LCAP + 8) * 8;
constexpr int OFF_W = 0;
constexpr int OFF_T1 = NWL * 1024;
constexpr int OFF_T2 = OFF_T1 + 32 * TPITCH;
constexpr int OFF_WA = NBLOB * 1024;
constexpr int OFF_R0 = NFRAG * 1024;
constexpr int OFF_R1 = OFF_R0 + 8 * RING_SLOT;
constexpr int OFF_L = OFF_R1 + 8 * RING_SLOT;
constexpr int OFF_RDY = OFF_L + 8 * LIST_BYTES;
constexpr int MAIN_LDS = OFF_RDY + 16;
static_assert(MAIN_LDS <= 160 * 1024, "LDS");
static_assert(KP_LDS <= 16 * RING_SLOT, "phase 1 lives in the ring");

__device__ __forceinline__ unsigned ldd(const unsigned* p) {
  return __hip_atomic_load(p, __ATOMIC_RELAXED, __HIP_MEMORY_SCOPE_AGENT);
}
__device__ __forceinline__ float add1(float t) { return 1.0f + t; }
__device__ __forceinline__ float mulf(int i, float a, float b) { return (i & 1) ? __builtin_fmaf(a, b, 0.0f) : a * b; }

__global__ __launch_bounds__(512, 2) void k_main(const unsigned short* __restrict__ x,
                                                 const u32x4* __restrict__ ES,
                                                 const int* __restrict__ src, const int* __restrict__ dst,
                                                 const unsigned short* __restrict__ wa_g,
                                                 const unsigned short* __restrict__ wb_g,
                                                 const unsigned short* __restrict__ wc_g,
                                                 const unsigned short* __restrict__ wu_g,
                                                 const unsigned short* __restrict__ wv_g,
                                                 u32x4* ENT, unsigned* TS, unsigned long long* FL,
                                                 unsigned short* __restrict__ out, int N, int E, int chunk
                                                 ) {
#define PT(i)
  PT(0);
  __shared__ __attribute__((aligned(16))) char smem[MAIN_LDS];
  const int tid0 = threadIdx.x;
  const int wave = __builtin_amdgcn_readfirstlane(tid0 >> 6);
  const int bk = blockIdx.x;
  const unsigned lds0 = (unsigned)(size_t)(LAS char*)smem;

  LAS char* sm = (LAS char*)smem + OFF_R0;
  LAS unsigned* hist = (LAS unsigned*)(sm + KP_HIST);
  LAS unsigned* ovc = (LAS unsigned*)(sm + KP_OVC);
  const int base = bk * chunk;
  const int ecnt = max(min(chunk, E - base), 0);
  int i0[2];
  u32x4 d[2], sv[2];
  u32x2 tag;
#pragma unroll
  for (int s = 0; s < 2; ++s) {
    i0[s] = max(min(base + 4 * (tid0 + 512 * s), min(base + ecnt, E) - 4), 0);
    d[s] = gload128nt(dst, (unsigned)i0[s] * 4u);
  }
  if (tid0 <= NBK) *(LAS u32x2*)(sm + KP_HDR + tid0 * 8) = u32x2{0u, 0u};
#pragma unroll
  for (int s = 0; s < 2; ++s) sv[s] = gload128nt(src, (unsigned)i0[s] * 4u);
  if (tid0 == 511) *(LAS u32x2*)((LAS char*)smem + OFF_RDY) = u32x2{0u, 0u};
  int tid = tid0;
  asm volatile("s_waitcnt lgkmcnt(0)\n\ts_barrier" : "+v"(tid) :: "memory");
  const int lane = tid & 63, r = lane & 31, h = lane >> 5;
  asm volatile("global_load_dwordx2 %0, %1, %2 sc1" : "=v"(tag) : "v"(NCH * 8u), "s"(FL) : "memory");
  const unsigned f1 = lds0 + OFF_W + ((wave < 4 ? 0 : NBLOB) + (wave & 3) * 2) * 1024;
  const unsigned f2 = lds0 + OFF_W + ((wave < 4 ? 8 : 18) + (wave & 3) * 2) * 1024;
  const unsigned f3 = lds0 + OFF_W + 16 * 1024;
  {
    const unsigned short* w1 = wave < 4 ? wb_g : wa_g;
    const unsigned short* w2 = wave < 4 ? wv_g : wu_g;
    const unsigned vo = lane * 16 + (wave & 3) * 1024;
    glds16(w1, vo, f1);
    glds16(w1 + 2048, vo, f1 + 1024);
    glds16(w2, vo, f2);
    glds16(w2 + 2048, vo, f2 + 1024);
    if (wave == 7) {
      glds16(wc_g, lane * 16, f3);
      glds16(wc_g + 512, lane * 16, f3 + 1024);
    }
  }
  u32x4 xg[4];
  unsigned xo = (unsigned)min(bk * 196 + 49 * (wave >> 1) + 25 * (wave & 1) + min(r, 24 - (wave & 1)), N - 1) * 128u + 64u * h;
  asm volatile("" : "+v"(xo));
  int warm = gload32(x, xo);
  unsigned tup[8];
  unsigned long long on[8];
  const unsigned sm0 = lds0 + OFF_R0;
#pragma unroll
  for (int s = 0; s < 2; ++s) {
    const int lo = base + 4 * (tid + 512 * s) - i0[s], hi4 = base + ecnt - i0[s];
#pragma unroll
    for (int k = 0; k < 4; ++k) {
      on[4 * s + k] = __ballot(k >= lo && k < hi4);
      tup[4 * s + k] = (unsigned)(i0[s] + k - base);
    }
  }
  const int bq = tid >> 2, p0 = (tid & 3) * 16;
  unsigned ha = sm0 + KP_HDR + bq * 8, ia = sm0 + KP_ENT + bq * CPIT + p0;
  unsigned go = (unsigned)((bq * NCH + bk) * CELL + p0);
  asm volatile("" : "+v"(tup[0]), "+v"(tup[1]), "+v"(tup[2]), "+v"(tup[3]), "+v"(tup[4]), "+v"(tup[5]), "+v"(tup[6]),
                    "+v"(tup[7]), "+v"(ha), "+v"(ia), "+v"(go), "+s"(on[0]), "+s"(on[1]), "+s"(on[2]), "+s"(on[3]),
                    "+s"(on[4]), "+s"(on[5]), "+s"(on[6]), "+s"(on[7]));
  {
    asm volatile("s_waitcnt vmcnt(8)" : "+v"(d[0]), "+v"(d[1]) :: "memory");
    PT(1);
    unsigned gi[8], rk[8], m0[8], m1[8], cb[8], sh[8], oh[8];
    auto count = [&](int s) {
      unsigned *g = gi + 4 * s, *rr = rk + 4 * s, *o = oh + 4 * s;
#pragma unroll
      for (int k = 0; k < 4; ++k) {
        g[k] = mulhi24(d[s][k] << 8, 684785u);
        sh[4 * s + k] = g[k] << 3;
        o[k] = 1u << (sh[4 * s + k] & 31u);
        rr[k] = 0u;
      }
      asm volatile(
          "s_mov_b64 exec, %12\n\t"
          "ds_add_rtn_u32 %0, %4, %8\n\t"
          "s_mov_b64 exec, %13\n\t"
          "ds_add_rtn_u32 %1, %5, %9\n\t"
          "s_mov_b64 exec, %14\n\t"
          "ds_add_rtn_u32 %2, %6, %10\n\t"
          "s_mov_b64 exec, %15\n\t"
          "ds_add_rtn_u32 %3, %7, %11\n\t"
          "s_mov_b64 exec, -1"
          : "+&v"(rr[0]), "+&v"(rr[1]), "+&v"(rr[2]), "+&v"(rr[3])
          : "v"((g[0] & ~3u) + sm0), "v"((g[1] & ~3u) + sm0), "v"((g[2] & ~3u) + sm0), "v"((g[3] & ~3u) + sm0),
            "v"(o[0]), "v"(o[1]), "v"(o[2]), "v"(o[3]), "s"(on[4 * s]), "s"(on[4 * s + 1]), "s"(on[4 * s + 2]),
            "s"(on[4 * s + 3])
          : "memory");
    };
    auto masks = [&](int s) {
#pragma unroll
      for (int k = 0; k < 4; ++k) {
        const int i = 4 * s + k;
        const unsigned slot = mad24(gi[i], -49 * 128, d[s][k] << 8) >> 8;
        tup[i] = mad24(slot, 3200, tup[i]);
        const unsigned hm = (unsigned)((int)(gi[i] << 29) >> 31);
        m0[i] = ((oh[i] - 1u) | hm) & 0x04040404u;
        m1[i] = (oh[i] - 1u) & hm & 0x04040404u;
        gi[i] &= ~7u;
        cb[i] = mul24(gi[i], CPIT / 8) + sm0;
        gi[i] += sm0;
      }
      const int i = 4 * s;
      asm volatile("" : "+v"(m0[i]), "+v"(m0[i + 1]), "+v"(m0[i + 2]), "+v"(m0[i + 3]), "+v"(m1[i]), "+v"(m1[i + 1]),
                        "+v"(m1[i + 2]), "+v"(m1[i + 3]), "+v"(cb[i]), "+v"(cb[i + 1]), "+v"(cb[i + 2]), "+v"(cb[i + 3]),
                        "+v"(gi[i]), "+v"(gi[i + 1]), "+v"(gi[i + 2]), "+v"(gi[i + 3]), "+v"(tup[i]), "+v"(tup[i + 1]),
                        "+v"(tup[i + 2]), "+v"(tup[i + 3]));
    };
    auto ranks = [&](int s) {
      unsigned* rr = rk + 4 * s;
#pragma unroll
      for (int k = 0; k < 4; ++k) asm("v_bfe_u32 %0, %0, %1, 8" : "+v"(rr[k]) : "v"(sh[4 * s + k]));
      const unsigned mx = max(max(rr[0], rr[1]), max(rr[2], rr[3]));
#pragma unroll
      for (int k = 0; k < 4; ++k) {
        rr[k] = cb[4 * s + k] + rr[k] * 4;
        cb[4 * s + k] += CCAP * 4;
      }
      return mx;
    };
    auto place = [&](int s, const u32x2* hd) {
      unsigned pa[4];
#pragma unroll
      for (int k = 0; k < 4; ++k)
        pa[k] = min(dot4(hd[k][0], m0[4 * s + k], dot4(hd[k][1], m1[4 * s + k], rk[4 * s + k])), cb[4 * s + k]);
      asm volatile(
          "s_mov_b64 exec, %8\n\t"
          "ds_write_b32 %0, %4 offset:%12\n\t"
          "s_mov_b64 exec, %9\n\t"
          "ds_write_b32 %1, %5 offset:%12\n\t"
          "s_mov_b64 exec, %10\n\t"
          "ds_write_b32 %2, %6 offset:%12\n\t"
          "s_mov_b64 exec, %11\n\t"
          "ds_write_b32 %3, %7 offset:%12\n\t"
          "s_mov_b64 exec, -1"
          :: "v"(pa[0]), "v"(pa[1]), "v"(pa[2]), "v"(pa[3]), "v"(tup[4 * s]), "v"(tup[4 * s + 1]),
             "v"(tup[4 * s + 2]), "v"(tup[4 * s + 3]), "s"(on[4 * s]), "s"(on[4 * s + 1]), "s"(on[4 * s + 2]),
             "s"(on[4 * s + 3]), "n"(KP_ENT + 8)
          : "memory");
    };
    auto addsrc = [&](int s) {
      asm volatile(
          "s_waitcnt vmcnt(1)\n\t"
          "v_mad_i32_i24 %0, %4, %8, %0\n\t"
          "v_mad_i32_i24 %1, %5, %8, %1\n\t"
          "v_mad_i32_i24 %2, %6, %8, %2\n\t"
          "v_mad_i32_i24 %3, %7, %8, %3"
          : "+v"(tup[4 * s]), "+v"(tup[4 * s + 1]), "+v"(tup[4 * s + 2]), "+v"(tup[4 * s + 3])
          : "v"(sv[s][0]), "v"(sv[s][1]), "v"(sv[s][2]), "v"(sv[s][3]), "s"(81920)
          : "memory");
    };
    u32x2 hc[2];
    if ((wave * 64 + 512) * 4 < ecnt) {
      count(0);
      count(1);
      masks(0);
      masks(1);
      SB();
      asm volatile("s_waitcnt lgkmcnt(0)" : "+v"(rk[0]), "+v"(rk[1]), "+v"(rk[2]), "+v"(rk[3]), "+v"(rk[4]),
                   "+v"(rk[5]), "+v"(rk[6]), "+v"(rk[7]) :: "memory");
      if (max(ranks(0), ranks(1)) >= (unsigned)CCAP) ovc[1] = 1u;
      addsrc(0);
      addsrc(1);
      SB();
      asm volatile("s_barrier" ::: "memory");
      PT(2);
      u32x2 hd[8];
#pragma unroll
      for (int i = 0; i < 8; ++i) hd[i] = *(const LAS u32x2*)(size_t)gi[i];
#pragma unroll
      for (int s = 0; s < 2; ++s) hc[s] = *(const LAS u32x2*)(size_t)(ha + s * 1024);
      place(0, hd);
      place(1, hd + 4);
    } else {
      count(0);
      masks(0);
      SB();
      asm volatile("s_waitcnt lgkmcnt(0)" : "+v"(rk[0]), "+v"(rk[1]), "+v"(rk[2]), "+v"(rk[3]) :: "memory");
      if (ranks(0) >= (unsigned)CCAP) ovc[1] = 1u;
      addsrc(0);
      asm volatile("" :: "v"(sv[1]));
      SB();
      asm volatile("s_barrier" ::: "memory");
      PT(2);
      u32x2 hd[4];
#pragma unroll
      for (int i = 0; i < 4; ++i) hd[i] = *(const LAS u32x2*)(size_t)gi[i];
#pragma unroll
      for (int s = 0; s < 2; ++s) hc[s] = *(const LAS u32x2*)(size_t)(ha + s * 1024);
      place(0, hd);
    }
    unsigned used[2];
#pragma unroll
    for (int s = 0; s < 2; ++s) used[s] = dot4(hc[s][0], 0x01010101u, dot4(hc[s][1], 0x01010101u, 0u));
    if (max(used[0], used[1]) > (unsigned)CCAP) ovc[1] = 1u;
    asm volatile("s_waitcnt lgkmcnt(0)\n\ts_barrier" : "+v"(tag) :: "memory");
    PT(3); PT(4);
    const unsigned ovf = ovc[1];
    u32x4 v0[2], v1[2];
#pragma unroll
    for (int s = 0; s < 2; ++s) {
      v0[s] = *(const LAS u32x4*)(size_t)(ia + s * 128 * CPIT);
      v1[s] = *(const LAS u32x4*)(size_t)(ia + s * 128 * CPIT + 64);
    }
    asm volatile("" : "+v"(v0[0]), "+v"(v1[0]), "+v"(v0[1]), "+v"(v1[1]));
    if (__builtin_expect(__builtin_amdgcn_readfirstlane(ovf) != 0u, 0)) {
      *(LAS u32x4*)(hist + tid * 4) = u32x4{0u, 0u, 0u, 0u};
      asm volatile("s_waitcnt lgkmcnt(0)\n\ts_barrier" ::: "memory");
#pragma unroll
      for (int i = 0; i < 8; ++i) {
        gi[i] = mulhi24(d[i >> 2][i & 3] << 8, 684785u);
        if ((on[i] >> lane) & 1ull)
          rk[i] = __hip_atomic_fetch_add(hist + gi[i], 1u, __ATOMIC_RELAXED, __HIP_MEMORY_SCOPE_WORKGROUP);
      }
      asm volatile("s_waitcnt lgkmcnt(0)\n\ts_barrier" ::: "memory");
      if (tid < NBK) {
        unsigned tot = 0;
        for (int i = 0; i < 8; ++i) tot += hist[tid * 8 + i];
        LAS unsigned* cp = (LAS unsigned*)(sm + KP_ENT + tid * CPIT);
        unsigned p = ~0u;
        if (tot > (unsigned)CCAP) {
          p = (unsigned)base + __hip_atomic_fetch_add(ovc, tot, __ATOMIC_RELAXED, __HIP_MEMORY_SCOPE_WORKGROUP);
          *(LAS u32x2*)(sm + KP_HDR + tid * 8) = u32x2{~0u, ~0u};
        }
        for (int i = 0; i < 8; ++i) {
          const unsigned n = hist[tid * 8 + i];
          hist[tid * 8 + i] = p;
          if (p != ~0u) {
            cp[2 + i] = 0u;
            cp[10 + i] = p;
            cp[18 + i] = n;
            p += n;
          }
        }
      }
      asm volatile("s_waitcnt lgkmcnt(0)\n\ts_barrier" ::: "memory");
#pragma unroll
      for (int i = 0; i < 8; ++i) {
        if ((on[i] >> lane) & 1ull) {
          const unsigned p = hist[gi[i]];
          if (p != ~0u) __hip_atomic_store(TS + p + rk[i], tup[i], __ATOMIC_RELAXED, __HIP_MEMORY_SCOPE_AGENT);
        }
      }
#pragma unroll
      for (int s = 0; s < 2; ++s) {
        hc[s] = *(const LAS u32x2*)(size_t)(ha + s * 1024);
        used[s] = dot4(hc[s][0], 0x01010101u, dot4(hc[s][1], 0x01010101u, 0u));
        v0[s] = *(const LAS u32x4*)(size_t)(ia + s * 128 * CPIT);
        v1[s] = *(const LAS u32x4*)(size_t)(ia + s * 128 * CPIT + 64);
      }
    }
#pragma unroll
    for (int s = 0; s < 2; ++s) {
      if (p0 == 0) { v0[s][0] = hc[s][0]; v0[s][1] = hc[s][1]; }
      gstore128wt(ENT, go + s * (128 * NCH * CELL), v0[s]);
    }
#pragma unroll
    for (int s = 0; s < 2; ++s) {
      const unsigned g2 = go + s * (128 * NCH * CELL);
      if (used[s] > 14u) gstore128wt(ENT, g2 + 64, v1[s]);
      if (__builtin_expect(used[s] > (unsigned)CIN, 0)) {
        gstore128wt(ENT, g2 + E2OFF, *(const LAS u32x4*)(size_t)(ia + s * 128 * CPIT + 128));
        gstore128wt(ENT, g2 + E2OFF + 64, *(const LAS u32x4*)(size_t)(ia + s * 128 * CPIT + 192));
      }
    }
    PT(5);
    const unsigned to = (unsigned)(h * 1024 + ((lane >> 2) & 3) * 128 + (lane & 1) * 64 + ((lane >> 4) & 1) * 16 +
                                   ((lane >> 1) & 1) * 8);
    auto prep = [&](unsigned f, bool scale) {
      u32x4 w[2];
#pragma unroll
      for (int ct = 0; ct < 2; ++ct) {
        const s16x4 a0 = __builtin_amdgcn_ds_read_tr16_b64_v4i16((LAS s16x4*)(size_t)(f + to + ct * 32));
        const s16x4 a1 = __builtin_amdgcn_ds_read_tr16_b64_v4i16((LAS s16x4*)(size_t)(f + to + ct * 32 + 512));
        w[ct] = __builtin_bit_cast(u32x4, __builtin_shufflevector(a0, a1, 0, 1, 2, 3, 4, 5, 6, 7));
      }
      asm volatile("s_waitcnt lgkmcnt(0)" : "+v"(w[0]), "+v"(w[1]) :: "memory");
#pragma unroll
      for (int ct = 0; ct < 2; ++ct) {
        if (scale) {
#pragma unroll
          for (int j = 0; j < 4; ++j)
            w[ct][j] = pack_bf16(__uint_as_float(w[ct][j] << 16) * NLOG2E, __uint_as_float(w[ct][j] & 0xffff0000u) * NLOG2E);
        }
        *(LAS u32x4*)(size_t)(f + ct * 1024 + lane * 16) = w[ct];
      }
    };
    {
      const unsigned long long t64 = (((unsigned long long)tag[1] << 32) | tag[0]) + 0x9E3779B97F4A7C15ull;
      tag = u32x2{(unsigned)t64, (unsigned)(t64 >> 32)};
    }
    prep(f1, true);
    prep(f2, false);
    if (wave == 7) prep(f3, true);
    asm volatile("s_waitcnt vmcnt(0) lgkmcnt(0)\n\ts_barrier" : "+v"(tag), "+v"(warm) :: "memory");
    if (tid < NREP) asm volatile("global_store_dwordx2 %0, %1, %2 sc1\n\ts_nop 1" :: "v"(4096u + tid * 2048u + bk * 8u), "v"(tag), "s"(FL) : "memory");
    PT(6);
#pragma unroll
    for (int t = 0; t < 4; ++t) xg[t] = gload128(x, xo + 16 * t);
    {
      unsigned long long m1, m2;
      int spin = 1 << 22;
      asm volatile("s_cmp_lg_u32 %7, 7\n\t"
                   "s_cbranch_scc1 3f\n\t"
                   "s_setprio 2\n"
                   "1:\n\t"
                   "global_load_dwordx4 v[240:243], %3, %4 sc1\n\t"
                   "global_load_dwordx4 v[244:247], %3, %4 offset:16 sc1\n\t"
                   "s_sub_u32 %2, %2, 1\n\t"
                   "s_cselect_b64 %1, -1, 0\n\t"
                   "s_waitcnt vmcnt(0)\n\t"
                   "v_cmp_eq_u64 vcc, v[240:241], %5\n\t"
                   "v_cmp_eq_u64 %0, v[242:243], %5\n\t"
                   "s_and_b64 vcc, vcc, %0\n\t"
                   "v_cmp_eq_u64 %0, v[244:245], %5\n\t"
                   "s_and_b64 vcc, vcc, %0\n\t"
                   "v_cmp_eq_u64 %0, v[246:247], %5\n\t"
                   "s_and_b64 vcc, vcc, %0\n\t"
                   "s_or_b64 %0, vcc, %1\n\t"
                   "s_lshr_b64 vcc, %0, 1\n\t"
                   "s_and_b64 vcc, vcc, %0\n\t"
                   "s_mov_b64 exec, 1\n\t"
                   "v_mov_b32 v240, vcc_lo\n\t"
                   "v_mov_b32 v241, vcc_hi\n\t"
                   "ds_write_b64 %6, v[240:241]\n\t"
                   "s_mov_b64 exec, -1\n\t"
                   "s_cmp_lg_u64 %0, -1\n\t"
                   "s_cbranch_scc1 1b\n"
                   "3:"
                   : "=&s"(m1), "=&s"(m2), "+&s"(spin)
                   : "v"((unsigned)(4096 + REPOF(bk) * 2048 + lane * 32)), "s"(FL), "v"(tag), "v"(lds0 + OFF_RDY), "s"(wave)
                   : "memory", "vcc", "scc", "v240", "v241", "v242", "v243", "v244", "v245", "v246", "v247");
    }
  }
  const LAS bf16x8* wl = (const LAS bf16x8*)(smem + OFF_W) + lane;
  const f32x16 zero16 = {0.f, 0.f, 0.f, 0.f, 0.f, 0.f, 0.f, 0.f, 0.f, 0.f, 0.f, 0.f, 0.f, 0.f, 0.f, 0.f};
  auto widx = [&](int m, int ct) { return m == 6 ? 16 + ct : m < 11 ? (m - 7) * 2 + ct : 8 + (m - 11) * 2 + ct; };
#define HELD ((1 << 6) | (1 << 8) | (1 << 10) | (1 << 12) | (1 << 14))
  bf16x8 wr[2][15];
  {
  if (tid >= 192) {
    const unsigned p = tid - 192, tb = p >= 160u, p2 = p - tb * 160u;
    const unsigned row = (p2 * 205u) >> 10, seg = p2 - row * 5u;
    const unsigned col = tb ? ((row >> 1) & 12u) | ((row << 2) & 16u) | (row & 3u) : row;
    const unsigned one = (col >> 3) == seg ? 0x3F80u << ((col & 1u) * 16u) : 0u;
    const unsigned dw = (col >> 1) & 3u;
    *(LAS u32x4*)((LAS char*)smem + OFF_T1 + p * 16) =
        u32x4{dw == 0u ? one : 0u, dw == 1u ? one : 0u, dw == 2u ? one : 0u, dw == 3u ? one : 0u};
  }
#pragma unroll
    for (int ct = 0; ct < 2; ++ct)
#pragma unroll
      for (int m = 6; m < 15; ++m)
        if ((HELD >> m) & 1) {
          wr[ct][m] = wl[widx(m, ct) * 64];
          asm volatile("" : "+v"(wr[ct][m]));
        }
    asm volatile("s_waitcnt vmcnt(0)" : "+v"(xg[0]), "+v"(xg[1]), "+v"(xg[2]), "+v"(xg[3]) :: "memory");
    f32x16 XA[2] = {zero16, zero16};
#pragma unroll
    for (int k = 0; k < 4; ++k)
#pragma unroll
      for (int ct = 0; ct < 2; ++ct)
        XA[ct] = MFMA32(__builtin_bit_cast(bf16x8, xg[k]), wl[(NBLOB + k * 2 + ct) * 64], XA[ct]);
#pragma unroll
    for (int ct = 0; ct < 2; ++ct)
#pragma unroll
      for (int s = 0; s < 2; ++s) {
        u32x4 p;
#pragma unroll
        for (int j = 0; j < 4; ++j) p[j] = pack_bf16(XA[ct][8 * s + 2 * j], XA[ct][8 * s + 2 * j + 1]);
        *(LAS u32x4*)((LAS char*)smem + OFF_R0 + wave * RING_SLOT + (s * 2 + ct) * 1024 + lane * 16) = p;
      }
  }
  u32x4 cv[5];
  const int pc0 = wave < 4 ? 5 * wave : 4 * wave + 4;
  const unsigned pskip = wave < 4 ? 0u : wave < 7 ? 0x100u : 0x155u;
  {
    unsigned done = pskip, t;
    const char* eb = reinterpret_cast<const char*>(ENT) + (size_t)bk * (NCH * CELL) + pc0 * 1024 + 2048;
    asm volatile("s_branch 2f\n"
                 "1:\n\t"
                 "s_sleep 1\n\t"
                 "ds_read_b64 v[240:241], %9\n\t"
                 "s_waitcnt lgkmcnt(0)\n\t"
                 "v_readfirstlane_b32 vcc_lo, v240\n\t"
                 "v_readfirstlane_b32 vcc_hi, v241\n\t"
                 "s_lshr_b64 vcc, vcc, %10\n\t"
                 "s_andn2_b32 %1, vcc_lo, %0\n\t"
                 "s_and_b32 %1, %1, 0x155\n\t"
                 "s_cbranch_scc0 1b\n\t"
                 "s_or_b32 %0, %0, %1\n\t"
                 "s_bitcmp1_b32 %1, 0\n\t"
                 "s_cbranch_scc0 3f\n\t"
                 "global_load_dwordx4 %2, %7, %8 offset:-2048 sc1\n"
                 "3:\n\t"
                 "s_bitcmp1_b32 %1, 2\n\t"
                 "s_cbranch_scc0 4f\n\t"
                 "global_load_dwordx4 %3, %7, %8 offset:-1024 sc1\n"
                 "4:\n\t"
                 "s_bitcmp1_b32 %1, 4\n\t"
                 "s_cbranch_scc0 5f\n\t"
                 "global_load_dwordx4 %4, %7, %8 sc1\n"
                 "5:\n\t"
                 "s_bitcmp1_b32 %1, 6\n\t"
                 "s_cbranch_scc0 6f\n\t"
                 "global_load_dwordx4 %5, %7, %8 offset:1024 sc1\n"
                 "6:\n\t"
                 "s_bitcmp1_b32 %1, 8\n\t"
                 "s_cbranch_scc0 2f\n\t"
                 "global_load_dwordx4 %6, %7, %8 offset:2048 sc1\n"
                 "2:\n\t"
                 "s_cmp_lg_u32 %0, 0x155\n\t"
                 "s_cbranch_scc1 1b"
                 : "+&s"(done), "=&s"(t), "=&v"(cv[0]), "=&v"(cv[1]), "=&v"(cv[2]), "=&v"(cv[3]), "=&v"(cv[4])
                 : "v"(lane * 16u), "s"(eb), "v"(lds0 + OFF_RDY), "s"(2 * pc0)
                 : "memory", "vcc", "scc", "v240", "v241");
  }
  LAS char* lbase = (LAS char*)smem + OFF_L + wave * LIST_BYTES;
  const unsigned la = (unsigned)(size_t)lbase;
  auto put = [&](int p, unsigned a, unsigned b) { *(LAS u32x2*)(lbase + LRAW + p * 8) = u32x2{a, b}; };
  unsigned aLX = la + (lane >> 3) * 16;
  unsigned aLE = la + LARR + ((((lane >> 1) & 7) << 2) | (lane >> 4)) * 4;
  unsigned aLS4 = la + (4 * h + ((lane >> 2) & 3)) * 16;
  unsigned aLS1 = la + (((r & 7) << 2) | (r >> 3)) * 4 + 2;
  unsigned cx0 = (unsigned)(((lane & 7) ^ (lane >> 4)) * 16);
  unsigned cx1 = cx0 ^ 64u;
  unsigned ce = (unsigned)((((lane & 1) ^ ((lane >> 4) & 1))) * 16);
  const unsigned ringd = 8 * RING_SLOT;
  unsigned cS = lds0 + OFF_T1 + (((lane >> 4) & 1) * 16 + (lane & 3) * 4) * 2;
  unsigned cSt = lds0 + OFF_T2 + h * 32;
  unsigned fo[4];
#pragma unroll
  for (int t = 0; t < 4; ++t) fo[t] = lds0 + OFF_R0 + (unsigned)(r * 128 + (((4 * h + t) ^ ((r >> 1) & 7)) * 16));
  unsigned foe = lds0 + OFF_R0 + (unsigned)(4096 + r * 32 + ((h ^ ((r >> 3) & 1)) * 16));
  unsigned sa = lds0 + OFF_R1 + r * SPIT + h * CELL;
  unsigned cb0 = (unsigned)((8 * r + h) * chunk);
  unsigned aR = la + LRAW + lane * 8, aF = la + lane * 4;
  unsigned aRX = la + LRAW + (lane >> 3) * 32, aRE = la + LRAW + ((((lane >> 1) & 7) << 2) | (lane >> 4)) * 8;
  f32x16 Y[4];
#pragma unroll
  for (int q = 0; q < 4; ++q)
#pragma unroll
    for (int i = 0; i < 16; ++i) Y[q][i] = 0.f;
  f32x16 G0, V0, G1, V1;
  bf16x8 S[2], St[2], as[4], ae, b[3];
  u32x4 pd[2], pn[2];
  float tq[16], rq[16], pq[16];
  auto pipe_zero = [&]() {
#pragma unroll
    for (int j = 0; j < 8; ++j) { S[0][j] = 0; S[1][j] = 0; }
#pragma unroll
    for (int j = 0; j < 4; ++j) { pd[0][j] = 0; pd[1][j] = 0; pn[0][j] = 0; pn[1][j] = 0; }
#pragma unroll
    for (int i = 0; i < 16; ++i) V1[i] = 0.f;
    rq[14] = 0.f; rq[15] = 0.f;
  };
  asm volatile("" : "+v"(aLX), "+v"(aLE), "+v"(aLS4), "+v"(aLS1), "+v"(cx0), "+v"(cx1), "+v"(ce), "+v"(cS), "+v"(cSt),
                    "+v"(fo[0]), "+v"(fo[1]), "+v"(fo[2]), "+v"(fo[3]), "+v"(foe), "+v"(sa), "+v"(cb0), "+v"(aR),
                    "+v"(aF), "+v"(aRX), "+v"(aRE));
  asm volatile("" : "+v"(Y[0]), "+v"(Y[1]), "+v"(Y[2]), "+v"(Y[3]));
  unsigned rsh = (lane & 3) * 8, rid = lane & 7;
  unsigned long long rhi = 0xf0f0f0f0f0f0f0f0ull;
  asm volatile("" : "+v"(rsh), "+v"(rid), "+s"(rhi));

  {
    asm volatile("s_cmp_eq_u32 %7, 0x155\n\t"
                 "s_cbranch_scc1 1f\n\t"
                 "s_waitcnt vmcnt(0)\n\t"
                 "ds_write_b128 %5, %0\n\t"
                 "ds_write_b128 %5, %1 offset:%6\n\t"
                 "ds_write_b128 %5, %2 offset:2*%6\n\t"
                 "ds_write_b128 %5, %3 offset:3*%6\n\t"
                 "s_cmp_lg_u32 %7, 0\n\t"
                 "s_cbranch_scc1 1f\n\t"
                 "ds_write_b128 %5, %4 offset:4*%6\n"
                 "1:\n\t"
                 "s_waitcnt lgkmcnt(0)\n\t"
                 "s_barrier"
                 :: "v"(cv[0]), "v"(cv[1]), "v"(cv[2]), "v"(cv[3]), "v"(cv[4]),
                    "v"(lds0 + OFF_R1 + pc0 * SPIT + lane * 16), "n"(SPIT), "s"(pskip)
                 : "memory", "scc");
    if (bk == 0 && tid == 0)
      asm volatile("global_store_dwordx2 %0, %1, %2 sc1\n\ts_nop 1" :: "v"(NCH * 8u), "v"(tag), "s"(FL) : "memory");
    if (wave >= 4) __builtin_amdgcn_s_setprio(1);
  }
  u32x2 cl[4][4], hd[4];
  int nr[4], st[4], ni[4];
  unsigned long long mk[4][4];
#pragma unroll
  for (int j = 0; j < 4; ++j) hd[j] = *(const LAS u32x2*)(size_t)(sa + 2 * j * CELL);
  int gb;
  {
    int lo = (int)(hd[0][0] + hd[1][0] + hd[2][0] + hd[3][0]), hi = (int)(hd[0][1] + hd[1][1] + hd[2][1] + hd[3][1]);
    lo += __builtin_amdgcn_update_dpp(0, lo, 0xb1, 0xf, 0xf, false);
    hi += __builtin_amdgcn_update_dpp(0, hi, 0xb1, 0xf, 0xf, false);
    lo += __builtin_amdgcn_update_dpp(0, lo, 0x4e, 0xf, 0xf, false);
    hi += __builtin_amdgcn_update_dpp(0, hi, 0x4e, 0xf, 0xf, false);
    lo += __builtin_amdgcn_update_dpp(0, lo, 0x141, 0xf, 0xf, false);
    hi += __builtin_amdgcn_update_dpp(0, hi, 0x141, 0xf, 0xf, false);
    lo += __builtin_amdgcn_update_dpp(0, lo, 0x140, 0xf, 0xf, false);
    hi += __builtin_amdgcn_update_dpp(0, hi, 0x140, 0xf, 0xf, false);
    unsigned c;
    asm("v_cndmask_b32 %0, %1, %2, %3\n\tv_bfe_u32 %0, %0, %4, 8" : "=&v"(c) : "v"(lo), "v"(hi), "s"(rhi), "v"(rsh));
    auto p16 = __builtin_amdgcn_permlane16_swap(c, c, false, false);
    c = p16[0] + p16[1];
    auto p32 = __builtin_amdgcn_permlane32_swap(c, c, false, false);
    const int key = (int)(((p32[0] + p32[1]) << 3) | rid);
    unsigned acc = 0u;
#define RK(k) acc = __builtin_amdgcn_alignbit(acc, (unsigned)(__builtin_amdgcn_update_dpp(0, key, 0x120 + k, 0xf, 0xf, false) - key), 31)
    RK(1); RK(2); RK(3); RK(4); RK(5); RK(6); RK(7);
#undef RK
    const int want = RANKOF(wave);
    gb = __builtin_ctzll(__ballot(__builtin_popcount(acc) == want)) & 7;
  }
  const int nb0 = bk * 196 + 49 * (gb >> 1) + 25 * (gb & 1);
  const int nodes = max(min(25 - (gb & 1), N - nb0), 0);
  const int n0 = min(nb0, N - 1);
  const unsigned mlo = gb >= 4 ? 0x01010101u : 0x01010101u & ((1u << (8 * gb)) - 1u);
  const unsigned mhi = gb <= 4 ? 0u : 0x01010101u & ((1u << (8 * (gb - 4))) - 1u);
  const unsigned ring0 = lds0 + OFF_R0 + gb * RING_SLOT;
  bf16x8 XAf[2][2];
#pragma unroll
  for (int q = 0; q < 4; ++q) XAf[q >> 1][q & 1] = *(const LAS bf16x8*)(size_t)(ring0 + q * 1024 + lane * 16);
#pragma unroll
  for (int t = 0; t < 4; ++t) fo[t] += gb * RING_SLOT;
  foe += gb * RING_SLOT;
  auto head = [&](int j) {
    nr[j] = (int)(((gb >= 4 ? hd[j][1] : hd[j][0]) >> (8 * (gb & 3))) & 0xffu);
    st[j] = (int)dot4(hd[j][0], mlo, dot4(hd[j][1], mhi, 0u));
    const int n = min(min(nr[j], 8), CIN - st[j]);
#pragma unroll
    for (int q = 0; q < 4; ++q) mk[j][q] = __ballot(n > 2 * q);
    ni[j] = max(n, 0);
  };
  auto fetch = [&](int j) {
    asm volatile(
        "s_mov_b64 exec, %5\n\t"
        "ds_read2_b32 %0, %4 offset0:%9 offset1:%9+1\n\t"
        "s_mov_b64 exec, %6\n\t"
        "ds_read2_b32 %1, %4 offset0:%9+2 offset1:%9+3\n\t"
        "s_mov_b64 exec, %7\n\t"
        "ds_read2_b32 %2, %4 offset0:%9+4 offset1:%9+5\n\t"
        "s_mov_b64 exec, %8\n\t"
        "ds_read2_b32 %3, %4 offset0:%9+6 offset1:%9+7\n\t"
        "s_mov_b64 exec, -1"
        : "=&v"(cl[j][0]), "=&v"(cl[j][1]), "=&v"(cl[j][2]), "=&v"(cl[j][3])
        : "v"(sa + st[j] * 4), "s"(mk[j][0]), "s"(mk[j][1]), "s"(mk[j][2]), "s"(mk[j][3]), "n"(j * 64 + 2)
        : "memory");
  };
  auto fill = [&](int j, int p) {
    asm volatile(
        "s_mov_b64 exec, %13\n\t"
        "ds_write2_b32 %0, %9, %1 offset0:14 offset1:15\n\t"
        "ds_write2_b32 %0, %8, %1 offset0:12 offset1:13\n\t"
        "s_mov_b64 exec, %12\n\t"
        "ds_write2_b32 %0, %7, %1 offset0:10 offset1:11\n\t"
        "ds_write2_b32 %0, %6, %1 offset0:8 offset1:9\n\t"
        "s_mov_b64 exec, %11\n\t"
        "ds_write2_b32 %0, %5, %1 offset0:6 offset1:7\n\t"
        "ds_write2_b32 %0, %4, %1 offset0:4 offset1:5\n\t"
        "s_mov_b64 exec, %10\n\t"
        "ds_write2_b32 %0, %3, %1 offset0:2 offset1:3\n\t"
        "ds_write2_b32 %0, %2, %1 offset0:0 offset1:1\n\t"
        "s_mov_b64 exec, -1"
        :: "v"(la + LRAW + p * 8), "v"(cb0 + 2 * j * chunk), "v"(cl[j][0][0]), "v"(cl[j][0][1]), "v"(cl[j][1][0]),
           "v"(cl[j][1][1]), "v"(cl[j][2][0]), "v"(cl[j][2][1]), "v"(cl[j][3][0]), "v"(cl[j][3][1]), "s"(mk[j][0]),
           "s"(mk[j][1]), "s"(mk[j][2]), "s"(mk[j][3])
        : "memory");
  };
  auto src_of = [&](unsigned v) { return mulhi24(v >> 8, 13421773u); };
  auto decode = [&](unsigned v, unsigned cb, unsigned& a, unsigned& e) {
    unsigned sl;
    asm("v_lshrrev_b32 %0, 8, %3\n\t"
        "v_mul_hi_u32_u24 %0, 0xcccccd, %0\n\t"
        "v_mad_i32_i24 %1, %0, %5, %3\n\t"
        "v_mul_hi_u32_u24 %2, 0x147ae2, %1\n\t"
        "v_add_lshl_u32 %1, %1, %4, 5\n\t"
        "v_mad_i32_i24 %0, %2, %6, %0\n\t"
        "v_mad_i32_i24 %1, %2, %7, %1"
        : "=&v"(a), "=&v"(e), "=&v"(sl)
        : "v"(v), "v"(cb), "s"(-81920), "s"((int)(TPITCH << 16)), "s"(-102400));
  };
  auto decode2 = [&](const u32x2 p, const u32x2 q, unsigned& a, unsigned& e, unsigned& c, unsigned& f) {
    unsigned sl, sm;
    asm("v_lshrrev_b32 %0, 8, %6\n\t"
        "v_lshrrev_b32 %3, 8, %8\n\t"
        "v_mul_hi_u32_u24 %0, 0xcccccd, %0\n\t"
        "v_mul_hi_u32_u24 %3, 0xcccccd, %3\n\t"
        "v_mad_i32_i24 %1, %0, %10, %6\n\t"
        "v_mad_i32_i24 %4, %3, %10, %8\n\t"
        "v_mul_hi_u32_u24 %2, 0x147ae2, %1\n\t"
        "v_mul_hi_u32_u24 %5, 0x147ae2, %4\n\t"
        "v_add_lshl_u32 %1, %1, %7, 5\n\t"
        "v_add_lshl_u32 %4, %4, %9, 5\n\t"
        "v_mad_i32_i24 %0, %2, %11, %0\n\t"
        "v_mad_i32_i24 %3, %5, %11, %3\n\t"
        "v_mad_i32_i24 %1, %2, %12, %1\n\t"
        "v_mad_i32_i24 %4, %5, %12, %4"
        : "=&v"(a), "=&v"(e), "=&v"(sl), "=&v"(c), "=&v"(f), "=&v"(sm)
        : "v"(p[0]), "v"(p[1]), "v"(q[0]), "v"(q[1]), "s"(-81920), "s"((int)(TPITCH << 16)), "s"(-102400));
  };
  auto ld128 = [&](unsigned a) { return *(const LAS u32x4*)(size_t)a; };
  auto gather5 = [&](const u32x4 sx, unsigned re, int rs) {
    const unsigned dstb = ring0 + rs * ringd;
    glds16x(x, sx[0], cx0, dstb);
    glds16x(x, sx[1], cx1, dstb + 1024);
    glds16x(x, sx[2], cx0, dstb + 2048);
    glds16x(x, sx[3], cx1, dstb + 3072);
    glds16e(ES, re, ce, dstb + 4096);
  };
  auto issue_gather = [&](int tile, int rs) {
    gather5(ld128(aLX + tile * 128), *(const LAS unsigned*)(size_t)(aLE + tile * 128), rs);
  };
  head(0);
  fetch(0);
  SB();
  const int s0 = wave_scan(ni[0]);
  head(1);
  head(2);
  head(3);
  const int tot0 = __builtin_amdgcn_readlane(s0, 63);
  const int pz = s0 - ni[0];
  SB();
  asm volatile("s_waitcnt lgkmcnt(0)" : "+v"(cl[0][0]), "+v"(cl[0][1]), "+v"(cl[0][2]), "+v"(cl[0][3]),
               "+v"(XAf[0][0]), "+v"(XAf[0][1]), "+v"(XAf[1][0]), "+v"(XAf[1][1]) :: "memory");
  fill(0, pz);
  bool early = tot0 >= 32;
  int s123;
  {
    unsigned v0 = *(const LAS unsigned*)(size_t)aRX, v1 = *(const LAS unsigned*)(size_t)(aRX + 8);
    unsigned v2 = *(const LAS unsigned*)(size_t)(aRX + 16), v3 = *(const LAS unsigned*)(size_t)(aRX + 24);
    u32x2 ve = *(const LAS u32x2*)(size_t)aRE;
    SB();
    s123 = wave_scan(ni[1] | (ni[2] << 10) | (ni[3] << 20));
    SB();
    asm volatile("" : "+v"(v0), "+v"(v1), "+v"(v2), "+v"(v3), "+v"(ve), "+v"(s123));
    if (__builtin_expect(early, 1)) {
      unsigned a, e;
      decode(ve[0], ve[1], a, e);
      gather5(u32x4{src_of(v0), src_of(v1), src_of(v2), src_of(v3)}, e, 0);
    } else {
      gather5(u32x4{0u, 0u, 0u, 0u}, 0u, 0);
    }
  }
  fetch(1);
  fetch(2);
  fetch(3);
  const int e123 = __builtin_amdgcn_readlane(s123, 63);
  const int tot1 = tot0 + (e123 & 0x3ff), tot2 = tot1 + ((e123 >> 10) & 0x3ff);
  int cnt_all = tot2 + (e123 >> 20);
  asm volatile("s_waitcnt lgkmcnt(0)\n\ts_barrier"
               : "+v"(cl[1][0]), "+v"(cl[1][1]), "+v"(cl[1][2]), "+v"(cl[1][3]), "+v"(cl[2][0]), "+v"(cl[2][1]),
                 "+v"(cl[2][2]), "+v"(cl[2][3]), "+v"(cl[3][0]), "+v"(cl[3][1]), "+v"(cl[3][2]), "+v"(cl[3][3])
               :: "memory");
  if (__builtin_expect(cnt_all > LCAP, 0)) {
#pragma unroll
    for (int j = 1; j < 4; ++j)
#pragma unroll
      for (int q = 0; q < 4; ++q) mk[j][q] = 0ull;
  }
  fill(1, tot0 + (s123 & 0x3ff) - ni[1]);
  fill(2, tot1 + ((s123 >> 10) & 0x3ff) - ni[2]);
  fill(3, tot2 + (s123 >> 20) - ni[3]);
  if (__builtin_expect(__ballot(nr[0] + nr[1] + nr[2] + nr[3] != ni[0] + ni[1] + ni[2] + ni[3]) != 0ull, 0)) {
#pragma unroll
    for (int j = 0; j < 4; ++j) {
      unsigned long long om = __ballot(nr[j] != ni[j]);
      while (om) {
        const int l = __builtin_ctzll(om);
        om &= om - 1;
        const int n = __builtin_amdgcn_readlane(nr[j] - ni[j], l);
        const int p = __builtin_amdgcn_readlane(st[j] + ni[j], l) + lane;
        const int cc = 8 * (l & 31) + (l >> 5) + 2 * j;
        if (n > CCAP) cnt_all = LCAP;
        if (cnt_all + n <= LCAP && lane < n) {
          const unsigned* cp = reinterpret_cast<const unsigned*>(reinterpret_cast<const char*>(ENT) +
                                                                 ((size_t)bk * NCH + cc) * CELL);
          put(cnt_all + lane, ldd(p < CIN ? cp + 2 + p : cp + E2OFF / 4 + p - CIN), (unsigned)(cc * chunk));
        }
        cnt_all += n;
      }
    }
    __builtin_amdgcn_s_waitcnt(0x0F70);
  }
  const int gbin = bk * 8 + wave;
  auto fill_slow = [&](int wb0) __attribute__((always_inline)) {
    int nn[4], sp[4], tot = 0;
    const unsigned* rp[4];
    int l2;
    asm volatile("v_mbcnt_lo_u32_b32 %0, -1, 0\n\tv_mbcnt_hi_u32_b32 %0, -1, %0" : "=v"(l2));
    const int ch0 = 8 * (l2 & 31) + (l2 >> 5);
#pragma unroll
    for (int j = 0; j < 4; ++j) {
      const unsigned* cp = reinterpret_cast<const unsigned*>(reinterpret_cast<const char*>(ENT) +
                                                             ((size_t)bk * NCH + ch0 + 2 * j) * CELL);
      const unsigned h0 = ldd(cp), h1 = ldd(cp + 1);
      nn[j] = (int)(((gb >= 4 ? h1 : h0) >> (8 * (gb & 3))) & 0xffu);
      sp[j] = (int)dot4(h0, mlo, dot4(h1, mhi, 0u));
      rp[j] = cp + 2 + sp[j];
      sp[j] = CIN - sp[j];
      if (nn[j] == 0xff) {
        rp[j] = TS + ldd(cp + 10 + gb);
        nn[j] = (int)ldd(cp + 18 + gb);
        sp[j] = nn[j];
      }
      tot += nn[j];
    }
    const int sc = wave_scan(tot);
    int pp = sc - tot - wb0;
#pragma unroll
    for (int j = 0; j < 4; ++j) {
      const int cb = (ch0 + 2 * j) * chunk;
      const int te = min(nn[j], LCAP - pp);
#pragma unroll 1
      for (int t = max(0, -pp); t < te; ++t) put(pp + t, ldd(rp[j] + (t < sp[j] ? t : t + (int)(E2OFF / 4) - 32)), (unsigned)cb);
      pp += nn[j];
    }
    __builtin_amdgcn_s_waitcnt(0x0F70);
    return __builtin_amdgcn_readlane(sc, 63);
  };
  if (__builtin_expect(cnt_all > LCAP, 0)) {
    asm volatile("s_waitcnt vmcnt(0)" ::: "memory");
    cnt_all = fill_slow(0);
    early = false;
  }
  cnt_all = __builtin_amdgcn_readfirstlane(cnt_all);
  int cnt = min(cnt_all, LCAP);
  auto convert = [&](int cnt, unsigned aR, unsigned aF, int lane) __attribute__((always_inline)) {
    u32x2 v[8];
#pragma unroll
    for (int k = 0; k < 8; ++k) v[k] = *(const LAS u32x2*)(size_t)(aR + k * 512);
    asm volatile("" ::: "memory");
#pragma unroll
    for (int k = 0; k < 6; k += 2) {
      unsigned a, e, c, f;
      decode2(v[k], v[k + 1], a, e, c, f);
      *(LAS unsigned*)(size_t)(aF + k * 256) = a;
      *(LAS unsigned*)(size_t)(aF + k * 256 + 256) = c;
      *(LAS unsigned*)(size_t)(aF + k * 256 + LARR) = e;
      *(LAS unsigned*)(size_t)(aF + k * 256 + LARR + 256) = f;
    }
#pragma unroll
    for (int k = 6; k < 8; ++k) {
      if (cnt > k * 64) {
        unsigned a, e;
        decode(v[k][0], v[k][1], a, e);
        *(LAS unsigned*)(size_t)(aF + k * 256) = a;
        *(LAS unsigned*)(size_t)(aF + k * 256 + LARR) = e;
      }
    }
    const int u = cnt + lane;
    const unsigned padA = (unsigned)(n0 + max(min(((u >> 2) & 7) + 8 * (u & 3), nodes - 1), 0)) | (31u * TPITCH << 16);
    const unsigned pa = aF + cnt * 4;
    *(LAS unsigned*)(size_t)pa = padA;
    *(LAS unsigned*)(size_t)(pa + 256) = padA;
    *(LAS unsigned*)(size_t)(pa + LARR) = 0u;
    *(LAS unsigned*)(size_t)(pa + LARR + 256) = 0u;
  };
  convert(cnt, aR, aF, lane);
  issue_gather(1, 1);
  if (__builtin_expect(!early, 0)) {
    asm volatile("s_waitcnt vmcnt(0)" ::: "memory");
    issue_gather(0, 0);
    issue_gather(1, 1);
  }
  auto rd128 = [&](unsigned a) { return *(const LAS bf16x8*)(size_t)a; };
  auto load_S = [&](const u32x4 ss) {
    const s16x4 a0 = __builtin_amdgcn_ds_read_tr16_b64_v4i16((LAS s16x4*)(size_t)((ss[0] >> 16) + cS));
    const s16x4 a1 = __builtin_amdgcn_ds_read_tr16_b64_v4i16((LAS s16x4*)(size_t)((ss[1] >> 16) + cS));
    const s16x4 a2 = __builtin_amdgcn_ds_read_tr16_b64_v4i16((LAS s16x4*)(size_t)((ss[2] >> 16) + cS));
    const s16x4 a3 = __builtin_amdgcn_ds_read_tr16_b64_v4i16((LAS s16x4*)(size_t)((ss[3] >> 16) + cS));
    S[0] = __builtin_shufflevector(a0, a1, 0, 1, 2, 3, 4, 5, 6, 7);
    S[1] = __builtin_shufflevector(a2, a3, 0, 1, 2, 3, 4, 5, 6, 7);
  };
  auto wf = [&](int m, int ct) { return ((HELD >> m) & 1) ? wr[ct][m] : b[(m - 6) % 3]; };

  auto ew = [&](int m, const f32x16& Gc, const f32x16& Vc, const f32x16& Gn, const f32x16& Vp) {
    if (m < 2) {
      const int i = 14 + m;
      pq[i] = mulf(i, Vp[i], rq[i]);
      if (m == 1) pn[1][3] = pack_bf16(pq[14], pq[15]);
    } else if (m == 2) {
      pq[0] = mulf(0, Vc[0], rq[0]);
      pq[1] = mulf(1, Vc[1], rq[1]);
      pn[0][0] = pack_bf16(pq[0], pq[1]);
    } else {
      const int i = m - 1;
      pq[i] = mulf(i, Vc[i], rq[i]);
      if (i & 1) pn[i >> 3][(i >> 1) & 3] = pack_bf16(pq[i - 1], pq[i]);
    }
    if (m == 0) pd[0][0] = pack_bf16(rq[0], rq[1]);
    else if ((m & 1) == 0) { const int i = m + 1; pd[i >> 3][(i >> 1) & 3] = pack_bf16(rq[i - 1], rq[i]); }
    if (m < 14) {
      const int i = m + 2;
      rq[i] = __builtin_amdgcn_rcpf(add1(tq[i]));
    } else {
      rq[0] = __builtin_amdgcn_rcpf(add1(tq[0]));
      rq[1] = __builtin_amdgcn_rcpf(add1(tq[1]));
    }
    if (m < 13) tq[m + 3] = __builtin_amdgcn_exp2f(Gc[m + 3]);
    else if (m == 13) { tq[0] = __builtin_amdgcn_exp2f(Gn[0]); tq[1] = __builtin_amdgcn_exp2f(Gn[1]); }
    else tq[2] = __builtin_amdgcn_exp2f(Gn[2]);
  };

  int wb0 = 0;
  while (true) {
    const int ntiles = (cnt + 31) >> 5;
    asm volatile("s_waitcnt vmcnt(5)" ::: "memory");
    {
      const unsigned sl = *(const LAS unsigned short*)(size_t)aLS1;
#pragma unroll
      for (int k = 0; k < 4; ++k) as[k] = rd128(fo[k]);
      ae = rd128(foe);
      St[0] = rd128(sl + cSt);
      St[1] = rd128(sl + cSt + 16);
      G0 = MFMA32(St[0], XAf[0][0], zero16);
      G0 = MFMA32(St[1], XAf[1][0], G0);
      G0 = MFMA32(ae, wl[widx(6, 0) * 64], G0);
#pragma unroll
      for (int k = 0; k < 4; ++k) G0 = MFMA32(as[k], wl[widx(7 + k, 0) * 64], G0);
      V0 = MFMA32(as[0], wl[widx(11, 0) * 64], zero16);
#pragma unroll
      for (int k = 1; k < 4; ++k) V0 = MFMA32(as[k], wl[widx(11 + k, 0) * 64], V0);
      asm volatile("s_waitcnt lgkmcnt(0)" ::: "memory");
      issue_gather(2, 0);
      pipe_zero();
      tq[0] = __builtin_amdgcn_exp2f(G0[0]);
      tq[1] = __builtin_amdgcn_exp2f(G0[1]);
      tq[2] = __builtin_amdgcn_exp2f(G0[2]);
      rq[0] = __builtin_amdgcn_rcpf(add1(tq[0]));
      rq[1] = __builtin_amdgcn_rcpf(add1(tq[1]));
      SB();
    }
    unsigned &lS4 = aLS4, &lS1 = aLS1, &lX = aLX, &lE = aLE;
    int adv = 0;
    auto body = [&](const int par) {
      const unsigned lo = par * 128;
      const unsigned ro = (1 - par) * ringd;
      {
        u32x4 ss;
        unsigned sl;
#pragma unroll
        for (int m = 0; m < 15; ++m) {
          if (m == 0) Y[1] = MFMA32(__builtin_bit_cast(bf16x8, pd[0]), S[0], Y[1]);
          else if (m == 1) Y[3] = MFMA32(__builtin_bit_cast(bf16x8, pn[0]), S[0], Y[3]);
          else if (m == 2) Y[1] = MFMA32(__builtin_bit_cast(bf16x8, pd[1]), S[1], Y[1]);
          else if (m == 3) Y[3] = MFMA32(__builtin_bit_cast(bf16x8, pn[1]), S[1], Y[3]);
          else if (m == 4) G1 = MFMA32(St[0], XAf[0][1], zero16);
          else if (m == 5) G1 = MFMA32(St[1], XAf[1][1], G1);
          else if (m == 6) G1 = MFMA32(ae, wf(m, 1), G1);
          else if (m < 11) G1 = MFMA32(as[m - 7], wf(m, 1), G1);
          else if (m == 11) V1 = MFMA32(as[0], wf(m, 1), zero16);
          else V1 = MFMA32(as[m - 11], wf(m, 1), V1);
          SB();
          if (m == 0) { ss = ld128(lS4 + lo); sl = *(const LAS unsigned short*)(size_t)(lS1 + lo + 128); }
          if (m >= 3 && m < 12 && !((HELD >> (m + 3)) & 1)) b[(m - 3) % 3] = wl[widx(m + 3, 1) * 64];
          if (m == 4) load_S(ss);
          if (m == 5) { St[0] = rd128(sl + cSt); St[1] = rd128(sl + cSt + 16); }
          if (m == 6) { asm volatile("s_waitcnt vmcnt(5)" ::: "memory"); ae = rd128(foe + ro); }
          if (m >= 11) as[m - 11] = rd128(fo[m - 11] + ro);
          ew(m, G0, V0, G1, V1);
          SB();
        }
      }
      {
        u32x4 sx;
        unsigned re;
#pragma unroll
        for (int m = 0; m < 15; ++m) {
          if (m == 0) Y[0] = MFMA32(__builtin_bit_cast(bf16x8, pd[0]), S[0], Y[0]);
          else if (m == 1) Y[2] = MFMA32(__builtin_bit_cast(bf16x8, pn[0]), S[0], Y[2]);
          else if (m == 2) Y[0] = MFMA32(__builtin_bit_cast(bf16x8, pd[1]), S[1], Y[0]);
          else if (m == 3) Y[2] = MFMA32(__builtin_bit_cast(bf16x8, pn[1]), S[1], Y[2]);
          else if (m == 4) G0 = MFMA32(St[0], XAf[0][0], zero16);
          else if (m == 5) G0 = MFMA32(St[1], XAf[1][0], G0);
          else if (m == 6) G0 = MFMA32(ae, wf(m, 0), G0);
          else if (m < 11) G0 = MFMA32(as[m - 7], wf(m, 0), G0);
          else if (m == 11) V0 = MFMA32(as[0], wf(m, 0), zero16);
          else V0 = MFMA32(as[m - 11], wf(m, 0), V0);
          SB();
          if (m == 4) { sx = ld128(lX + lo + 3 * 128); re = *(const LAS unsigned*)(size_t)(lE + lo + 3 * 128); }
          if (m >= 3 && m < 12 && !((HELD >> (m + 3)) & 1)) b[(m - 3) % 3] = wl[widx(m + 3, 0) * 64];
          if (m == 7) glds16x(x, sx[0], cx0, ring0 + ro);
          if (m == 8) glds16x(x, sx[1], cx1, ring0 + ro + 1024);
          if (m == 9) glds16x(x, sx[2], cx0, ring0 + ro + 2048);
          if (m == 10) glds16x(x, sx[3], cx1, ring0 + ro + 3072);
          if (m == 11) glds16e(ES, re, ce, ring0 + ro + 4096);
          ew(m, G1, V1, G0, V0);
          SB();
        }
      }
    };
    for (int t = 0; t < ntiles; t += 2) {
      asm volatile("s_cmp_lt_u32 %0, 4\n\ts_cbranch_scc1 1f\n\ts_setprio 1\n1:" :: "s"(wave) : "scc");
      body(0);
      if (t + 1 >= ntiles) break;
      __builtin_amdgcn_s_setprio(0);
      body(1);
      lS4 += 256; lS1 += 256; lX += 256; lE += 256;
      asm volatile("" : "+v"(lS4), "+v"(lS1), "+v"(lX), "+v"(lE));
      adv += 256;
    }
    pq[14] = mulf(0, V1[14], rq[14]);
    pq[15] = mulf(1, V1[15], rq[15]);
    pn[1][3] = pack_bf16(pq[14], pq[15]);
    Y[1] = MFMA32(__builtin_bit_cast(bf16x8, pd[0]), S[0], Y[1]);
    Y[3] = MFMA32(__builtin_bit_cast(bf16x8, pn[0]), S[0], Y[3]);
    Y[1] = MFMA32(__builtin_bit_cast(bf16x8, pd[1]), S[1], Y[1]);
    Y[3] = MFMA32(__builtin_bit_cast(bf16x8, pn[1]), S[1], Y[3]);
    wb0 += LCAP;
    if (__builtin_expect(wb0 >= cnt_all, 1)) break;
    asm volatile("s_waitcnt vmcnt(0)" ::: "memory");
    cnt = min(cnt_all - wb0, LCAP);
    lS4 -= adv; lS1 -= adv; lX -= adv; lE -= adv;
    fill_slow(wb0);
    {
      int l2;
      asm volatile("v_mbcnt_lo_u32_b32 %0, -1, 0\n\tv_mbcnt_hi_u32_b32 %0, -1, %0" : "=v"(l2));
      convert(cnt, la + LRAW + l2 * 8, la + l2 * 4, l2);
    }
    issue_gather(0, 0);
    issue_gather(1, 1);
  }

  if (nodes > 0) {
    int lane;
    asm volatile("v_mbcnt_lo_u32_b32 %0, -1, 0\n\tv_mbcnt_hi_u32_b32 %0, -1, %0" : "=v"(lane));
    const int r = lane & 31, h = lane >> 5;
#pragma unroll
    for (int ct = 0; ct < 2; ++ct) {
      f32x16 U = zero16;
#pragma unroll
      for (int k = 0; k < 4; ++k) U = MFMA32(wl[(18 + k * 2 + ct) * 64], as[k], U);
#pragma unroll
      for (int q = 0; q < 2; ++q) {
        const u32x4 xv = __builtin_bit_cast(u32x4, as[2 * ct + q]);
        u32x4 ov;
#pragma unroll
        for (int j = 0; j < 4; ++j) {
          const int i = 8 * q + 2 * j;
          const float z0 = U[i] + Y[2 + ct][i] * __builtin_amdgcn_rcpf(Y[ct][i] + 1e-6f);
          const float z1 = U[i + 1] + Y[2 + ct][i + 1] * __builtin_amdgcn_rcpf(Y[ct][i + 1] + 1e-6f);
          const float o0 = __uint_as_float(xv[j] << 16) + (z0 > 0.f ? z0 : 0.f);
          const float o1 = __uint_as_float(xv[j] & 0xffff0000u) + (z1 > 0.f ? z1 : 0.f);
          ov[j] = pack_bf16(o0, o1);
        }
        *(LAS u32x4*)(size_t)(la + r * 128 + (((4 * h + 2 * ct + q) ^ (r & 7)) * 16)) = ov;
      }
    }
#pragma unroll
    for (int q = 0; q < 4; ++q) {
      const int row = 8 * q + (lane >> 3);
      const u32x4 ov = *(const LAS u32x4*)(size_t)(la + row * 128 + (((lane & 7) ^ (row & 7)) * 16));
      if (row < nodes) gstore128wt(out, (unsigned)n0 * 128u + q * 1024u + lane * 16u, ov);
    }
  }
  asm volatile("s_waitcnt vmcnt(0)" ::: "memory");
}

extern "C" void kernel_launch(void* const* d_in, const int* in_sizes, int n_in,
                              void* d_out, int out_size, void* d_ws, size_t ws_size,
                              hipStream_t stream) {
  const unsigned short* x = (const unsigned short*)d_in[0];
  const u32x4* eattr = (const u32x4*)d_in[1];
  const int* src = (const int*)d_in[2];
  const int* dst = (const int*)d_in[3];
  const unsigned short* wa = (const unsigned short*)d_in[4];
  const unsigned short* wb = (const unsigned short*)d_in[5];
  const unsigned short* wc = (const unsigned short*)d_in[6];
  const unsigned short* wu = (const unsigned short*)d_in[7];
  const unsigned short* wv = (const unsigned short*)d_in[8];
  unsigned short* out = (unsigned short*)d_out;
  const int N = in_sizes[0] / 64;
  const int E = in_sizes[2];
  const int chunk = (((E + NCH - 1) / NCH) + 3) & ~3;

  char* ws = (char*)d_ws;
  size_t off = 0;
  off = 1 << 20;
  unsigned long long* FL = (unsigned long long*)(ws + off);   off += 1 << 20;
  u32x4* ENT = (u32x4*)(ws + off);                       off += (size_t)2 * NBK * NCH * CELL;
  unsigned* TS = (unsigned*)(ws + off);

  k_main<<<NBK, 512, 0, stream>>>(x, eattr, src, dst, wa, wb, wc, wu, wv, ENT, TS, FL, out, N, E, chunk
     );
}
```

```cpp
#include <hip/hip_runtime.h>
#include <hip/hip_bf16.h>
#include <stdint.h>

using bf16x8 = __attribute__((ext_vector_type(8))) short;
using s16x4 = __attribute__((ext_vector_type(4))) short;
using f32x16 = __attribute__((ext_vector_type(16))) float;
using u32x4 = __attribute__((ext_vector_type(4))) unsigned;
using u32x2 = __attribute__((ext_vector_type(2))) unsigned;
using i32x4 = __attribute__((ext_vector_type(4))) int;
using u16x2 = __attribute__((ext_vector_type(2))) unsigned short;

#define MFMA32(a, b, c) __builtin_amdgcn_mfma_f32_32x32x16_bf16((a), (b), (c), 0, 0, 0)
#define LAS __attribute__((address_space(3)))
#define RANKOF(w) ((w) < 4 ? 7 - (w) : (w) - 4)
#define SLEEP "s_sleep 2\n\t"
#define NREP 8
#define REPOF(b) ((b) % NREP)
#define POLLAT 2
#define PRE 0
#define PREPOL "sc1"
#define G1W 4
#define DBGWAIT 0
#define XWARM 0
#define XAT 3
#define CPF 32
#define CPFB 22016
#define WEARLY 0
#define SRCLATE 1
#define XGAT 0
#define SB() __builtin_amdgcn_sched_barrier(0)

constexpr int NCH = 256;
constexpr int NBK = 256;
constexpr int NBIN = NBK * 8;
constexpr int NFRAG = 39;
constexpr int CHMAX = 3200;
constexpr float NLOG2E = -1.44269504088896340736f;
constexpr unsigned TPITCH = 80;

__device__ __forceinline__ unsigned pack_bf16(float lo, float hi) {
  typedef __attribute__((ext_vector_type(2))) float f2;
  typedef __attribute__((ext_vector_type(2))) __bf16 b2;
  f2 v = {lo, hi};
  b2 r = __builtin_convertvector(v, b2);
  return __builtin_bit_cast(unsigned, r);
}

__device__ __forceinline__ int wave_scan(int v) {
  v += __builtin_amdgcn_update_dpp(0, v, 0x111, 0xf, 0xf, false);
  v += __builtin_amdgcn_update_dpp(0, v, 0x112, 0xf, 0xf, false);
  v += __builtin_amdgcn_update_dpp(0, v, 0x114, 0xf, 0xf, false);
  v += __builtin_amdgcn_update_dpp(0, v, 0x118, 0xf, 0xf, false);
  v += __builtin_amdgcn_update_dpp(0, v, 0x142, 0xa, 0xf, false);
  v += __builtin_amdgcn_update_dpp(0, v, 0x143, 0xc, 0xf, false);
  return v;
}

__device__ __forceinline__ void glds16(const void* sbase, unsigned voff, unsigned lds_dst) {
  asm volatile(
      "s_mov_b32 m0, %2\n\t"
      "s_nop 0\n\t"
      "global_load_lds_dwordx4 %0, %1"
      :: "v"(voff), "s"(sbase), "s"(lds_dst) : "memory", "m0");
}
__device__ __forceinline__ void glds16nt(const void* sbase, unsigned voff, unsigned lds_dst) {
  asm volatile(
      "s_mov_b32 m0, %2\n\t"
      "s_nop 0\n\t"
      "global_load_lds_dwordx4 %0, %1 nt"
      :: "v"(voff), "s"(sbase), "s"(lds_dst) : "memory", "m0");
}
__device__ __forceinline__ void glds16x(const void* sbase, unsigned a, unsigned c, unsigned lds_dst) {
  unsigned tmp;
  asm volatile(
      "s_mov_b32 m0, %4\n\t"
      "v_mad_u32_u16 %0, %1, %5, %2\n\t"
      "global_load_lds_dwordx4 %0, %3"
      : "=&v"(tmp) : "v"(a), "v"(c), "s"(sbase), "s"(lds_dst), "s"(128u) : "memory", "m0");
}
__device__ __forceinline__ void glds16e(const void* sbase, unsigned a, unsigned c, unsigned lds_dst) {
  unsigned tmp;
  asm volatile(
      "s_mov_b32 m0, %4\n\t"
      "v_add_u32 %0, %1, %2\n\t"
      "global_load_lds_dwordx4 %0, %3"
      : "=&v"(tmp) : "v"(a), "v"(c), "s"(sbase), "s"(lds_dst) : "memory", "m0");
}
__device__ __forceinline__ int gload32(const void* sbase, unsigned voff) {
  int r;
  asm volatile("global_load_dword %0, %1, %2" : "=v"(r) : "v"(voff), "s"(sbase) : "memory");
  return r;
}
__device__ __forceinline__ u32x4 gload128(const void* sbase, unsigned voff) {
  u32x4 r;
  asm volatile("global_load_dwordx4 %0, %1, %2" : "=v"(r) : "v"(voff), "s"(sbase) : "memory");
  return r;
}

__device__ __forceinline__ u32x4 gload128nt(const void* sbase, unsigned voff) {
  u32x4 r;
  asm volatile("global_load_dwordx4 %0, %1, %2 nt" : "=v"(r) : "v"(voff), "s"(sbase) : "memory");
  return r;
}
__device__ __forceinline__ void gstore128wt(void* sbase, unsigned voff, u32x4 v) {
  asm volatile("global_store_dwordx4 %0, %1, %2 sc1\n\ts_nop 1" :: "v"(voff), "v"(v), "s"(sbase) : "memory");
}

constexpr int IBOX = 32768;
constexpr int IEXT = 256 * IBOX;
constexpr int CPIT = 208;
constexpr unsigned RINC = 87382;
constexpr int KP_HDR = 0;
constexpr int KP_ENT = KP_HDR + NBK * 4;
constexpr int KP_POS = KP_ENT + NBK * CPIT;
constexpr int KP_LDS = KP_POS + NBK * 4;
constexpr int NWL = 26;
constexpr int NBLOB = 31;

__device__ __forceinline__ unsigned mad24(unsigned a, int b, unsigned c) {
  unsigned r;
  asm("v_mad_i32_i24 %0, %1, %2, %3" : "=v"(r) : "v"(a), "s"(b), "v"(c));
  return r;
}
__device__ __forceinline__ unsigned mul24(unsigned a, unsigned b) {
  unsigned r;
  asm("v_mul_u32_u24 %0, %1, %2" : "=v"(r) : "n"(b), "v"(a));
  return r;
}
__device__ __forceinline__ unsigned mul24s(unsigned a, unsigned b) {
  unsigned r;
  asm("v_mul_u32_u24 %0, %1, %2" : "=v"(r) : "s"(b), "v"(a));
  return r;
}
__device__ __forceinline__ unsigned byte3x(unsigned a, unsigned b) {
  unsigned r;
  asm("v_mul_u32_u24_sdwa %0, %1, %2 dst_sel:DWORD dst_unused:UNUSED_PAD src0_sel:DWORD src1_sel:BYTE_3" : "=v"(r) : "s"(b), "v"(a));
  return r;
}
__device__ __forceinline__ unsigned mulhi24(unsigned a, unsigned b) {
  return (unsigned)(((unsigned long long)(a & 0xffffffu) * (unsigned long long)b) >> 32);
}
__device__ __forceinline__ unsigned dot4(unsigned a, unsigned b, unsigned c) {
  return __builtin_amdgcn_udot4(a, b, c, false);
}

constexpr int RING_SLOT = 5 * 1024;
constexpr int LCAP = 512;
constexpr int LARR = 2560;
constexpr int LIST_BYTES = 2 * LARR;
constexpr int OFF_CNT = 0;
constexpr int OFF_XF = 256;
constexpr int OFF_W = 512;
constexpr int OFF_T1 = OFF_W + NWL * 1024;
constexpr int OFF_T2 = OFF_T1 + 32 * TPITCH;
constexpr int OFF_R0 = OFF_W + NFRAG * 1024;
constexpr int OFF_R1 = OFF_R0 + 8 * RING_SLOT;
constexpr int OFF_L = OFF_R1 + 8 * RING_SLOT;
constexpr int MAIN_LDS = OFF_L + 8 * LIST_BYTES;
static_assert(MAIN_LDS <= 160 * 1024, "LDS");
static_assert(KP_LDS <= 16 * RING_SLOT, "phase 1 lives in the ring");

__device__ __forceinline__ unsigned ldd(const unsigned* p) {
  return __hip_atomic_load(p, __ATOMIC_RELAXED, __HIP_MEMORY_SCOPE_AGENT);
}
__device__ __forceinline__ unsigned long long ldq(const unsigned long long* p) {
  return __hip_atomic_load(p, __ATOMIC_RELAXED, __HIP_MEMORY_SCOPE_AGENT);
}
__device__ __forceinline__ bf16x8 rawfrag(unsigned a) {
  const s16x4 a0 = __builtin_amdgcn_ds_read_tr16_b64_v4i16((LAS s16x4*)(size_t)a);
  const s16x4 a1 = __builtin_amdgcn_ds_read_tr16_b64_v4i16((LAS s16x4*)(size_t)(a + 512));
  return __builtin_shufflevector(a0, a1, 0, 1, 2, 3, 4, 5, 6, 7);
}
__device__ __forceinline__ float add1(float t) { return 1.0f + t; }
__device__ __forceinline__ float mulf(int i, float a, float b) { return (i & 1) ? __builtin_fmaf(a, b, 0.0f) : a * b; }

__global__ __launch_bounds__(512, 2) void k_main(const unsigned short* __restrict__ x,
                                                 const u32x4* __restrict__ ES,
                                                 const int* __restrict__ src, const int* __restrict__ dst,
                                                 const unsigned short* __restrict__ wa_g,
                                                 const unsigned short* __restrict__ wb_g,
                                                 const unsigned short* __restrict__ wc_g,
                                                 const unsigned short* __restrict__ wu_g,
                                                 const unsigned short* __restrict__ wv_g,
                                                 u32x4* ENT, unsigned long long* TS, unsigned long long* FL,
                                                 unsigned short* __restrict__ out, int N, int E, int chunk,
                                                 unsigned M, float MF
                                                 ) {
#define PT(i)
  PT(0);
  __shared__ __attribute__((aligned(16))) char smem[MAIN_LDS];
  const int tid0 = threadIdx.x;
  const int wave = __builtin_amdgcn_readfirstlane(tid0 >> 6);
  const int bk = blockIdx.x;
  const unsigned lds0 = (unsigned)(size_t)(LAS char*)smem;

  asm volatile("" :: "s"(x), "s"(ES), "s"(src), "s"(dst), "s"(wa_g), "s"(wb_g), "s"(wc_g), "s"(wu_g), "s"(wv_g), "s"(ENT),
               "s"(TS), "s"(FL), "s"(out), "s"(N), "s"(E), "s"(chunk), "s"(M), "s"(MF));
  LAS char* sm = (LAS char*)smem + OFF_R0;
  const unsigned sm0 = lds0 + OFF_R0;
  const int base = bk * chunk;
  const int ecnt = max(min(chunk, E - base), 0);
  char* ib = reinterpret_cast<char*>(ENT) + (size_t)bk * IBOX;
  const int tid = tid0, lane = tid & 63, r = lane & 31, h = lane >> 5;
  const unsigned pc = tid & 7, bq = tid >> 3;
  unsigned ia = sm0 + KP_ENT + bq * CPIT + pc * 16;
  auto nodeof = [&](unsigned c) { return min((unsigned)__builtin_fmaf((float)c, MF, 0.5f * MF + 0.5f), (unsigned)N - 1u); };
  auto init = [&]() {
#pragma unroll
    for (int s = 0; s < 4; ++s) *(LAS u32x4*)(size_t)(ia + s * 64 * CPIT) = u32x4{~0u, ~0u, ~0u, ~0u};
    asm volatile("s_waitcnt lgkmcnt(0)\n\ts_barrier" : "+v"(ia) :: "memory");
  };
  unsigned tup[8], val[8], pm[8];
  unsigned long long on[2];
  u32x4 xg[4];
  auto xload = [&]() {
    const unsigned xo = nodeof((bk * 8 + wave) * 32 + r) * 128u + 64u * h;
    asm volatile("global_load_dwordx4 %0, %4, %5\n\t"
                 "global_load_dwordx4 %1, %4, %5 offset:16\n\t"
                 "global_load_dwordx4 %2, %4, %5 offset:32\n\t"
                 "global_load_dwordx4 %3, %4, %5 offset:48"
                 : "=&{v[160:163]}"(xg[0]), "=&{v[164:167]}"(xg[1]), "=&{v[168:171]}"(xg[2]), "=&{v[172:175]}"(xg[3])
                 : "v"(xo), "s"(x) : "memory");
  };
  const unsigned xn0 = __builtin_amdgcn_readfirstlane(nodeof((bk * 8 + wave) * 32));
  unsigned xd[4];
  auto xprep = [&]() {
#pragma unroll
    for (int i = 0; i < 4; ++i)
      xd[i] = min(xn0 + 8 * i + (lane >> 3), N - 1u) * 128u + ((((lane & 7) ^ (lane >> 4)) * 16) ^ (i & 1) * 64) - i * 1024u;
    asm volatile("" : "+v"(xd[0]), "+v"(xd[1]), "+v"(xd[2]), "+v"(xd[3]));
  };
  auto xdma = [&]() {
    asm volatile("s_mov_b32 m0, %5\n\ts_nop 0\n\t"
                 "global_load_lds_dwordx4 %0, %4\n\t"
                 "global_load_lds_dwordx4 %1, %4 offset:1024\n\t"
                 "global_load_lds_dwordx4 %2, %4 offset:2048\n\t"
                 "global_load_lds_dwordx4 %3, %4 offset:3072"
                 :: "v"(xd[0]), "v"(xd[1]), "v"(xd[2]), "v"(xd[3]), "s"(x), "s"(lds0 + OFF_L + wave * LIST_BYTES)
                 : "memory", "m0");
  };
  auto wload = [&](const unsigned short* w, int f) {
    asm volatile("s_mov_b32 m0, %3\n\ts_nop 0\n\t"
                 "global_load_lds_dwordx4 %0, %2\n\t"
                 "global_load_lds_dwordx4 %0, %2 offset:1024\n\t"
                 "global_load_lds_dwordx4 %0, %2 offset:2048\n\t"
                 "global_load_lds_dwordx4 %0, %2 offset:3072\n\t"
                 "s_addk_i32 m0, 4096\n\ts_nop 0\n\t"
                 "global_load_lds_dwordx4 %1, %2\n\t"
                 "global_load_lds_dwordx4 %1, %2 offset:1024\n\t"
                 "global_load_lds_dwordx4 %1, %2 offset:2048\n\t"
                 "global_load_lds_dwordx4 %1, %2 offset:3072"
                 :: "v"(lane * 16u), "v"(lane * 16u + 4096u), "s"(w), "s"(lds0 + OFF_W + f * 1024) : "memory", "m0", "scc");
  };
  auto wall = [&]() {
    wload(wa_g, NBLOB);
    wload(wb_g, 0);
    wload(wv_g, 8);
    glds16(wc_g, lane * 16, lds0 + OFF_W + 16 * 1024);
    glds16(wc_g + 512, lane * 16, lds0 + OFF_W + 17 * 1024);
  };
  int spin = 1 << 20;
  u32x2 ep = {0u, 0u};
  if (wave == 7) {
    u32x2 e0;
    asm volatile("global_load_dwordx2 %0, %1, %2 sc1" : "=v"(e0) : "v"(0u), "s"(FL) : "memory");
    {
      unsigned long long pc;
      asm volatile("s_getpc_b64 %0\n\t"
                   "s_mov_b32 m0, %4\n\t"
                   "s_mov_b64 exec, %5\n\t"
                   "global_load_lds_dword %1, %0\n\t"
                   "global_load_lds_dword %2, %0\n\t"
                   "global_load_lds_dword %3, %0\n\t"
                   "s_mov_b64 exec, -1"
                   : "=&s"(pc)
                   : "v"(lane * 128u), "v"(lane * 128u + 8192u), "v"(min(lane * 128u + 16384u, CPFB - 128u)),
                     "s"(lds0 + OFF_L + 7 * LIST_BYTES + 4096), "s"(__ballot(bk < CPF))
                   : "memory", "m0");
    }
    *(LAS u32x4*)(sm + KP_HDR + lane * 16) = u32x4{0u, 0u, 0u, 0u};
    *(LAS u32x4*)((LAS char*)smem + OFF_CNT + r * 16) =
        r < 8 ? u32x4{lds0 + OFF_L, lds0 + OFF_L + 4, lds0 + OFF_L + 8, lds0 + OFF_L + 12} : u32x4{0u, 0u, 0u, 0u};
    init();
    {
      const unsigned col = h ? ((r >> 1) & 12u) | ((r << 2) & 16u) | (r & 3u) : r;
      LAS char* tr = (LAS char*)smem + OFF_T1 + lane * TPITCH;
#pragma unroll
      for (int j = 0; j < 4; ++j) *(LAS u32x4*)(tr + j * 16) = u32x4{0u, 0u, 0u, 0u};
      *(LAS unsigned short*)(tr + col * 2) = 0x3F80;
    }
#pragma unroll
    for (int i = 0; i < 8; ++i) { tup[i] = 0u; val[i] = 0u; pm[i] = 0u; }
    on[0] = 0ull; on[1] = 0ull;
    asm volatile("s_waitcnt vmcnt(%1)" : "+v"(e0) : "n"((XWARM ? 4 : 0) + (CPF ? 3 : 0) + (WEARLY ? 26 : 0)) : "memory");
    wall();
    ep[0] = __builtin_amdgcn_readfirstlane(e0[0]) + 0x9E3779B1u;
    ep[1] = __builtin_amdgcn_readfirstlane(e0[1]) + 0x7F4A7C15u;
    *(LAS u32x2*)((LAS char*)smem + OFF_CNT + 168) = u32x2{ep[0], ep[1] >> 24};
    if (__builtin_expect(ecnt & 3, 0)) {
      const int i = (ecnt & ~3) + lane;
      on[0] = __ballot(i < ecnt);
      if (i < ecnt) {
        pm[0] = (unsigned)dst[base + i] * M;
        tup[0] = (unsigned)src[base + i] | (unsigned)i << 16;
        val[0] = __hip_atomic_fetch_add((LAS unsigned*)(sm + KP_HDR) + (pm[0] >> 24), RINC, __ATOMIC_RELAXED, __HIP_MEMORY_SCOPE_WORKGROUP);
        const unsigned u = min(val[0] >> 16, 48u);
        *(LAS unsigned*)(sm + KP_ENT + (pm[0] >> 24) * CPIT + u * 4) = tup[0];
        *(LAS unsigned char*)(sm + KP_ENT + (pm[0] >> 24) * CPIT + ((u * 4) | (u & 3) | 12)) = pm[0] >> 16;
      }
      __builtin_amdgcn_s_waitcnt(0x0F70);
    }
    PT(1);
    xprep();
    xdma();
  } else {
    u32x4 d[2], sv[2];
    on[0] = __ballot(4 * tid + 3 < ecnt);
    on[1] = __ballot(4 * tid + 1795 < ecnt);
    auto ld2 = [&](u32x4* d, const int* p) {
      asm volatile("s_mov_b64 exec, %5\n\t"
                   "global_load_dwordx4 %0, %2, %4 nt\n\t"
                   "s_mov_b64 exec, %6\n\t"
                   "global_load_dwordx4 %1, %3, %4 nt\n\t"
                   "s_mov_b64 exec, -1"
                   : "=&v"(d[0]), "=&v"(d[1])
                   : "v"((base + 4 * tid) * 4u), "v"((base + 4 * tid) * 4u + 7168u), "s"(p), "s"(on[0]), "s"(on[1])
                   : "memory");
    };
    ld2(d, dst);
    init();
    ld2(sv, src);
    xprep();
    unsigned inc = RINC, t0 = (unsigned)tid << 18;
    asm volatile("s_waitcnt vmcnt(%4)" : "+v"(d[0]), "+v"(d[1]), "+v"(inc), "+v"(t0) : "n"(XAT == 5 ? 6 : 2) : "memory");
    PT(1);
    unsigned cb[8];
#pragma unroll
    for (int s = 0; s < 2; ++s) {
      unsigned* rr = val + 4 * s;
#pragma unroll
      for (int k = 0; k < 4; ++k) pm[4 * s + k] = mul24s(d[s][k], M);
#pragma unroll
      for (int k = 0; k < 4; ++k) rr[k] = byte3x(pm[4 * s + k], 4u);
      asm volatile(
          "s_mov_b64 exec, %5\n\t"
          "ds_add_rtn_u32 %0, %0, %4 offset:%6\n\t"
          "ds_add_rtn_u32 %1, %1, %4 offset:%6\n\t"
          "ds_add_rtn_u32 %2, %2, %4 offset:%6\n\t"
          "ds_add_rtn_u32 %3, %3, %4 offset:%6\n\t"
          "s_mov_b64 exec, -1"
          : "+v"(rr[0]), "+v"(rr[1]), "+v"(rr[2]), "+v"(rr[3])
          : "v"(inc), "s"(on[s]), "n"(OFF_R0 + KP_HDR)
          : "memory");
    }
#pragma unroll
    for (int i = 0; i < 8; ++i) cb[i] = byte3x(pm[i], (unsigned)CPIT);
    asm volatile("s_waitcnt vmcnt(%10)"
                 : "+v"(sv[0]), "+v"(sv[1]), "+v"(cb[0]), "+v"(cb[1]), "+v"(cb[2]), "+v"(cb[3]), "+v"(cb[4]), "+v"(cb[5]),
                   "+v"(cb[6]), "+v"(cb[7])
                 : "n"(XAT >= 4 ? 4 : 0) : "memory");
    xdma();
#pragma unroll
    for (int i = 0; i < 8; ++i) tup[i] = sv[i >> 2][i & 3] + t0 + (((i >> 2) * 1792u + (i & 3)) << 16);
#pragma unroll
    for (int s = 0; s < 2; ++s) {
      asm volatile("s_waitcnt lgkmcnt(%8)"
                   : "+v"(val[4 * s]), "+v"(val[4 * s + 1]), "+v"(val[4 * s + 2]), "+v"(val[4 * s + 3]), "+v"(tup[4 * s]),
                     "+v"(tup[4 * s + 1]), "+v"(tup[4 * s + 2]), "+v"(tup[4 * s + 3])
                   : "n"(DBGWAIT ? 0 : s ? 8 : 4) : "memory");
      unsigned pa[4], pb[4];
#pragma unroll
      for (int k = 0; k < 4; ++k) {
        const unsigned u = min(val[4 * s + k], 48u << 16);
        asm("v_mad_u32_u16 %0, %1, 4, %2 op_sel:[1,0,0,0]" : "=v"(pa[k]) : "v"(u), "v"(cb[4 * s + k]));
        pb[k] = pa[k] | ((u >> 16) & 3u) | 12u;
      }
      asm volatile(
          "s_mov_b64 exec, %16\n\t"
          "ds_write_b32 %0, %8 offset:%17\n\t"
          "ds_write_b8_d16_hi %4, %12 offset:%17\n\t"
          "ds_write_b32 %1, %9 offset:%17\n\t"
          "ds_write_b8_d16_hi %5, %13 offset:%17\n\t"
          "ds_write_b32 %2, %10 offset:%17\n\t"
          "ds_write_b8_d16_hi %6, %14 offset:%17\n\t"
          "ds_write_b32 %3, %11 offset:%17\n\t"
          "ds_write_b8_d16_hi %7, %15 offset:%17\n\t"
          "s_mov_b64 exec, -1"
          :: "v"(pa[0]), "v"(pa[1]), "v"(pa[2]), "v"(pa[3]), "v"(pb[0]), "v"(pb[1]), "v"(pb[2]), "v"(pb[3]),
             "v"(tup[4 * s]), "v"(tup[4 * s + 1]), "v"(tup[4 * s + 2]), "v"(tup[4 * s + 3]), "v"(pm[4 * s]),
             "v"(pm[4 * s + 1]), "v"(pm[4 * s + 2]), "v"(pm[4 * s + 3]), "s"(on[s]), "n"(OFF_R0 + KP_ENT)
          : "memory");
    }
  }
  PT(2);
  u32x2 tag;
  {
    unsigned ha = sm0 + KP_HDR + bq * 4, hb = sm0 + KP_HDR + lane * 16;
    unsigned go = bq * IBOX + bk * 128 + pc * 16;
    asm volatile("s_waitcnt lgkmcnt(0)\n\ts_barrier" : "+v"(ha), "+v"(hb), "+v"(go) :: "memory");
    PT(3);
    tag = *(const LAS u32x2*)((LAS char*)smem + OFF_CNT + 168);
    const u32x4 call = *(const LAS u32x4*)(size_t)hb;
    unsigned cn[4];
    u32x4 v[4];
#pragma unroll
    for (int s = 0; s < 4; ++s) {
      cn[s] = *(const LAS unsigned*)(size_t)(ha + s * 256);
      v[s] = *(const LAS u32x4*)(size_t)(ia + s * 64 * CPIT);
    }
    asm volatile("" : "+v"(v[0]), "+v"(v[1]), "+v"(v[2]), "+v"(v[3]), "+v"(tag));
    if (__builtin_expect(__ballot(max(max(call[0], call[1]), max(call[2], call[3])) > 36u * RINC) != 0ull, 0)) {
      LAS unsigned* ovc = (LAS unsigned*)((LAS char*)smem + OFF_CNT + 164);
      LAS unsigned* pos = (LAS unsigned*)(sm + KP_POS);
      if (tid < NBK) {
        const unsigned u = *(const LAS unsigned*)(sm + KP_HDR + tid * 4) >> 16, n = u - (u >> 2);
        if (n > 36u) {
          const unsigned p = (unsigned)base + __hip_atomic_fetch_add(ovc, n - 36u, __ATOMIC_RELAXED, __HIP_MEMORY_SCOPE_WORKGROUP);
          pos[tid] = p;
          __hip_atomic_store(FL + 512 + tid * NCH + bk, ((unsigned long long)(n - 36u) << 32) | p, __ATOMIC_RELAXED,
                             __HIP_MEMORY_SCOPE_AGENT);
        }
      }
      asm volatile("s_waitcnt lgkmcnt(0)\n\ts_barrier" ::: "memory");
#pragma unroll
      for (int i = 0; i < 8; ++i) {
        const unsigned u = val[i] >> 16, n = u - (u >> 2);
        if (((on[i >> 2] >> lane) & 1ull) && n >= 36u)
          __hip_atomic_store(TS + pos[pm[i] >> 24] + (n - 36u), ((unsigned long long)((pm[i] >> 16) & 0xffu) << 32) | tup[i],
                             __ATOMIC_RELAXED, __HIP_MEMORY_SCOPE_AGENT);
      }
      __builtin_amdgcn_s_waitcnt(0x0F70);
      asm volatile("s_waitcnt vmcnt(0)\n\ts_barrier" ::: "memory");
    }
    auto seal = [&](u32x4& w, bool more) {
      w[3] = __builtin_amdgcn_perm(tag[1], w[3], 0x04020100u);
      unsigned sm_ = w[0] + w[1] + w[2] + w[3] + tag[0];
      sm_ += __builtin_amdgcn_update_dpp(0u, sm_, 0xB1, 0xf, 0xf, false);
      sm_ += __builtin_amdgcn_update_dpp(0u, sm_, 0x4E, 0xf, 0xf, false);
      sm_ += __builtin_amdgcn_update_dpp(0u, sm_, 0x141, 0xf, 0xf, false);
      w[2] = pc == 7u ? w[2] - sm_ + (more ? 1u : 0u) : w[2];
    };
#pragma unroll
    for (int s = 0; s < 4; ++s) seal(v[s], cn[s] > 23u * RINC);
    asm volatile("global_store_dwordx4 %0, %1, %5 sc1\n\t"
                 "global_store_dwordx4 %0, %2, %6 sc1\n\t"
                 "global_store_dwordx4 %0, %3, %7 sc1\n\t"
                 "global_store_dwordx4 %0, %4, %8 sc1\n\t"
                 "s_nop 1"
                 :: "v"(go), "v"(v[0]), "v"(v[1]), "v"(v[2]), "v"(v[3]), "s"(ENT), "s"(ENT + 4 * IBOX), "s"(ENT + 8 * IBOX),
                    "s"(ENT + 12 * IBOX)
                 : "memory");
    PT(4);
    if (__builtin_expect(__ballot(max(max(cn[0], cn[1]), max(cn[2], cn[3])) > 23u * RINC) != 0ull, 0)) {
#pragma unroll 1
      for (int s = 0; s < 4; ++s) {
        const unsigned c = *(const LAS unsigned*)(size_t)(ha + s * 256);
        u32x4 w = *(const LAS u32x4*)(size_t)(ia + s * 64 * CPIT + (pc < 4u ? 128 : 0));
        if (pc == 7u) w = u32x4{w[2], ~0u, ~0u, w[3] >> 16};
        else {
#pragma unroll
          for (int j = 0; j < 3; ++j) w[j] = pc < 4u && 32u + 4u * pc + j < (c >> 16) ? w[j] : ~0u;
        }
        seal(w, c > 36u * RINC);
        if (c > 23u * RINC) gstore128wt(ENT, go + s * (64 * IBOX) + IEXT, w);
      }
    }
    PT(5);
  }
  u32x4 cv[4];
  unsigned vo[4], eb[4];
  unsigned long long pend[4];
#pragma unroll
  for (int k = 0; k < 4; ++k) {
    vo[k] = wave * 4096u + k * 1024u + lane * 16u;
    eb[k] = (unsigned)((wave * 32 + k * 8 + (lane >> 3)) * chunk) * 32u;
  }
#define POLL0()                                                                                                       \
  asm volatile("global_load_dwordx4 %0, %4, %8 sc1\n\t"                                                               \
               "global_load_dwordx4 %1, %5, %8 sc1\n\t"                                                               \
               "global_load_dwordx4 %2, %6, %8 sc1\n\t"                                                               \
               "global_load_dwordx4 %3, %7, %8 sc1"                                                                   \
               : "=&{v[240:243]}"(cv[0]), "=&{v[244:247]}"(cv[1]), "=&{v[248:251]}"(cv[2]), "=&{v[252:255]}"(cv[3])   \
               : "v"(vo[0]), "v"(vo[1]), "v"(vo[2]), "v"(vo[3]), "s"(ib)                                              \
               : "memory")
  const LAS bf16x8* wl = (const LAS bf16x8*)(smem + OFF_W) + lane;
  const f32x16 zero16 = {0.f, 0.f, 0.f, 0.f, 0.f, 0.f, 0.f, 0.f, 0.f, 0.f, 0.f, 0.f, 0.f, 0.f, 0.f, 0.f};
  auto widx = [&](int m, int ct) { return m == 6 ? 16 + ct : m < 11 ? m - 7 + 4 * ct : 8 + m - 11 + 4 * ct; };
#define HELD ((1 << 6) | (1 << 8) | (1 << 10) | (1 << 12) | (1 << 14))
  bf16x8 wr[2][15];
  const unsigned to = lds0 + OFF_W + ((lane >> 2) & 3) * 128 + (lane & 1) * 64 + ((lane >> 4) & 1) * 16 + ((lane >> 1) & 1) * 8;
  auto xa = [&]() {
    bf16x8 wa[8];
#pragma unroll
    for (int k = 0; k < 8; ++k) wa[k] = rawfrag(to + (NBLOB + (k >> 1)) * 1024 + h * 4096 + (k & 1) * 32);
    asm volatile("" : "+v"(wa[0]), "+v"(wa[1]), "+v"(wa[2]), "+v"(wa[3]), "+v"(wa[4]), "+v"(wa[5]), "+v"(wa[6]), "+v"(wa[7]),
                      "+{v[160:163]}"(xg[0]), "+{v[164:167]}"(xg[1]), "+{v[168:171]}"(xg[2]), "+{v[172:175]}"(xg[3]));
    f32x16 XA[2] = {zero16, zero16};
#pragma unroll
    for (int k = 0; k < 4; ++k)
#pragma unroll
      for (int ct = 0; ct < 2; ++ct) XA[ct] = MFMA32(__builtin_bit_cast(bf16x8, xg[k]), wa[k * 2 + ct], XA[ct]);
#pragma unroll
    for (int q = 0; q < 4; ++q) {
      u32x4 xp;
#pragma unroll
      for (int j = 0; j < 4; ++j)
        xp[j] = pack_bf16(XA[q & 1][8 * (q >> 1) + 2 * j] * NLOG2E, XA[q & 1][8 * (q >> 1) + 2 * j + 1] * NLOG2E);
      *(LAS u32x4*)((LAS char*)smem + OFF_R1 + wave * RING_SLOT + q * 1024 + lane * 16) = xp;
    }
  };
  {
    auto prep = [&](unsigned f, unsigned hs, bool scale) {
      u32x4 w[2];
#pragma unroll
      for (int ct = 0; ct < 2; ++ct) w[ct] = __builtin_bit_cast(u32x4, rawfrag(to + f + h * hs + ct * 32));
      asm volatile("s_waitcnt lgkmcnt(0)" : "+v"(w[0]), "+v"(w[1]) :: "memory");
#pragma unroll
      for (int ct = 0; ct < 2; ++ct) {
        if (scale) {
#pragma unroll
          for (int j = 0; j < 4; ++j)
            w[ct][j] = pack_bf16(__uint_as_float(w[ct][j] << 16) * NLOG2E, __uint_as_float(w[ct][j] & 0xffff0000u) * NLOG2E);
        }
        *(LAS u32x4*)((LAS char*)smem + OFF_W + f + ct * hs + lane * 16) = w[ct];
      }
    };
    {
      const unsigned xr = min(nodeof((bk * 8 + wave) * 32 + r) - xn0, 31u);
      unsigned xa_ = lds0 + OFF_L + wave * LIST_BYTES + xr * 128 + (((4 * h) ^ ((xr >> 1) & 7)) * 16);
      asm volatile("s_waitcnt vmcnt(4)" : "+v"(xa_) :: "memory");
#pragma unroll
      for (int t = 0; t < 4; ++t) xg[t] = *(const LAS u32x4*)(size_t)(xa_ ^ (t * 16));
      asm volatile("" : "+v"(xg[0]), "+v"(xg[1]), "+v"(xg[2]), "+v"(xg[3]) :: "memory");
      *(LAS unsigned*)((LAS char*)smem + OFF_L + wave * LIST_BYTES + h * LARR + r * 4) = ~0u;
    }
    asm volatile("s_waitcnt vmcnt(4) lgkmcnt(0)\n\ts_barrier"
                 : "+v"(vo[0]), "+v"(vo[1]), "+v"(vo[2]), "+v"(vo[3]), "+v"(eb[0]), "+v"(eb[1]), "+v"(eb[2]), "+v"(eb[3])
                 :: "memory");
    xa();
    if (wave < 4) prep(wave * 1024, 4096, true);
    else {
      prep((wave + 4) * 1024, 4096, false);
      if (wave == 4) prep(16 * 1024, 1024, true);
    }
    PT(6);
    POLL0();
  }
  const unsigned l0 = lds0 + OFF_L;
  unsigned aLX = l0 + (lane >> 3) * 16;
  unsigned aLE = l0 + LARR + ((((lane >> 1) & 7) << 2) | (lane >> 4)) * 4;
  unsigned aLS4 = l0 + (4 * h + ((lane >> 2) & 3)) * 16;
  unsigned aLS1 = l0 + (((r & 7) << 2) | (r >> 3)) * 4 + 2;
  unsigned cx0 = (unsigned)(((lane & 7) ^ (lane >> 4)) * 16);
  unsigned cx1 = cx0 ^ 64u;
  unsigned ce = (unsigned)((((lane & 1) ^ ((lane >> 4) & 1))) * 16);
  const unsigned ringd = 8 * RING_SLOT;
  unsigned cS = lds0 + OFF_T1 + (((lane >> 4) & 1) * 16 + (lane & 3) * 4) * 2;
  unsigned cSt = lds0 + OFF_T2 + h * 32;
  unsigned fo[4];
#pragma unroll
  for (int t = 0; t < 4; ++t) fo[t] = lds0 + OFF_R0 + (unsigned)(r * 128 + (((4 * h + t) ^ ((r >> 1) & 7)) * 16));
  unsigned foe = lds0 + OFF_R0 + (unsigned)(4096 + r * 32 + ((h ^ ((r >> 3) & 1)) * 16));
  f32x16 Y[4];
#pragma unroll
  for (int q = 0; q < 4; ++q)
#pragma unroll
    for (int i = 0; i < 16; ++i) Y[q][i] = 0.f;
  f32x16 G0, V0, G1, V1;
  bf16x8 S[2], St[2], as[4], ae, b[3];
  u32x4 pd[2], pn[2];
  float tq[16], rq[16], pq[16];
  auto pipe_zero = [&]() {
#pragma unroll
    for (int j = 0; j < 8; ++j) { S[0][j] = 0; S[1][j] = 0; }
#pragma unroll
    for (int j = 0; j < 4; ++j) { pd[0][j] = 0; pd[1][j] = 0; pn[0][j] = 0; pn[1][j] = 0; }
#pragma unroll
    for (int i = 0; i < 16; ++i) V1[i] = 0.f;
    rq[14] = 0.f; rq[15] = 0.f;
  };
  unsigned rid = lane & 7;
  asm volatile("" : "+v"(aLX), "+v"(aLE), "+v"(aLS4), "+v"(aLS1), "+v"(cx0), "+v"(cx1), "+v"(ce), "+v"(cS), "+v"(cSt),
                    "+v"(fo[0]), "+v"(fo[1]), "+v"(fo[2]), "+v"(fo[3]), "+v"(foe), "+v"(rid));
  asm volatile("" : "+v"(Y[0]), "+v"(Y[1]), "+v"(Y[2]), "+v"(Y[3]));
  auto ld128 = [&](unsigned a) { return *(const LAS u32x4*)(size_t)a; };
  auto gather5 = [&](const u32x4 sx, unsigned re, unsigned dstb) {
    glds16x(x, sx[0], cx0, dstb);
    glds16x(x, sx[1], cx1, dstb + 1024);
    glds16x(x, sx[2], cx0, dstb + 2048);
    glds16x(x, sx[3], cx1, dstb + 3072);
    glds16e(ES, re, ce, dstb + 4096);
  };
  unsigned rq4 = ((lane >> 3) & 3) * 4;
  unsigned early, just;
  asm volatile(
        "s_mov_b32 s96, 0x800000\n\t"
        "s_mov_b32 s97, %18\n\t"
        "s_movk_i32 s98, 5120\n\t"
        "s_mov_b32 s99, 0x0c040100\n\t"
        "s_mov_b32 s100, 0x0c050100\n\t"
        "s_mov_b32 s101, 0x0c060100\n\t"
        "s_mov_b32 s52, 0x7f7f7f7f\n\t"
        "s_mov_b32 s53, 0x7f7f7f7f\n\t"
        "s_mov_b64 s[54:55], 0\n\t"
        "s_mov_b32 %12, 0\n\t"
        "v_mov_b32 v191, 16\n\t"
        "s_waitcnt vmcnt(%30)\n\t"
        "v_add3_u32 v228, v240, v241, v242\n\t"
        "v_add3_u32 v231, v244, v245, v246\n\t"
        "v_add3_u32 v228, v228, v243, %19\n\t"
        "v_add3_u32 v231, v231, v247, %19\n\t"
        "v_cmp_ne_u32_sdwa s[72:73], v243, %20 src0_sel:BYTE_3 src1_sel:DWORD\n\t"
        "v_add_u32_dpp v228, v228, v228 quad_perm:[1,0,3,2] row_mask:0xf bank_mask:0xf\n\t"
        "v_add_u32_dpp v231, v231, v231 quad_perm:[1,0,3,2] row_mask:0xf bank_mask:0xf\n\t"
        "v_cmp_ne_u32_sdwa s[78:79], v247, %20 src0_sel:BYTE_3 src1_sel:DWORD\n\t"
        "v_add_u32_dpp v228, v228, v228 quad_perm:[2,3,0,1] row_mask:0xf bank_mask:0xf\n\t"
        "v_add_u32_dpp v231, v231, v231 quad_perm:[2,3,0,1] row_mask:0xf bank_mask:0xf\n\t"
        "s_nop 0\n\t"
        "v_add_u32_dpp v228, v228, v228 row_half_mirror row_mask:0xf bank_mask:0xf\n\t"
        "v_add_u32_dpp v231, v231, v231 row_half_mirror row_mask:0xf bank_mask:0xf\n\t"
        "v_cmp_gt_u32 s[64:65], 2, v228\n\t"
        "v_cmp_eq_u32 s[56:57], 1, v228\n\t"
        "v_cmp_gt_u32 s[66:67], 2, v231\n\t"
        "v_cmp_eq_u32 s[58:59], 1, v231\n\t"
        "s_and_b64 s[72:73], s[72:73], s[64:65]\n\t"
        "s_cselect_b64 s[64:65], 0, s[64:65]\n\t"
        "s_and_b64 s[56:57], s[56:57], s[64:65]\n\t"
        "s_not_b64 %0, s[64:65]\n\t"
        "s_or_b64 s[54:55], s[54:55], s[56:57]\n\t"
        "s_or_b64 s[54:55], s[54:55], %0\n\t"
        "s_and_b64 s[78:79], s[78:79], s[66:67]\n\t"
        "s_cselect_b64 s[66:67], 0, s[66:67]\n\t"
        "s_and_b64 s[58:59], s[58:59], s[66:67]\n\t"
        "s_not_b64 %1, s[66:67]\n\t"
        "s_or_b64 s[54:55], s[54:55], s[58:59]\n\t"
        "s_or_b64 s[54:55], s[54:55], %1\n\t"
        "s_mov_b64 exec, s[64:65]\n\t"
        "v_cmpx_ne_u32 s[72:73], -1, v240\n\t"
        "v_bfe_u32 v192, v243, 5, 3\n\t"
        "v_lshl_add_u32 v204, v192, 4, %21\n\t"
        "ds_add_rtn_u32 v204, v204, v191\n\t"
        "v_perm_b32 v216, v243, v240, s99\n\t"
        "v_mad_u32_u16 v228, v240, 32, %13 op_sel:[1,0,0,0]\n\t"
        "v_cmpx_ne_u32 s[74:75], -1, v241\n\t"
        "v_bfe_u32 v193, v243, 13, 3\n\t"
        "v_lshl_add_u32 v205, v193, 4, %21\n\t"
        "ds_add_rtn_u32 v205, v205, v191\n\t"
        "v_perm_b32 v217, v243, v241, s100\n\t"
        "v_mad_u32_u16 v229, v241, 32, %13 op_sel:[1,0,0,0]\n\t"
        "s_and_b64 exec, exec, s[52:53]\n\t"
        "v_cmpx_ne_u32 s[76:77], -1, v242\n\t"
        "v_bfe_u32 v194, v243, 21, 3\n\t"
        "v_lshl_add_u32 v206, v194, 4, %21\n\t"
        "ds_add_rtn_u32 v206, v206, v191\n\t"
        "v_perm_b32 v218, v243, v242, s101\n\t"
        "v_mad_u32_u16 v230, v242, 32, %13 op_sel:[1,0,0,0]\n\t"
        "s_mov_b64 exec, s[66:67]\n\t"
        "v_cmpx_ne_u32 s[78:79], -1, v244\n\t"
        "v_bfe_u32 v195, v247, 5, 3\n\t"
        "v_lshl_add_u32 v207, v195, 4, %21\n\t"
        "ds_add_rtn_u32 v207, v207, v191\n\t"
        "v_perm_b32 v219, v247, v244, s99\n\t"
        "v_mad_u32_u16 v231, v244, 32, %14 op_sel:[1,0,0,0]\n\t"
        "v_cmpx_ne_u32 s[80:81], -1, v245\n\t"
        "v_bfe_u32 v196, v247, 13, 3\n\t"
        "v_lshl_add_u32 v208, v196, 4, %21\n\t"
        "ds_add_rtn_u32 v208, v208, v191\n\t"
        "v_perm_b32 v220, v247, v245, s100\n\t"
        "v_mad_u32_u16 v232, v245, 32, %14 op_sel:[1,0,0,0]\n\t"
        "s_and_b64 exec, exec, s[52:53]\n\t"
        "v_cmpx_ne_u32 s[82:83], -1, v246\n\t"
        "v_bfe_u32 v197, v247, 21, 3\n\t"
        "v_lshl_add_u32 v209, v197, 4, %21\n\t"
        "ds_add_rtn_u32 v209, v209, v191\n\t"
        "v_perm_b32 v221, v247, v246, s101\n\t"
        "v_mad_u32_u16 v233, v246, 32, %14 op_sel:[1,0,0,0]\n\t"
        "s_mov_b64 exec, -1\n\t"
        "s_waitcnt vmcnt(%31)\n\t"
        "v_add3_u32 v234, v248, v249, v250\n\t"
        "v_add3_u32 v237, v252, v253, v254\n\t"
        "v_add3_u32 v234, v234, v251, %19\n\t"
        "v_add3_u32 v237, v237, v255, %19\n\t"
        "v_cmp_ne_u32_sdwa s[84:85], v251, %20 src0_sel:BYTE_3 src1_sel:DWORD\n\t"
        "v_add_u32_dpp v234, v234, v234 quad_perm:[1,0,3,2] row_mask:0xf bank_mask:0xf\n\t"
        "v_add_u32_dpp v237, v237, v237 quad_perm:[1,0,3,2] row_mask:0xf bank_mask:0xf\n\t"
        "v_cmp_ne_u32_sdwa s[90:91], v255, %20 src0_sel:BYTE_3 src1_sel:DWORD\n\t"
        "v_add_u32_dpp v234, v234, v234 quad_perm:[2,3,0,1] row_mask:0xf bank_mask:0xf\n\t"
        "v_add_u32_dpp v237, v237, v237 quad_perm:[2,3,0,1] row_mask:0xf bank_mask:0xf\n\t"
        "s_nop 0\n\t"
        "v_add_u32_dpp v234, v234, v234 row_half_mirror row_mask:0xf bank_mask:0xf\n\t"
        "v_add_u32_dpp v237, v237, v237 row_half_mirror row_mask:0xf bank_mask:0xf\n\t"
        "v_cmp_gt_u32 s[68:69], 2, v234\n\t"
        "v_cmp_eq_u32 s[60:61], 1, v234\n\t"
        "v_cmp_gt_u32 s[70:71], 2, v237\n\t"
        "v_cmp_eq_u32 s[62:63], 1, v237\n\t"
        "s_and_b64 s[84:85], s[84:85], s[68:69]\n\t"
        "s_cselect_b64 s[68:69], 0, s[68:69]\n\t"
        "s_and_b64 s[60:61], s[60:61], s[68:69]\n\t"
        "s_not_b64 %2, s[68:69]\n\t"
        "s_or_b64 s[54:55], s[54:55], s[60:61]\n\t"
        "s_or_b64 s[54:55], s[54:55], %2\n\t"
        "s_and_b64 s[90:91], s[90:91], s[70:71]\n\t"
        "s_cselect_b64 s[70:71], 0, s[70:71]\n\t"
        "s_and_b64 s[62:63], s[62:63], s[70:71]\n\t"
        "s_not_b64 %3, s[70:71]\n\t"
        "s_or_b64 s[54:55], s[54:55], s[62:63]\n\t"
        "s_or_b64 s[54:55], s[54:55], %3\n\t"
        "s_mov_b64 exec, s[72:73]\n\t"
        "s_waitcnt lgkmcnt(3)\n\t"
        "v_min_u32 v204, s97, v204\n\t"
        "v_min_u32 v205, s97, v205\n\t"
        "v_min_u32 v206, s97, v206\n\t"
        "v_mad_u32_u24 v204, v192, s98, v204\n\t"
        "v_mad_u32_u24 v205, v193, s98, v205\n\t"
        "v_mad_u32_u24 v206, v194, s98, v206\n\t"
        "ds_write2st64_b32 v204, v216, v228 offset1:10\n\t"
        "s_mov_b64 exec, s[74:75]\n\t"
        "ds_write2st64_b32 v205, v217, v229 offset1:10\n\t"
        "s_mov_b64 exec, s[76:77]\n\t"
        "ds_write2st64_b32 v206, v218, v230 offset1:10\n\t"
        "s_mov_b64 exec, s[68:69]\n\t"
        "v_cmpx_ne_u32 s[84:85], -1, v248\n\t"
        "v_bfe_u32 v198, v251, 5, 3\n\t"
        "v_lshl_add_u32 v210, v198, 4, %21\n\t"
        "ds_add_rtn_u32 v210, v210, v191\n\t"
        "v_perm_b32 v222, v251, v248, s99\n\t"
        "v_mad_u32_u16 v234, v248, 32, %15 op_sel:[1,0,0,0]\n\t"
        "v_cmpx_ne_u32 s[86:87], -1, v249\n\t"
        "v_bfe_u32 v199, v251, 13, 3\n\t"
        "v_lshl_add_u32 v211, v199, 4, %21\n\t"
        "ds_add_rtn_u32 v211, v211, v191\n\t"
        "v_perm_b32 v223, v251, v249, s100\n\t"
        "v_mad_u32_u16 v235, v249, 32, %15 op_sel:[1,0,0,0]\n\t"
        "s_and_b64 exec, exec, s[52:53]\n\t"
        "v_cmpx_ne_u32 s[88:89], -1, v250\n\t"
        "v_bfe_u32 v200, v251, 21, 3\n\t"
        "v_lshl_add_u32 v212, v200, 4, %21\n\t"
        "ds_add_rtn_u32 v212, v212, v191\n\t"
        "v_perm_b32 v224, v251, v250, s101\n\t"
        "v_mad_u32_u16 v236, v250, 32, %15 op_sel:[1,0,0,0]\n\t"
        "s_mov_b64 exec, s[78:79]\n\t"
        "s_waitcnt lgkmcnt(6)\n\t"
        "v_min_u32 v207, s97, v207\n\t"
        "v_min_u32 v208, s97, v208\n\t"
        "v_min_u32 v209, s97, v209\n\t"
        "v_mad_u32_u24 v207, v195, s98, v207\n\t"
        "v_mad_u32_u24 v208, v196, s98, v208\n\t"
        "v_mad_u32_u24 v209, v197, s98, v209\n\t"
        "ds_write2st64_b32 v207, v219, v231 offset1:10\n\t"
        "s_mov_b64 exec, s[80:81]\n\t"
        "ds_write2st64_b32 v208, v220, v232 offset1:10\n\t"
        "s_mov_b64 exec, s[82:83]\n\t"
        "ds_write2st64_b32 v209, v221, v233 offset1:10\n\t"
        "s_mov_b64 exec, -1\n\t"
        "ds_read_b128 v[184:187], %22\n\t"
        "ds_read_b32 v183, %23\n\t"
        "s_mov_b64 exec, s[70:71]\n\t"
        "v_cmpx_ne_u32 s[90:91], -1, v252\n\t"
        "v_bfe_u32 v201, v255, 5, 3\n\t"
        "v_lshl_add_u32 v213, v201, 4, %21\n\t"
        "ds_add_rtn_u32 v213, v213, v191\n\t"
        "v_perm_b32 v225, v255, v252, s99\n\t"
        "v_mad_u32_u16 v237, v252, 32, %16 op_sel:[1,0,0,0]\n\t"
        "v_cmpx_ne_u32 s[92:93], -1, v253\n\t"
        "v_bfe_u32 v202, v255, 13, 3\n\t"
        "v_lshl_add_u32 v214, v202, 4, %21\n\t"
        "ds_add_rtn_u32 v214, v214, v191\n\t"
        "v_perm_b32 v226, v255, v253, s100\n\t"
        "v_mad_u32_u16 v238, v253, 32, %16 op_sel:[1,0,0,0]\n\t"
        "s_and_b64 exec, exec, s[52:53]\n\t"
        "v_cmpx_ne_u32 s[94:95], -1, v254\n\t"
        "v_bfe_u32 v203, v255, 21, 3\n\t"
        "v_lshl_add_u32 v215, v203, 4, %21\n\t"
        "ds_add_rtn_u32 v215, v215, v191\n\t"
        "v_perm_b32 v227, v255, v254, s101\n\t"
        "v_mad_u32_u16 v239, v254, 32, %16 op_sel:[1,0,0,0]\n\t"
        "s_cmp_eq_u64 s[54:55], 0\n\t"
        "s_cbranch_scc1 59f\n\t"
        "s_mov_b64 exec, s[56:57]\n\t"
        "s_cbranch_execz 40f\n\t"
        "v_bfe_u32 v188, %4, 7, 8\n\t"
        "v_mov_b32 v189, 1\n\t"
        "ds_write_b8 v188, v189 offset:256\n\t"
        "v_add_u32 %4, s96, %4\n\t"
        "s_or_b64 %0, %0, exec\n\t"
        "40:\n\t"
        "s_mov_b64 exec, %0\n\t"
        "s_cbranch_execz 50f\n\t"
        "global_load_dwordx4 v[240:243], %4, %17 sc1\n\t"
        "50:\n\t"
        "s_mov_b64 exec, s[58:59]\n\t"
        "s_cbranch_execz 41f\n\t"
        "v_bfe_u32 v188, %5, 7, 8\n\t"
        "v_mov_b32 v189, 1\n\t"
        "ds_write_b8 v188, v189 offset:256\n\t"
        "v_add_u32 %5, s96, %5\n\t"
        "s_or_b64 %1, %1, exec\n\t"
        "41:\n\t"
        "s_mov_b64 exec, %1\n\t"
        "s_cbranch_execz 51f\n\t"
        "global_load_dwordx4 v[244:247], %5, %17 sc1\n\t"
        "51:\n\t"
        "s_mov_b64 exec, s[60:61]\n\t"
        "s_cbranch_execz 42f\n\t"
        "v_bfe_u32 v188, %6, 7, 8\n\t"
        "v_mov_b32 v189, 1\n\t"
        "ds_write_b8 v188, v189 offset:256\n\t"
        "v_add_u32 %6, s96, %6\n\t"
        "s_or_b64 %2, %2, exec\n\t"
        "42:\n\t"
        "s_mov_b64 exec, %2\n\t"
        "s_cbranch_execz 52f\n\t"
        "global_load_dwordx4 v[248:251], %6, %17 sc1\n\t"
        "52:\n\t"
        "s_mov_b64 exec, s[62:63]\n\t"
        "s_cbranch_execz 43f\n\t"
        "v_bfe_u32 v188, %7, 7, 8\n\t"
        "v_mov_b32 v189, 1\n\t"
        "ds_write_b8 v188, v189 offset:256\n\t"
        "v_add_u32 %7, s96, %7\n\t"
        "s_or_b64 %3, %3, exec\n\t"
        "43:\n\t"
        "s_mov_b64 exec, %3\n\t"
        "s_cbranch_execz 53f\n\t"
        "global_load_dwordx4 v[252:255], %7, %17 sc1\n\t"
        "53:\n\t"
        "s_waitcnt lgkmcnt(0)\n\t"
        "59:\n\t"
        "s_mov_b64 exec, s[84:85]\n\t"
        "s_waitcnt lgkmcnt(8)\n\t"
        "v_min_u32 v210, s97, v210\n\t"
        "v_min_u32 v211, s97, v211\n\t"
        "v_min_u32 v212, s97, v212\n\t"
        "v_mad_u32_u24 v210, v198, s98, v210\n\t"
        "v_mad_u32_u24 v211, v199, s98, v211\n\t"
        "v_mad_u32_u24 v212, v200, s98, v212\n\t"
        "ds_write2st64_b32 v210, v222, v234 offset1:10\n\t"
        "s_mov_b64 exec, s[86:87]\n\t"
        "ds_write2st64_b32 v211, v223, v235 offset1:10\n\t"
        "s_mov_b64 exec, s[88:89]\n\t"
        "ds_write2st64_b32 v212, v224, v236 offset1:10\n\t"
        "s_mov_b64 exec, -1\n\t"
        "s_movk_i32 s96, 128\n\t"
        "s_mov_b32 m0, %29\n\t"
        "s_waitcnt lgkmcnt(6)\n\t"
        "v_max3_u32 v182, v184, v185, v186\n\t"
        "v_max3_u32 v182, v182, v187, v183\n\t"
        "v_cmp_eq_u32 vcc, -1, v182\n\t"
        "v_mad_u32_u16 v184, v184, s96, %24\n\t"
        "v_mad_u32_u16 v185, v185, s96, %25\n\t"
        "v_mad_u32_u16 v186, v186, s96, %24\n\t"
        "v_mad_u32_u16 v187, v187, s96, %25\n\t"
        "v_add_u32 v183, v183, %26\n\t"
        "s_cbranch_vccnz 60f\n\t"
        "global_load_lds_dwordx4 v184, %27\n\t"
        "s_addk_i32 m0, 1024\n\t"
        "s_nop 0\n\t"
        "global_load_lds_dwordx4 v185, %27\n\t"
        "s_addk_i32 m0, 1024\n\t"
        "s_nop 0\n\t"
        "global_load_lds_dwordx4 v186, %27\n\t"
        "s_addk_i32 m0, 1024\n\t"
        "s_nop 0\n\t"
        "global_load_lds_dwordx4 v187, %27\n\t"
        "s_addk_i32 m0, 1024\n\t"
        "s_nop 0\n\t"
        "global_load_lds_dwordx4 v183, %28\n\t"
        "s_mov_b32 %12, 1\n\t"
        "60:\n\t"
        "s_mov_b64 exec, s[90:91]\n\t"
        "s_waitcnt lgkmcnt(3)\n\t"
        "v_min_u32 v213, s97, v213\n\t"
        "v_min_u32 v214, s97, v214\n\t"
        "v_min_u32 v215, s97, v215\n\t"
        "v_mad_u32_u24 v213, v201, s98, v213\n\t"
        "v_mad_u32_u24 v214, v202, s98, v214\n\t"
        "v_mad_u32_u24 v215, v203, s98, v215\n\t"
        "ds_write2st64_b32 v213, v225, v237 offset1:10\n\t"
        "s_mov_b64 exec, s[92:93]\n\t"
        "ds_write2st64_b32 v214, v226, v238 offset1:10\n\t"
        "s_mov_b64 exec, s[94:95]\n\t"
        "ds_write2st64_b32 v215, v227, v239 offset1:10\n\t"
        "s_mov_b64 exec, -1\n\t"
        : "=&s"(pend[0]), "=&s"(pend[1]), "=&s"(pend[2]), "=&s"(pend[3]), "+v"(vo[0]), "+v"(vo[1]), "+v"(vo[2]),
          "+v"(vo[3]), "+{v[240:243]}"(cv[0]), "+{v[244:247]}"(cv[1]), "+{v[248:251]}"(cv[2]), "+{v[252:255]}"(cv[3]),
          "=&s"(early)
        : "v"(eb[0]), "v"(eb[1]), "v"(eb[2]), "v"(eb[3]), "s"(ib), "n"(OFF_L + LARR - 16), "v"(tag[0]), "v"(tag[1]),
          "v"(rq4), "v"(aLX + wave * LIST_BYTES), "v"(aLE + wave * LIST_BYTES), "v"(cx0), "v"(cx1), "v"(ce), "s"(x),
          "s"(ES), "s"(lds0 + OFF_R0 + wave * RING_SLOT), "n"(XAT == 2 ? 6 : 2), "n"(XAT == 2 ? 4 : 0)
        :
          "memory", "vcc", "scc", "m0", "v182", "v183", "v184", "v185", "v186", "v187", "v188", "v189", "v190",
          "v191", "v192", "v193", "v194", "v195", "v196", "v197", "v198", "v199", "v200", "v201", "v202", "v203",
          "v204", "v205", "v206", "v207", "v208", "v209", "v210", "v211", "v212", "v213", "v214", "v215", "v216",
          "v217", "v218", "v219", "v220", "v221", "v222", "v223", "v224", "v225", "v226", "v227", "v228", "v229",
          "v230", "v231", "v232", "v233", "v234", "v235", "v236", "v237", "v238", "v239", "s52", "s53", "s54", "s55",
          "s56", "s57", "s58", "s59", "s60", "s61", "s62", "s63", "s64", "s65", "s66", "s67", "s68", "s69", "s70",
          "s71", "s72", "s73", "s74", "s75", "s76", "s77", "s78", "s79", "s80", "s81", "s82", "s83", "s84", "s85",
          "s86", "s87", "s88", "s89", "s90", "s91", "s92", "s93", "s94", "s95", "s96", "s97", "s98", "s99", "s100",
          "s101");
  auto uni = [&]() {
#pragma unroll
    for (int k = 0; k < 4; ++k)
      pend[k] = ((unsigned long long)(unsigned)__builtin_amdgcn_readfirstlane((unsigned)(pend[k] >> 32)) << 32) |
                (unsigned)__builtin_amdgcn_readfirstlane((unsigned)pend[k]);
  };
  uni();
  early = __builtin_amdgcn_readfirstlane(early);
  just = early;
  while (true) {
    if (__builtin_expect(!early, 0)) {
      const u32x4 sx = ld128(aLX + wave * LIST_BYTES);
      const unsigned re = *(const LAS unsigned*)(size_t)(aLE + wave * LIST_BYTES);
      if (__ballot(max(max(max(sx[0], sx[1]), max(sx[2], sx[3])), re) == ~0u) == 0ull) {
        gather5(sx, re, lds0 + OFF_R0 + wave * RING_SLOT);
        early = 1u;
        just = 1u;
      }
    }
    if (__builtin_expect((pend[0] | pend[1] | pend[2] | pend[3]) == 0ull, 1) || --spin < 0) break;
    asm volatile(
        "s_mov_b32 s96, 0x800000\n\t"
        "s_mov_b32 s97, %17\n\t"
        "s_movk_i32 s98, 5120\n\t"
        "s_mov_b32 s99, 0x0c040100\n\t"
        "s_mov_b32 s100, 0x0c050100\n\t"
        "s_mov_b32 s101, 0x0c060100\n\t"
        "s_mov_b32 s52, 0x7f7f7f7f\n\t"
        "s_mov_b32 s53, 0x7f7f7f7f\n\t"
        "v_mov_b32 v191, 16\n\t"
        "s_waitcnt vmcnt(5)\n\t"
        "s_cmp_lg_u32 %21, 0\n\t"
        "s_cbranch_scc1 9f\n\t"
        "s_waitcnt vmcnt(0)\n\t"
        "9:\n\t"
        "v_add3_u32 v228, v240, v241, v242\n\t"
        "v_add3_u32 v229, v244, v245, v246\n\t"
        "v_add3_u32 v230, v248, v249, v250\n\t"
        "v_add3_u32 v231, v252, v253, v254\n\t"
        "v_add3_u32 v228, v228, v243, %18\n\t"
        "v_add3_u32 v229, v229, v247, %18\n\t"
        "v_add3_u32 v230, v230, v251, %18\n\t"
        "v_add3_u32 v231, v231, v255, %18\n\t"
        "v_add_u32_dpp v228, v228, v228 quad_perm:[1,0,3,2] row_mask:0xf bank_mask:0xf\n\t"
        "v_add_u32_dpp v229, v229, v229 quad_perm:[1,0,3,2] row_mask:0xf bank_mask:0xf\n\t"
        "v_add_u32_dpp v230, v230, v230 quad_perm:[1,0,3,2] row_mask:0xf bank_mask:0xf\n\t"
        "v_add_u32_dpp v231, v231, v231 quad_perm:[1,0,3,2] row_mask:0xf bank_mask:0xf\n\t"
        "v_add_u32_dpp v228, v228, v228 quad_perm:[2,3,0,1] row_mask:0xf bank_mask:0xf\n\t"
        "v_add_u32_dpp v229, v229, v229 quad_perm:[2,3,0,1] row_mask:0xf bank_mask:0xf\n\t"
        "v_add_u32_dpp v230, v230, v230 quad_perm:[2,3,0,1] row_mask:0xf bank_mask:0xf\n\t"
        "v_add_u32_dpp v231, v231, v231 quad_perm:[2,3,0,1] row_mask:0xf bank_mask:0xf\n\t"
        "v_add_u32_dpp v228, v228, v228 row_half_mirror row_mask:0xf bank_mask:0xf\n\t"
        "v_add_u32_dpp v229, v229, v229 row_half_mirror row_mask:0xf bank_mask:0xf\n\t"
        "v_add_u32_dpp v230, v230, v230 row_half_mirror row_mask:0xf bank_mask:0xf\n\t"
        "v_add_u32_dpp v231, v231, v231 row_half_mirror row_mask:0xf bank_mask:0xf\n\t"
        "v_cmp_ne_u32_sdwa s[72:73], v243, %19 src0_sel:BYTE_3 src1_sel:DWORD\n\t"
        "v_cmp_gt_u32 s[64:65], 2, v228\n\t"
        "v_cmp_eq_u32 s[56:57], 1, v228\n\t"
        "v_cmp_ne_u32_sdwa s[74:75], v247, %19 src0_sel:BYTE_3 src1_sel:DWORD\n\t"
        "v_cmp_gt_u32 s[66:67], 2, v229\n\t"
        "v_cmp_eq_u32 s[58:59], 1, v229\n\t"
        "v_cmp_ne_u32_sdwa s[76:77], v251, %19 src0_sel:BYTE_3 src1_sel:DWORD\n\t"
        "v_cmp_gt_u32 s[68:69], 2, v230\n\t"
        "v_cmp_eq_u32 s[60:61], 1, v230\n\t"
        "v_cmp_ne_u32_sdwa s[78:79], v255, %19 src0_sel:BYTE_3 src1_sel:DWORD\n\t"
        "v_cmp_gt_u32 s[70:71], 2, v231\n\t"
        "v_cmp_eq_u32 s[62:63], 1, v231\n\t"
        "s_and_b64 s[64:65], s[64:65], %0\n\t"
        "s_and_b64 s[72:73], s[72:73], s[64:65]\n\t"
        "s_cselect_b64 s[64:65], 0, s[64:65]\n\t"
        "s_and_b64 s[56:57], s[56:57], s[64:65]\n\t"
        "s_andn2_b64 %0, %0, s[64:65]\n\t"
        "s_cbranch_scc0 10f\n\t"
        "s_mov_b64 exec, %0\n\t"
        "global_load_dwordx4 v[240:243], %4, %16 sc1\n\t"
        "10:\n\t"
        "s_and_b64 s[66:67], s[66:67], %1\n\t"
        "s_and_b64 s[74:75], s[74:75], s[66:67]\n\t"
        "s_cselect_b64 s[66:67], 0, s[66:67]\n\t"
        "s_and_b64 s[58:59], s[58:59], s[66:67]\n\t"
        "s_andn2_b64 %1, %1, s[66:67]\n\t"
        "s_cbranch_scc0 11f\n\t"
        "s_mov_b64 exec, %1\n\t"
        "global_load_dwordx4 v[244:247], %5, %16 sc1\n\t"
        "11:\n\t"
        "s_and_b64 s[68:69], s[68:69], %2\n\t"
        "s_and_b64 s[76:77], s[76:77], s[68:69]\n\t"
        "s_cselect_b64 s[68:69], 0, s[68:69]\n\t"
        "s_and_b64 s[60:61], s[60:61], s[68:69]\n\t"
        "s_andn2_b64 %2, %2, s[68:69]\n\t"
        "s_cbranch_scc0 12f\n\t"
        "s_mov_b64 exec, %2\n\t"
        "global_load_dwordx4 v[248:251], %6, %16 sc1\n\t"
        "12:\n\t"
        "s_and_b64 s[70:71], s[70:71], %3\n\t"
        "s_and_b64 s[78:79], s[78:79], s[70:71]\n\t"
        "s_cselect_b64 s[70:71], 0, s[70:71]\n\t"
        "s_and_b64 s[62:63], s[62:63], s[70:71]\n\t"
        "s_andn2_b64 %3, %3, s[70:71]\n\t"
        "s_cbranch_scc0 13f\n\t"
        "s_mov_b64 exec, %3\n\t"
        "global_load_dwordx4 v[252:255], %7, %16 sc1\n\t"
        "13:\n\t"
        "s_mov_b64 s[54:55], 0\n\t"
        "s_mov_b64 s[72:73], 0\n\t"
        "s_mov_b64 exec, s[64:65]\n\t"
        "s_cbranch_execz 20f\n\t"
        "v_cmpx_ne_u32 s[72:73], -1, v240\n\t"
        "v_bfe_u32 v192, v243, 5, 3\n\t"
        "v_lshl_add_u32 v204, v192, 4, %20\n\t"
        "ds_add_rtn_u32 v204, v204, v191\n\t"
        "v_perm_b32 v216, v243, v240, s99\n\t"
        "v_mad_u32_u16 v228, v240, 32, %12 op_sel:[1,0,0,0]\n\t"
        "v_cmpx_ne_u32 s[74:75], -1, v241\n\t"
        "v_bfe_u32 v193, v243, 13, 3\n\t"
        "v_lshl_add_u32 v205, v193, 4, %20\n\t"
        "ds_add_rtn_u32 v205, v205, v191\n\t"
        "v_perm_b32 v217, v243, v241, s100\n\t"
        "v_mad_u32_u16 v229, v241, 32, %12 op_sel:[1,0,0,0]\n\t"
        "s_and_b64 exec, exec, s[52:53]\n\t"
        "v_cmpx_ne_u32 s[76:77], -1, v242\n\t"
        "v_bfe_u32 v194, v243, 21, 3\n\t"
        "v_lshl_add_u32 v206, v194, 4, %20\n\t"
        "ds_add_rtn_u32 v206, v206, v191\n\t"
        "v_perm_b32 v218, v243, v242, s101\n\t"
        "v_mad_u32_u16 v230, v242, 32, %12 op_sel:[1,0,0,0]\n\t"
        "s_or_b64 s[54:55], s[54:55], s[56:57]\n\t"
        "20:\n\t"
        "s_mov_b64 s[78:79], 0\n\t"
        "s_mov_b64 exec, s[66:67]\n\t"
        "s_cbranch_execz 21f\n\t"
        "v_cmpx_ne_u32 s[78:79], -1, v244\n\t"
        "v_bfe_u32 v195, v247, 5, 3\n\t"
        "v_lshl_add_u32 v207, v195, 4, %20\n\t"
        "ds_add_rtn_u32 v207, v207, v191\n\t"
        "v_perm_b32 v219, v247, v244, s99\n\t"
        "v_mad_u32_u16 v231, v244, 32, %13 op_sel:[1,0,0,0]\n\t"
        "v_cmpx_ne_u32 s[80:81], -1, v245\n\t"
        "v_bfe_u32 v196, v247, 13, 3\n\t"
        "v_lshl_add_u32 v208, v196, 4, %20\n\t"
        "ds_add_rtn_u32 v208, v208, v191\n\t"
        "v_perm_b32 v220, v247, v245, s100\n\t"
        "v_mad_u32_u16 v232, v245, 32, %13 op_sel:[1,0,0,0]\n\t"
        "s_and_b64 exec, exec, s[52:53]\n\t"
        "v_cmpx_ne_u32 s[82:83], -1, v246\n\t"
        "v_bfe_u32 v197, v247, 21, 3\n\t"
        "v_lshl_add_u32 v209, v197, 4, %20\n\t"
        "ds_add_rtn_u32 v209, v209, v191\n\t"
        "v_perm_b32 v221, v247, v246, s101\n\t"
        "v_mad_u32_u16 v233, v246, 32, %13 op_sel:[1,0,0,0]\n\t"
        "s_or_b64 s[54:55], s[54:55], s[58:59]\n\t"
        "21:\n\t"
        "s_mov_b64 s[84:85], 0\n\t"
        "s_mov_b64 exec, s[68:69]\n\t"
        "s_cbranch_execz 22f\n\t"
        "v_cmpx_ne_u32 s[84:85], -1, v248\n\t"
        "v_bfe_u32 v198, v251, 5, 3\n\t"
        "v_lshl_add_u32 v210, v198, 4, %20\n\t"
        "ds_add_rtn_u32 v210, v210, v191\n\t"
        "v_perm_b32 v222, v251, v248, s99\n\t"
        "v_mad_u32_u16 v234, v248, 32, %14 op_sel:[1,0,0,0]\n\t"
        "v_cmpx_ne_u32 s[86:87], -1, v249\n\t"
        "v_bfe_u32 v199, v251, 13, 3\n\t"
        "v_lshl_add_u32 v211, v199, 4, %20\n\t"
        "ds_add_rtn_u32 v211, v211, v191\n\t"
        "v_perm_b32 v223, v251, v249, s100\n\t"
        "v_mad_u32_u16 v235, v249, 32, %14 op_sel:[1,0,0,0]\n\t"
        "s_and_b64 exec, exec, s[52:53]\n\t"
        "v_cmpx_ne_u32 s[88:89], -1, v250\n\t"
        "v_bfe_u32 v200, v251, 21, 3\n\t"
        "v_lshl_add_u32 v212, v200, 4, %20\n\t"
        "ds_add_rtn_u32 v212, v212, v191\n\t"
        "v_perm_b32 v224, v251, v250, s101\n\t"
        "v_mad_u32_u16 v236, v250, 32, %14 op_sel:[1,0,0,0]\n\t"
        "s_or_b64 s[54:55], s[54:55], s[60:61]\n\t"
        "22:\n\t"
        "s_mov_b64 s[90:91], 0\n\t"
        "s_mov_b64 exec, s[70:71]\n\t"
        "s_cbranch_execz 23f\n\t"
        "v_cmpx_ne_u32 s[90:91], -1, v252\n\t"
        "v_bfe_u32 v201, v255, 5, 3\n\t"
        "v_lshl_add_u32 v213, v201, 4, %20\n\t"
        "ds_add_rtn_u32 v213, v213, v191\n\t"
        "v_perm_b32 v225, v255, v252, s99\n\t"
        "v_mad_u32_u16 v237, v252, 32, %15 op_sel:[1,0,0,0]\n\t"
        "v_cmpx_ne_u32 s[92:93], -1, v253\n\t"
        "v_bfe_u32 v202, v255, 13, 3\n\t"
        "v_lshl_add_u32 v214, v202, 4, %20\n\t"
        "ds_add_rtn_u32 v214, v214, v191\n\t"
        "v_perm_b32 v226, v255, v253, s100\n\t"
        "v_mad_u32_u16 v238, v253, 32, %15 op_sel:[1,0,0,0]\n\t"
        "s_and_b64 exec, exec, s[52:53]\n\t"
        "v_cmpx_ne_u32 s[94:95], -1, v254\n\t"
        "v_bfe_u32 v203, v255, 21, 3\n\t"
        "v_lshl_add_u32 v215, v203, 4, %20\n\t"
        "ds_add_rtn_u32 v215, v215, v191\n\t"
        "v_perm_b32 v227, v255, v254, s101\n\t"
        "v_mad_u32_u16 v239, v254, 32, %15 op_sel:[1,0,0,0]\n\t"
        "s_or_b64 s[54:55], s[54:55], s[62:63]\n\t"
        "23:\n\t"
        "s_cmp_eq_u64 s[54:55], 0\n\t"
        "s_cbranch_scc1 59f\n\t"
        "s_mov_b64 exec, s[56:57]\n\t"
        "s_cbranch_execz 40f\n\t"
        "v_bfe_u32 v188, %4, 7, 8\n\t"
        "v_lshrrev_b32 v189, 23, %4\n\t"
        "v_add_u32 v189, 1, v189\n\t"
        "ds_write_b8 v188, v189 offset:256\n\t"
        "v_add_u32 %4, s96, %4\n\t"
        "v_mov_b32 v190, 0\n\t"
        "v_cmpx_eq_u32 vcc, 2, v189\n\t"
        "ds_write_b32 v190, v191 offset:160\n\t"
        "s_andn2_b64 exec, s[56:57], vcc\n\t"
        "s_cbranch_execz 40f\n\t"
        "s_or_b64 %0, %0, exec\n\t"
        "global_load_dwordx4 v[240:243], %4, %16 sc1\n\t"
        "40:\n\t"
        "s_mov_b64 exec, s[58:59]\n\t"
        "s_cbranch_execz 41f\n\t"
        "v_bfe_u32 v188, %5, 7, 8\n\t"
        "v_lshrrev_b32 v189, 23, %5\n\t"
        "v_add_u32 v189, 1, v189\n\t"
        "ds_write_b8 v188, v189 offset:256\n\t"
        "v_add_u32 %5, s96, %5\n\t"
        "v_mov_b32 v190, 0\n\t"
        "v_cmpx_eq_u32 vcc, 2, v189\n\t"
        "ds_write_b32 v190, v191 offset:160\n\t"
        "s_andn2_b64 exec, s[58:59], vcc\n\t"
        "s_cbranch_execz 41f\n\t"
        "s_or_b64 %1, %1, exec\n\t"
        "global_load_dwordx4 v[244:247], %5, %16 sc1\n\t"
        "41:\n\t"
        "s_mov_b64 exec, s[60:61]\n\t"
        "s_cbranch_execz 42f\n\t"
        "v_bfe_u32 v188, %6, 7, 8\n\t"
        "v_lshrrev_b32 v189, 23, %6\n\t"
        "v_add_u32 v189, 1, v189\n\t"
        "ds_write_b8 v188, v189 offset:256\n\t"
        "v_add_u32 %6, s96, %6\n\t"
        "v_mov_b32 v190, 0\n\t"
        "v_cmpx_eq_u32 vcc, 2, v189\n\t"
        "ds_write_b32 v190, v191 offset:160\n\t"
        "s_andn2_b64 exec, s[60:61], vcc\n\t"
        "s_cbranch_execz 42f\n\t"
        "s_or_b64 %2, %2, exec\n\t"
        "global_load_dwordx4 v[248:251], %6, %16 sc1\n\t"
        "42:\n\t"
        "s_mov_b64 exec, s[62:63]\n\t"
        "s_cbranch_execz 43f\n\t"
        "v_bfe_u32 v188, %7, 7, 8\n\t"
        "v_lshrrev_b32 v189, 23, %7\n\t"
        "v_add_u32 v189, 1, v189\n\t"
        "ds_write_b8 v188, v189 offset:256\n\t"
        "v_add_u32 %7, s96, %7\n\t"
        "v_mov_b32 v190, 0\n\t"
        "v_cmpx_eq_u32 vcc, 2, v189\n\t"
        "ds_write_b32 v190, v191 offset:160\n\t"
        "s_andn2_b64 exec, s[62:63], vcc\n\t"
        "s_cbranch_execz 43f\n\t"
        "s_or_b64 %3, %3, exec\n\t"
        "global_load_dwordx4 v[252:255], %7, %16 sc1\n\t"
        "43:\n\t"
        "59:\n\t"
        "s_waitcnt lgkmcnt(0)\n\t"
        "s_mov_b64 exec, s[72:73]\n\t"
        "s_cbranch_execz 30f\n\t"
        "v_min_u32 v204, s97, v204\n\t"
        "v_min_u32 v205, s97, v205\n\t"
        "v_min_u32 v206, s97, v206\n\t"
        "v_mad_u32_u24 v204, v192, s98, v204\n\t"
        "v_mad_u32_u24 v205, v193, s98, v205\n\t"
        "v_mad_u32_u24 v206, v194, s98, v206\n\t"
        "ds_write2st64_b32 v204, v216, v228 offset1:10\n\t"
        "s_mov_b64 exec, s[74:75]\n\t"
        "ds_write2st64_b32 v205, v217, v229 offset1:10\n\t"
        "s_mov_b64 exec, s[76:77]\n\t"
        "ds_write2st64_b32 v206, v218, v230 offset1:10\n\t"
        "30:\n\t"
        "s_mov_b64 exec, s[78:79]\n\t"
        "s_cbranch_execz 31f\n\t"
        "v_min_u32 v207, s97, v207\n\t"
        "v_min_u32 v208, s97, v208\n\t"
        "v_min_u32 v209, s97, v209\n\t"
        "v_mad_u32_u24 v207, v195, s98, v207\n\t"
        "v_mad_u32_u24 v208, v196, s98, v208\n\t"
        "v_mad_u32_u24 v209, v197, s98, v209\n\t"
        "ds_write2st64_b32 v207, v219, v231 offset1:10\n\t"
        "s_mov_b64 exec, s[80:81]\n\t"
        "ds_write2st64_b32 v208, v220, v232 offset1:10\n\t"
        "s_mov_b64 exec, s[82:83]\n\t"
        "ds_write2st64_b32 v209, v221, v233 offset1:10\n\t"
        "31:\n\t"
        "s_mov_b64 exec, s[84:85]\n\t"
        "s_cbranch_execz 32f\n\t"
        "v_min_u32 v210, s97, v210\n\t"
        "v_min_u32 v211, s97, v211\n\t"
        "v_min_u32 v212, s97, v212\n\t"
        "v_mad_u32_u24 v210, v198, s98, v210\n\t"
        "v_mad_u32_u24 v211, v199, s98, v211\n\t"
        "v_mad_u32_u24 v212, v200, s98, v212\n\t"
        "ds_write2st64_b32 v210, v222, v234 offset1:10\n\t"
        "s_mov_b64 exec, s[86:87]\n\t"
        "ds_write2st64_b32 v211, v223, v235 offset1:10\n\t"
        "s_mov_b64 exec, s[88:89]\n\t"
        "ds_write2st64_b32 v212, v224, v236 offset1:10\n\t"
        "32:\n\t"
        "s_mov_b64 exec, s[90:91]\n\t"
        "s_cbranch_execz 33f\n\t"
        "v_min_u32 v213, s97, v213\n\t"
        "v_min_u32 v214, s97, v214\n\t"
        "v_min_u32 v215, s97, v215\n\t"
        "v_mad_u32_u24 v213, v201, s98, v213\n\t"
        "v_mad_u32_u24 v214, v202, s98, v214\n\t"
        "v_mad_u32_u24 v215, v203, s98, v215\n\t"
        "ds_write2st64_b32 v213, v225, v237 offset1:10\n\t"
        "s_mov_b64 exec, s[92:93]\n\t"
        "ds_write2st64_b32 v214, v226, v238 offset1:10\n\t"
        "s_mov_b64 exec, s[94:95]\n\t"
        "ds_write2st64_b32 v215, v227, v239 offset1:10\n\t"
        "33:\n\t"
        "s_mov_b64 exec, -1\n\t"
        : "+&s"(pend[0]), "+&s"(pend[1]), "+&s"(pend[2]), "+&s"(pend[3]), "+v"(vo[0]), "+v"(vo[1]), "+v"(vo[2]),
          "+v"(vo[3]), "+{v[240:243]}"(cv[0]), "+{v[244:247]}"(cv[1]), "+{v[248:251]}"(cv[2]), "+{v[252:255]}"(cv[3])
        : "v"(eb[0]), "v"(eb[1]), "v"(eb[2]), "v"(eb[3]), "s"(ib), "n"(OFF_L + LARR - 16), "v"(tag[0]), "v"(tag[1]),
          "v"(rq4), "s"(just)
        :
          "memory", "vcc", "scc", "v188", "v189", "v190", "v191", "v192", "v193", "v194", "v195", "v196", "v197",
          "v198", "v199", "v200", "v201", "v202", "v203", "v204", "v205", "v206", "v207", "v208", "v209", "v210",
          "v211", "v212", "v213", "v214", "v215", "v216", "v217", "v218", "v219", "v220", "v221", "v222", "v223",
          "v224", "v225", "v226", "v227", "v228", "v229", "v230", "v231", "v232", "v233", "v234", "v235", "v236",
          "v237", "v238", "v239", "s52", "s53", "s54", "s55", "s56", "s57", "s58", "s59", "s60", "s61", "s62", "s63",
          "s64", "s65", "s66", "s67", "s68", "s69", "s70", "s71", "s72", "s73", "s74", "s75", "s76", "s77", "s78",
          "s79", "s80", "s81", "s82", "s83", "s84", "s85", "s86", "s87", "s88", "s89", "s90", "s91", "s92", "s93",
          "s94", "s95", "s96", "s97", "s98", "s99", "s100", "s101");
    uni();
    just = 0u;
  }
  if (wave == 3) wload(wu_g, 18);
  *(LAS unsigned*)((LAS char*)smem + OFF_CNT + 128 + wave * 4) = early;
  asm volatile("s_waitcnt lgkmcnt(0)\n\ts_barrier" ::: "memory");
  if (wave >= 4) __builtin_amdgcn_s_setprio(1);
  const u32x4 cw = *(const LAS u32x4*)((LAS char*)smem + OFF_CNT + rid * 16);
  const unsigned ef = *(const LAS unsigned*)((LAS char*)smem + OFF_CNT + 128 + rid * 4);
  unsigned slowf = *(const LAS unsigned*)((LAS char*)smem + OFF_CNT + 160);
  asm volatile("" : "+v"(slowf));
  const int key = (int)((cw[0] + cw[1] + cw[2] + cw[3]) | rid);
  int gb;
  {
    unsigned acc = 0u;
#define RK(k) acc = __builtin_amdgcn_alignbit(acc, (unsigned)(__builtin_amdgcn_update_dpp(0, key, 0x120 + k, 0xf, 0xf, false) - key), 31)
    RK(1); RK(2); RK(3); RK(4); RK(5); RK(6); RK(7);
#undef RK
    const int want = RANKOF(wave);
    gb = __builtin_ctzll(__ballot(__builtin_popcount(acc) == want)) & 7;
  }
  const unsigned ring0 = lds0 + OFF_R0 + gb * RING_SLOT;
  const unsigned la = l0 + gb * LIST_BYTES;
  bf16x8 XAf[2][2];
#pragma unroll
  for (int q = 0; q < 4; ++q) XAf[q >> 1][q & 1] = *(const LAS bf16x8*)(size_t)(ring0 + ringd + q * 1024 + lane * 16);
  int cnt = (int)(__builtin_amdgcn_readlane(key, gb) - 4u * l0 - 24u) >> 4;
  const unsigned cmn = __builtin_amdgcn_readlane(min(min(cw[0], cw[1]), min(cw[2], cw[3])), gb) & ~15u;
  const unsigned cmx = __builtin_amdgcn_readlane(max(max(cw[0], cw[1]), max(cw[2], cw[3])), gb) & ~15u;
  const bool slow = cnt > LCAP || cmx > l0 + LARR - 16 || __builtin_amdgcn_readfirstlane(slowf) != 0u;
  const unsigned cg = (bk * 8 + gb) * 32;
  aLX += gb * LIST_BYTES; aLE += gb * LIST_BYTES; aLS4 += gb * LIST_BYTES; aLS1 += gb * LIST_BYTES;
  auto issue_gather = [&](int tile, int rs) {
    gather5(ld128(aLX + tile * 128), *(const LAS unsigned*)(size_t)(aLE + tile * 128), ring0 + rs * ringd);
  };
  auto pads = [&](int cnt, int lane) __attribute__((always_inline)) {
    const int u = cnt + lane;
    const unsigned c = cg + ((u >> 2) & 7) + 8 * (u & 3), n = nodeof(c);
    const unsigned padA = n | (__builtin_amdgcn_readlane(c, __builtin_ctzll(__ballot((n * M) >> 16 != c))) & 0xffu) << 16;
    const unsigned pa = la + u * 4;
    *(LAS unsigned*)(size_t)pa = padA;
    *(LAS unsigned*)(size_t)(pa + 256) = padA;
    *(LAS unsigned*)(size_t)(pa + LARR) = 0u;
    *(LAS unsigned*)(size_t)(pa + LARR + 256) = 0u;
  };
  constexpr int GEND = 64 + NCH;
  int gs = GEND, gp = 0;
  auto gen = [&]() __attribute__((always_inline)) {
    int l2, n = 0;
    asm volatile("v_mbcnt_lo_u32_b32 %0, -1, 0\n\tv_mbcnt_hi_u32_b32 %0, -1, %0" : "=v"(l2));
    const LAS unsigned char* xf = (const LAS unsigned char*)smem + OFF_XF;
    auto put = [&](bool ok, unsigned t, unsigned c, int cc) {
      ok = ok && (int)(c >> 5) == gb;
      const unsigned long long bm = __ballot(ok);
      const unsigned pa = la + 4u * (n + __builtin_amdgcn_mbcnt_hi((unsigned)(bm >> 32), __builtin_amdgcn_mbcnt_lo((unsigned)bm, 0u)));
      if (ok) {
        *(LAS unsigned*)(size_t)pa = (t & 0xffffu) | (c << 16);
        *(LAS unsigned*)(size_t)(pa + LARR) = ((unsigned)(cc * chunk) + (t >> 16)) * 32u;
      }
      n += __builtin_popcountll(bm);
    };
#pragma unroll 1
    while (gs < GEND && n <= LCAP - 192) {
      if (gs < 64) {
        const int cc = (gs & 31) * 8 + (l2 >> 3);
        const unsigned* pp = reinterpret_cast<const unsigned*>(ib + (gs >> 5) * IEXT + (gs & 31) * 1024 + l2 * 16);
        const bool live = gs < 32 || xf[cc] != 0;
        const unsigned m = ldd(pp + 3);
#pragma unroll 1
        for (int j = 0; j < 3; ++j) {
          const unsigned t = ldd(pp + j);
          put(live && t != ~0u && (j < 2 || (l2 & 7) != 7), t, (m >> (8 * j)) & 0xffu, cc);
        }
        ++gs;
      } else {
        const int c2 = gs - 64 + l2;
        const unsigned long long fm = __ballot(c2 < NCH && xf[min(c2, NCH - 1)] == 2);
        if (fm == 0ull) { gs = min(gs + 64, GEND); continue; }
        gs += __builtin_ctzll(fm);
        const int cc = gs - 64;
        const unsigned long long ds = ldq(FL + 512 + bk * NCH + cc);
        const int len = __builtin_amdgcn_readfirstlane((int)(ds >> 32));
        const unsigned long long* tp = TS + (unsigned)ds;
#pragma unroll 1
        for (; gp < len && n <= LCAP - 64; gp += 64) {
          const unsigned long long t = ldq(tp + min(gp + l2, len - 1));
          put(gp + l2 < len, (unsigned)t, (unsigned)(t >> 32), cc);
        }
        if (gp >= len) { gp = 0; ++gs; }
      }
    }
    __builtin_amdgcn_s_waitcnt(0x0F70);
    pads(n, l2);
    return n;
  };
  bool g1 = false;
  if (__builtin_expect(!slow, 1)) {
    const bool quick = __builtin_amdgcn_readlane(ef, gb) != 0u && cmn >= l0 + 256u;
    const unsigned cl = *(const LAS unsigned*)((LAS char*)smem + OFF_CNT + gb * 16 + (lane & 3) * 4);
    if (quick && wave >= G1W) {
      const u32x4 sx = ld128(aLX + 128);
      const unsigned re = *(const LAS unsigned*)(size_t)(aLE + 128);
      asm volatile("" : "+v"(XAf[0][0]), "+v"(XAf[0][1]), "+v"(XAf[1][0]), "+v"(XAf[1][1]));
      gather5(sx, re, ring0 + ringd);
    }
    unsigned q = cmn + gb * LIST_BYTES;
#pragma unroll 1
    for (unsigned i = cmn; i < cmx; i += 256u) {
      const unsigned pp = i + lane * 4u;
      const unsigned ea = *(const LAS unsigned*)(size_t)(pp + gb * LIST_BYTES);
      const unsigned eb_ = *(const LAS unsigned*)(size_t)(pp + gb * LIST_BYTES + LARR);
      const unsigned long long bm = __ballot(pp < cl);
      const unsigned pa = q + 4u * __builtin_amdgcn_mbcnt_hi((unsigned)(bm >> 32), __builtin_amdgcn_mbcnt_lo((unsigned)bm, 0u));
      if (pp < cl) {
        *(LAS unsigned*)(size_t)pa = ea;
        *(LAS unsigned*)(size_t)(pa + LARR) = eb_;
      }
      q += 4u * __builtin_popcountll(bm);
    }
    pads(cnt, lane);
    g1 = quick && wave >= G1W;
  }
  if (!g1) asm volatile("s_waitcnt vmcnt(0)" ::: "memory");
#pragma unroll
  for (int ct = 0; ct < 2; ++ct)
#pragma unroll
    for (int m = 6; m < 15; ++m)
      if ((HELD >> m) & 1) wr[ct][m] = wl[widx(m, ct) * 64];
#pragma unroll
  for (int ct = 0; ct < 2; ++ct)
#pragma unroll
    for (int m = 6; m < 15; ++m)
      if ((HELD >> m) & 1) asm volatile("" : "+v"(wr[ct][m]));
#pragma unroll
  for (int t = 0; t < 4; ++t) fo[t] += gb * RING_SLOT;
  foe += gb * RING_SLOT;
  cS -= gb * 32 * TPITCH;
  cSt -= gb * 32 * TPITCH;
  asm volatile("s_waitcnt vmcnt(5)\n\ts_barrier"
               : "+v"(fo[0]), "+v"(fo[1]), "+v"(fo[2]), "+v"(fo[3]), "+v"(foe), "+v"(cS), "+v"(cSt) :: "memory");
  if (!g1) {
    if (__builtin_expect(slow, 0)) {
      gs = 0;
      cnt = gen();
    }
    if (__builtin_expect(slow || __builtin_amdgcn_readlane(ef, gb) == 0u, 0)) issue_gather(0, 0);
    asm volatile("" : "+v"(XAf[0][0]), "+v"(XAf[0][1]), "+v"(XAf[1][0]), "+v"(XAf[1][1]));
    issue_gather(1, 1);
  }
  const int gbin = bk * 8 + wave;
  auto rd128 = [&](unsigned a) { return *(const LAS bf16x8*)(size_t)a; };
  auto trow = [&](unsigned a, unsigned c) {
    unsigned t;
    asm("v_mad_u32_u16 %0, %1, %2, %3 op_sel:[1,0,0,0]" : "=v"(t) : "v"(a), "s"(TPITCH), "v"(c));
    return t;
  };
  auto load_S = [&](const u32x4 ss) {
    const s16x4 a0 = __builtin_amdgcn_ds_read_tr16_b64_v4i16((LAS s16x4*)(size_t)trow(ss[0], cS));
    const s16x4 a1 = __builtin_amdgcn_ds_read_tr16_b64_v4i16((LAS s16x4*)(size_t)trow(ss[1], cS));
    const s16x4 a2 = __builtin_amdgcn_ds_read_tr16_b64_v4i16((LAS s16x4*)(size_t)trow(ss[2], cS));
    const s16x4 a3 = __builtin_amdgcn_ds_read_tr16_b64_v4i16((LAS s16x4*)(size_t)trow(ss[3], cS));
    S[0] = __builtin_shufflevector(a0, a1, 0, 1, 2, 3, 4, 5, 6, 7);
    S[1] = __builtin_shufflevector(a2, a3, 0, 1, 2, 3, 4, 5, 6, 7);
  };
  auto wf = [&](int m, int ct) { return ((HELD >> m) & 1) ? wr[ct][m] : b[(m - 6) % 3]; };

  auto ew = [&](int m, const f32x16& Gc, const f32x16& Vc, const f32x16& Gn, const f32x16& Vp) {
    if (m < 2) {
      const int i = 14 + m;
      pq[i] = mulf(i, Vp[i], rq[i]);
      if (m == 1) pn[1][3] = pack_bf16(pq[14], pq[15]);
    } else if (m == 2) {
      pq[0] = mulf(0, Vc[0], rq[0]);
      pq[1] = mulf(1, Vc[1], rq[1]);
      pn[0][0] = pack_bf16(pq[0], pq[1]);
    } else {
      const int i = m - 1;
      pq[i] = mulf(i, Vc[i], rq[i]);
      if (i & 1) pn[i >> 3][(i >> 1) & 3] = pack_bf16(pq[i - 1], pq[i]);
    }
    if (m == 0) pd[0][0] = pack_bf16(rq[0], rq[1]);
    else if ((m & 1) == 0) { const int i = m + 1; pd[i >> 3][(i >> 1) & 3] = pack_bf16(rq[i - 1], rq[i]); }
    if (m < 14) {
      const int i = m + 2;
      rq[i] = __builtin_amdgcn_rcpf(add1(tq[i]));
    } else {
      rq[0] = __builtin_amdgcn_rcpf(add1(tq[0]));
      rq[1] = __builtin_amdgcn_rcpf(add1(tq[1]));
    }
    if (m < 13) tq[m + 3] = __builtin_amdgcn_exp2f(Gc[m + 3]);
    else if (m == 13) { tq[0] = __builtin_amdgcn_exp2f(Gn[0]); tq[1] = __builtin_amdgcn_exp2f(Gn[1]); }
    else tq[2] = __builtin_amdgcn_exp2f(Gn[2]);
  };

  while (true) {
    const int ntiles = (cnt + 31) >> 5;
    asm volatile("s_waitcnt vmcnt(5)" ::: "memory");
    {
      const unsigned sl = *(const LAS unsigned short*)(size_t)aLS1;
#pragma unroll
      for (int k = 0; k < 4; ++k) as[k] = rd128(fo[k]);
      ae = rd128(foe);
      St[0] = rd128(sl * TPITCH + cSt);
      St[1] = rd128(sl * TPITCH + cSt + 16);
      G0 = MFMA32(St[0], XAf[0][0], zero16);
      G0 = MFMA32(St[1], XAf[1][0], G0);
      G0 = MFMA32(ae, wl[widx(6, 0) * 64], G0);
#pragma unroll
      for (int k = 0; k < 4; ++k) G0 = MFMA32(as[k], wl[widx(7 + k, 0) * 64], G0);
      V0 = MFMA32(as[0], wl[widx(11, 0) * 64], zero16);
#pragma unroll
      for (int k = 1; k < 4; ++k) V0 = MFMA32(as[k], wl[widx(11 + k, 0) * 64], V0);
      asm volatile("s_waitcnt lgkmcnt(0)" ::: "memory");
      issue_gather(2, 0);
      pipe_zero();
      tq[0] = __builtin_amdgcn_exp2f(G0[0]);
      tq[1] = __builtin_amdgcn_exp2f(G0[1]);
      tq[2] = __builtin_amdgcn_exp2f(G0[2]);
      rq[0] = __builtin_amdgcn_rcpf(add1(tq[0]));
      rq[1] = __builtin_amdgcn_rcpf(add1(tq[1]));
      SB();
    }
    unsigned &lS4 = aLS4, &lS1 = aLS1, &lX = aLX, &lE = aLE;
    int adv = 0;
    auto body = [&](const int par) {
      const unsigned lo = par * 128;
      const unsigned ro = (1 - par) * ringd;
      {
        u32x4 ss;
        unsigned sl;
#pragma unroll
        for (int m = 0; m < 15; ++m) {
          if (m == 0) Y[1] = MFMA32(__builtin_bit_cast(bf16x8, pd[0]), S[0], Y[1]);
          else if (m == 1) Y[3] = MFMA32(__builtin_bit_cast(bf16x8, pn[0]), S[0], Y[3]);
          else if (m == 2) Y[1] = MFMA32(__builtin_bit_cast(bf16x8, pd[1]), S[1], Y[1]);
          else if (m == 3) Y[3] = MFMA32(__builtin_bit_cast(bf16x8, pn[1]), S[1], Y[3]);
          else if (m == 4) G1 = MFMA32(St[0], XAf[0][1], zero16);
          else if (m == 5) G1 = MFMA32(St[1], XAf[1][1], G1);
          else if (m == 6) G1 = MFMA32(ae, wf(m, 1), G1);
          else if (m < 11) G1 = MFMA32(as[m - 7], wf(m, 1), G1);
          else if (m == 11) V1 = MFMA32(as[0], wf(m, 1), zero16);
          else V1 = MFMA32(as[m - 11], wf(m, 1), V1);
          SB();
          if (m == 0) { ss = ld128(lS4 + lo); sl = *(const LAS unsigned short*)(size_t)(lS1 + lo + 128); }
          if (m >= 3 && m < 12 && !((HELD >> (m + 3)) & 1)) b[(m - 3) % 3] = wl[widx(m + 3, 1) * 64];
          if (m == 4) load_S(ss);
          if (m == 5) { St[0] = rd128(sl * TPITCH + cSt); St[1] = rd128(sl * TPITCH + cSt + 16); }
          if (m == 6) { asm volatile("s_waitcnt vmcnt(5)" ::: "memory"); ae = rd128(foe + ro); }
          if (m >= 11) as[m - 11] = rd128(fo[m - 11] + ro);
          ew(m, G0, V0, G1, V1);
          SB();
        }
      }
      {
        u32x4 sx;
        unsigned re;
#pragma unroll
        for (int m = 0; m < 15; ++m) {
          if (m == 0) Y[0] = MFMA32(__builtin_bit_cast(bf16x8, pd[0]), S[0], Y[0]);
          else if (m == 1) Y[2] = MFMA32(__builtin_bit_cast(bf16x8, pn[0]), S[0], Y[2]);
          else if (m == 2) Y[0] = MFMA32(__builtin_bit_cast(bf16x8, pd[1]), S[1], Y[0]);
          else if (m == 3) Y[2] = MFMA32(__builtin_bit_cast(bf16x8, pn[1]), S[1], Y[2]);
          else if (m == 4) G0 = MFMA32(St[0], XAf[0][0], zero16);
          else if (m == 5) G0 = MFMA32(St[1], XAf[1][0], G0);
          else if (m == 6) G0 = MFMA32(ae, wf(m, 0), G0);
          else if (m < 11) G0 = MFMA32(as[m - 7], wf(m, 0), G0);
          else if (m == 11) V0 = MFMA32(as[0], wf(m, 0), zero16);
          else V0 = MFMA32(as[m - 11], wf(m, 0), V0);
          SB();
          if (m == 4) { sx = ld128(lX + lo + 3 * 128); re = *(const LAS unsigned*)(size_t)(lE + lo + 3 * 128); }
          if (m >= 3 && m < 12 && !((HELD >> (m + 3)) & 1)) b[(m - 3) % 3] = wl[widx(m + 3, 0) * 64];
          if (m == 7) glds16x(x, sx[0], cx0, ring0 + ro);
          if (m == 8) glds16x(x, sx[1], cx1, ring0 + ro + 1024);
          if (m == 9) glds16x(x, sx[2], cx0, ring0 + ro + 2048);
          if (m == 10) glds16x(x, sx[3], cx1, ring0 + ro + 3072);
          if (m == 11) glds16e(ES, re, ce, ring0 + ro + 4096);
          ew(m, G1, V1, G0, V0);
          SB();
        }
      }
    };
    for (int t = 0; t < ntiles; t += 2) {
      asm volatile("s_cmp_lt_u32 %0, 4\n\ts_cbranch_scc1 1f\n\ts_setprio 1\n1:" :: "s"(wave) : "scc");
      body(0);
      if (t + 1 >= ntiles) break;
      __builtin_amdgcn_s_setprio(0);
      body(1);
      lS4 += 256; lS1 += 256; lX += 256; lE += 256;
      asm volatile("" : "+v"(lS4), "+v"(lS1), "+v"(lX), "+v"(lE));
      adv += 256;
    }
    pq[14] = mulf(0, V1[14], rq[14]);
    pq[15] = mulf(1, V1[15], rq[15]);
    pn[1][3] = pack_bf16(pq[14], pq[15]);
    Y[1] = MFMA32(__builtin_bit_cast(bf16x8, pd[0]), S[0], Y[1]);
    Y[3] = MFMA32(__builtin_bit_cast(bf16x8, pn[0]), S[0], Y[3]);
    Y[1] = MFMA32(__builtin_bit_cast(bf16x8, pd[1]), S[1], Y[1]);
    Y[3] = MFMA32(__builtin_bit_cast(bf16x8, pn[1]), S[1], Y[3]);
    if (__builtin_expect(gs >= GEND, 1)) break;
    asm volatile("s_waitcnt vmcnt(0)" ::: "memory");
    lS4 -= adv; lS1 -= adv; lX -= adv; lE -= adv;
    cnt = gen();
    issue_gather(0, 0);
    issue_gather(1, 1);
  }

  {
    int lane;
    asm volatile("v_mbcnt_lo_u32_b32 %0, -1, 0\n\tv_mbcnt_hi_u32_b32 %0, -1, %0" : "=v"(lane));
    const int h = lane >> 5;
    const unsigned tu = lds0 + OFF_W + 18 * 1024 + h * 4096 + ((lane >> 2) & 3) * 128 + (lane & 1) * 64 +
                        ((lane >> 4) & 1) * 16 + ((lane >> 1) & 1) * 8;
    const unsigned nd = nodeof(cg + (lane & 31));
    const bool live = (nd * M) >> 16 == cg + (lane & 31);
    const unsigned lm = (unsigned)__ballot(live);
    const int nodes = __builtin_popcount(lm);
    const unsigned n0 = __builtin_amdgcn_readlane(nd, __builtin_ctz(lm | 0x80000000u));
    const int r = live ? nd - n0 : 31;
    bf16x8 wu[8];
#pragma unroll
    for (int k = 0; k < 8; ++k) wu[k] = rawfrag(tu + (k >> 1) * 1024 + (k & 1) * 32);
    asm volatile("" : "+v"(wu[0]), "+v"(wu[1]), "+v"(wu[2]), "+v"(wu[3]), "+v"(wu[4]), "+v"(wu[5]), "+v"(wu[6]), "+v"(wu[7]));
    f32x16 U[2] = {zero16, zero16};
#pragma unroll
    for (int k = 0; k < 8; ++k) U[k & 1] = MFMA32(wu[k], as[k >> 1], U[k & 1]);
#pragma unroll
    for (int ct = 0; ct < 2; ++ct) {
#pragma unroll
      for (int q = 0; q < 2; ++q) {
        const u32x4 xv = __builtin_bit_cast(u32x4, as[2 * ct + q]);
        u32x4 ov;
#pragma unroll
        for (int j = 0; j < 4; ++j) {
          const int i = 8 * q + 2 * j;
          const float z0 = U[ct][i] + Y[2 + ct][i] * __builtin_amdgcn_rcpf(Y[ct][i] + 1e-6f);
          const float z1 = U[ct][i + 1] + Y[2 + ct][i + 1] * __builtin_amdgcn_rcpf(Y[ct][i + 1] + 1e-6f);
          const float o0 = __uint_as_float(xv[j] << 16) + (z0 > 0.f ? z0 : 0.f);
          const float o1 = __uint_as_float(xv[j] & 0xffff0000u) + (z1 > 0.f ? z1 : 0.f);
          ov[j] = pack_bf16(o0, o1);
        }
        *(LAS u32x4*)(size_t)(la + r * 128 + (((4 * h + 2 * ct + q) ^ (r & 7)) * 16)) = ov;
      }
    }
    u32x4 ov[4];
#pragma unroll
    for (int q = 0; q < 4; ++q)
      ov[q] = *(const LAS u32x4*)(size_t)(la + (8 * q + (lane >> 3)) * 128 + (((lane & 7) ^ (lane >> 3)) * 16));
    asm volatile("" : "+v"(ov[0]), "+v"(ov[1]), "+v"(ov[2]), "+v"(ov[3]));
#pragma unroll
    for (int q = 0; q < 4; ++q)
      if (8 * q + (lane >> 3) < nodes) gstore128wt(out, (unsigned)n0 * 128u + q * 1024u + lane * 16u, ov[q]);
  }
  if (bk == 0 && wave == 7) asm volatile("global_store_dwordx2 %0, %1, %2 sc1" :: "v"(0u), "v"(ep), "s"(FL) : "memory");
  asm volatile("s_waitcnt vmcnt(0)" ::: "memory");
}

extern "C" void kernel_launch(void* const* d_in, const int* in_sizes, int n_in,
                              void* d_out, int out_size, void* d_ws, size_t ws_size,
                              hipStream_t stream) {
  const unsigned short* x = (const unsigned short*)d_in[0];
  const u32x4* eattr = (const u32x4*)d_in[1];
  const int* src = (const int*)d_in[2];
  const int* dst = (const int*)d_in[3];
  const unsigned short* wa = (const unsigned short*)d_in[4];
  const unsigned short* wb = (const unsigned short*)d_in[5];
  const unsigned short* wc = (const unsigned short*)d_in[6];
  const unsigned short* wu = (const unsigned short*)d_in[7];
  const unsigned short* wv = (const unsigned short*)d_in[8];
  unsigned short* out = (unsigned short*)d_out;
  const int N = in_sizes[0] / 64;
  const int E = in_sizes[2];
  const int chunk = (((E + NCH - 1) / NCH) + 3) & ~3;

  const unsigned M = N > 256 ? 0xffffffffu / (unsigned)N : 0xffffffu;

  char* ws = (char*)d_ws;
  size_t off = 0;
  off = 1 << 20;
  unsigned long long* FL = (unsigned long long*)(ws + off);   off += 1 << 20;
  u32x4* ENT = (u32x4*)(ws + off);                       off += (size_t)2 * NBK * IBOX;
  unsigned long long* TS = (unsigned long long*)(ws + off);

  k_main<<<NBK, 512, 0, stream>>>(x, eattr, src, dst, wa, wb, wc, wu, wv, ENT, TS, FL, out, N, E, chunk, M, 65536.0f / (float)M
     );
}
```

```cpp
#include <hip/hip_runtime.h>
#include <hip/hip_bf16.h>
#include <stdint.h>

using bf16x8 = __attribute__((ext_vector_type(8))) short;
using s16x4 = __attribute__((ext_vector_type(4))) short;
using f32x16 = __attribute__((ext_vector_type(16))) float;
using f32x2 = __attribute__((ext_vector_type(2))) float;
using u32x4 = __attribute__((ext_vector_type(4))) unsigned;
using u32x2 = __attribute__((ext_vector_type(2))) unsigned;

#define MFMA32(a, b, c) __builtin_amdgcn_mfma_f32_32x32x16_bf16((a), (b), (c), 0, 0, 0)
#define LAS __attribute__((address_space(3)))
#define RANKOF(w) ((w) < 4 ? 7 - (w) : (w) - 4)
#define G1W 4
#define CPF 32
#define CPFB 18688
#define SKEWF 0.015
#define SB() __builtin_amdgcn_sched_barrier(0)

constexpr int NCH = 256;
constexpr int NBK = 256;
constexpr int NFRAG = 39;
constexpr float NLOG2E = -1.44269504088896340736f;
constexpr unsigned TPITCH = 80;

__device__ __forceinline__ unsigned pack_bf16(float lo, float hi) {
  typedef __attribute__((ext_vector_type(2))) float f2;
  typedef __attribute__((ext_vector_type(2))) __bf16 b2;
  f2 v = {lo, hi};
  b2 r = __builtin_convertvector(v, b2);
  return __builtin_bit_cast(unsigned, r);
}

__device__ __forceinline__ void glds16(const void* sbase, unsigned voff, unsigned lds_dst) {
  asm volatile(
      "s_mov_b32 m0, %2\n\t"
      "s_nop 0\n\t"
      "global_load_lds_dwordx4 %0, %1"
      :: "v"(voff), "s"(sbase), "s"(lds_dst) : "memory", "m0");
}
__device__ __forceinline__ void glds16x(const void* sbase, unsigned a, unsigned c, unsigned lds_dst) {
  unsigned tmp;
  asm volatile(
      "s_mov_b32 m0, %4\n\t"
      "v_mad_u32_u16 %0, %1, %5, %2\n\t"
      "global_load_lds_dwordx4 %0, %3"
      : "=&v"(tmp) : "v"(a), "v"(c), "s"(sbase), "s"(lds_dst), "s"(128u) : "memory", "m0");
}
__device__ __forceinline__ void glds16e(const void* sbase, unsigned a, unsigned c, unsigned lds_dst) {
  unsigned tmp;
  asm volatile(
      "s_mov_b32 m0, %4\n\t"
      "v_add_u32 %0, %1, %2\n\t"
      "global_load_lds_dwordx4 %0, %3"
      : "=&v"(tmp) : "v"(a), "v"(c), "s"(sbase), "s"(lds_dst) : "memory", "m0");
}
__device__ __forceinline__ void gstore128wt(void* sbase, unsigned voff, u32x4 v) {
  asm volatile("global_store_dwordx4 %0, %1, %2 sc1\n\ts_nop 1" :: "v"(voff), "v"(v), "s"(sbase) : "memory");
}

constexpr int IBOX = 32768;
constexpr int IEXT = 256 * IBOX;
constexpr int CPIT = 208;
constexpr unsigned RINC = 87382;
constexpr int KP_HDR = 0;
constexpr int KP_ENT = KP_HDR + NBK * 4;
constexpr int KP_POS = KP_ENT + NBK * CPIT;
constexpr int KP_LDS = KP_POS + NBK * 4;
constexpr int NWL = 26;
constexpr int NBLOB = 31;

__device__ __forceinline__ unsigned mul24s(unsigned a, unsigned b) {
  unsigned r;
  asm("v_mul_u32_u24 %0, %1, %2" : "=v"(r) : "s"(b), "v"(a));
  return r;
}
__device__ __forceinline__ unsigned byte3x(unsigned a, unsigned b) {
  unsigned r;
  asm("v_mul_u32_u24_sdwa %0, %1, %2 dst_sel:DWORD dst_unused:UNUSED_PAD src0_sel:DWORD src1_sel:BYTE_3" : "=v"(r) : "s"(b), "v"(a));
  return r;
}
constexpr int RING_SLOT = 5 * 1024;
constexpr int LCAP = 512;
constexpr int LARR = 2560;
constexpr int LIST_BYTES = 2 * LARR;
constexpr int OFF_CNT = 0;
constexpr int OFF_XF = 256;
constexpr int OFF_W = 512;
constexpr int OFF_T1 = OFF_W + NWL * 1024;
constexpr int OFF_T2 = OFF_T1 + 32 * TPITCH;
constexpr int OFF_R0 = OFF_W + NFRAG * 1024;
constexpr int OFF_R1 = OFF_R0 + 8 * RING_SLOT;
constexpr int OFF_L = OFF_R1 + 8 * RING_SLOT;
constexpr int MAIN_LDS = OFF_L + 8 * LIST_BYTES;
static_assert(MAIN_LDS <= 160 * 1024, "LDS");
static_assert(KP_LDS <= 16 * RING_SLOT, "phase 1 lives in the ring");

__device__ __forceinline__ unsigned ldd(const unsigned* p) {
  return __hip_atomic_load(p, __ATOMIC_RELAXED, __HIP_MEMORY_SCOPE_AGENT);
}
__device__ __forceinline__ unsigned long long ldq(const unsigned long long* p) {
  return __hip_atomic_load(p, __ATOMIC_RELAXED, __HIP_MEMORY_SCOPE_AGENT);
}
__device__ __forceinline__ bf16x8 rawfrag(unsigned a) {
  const s16x4 a0 = __builtin_amdgcn_ds_read_tr16_b64_v4i16((LAS s16x4*)(size_t)a);
  const s16x4 a1 = __builtin_amdgcn_ds_read_tr16_b64_v4i16((LAS s16x4*)(size_t)(a + 512));
  return __builtin_shufflevector(a0, a1, 0, 1, 2, 3, 4, 5, 6, 7);
}
__device__ __forceinline__ float maxf(float a, float b) {
  float r;
  asm("v_max_f32 %0, %1, %2" : "=v"(r) : "v"(a), "v"(b));
  return r;
}
__device__ __forceinline__ float add1(float t) { return 1.0f + t; }
__device__ __forceinline__ float mulf(int i, float a, float b) { return (i & 1) ? __builtin_fmaf(a, b, 0.0f) : a * b; }

__global__ __launch_bounds__(512, 2) void k_main(const unsigned short* __restrict__ x,
                                                 const u32x4* __restrict__ ES,
                                                 const int* __restrict__ src, const int* __restrict__ dst,
                                                 const unsigned short* __restrict__ wa_g,
                                                 const unsigned short* __restrict__ wb_g,
                                                 const unsigned short* __restrict__ wc_g,
                                                 const unsigned short* __restrict__ wu_g,
                                                 const unsigned short* __restrict__ wv_g,
                                                 u32x4* ENT, unsigned long long* TS, unsigned long long* FL,
                                                 unsigned short* __restrict__ out, int N, int E, int chunk,
                                                 unsigned M, unsigned N1, unsigned DM, unsigned MF0, unsigned MF1,
                                                 unsigned MB0, unsigned MB1
                                                 ) {
  __shared__ __attribute__((aligned(16))) char smem[MAIN_LDS];
  const int tid0 = threadIdx.x;
  const int wave = __builtin_amdgcn_readfirstlane(tid0 >> 6);
  const int bk = blockIdx.x;
  const unsigned lds0 = (unsigned)(size_t)(LAS char*)smem;

  LAS char* sm = (LAS char*)smem + OFF_R0;
  const unsigned sm0 = lds0 + OFF_R0;
  const int tid = tid0, lane = tid & 63, r = lane & 31, h = lane >> 5;
  const unsigned pc = tid & 7, bq = tid >> 3;
  unsigned ia = sm0 + KP_ENT + bq * CPIT + pc * 16;
#pragma unroll
  for (int s = 0; s < 4; ++s) *(LAS u32x4*)(size_t)(ia + s * 64 * CPIT) = u32x4{~0u, ~0u, ~0u, ~0u};
  *(LAS unsigned*)(sm + KP_HDR + (tid & 255) * 4) = 0u;
  int q0 = 4 * tid + 3, q1 = 4 * tid + 1795;
  unsigned a0 = tid * 16, a1 = tid * 16 + 7168, z0 = 0u;
  unsigned long long m7 = __ballot(tid == 448);
  int n7 = wave == 7 ? 0 : 0x7fffffff;
  unsigned f0 = lane * 128u, f1 = lane * 128u + 8192u, f2 = min(lane * 128u + 16384u, CPFB - 512u);
  unsigned long long p7 = __ballot((wave | (bk & -CPF)) == 7);
  asm volatile("" : "+v"(f0), "+v"(f1), "+v"(f2), "+s"(p7));
  asm volatile("" : "+v"(ia), "+v"(q0), "+v"(q1), "+v"(a0), "+v"(a1), "+v"(z0), "+s"(m7), "+s"(n7) :: "memory");
  SB();
  asm volatile("" :: "s"(x), "s"(ES), "s"(src), "s"(dst), "s"(wa_g), "s"(wb_g), "s"(wc_g), "s"(wu_g), "s"(wv_g), "s"(ENT),
               "s"(TS), "s"(FL), "s"(out), "s"(N), "s"(E), "s"(chunk), "s"(M), "s"(N1), "s"(DM), "s"(MF0), "s"(MF1), "s"(MB0), "s"(MB1));
  const int base = bk * chunk;
  const int ecnt = max(min(chunk, E - base), 0);
  unsigned long long on[2];
  on[0] = __ballot(q0 < min(min(chunk, E - base), n7));
  on[1] = __ballot(q1 < min(min(chunk, E - base), n7));
  a0 += base * 4u;
  a1 += base * 4u;
  u32x4 d[2], sv[2];
  u32x2 e0;
  asm volatile("s_mov_b64 exec, %11\n\t"
               "global_load_dwordx4 %0, %5, %8 nt\n\t"
               "s_mov_b64 exec, %12\n\t"
               "global_load_dwordx4 %1, %6, %8 nt\n\t"
               "s_mov_b64 exec, %13\n\t"
               "global_load_dwordx2 %2, %7, %9 sc1\n\t"
               "s_getpc_b64 s[96:97]\n\t"
               "s_mov_b32 m0, %17\n\t"
               "s_mov_b64 exec, %18\n\t"
               "global_load_lds_dword %14, s[96:97]\n\t"
               "global_load_lds_dword %15, s[96:97]\n\t"
               "global_load_lds_dword %16, s[96:97]\n\t"
               "s_mov_b64 exec, %11\n\t"
               "s_waitcnt lgkmcnt(0)\n\t"
               "s_barrier\n\t"
               "global_load_dwordx4 %3, %5, %10 nt\n\t"
               "s_mov_b64 exec, %12\n\t"
               "global_load_dwordx4 %4, %6, %10 nt\n\t"
               "s_mov_b64 exec, -1"
               : "=&{v[240:243]}"(d[0]), "=&{v[244:247]}"(d[1]), "=&{v[238:239]}"(e0), "=&{v[248:251]}"(sv[0]),
                 "=&{v[252:255]}"(sv[1])
               : "v"(a0), "v"(a1), "v"(z0), "s"(dst), "s"(FL), "s"(src), "s"(on[0]), "s"(on[1]), "s"(m7)
                 , "v"(f0), "v"(f1), "v"(f2), "n"(OFF_L + 7 * LIST_BYTES + 4096), "s"(p7)
               : "memory", "m0", "s96", "s97");
  SB();
  const int bu = (bk >> 1) | ((bk & 1) << 7);
  const unsigned Mw = bk & 1 ? M + DM : M, Cw = bk & 1 ? 0u - N1 * DM : 0u;
  unsigned MF = bk & 1 ? MF1 : MF0, MB = bk & 1 ? MB1 : MB0;
  asm volatile("" : "+s"(MF), "+s"(MB));
  char* ib = reinterpret_cast<char*>(ENT) + (size_t)bu * IBOX;
  auto nodeof = [&](unsigned c) { return min((unsigned)__builtin_fmaf((float)c, __uint_as_float(MF), __uint_as_float(MB)), (unsigned)N - 1u); };
  unsigned tup[8], val[8], pm[8];
  u32x4 xg[4];
  const unsigned xn0 = __builtin_amdgcn_readfirstlane(nodeof((bu * 8 + wave) * 32));
  unsigned xd[4];
  auto xprep = [&]() {
#pragma unroll
    for (int i = 0; i < 4; ++i)
      xd[i] = min(xn0 + 8 * i + (lane >> 3), N - 1u) * 128u + ((((lane & 7) ^ (lane >> 4)) * 16) ^ (i & 1) * 64) - i * 1024u;
    asm volatile("" : "+v"(xd[0]), "+v"(xd[1]), "+v"(xd[2]), "+v"(xd[3]));
  };
  auto xdma = [&]() {
    asm volatile("s_mov_b32 m0, %5\n\ts_nop 0\n\t"
                 "global_load_lds_dwordx4 %0, %4\n\t"
                 "global_load_lds_dwordx4 %1, %4 offset:1024\n\t"
                 "global_load_lds_dwordx4 %2, %4 offset:2048\n\t"
                 "global_load_lds_dwordx4 %3, %4 offset:3072"
                 :: "v"(xd[0]), "v"(xd[1]), "v"(xd[2]), "v"(xd[3]), "s"(x), "s"(lds0 + OFF_L + wave * LIST_BYTES)
                 : "memory", "m0");
  };
  auto wload = [&](const unsigned short* w, int f) {
    asm volatile("s_mov_b32 m0, %3\n\ts_nop 0\n\t"
                 "global_load_lds_dwordx4 %0, %2\n\t"
                 "global_load_lds_dwordx4 %0, %2 offset:1024\n\t"
                 "global_load_lds_dwordx4 %0, %2 offset:2048\n\t"
                 "global_load_lds_dwordx4 %0, %2 offset:3072\n\t"
                 "s_addk_i32 m0, 4096\n\ts_nop 0\n\t"
                 "global_load_lds_dwordx4 %1, %2\n\t"
                 "global_load_lds_dwordx4 %1, %2 offset:1024\n\t"
                 "global_load_lds_dwordx4 %1, %2 offset:2048\n\t"
                 "global_load_lds_dwordx4 %1, %2 offset:3072"
                 :: "v"(lane * 16u), "v"(lane * 16u + 4096u), "s"(w), "s"(lds0 + OFF_W + f * 1024) : "memory", "m0", "scc");
  };
  auto wall = [&]() {
    wload(wa_g, NBLOB);
    wload(wb_g, 0);
    wload(wv_g, 8);
    glds16(wc_g, lane * 16, lds0 + OFF_W + 16 * 1024);
    glds16(wc_g + 512, lane * 16, lds0 + OFF_W + 17 * 1024);
  };
  int spin = 1 << 20;
  u32x2 ep = {0u, 0u};
  if (wave == 7) {
    wall();
    *(LAS u32x4*)((LAS char*)smem + OFF_CNT + r * 16) =
        r < 8 ? u32x4{lds0 + OFF_L, lds0 + OFF_L + 4, lds0 + OFF_L + 8, lds0 + OFF_L + 12} : u32x4{0u, 0u, 0u, 0u};
    {
      const unsigned col = h ? ((r >> 1) & 12u) | ((r << 2) & 16u) | (r & 3u) : r;
      LAS char* tr = (LAS char*)smem + OFF_T1 + lane * TPITCH;
#pragma unroll
      for (int j = 0; j < 4; ++j) *(LAS u32x4*)(tr + j * 16) = u32x4{0u, 0u, 0u, 0u};
      *(LAS unsigned short*)(tr + col * 2) = 0x3F80;
    }
#pragma unroll
    for (int i = 0; i < 8; ++i) { tup[i] = 0u; val[i] = 0u; pm[i] = 0u; }
    asm volatile("s_waitcnt vmcnt(%1)" : "+{v[238:239]}"(e0) : "n"(31) : "memory");
    ep[0] = __builtin_amdgcn_readfirstlane(e0[0]) + 0x9E3779B1u;
    ep[1] = __builtin_amdgcn_readfirstlane(e0[1]) + 0x7F4A7C15u;
    *(LAS u32x2*)((LAS char*)smem + OFF_CNT + 168) = u32x2{ep[0], ep[1] >> 24};
    if (__builtin_expect(ecnt & 3, 0)) {
      const int i = (ecnt & ~3) + lane;
      on[0] = __ballot(i < ecnt);
      if (i < ecnt) {
        pm[0] = (unsigned)dst[base + i] * M + (unsigned)max(dst[base + i] - (int)N1, 0) * DM;
        tup[0] = (unsigned)src[base + i] | (unsigned)i << 16;
        val[0] = __hip_atomic_fetch_add((LAS unsigned*)(sm + KP_HDR) + (pm[0] >> 24), RINC, __ATOMIC_RELAXED, __HIP_MEMORY_SCOPE_WORKGROUP);
        const unsigned u = min(val[0] >> 16, 48u);
        *(LAS unsigned*)(sm + KP_ENT + (pm[0] >> 24) * CPIT + u * 4) = tup[0];
        *(LAS unsigned char*)(sm + KP_ENT + (pm[0] >> 24) * CPIT + ((u * 4) | (u & 3) | 12)) = pm[0] >> 16;
      }
      __builtin_amdgcn_s_waitcnt(0x0F70);
    }
    xprep();
    xdma();
  } else {
    xprep();
    unsigned inc = RINC, t0 = (unsigned)tid << 18;
    asm volatile("s_waitcnt vmcnt(2)" : "+{v[240:243]}"(d[0]), "+{v[244:247]}"(d[1]), "+v"(inc), "+v"(t0) :: "memory");
    unsigned cb[8];
#pragma unroll
    for (int s = 0; s < 2; ++s) {
      unsigned* rr = val + 4 * s;
#pragma unroll
      for (int k = 0; k < 4; ++k) {
        unsigned t;
        asm("v_sub_u32 %0, %1, %2 clamp" : "=v"(t) : "v"(d[s][k]), "s"(N1));
        asm("v_mad_u32_u24 %0, %1, %2, %3" : "=v"(pm[4 * s + k]) : "v"(t), "s"(DM), "v"(mul24s(d[s][k], M)));
      }
#pragma unroll
      for (int k = 0; k < 4; ++k) rr[k] = byte3x(pm[4 * s + k], 4u);
      asm volatile(
          "s_mov_b64 exec, %5\n\t"
          "ds_add_rtn_u32 %0, %0, %4 offset:%6\n\t"
          "ds_add_rtn_u32 %1, %1, %4 offset:%6\n\t"
          "ds_add_rtn_u32 %2, %2, %4 offset:%6\n\t"
          "ds_add_rtn_u32 %3, %3, %4 offset:%6\n\t"
          "s_mov_b64 exec, -1"
          : "+v"(rr[0]), "+v"(rr[1]), "+v"(rr[2]), "+v"(rr[3])
          : "v"(inc), "s"(on[s]), "n"(OFF_R0 + KP_HDR)
          : "memory");
    }
#pragma unroll
    for (int i = 0; i < 8; ++i) cb[i] = byte3x(pm[i], (unsigned)CPIT);
    asm volatile("s_waitcnt vmcnt(0)"
                 : "+{v[248:251]}"(sv[0]), "+{v[252:255]}"(sv[1]), "+v"(cb[0]), "+v"(cb[1]), "+v"(cb[2]), "+v"(cb[3]), "+v"(cb[4]), "+v"(cb[5]),
                   "+v"(cb[6]), "+v"(cb[7])
                 :: "memory");
    xdma();
#pragma unroll
    for (int i = 0; i < 8; ++i) tup[i] = sv[i >> 2][i & 3] + t0 + (((i >> 2) * 1792u + (i & 3)) << 16);
#pragma unroll
    for (int s = 0; s < 2; ++s) {
      asm volatile("s_waitcnt lgkmcnt(%8)"
                   : "+v"(val[4 * s]), "+v"(val[4 * s + 1]), "+v"(val[4 * s + 2]), "+v"(val[4 * s + 3]), "+v"(tup[4 * s]),
                     "+v"(tup[4 * s + 1]), "+v"(tup[4 * s + 2]), "+v"(tup[4 * s + 3])
                   : "n"(s ? 8 : 4) : "memory");
      unsigned pa[4], pb[4];
#pragma unroll
      for (int k = 0; k < 4; ++k) {
        const unsigned u = min(val[4 * s + k], 48u << 16);
        asm("v_mad_u32_u16 %0, %1, 4, %2 op_sel:[1,0,0,0]" : "=v"(pa[k]) : "v"(u), "v"(cb[4 * s + k]));
        pb[k] = pa[k] | ((u >> 16) & 3u) | 12u;
      }
      asm volatile(
          "s_mov_b64 exec, %16\n\t"
          "ds_write_b32 %0, %8 offset:%17\n\t"
          "ds_write_b8_d16_hi %4, %12 offset:%17\n\t"
          "ds_write_b32 %1, %9 offset:%17\n\t"
          "ds_write_b8_d16_hi %5, %13 offset:%17\n\t"
          "ds_write_b32 %2, %10 offset:%17\n\t"
          "ds_write_b8_d16_hi %6, %14 offset:%17\n\t"
          "ds_write_b32 %3, %11 offset:%17\n\t"
          "ds_write_b8_d16_hi %7, %15 offset:%17\n\t"
          "s_mov_b64 exec, -1"
          :: "v"(pa[0]), "v"(pa[1]), "v"(pa[2]), "v"(pa[3]), "v"(pb[0]), "v"(pb[1]), "v"(pb[2]), "v"(pb[3]),
             "v"(tup[4 * s]), "v"(tup[4 * s + 1]), "v"(tup[4 * s + 2]), "v"(tup[4 * s + 3]), "v"(pm[4 * s]),
             "v"(pm[4 * s + 1]), "v"(pm[4 * s + 2]), "v"(pm[4 * s + 3]), "s"(on[s]), "n"(OFF_R0 + KP_ENT)
          : "memory");
    }
  }
  u32x2 tag;
  {
    unsigned ha = sm0 + KP_HDR + bq * 4, hb = sm0 + KP_HDR + lane * 16;
    unsigned go = bq * IBOX + bk * 128 + pc * 16;
    asm volatile("s_waitcnt lgkmcnt(0)\n\ts_barrier" : "+v"(ha), "+v"(hb), "+v"(go) :: "memory");
    tag = *(const LAS u32x2*)((LAS char*)smem + OFF_CNT + 168);
    const u32x4 call = *(const LAS u32x4*)(size_t)hb;
    unsigned cn[4];
    u32x4 v[4];
#pragma unroll
    for (int s = 0; s < 4; ++s) {
      cn[s] = *(const LAS unsigned*)(size_t)(ha + s * 256);
      v[s] = *(const LAS u32x4*)(size_t)(ia + s * 64 * CPIT);
    }
    asm volatile("" : "+v"(v[0]), "+v"(v[1]), "+v"(v[2]), "+v"(v[3]), "+v"(tag));
    if (__builtin_expect(__ballot(max(max(call[0], call[1]), max(call[2], call[3])) > 36u * RINC) != 0ull, 0)) {
      LAS unsigned* ovc = (LAS unsigned*)((LAS char*)smem + OFF_CNT + 164);
      LAS unsigned* pos = (LAS unsigned*)(sm + KP_POS);
      if (tid < NBK) {
        const unsigned u = *(const LAS unsigned*)(sm + KP_HDR + tid * 4) >> 16, n = u - (u >> 2);
        if (n > 36u) {
          const unsigned p = (unsigned)base + __hip_atomic_fetch_add(ovc, n - 36u, __ATOMIC_RELAXED, __HIP_MEMORY_SCOPE_WORKGROUP);
          pos[tid] = p;
          __hip_atomic_store(FL + 512 + tid * NCH + bk, ((unsigned long long)(n - 36u) << 32) | p, __ATOMIC_RELAXED,
                             __HIP_MEMORY_SCOPE_AGENT);
        }
      }
      asm volatile("s_waitcnt lgkmcnt(0)\n\ts_barrier" ::: "memory");
#pragma unroll
      for (int i = 0; i < 8; ++i) {
        const unsigned u = val[i] >> 16, n = u - (u >> 2);
        if (((on[i >> 2] >> lane) & 1ull) && n >= 36u)
          __hip_atomic_store(TS + pos[pm[i] >> 24] + (n - 36u), ((unsigned long long)((pm[i] >> 16) & 0xffu) << 32) | tup[i],
                             __ATOMIC_RELAXED, __HIP_MEMORY_SCOPE_AGENT);
      }
      __builtin_amdgcn_s_waitcnt(0x0F70);
      asm volatile("s_waitcnt vmcnt(0)\n\ts_barrier" ::: "memory");
    }
    auto seal = [&](u32x4& w, bool more) {
      w[3] = __builtin_amdgcn_perm(tag[1], w[3], 0x04020100u);
      unsigned sm_ = w[0] + w[1] + w[2] + w[3] + tag[0];
      sm_ += __builtin_amdgcn_update_dpp(0u, sm_, 0xB1, 0xf, 0xf, false);
      sm_ += __builtin_amdgcn_update_dpp(0u, sm_, 0x4E, 0xf, 0xf, false);
      sm_ += __builtin_amdgcn_update_dpp(0u, sm_, 0x141, 0xf, 0xf, false);
      w[2] = pc == 7u ? w[2] - sm_ + (more ? 1u : 0u) : w[2];
    };
#pragma unroll
    for (int s = 0; s < 4; ++s) seal(v[s], cn[s] > 23u * RINC);
    asm volatile("global_store_dwordx4 %0, %1, %5 sc1\n\t"
                 "global_store_dwordx4 %0, %2, %6 sc1\n\t"
                 "global_store_dwordx4 %0, %3, %7 sc1\n\t"
                 "global_store_dwordx4 %0, %4, %8 sc1\n\t"
                 "s_nop 1"
                 :: "v"(go), "v"(v[0]), "v"(v[1]), "v"(v[2]), "v"(v[3]), "s"(ENT), "s"(ENT + 4 * IBOX), "s"(ENT + 8 * IBOX),
                    "s"(ENT + 12 * IBOX)
                 : "memory");
    if (__builtin_expect(__ballot(max(max(cn[0], cn[1]), max(cn[2], cn[3])) > 23u * RINC) != 0ull, 0)) {
#pragma unroll 1
      for (int s = 0; s < 4; ++s) {
        const unsigned c = *(const LAS unsigned*)(size_t)(ha + s * 256);
        u32x4 w = *(const LAS u32x4*)(size_t)(ia + s * 64 * CPIT + (pc < 4u ? 128 : 0));
        if (pc == 7u) w = u32x4{w[2], ~0u, ~0u, w[3] >> 16};
        else {
#pragma unroll
          for (int j = 0; j < 3; ++j) w[j] = pc < 4u && 32u + 4u * pc + j < (c >> 16) ? w[j] : ~0u;
        }
        seal(w, c > 36u * RINC);
        if (c > 23u * RINC) gstore128wt(ENT, go + s * (64 * IBOX) + IEXT, w);
      }
    }
  }
  u32x4 cv[4];
  unsigned vo[4], eb[4];
  unsigned long long pend[4];
#pragma unroll
  for (int k = 0; k < 4; ++k) {
    vo[k] = wave * 4096u + k * 1024u + lane * 16u;
    eb[k] = (unsigned)((wave * 32 + k * 8 + (lane >> 3)) * chunk) * 32u;
  }
#define POLL0()                                                                                                       \
  asm volatile("global_load_dwordx4 %0, %4, %8 sc1\n\t"                                                               \
               "global_load_dwordx4 %1, %5, %8 sc1\n\t"                                                               \
               "global_load_dwordx4 %2, %6, %8 sc1\n\t"                                                               \
               "global_load_dwordx4 %3, %7, %8 sc1"                                                                   \
               : "=&{v[240:243]}"(cv[0]), "=&{v[244:247]}"(cv[1]), "=&{v[248:251]}"(cv[2]), "=&{v[252:255]}"(cv[3])   \
               : "v"(vo[0]), "v"(vo[1]), "v"(vo[2]), "v"(vo[3]), "s"(ib)                                              \
               : "memory")
  const LAS bf16x8* wl = (const LAS bf16x8*)(smem + OFF_W) + lane;
  const f32x16 zero16 = {0.f, 0.f, 0.f, 0.f, 0.f, 0.f, 0.f, 0.f, 0.f, 0.f, 0.f, 0.f, 0.f, 0.f, 0.f, 0.f};
  auto widx = [&](int m, int ct) { return m == 6 ? 16 + ct : m < 11 ? m - 7 + 4 * ct : 8 + m - 11 + 4 * ct; };
#define HELD ((1 << 6) | (1 << 8) | (1 << 10) | (1 << 12) | (1 << 14))
  bf16x8 wr[2][15];
  const unsigned to = lds0 + OFF_W + ((lane >> 2) & 3) * 128 + (lane & 1) * 64 + ((lane >> 4) & 1) * 16 + ((lane >> 1) & 1) * 8;
  auto xa = [&]() {
    bf16x8 wa[8];
#pragma unroll
    for (int k = 0; k < 8; ++k) wa[k] = rawfrag(to + (NBLOB + (k >> 1)) * 1024 + h * 4096 + (k & 1) * 32);
    asm volatile("" : "+v"(wa[0]), "+v"(wa[1]), "+v"(wa[2]), "+v"(wa[3]), "+v"(wa[4]), "+v"(wa[5]), "+v"(wa[6]), "+v"(wa[7]),
                      "+{v[160:163]}"(xg[0]), "+{v[164:167]}"(xg[1]), "+{v[168:171]}"(xg[2]), "+{v[172:175]}"(xg[3]));
    f32x16 XA[2] = {zero16, zero16};
#pragma unroll
    for (int k = 0; k < 4; ++k)
#pragma unroll
      for (int ct = 0; ct < 2; ++ct) XA[ct] = MFMA32(__builtin_bit_cast(bf16x8, xg[k]), wa[k * 2 + ct], XA[ct]);
#pragma unroll
    for (int q = 0; q < 4; ++q) {
      u32x4 xp;
#pragma unroll
      for (int j = 0; j < 4; ++j)
        xp[j] = pack_bf16(XA[q & 1][8 * (q >> 1) + 2 * j] * NLOG2E, XA[q & 1][8 * (q >> 1) + 2 * j + 1] * NLOG2E);
      *(LAS u32x4*)((LAS char*)smem + OFF_R1 + wave * RING_SLOT + q * 1024 + lane * 16) = xp;
    }
  };
  {
    auto prep = [&](unsigned f, unsigned hs, float scale) {
      u32x4 w[2];
#pragma unroll
      for (int ct = 0; ct < 2; ++ct) w[ct] = __builtin_bit_cast(u32x4, rawfrag(to + f + h * hs + ct * 32));
      asm volatile("s_waitcnt lgkmcnt(0)" : "+v"(w[0]), "+v"(w[1]) :: "memory");
#pragma unroll
      for (int ct = 0; ct < 2; ++ct) {
#pragma unroll
        for (int j = 0; j < 4; ++j)
          w[ct][j] = pack_bf16(__uint_as_float(w[ct][j] << 16) * scale, __uint_as_float(w[ct][j] & 0xffff0000u) * scale);
        *(LAS u32x4*)((LAS char*)smem + OFF_W + f + ct * hs + lane * 16) = w[ct];
      }
    };
    {
      const unsigned xr = min(nodeof((bu * 8 + wave) * 32 + r) - xn0, 31u);
      unsigned xa_ = lds0 + OFF_L + wave * LIST_BYTES + xr * 128 + (((4 * h) ^ ((xr >> 1) & 7)) * 16);
      asm volatile("s_waitcnt vmcnt(4)" : "+v"(xa_) :: "memory");
#pragma unroll
      for (int t = 0; t < 4; ++t) xg[t] = *(const LAS u32x4*)(size_t)(xa_ ^ (t * 16));
      asm volatile("" : "+v"(xg[0]), "+v"(xg[1]), "+v"(xg[2]), "+v"(xg[3]) :: "memory");
      *(LAS unsigned*)((LAS char*)smem + OFF_L + wave * LIST_BYTES + h * LARR + r * 4) = ~0u;
    }
    asm volatile("s_waitcnt vmcnt(4) lgkmcnt(0)\n\ts_barrier"
                 : "+v"(vo[0]), "+v"(vo[1]), "+v"(vo[2]), "+v"(vo[3]), "+v"(eb[0]), "+v"(eb[1]), "+v"(eb[2]), "+v"(eb[3])
                 :: "memory");
    xa();
    {
      unsigned f = (wave + (wave & 4)) * 1024, hs = 4096;
      const float scale = wave < 4 ? NLOG2E : 1.0f;
#pragma unroll 1
      while (true) {
        asm volatile("" : "+s"(f), "+s"(hs));
        prep(f, hs, scale);
        if (f != 0u) break;
        f = 16 * 1024;
        hs = 1024;
      }
    }
    POLL0();
    glds16(wu_g + wave * 512, lane * 16, lds0 + OFF_W + (18 + wave) * 1024);
  }
  const unsigned l0 = lds0 + OFF_L;
  unsigned aLX = l0 + (lane >> 3) * 16;
  unsigned aLE = l0 + LARR + ((((lane >> 1) & 7) << 2) | (lane >> 4)) * 4;
  unsigned aLS4 = l0 + (4 * h + ((lane >> 2) & 3)) * 16;
  unsigned aLS1 = l0 + (((r & 7) << 2) | (r >> 3)) * 4 + 2;
  unsigned cx0 = (unsigned)(((lane & 7) ^ (lane >> 4)) * 16);
  unsigned cx1 = cx0 ^ 64u;
  unsigned ce = (unsigned)((((lane & 1) ^ ((lane >> 4) & 1))) * 16);
  const unsigned ringd = 8 * RING_SLOT;
  unsigned cS = lds0 + OFF_T1 + (((lane >> 4) & 1) * 16 + (lane & 3) * 4) * 2;
  unsigned cSt = lds0 + OFF_T2 + h * 32;
  unsigned fo[4];
#pragma unroll
  for (int t = 0; t < 4; ++t) fo[t] = lds0 + OFF_R0 + (unsigned)(r * 128 + (((4 * h + t) ^ ((r >> 1) & 7)) * 16));
  unsigned foe = lds0 + OFF_R0 + (unsigned)(4096 + r * 32 + ((h ^ ((r >> 3) & 1)) * 16));
  f32x16 Y[4];
#pragma unroll
  for (int q = 0; q < 4; ++q)
#pragma unroll
    for (int i = 0; i < 16; i += 2) {
      f32x2 y;
      if (q < 2) asm volatile("v_mov_b64 %0, %1" : "=v"(y) : "s"(0x358637bd358637bdull));
      else asm volatile("v_mov_b64 %0, 0" : "=v"(y));
      Y[q][i] = y[0];
      Y[q][i + 1] = y[1];
    }
  f32x16 G0, V0, G1, V1;
  bf16x8 S[2], St[2], as[4], ae, b[3];
  u32x4 pd[2], pn[2];
  float tq[16], rq[16], pq[16];
  auto pipe_zero = [&]() {
#pragma unroll
    for (int j = 0; j < 8; ++j) { S[0][j] = 0; S[1][j] = 0; }
#pragma unroll
    for (int j = 0; j < 4; ++j) { pd[0][j] = 0; pd[1][j] = 0; pn[0][j] = 0; pn[1][j] = 0; }
#pragma unroll
    for (int i = 0; i < 16; ++i) V1[i] = 0.f;
    rq[14] = 0.f; rq[15] = 0.f;
  };
  unsigned rid = lane & 7;
  asm volatile("" : "+v"(aLX), "+v"(aLE), "+v"(aLS4), "+v"(aLS1), "+v"(cx0), "+v"(cx1), "+v"(ce), "+v"(cS), "+v"(cSt),
                    "+v"(fo[0]), "+v"(fo[1]), "+v"(fo[2]), "+v"(fo[3]), "+v"(foe), "+v"(rid));
  asm volatile("" : "+v"(Y[0]), "+v"(Y[1]), "+v"(Y[2]), "+v"(Y[3]));
  auto ld128 = [&](unsigned a) { return *(const LAS u32x4*)(size_t)a; };
  auto gather5 = [&](const u32x4 sx, unsigned re, unsigned dstb) {
    glds16x(x, sx[0], cx0, dstb);
    glds16x(x, sx[1], cx1, dstb + 1024);
    glds16x(x, sx[2], cx0, dstb + 2048);
    glds16x(x, sx[3], cx1, dstb + 3072);
    glds16e(ES, re, ce, dstb + 4096);
  };
  unsigned rq4 = ((lane >> 3) & 3) * 4;
  unsigned early, just;
  asm volatile(
        "s_mov_b32 s96, 0x800000\n\t"
        "v_mov_b32 v181, %18\n\t"
        "v_mov_b32 v180, 5120\n\t"
        "v_mov_b32 v177, 0x0c040100\n\t"
        "v_mov_b32 v178, 0x0c050100\n\t"
        "v_mov_b32 v179, 0x0c060100\n\t"
        "s_mov_b32 s52, 0x7f7f7f7f\n\t"
        "s_mov_b32 s53, 0x7f7f7f7f\n\t"
        "s_mov_b64 s[54:55], 0\n\t"
        "s_mov_b32 %12, 0\n\t"
        "v_mov_b32 v191, 16\n\t"
        "s_waitcnt vmcnt(%30)\n\t"
        "v_add3_u32 v228, v240, v241, v242\n\t"
        "v_add3_u32 v231, v244, v245, v246\n\t"
        "v_add3_u32 v228, v228, v243, %19\n\t"
        "v_add3_u32 v231, v231, v247, %19\n\t"
        "v_cmp_ne_u32_sdwa s[72:73], v243, %20 src0_sel:BYTE_3 src1_sel:DWORD\n\t"
        "v_add_u32_dpp v228, v228, v228 quad_perm:[1,0,3,2] row_mask:0xf bank_mask:0xf\n\t"
        "v_add_u32_dpp v231, v231, v231 quad_perm:[1,0,3,2] row_mask:0xf bank_mask:0xf\n\t"
        "v_cmp_ne_u32_sdwa s[78:79], v247, %20 src0_sel:BYTE_3 src1_sel:DWORD\n\t"
        "v_add_u32_dpp v228, v228, v228 quad_perm:[2,3,0,1] row_mask:0xf bank_mask:0xf\n\t"
        "v_add_u32_dpp v231, v231, v231 quad_perm:[2,3,0,1] row_mask:0xf bank_mask:0xf\n\t"
        "s_nop 0\n\t"
        "v_add_u32_dpp v228, v228, v228 row_half_mirror row_mask:0xf bank_mask:0xf\n\t"
        "v_add_u32_dpp v231, v231, v231 row_half_mirror row_mask:0xf bank_mask:0xf\n\t"
        "v_cmp_gt_u32 s[64:65], 2, v228\n\t"
        "v_cmp_eq_u32 s[56:57], 1, v228\n\t"
        "v_cmp_gt_u32 s[66:67], 2, v231\n\t"
        "v_cmp_eq_u32 s[58:59], 1, v231\n\t"
        "s_and_b64 s[72:73], s[72:73], s[64:65]\n\t"
        "s_cselect_b64 s[64:65], 0, s[64:65]\n\t"
        "s_and_b64 s[56:57], s[56:57], s[64:65]\n\t"
        "s_not_b64 %0, s[64:65]\n\t"
        "s_or_b64 s[54:55], s[54:55], s[56:57]\n\t"
        "s_or_b64 s[54:55], s[54:55], %0\n\t"
        "s_and_b64 s[78:79], s[78:79], s[66:67]\n\t"
        "s_cselect_b64 s[66:67], 0, s[66:67]\n\t"
        "s_and_b64 s[58:59], s[58:59], s[66:67]\n\t"
        "s_not_b64 %1, s[66:67]\n\t"
        "s_or_b64 s[54:55], s[54:55], s[58:59]\n\t"
        "s_or_b64 s[54:55], s[54:55], %1\n\t"
        "s_mov_b64 exec, s[64:65]\n\t"
        "v_cmpx_ne_u32 s[72:73], -1, v240\n\t"
        "v_bfe_u32 v192, v243, 5, 3\n\t"
        "v_lshl_add_u32 v204, v192, 4, %21\n\t"
        "ds_add_rtn_u32 v204, v204, v191\n\t"
        "v_perm_b32 v216, v243, v240, v177\n\t"
        "v_mad_u32_u16 v228, v240, 32, %13 op_sel:[1,0,0,0]\n\t"
        "v_cmpx_ne_u32 s[74:75], -1, v241\n\t"
        "v_bfe_u32 v193, v243, 13, 3\n\t"
        "v_lshl_add_u32 v205, v193, 4, %21\n\t"
        "ds_add_rtn_u32 v205, v205, v191\n\t"
        "v_perm_b32 v217, v243, v241, v178\n\t"
        "v_mad_u32_u16 v229, v241, 32, %13 op_sel:[1,0,0,0]\n\t"
        "s_and_b64 exec, exec, s[52:53]\n\t"
        "v_cmpx_ne_u32 s[76:77], -1, v242\n\t"
        "v_bfe_u32 v194, v243, 21, 3\n\t"
        "v_lshl_add_u32 v206, v194, 4, %21\n\t"
        "ds_add_rtn_u32 v206, v206, v191\n\t"
        "v_perm_b32 v218, v243, v242, v179\n\t"
        "v_mad_u32_u16 v230, v242, 32, %13 op_sel:[1,0,0,0]\n\t"
        "s_mov_b64 exec, s[66:67]\n\t"
        "v_cmpx_ne_u32 s[78:79], -1, v244\n\t"
        "v_bfe_u32 v195, v247, 5, 3\n\t"
        "v_lshl_add_u32 v207, v195, 4, %21\n\t"
        "ds_add_rtn_u32 v207, v207, v191\n\t"
        "v_perm_b32 v219, v247, v244, v177\n\t"
        "v_mad_u32_u16 v231, v244, 32, %14 op_sel:[1,0,0,0]\n\t"
        "v_cmpx_ne_u32 s[80:81], -1, v245\n\t"
        "v_bfe_u32 v196, v247, 13, 3\n\t"
        "v_lshl_add_u32 v208, v196, 4, %21\n\t"
        "ds_add_rtn_u32 v208, v208, v191\n\t"
        "v_perm_b32 v220, v247, v245, v178\n\t"
        "v_mad_u32_u16 v232, v245, 32, %14 op_sel:[1,0,0,0]\n\t"
        "s_and_b64 exec, exec, s[52:53]\n\t"
        "v_cmpx_ne_u32 s[82:83], -1, v246\n\t"
        "v_bfe_u32 v197, v247, 21, 3\n\t"
        "v_lshl_add_u32 v209, v197, 4, %21\n\t"
        "ds_add_rtn_u32 v209, v209, v191\n\t"
        "v_perm_b32 v221, v247, v246, v179\n\t"
        "v_mad_u32_u16 v233, v246, 32, %14 op_sel:[1,0,0,0]\n\t"
        "s_mov_b64 exec, -1\n\t"
        "s_waitcnt vmcnt(%31)\n\t"
        "v_add3_u32 v234, v248, v249, v250\n\t"
        "v_add3_u32 v237, v252, v253, v254\n\t"
        "v_add3_u32 v234, v234, v251, %19\n\t"
        "v_add3_u32 v237, v237, v255, %19\n\t"
        "v_cmp_ne_u32_sdwa s[84:85], v251, %20 src0_sel:BYTE_3 src1_sel:DWORD\n\t"
        "v_add_u32_dpp v234, v234, v234 quad_perm:[1,0,3,2] row_mask:0xf bank_mask:0xf\n\t"
        "v_add_u32_dpp v237, v237, v237 quad_perm:[1,0,3,2] row_mask:0xf bank_mask:0xf\n\t"
        "v_cmp_ne_u32_sdwa s[90:91], v255, %20 src0_sel:BYTE_3 src1_sel:DWORD\n\t"
        "v_add_u32_dpp v234, v234, v234 quad_perm:[2,3,0,1] row_mask:0xf bank_mask:0xf\n\t"
        "v_add_u32_dpp v237, v237, v237 quad_perm:[2,3,0,1] row_mask:0xf bank_mask:0xf\n\t"
        "s_nop 0\n\t"
        "v_add_u32_dpp v234, v234, v234 row_half_mirror row_mask:0xf bank_mask:0xf\n\t"
        "v_add_u32_dpp v237, v237, v237 row_half_mirror row_mask:0xf bank_mask:0xf\n\t"
        "v_cmp_gt_u32 s[68:69], 2, v234\n\t"
        "v_cmp_eq_u32 s[60:61], 1, v234\n\t"
        "v_cmp_gt_u32 s[70:71], 2, v237\n\t"
        "v_cmp_eq_u32 s[62:63], 1, v237\n\t"
        "s_and_b64 s[84:85], s[84:85], s[68:69]\n\t"
        "s_cselect_b64 s[68:69], 0, s[68:69]\n\t"
        "s_and_b64 s[60:61], s[60:61], s[68:69]\n\t"
        "s_not_b64 %2, s[68:69]\n\t"
        "s_or_b64 s[54:55], s[54:55], s[60:61]\n\t"
        "s_or_b64 s[54:55], s[54:55], %2\n\t"
        "s_and_b64 s[90:91], s[90:91], s[70:71]\n\t"
        "s_cselect_b64 s[70:71], 0, s[70:71]\n\t"
        "s_and_b64 s[62:63], s[62:63], s[70:71]\n\t"
        "s_not_b64 %3, s[70:71]\n\t"
        "s_or_b64 s[54:55], s[54:55], s[62:63]\n\t"
        "s_or_b64 s[54:55], s[54:55], %3\n\t"
        "s_mov_b64 exec, s[72:73]\n\t"
        "s_waitcnt lgkmcnt(3)\n\t"
        "v_min_u32 v204, v181, v204\n\t"
        "v_min_u32 v205, v181, v205\n\t"
        "v_min_u32 v206, v181, v206\n\t"
        "v_mad_u32_u24 v204, v192, v180, v204\n\t"
        "v_mad_u32_u24 v205, v193, v180, v205\n\t"
        "v_mad_u32_u24 v206, v194, v180, v206\n\t"
        "ds_write2st64_b32 v204, v216, v228 offset1:10\n\t"
        "s_mov_b64 exec, s[74:75]\n\t"
        "ds_write2st64_b32 v205, v217, v229 offset1:10\n\t"
        "s_mov_b64 exec, s[76:77]\n\t"
        "ds_write2st64_b32 v206, v218, v230 offset1:10\n\t"
        "s_mov_b64 exec, s[68:69]\n\t"
        "v_cmpx_ne_u32 s[84:85], -1, v248\n\t"
        "v_bfe_u32 v198, v251, 5, 3\n\t"
        "v_lshl_add_u32 v210, v198, 4, %21\n\t"
        "ds_add_rtn_u32 v210, v210, v191\n\t"
        "v_perm_b32 v222, v251, v248, v177\n\t"
        "v_mad_u32_u16 v234, v248, 32, %15 op_sel:[1,0,0,0]\n\t"
        "v_cmpx_ne_u32 s[86:87], -1, v249\n\t"
        "v_bfe_u32 v199, v251, 13, 3\n\t"
        "v_lshl_add_u32 v211, v199, 4, %21\n\t"
        "ds_add_rtn_u32 v211, v211, v191\n\t"
        "v_perm_b32 v223, v251, v249, v178\n\t"
        "v_mad_u32_u16 v235, v249, 32, %15 op_sel:[1,0,0,0]\n\t"
        "s_and_b64 exec, exec, s[52:53]\n\t"
        "v_cmpx_ne_u32 s[88:89], -1, v250\n\t"
        "v_bfe_u32 v200, v251, 21, 3\n\t"
        "v_lshl_add_u32 v212, v200, 4, %21\n\t"
        "ds_add_rtn_u32 v212, v212, v191\n\t"
        "v_perm_b32 v224, v251, v250, v179\n\t"
        "v_mad_u32_u16 v236, v250, 32, %15 op_sel:[1,0,0,0]\n\t"
        "s_mov_b64 exec, s[78:79]\n\t"
        "s_waitcnt lgkmcnt(6)\n\t"
        "v_min_u32 v207, v181, v207\n\t"
        "v_min_u32 v208, v181, v208\n\t"
        "v_min_u32 v209, v181, v209\n\t"
        "v_mad_u32_u24 v207, v195, v180, v207\n\t"
        "v_mad_u32_u24 v208, v196, v180, v208\n\t"
        "v_mad_u32_u24 v209, v197, v180, v209\n\t"
        "ds_write2st64_b32 v207, v219, v231 offset1:10\n\t"
        "s_mov_b64 exec, s[80:81]\n\t"
        "ds_write2st64_b32 v208, v220, v232 offset1:10\n\t"
        "s_mov_b64 exec, s[82:83]\n\t"
        "ds_write2st64_b32 v209, v221, v233 offset1:10\n\t"
        "s_mov_b64 exec, -1\n\t"
        "ds_read_b128 v[184:187], %22\n\t"
        "ds_read_b32 v183, %23\n\t"
        "s_mov_b64 exec, s[70:71]\n\t"
        "v_cmpx_ne_u32 s[90:91], -1, v252\n\t"
        "v_bfe_u32 v201, v255, 5, 3\n\t"
        "v_lshl_add_u32 v213, v201, 4, %21\n\t"
        "ds_add_rtn_u32 v213, v213, v191\n\t"
        "v_perm_b32 v225, v255, v252, v177\n\t"
        "v_mad_u32_u16 v237, v252, 32, %16 op_sel:[1,0,0,0]\n\t"
        "v_cmpx_ne_u32 s[92:93], -1, v253\n\t"
        "v_bfe_u32 v202, v255, 13, 3\n\t"
        "v_lshl_add_u32 v214, v202, 4, %21\n\t"
        "ds_add_rtn_u32 v214, v214, v191\n\t"
        "v_perm_b32 v226, v255, v253, v178\n\t"
        "v_mad_u32_u16 v238, v253, 32, %16 op_sel:[1,0,0,0]\n\t"
        "s_and_b64 exec, exec, s[52:53]\n\t"
        "v_cmpx_ne_u32 s[94:95], -1, v254\n\t"
        "v_bfe_u32 v203, v255, 21, 3\n\t"
        "v_lshl_add_u32 v215, v203, 4, %21\n\t"
        "ds_add_rtn_u32 v215, v215, v191\n\t"
        "v_perm_b32 v227, v255, v254, v179\n\t"
        "v_mad_u32_u16 v239, v254, 32, %16 op_sel:[1,0,0,0]\n\t"
        "s_cmp_eq_u64 s[54:55], 0\n\t"
        "s_cbranch_scc1 59f\n\t"
        "s_mov_b64 exec, s[56:57]\n\t"
        "s_cbranch_execz 40f\n\t"
        "v_bfe_u32 v188, %4, 7, 8\n\t"
        "v_mov_b32 v189, 1\n\t"
        "ds_write_b8 v188, v189 offset:256\n\t"
        "v_add_u32 %4, s96, %4\n\t"
        "s_or_b64 %0, %0, exec\n\t"
        "40:\n\t"
        "s_mov_b64 exec, %0\n\t"
        "s_cbranch_execz 50f\n\t"
        "global_load_dwordx4 v[240:243], %4, %17 sc1\n\t"
        "50:\n\t"
        "s_mov_b64 exec, s[58:59]\n\t"
        "s_cbranch_execz 41f\n\t"
        "v_bfe_u32 v188, %5, 7, 8\n\t"
        "v_mov_b32 v189, 1\n\t"
        "ds_write_b8 v188, v189 offset:256\n\t"
        "v_add_u32 %5, s96, %5\n\t"
        "s_or_b64 %1, %1, exec\n\t"
        "41:\n\t"
        "s_mov_b64 exec, %1\n\t"
        "s_cbranch_execz 51f\n\t"
        "global_load_dwordx4 v[244:247], %5, %17 sc1\n\t"
        "51:\n\t"
        "s_mov_b64 exec, s[60:61]\n\t"
        "s_cbranch_execz 42f\n\t"
        "v_bfe_u32 v188, %6, 7, 8\n\t"
        "v_mov_b32 v189, 1\n\t"
        "ds_write_b8 v188, v189 offset:256\n\t"
        "v_add_u32 %6, s96, %6\n\t"
        "s_or_b64 %2, %2, exec\n\t"
        "42:\n\t"
        "s_mov_b64 exec, %2\n\t"
        "s_cbranch_execz 52f\n\t"
        "global_load_dwordx4 v[248:251], %6, %17 sc1\n\t"
        "52:\n\t"
        "s_mov_b64 exec, s[62:63]\n\t"
        "s_cbranch_execz 43f\n\t"
        "v_bfe_u32 v188, %7, 7, 8\n\t"
        "v_mov_b32 v189, 1\n\t"
        "ds_write_b8 v188, v189 offset:256\n\t"
        "v_add_u32 %7, s96, %7\n\t"
        "s_or_b64 %3, %3, exec\n\t"
        "43:\n\t"
        "s_mov_b64 exec, %3\n\t"
        "s_cbranch_execz 53f\n\t"
        "global_load_dwordx4 v[252:255], %7, %17 sc1\n\t"
        "53:\n\t"
        "s_waitcnt lgkmcnt(0)\n\t"
        "59:\n\t"
        "s_mov_b64 exec, s[84:85]\n\t"
        "s_waitcnt lgkmcnt(8)\n\t"
        "v_min_u32 v210, v181, v210\n\t"
        "v_min_u32 v211, v181, v211\n\t"
        "v_min_u32 v212, v181, v212\n\t"
        "v_mad_u32_u24 v210, v198, v180, v210\n\t"
        "v_mad_u32_u24 v211, v199, v180, v211\n\t"
        "v_mad_u32_u24 v212, v200, v180, v212\n\t"
        "ds_write2st64_b32 v210, v222, v234 offset1:10\n\t"
        "s_mov_b64 exec, s[86:87]\n\t"
        "ds_write2st64_b32 v211, v223, v235 offset1:10\n\t"
        "s_mov_b64 exec, s[88:89]\n\t"
        "ds_write2st64_b32 v212, v224, v236 offset1:10\n\t"
        "s_mov_b64 exec, -1\n\t"
        "s_movk_i32 s96, 128\n\t"
        "s_mov_b32 m0, %29\n\t"
        "s_waitcnt lgkmcnt(6)\n\t"
        "v_max3_u32 v182, v184, v185, v186\n\t"
        "v_max3_u32 v182, v182, v187, v183\n\t"
        "v_cmp_eq_u32 vcc, -1, v182\n\t"
        "v_mad_u32_u16 v184, v184, s96, %24\n\t"
        "v_mad_u32_u16 v185, v185, s96, %25\n\t"
        "v_mad_u32_u16 v186, v186, s96, %24\n\t"
        "v_mad_u32_u16 v187, v187, s96, %25\n\t"
        "v_add_u32 v183, v183, %26\n\t"
        "s_cbranch_vccnz 60f\n\t"
        "global_load_lds_dwordx4 v184, %27\n\t"
        "s_addk_i32 m0, 1024\n\t"
        "s_nop 0\n\t"
        "global_load_lds_dwordx4 v185, %27\n\t"
        "s_addk_i32 m0, 1024\n\t"
        "s_nop 0\n\t"
        "global_load_lds_dwordx4 v186, %27\n\t"
        "s_addk_i32 m0, 1024\n\t"
        "s_nop 0\n\t"
        "global_load_lds_dwordx4 v187, %27\n\t"
        "s_addk_i32 m0, 1024\n\t"
        "s_nop 0\n\t"
        "global_load_lds_dwordx4 v183, %28\n\t"
        "s_mov_b32 %12, 1\n\t"
        "60:\n\t"
        "s_mov_b64 exec, s[90:91]\n\t"
        "s_waitcnt lgkmcnt(3)\n\t"
        "v_min_u32 v213, v181, v213\n\t"
        "v_min_u32 v214, v181, v214\n\t"
        "v_min_u32 v215, v181, v215\n\t"
        "v_mad_u32_u24 v213, v201, v180, v213\n\t"
        "v_mad_u32_u24 v214, v202, v180, v214\n\t"
        "v_mad_u32_u24 v215, v203, v180, v215\n\t"
        "ds_write2st64_b32 v213, v225, v237 offset1:10\n\t"
        "s_mov_b64 exec, s[92:93]\n\t"
        "ds_write2st64_b32 v214, v226, v238 offset1:10\n\t"
        "s_mov_b64 exec, s[94:95]\n\t"
        "ds_write2st64_b32 v215, v227, v239 offset1:10\n\t"
        "s_mov_b64 exec, -1\n\t"
        : "=&s"(pend[0]), "=&s"(pend[1]), "=&s"(pend[2]), "=&s"(pend[3]), "+v"(vo[0]), "+v"(vo[1]), "+v"(vo[2]),
          "+v"(vo[3]), "+{v[240:243]}"(cv[0]), "+{v[244:247]}"(cv[1]), "+{v[248:251]}"(cv[2]), "+{v[252:255]}"(cv[3]),
          "=&s"(early)
        : "v"(eb[0]), "v"(eb[1]), "v"(eb[2]), "v"(eb[3]), "s"(ib), "n"(OFF_L + LARR - 16), "v"(tag[0]), "v"(tag[1]),
          "v"(rq4), "v"(aLX + wave * LIST_BYTES), "v"(aLE + wave * LIST_BYTES), "v"(cx0), "v"(cx1), "v"(ce), "s"(x),
          "s"(ES), "s"(lds0 + OFF_R0 + wave * RING_SLOT), "n"(3), "n"(1)
        :
          "memory", "vcc", "scc", "m0", "v177", "v178", "v179", "v180", "v181", "v182", "v183", "v184", "v185",
          "v186", "v187", "v188", "v189", "v190", "v191", "v192", "v193", "v194", "v195", "v196", "v197", "v198",
          "v199", "v200", "v201", "v202", "v203", "v204", "v205", "v206", "v207", "v208", "v209", "v210", "v211",
          "v212", "v213", "v214", "v215", "v216", "v217", "v218", "v219", "v220", "v221", "v222", "v223", "v224",
          "v225", "v226", "v227", "v228", "v229", "v230", "v231", "v232", "v233", "v234", "v235", "v236", "v237",
          "v238", "v239", "s52", "s53", "s54", "s55", "s56", "s57", "s58", "s59", "s60", "s61", "s62", "s63", "s64",
          "s65", "s66", "s67", "s68", "s69", "s70", "s71", "s72", "s73", "s74", "s75", "s76", "s77", "s78", "s79",
          "s80", "s81", "s82", "s83", "s84", "s85", "s86", "s87", "s88", "s89", "s90", "s91", "s92", "s93", "s94",
          "s95", "s96");
  auto uni = [&]() {
#pragma unroll
    for (int k = 0; k < 4; ++k)
      pend[k] = ((unsigned long long)(unsigned)__builtin_amdgcn_readfirstlane((unsigned)(pend[k] >> 32)) << 32) |
                (unsigned)__builtin_amdgcn_readfirstlane((unsigned)pend[k]);
  };
  uni();
  early = __builtin_amdgcn_readfirstlane(early);
  just = early;
  if (__builtin_expect((pend[0] | pend[1] | pend[2] | pend[3] | (unsigned long long)(early ^ 1u)) != 0ull, 0))
#pragma clang loop unroll(disable)
  while (true) {
    if (!early) {
      const u32x4 sx = ld128(aLX + wave * LIST_BYTES);
      const unsigned re = *(const LAS unsigned*)(size_t)(aLE + wave * LIST_BYTES);
      if (__ballot(max(max(max(sx[0], sx[1]), max(sx[2], sx[3])), re) == ~0u) == 0ull) {
        gather5(sx, re, lds0 + OFF_R0 + wave * RING_SLOT);
        early = 1u;
        just = 1u;
      }
    }
    if ((pend[0] | pend[1] | pend[2] | pend[3]) == 0ull || --spin < 0) break;
    asm volatile(
        "s_mov_b32 s96, 0x800000\n\t"
        "v_mov_b32 v181, %17\n\t"
        "v_mov_b32 v180, 5120\n\t"
        "v_mov_b32 v177, 0x0c040100\n\t"
        "v_mov_b32 v178, 0x0c050100\n\t"
        "v_mov_b32 v179, 0x0c060100\n\t"
        "s_mov_b32 s52, 0x7f7f7f7f\n\t"
        "s_mov_b32 s53, 0x7f7f7f7f\n\t"
        "v_mov_b32 v191, 16\n\t"
        "s_waitcnt vmcnt(5)\n\t"
        "s_cmp_lg_u32 %21, 0\n\t"
        "s_cbranch_scc1 9f\n\t"
        "s_waitcnt vmcnt(0)\n\t"
        "9:\n\t"
        "v_add3_u32 v228, v240, v241, v242\n\t"
        "v_add3_u32 v229, v244, v245, v246\n\t"
        "v_add3_u32 v230, v248, v249, v250\n\t"
        "v_add3_u32 v231, v252, v253, v254\n\t"
        "v_add3_u32 v228, v228, v243, %18\n\t"
        "v_add3_u32 v229, v229, v247, %18\n\t"
        "v_add3_u32 v230, v230, v251, %18\n\t"
        "v_add3_u32 v231, v231, v255, %18\n\t"
        "v_add_u32_dpp v228, v228, v228 quad_perm:[1,0,3,2] row_mask:0xf bank_mask:0xf\n\t"
        "v_add_u32_dpp v229, v229, v229 quad_perm:[1,0,3,2] row_mask:0xf bank_mask:0xf\n\t"
        "v_add_u32_dpp v230, v230, v230 quad_perm:[1,0,3,2] row_mask:0xf bank_mask:0xf\n\t"
        "v_add_u32_dpp v231, v231, v231 quad_perm:[1,0,3,2] row_mask:0xf bank_mask:0xf\n\t"
        "v_add_u32_dpp v228, v228, v228 quad_perm:[2,3,0,1] row_mask:0xf bank_mask:0xf\n\t"
        "v_add_u32_dpp v229, v229, v229 quad_perm:[2,3,0,1] row_mask:0xf bank_mask:0xf\n\t"
        "v_add_u32_dpp v230, v230, v230 quad_perm:[2,3,0,1] row_mask:0xf bank_mask:0xf\n\t"
        "v_add_u32_dpp v231, v231, v231 quad_perm:[2,3,0,1] row_mask:0xf bank_mask:0xf\n\t"
        "v_add_u32_dpp v228, v228, v228 row_half_mirror row_mask:0xf bank_mask:0xf\n\t"
        "v_add_u32_dpp v229, v229, v229 row_half_mirror row_mask:0xf bank_mask:0xf\n\t"
        "v_add_u32_dpp v230, v230, v230 row_half_mirror row_mask:0xf bank_mask:0xf\n\t"
        "v_add_u32_dpp v231, v231, v231 row_half_mirror row_mask:0xf bank_mask:0xf\n\t"
        "v_cmp_ne_u32_sdwa s[72:73], v243, %19 src0_sel:BYTE_3 src1_sel:DWORD\n\t"
        "v_cmp_gt_u32 s[64:65], 2, v228\n\t"
        "v_cmp_eq_u32 s[56:57], 1, v228\n\t"
        "v_cmp_ne_u32_sdwa s[74:75], v247, %19 src0_sel:BYTE_3 src1_sel:DWORD\n\t"
        "v_cmp_gt_u32 s[66:67], 2, v229\n\t"
        "v_cmp_eq_u32 s[58:59], 1, v229\n\t"
        "v_cmp_ne_u32_sdwa s[76:77], v251, %19 src0_sel:BYTE_3 src1_sel:DWORD\n\t"
        "v_cmp_gt_u32 s[68:69], 2, v230\n\t"
        "v_cmp_eq_u32 s[60:61], 1, v230\n\t"
        "v_cmp_ne_u32_sdwa s[78:79], v255, %19 src0_sel:BYTE_3 src1_sel:DWORD\n\t"
        "v_cmp_gt_u32 s[70:71], 2, v231\n\t"
        "v_cmp_eq_u32 s[62:63], 1, v231\n\t"
        "s_and_b64 s[64:65], s[64:65], %0\n\t"
        "s_and_b64 s[72:73], s[72:73], s[64:65]\n\t"
        "s_cselect_b64 s[64:65], 0, s[64:65]\n\t"
        "s_and_b64 s[56:57], s[56:57], s[64:65]\n\t"
        "s_andn2_b64 %0, %0, s[64:65]\n\t"
        "s_cbranch_scc0 10f\n\t"
        "s_mov_b64 exec, %0\n\t"
        "global_load_dwordx4 v[240:243], %4, %16 sc1\n\t"
        "10:\n\t"
        "s_and_b64 s[66:67], s[66:67], %1\n\t"
        "s_and_b64 s[74:75], s[74:75], s[66:67]\n\t"
        "s_cselect_b64 s[66:67], 0, s[66:67]\n\t"
        "s_and_b64 s[58:59], s[58:59], s[66:67]\n\t"
        "s_andn2_b64 %1, %1, s[66:67]\n\t"
        "s_cbranch_scc0 11f\n\t"
        "s_mov_b64 exec, %1\n\t"
        "global_load_dwordx4 v[244:247], %5, %16 sc1\n\t"
        "11:\n\t"
        "s_and_b64 s[68:69], s[68:69], %2\n\t"
        "s_and_b64 s[76:77], s[76:77], s[68:69]\n\t"
        "s_cselect_b64 s[68:69], 0, s[68:69]\n\t"
        "s_and_b64 s[60:61], s[60:61], s[68:69]\n\t"
        "s_andn2_b64 %2, %2, s[68:69]\n\t"
        "s_cbranch_scc0 12f\n\t"
        "s_mov_b64 exec, %2\n\t"
        "global_load_dwordx4 v[248:251], %6, %16 sc1\n\t"
        "12:\n\t"
        "s_and_b64 s[70:71], s[70:71], %3\n\t"
        "s_and_b64 s[78:79], s[78:79], s[70:71]\n\t"
        "s_cselect_b64 s[70:71], 0, s[70:71]\n\t"
        "s_and_b64 s[62:63], s[62:63], s[70:71]\n\t"
        "s_andn2_b64 %3, %3, s[70:71]\n\t"
        "s_cbranch_scc0 13f\n\t"
        "s_mov_b64 exec, %3\n\t"
        "global_load_dwordx4 v[252:255], %7, %16 sc1\n\t"
        "13:\n\t"
        "s_mov_b64 s[54:55], 0\n\t"
        "s_mov_b64 s[72:73], 0\n\t"
        "s_mov_b64 exec, s[64:65]\n\t"
        "s_cbranch_execz 20f\n\t"
        "v_cmpx_ne_u32 s[72:73], -1, v240\n\t"
        "v_bfe_u32 v192, v243, 5, 3\n\t"
        "v_lshl_add_u32 v204, v192, 4, %20\n\t"
        "ds_add_rtn_u32 v204, v204, v191\n\t"
        "v_perm_b32 v216, v243, v240, v177\n\t"
        "v_mad_u32_u16 v228, v240, 32, %12 op_sel:[1,0,0,0]\n\t"
        "v_cmpx_ne_u32 s[74:75], -1, v241\n\t"
        "v_bfe_u32 v193, v243, 13, 3\n\t"
        "v_lshl_add_u32 v205, v193, 4, %20\n\t"
        "ds_add_rtn_u32 v205, v205, v191\n\t"
        "v_perm_b32 v217, v243, v241, v178\n\t"
        "v_mad_u32_u16 v229, v241, 32, %12 op_sel:[1,0,0,0]\n\t"
        "s_and_b64 exec, exec, s[52:53]\n\t"
        "v_cmpx_ne_u32 s[76:77], -1, v242\n\t"
        "v_bfe_u32 v194, v243, 21, 3\n\t"
        "v_lshl_add_u32 v206, v194, 4, %20\n\t"
        "ds_add_rtn_u32 v206, v206, v191\n\t"
        "v_perm_b32 v218, v243, v242, v179\n\t"
        "v_mad_u32_u16 v230, v242, 32, %12 op_sel:[1,0,0,0]\n\t"
        "s_or_b64 s[54:55], s[54:55], s[56:57]\n\t"
        "20:\n\t"
        "s_mov_b64 s[78:79], 0\n\t"
        "s_mov_b64 exec, s[66:67]\n\t"
        "s_cbranch_execz 21f\n\t"
        "v_cmpx_ne_u32 s[78:79], -1, v244\n\t"
        "v_bfe_u32 v195, v247, 5, 3\n\t"
        "v_lshl_add_u32 v207, v195, 4, %20\n\t"
        "ds_add_rtn_u32 v207, v207, v191\n\t"
        "v_perm_b32 v219, v247, v244, v177\n\t"
        "v_mad_u32_u16 v231, v244, 32, %13 op_sel:[1,0,0,0]\n\t"
        "v_cmpx_ne_u32 s[80:81], -1, v245\n\t"
        "v_bfe_u32 v196, v247, 13, 3\n\t"
        "v_lshl_add_u32 v208, v196, 4, %20\n\t"
        "ds_add_rtn_u32 v208, v208, v191\n\t"
        "v_perm_b32 v220, v247, v245, v178\n\t"
        "v_mad_u32_u16 v232, v245, 32, %13 op_sel:[1,0,0,0]\n\t"
        "s_and_b64 exec, exec, s[52:53]\n\t"
        "v_cmpx_ne_u32 s[82:83], -1, v246\n\t"
        "v_bfe_u32 v197, v247, 21, 3\n\t"
        "v_lshl_add_u32 v209, v197, 4, %20\n\t"
        "ds_add_rtn_u32 v209, v209, v191\n\t"
        "v_perm_b32 v221, v247, v246, v179\n\t"
        "v_mad_u32_u16 v233, v246, 32, %13 op_sel:[1,0,0,0]\n\t"
        "s_or_b64 s[54:55], s[54:55], s[58:59]\n\t"
        "21:\n\t"
        "s_mov_b64 s[84:85], 0\n\t"
        "s_mov_b64 exec, s[68:69]\n\t"
        "s_cbranch_execz 22f\n\t"
        "v_cmpx_ne_u32 s[84:85], -1, v248\n\t"
        "v_bfe_u32 v198, v251, 5, 3\n\t"
        "v_lshl_add_u32 v210, v198, 4, %20\n\t"
        "ds_add_rtn_u32 v210, v210, v191\n\t"
        "v_perm_b32 v222, v251, v248, v177\n\t"
        "v_mad_u32_u16 v234, v248, 32, %14 op_sel:[1,0,0,0]\n\t"
        "v_cmpx_ne_u32 s[86:87], -1, v249\n\t"
        "v_bfe_u32 v199, v251, 13, 3\n\t"
        "v_lshl_add_u32 v211, v199, 4, %20\n\t"
        "ds_add_rtn_u32 v211, v211, v191\n\t"
        "v_perm_b32 v223, v251, v249, v178\n\t"
        "v_mad_u32_u16 v235, v249, 32, %14 op_sel:[1,0,0,0]\n\t"
        "s_and_b64 exec, exec, s[52:53]\n\t"
        "v_cmpx_ne_u32 s[88:89], -1, v250\n\t"
        "v_bfe_u32 v200, v251, 21, 3\n\t"
        "v_lshl_add_u32 v212, v200, 4, %20\n\t"
        "ds_add_rtn_u32 v212, v212, v191\n\t"
        "v_perm_b32 v224, v251, v250, v179\n\t"
        "v_mad_u32_u16 v236, v250, 32, %14 op_sel:[1,0,0,0]\n\t"
        "s_or_b64 s[54:55], s[54:55], s[60:61]\n\t"
        "22:\n\t"
        "s_mov_b64 s[90:91], 0\n\t"
        "s_mov_b64 exec, s[70:71]\n\t"
        "s_cbranch_execz 23f\n\t"
        "v_cmpx_ne_u32 s[90:91], -1, v252\n\t"
        "v_bfe_u32 v201, v255, 5, 3\n\t"
        "v_lshl_add_u32 v213, v201, 4, %20\n\t"
        "ds_add_rtn_u32 v213, v213, v191\n\t"
        "v_perm_b32 v225, v255, v252, v177\n\t"
        "v_mad_u32_u16 v237, v252, 32, %15 op_sel:[1,0,0,0]\n\t"
        "v_cmpx_ne_u32 s[92:93], -1, v253\n\t"
        "v_bfe_u32 v202, v255, 13, 3\n\t"
        "v_lshl_add_u32 v214, v202, 4, %20\n\t"
        "ds_add_rtn_u32 v214, v214, v191\n\t"
        "v_perm_b32 v226, v255, v253, v178\n\t"
        "v_mad_u32_u16 v238, v253, 32, %15 op_sel:[1,0,0,0]\n\t"
        "s_and_b64 exec, exec, s[52:53]\n\t"
        "v_cmpx_ne_u32 s[94:95], -1, v254\n\t"
        "v_bfe_u32 v203, v255, 21, 3\n\t"
        "v_lshl_add_u32 v215, v203, 4, %20\n\t"
        "ds_add_rtn_u32 v215, v215, v191\n\t"
        "v_perm_b32 v227, v255, v254, v179\n\t"
        "v_mad_u32_u16 v239, v254, 32, %15 op_sel:[1,0,0,0]\n\t"
        "s_or_b64 s[54:55], s[54:55], s[62:63]\n\t"
        "23:\n\t"
        "s_cmp_eq_u64 s[54:55], 0\n\t"
        "s_cbranch_scc1 59f\n\t"
        "s_mov_b64 exec, s[56:57]\n\t"
        "s_cbranch_execz 40f\n\t"
        "v_bfe_u32 v188, %4, 7, 8\n\t"
        "v_lshrrev_b32 v189, 23, %4\n\t"
        "v_add_u32 v189, 1, v189\n\t"
        "ds_write_b8 v188, v189 offset:256\n\t"
        "v_add_u32 %4, s96, %4\n\t"
        "v_mov_b32 v190, 0\n\t"
        "v_cmpx_eq_u32 vcc, 2, v189\n\t"
        "ds_write_b32 v190, v191 offset:160\n\t"
        "s_andn2_b64 exec, s[56:57], vcc\n\t"
        "s_cbranch_execz 40f\n\t"
        "s_or_b64 %0, %0, exec\n\t"
        "global_load_dwordx4 v[240:243], %4, %16 sc1\n\t"
        "40:\n\t"
        "s_mov_b64 exec, s[58:59]\n\t"
        "s_cbranch_execz 41f\n\t"
        "v_bfe_u32 v188, %5, 7, 8\n\t"
        "v_lshrrev_b32 v189, 23, %5\n\t"
        "v_add_u32 v189, 1, v189\n\t"
        "ds_write_b8 v188, v189 offset:256\n\t"
        "v_add_u32 %5, s96, %5\n\t"
        "v_mov_b32 v190, 0\n\t"
        "v_cmpx_eq_u32 vcc, 2, v189\n\t"
        "ds_write_b32 v190, v191 offset:160\n\t"
        "s_andn2_b64 exec, s[58:59], vcc\n\t"
        "s_cbranch_execz 41f\n\t"
        "s_or_b64 %1, %1, exec\n\t"
        "global_load_dwordx4 v[244:247], %5, %16 sc1\n\t"
        "41:\n\t"
        "s_mov_b64 exec, s[60:61]\n\t"
        "s_cbranch_execz 42f\n\t"
        "v_bfe_u32 v188, %6, 7, 8\n\t"
        "v_lshrrev_b32 v189, 23, %6\n\t"
        "v_add_u32 v189, 1, v189\n\t"
        "ds_write_b8 v188, v189 offset:256\n\t"
        "v_add_u32 %6, s96, %6\n\t"
        "v_mov_b32 v190, 0\n\t"
        "v_cmpx_eq_u32 vcc, 2, v189\n\t"
        "ds_write_b32 v190, v191 offset:160\n\t"
        "s_andn2_b64 exec, s[60:61], vcc\n\t"
        "s_cbranch_execz 42f\n\t"
        "s_or_b64 %2, %2, exec\n\t"
        "global_load_dwordx4 v[248:251], %6, %16 sc1\n\t"
        "42:\n\t"
        "s_mov_b64 exec, s[62:63]\n\t"
        "s_cbranch_execz 43f\n\t"
        "v_bfe_u32 v188, %7, 7, 8\n\t"
        "v_lshrrev_b32 v189, 23, %7\n\t"
        "v_add_u32 v189, 1, v189\n\t"
        "ds_write_b8 v188, v189 offset:256\n\t"
        "v_add_u32 %7, s96, %7\n\t"
        "v_mov_b32 v190, 0\n\t"
        "v_cmpx_eq_u32 vcc, 2, v189\n\t"
        "ds_write_b32 v190, v191 offset:160\n\t"
        "s_andn2_b64 exec, s[62:63], vcc\n\t"
        "s_cbranch_execz 43f\n\t"
        "s_or_b64 %3, %3, exec\n\t"
        "global_load_dwordx4 v[252:255], %7, %16 sc1\n\t"
        "43:\n\t"
        "59:\n\t"
        "s_waitcnt lgkmcnt(0)\n\t"
        "s_mov_b64 exec, s[72:73]\n\t"
        "s_cbranch_execz 30f\n\t"
        "v_min_u32 v204, v181, v204\n\t"
        "v_min_u32 v205, v181, v205\n\t"
        "v_min_u32 v206, v181, v206\n\t"
        "v_mad_u32_u24 v204, v192, v180, v204\n\t"
        "v_mad_u32_u24 v205, v193, v180, v205\n\t"
        "v_mad_u32_u24 v206, v194, v180, v206\n\t"
        "ds_write2st64_b32 v204, v216, v228 offset1:10\n\t"
        "s_mov_b64 exec, s[74:75]\n\t"
        "ds_write2st64_b32 v205, v217, v229 offset1:10\n\t"
        "s_mov_b64 exec, s[76:77]\n\t"
        "ds_write2st64_b32 v206, v218, v230 offset1:10\n\t"
        "30:\n\t"
        "s_mov_b64 exec, s[78:79]\n\t"
        "s_cbranch_execz 31f\n\t"
        "v_min_u32 v207, v181, v207\n\t"
        "v_min_u32 v208, v181, v208\n\t"
        "v_min_u32 v209, v181, v209\n\t"
        "v_mad_u32_u24 v207, v195, v180, v207\n\t"
        "v_mad_u32_u24 v208, v196, v180, v208\n\t"
        "v_mad_u32_u24 v209, v197, v180, v209\n\t"
        "ds_write2st64_b32 v207, v219, v231 offset1:10\n\t"
        "s_mov_b64 exec, s[80:81]\n\t"
        "ds_write2st64_b32 v208, v220, v232 offset1:10\n\t"
        "s_mov_b64 exec, s[82:83]\n\t"
        "ds_write2st64_b32 v209, v221, v233 offset1:10\n\t"
        "31:\n\t"
        "s_mov_b64 exec, s[84:85]\n\t"
        "s_cbranch_execz 32f\n\t"
        "v_min_u32 v210, v181, v210\n\t"
        "v_min_u32 v211, v181, v211\n\t"
        "v_min_u32 v212, v181, v212\n\t"
        "v_mad_u32_u24 v210, v198, v180, v210\n\t"
        "v_mad_u32_u24 v211, v199, v180, v211\n\t"
        "v_mad_u32_u24 v212, v200, v180, v212\n\t"
        "ds_write2st64_b32 v210, v222, v234 offset1:10\n\t"
        "s_mov_b64 exec, s[86:87]\n\t"
        "ds_write2st64_b32 v211, v223, v235 offset1:10\n\t"
        "s_mov_b64 exec, s[88:89]\n\t"
        "ds_write2st64_b32 v212, v224, v236 offset1:10\n\t"
        "32:\n\t"
        "s_mov_b64 exec, s[90:91]\n\t"
        "s_cbranch_execz 33f\n\t"
        "v_min_u32 v213, v181, v213\n\t"
        "v_min_u32 v214, v181, v214\n\t"
        "v_min_u32 v215, v181, v215\n\t"
        "v_mad_u32_u24 v213, v201, v180, v213\n\t"
        "v_mad_u32_u24 v214, v202, v180, v214\n\t"
        "v_mad_u32_u24 v215, v203, v180, v215\n\t"
        "ds_write2st64_b32 v213, v225, v237 offset1:10\n\t"
        "s_mov_b64 exec, s[92:93]\n\t"
        "ds_write2st64_b32 v214, v226, v238 offset1:10\n\t"
        "s_mov_b64 exec, s[94:95]\n\t"
        "ds_write2st64_b32 v215, v227, v239 offset1:10\n\t"
        "33:\n\t"
        "s_mov_b64 exec, -1\n\t"
        : "+&s"(pend[0]), "+&s"(pend[1]), "+&s"(pend[2]), "+&s"(pend[3]), "+v"(vo[0]), "+v"(vo[1]), "+v"(vo[2]),
          "+v"(vo[3]), "+{v[240:243]}"(cv[0]), "+{v[244:247]}"(cv[1]), "+{v[248:251]}"(cv[2]), "+{v[252:255]}"(cv[3])
        : "v"(eb[0]), "v"(eb[1]), "v"(eb[2]), "v"(eb[3]), "s"(ib), "n"(OFF_L + LARR - 16), "v"(tag[0]), "v"(tag[1]),
          "v"(rq4), "s"(just)
        :
          "memory", "vcc", "scc", "v177", "v178", "v179", "v180", "v181", "v188", "v189", "v190", "v191", "v192",
          "v193", "v194", "v195", "v196", "v197", "v198", "v199", "v200", "v201", "v202", "v203", "v204", "v205",
          "v206", "v207", "v208", "v209", "v210", "v211", "v212", "v213", "v214", "v215", "v216", "v217", "v218",
          "v219", "v220", "v221", "v222", "v223", "v224", "v225", "v226", "v227", "v228", "v229", "v230", "v231",
          "v232", "v233", "v234", "v235", "v236", "v237", "v238", "v239", "s52", "s53", "s54", "s55", "s56", "s57",
          "s58", "s59", "s60", "s61", "s62", "s63", "s64", "s65", "s66", "s67", "s68", "s69", "s70", "s71", "s72",
          "s73", "s74", "s75", "s76", "s77", "s78", "s79", "s80", "s81", "s82", "s83", "s84", "s85", "s86", "s87",
          "s88", "s89", "s90", "s91", "s92", "s93", "s94", "s95", "s96");
    uni();
    just = 0u;
  }
  *(LAS unsigned*)((LAS char*)smem + OFF_CNT + 128 + wave * 4) = early;
  asm volatile("s_waitcnt lgkmcnt(0)\n\ts_barrier" ::: "memory");
  if (wave >= 4) __builtin_amdgcn_s_setprio(1);
  const u32x4 cw = *(const LAS u32x4*)((LAS char*)smem + OFF_CNT + rid * 16);
  const unsigned ef = *(const LAS unsigned*)((LAS char*)smem + OFF_CNT + 128 + rid * 4);
  unsigned slowf = *(const LAS unsigned*)((LAS char*)smem + OFF_CNT + 160);
  asm volatile("" : "+v"(slowf));
  const int key = (int)((cw[0] + cw[1] + cw[2] + cw[3]) | rid);
  int gb;
  {
    unsigned acc = 0u;
#define RK(k) acc = __builtin_amdgcn_alignbit(acc, (unsigned)(__builtin_amdgcn_update_dpp(0, key, 0x120 + k, 0xf, 0xf, false) - key), 31)
    RK(1); RK(2); RK(3); RK(4); RK(5); RK(6); RK(7);
#undef RK
    const int want = RANKOF(wave);
    gb = __builtin_ctzll(__ballot(__builtin_popcount(acc) == want)) & 7;
  }
  const unsigned ring0 = lds0 + OFF_R0 + gb * RING_SLOT;
  const unsigned la = l0 + gb * LIST_BYTES;
  bf16x8 XAf[2][2];
#pragma unroll
  for (int q = 0; q < 4; ++q) XAf[q >> 1][q & 1] = *(const LAS bf16x8*)(size_t)(ring0 + ringd + q * 1024 + lane * 16);
  int cnt = (int)(__builtin_amdgcn_readlane(key, gb) - 4u * l0 - 24u) >> 4;
  const unsigned cmn = __builtin_amdgcn_readlane(min(min(cw[0], cw[1]), min(cw[2], cw[3])), gb) & ~15u;
  const unsigned cmx = __builtin_amdgcn_readlane(max(max(cw[0], cw[1]), max(cw[2], cw[3])), gb) & ~15u;
  const bool slow = cnt > LCAP || cmx > l0 + LARR - 16 || __builtin_amdgcn_readfirstlane(slowf) != 0u;
  const unsigned cg = (bu * 8 + gb) * 32;
  aLX += gb * LIST_BYTES; aLE += gb * LIST_BYTES; aLS4 += gb * LIST_BYTES; aLS1 += gb * LIST_BYTES;
  auto issue_gather = [&](int tile, int rs) {
    gather5(ld128(aLX + tile * 128), *(const LAS unsigned*)(size_t)(aLE + tile * 128), ring0 + rs * ringd);
  };
  auto pads = [&](int cnt, int lane) __attribute__((always_inline)) {
    const int u = cnt + lane;
    const unsigned c = cg + ((u >> 2) & 7) + 8 * (u & 3), n = nodeof(c);
    const unsigned padA = n | (__builtin_amdgcn_readlane(c, __builtin_ctzll(__ballot((n * Mw + Cw) >> 16 != c))) & 0xffu) << 16;
    const unsigned pa = la + u * 4;
    *(LAS unsigned*)(size_t)pa = padA;
    *(LAS unsigned*)(size_t)(pa + 256) = padA;
    *(LAS unsigned*)(size_t)(pa + LARR) = 0u;
    *(LAS unsigned*)(size_t)(pa + LARR + 256) = 0u;
  };
  constexpr int GEND = 64 + NCH;
  int gs = slow ? 0 : GEND, gp = 0;
  auto gen = [&]() __attribute__((always_inline)) {
    int l2, n = 0;
    asm volatile("v_mbcnt_lo_u32_b32 %0, -1, 0\n\tv_mbcnt_hi_u32_b32 %0, -1, %0" : "=v"(l2));
    const LAS unsigned char* xf = (const LAS unsigned char*)smem + OFF_XF;
    auto put = [&](bool ok, unsigned t, unsigned c, int cc) {
      ok = ok && (int)(c >> 5) == gb;
      const unsigned long long bm = __ballot(ok);
      const unsigned pa = la + 4u * (n + __builtin_amdgcn_mbcnt_hi((unsigned)(bm >> 32), __builtin_amdgcn_mbcnt_lo((unsigned)bm, 0u)));
      if (ok) {
        *(LAS unsigned*)(size_t)pa = (t & 0xffffu) | (c << 16);
        *(LAS unsigned*)(size_t)(pa + LARR) = ((unsigned)(cc * chunk) + (t >> 16)) * 32u;
      }
      n += __builtin_popcountll(bm);
    };
#pragma unroll 1
    while (gs < GEND && n <= LCAP - 192) {
      if (gs < 64) {
        const int cc = (gs & 31) * 8 + (l2 >> 3);
        const unsigned* pp = reinterpret_cast<const unsigned*>(ib + (gs >> 5) * IEXT + (gs & 31) * 1024 + l2 * 16);
        const bool live = gs < 32 || xf[cc] != 0;
        const unsigned m = ldd(pp + 3);
#pragma unroll 1
        for (int j = 0; j < 3; ++j) {
          const unsigned t = ldd(pp + j);
          put(live && t != ~0u && (j < 2 || (l2 & 7) != 7), t, (m >> (8 * j)) & 0xffu, cc);
        }
        ++gs;
      } else {
        const int c2 = gs - 64 + l2;
        const unsigned long long fm = __ballot(c2 < NCH && xf[min(c2, NCH - 1)] == 2);
        if (fm == 0ull) { gs = min(gs + 64, GEND); continue; }
        gs += __builtin_ctzll(fm);
        const int cc = gs - 64;
        const unsigned long long ds = ldq(FL + 512 + bu * NCH + cc);
        const int len = __builtin_amdgcn_readfirstlane((int)(ds >> 32));
        const unsigned long long* tp = TS + (unsigned)ds;
#pragma unroll 1
        for (; gp < len && n <= LCAP - 64; gp += 64) {
          const unsigned long long t = ldq(tp + min(gp + l2, len - 1));
          put(gp + l2 < len, (unsigned)t, (unsigned)(t >> 32), cc);
        }
        if (gp >= len) { gp = 0; ++gs; }
      }
    }
    __builtin_amdgcn_s_waitcnt(0x0F70);
    pads(n, l2);
    return n;
  };
  const bool quick = !slow && cmn >= l0 + 256u && __builtin_amdgcn_readlane(ef, gb) != 0u;
  auto gather1 = [&](unsigned skip) {
    u32x4 sx = ld128(aLX + 128);
    unsigned re = *(const LAS unsigned*)(size_t)(aLE + 128);
    asm volatile("s_cmp_lg_u32 %16, 0\n\t"
                 "s_cbranch_scc1 1f\n\t"
                 "s_mov_b32 m0, %14\n\t"
                 "v_mad_u32_u16 %0, %0, %15, %9\n\t"
                 "v_mad_u32_u16 %1, %1, %15, %10\n\t"
                 "global_load_lds_dwordx4 %0, %12\n\t"
                 "s_addk_i32 m0, 1024\n\t"
                 "v_mad_u32_u16 %2, %2, %15, %9\n\t"
                 "global_load_lds_dwordx4 %1, %12\n\t"
                 "s_addk_i32 m0, 1024\n\t"
                 "v_mad_u32_u16 %3, %3, %15, %10\n\t"
                 "global_load_lds_dwordx4 %2, %12\n\t"
                 "s_addk_i32 m0, 1024\n\t"
                 "v_add_u32 %4, %4, %11\n\t"
                 "global_load_lds_dwordx4 %3, %12\n\t"
                 "s_addk_i32 m0, 1024\n\t"
                 "s_nop 0\n\t"
                 "global_load_lds_dwordx4 %4, %13\n"
                 "1:"
                 : "+v"(sx[0]), "+v"(sx[1]), "+v"(sx[2]), "+v"(sx[3]), "+v"(re), "+v"(XAf[0][0]), "+v"(XAf[0][1]),
                   "+v"(XAf[1][0]), "+v"(XAf[1][1])
                 : "v"(cx0), "v"(cx1), "v"(ce), "s"(x), "s"(ES), "s"(ring0 + ringd), "s"(128u), "s"(skip)
                 : "memory", "m0", "scc");
  };
  const unsigned g1 = __builtin_amdgcn_readfirstlane(quick && wave >= G1W ? 1u : 0u);
  gather1(g1 ^ 1u);
  if (__builtin_expect(!slow, 1)) {
    const unsigned cl = *(const LAS unsigned*)((LAS char*)smem + OFF_CNT + gb * 16 + (lane & 3) * 4);
    unsigned q = cmn + gb * LIST_BYTES;
#pragma unroll 1
    for (unsigned i = cmn; i < cmx; i += 256u) {
      const unsigned pp = i + lane * 4u;
      const unsigned ea = *(const LAS unsigned*)(size_t)(pp + gb * LIST_BYTES);
      const unsigned eb_ = *(const LAS unsigned*)(size_t)(pp + gb * LIST_BYTES + LARR);
      const unsigned long long bm = __ballot(pp < cl);
      const unsigned pa = q + 4u * __builtin_amdgcn_mbcnt_hi((unsigned)(bm >> 32), __builtin_amdgcn_mbcnt_lo((unsigned)bm, 0u));
      if (pp < cl) {
        *(LAS unsigned*)(size_t)pa = ea;
        *(LAS unsigned*)(size_t)(pa + LARR) = eb_;
      }
      q += 4u * __builtin_popcountll(bm);
    }
    pads(cnt, lane);
  }
#pragma unroll
  for (int ct = 0; ct < 2; ++ct)
#pragma unroll
    for (int m = 6; m < 15; ++m)
      if ((HELD >> m) & 1) wr[ct][m] = wl[widx(m, ct) * 64];
#pragma unroll
  for (int ct = 0; ct < 2; ++ct)
#pragma unroll
    for (int m = 6; m < 15; ++m)
      if ((HELD >> m) & 1) asm volatile("" : "+v"(wr[ct][m]));
#pragma unroll
  for (int t = 0; t < 4; ++t) fo[t] += gb * RING_SLOT;
  foe += gb * RING_SLOT;
  cS -= gb * 32 * TPITCH;
  cSt -= gb * 32 * TPITCH;
  asm volatile("s_waitcnt vmcnt(5)\n\ts_cmp_lg_u32 %7, 0\n\ts_cbranch_scc1 1f\n\ts_waitcnt vmcnt(0)\n1:\n\ts_barrier"
               : "+v"(fo[0]), "+v"(fo[1]), "+v"(fo[2]), "+v"(fo[3]), "+v"(foe), "+v"(cS), "+v"(cSt) : "s"(g1) : "memory", "scc");
  bool redo = !quick;
  gather1(__builtin_amdgcn_readfirstlane(quick ? g1 : 1u));
  auto rd128 = [&](unsigned a) { return *(const LAS bf16x8*)(size_t)a; };
  auto trow = [&](unsigned a, unsigned c) {
    unsigned t;
    asm("v_mad_u32_u16 %0, %1, %2, %3 op_sel:[1,0,0,0]" : "=v"(t) : "v"(a), "s"(TPITCH), "v"(c));
    return t;
  };
  auto load_S = [&](const u32x4 ss) {
    const s16x4 a0 = __builtin_amdgcn_ds_read_tr16_b64_v4i16((LAS s16x4*)(size_t)trow(ss[0], cS));
    const s16x4 a1 = __builtin_amdgcn_ds_read_tr16_b64_v4i16((LAS s16x4*)(size_t)trow(ss[1], cS));
    const s16x4 a2 = __builtin_amdgcn_ds_read_tr16_b64_v4i16((LAS s16x4*)(size_t)trow(ss[2], cS));
    const s16x4 a3 = __builtin_amdgcn_ds_read_tr16_b64_v4i16((LAS s16x4*)(size_t)trow(ss[3], cS));
    S[0] = __builtin_shufflevector(a0, a1, 0, 1, 2, 3, 4, 5, 6, 7);
    S[1] = __builtin_shufflevector(a2, a3, 0, 1, 2, 3, 4, 5, 6, 7);
  };
  auto wf = [&](int m, int ct) { return ((HELD >> m) & 1) ? wr[ct][m] : b[(m - 6) % 3]; };

  auto ew = [&](int m, const f32x16& Gc, const f32x16& Vc, const f32x16& Gn, const f32x16& Vp) {
    if (m < 2) {
      const int i = 14 + m;
      pq[i] = mulf(i, Vp[i], rq[i]);
      if (m == 1) pn[1][3] = pack_bf16(pq[14], pq[15]);
    } else if (m == 2) {
      pq[0] = mulf(0, Vc[0], rq[0]);
      pq[1] = mulf(1, Vc[1], rq[1]);
      pn[0][0] = pack_bf16(pq[0], pq[1]);
    } else {
      const int i = m - 1;
      pq[i] = mulf(i, Vc[i], rq[i]);
      if (i & 1) pn[i >> 3][(i >> 1) & 3] = pack_bf16(pq[i - 1], pq[i]);
    }
    if (m == 0) pd[0][0] = pack_bf16(rq[0], rq[1]);
    else if ((m & 1) == 0) { const int i = m + 1; pd[i >> 3][(i >> 1) & 3] = pack_bf16(rq[i - 1], rq[i]); }
    if (m < 14) {
      const int i = m + 2;
      rq[i] = __builtin_amdgcn_rcpf(add1(tq[i]));
    } else {
      rq[0] = __builtin_amdgcn_rcpf(add1(tq[0]));
      rq[1] = __builtin_amdgcn_rcpf(add1(tq[1]));
    }
    if (m < 13) tq[m + 3] = __builtin_amdgcn_exp2f(Gc[m + 3]);
    else if (m == 13) { tq[0] = __builtin_amdgcn_exp2f(Gn[0]); tq[1] = __builtin_amdgcn_exp2f(Gn[1]); }
    else tq[2] = __builtin_amdgcn_exp2f(Gn[2]);
  };

  auto drain = [&]() {
    pq[14] = mulf(0, V1[14], rq[14]);
    pq[15] = mulf(1, V1[15], rq[15]);
    pn[1][3] = pack_bf16(pq[14], pq[15]);
    Y[1] = MFMA32(__builtin_bit_cast(bf16x8, pd[0]), S[0], Y[1]);
    Y[3] = MFMA32(__builtin_bit_cast(bf16x8, pn[0]), S[0], Y[3]);
    Y[1] = MFMA32(__builtin_bit_cast(bf16x8, pd[1]), S[1], Y[1]);
    Y[3] = MFMA32(__builtin_bit_cast(bf16x8, pn[1]), S[1], Y[3]);
  };
  while (true) {
    if (__builtin_expect(redo, 0)) {
      asm volatile("s_waitcnt vmcnt(0)" ::: "memory");
      if (slow) cnt = gen();
      issue_gather(0, 0);
      issue_gather(1, 1);
    }
    const int ntiles = (cnt + 31) >> 5;
    {
      const unsigned sl = *(const LAS unsigned short*)(size_t)aLS1;
      bf16x8 w0[15];
#pragma unroll
      for (int m = 6; m < 15; ++m) w0[m] = ((HELD >> m) & 1) ? wr[0][m] : wl[widx(m, 0) * 64];
      u32x4 sx = ld128(aLX + 256);
      unsigned re = *(const LAS unsigned*)(size_t)(aLE + 256);
      St[0] = rd128(sl * TPITCH + cSt);
      St[1] = rd128(sl * TPITCH + cSt + 16);
      G0 = MFMA32(St[0], XAf[0][0], zero16);
      G0 = MFMA32(St[1], XAf[1][0], G0);
      pipe_zero();
#pragma unroll
      for (int m = 6; m < 15; ++m)
        if (!((HELD >> m) & 1)) asm volatile("" : "+v"(w0[m]));
      asm volatile("s_waitcnt vmcnt(5)" : "+v"(sx), "+v"(re), "+v"(G0) :: "memory");
#pragma unroll
      for (int k = 0; k < 4; ++k) as[k] = rd128(fo[k]);
      ae = rd128(foe);
      SB();
#pragma unroll
      for (int k = 0; k < 4; ++k) G0 = MFMA32(as[k], w0[7 + k], G0);
      G0 = MFMA32(ae, w0[6], G0);
      V0 = MFMA32(as[0], w0[11], zero16);
      SB();
      glds16x(x, sx[0], cx0, ring0);
      glds16x(x, sx[1], cx1, ring0 + 1024);
      V0 = MFMA32(as[1], w0[12], V0);
      SB();
      glds16x(x, sx[2], cx0, ring0 + 2048);
      tq[0] = __builtin_amdgcn_exp2f(G0[0]);
      tq[1] = __builtin_amdgcn_exp2f(G0[1]);
      V0 = MFMA32(as[2], w0[13], V0);
      SB();
      glds16x(x, sx[3], cx1, ring0 + 3072);
      tq[2] = __builtin_amdgcn_exp2f(G0[2]);
      V0 = MFMA32(as[3], w0[14], V0);
      SB();
      glds16e(ES, re, ce, ring0 + 4096);
      rq[0] = __builtin_amdgcn_rcpf(add1(tq[0]));
      rq[1] = __builtin_amdgcn_rcpf(add1(tq[1]));
      SB();
    }
    unsigned &lS4 = aLS4, &lS1 = aLS1, &lX = aLX, &lE = aLE;
    int adv = 0;
    auto body = [&](const int par) {
      const unsigned lo = par * 128;
      const unsigned ro = (1 - par) * ringd;
      {
        u32x4 ss;
        unsigned sl;
#pragma unroll
        for (int m = 0; m < 15; ++m) {
          if (m == 0) Y[1] = MFMA32(__builtin_bit_cast(bf16x8, pd[0]), S[0], Y[1]);
          else if (m == 1) Y[3] = MFMA32(__builtin_bit_cast(bf16x8, pn[0]), S[0], Y[3]);
          else if (m == 2) Y[1] = MFMA32(__builtin_bit_cast(bf16x8, pd[1]), S[1], Y[1]);
          else if (m == 3) Y[3] = MFMA32(__builtin_bit_cast(bf16x8, pn[1]), S[1], Y[3]);
          else if (m == 4) G1 = MFMA32(St[0], XAf[0][1], zero16);
          else if (m == 5) G1 = MFMA32(St[1], XAf[1][1], G1);
          else if (m == 6) G1 = MFMA32(ae, wf(m, 1), G1);
          else if (m < 11) G1 = MFMA32(as[m - 7], wf(m, 1), G1);
          else if (m == 11) V1 = MFMA32(as[0], wf(m, 1), zero16);
          else V1 = MFMA32(as[m - 11], wf(m, 1), V1);
          SB();
          if (m == 0) { ss = ld128(lS4 + lo); sl = *(const LAS unsigned short*)(size_t)(lS1 + lo + 128); }
          if (m >= 3 && m < 12 && !((HELD >> (m + 3)) & 1)) b[(m - 3) % 3] = wl[widx(m + 3, 1) * 64];
          if (m == 4) load_S(ss);
          if (m == 5) { St[0] = rd128(sl * TPITCH + cSt); St[1] = rd128(sl * TPITCH + cSt + 16); }
          if (m == 6) { asm volatile("s_waitcnt vmcnt(5)" ::: "memory"); ae = rd128(foe + ro); }
          if (m >= 11) as[m - 11] = rd128(fo[m - 11] + ro);
          ew(m, G0, V0, G1, V1);
          SB();
        }
      }
      {
        u32x4 sx;
        unsigned re;
#pragma unroll
        for (int m = 0; m < 15; ++m) {
          if (m == 0) Y[0] = MFMA32(__builtin_bit_cast(bf16x8, pd[0]), S[0], Y[0]);
          else if (m == 1) Y[2] = MFMA32(__builtin_bit_cast(bf16x8, pn[0]), S[0], Y[2]);
          else if (m == 2) Y[0] = MFMA32(__builtin_bit_cast(bf16x8, pd[1]), S[1], Y[0]);
          else if (m == 3) Y[2] = MFMA32(__builtin_bit_cast(bf16x8, pn[1]), S[1], Y[2]);
          else if (m == 4) G0 = MFMA32(St[0], XAf[0][0], zero16);
          else if (m == 5) G0 = MFMA32(St[1], XAf[1][0], G0);
          else if (m == 6) G0 = MFMA32(ae, wf(m, 0), G0);
          else if (m < 11) G0 = MFMA32(as[m - 7], wf(m, 0), G0);
          else if (m == 11) V0 = MFMA32(as[0], wf(m, 0), zero16);
          else V0 = MFMA32(as[m - 11], wf(m, 0), V0);
          SB();
          if (m == 4) { sx = ld128(lX + lo + 3 * 128); re = *(const LAS unsigned*)(size_t)(lE + lo + 3 * 128); }
          if (m >= 3 && m < 12 && !((HELD >> (m + 3)) & 1)) b[(m - 3) % 3] = wl[widx(m + 3, 0) * 64];
          if (m == 7) glds16x(x, sx[0], cx0, ring0 + ro);
          if (m == 8) glds16x(x, sx[1], cx1, ring0 + ro + 1024);
          if (m == 9) glds16x(x, sx[2], cx0, ring0 + ro + 2048);
          if (m == 10) glds16x(x, sx[3], cx1, ring0 + ro + 3072);
          if (m == 11) glds16e(ES, re, ce, ring0 + ro + 4096);
          ew(m, G1, V1, G0, V0);
          SB();
        }
      }
    };
    for (int t = 0; t < ntiles; t += 2) {
      asm volatile("s_cmp_lt_u32 %0, 4\n\ts_cbranch_scc1 1f\n\ts_setprio 1\n1:" :: "s"(wave) : "scc");
      body(0);
      if (t + 1 >= ntiles) break;
      __builtin_amdgcn_s_setprio(0);
      body(1);
      lS4 += 256; lS1 += 256; lX += 256; lE += 256;
      asm volatile("" : "+v"(lS4), "+v"(lS1), "+v"(lX), "+v"(lE));
      adv += 256;
    }
    if (__builtin_expect(gs >= GEND, 1)) break;
    drain();
    lS4 -= adv; lS1 -= adv; lX -= adv; lE -= adv;
    redo = true;
  }

  {
    int lane;
    asm volatile("v_mbcnt_lo_u32_b32 %0, -1, 0\n\tv_mbcnt_hi_u32_b32 %0, -1, %0" : "=v"(lane));
    const int h = lane >> 5;
    const unsigned tu = lds0 + OFF_W + 18 * 1024 + h * 4096 + ((lane >> 2) & 3) * 128 + (lane & 1) * 64 +
                        ((lane >> 4) & 1) * 16 + ((lane >> 1) & 1) * 8;
    bf16x8 wu[8];
#pragma unroll
    for (int k = 0; k < 8; ++k) wu[k] = rawfrag(tu + (k & 3) * 1024 + (k >> 2) * 32);
    SB();
    drain();
    f32x16 X[2], U[2];
#pragma unroll
    for (int i = 0; i < 16; ++i) {
      const unsigned xv = __builtin_bit_cast(u32x4, as[i >> 3])[(i >> 1) & 3];
      X[0][i] = __uint_as_float(i & 1 ? xv & 0xffff0000u : xv << 16);
    }
    SB();
    asm volatile("s_nop 0\n\tv_mfma_f32_32x32x16_bf16 %0, %1, %2, %3" : "=&v"(U[0]) : "v"(wu[0]), "v"(as[0]), "v"(X[0]));
#pragma unroll
    for (int k = 1; k < 4; ++k) U[0] = MFMA32(wu[k], as[k], U[0]);
#pragma unroll
    for (int i = 0; i < 16; ++i) {
      const unsigned xv = __builtin_bit_cast(u32x4, as[2 + (i >> 3)])[(i >> 1) & 3];
      X[1][i] = __uint_as_float(i & 1 ? xv & 0xffff0000u : xv << 16);
    }
    const unsigned nd = nodeof(cg + (lane & 31));
    const bool live = (nd * Mw + Cw) >> 16 == cg + (lane & 31);
    const unsigned lm = (unsigned)__ballot(live);
    const int nodes = __builtin_popcount(lm);
    const unsigned n0 = __builtin_amdgcn_readlane(nd, __builtin_ctz(lm | 0x80000000u));
    const int r = live ? nd - n0 : 31;
    SB();
    asm volatile("s_nop 0\n\tv_mfma_f32_32x32x16_bf16 %0, %1, %2, %3" : "=&v"(U[1]) : "v"(wu[4]), "v"(as[0]), "v"(X[1]));
#pragma unroll
    for (int k = 1; k < 4; ++k) U[1] = MFMA32(wu[4 + k], as[k], U[1]);
#pragma unroll
    for (int i = 0; i < 16; ++i) Y[0][i] = __builtin_amdgcn_rcpf(Y[0][i]);
    SB();
#pragma unroll
    for (int ct = 0; ct < 2; ++ct) {
#pragma unroll
      for (int q = 0; q < 2; ++q) {
        u32x4 ov;
#pragma unroll
        for (int j = 0; j < 4; ++j) {
          const int i = 8 * q + 2 * j;
          if (ct == 0) { Y[1][i] = __builtin_amdgcn_rcpf(Y[1][i]); Y[1][i + 1] = __builtin_amdgcn_rcpf(Y[1][i + 1]); }
          const f32x2 z = __builtin_elementwise_fma(f32x2{Y[2 + ct][i], Y[2 + ct][i + 1]}, f32x2{Y[ct][i], Y[ct][i + 1]},
                                                    f32x2{U[ct][i], U[ct][i + 1]});
          ov[j] = pack_bf16(maxf(z[0], X[ct][i]), maxf(z[1], X[ct][i + 1]));
        }
        *(LAS u32x4*)(size_t)(la + r * 128 + (((4 * h + 2 * ct + q) ^ (r & 7)) * 16)) = ov;
      }
    }
    u32x4 ov[4];
#pragma unroll
    for (int q = 0; q < 4; ++q)
      ov[q] = *(const LAS u32x4*)(size_t)(la + (8 * q + (lane >> 3)) * 128 + (((lane & 7) ^ (lane >> 3)) * 16));
    asm volatile("v_cmpx_lt_i32 vcc, 0, %6\n\t"
                 "global_store_dwordx4 %0, %1, %5 sc1\n\t"
                 "v_cmpx_lt_i32 vcc, 8, %6\n\t"
                 "global_store_dwordx4 %0, %2, %5 offset:1024 sc1\n\t"
                 "v_cmpx_lt_i32 vcc, 16, %6\n\t"
                 "global_store_dwordx4 %0, %3, %5 offset:2048 sc1\n\t"
                 "v_cmpx_lt_i32 vcc, 24, %6\n\t"
                 "global_store_dwordx4 %0, %4, %5 offset:3072 sc1\n\t"
                 "s_mov_b64 exec, -1"
                 :: "v"((unsigned)n0 * 128u + lane * 16u), "v"(ov[0]), "v"(ov[1]), "v"(ov[2]), "v"(ov[3]), "s"(out),
                    "v"(nodes - (lane >> 3))
                 : "memory", "vcc");
  }
  if (bk == 0 && wave == 7) asm volatile("global_store_dwordx2 %0, %1, %2 sc1" :: "v"(0u), "v"(ep), "s"(FL) : "memory");
  asm volatile("s_waitcnt vmcnt(0)" ::: "memory");
}

extern "C" void kernel_launch(void* const* d_in, const int* in_sizes, int n_in,
                              void* d_out, int out_size, void* d_ws, size_t ws_size,
                              hipStream_t stream) {
  const unsigned short* x = (const unsigned short*)d_in[0];
  const u32x4* eattr = (const u32x4*)d_in[1];
  const int* src = (const int*)d_in[2];
  const int* dst = (const int*)d_in[3];
  const unsigned short* wa = (const unsigned short*)d_in[4];
  const unsigned short* wb = (const unsigned short*)d_in[5];
  const unsigned short* wc = (const unsigned short*)d_in[6];
  const unsigned short* wu = (const unsigned short*)d_in[7];
  const unsigned short* wv = (const unsigned short*)d_in[8];
  unsigned short* out = (unsigned short*)d_out;
  const int N = in_sizes[0] / 64;
  const int E = in_sizes[2];
  const int chunk = (((E + NCH - 1) / NCH) + 3) & ~3;

  const bool skew = N > 4096 && N < 60000;
  const unsigned M = N > 256 ? (unsigned)(4294967295.0 / ((double)N * (skew ? 1.0 + SKEWF : 1.0))) : 0xffffffu;
  const unsigned N1 = (0x7fffffffu / M) + 1u;
  const unsigned M2 = skew ? (0xffffffffu - N1 * M) / ((unsigned)N - N1) : M;

  auto fb = [](double v) { return __builtin_bit_cast(unsigned, (float)v); };
  char* ws = (char*)d_ws;
  size_t off = 0;
  off = 1 << 20;
  unsigned long long* FL = (unsigned long long*)(ws + off);   off += 1 << 20;
  u32x4* ENT = (u32x4*)(ws + off);                       off += (size_t)2 * NBK * IBOX;
  unsigned long long* TS = (unsigned long long*)(ws + off);

  k_main<<<NBK, 512, 0, stream>>>(x, eattr, src, dst, wa, wb, wc, wu, wv, ENT, TS, FL, out, N, E, chunk, M, N1,
                                  M2 - M, fb(65536.0 / M), fb(65536.0 / M2), fb(32768.0 / M + 0.5),
                                  fb((double)N1 + (32768.0 - (double)N1 * M) / M2 + 0.5)
     );
}
```

```cpp
#define QSEL 99
#define DBGDUMP 1
#define HELD 0x5500
#define KSTAMP
#include <hip/hip_runtime.h>
#include <hip/hip_bf16.h>
#include <stdint.h>

using bf16x8 = __attribute__((ext_vector_type(8))) short;
using s16x4 = __attribute__((ext_vector_type(4))) short;
using f32x16 = __attribute__((ext_vector_type(16))) float;
using f32x2 = __attribute__((ext_vector_type(2))) float;
using u32x4 = __attribute__((ext_vector_type(4))) unsigned;
using u32x2 = __attribute__((ext_vector_type(2))) unsigned;
using i32x4 = __attribute__((ext_vector_type(4))) int;
using u16x2 = __attribute__((ext_vector_type(2))) unsigned short;

#define MFMA32(a, b, c) __builtin_amdgcn_mfma_f32_32x32x16_bf16((a), (b), (c), 0, 0, 0)
#define LAS __attribute__((address_space(3)))
#define RANKOF(w) ((w) < 4 ? 7 - (w) : (w) - 4)
#define SLEEP "s_sleep 2\n\t"
#define NREP 8
#define REPOF(b) ((b) % NREP)
#define POLLAT 2
#define PRE 0
#define PREPOL "sc1"
#define G1W 4
#define DBGWAIT 0
#define XWARM 0
#define XAT 3
#define CPF 32
#define CPFB 18688
#define WEARLY 1
#define FR 0
#define XAP 0
#define TPOLL 0
#define SKEWF 0.015
#define PFAT 1
#define TW 0
#define TWN 256
#define SRCLATE 1
#define XGAT 0
#define KV (tmK-tm0)
#define SB() __builtin_amdgcn_sched_barrier(0)

constexpr int NCH = 256;
constexpr int NBK = 256;
constexpr int NBIN = NBK * 8;
constexpr int NFRAG = 39;
constexpr int CHMAX = 3200;
constexpr float NLOG2E = -1.44269504088896340736f;
constexpr unsigned TPITCH = 80;

__device__ __forceinline__ unsigned pack_bf16(float lo, float hi) {
  typedef __attribute__((ext_vector_type(2))) float f2;
  typedef __attribute__((ext_vector_type(2))) __bf16 b2;
  f2 v = {lo, hi};
  b2 r = __builtin_convertvector(v, b2);
  return __builtin_bit_cast(unsigned, r);
}

__device__ __forceinline__ int wave_scan(int v) {
  v += __builtin_amdgcn_update_dpp(0, v, 0x111, 0xf, 0xf, false);
  v += __builtin_amdgcn_update_dpp(0, v, 0x112, 0xf, 0xf, false);
  v += __builtin_amdgcn_update_dpp(0, v, 0x114, 0xf, 0xf, false);
  v += __builtin_amdgcn_update_dpp(0, v, 0x118, 0xf, 0xf, false);
  v += __builtin_amdgcn_update_dpp(0, v, 0x142, 0xa, 0xf, false);
  v += __builtin_amdgcn_update_dpp(0, v, 0x143, 0xc, 0xf, false);
  return v;
}

__device__ __forceinline__ void glds16(const void* sbase, unsigned voff, unsigned lds_dst) {
  asm volatile(
      "s_mov_b32 m0, %2\n\t"
      "s_nop 0\n\t"
      "global_load_lds_dwordx4 %0, %1"
      :: "v"(voff), "s"(sbase), "s"(lds_dst) : "memory", "m0");
}
__device__ __forceinline__ void glds16nt(const void* sbase, unsigned voff, unsigned lds_dst) {
  asm volatile(
      "s_mov_b32 m0, %2\n\t"
      "s_nop 0\n\t"
      "global_load_lds_dwordx4 %0, %1 nt"
      :: "v"(voff), "s"(sbase), "s"(lds_dst) : "memory", "m0");
}
__device__ __forceinline__ void glds16x(const void* sbase, unsigned a, unsigned c, unsigned lds_dst) {
  unsigned tmp;
  asm volatile(
      "s_mov_b32 m0, %4\n\t"
      "v_mad_u32_u16 %0, %1, %5, %2\n\t"
      "global_load_lds_dwordx4 %0, %3"
      : "=&v"(tmp) : "v"(a), "v"(c), "s"(sbase), "s"(lds_dst), "s"(128u) : "memory", "m0");
}
__device__ __forceinline__ void glds16e(const void* sbase, unsigned a, unsigned c, unsigned lds_dst) {
  unsigned tmp;
  asm volatile(
      "s_mov_b32 m0, %4\n\t"
      "v_add_u32 %0, %1, %2\n\t"
      "global_load_lds_dwordx4 %0, %3"
      : "=&v"(tmp) : "v"(a), "v"(c), "s"(sbase), "s"(lds_dst) : "memory", "m0");
}
__device__ __forceinline__ int gload32(const void* sbase, unsigned voff) {
  int r;
  asm volatile("global_load_dword %0, %1, %2" : "=v"(r) : "v"(voff), "s"(sbase) : "memory");
  return r;
}
__device__ __forceinline__ u32x4 gload128(const void* sbase, unsigned voff) {
  u32x4 r;
  asm volatile("global_load_dwordx4 %0, %1, %2" : "=v"(r) : "v"(voff), "s"(sbase) : "memory");
  return r;
}

__device__ __forceinline__ u32x4 gload128nt(const void* sbase, unsigned voff) {
  u32x4 r;
  asm volatile("global_load_dwordx4 %0, %1, %2 nt" : "=v"(r) : "v"(voff), "s"(sbase) : "memory");
  return r;
}
__device__ __forceinline__ void gstore128wt(void* sbase, unsigned voff, u32x4 v) {
  asm volatile("global_store_dwordx4 %0, %1, %2 sc1\n\ts_nop 1" :: "v"(voff), "v"(v), "s"(sbase) : "memory");
}

constexpr int IBOX = 32768;
constexpr int IEXT = 256 * IBOX;
constexpr int CPIT = 208;
constexpr unsigned RINC = 87382;
constexpr int KP_HDR = 0;
constexpr int KP_ENT = KP_HDR + NBK * 4;
constexpr int KP_POS = KP_ENT + NBK * CPIT;
constexpr int KP_LDS = KP_POS + NBK * 4;
constexpr int NWL = 26;
constexpr int NBLOB = 31;

__device__ __forceinline__ unsigned mad24(unsigned a, int b, unsigned c) {
  unsigned r;
  asm("v_mad_i32_i24 %0, %1, %2, %3" : "=v"(r) : "v"(a), "s"(b), "v"(c));
  return r;
}
__device__ __forceinline__ unsigned mul24(unsigned a, unsigned b) {
  unsigned r;
  asm("v_mul_u32_u24 %0, %1, %2" : "=v"(r) : "n"(b), "v"(a));
  return r;
}
__device__ __forceinline__ unsigned mul24s(unsigned a, unsigned b) {
  unsigned r;
  asm("v_mul_u32_u24 %0, %1, %2" : "=v"(r) : "s"(b), "v"(a));
  return r;
}
__device__ __forceinline__ unsigned byte3x(unsigned a, unsigned b) {
  unsigned r;
  asm("v_mul_u32_u24_sdwa %0, %1, %2 dst_sel:DWORD dst_unused:UNUSED_PAD src0_sel:DWORD src1_sel:BYTE_3" : "=v"(r) : "s"(b), "v"(a));
  return r;
}
__device__ __forceinline__ unsigned mulhi24(unsigned a, unsigned b) {
  return (unsigned)(((unsigned long long)(a & 0xffffffu) * (unsigned long long)b) >> 32);
}
__device__ __forceinline__ unsigned dot4(unsigned a, unsigned b, unsigned c) {
  return __builtin_amdgcn_udot4(a, b, c, false);
}

constexpr int RING_SLOT = 5 * 1024;
constexpr int LCAP = 512;
constexpr int LARR = 2560;
constexpr int LIST_BYTES = 2 * LARR;
constexpr int OFF_CNT = 0;
constexpr int OFF_XF = 256;
constexpr int OFF_W = 512;
constexpr int OFF_T1 = OFF_W + NWL * 1024;
constexpr int OFF_T2 = OFF_T1 + 32 * TPITCH;
constexpr int OFF_R0 = OFF_W + NFRAG * 1024;
constexpr int OFF_R1 = OFF_R0 + 8 * RING_SLOT;
constexpr int OFF_L = OFF_R1 + 8 * RING_SLOT;
constexpr int MAIN_LDS = OFF_L + 8 * LIST_BYTES;
static_assert(MAIN_LDS <= 160 * 1024, "LDS");
static_assert(KP_LDS <= 16 * RING_SLOT, "phase 1 lives in the ring");

__device__ __forceinline__ unsigned ldd(const unsigned* p) {
  return __hip_atomic_load(p, __ATOMIC_RELAXED, __HIP_MEMORY_SCOPE_AGENT);
}
__device__ __forceinline__ unsigned long long ldq(const unsigned long long* p) {
  return __hip_atomic_load(p, __ATOMIC_RELAXED, __HIP_MEMORY_SCOPE_AGENT);
}
__device__ __forceinline__ bf16x8 rawfrag(unsigned a) {
  const s16x4 a0 = __builtin_amdgcn_ds_read_tr16_b64_v4i16((LAS s16x4*)(size_t)a);
  const s16x4 a1 = __builtin_amdgcn_ds_read_tr16_b64_v4i16((LAS s16x4*)(size_t)(a + 512));
  return __builtin_shufflevector(a0, a1, 0, 1, 2, 3, 4, 5, 6, 7);
}
__device__ __forceinline__ float maxf(float a, float b) {
  float r;
  asm("v_max_f32 %0, %1, %2" : "=v"(r) : "v"(a), "v"(b));
  return r;
}
__device__ __forceinline__ float add1(float t) { return 1.0f + t; }
__device__ __forceinline__ float mulf(int i, float a, float b) { return (i & 1) ? __builtin_fmaf(a, b, 0.0f) : a * b; }

__global__ __launch_bounds__(512, 2) void k_main(const unsigned short* __restrict__ x,
                                                 const u32x4* __restrict__ ES,
                                                 const int* __restrict__ src, const int* __restrict__ dst,
                                                 const unsigned short* __restrict__ wa_g,
                                                 const unsigned short* __restrict__ wb_g,
                                                 const unsigned short* __restrict__ wc_g,
                                                 const unsigned short* __restrict__ wu_g,
                                                 const unsigned short* __restrict__ wv_g,
                                                 u32x4* ENT, unsigned long long* TS, unsigned long long* FL,
                                                 unsigned short* __restrict__ out, int N, int E, int chunk,
                                                 unsigned M, unsigned N1, unsigned DM, unsigned MF0, unsigned MF1,
                                                 unsigned MB0, unsigned MB1
                                                 , long long* dbg
                                                 ) {
  const long long tm0 = __builtin_amdgcn_s_memrealtime(); __builtin_amdgcn_sched_barrier(0);
#define PT(i)
  PT(0);
  __shared__ __attribute__((aligned(16))) char smem[MAIN_LDS];
  const int tid0 = threadIdx.x;
  const int wave = __builtin_amdgcn_readfirstlane(tid0 >> 6);
  const int bk = blockIdx.x;
  const unsigned lds0 = (unsigned)(size_t)(LAS char*)smem;

  LAS char* sm = (LAS char*)smem + OFF_R0;
  const unsigned sm0 = lds0 + OFF_R0;
  const int tid = tid0, lane = tid & 63, r = lane & 31, h = lane >> 5;
  const unsigned pc = tid & 7, bq = tid >> 3;
  unsigned ia = sm0 + KP_ENT + bq * CPIT + pc * 16;
#pragma unroll
  for (int s = 0; s < 4; ++s) *(LAS u32x4*)(size_t)(ia + s * 64 * CPIT) = u32x4{~0u, ~0u, ~0u, ~0u};
  *(LAS unsigned*)(sm + KP_HDR + (tid & 255) * 4) = 0u;
  int q0 = 4 * tid + 3, q1 = 4 * tid + 1795;
  unsigned a0 = tid * 16, a1 = tid * 16 + 7168, z0 = 0u;
  unsigned long long m7 = __ballot(tid == 448);
  int n7 = wave == 7 ? 0 : 0x7fffffff;
  unsigned f0 = lane * 128u, f1 = lane * 128u + 8192u, f2 = min(lane * 128u + 16384u, CPFB - 512u);
  unsigned long long p7 = __ballot((wave | (bk & -CPF)) == 7);
  asm volatile("" : "+v"(f0), "+v"(f1), "+v"(f2), "+s"(p7));
  asm volatile("" : "+v"(ia), "+v"(q0), "+v"(q1), "+v"(a0), "+v"(a1), "+v"(z0), "+s"(m7), "+s"(n7) :: "memory");
  SB();
  asm volatile("" :: "s"(x), "s"(ES), "s"(src), "s"(dst), "s"(wa_g), "s"(wb_g), "s"(wc_g), "s"(wu_g), "s"(wv_g), "s"(ENT),
               "s"(TS), "s"(FL), "s"(out), "s"(N), "s"(E), "s"(chunk), "s"(M), "s"(N1), "s"(DM), "s"(MF0), "s"(MF1), "s"(MB0), "s"(MB1));
  const long long tmK = __builtin_amdgcn_s_memrealtime(); SB();
  const int base = bk * chunk;
  const int ecnt = max(min(chunk, E - base), 0);
  unsigned long long on[2];
  on[0] = __ballot(q0 < min(min(chunk, E - base), n7));
  on[1] = __ballot(q1 < min(min(chunk, E - base), n7));
  a0 += base * 4u;
  a1 += base * 4u;
  u32x4 d[2], sv[2];
  u32x2 e0;
  asm volatile("s_mov_b64 exec, %11\n\t"
               "global_load_dwordx4 %0, %5, %8 nt\n\t"
               "s_mov_b64 exec, %12\n\t"
               "global_load_dwordx4 %1, %6, %8 nt\n\t"
               "s_mov_b64 exec, %13\n\t"
               "global_load_dwordx2 %2, %7, %9 sc1\n\t"
               "s_getpc_b64 s[96:97]\n\t"
               "s_mov_b32 m0, %17\n\t"
               "s_mov_b64 exec, %18\n\t"
               "global_load_lds_dword %14, s[96:97]\n\t"
               "global_load_lds_dword %15, s[96:97]\n\t"
               "global_load_lds_dword %16, s[96:97]\n\t"
               "s_mov_b64 exec, %11\n\t"
               "s_waitcnt lgkmcnt(0)\n\t"
               "s_barrier\n\t"
               "global_load_dwordx4 %3, %5, %10 nt\n\t"
               "s_mov_b64 exec, %12\n\t"
               "global_load_dwordx4 %4, %6, %10 nt\n\t"
               "s_mov_b64 exec, -1"
               : "=&{v[240:243]}"(d[0]), "=&{v[244:247]}"(d[1]), "=&{v[238:239]}"(e0), "=&{v[248:251]}"(sv[0]),
                 "=&{v[252:255]}"(sv[1])
               : "v"(a0), "v"(a1), "v"(z0), "s"(dst), "s"(FL), "s"(src), "s"(on[0]), "s"(on[1]), "s"(m7)
                 , "v"(f0), "v"(f1), "v"(f2), "n"(OFF_L + 7 * LIST_BYTES + 4096), "s"(p7)
               : "memory", "m0", "s96", "s97");
  SB();
  const int bu = (bk >> 1) | ((bk & 1) << 7);
  const unsigned Mw = bk & 1 ? M + DM : M, Cw = bk & 1 ? 0u - N1 * DM : 0u;
  unsigned MF = bk & 1 ? MF1 : MF0, MB = bk & 1 ? MB1 : MB0;
  asm volatile("" : "+s"(MF), "+s"(MB));
  char* ib = reinterpret_cast<char*>(ENT) + (size_t)bu * IBOX;
  auto nodeof = [&](unsigned c) { return min((unsigned)__builtin_fmaf((float)c, __uint_as_float(MF), __uint_as_float(MB)), (unsigned)N - 1u); };
  unsigned tup[8], val[8], pm[8];
  u32x4 xg[4];
  const unsigned xn0 = __builtin_amdgcn_readfirstlane(nodeof((bu * 8 + wave) * 32));
  unsigned xd[4];
  auto xprep = [&]() {
#pragma unroll
    for (int i = 0; i < 4; ++i)
      xd[i] = min(xn0 + 8 * i + (lane >> 3), N - 1u) * 128u + ((((lane & 7) ^ (lane >> 4)) * 16) ^ (i & 1) * 64) - i * 1024u;
    asm volatile("" : "+v"(xd[0]), "+v"(xd[1]), "+v"(xd[2]), "+v"(xd[3]));
  };
  auto xdma = [&]() {
    asm volatile("s_mov_b32 m0, %5\n\ts_nop 0\n\t"
                 "global_load_lds_dwordx4 %0, %4\n\t"
                 "global_load_lds_dwordx4 %1, %4 offset:1024\n\t"
                 "global_load_lds_dwordx4 %2, %4 offset:2048\n\t"
                 "global_load_lds_dwordx4 %3, %4 offset:3072"
                 :: "v"(xd[0]), "v"(xd[1]), "v"(xd[2]), "v"(xd[3]), "s"(x), "s"(lds0 + OFF_L + wave * LIST_BYTES)
                 : "memory", "m0");
  };
  auto wload = [&](const unsigned short* w, int f) {
    asm volatile("s_mov_b32 m0, %3\n\ts_nop 0\n\t"
                 "global_load_lds_dwordx4 %0, %2\n\t"
                 "global_load_lds_dwordx4 %0, %2 offset:1024\n\t"
                 "global_load_lds_dwordx4 %0, %2 offset:2048\n\t"
                 "global_load_lds_dwordx4 %0, %2 offset:3072\n\t"
                 "s_addk_i32 m0, 4096\n\ts_nop 0\n\t"
                 "global_load_lds_dwordx4 %1, %2\n\t"
                 "global_load_lds_dwordx4 %1, %2 offset:1024\n\t"
                 "global_load_lds_dwordx4 %1, %2 offset:2048\n\t"
                 "global_load_lds_dwordx4 %1, %2 offset:3072"
                 :: "v"(lane * 16u), "v"(lane * 16u + 4096u), "s"(w), "s"(lds0 + OFF_W + f * 1024) : "memory", "m0", "scc");
  };
  auto wall = [&]() {
    wload(wa_g, NBLOB);
    wload(wb_g, 0);
    wload(wv_g, 8);
    glds16(wc_g, lane * 16, lds0 + OFF_W + 16 * 1024);
    glds16(wc_g + 512, lane * 16, lds0 + OFF_W + 17 * 1024);
  };
  int spin = 1 << 20;
  u32x2 ep = {0u, 0u};
  if (wave == 7) {
    wall();
    auto touch = [&](const void* p, unsigned stride) {
      asm volatile("s_mov_b32 m0, %2\n\ts_mov_b64 exec, %3\n\tglobal_load_lds_dword %0, %1 sc1\n\ts_mov_b64 exec, -1"
                   :: "v"(lane * stride), "s"(p), "n"(OFF_L + 7 * LIST_BYTES + 4096), "s"(__ballot(bk < TWN)) : "memory", "m0");
    };
    if (TW & 4) touch(ENT, IEXT / 64);
    if (TW & 2) touch(x, (unsigned)(N / 64) * 128u);
    if (TW & 1) touch(ES, (unsigned)(E / 64) * 32u);
    if (TW & 8) touch(out, (unsigned)(N / 64) * 128u);
    *(LAS u32x4*)((LAS char*)smem + OFF_CNT + r * 16) =
        r < 8 ? u32x4{lds0 + OFF_L, lds0 + OFF_L + 4, lds0 + OFF_L + 8, lds0 + OFF_L + 12} : u32x4{0u, 0u, 0u, 0u};
    {
      const unsigned col = h ? ((r >> 1) & 12u) | ((r << 2) & 16u) | (r & 3u) : r;
      LAS char* tr = (LAS char*)smem + OFF_T1 + lane * TPITCH;
#pragma unroll
      for (int j = 0; j < 4; ++j) *(LAS u32x4*)(tr + j * 16) = u32x4{0u, 0u, 0u, 0u};
      *(LAS unsigned short*)(tr + col * 2) = 0x3F80;
    }
#pragma unroll
    for (int i = 0; i < 8; ++i) { tup[i] = 0u; val[i] = 0u; pm[i] = 0u; }
    asm volatile("s_waitcnt vmcnt(%1)" : "+{v[238:239]}"(e0) : "n"((WEARLY ? 26 : 0) + (PFAT ? 5 : 0) + __builtin_popcount(TW)) : "memory");
    ep[0] = __builtin_amdgcn_readfirstlane(e0[0]) + 0x9E3779B1u;
    ep[1] = __builtin_amdgcn_readfirstlane(e0[1]) + 0x7F4A7C15u;
    *(LAS u32x2*)((LAS char*)smem + OFF_CNT + 168) = u32x2{ep[0], ep[1] >> 24};
    if (__builtin_expect(ecnt & 3, 0)) {
      const int i = (ecnt & ~3) + lane;
      on[0] = __ballot(i < ecnt);
      if (i < ecnt) {
        pm[0] = (unsigned)dst[base + i] * M + (unsigned)max(dst[base + i] - (int)N1, 0) * DM;
        tup[0] = (unsigned)src[base + i] | (unsigned)i << 16;
        val[0] = __hip_atomic_fetch_add((LAS unsigned*)(sm + KP_HDR) + (pm[0] >> 24), RINC, __ATOMIC_RELAXED, __HIP_MEMORY_SCOPE_WORKGROUP);
        const unsigned u = min(val[0] >> 16, 48u);
        *(LAS unsigned*)(sm + KP_ENT + (pm[0] >> 24) * CPIT + u * 4) = tup[0];
        *(LAS unsigned char*)(sm + KP_ENT + (pm[0] >> 24) * CPIT + ((u * 4) | (u & 3) | 12)) = pm[0] >> 16;
      }
      __builtin_amdgcn_s_waitcnt(0x0F70);
    }
    PT(1);
    xprep();
    xdma();
  } else {
    xprep();
    unsigned inc = RINC, t0 = (unsigned)tid << 18;
    asm volatile("s_waitcnt vmcnt(2)" : "+{v[240:243]}"(d[0]), "+{v[244:247]}"(d[1]), "+v"(inc), "+v"(t0) :: "memory");
    PT(1);
    unsigned cb[8];
#pragma unroll
    for (int s = 0; s < 2; ++s) {
      unsigned* rr = val + 4 * s;
#pragma unroll
      for (int k = 0; k < 4; ++k) {
        unsigned t;
        asm("v_sub_u32 %0, %1, %2 clamp" : "=v"(t) : "v"(d[s][k]), "s"(N1));
        asm("v_mad_u32_u24 %0, %1, %2, %3" : "=v"(pm[4 * s + k]) : "v"(t), "s"(DM), "v"(mul24s(d[s][k], M)));
      }
#pragma unroll
      for (int k = 0; k < 4; ++k) rr[k] = byte3x(pm[4 * s + k], 4u);
      asm volatile(
          "s_mov_b64 exec, %5\n\t"
          "ds_add_rtn_u32 %0, %0, %4 offset:%6\n\t"
          "ds_add_rtn_u32 %1, %1, %4 offset:%6\n\t"
          "ds_add_rtn_u32 %2, %2, %4 offset:%6\n\t"
          "ds_add_rtn_u32 %3, %3, %4 offset:%6\n\t"
          "s_mov_b64 exec, -1"
          : "+v"(rr[0]), "+v"(rr[1]), "+v"(rr[2]), "+v"(rr[3])
          : "v"(inc), "s"(on[s]), "n"(OFF_R0 + KP_HDR)
          : "memory");
    }
#pragma unroll
    for (int i = 0; i < 8; ++i) cb[i] = byte3x(pm[i], (unsigned)CPIT);
    asm volatile("s_waitcnt vmcnt(0)"
                 : "+{v[248:251]}"(sv[0]), "+{v[252:255]}"(sv[1]), "+v"(cb[0]), "+v"(cb[1]), "+v"(cb[2]), "+v"(cb[3]), "+v"(cb[4]), "+v"(cb[5]),
                   "+v"(cb[6]), "+v"(cb[7])
                 :: "memory");
    xdma();
#pragma unroll
    for (int i = 0; i < 8; ++i) tup[i] = sv[i >> 2][i & 3] + t0 + (((i >> 2) * 1792u + (i & 3)) << 16);
#pragma unroll
    for (int s = 0; s < 2; ++s) {
      asm volatile("s_waitcnt lgkmcnt(%8)"
                   : "+v"(val[4 * s]), "+v"(val[4 * s + 1]), "+v"(val[4 * s + 2]), "+v"(val[4 * s + 3]), "+v"(tup[4 * s]),
                     "+v"(tup[4 * s + 1]), "+v"(tup[4 * s + 2]), "+v"(tup[4 * s + 3])
                   : "n"(DBGWAIT ? 0 : s ? 8 : 4) : "memory");
      unsigned pa[4], pb[4];
#pragma unroll
      for (int k = 0; k < 4; ++k) {
        const unsigned u = min(val[4 * s + k], 48u << 16);
        asm("v_mad_u32_u16 %0, %1, 4, %2 op_sel:[1,0,0,0]" : "=v"(pa[k]) : "v"(u), "v"(cb[4 * s + k]));
        pb[k] = pa[k] | ((u >> 16) & 3u) | 12u;
      }
      asm volatile(
          "s_mov_b64 exec, %16\n\t"
          "ds_write_b32 %0, %8 offset:%17\n\t"
          "ds_write_b8_d16_hi %4, %12 offset:%17\n\t"
          "ds_write_b32 %1, %9 offset:%17\n\t"
          "ds_write_b8_d16_hi %5, %13 offset:%17\n\t"
          "ds_write_b32 %2, %10 offset:%17\n\t"
          "ds_write_b8_d16_hi %6, %14 offset:%17\n\t"
          "ds_write_b32 %3, %11 offset:%17\n\t"
          "ds_write_b8_d16_hi %7, %15 offset:%17\n\t"
          "s_mov_b64 exec, -1"
          :: "v"(pa[0]), "v"(pa[1]), "v"(pa[2]), "v"(pa[3]), "v"(pb[0]), "v"(pb[1]), "v"(pb[2]), "v"(pb[3]),
             "v"(tup[4 * s]), "v"(tup[4 * s + 1]), "v"(tup[4 * s + 2]), "v"(tup[4 * s + 3]), "v"(pm[4 * s]),
             "v"(pm[4 * s + 1]), "v"(pm[4 * s + 2]), "v"(pm[4 * s + 3]), "s"(on[s]), "n"(OFF_R0 + KP_ENT)
          : "memory");
    }
  }
  PT(2);
  u32x2 tag;
  {
    unsigned ha = sm0 + KP_HDR + bq * 4, hb = sm0 + KP_HDR + lane * 16;
    unsigned go = bq * IBOX + bk * 128 + pc * 16;
    asm volatile("s_waitcnt lgkmcnt(0)\n\ts_barrier" : "+v"(ha), "+v"(hb), "+v"(go) :: "memory");
    PT(3);
    tag = *(const LAS u32x2*)((LAS char*)smem + OFF_CNT + 168);
    const u32x4 call = *(const LAS u32x4*)(size_t)hb;
    unsigned cn[4];
    u32x4 v[4];
#pragma unroll
    for (int s = 0; s < 4; ++s) {
      cn[s] = *(const LAS unsigned*)(size_t)(ha + s * 256);
      v[s] = *(const LAS u32x4*)(size_t)(ia + s * 64 * CPIT);
    }
    asm volatile("" : "+v"(v[0]), "+v"(v[1]), "+v"(v[2]), "+v"(v[3]), "+v"(tag));
    if (__builtin_expect(__ballot(max(max(call[0], call[1]), max(call[2], call[3])) > 36u * RINC) != 0ull, 0)) {
      LAS unsigned* ovc = (LAS unsigned*)((LAS char*)smem + OFF_CNT + 164);
      LAS unsigned* pos = (LAS unsigned*)(sm + KP_POS);
      if (tid < NBK) {
        const unsigned u = *(const LAS unsigned*)(sm + KP_HDR + tid * 4) >> 16, n = u - (u >> 2);
        if (n > 36u) {
          const unsigned p = (unsigned)base + __hip_atomic_fetch_add(ovc, n - 36u, __ATOMIC_RELAXED, __HIP_MEMORY_SCOPE_WORKGROUP);
          pos[tid] = p;
          __hip_atomic_store(FL + 512 + tid * NCH + bk, ((unsigned long long)(n - 36u) << 32) | p, __ATOMIC_RELAXED,
                             __HIP_MEMORY_SCOPE_AGENT);
        }
      }
      asm volatile("s_waitcnt lgkmcnt(0)\n\ts_barrier" ::: "memory");
#pragma unroll
      for (int i = 0; i < 8; ++i) {
        const unsigned u = val[i] >> 16, n = u - (u >> 2);
        if (((on[i >> 2] >> lane) & 1ull) && n >= 36u)
          __hip_atomic_store(TS + pos[pm[i] >> 24] + (n - 36u), ((unsigned long long)((pm[i] >> 16) & 0xffu) << 32) | tup[i],
                             __ATOMIC_RELAXED, __HIP_MEMORY_SCOPE_AGENT);
      }
      __builtin_amdgcn_s_waitcnt(0x0F70);
      asm volatile("s_waitcnt vmcnt(0)\n\ts_barrier" ::: "memory");
    }
    auto seal = [&](u32x4& w, bool more) {
      w[3] = __builtin_amdgcn_perm(tag[1], w[3], 0x04020100u);
      unsigned sm_ = w[0] + w[1] + w[2] + w[3] + tag[0];
      sm_ += __builtin_amdgcn_update_dpp(0u, sm_, 0xB1, 0xf, 0xf, false);
      sm_ += __builtin_amdgcn_update_dpp(0u, sm_, 0x4E, 0xf, 0xf, false);
      sm_ += __builtin_amdgcn_update_dpp(0u, sm_, 0x141, 0xf, 0xf, false);
      w[2] = pc == 7u ? w[2] - sm_ + (more ? 1u : 0u) : w[2];
    };
#pragma unroll
    for (int s = 0; s < 4; ++s) seal(v[s], cn[s] > 23u * RINC);
    asm volatile("global_store_dwordx4 %0, %1, %5 sc1\n\t"
                 "global_store_dwordx4 %0, %2, %6 sc1\n\t"
                 "global_store_dwordx4 %0, %3, %7 sc1\n\t"
                 "global_store_dwordx4 %0, %4, %8 sc1\n\t"
                 "s_nop 1"
                 :: "v"(go), "v"(v[0]), "v"(v[1]), "v"(v[2]), "v"(v[3]), "s"(ENT), "s"(ENT + 4 * IBOX), "s"(ENT + 8 * IBOX),
                    "s"(ENT + 12 * IBOX)
                 : "memory");
    PT(4);
    if (__builtin_expect(__ballot(max(max(cn[0], cn[1]), max(cn[2], cn[3])) > 23u * RINC) != 0ull, 0)) {
#pragma unroll 1
      for (int s = 0; s < 4; ++s) {
        const unsigned c = *(const LAS unsigned*)(size_t)(ha + s * 256);
        u32x4 w = *(const LAS u32x4*)(size_t)(ia + s * 64 * CPIT + (pc < 4u ? 128 : 0));
        if (pc == 7u) w = u32x4{w[2], ~0u, ~0u, w[3] >> 16};
        else {
#pragma unroll
          for (int j = 0; j < 3; ++j) w[j] = pc < 4u && 32u + 4u * pc + j < (c >> 16) ? w[j] : ~0u;
        }
        seal(w, c > 36u * RINC);
        if (c > 23u * RINC) gstore128wt(ENT, go + s * (64 * IBOX) + IEXT, w);
      }
    }
    PT(5);
  }
  u32x4 cv[4];
  unsigned vo[4], eb[4];
  unsigned long long pend[4];
#pragma unroll
  for (int k = 0; k < 4; ++k) {
    vo[k] = wave * 4096u + k * 1024u + lane * 16u;
    eb[k] = (unsigned)((wave * 32 + k * 8 + (lane >> 3)) * chunk) * 32u;
  }
#define POLL0()                                                                                                       \
  asm volatile("global_load_dwordx4 %0, %4, %8 sc1\n\t"                                                               \
               "global_load_dwordx4 %1, %5, %8 sc1\n\t"                                                               \
               "global_load_dwordx4 %2, %6, %8 sc1\n\t"                                                               \
               "global_load_dwordx4 %3, %7, %8 sc1"                                                                   \
               : "=&{v[240:243]}"(cv[0]), "=&{v[244:247]}"(cv[1]), "=&{v[248:251]}"(cv[2]), "=&{v[252:255]}"(cv[3])   \
               : "v"(vo[0]), "v"(vo[1]), "v"(vo[2]), "v"(vo[3]), "s"(ib)                                              \
               : "memory")
  const LAS bf16x8* wl = (const LAS bf16x8*)(smem + OFF_W) + lane;
  const f32x16 zero16 = {0.f, 0.f, 0.f, 0.f, 0.f, 0.f, 0.f, 0.f, 0.f, 0.f, 0.f, 0.f, 0.f, 0.f, 0.f, 0.f};
  auto widx = [&](int m, int ct) { return m == 6 ? 16 + ct : m < 11 ? m - 7 + 4 * ct : 8 + m - 11 + 4 * ct; };
  bf16x8 wr[2][15];
  const unsigned to = lds0 + OFF_W + ((lane >> 2) & 3) * 128 + (lane & 1) * 64 + ((lane >> 4) & 1) * 16 + ((lane >> 1) & 1) * 8;
  auto xa = [&]() {
    bf16x8 wa[8];
#pragma unroll
    for (int k = 0; k < 8; ++k) wa[k] = rawfrag(to + (NBLOB + (k >> 1)) * 1024 + h * 4096 + (k & 1) * 32);
    asm volatile("" : "+v"(wa[0]), "+v"(wa[1]), "+v"(wa[2]), "+v"(wa[3]), "+v"(wa[4]), "+v"(wa[5]), "+v"(wa[6]), "+v"(wa[7]),
                      "+{v[160:163]}"(xg[0]), "+{v[164:167]}"(xg[1]), "+{v[168:171]}"(xg[2]), "+{v[172:175]}"(xg[3]));
    f32x16 XA[2] = {zero16, zero16};
#pragma unroll
    for (int k = 0; k < 4; ++k)
#pragma unroll
      for (int ct = 0; ct < 2; ++ct) XA[ct] = MFMA32(__builtin_bit_cast(bf16x8, xg[k]), wa[k * 2 + ct], XA[ct]);
#pragma unroll
    for (int q = 0; q < 4; ++q) {
      u32x4 xp;
#pragma unroll
      for (int j = 0; j < 4; ++j)
        xp[j] = pack_bf16(XA[q & 1][8 * (q >> 1) + 2 * j] * NLOG2E, XA[q & 1][8 * (q >> 1) + 2 * j + 1] * NLOG2E);
      *(LAS u32x4*)((LAS char*)smem + OFF_R1 + wave * RING_SLOT + q * 1024 + lane * 16) = xp;
    }
  };
  {
    auto prep = [&](unsigned f, unsigned hs, float scale) {
      u32x4 w[2];
#pragma unroll
      for (int ct = 0; ct < 2; ++ct) w[ct] = __builtin_bit_cast(u32x4, rawfrag(to + f + h * hs + ct * 32));
      asm volatile("s_waitcnt lgkmcnt(0)" : "+v"(w[0]), "+v"(w[1]) :: "memory");
#pragma unroll
      for (int ct = 0; ct < 2; ++ct) {
#pragma unroll
        for (int j = 0; j < 4; ++j)
          w[ct][j] = pack_bf16(__uint_as_float(w[ct][j] << 16) * scale, __uint_as_float(w[ct][j] & 0xffff0000u) * scale);
        *(LAS u32x4*)((LAS char*)smem + OFF_W + f + ct * hs + lane * 16) = w[ct];
      }
    };
    {
      const unsigned xr = min(nodeof((bu * 8 + wave) * 32 + r) - xn0, 31u);
      unsigned xa_ = lds0 + OFF_L + wave * LIST_BYTES + xr * 128 + (((4 * h) ^ ((xr >> 1) & 7)) * 16);
      asm volatile("s_waitcnt vmcnt(4)" : "+v"(xa_) :: "memory");
#pragma unroll
      for (int t = 0; t < 4; ++t) xg[t] = *(const LAS u32x4*)(size_t)(xa_ ^ (t * 16));
      asm volatile("" : "+v"(xg[0]), "+v"(xg[1]), "+v"(xg[2]), "+v"(xg[3]) :: "memory");
      *(LAS unsigned*)((LAS char*)smem + OFF_L + wave * LIST_BYTES + h * LARR + r * 4) = ~0u;
    }
    asm volatile("s_waitcnt vmcnt(4) lgkmcnt(0)\n\ts_barrier"
                 : "+v"(vo[0]), "+v"(vo[1]), "+v"(vo[2]), "+v"(vo[3]), "+v"(eb[0]), "+v"(eb[1]), "+v"(eb[2]), "+v"(eb[3])
                 :: "memory");
    xa();
    {
      unsigned f = (wave + (wave & 4)) * 1024, hs = 4096;
      const float scale = wave < 4 ? NLOG2E : 1.0f;
#pragma unroll 1
      while (true) {
        asm volatile("" : "+s"(f), "+s"(hs));
        prep(f, hs, scale);
        if (f != 0u) break;
        f = 16 * 1024;
        hs = 1024;
      }
    }
    PT(6);
    POLL0();
    glds16(wu_g + wave * 512, lane * 16, lds0 + OFF_W + (18 + wave) * 1024);
  }
  const unsigned l0 = lds0 + OFF_L;
  unsigned aLX = l0 + (lane >> 3) * 16;
  unsigned aLE = l0 + LARR + ((((lane >> 1) & 7) << 2) | (lane >> 4)) * 4;
  unsigned aLS4 = l0 + (4 * h + ((lane >> 2) & 3)) * 16;
  unsigned aLS1 = l0 + (((r & 7) << 2) | (r >> 3)) * 4 + 2;
  unsigned cx0 = (unsigned)(((lane & 7) ^ (lane >> 4)) * 16);
  unsigned cx1 = cx0 ^ 64u;
  unsigned ce = (unsigned)((((lane & 1) ^ ((lane >> 4) & 1))) * 16);
  const unsigned ringd = 8 * RING_SLOT;
  unsigned cS = lds0 + OFF_T1 + (((lane >> 4) & 1) * 16 + (lane & 3) * 4) * 2;
  unsigned cSt = lds0 + OFF_T2 + h * 32;
  unsigned fo[4];
#pragma unroll
  for (int t = 0; t < 4; ++t) fo[t] = lds0 + OFF_R0 + (unsigned)(r * 128 + (((4 * h + t) ^ ((r >> 1) & 7)) * 16));
  unsigned foe = lds0 + OFF_R0 + (unsigned)(4096 + r * 32 + ((h ^ ((r >> 3) & 1)) * 16));
  f32x16 Y[4];
#pragma unroll
  for (int q = 0; q < 4; ++q)
#pragma unroll
    for (int i = 0; i < 16; i += 2) {
      f32x2 y;
      if (q < 2) asm volatile("v_mov_b64 %0, %1" : "=v"(y) : "s"(0x358637bd358637bdull));
      else asm volatile("v_mov_b64 %0, 0" : "=v"(y));
      Y[q][i] = y[0];
      Y[q][i + 1] = y[1];
    }
  f32x16 G0, V0, G1, V1;
  bf16x8 S[2], St[2], as[4], ae, b[3];
  u32x4 pd[2], pn[2];
  float tq[16], rq[16], pq[16];
  auto pipe_zero = [&]() {
#pragma unroll
    for (int j = 0; j < 8; ++j) { S[0][j] = 0; S[1][j] = 0; }
#pragma unroll
    for (int j = 0; j < 4; ++j) { pd[0][j] = 0; pd[1][j] = 0; pn[0][j] = 0; pn[1][j] = 0; }
#pragma unroll
    for (int i = 0; i < 16; ++i) V1[i] = 0.f;
    rq[14] = 0.f; rq[15] = 0.f;
  };
  unsigned rid = lane & 7;
  asm volatile("" : "+v"(aLX), "+v"(aLE), "+v"(aLS4), "+v"(aLS1), "+v"(cx0), "+v"(cx1), "+v"(ce), "+v"(cS), "+v"(cSt),
                    "+v"(fo[0]), "+v"(fo[1]), "+v"(fo[2]), "+v"(fo[3]), "+v"(foe), "+v"(rid));
  asm volatile("" : "+v"(Y[0]), "+v"(Y[1]), "+v"(Y[2]), "+v"(Y[3]));
  auto ld128 = [&](unsigned a) { return *(const LAS u32x4*)(size_t)a; };
  auto gather5 = [&](const u32x4 sx, unsigned re, unsigned dstb) {
    glds16x(x, sx[0], cx0, dstb);
    glds16x(x, sx[1], cx1, dstb + 1024);
    glds16x(x, sx[2], cx0, dstb + 2048);
    glds16x(x, sx[3], cx1, dstb + 3072);
    glds16e(ES, re, ce, dstb + 4096);
  };
  unsigned rq4 = ((lane >> 3) & 3) * 4;
  unsigned early, just;
  asm volatile(
        "s_mov_b32 s96, 0x800000\n\t"
        "v_mov_b32 v181, %18\n\t"
        "v_mov_b32 v180, 5120\n\t"
        "v_mov_b32 v177, 0x0c040100\n\t"
        "v_mov_b32 v178, 0x0c050100\n\t"
        "v_mov_b32 v179, 0x0c060100\n\t"
        "s_mov_b32 s52, 0x7f7f7f7f\n\t"
        "s_mov_b32 s53, 0x7f7f7f7f\n\t"
        "s_mov_b64 s[54:55], 0\n\t"
        "s_mov_b32 %12, 0\n\t"
        "v_mov_b32 v191, 16\n\t"
        "s_waitcnt vmcnt(%30)\n\t"
        "v_add3_u32 v228, v240, v241, v242\n\t"
        "v_add3_u32 v231, v244, v245, v246\n\t"
        "v_add3_u32 v228, v228, v243, %19\n\t"
        "v_add3_u32 v231, v231, v247, %19\n\t"
        "v_cmp_ne_u32_sdwa s[72:73], v243, %20 src0_sel:BYTE_3 src1_sel:DWORD\n\t"
        "v_add_u32_dpp v228, v228, v228 quad_perm:[1,0,3,2] row_mask:0xf bank_mask:0xf\n\t"
        "v_add_u32_dpp v231, v231, v231 quad_perm:[1,0,3,2] row_mask:0xf bank_mask:0xf\n\t"
        "v_cmp_ne_u32_sdwa s[78:79], v247, %20 src0_sel:BYTE_3 src1_sel:DWORD\n\t"
        "v_add_u32_dpp v228, v228, v228 quad_perm:[2,3,0,1] row_mask:0xf bank_mask:0xf\n\t"
        "v_add_u32_dpp v231, v231, v231 quad_perm:[2,3,0,1] row_mask:0xf bank_mask:0xf\n\t"
        "s_nop 0\n\t"
        "v_add_u32_dpp v228, v228, v228 row_half_mirror row_mask:0xf bank_mask:0xf\n\t"
        "v_add_u32_dpp v231, v231, v231 row_half_mirror row_mask:0xf bank_mask:0xf\n\t"
        "v_cmp_gt_u32 s[64:65], 2, v228\n\t"
        "v_cmp_eq_u32 s[56:57], 1, v228\n\t"
        "v_cmp_gt_u32 s[66:67], 2, v231\n\t"
        "v_cmp_eq_u32 s[58:59], 1, v231\n\t"
        "s_and_b64 s[72:73], s[72:73], s[64:65]\n\t"
        "s_cselect_b64 s[64:65], 0, s[64:65]\n\t"
        "s_and_b64 s[56:57], s[56:57], s[64:65]\n\t"
        "s_not_b64 %0, s[64:65]\n\t"
        "s_or_b64 s[54:55], s[54:55], s[56:57]\n\t"
        "s_or_b64 s[54:55], s[54:55], %0\n\t"
        "s_and_b64 s[78:79], s[78:79], s[66:67]\n\t"
        "s_cselect_b64 s[66:67], 0, s[66:67]\n\t"
        "s_and_b64 s[58:59], s[58:59], s[66:67]\n\t"
        "s_not_b64 %1, s[66:67]\n\t"
        "s_or_b64 s[54:55], s[54:55], s[58:59]\n\t"
        "s_or_b64 s[54:55], s[54:55], %1\n\t"
        "s_mov_b64 exec, s[64:65]\n\t"
        "v_cmpx_ne_u32 s[72:73], -1, v240\n\t"
        "v_bfe_u32 v192, v243, 5, 3\n\t"
        "v_lshl_add_u32 v204, v192, 4, %21\n\t"
        "ds_add_rtn_u32 v204, v204, v191\n\t"
        "v_perm_b32 v216, v243, v240, v177\n\t"
        "v_mad_u32_u16 v228, v240, 32, %13 op_sel:[1,0,0,0]\n\t"
        "v_cmpx_ne_u32 s[74:75], -1, v241\n\t"
        "v_bfe_u32 v193, v243, 13, 3\n\t"
        "v_lshl_add_u32 v205, v193, 4, %21\n\t"
        "ds_add_rtn_u32 v205, v205, v191\n\t"
        "v_perm_b32 v217, v243, v241, v178\n\t"
        "v_mad_u32_u16 v229, v241, 32, %13 op_sel:[1,0,0,0]\n\t"
        "s_and_b64 exec, exec, s[52:53]\n\t"
        "v_cmpx_ne_u32 s[76:77], -1, v242\n\t"
        "v_bfe_u32 v194, v243, 21, 3\n\t"
        "v_lshl_add_u32 v206, v194, 4, %21\n\t"
        "ds_add_rtn_u32 v206, v206, v191\n\t"
        "v_perm_b32 v218, v243, v242, v179\n\t"
        "v_mad_u32_u16 v230, v242, 32, %13 op_sel:[1,0,0,0]\n\t"
        "s_mov_b64 exec, s[66:67]\n\t"
        "v_cmpx_ne_u32 s[78:79], -1, v244\n\t"
        "v_bfe_u32 v195, v247, 5, 3\n\t"
        "v_lshl_add_u32 v207, v195, 4, %21\n\t"
        "ds_add_rtn_u32 v207, v207, v191\n\t"
        "v_perm_b32 v219, v247, v244, v177\n\t"
        "v_mad_u32_u16 v231, v244, 32, %14 op_sel:[1,0,0,0]\n\t"
        "v_cmpx_ne_u32 s[80:81], -1, v245\n\t"
        "v_bfe_u32 v196, v247, 13, 3\n\t"
        "v_lshl_add_u32 v208, v196, 4, %21\n\t"
        "ds_add_rtn_u32 v208, v208, v191\n\t"
        "v_perm_b32 v220, v247, v245, v178\n\t"
        "v_mad_u32_u16 v232, v245, 32, %14 op_sel:[1,0,0,0]\n\t"
        "s_and_b64 exec, exec, s[52:53]\n\t"
        "v_cmpx_ne_u32 s[82:83], -1, v246\n\t"
        "v_bfe_u32 v197, v247, 21, 3\n\t"
        "v_lshl_add_u32 v209, v197, 4, %21\n\t"
        "ds_add_rtn_u32 v209, v209, v191\n\t"
        "v_perm_b32 v221, v247, v246, v179\n\t"
        "v_mad_u32_u16 v233, v246, 32, %14 op_sel:[1,0,0,0]\n\t"
        "s_mov_b64 exec, -1\n\t"
        "s_waitcnt vmcnt(%31)\n\t"
        "v_add3_u32 v234, v248, v249, v250\n\t"
        "v_add3_u32 v237, v252, v253, v254\n\t"
        "v_add3_u32 v234, v234, v251, %19\n\t"
        "v_add3_u32 v237, v237, v255, %19\n\t"
        "v_cmp_ne_u32_sdwa s[84:85], v251, %20 src0_sel:BYTE_3 src1_sel:DWORD\n\t"
        "v_add_u32_dpp v234, v234, v234 quad_perm:[1,0,3,2] row_mask:0xf bank_mask:0xf\n\t"
        "v_add_u32_dpp v237, v237, v237 quad_perm:[1,0,3,2] row_mask:0xf bank_mask:0xf\n\t"
        "v_cmp_ne_u32_sdwa s[90:91], v255, %20 src0_sel:BYTE_3 src1_sel:DWORD\n\t"
        "v_add_u32_dpp v234, v234, v234 quad_perm:[2,3,0,1] row_mask:0xf bank_mask:0xf\n\t"
        "v_add_u32_dpp v237, v237, v237 quad_perm:[2,3,0,1] row_mask:0xf bank_mask:0xf\n\t"
        "s_nop 0\n\t"
        "v_add_u32_dpp v234, v234, v234 row_half_mirror row_mask:0xf bank_mask:0xf\n\t"
        "v_add_u32_dpp v237, v237, v237 row_half_mirror row_mask:0xf bank_mask:0xf\n\t"
        "v_cmp_gt_u32 s[68:69], 2, v234\n\t"
        "v_cmp_eq_u32 s[60:61], 1, v234\n\t"
        "v_cmp_gt_u32 s[70:71], 2, v237\n\t"
        "v_cmp_eq_u32 s[62:63], 1, v237\n\t"
        "s_and_b64 s[84:85], s[84:85], s[68:69]\n\t"
        "s_cselect_b64 s[68:69], 0, s[68:69]\n\t"
        "s_and_b64 s[60:61], s[60:61], s[68:69]\n\t"
        "s_not_b64 %2, s[68:69]\n\t"
        "s_or_b64 s[54:55], s[54:55], s[60:61]\n\t"
        "s_or_b64 s[54:55], s[54:55], %2\n\t"
        "s_and_b64 s[90:91], s[90:91], s[70:71]\n\t"
        "s_cselect_b64 s[70:71], 0, s[70:71]\n\t"
        "s_and_b64 s[62:63], s[62:63], s[70:71]\n\t"
        "s_not_b64 %3, s[70:71]\n\t"
        "s_or_b64 s[54:55], s[54:55], s[62:63]\n\t"
        "s_or_b64 s[54:55], s[54:55], %3\n\t"
        "s_mov_b64 exec, s[72:73]\n\t"
        "s_waitcnt lgkmcnt(3)\n\t"
        "v_min_u32 v204, v181, v204\n\t"
        "v_min_u32 v205, v181, v205\n\t"
        "v_min_u32 v206, v181, v206\n\t"
        "v_mad_u32_u24 v204, v192, v180, v204\n\t"
        "v_mad_u32_u24 v205, v193, v180, v205\n\t"
        "v_mad_u32_u24 v206, v194, v180, v206\n\t"
        "ds_write2st64_b32 v204, v216, v228 offset1:10\n\t"
        "s_mov_b64 exec, s[74:75]\n\t"
        "ds_write2st64_b32 v205, v217, v229 offset1:10\n\t"
        "s_mov_b64 exec, s[76:77]\n\t"
        "ds_write2st64_b32 v206, v218, v230 offset1:10\n\t"
        "s_mov_b64 exec, s[68:69]\n\t"
        "v_cmpx_ne_u32 s[84:85], -1, v248\n\t"
        "v_bfe_u32 v198, v251, 5, 3\n\t"
        "v_lshl_add_u32 v210, v198, 4, %21\n\t"
        "ds_add_rtn_u32 v210, v210, v191\n\t"
        "v_perm_b32 v222, v251, v248, v177\n\t"
        "v_mad_u32_u16 v234, v248, 32, %15 op_sel:[1,0,0,0]\n\t"
        "v_cmpx_ne_u32 s[86:87], -1, v249\n\t"
        "v_bfe_u32 v199, v251, 13, 3\n\t"
        "v_lshl_add_u32 v211, v199, 4, %21\n\t"
        "ds_add_rtn_u32 v211, v211, v191\n\t"
        "v_perm_b32 v223, v251, v249, v178\n\t"
        "v_mad_u32_u16 v235, v249, 32, %15 op_sel:[1,0,0,0]\n\t"
        "s_and_b64 exec, exec, s[52:53]\n\t"
        "v_cmpx_ne_u32 s[88:89], -1, v250\n\t"
        "v_bfe_u32 v200, v251, 21, 3\n\t"
        "v_lshl_add_u32 v212, v200, 4, %21\n\t"
        "ds_add_rtn_u32 v212, v212, v191\n\t"
        "v_perm_b32 v224, v251, v250, v179\n\t"
        "v_mad_u32_u16 v236, v250, 32, %15 op_sel:[1,0,0,0]\n\t"
        "s_mov_b64 exec, s[78:79]\n\t"
        "s_waitcnt lgkmcnt(6)\n\t"
        "v_min_u32 v207, v181, v207\n\t"
        "v_min_u32 v208, v181, v208\n\t"
        "v_min_u32 v209, v181, v209\n\t"
        "v_mad_u32_u24 v207, v195, v180, v207\n\t"
        "v_mad_u32_u24 v208, v196, v180, v208\n\t"
        "v_mad_u32_u24 v209, v197, v180, v209\n\t"
        "ds_write2st64_b32 v207, v219, v231 offset1:10\n\t"
        "s_mov_b64 exec, s[80:81]\n\t"
        "ds_write2st64_b32 v208, v220, v232 offset1:10\n\t"
        "s_mov_b64 exec, s[82:83]\n\t"
        "ds_write2st64_b32 v209, v221, v233 offset1:10\n\t"
        "s_mov_b64 exec, -1\n\t"
        "ds_read_b128 v[184:187], %22\n\t"
        "ds_read_b32 v183, %23\n\t"
        "s_mov_b64 exec, s[70:71]\n\t"
        "v_cmpx_ne_u32 s[90:91], -1, v252\n\t"
        "v_bfe_u32 v201, v255, 5, 3\n\t"
        "v_lshl_add_u32 v213, v201, 4, %21\n\t"
        "ds_add_rtn_u32 v213, v213, v191\n\t"
        "v_perm_b32 v225, v255, v252, v177\n\t"
        "v_mad_u32_u16 v237, v252, 32, %16 op_sel:[1,0,0,0]\n\t"
        "v_cmpx_ne_u32 s[92:93], -1, v253\n\t"
        "v_bfe_u32 v202, v255, 13, 3\n\t"
        "v_lshl_add_u32 v214, v202, 4, %21\n\t"
        "ds_add_rtn_u32 v214, v214, v191\n\t"
        "v_perm_b32 v226, v255, v253, v178\n\t"
        "v_mad_u32_u16 v238, v253, 32, %16 op_sel:[1,0,0,0]\n\t"
        "s_and_b64 exec, exec, s[52:53]\n\t"
        "v_cmpx_ne_u32 s[94:95], -1, v254\n\t"
        "v_bfe_u32 v203, v255, 21, 3\n\t"
        "v_lshl_add_u32 v215, v203, 4, %21\n\t"
        "ds_add_rtn_u32 v215, v215, v191\n\t"
        "v_perm_b32 v227, v255, v254, v179\n\t"
        "v_mad_u32_u16 v239, v254, 32, %16 op_sel:[1,0,0,0]\n\t"
        "s_cmp_eq_u64 s[54:55], 0\n\t"
        "s_cbranch_scc1 59f\n\t"
        "s_mov_b64 exec, s[56:57]\n\t"
        "s_cbranch_execz 40f\n\t"
        "v_bfe_u32 v188, %4, 7, 8\n\t"
        "v_mov_b32 v189, 1\n\t"
        "ds_write_b8 v188, v189 offset:256\n\t"
        "v_add_u32 %4, s96, %4\n\t"
        "s_or_b64 %0, %0, exec\n\t"
        "40:\n\t"
        "s_mov_b64 exec, %0\n\t"
        "s_cbranch_execz 50f\n\t"
        "global_load_dwordx4 v[240:243], %4, %17 sc1\n\t"
        "50:\n\t"
        "s_mov_b64 exec, s[58:59]\n\t"
        "s_cbranch_execz 41f\n\t"
        "v_bfe_u32 v188, %5, 7, 8\n\t"
        "v_mov_b32 v189, 1\n\t"
        "ds_write_b8 v188, v189 offset:256\n\t"
        "v_add_u32 %5, s96, %5\n\t"
        "s_or_b64 %1, %1, exec\n\t"
        "41:\n\t"
        "s_mov_b64 exec, %1\n\t"
        "s_cbranch_execz 51f\n\t"
        "global_load_dwordx4 v[244:247], %5, %17 sc1\n\t"
        "51:\n\t"
        "s_mov_b64 exec, s[60:61]\n\t"
        "s_cbranch_execz 42f\n\t"
        "v_bfe_u32 v188, %6, 7, 8\n\t"
        "v_mov_b32 v189, 1\n\t"
        "ds_write_b8 v188, v189 offset:256\n\t"
        "v_add_u32 %6, s96, %6\n\t"
        "s_or_b64 %2, %2, exec\n\t"
        "42:\n\t"
        "s_mov_b64 exec, %2\n\t"
        "s_cbranch_execz 52f\n\t"
        "global_load_dwordx4 v[248:251], %6, %17 sc1\n\t"
        "52:\n\t"
        "s_mov_b64 exec, s[62:63]\n\t"
        "s_cbranch_execz 43f\n\t"
        "v_bfe_u32 v188, %7, 7, 8\n\t"
        "v_mov_b32 v189, 1\n\t"
        "ds_write_b8 v188, v189 offset:256\n\t"
        "v_add_u32 %7, s96, %7\n\t"
        "s_or_b64 %3, %3, exec\n\t"
        "43:\n\t"
        "s_mov_b64 exec, %3\n\t"
        "s_cbranch_execz 53f\n\t"
        "global_load_dwordx4 v[252:255], %7, %17 sc1\n\t"
        "53:\n\t"
        "s_waitcnt lgkmcnt(0)\n\t"
        "59:\n\t"
        "s_mov_b64 exec, s[84:85]\n\t"
        "s_waitcnt lgkmcnt(8)\n\t"
        "v_min_u32 v210, v181, v210\n\t"
        "v_min_u32 v211, v181, v211\n\t"
        "v_min_u32 v212, v181, v212\n\t"
        "v_mad_u32_u24 v210, v198, v180, v210\n\t"
        "v_mad_u32_u24 v211, v199, v180, v211\n\t"
        "v_mad_u32_u24 v212, v200, v180, v212\n\t"
        "ds_write2st64_b32 v210, v222, v234 offset1:10\n\t"
        "s_mov_b64 exec, s[86:87]\n\t"
        "ds_write2st64_b32 v211, v223, v235 offset1:10\n\t"
        "s_mov_b64 exec, s[88:89]\n\t"
        "ds_write2st64_b32 v212, v224, v236 offset1:10\n\t"
        "s_mov_b64 exec, -1\n\t"
        "s_movk_i32 s96, 128\n\t"
        "s_mov_b32 m0, %29\n\t"
        "s_waitcnt lgkmcnt(6)\n\t"
        "v_max3_u32 v182, v184, v185, v186\n\t"
        "v_max3_u32 v182, v182, v187, v183\n\t"
        "v_cmp_eq_u32 vcc, -1, v182\n\t"
        "v_mad_u32_u16 v184, v184, s96, %24\n\t"
        "v_mad_u32_u16 v185, v185, s96, %25\n\t"
        "v_mad_u32_u16 v186, v186, s96, %24\n\t"
        "v_mad_u32_u16 v187, v187, s96, %25\n\t"
        "v_add_u32 v183, v183, %26\n\t"
        "s_cbranch_vccnz 60f\n\t"
        "global_load_lds_dwordx4 v184, %27\n\t"
        "s_addk_i32 m0, 1024\n\t"
        "s_nop 0\n\t"
        "global_load_lds_dwordx4 v185, %27\n\t"
        "s_addk_i32 m0, 1024\n\t"
        "s_nop 0\n\t"
        "global_load_lds_dwordx4 v186, %27\n\t"
        "s_addk_i32 m0, 1024\n\t"
        "s_nop 0\n\t"
        "global_load_lds_dwordx4 v187, %27\n\t"
        "s_addk_i32 m0, 1024\n\t"
        "s_nop 0\n\t"
        "global_load_lds_dwordx4 v183, %28\n\t"
        "s_mov_b32 %12, 1\n\t"
        "60:\n\t"
        "s_mov_b64 exec, s[90:91]\n\t"
        "s_waitcnt lgkmcnt(3)\n\t"
        "v_min_u32 v213, v181, v213\n\t"
        "v_min_u32 v214, v181, v214\n\t"
        "v_min_u32 v215, v181, v215\n\t"
        "v_mad_u32_u24 v213, v201, v180, v213\n\t"
        "v_mad_u32_u24 v214, v202, v180, v214\n\t"
        "v_mad_u32_u24 v215, v203, v180, v215\n\t"
        "ds_write2st64_b32 v213, v225, v237 offset1:10\n\t"
        "s_mov_b64 exec, s[92:93]\n\t"
        "ds_write2st64_b32 v214, v226, v238 offset1:10\n\t"
        "s_mov_b64 exec, s[94:95]\n\t"
        "ds_write2st64_b32 v215, v227, v239 offset1:10\n\t"
        "s_mov_b64 exec, -1\n\t"
        : "=&s"(pend[0]), "=&s"(pend[1]), "=&s"(pend[2]), "=&s"(pend[3]), "+v"(vo[0]), "+v"(vo[1]), "+v"(vo[2]),
          "+v"(vo[3]), "+{v[240:243]}"(cv[0]), "+{v[244:247]}"(cv[1]), "+{v[248:251]}"(cv[2]), "+{v[252:255]}"(cv[3]),
          "=&s"(early)
        : "v"(eb[0]), "v"(eb[1]), "v"(eb[2]), "v"(eb[3]), "s"(ib), "n"(OFF_L + LARR - 16), "v"(tag[0]), "v"(tag[1]),
          "v"(rq4), "v"(aLX + wave * LIST_BYTES), "v"(aLE + wave * LIST_BYTES), "v"(cx0), "v"(cx1), "v"(ce), "s"(x),
          "s"(ES), "s"(lds0 + OFF_R0 + wave * RING_SLOT), "n"(3), "n"(1)
        :
          "memory", "vcc", "scc", "m0", "v177", "v178", "v179", "v180", "v181", "v182", "v183", "v184", "v185",
          "v186", "v187", "v188", "v189", "v190", "v191", "v192", "v193", "v194", "v195", "v196", "v197", "v198",
          "v199", "v200", "v201", "v202", "v203", "v204", "v205", "v206", "v207", "v208", "v209", "v210", "v211",
          "v212", "v213", "v214", "v215", "v216", "v217", "v218", "v219", "v220", "v221", "v222", "v223", "v224",
          "v225", "v226", "v227", "v228", "v229", "v230", "v231", "v232", "v233", "v234", "v235", "v236", "v237",
          "v238", "v239", "s52", "s53", "s54", "s55", "s56", "s57", "s58", "s59", "s60", "s61", "s62", "s63", "s64",
          "s65", "s66", "s67", "s68", "s69", "s70", "s71", "s72", "s73", "s74", "s75", "s76", "s77", "s78", "s79",
          "s80", "s81", "s82", "s83", "s84", "s85", "s86", "s87", "s88", "s89", "s90", "s91", "s92", "s93", "s94",
          "s95", "s96");
  auto uni = [&]() {
#pragma unroll
    for (int k = 0; k < 4; ++k)
      pend[k] = ((unsigned long long)(unsigned)__builtin_amdgcn_readfirstlane((unsigned)(pend[k] >> 32)) << 32) |
                (unsigned)__builtin_amdgcn_readfirstlane((unsigned)pend[k]);
  };
  uni();
  early = __builtin_amdgcn_readfirstlane(early);
  just = early;
  if (__builtin_expect((pend[0] | pend[1] | pend[2] | pend[3] | (unsigned long long)(early ^ 1u)) != 0ull, 0))
#pragma clang loop unroll(disable)
  while (true) {
    if (!early) {
      const u32x4 sx = ld128(aLX + wave * LIST_BYTES);
      const unsigned re = *(const LAS unsigned*)(size_t)(aLE + wave * LIST_BYTES);
      if (__ballot(max(max(max(sx[0], sx[1]), max(sx[2], sx[3])), re) == ~0u) == 0ull) {
        gather5(sx, re, lds0 + OFF_R0 + wave * RING_SLOT);
        early = 1u;
        just = 1u;
      }
    }
    if ((pend[0] | pend[1] | pend[2] | pend[3]) == 0ull || --spin < 0) break;
    asm volatile(
        "s_mov_b32 s96, 0x800000\n\t"
        "v_mov_b32 v181, %17\n\t"
        "v_mov_b32 v180, 5120\n\t"
        "v_mov_b32 v177, 0x0c040100\n\t"
        "v_mov_b32 v178, 0x0c050100\n\t"
        "v_mov_b32 v179, 0x0c060100\n\t"
        "s_mov_b32 s52, 0x7f7f7f7f\n\t"
        "s_mov_b32 s53, 0x7f7f7f7f\n\t"
        "v_mov_b32 v191, 16\n\t"
        "s_waitcnt vmcnt(5)\n\t"
        "s_cmp_lg_u32 %21, 0\n\t"
        "s_cbranch_scc1 9f\n\t"
        "s_waitcnt vmcnt(0)\n\t"
        "9:\n\t"
        "v_add3_u32 v228, v240, v241, v242\n\t"
        "v_add3_u32 v229, v244, v245, v246\n\t"
        "v_add3_u32 v230, v248, v249, v250\n\t"
        "v_add3_u32 v231, v252, v253, v254\n\t"
        "v_add3_u32 v228, v228, v243, %18\n\t"
        "v_add3_u32 v229, v229, v247, %18\n\t"
        "v_add3_u32 v230, v230, v251, %18\n\t"
        "v_add3_u32 v231, v231, v255, %18\n\t"
        "v_add_u32_dpp v228, v228, v228 quad_perm:[1,0,3,2] row_mask:0xf bank_mask:0xf\n\t"
        "v_add_u32_dpp v229, v229, v229 quad_perm:[1,0,3,2] row_mask:0xf bank_mask:0xf\n\t"
        "v_add_u32_dpp v230, v230, v230 quad_perm:[1,0,3,2] row_mask:0xf bank_mask:0xf\n\t"
        "v_add_u32_dpp v231, v231, v231 quad_perm:[1,0,3,2] row_mask:0xf bank_mask:0xf\n\t"
        "v_add_u32_dpp v228, v228, v228 quad_perm:[2,3,0,1] row_mask:0xf bank_mask:0xf\n\t"
        "v_add_u32_dpp v229, v229, v229 quad_perm:[2,3,0,1] row_mask:0xf bank_mask:0xf\n\t"
        "v_add_u32_dpp v230, v230, v230 quad_perm:[2,3,0,1] row_mask:0xf bank_mask:0xf\n\t"
        "v_add_u32_dpp v231, v231, v231 quad_perm:[2,3,0,1] row_mask:0xf bank_mask:0xf\n\t"
        "v_add_u32_dpp v228, v228, v228 row_half_mirror row_mask:0xf bank_mask:0xf\n\t"
        "v_add_u32_dpp v229, v229, v229 row_half_mirror row_mask:0xf bank_mask:0xf\n\t"
        "v_add_u32_dpp v230, v230, v230 row_half_mirror row_mask:0xf bank_mask:0xf\n\t"
        "v_add_u32_dpp v231, v231, v231 row_half_mirror row_mask:0xf bank_mask:0xf\n\t"
        "v_cmp_ne_u32_sdwa s[72:73], v243, %19 src0_sel:BYTE_3 src1_sel:DWORD\n\t"
        "v_cmp_gt_u32 s[64:65], 2, v228\n\t"
        "v_cmp_eq_u32 s[56:57], 1, v228\n\t"
        "v_cmp_ne_u32_sdwa s[74:75], v247, %19 src0_sel:BYTE_3 src1_sel:DWORD\n\t"
        "v_cmp_gt_u32 s[66:67], 2, v229\n\t"
        "v_cmp_eq_u32 s[58:59], 1, v229\n\t"
        "v_cmp_ne_u32_sdwa s[76:77], v251, %19 src0_sel:BYTE_3 src1_sel:DWORD\n\t"
        "v_cmp_gt_u32 s[68:69], 2, v230\n\t"
        "v_cmp_eq_u32 s[60:61], 1, v230\n\t"
        "v_cmp_ne_u32_sdwa s[78:79], v255, %19 src0_sel:BYTE_3 src1_sel:DWORD\n\t"
        "v_cmp_gt_u32 s[70:71], 2, v231\n\t"
        "v_cmp_eq_u32 s[62:63], 1, v231\n\t"
        "s_and_b64 s[64:65], s[64:65], %0\n\t"
        "s_and_b64 s[72:73], s[72:73], s[64:65]\n\t"
        "s_cselect_b64 s[64:65], 0, s[64:65]\n\t"
        "s_and_b64 s[56:57], s[56:57], s[64:65]\n\t"
        "s_andn2_b64 %0, %0, s[64:65]\n\t"
        "s_cbranch_scc0 10f\n\t"
        "s_mov_b64 exec, %0\n\t"
        "global_load_dwordx4 v[240:243], %4, %16 sc1\n\t"
        "10:\n\t"
        "s_and_b64 s[66:67], s[66:67], %1\n\t"
        "s_and_b64 s[74:75], s[74:75], s[66:67]\n\t"
        "s_cselect_b64 s[66:67], 0, s[66:67]\n\t"
        "s_and_b64 s[58:59], s[58:59], s[66:67]\n\t"
        "s_andn2_b64 %1, %1, s[66:67]\n\t"
        "s_cbranch_scc0 11f\n\t"
        "s_mov_b64 exec, %1\n\t"
        "global_load_dwordx4 v[244:247], %5, %16 sc1\n\t"
        "11:\n\t"
        "s_and_b64 s[68:69], s[68:69], %2\n\t"
        "s_and_b64 s[76:77], s[76:77], s[68:69]\n\t"
        "s_cselect_b64 s[68:69], 0, s[68:69]\n\t"
        "s_and_b64 s[60:61], s[60:61], s[68:69]\n\t"
        "s_andn2_b64 %2, %2, s[68:69]\n\t"
        "s_cbranch_scc0 12f\n\t"
        "s_mov_b64 exec, %2\n\t"
        "global_load_dwordx4 v[248:251], %6, %16 sc1\n\t"
        "12:\n\t"
        "s_and_b64 s[70:71], s[70:71], %3\n\t"
        "s_and_b64 s[78:79], s[78:79], s[70:71]\n\t"
        "s_cselect_b64 s[70:71], 0, s[70:71]\n\t"
        "s_and_b64 s[62:63], s[62:63], s[70:71]\n\t"
        "s_andn2_b64 %3, %3, s[70:71]\n\t"
        "s_cbranch_scc0 13f\n\t"
        "s_mov_b64 exec, %3\n\t"
        "global_load_dwordx4 v[252:255], %7, %16 sc1\n\t"
        "13:\n\t"
        "s_mov_b64 s[54:55], 0\n\t"
        "s_mov_b64 s[72:73], 0\n\t"
        "s_mov_b64 exec, s[64:65]\n\t"
        "s_cbranch_execz 20f\n\t"
        "v_cmpx_ne_u32 s[72:73], -1, v240\n\t"
        "v_bfe_u32 v192, v243, 5, 3\n\t"
        "v_lshl_add_u32 v204, v192, 4, %20\n\t"
        "ds_add_rtn_u32 v204, v204, v191\n\t"
        "v_perm_b32 v216, v243, v240, v177\n\t"
        "v_mad_u32_u16 v228, v240, 32, %12 op_sel:[1,0,0,0]\n\t"
        "v_cmpx_ne_u32 s[74:75], -1, v241\n\t"
        "v_bfe_u32 v193, v243, 13, 3\n\t"
        "v_lshl_add_u32 v205, v193, 4, %20\n\t"
        "ds_add_rtn_u32 v205, v205, v191\n\t"
        "v_perm_b32 v217, v243, v241, v178\n\t"
        "v_mad_u32_u16 v229, v241, 32, %12 op_sel:[1,0,0,0]\n\t"
        "s_and_b64 exec, exec, s[52:53]\n\t"
        "v_cmpx_ne_u32 s[76:77], -1, v242\n\t"
        "v_bfe_u32 v194, v243, 21, 3\n\t"
        "v_lshl_add_u32 v206, v194, 4, %20\n\t"
        "ds_add_rtn_u32 v206, v206, v191\n\t"
        "v_perm_b32 v218, v243, v242, v179\n\t"
        "v_mad_u32_u16 v230, v242, 32, %12 op_sel:[1,0,0,0]\n\t"
        "s_or_b64 s[54:55], s[54:55], s[56:57]\n\t"
        "20:\n\t"
        "s_mov_b64 s[78:79], 0\n\t"
        "s_mov_b64 exec, s[66:67]\n\t"
        "s_cbranch_execz 21f\n\t"
        "v_cmpx_ne_u32 s[78:79], -1, v244\n\t"
        "v_bfe_u32 v195, v247, 5, 3\n\t"
        "v_lshl_add_u32 v207, v195, 4, %20\n\t"
        "ds_add_rtn_u32 v207, v207, v191\n\t"
        "v_perm_b32 v219, v247, v244, v177\n\t"
        "v_mad_u32_u16 v231, v244, 32, %13 op_sel:[1,0,0,0]\n\t"
        "v_cmpx_ne_u32 s[80:81], -1, v245\n\t"
        "v_bfe_u32 v196, v247, 13, 3\n\t"
        "v_lshl_add_u32 v208, v196, 4, %20\n\t"
        "ds_add_rtn_u32 v208, v208, v191\n\t"
        "v_perm_b32 v220, v247, v245, v178\n\t"
        "v_mad_u32_u16 v232, v245, 32, %13 op_sel:[1,0,0,0]\n\t"
        "s_and_b64 exec, exec, s[52:53]\n\t"
        "v_cmpx_ne_u32 s[82:83], -1, v246\n\t"
        "v_bfe_u32 v197, v247, 21, 3\n\t"
        "v_lshl_add_u32 v209, v197, 4, %20\n\t"
        "ds_add_rtn_u32 v209, v209, v191\n\t"
        "v_perm_b32 v221, v247, v246, v179\n\t"
        "v_mad_u32_u16 v233, v246, 32, %13 op_sel:[1,0,0,0]\n\t"
        "s_or_b64 s[54:55], s[54:55], s[58:59]\n\t"
        "21:\n\t"
        "s_mov_b64 s[84:85], 0\n\t"
        "s_mov_b64 exec, s[68:69]\n\t"
        "s_cbranch_execz 22f\n\t"
        "v_cmpx_ne_u32 s[84:85], -1, v248\n\t"
        "v_bfe_u32 v198, v251, 5, 3\n\t"
        "v_lshl_add_u32 v210, v198, 4, %20\n\t"
        "ds_add_rtn_u32 v210, v210, v191\n\t"
        "v_perm_b32 v222, v251, v248, v177\n\t"
        "v_mad_u32_u16 v234, v248, 32, %14 op_sel:[1,0,0,0]\n\t"
        "v_cmpx_ne_u32 s[86:87], -1, v249\n\t"
        "v_bfe_u32 v199, v251, 13, 3\n\t"
        "v_lshl_add_u32 v211, v199, 4, %20\n\t"
        "ds_add_rtn_u32 v211, v211, v191\n\t"
        "v_perm_b32 v223, v251, v249, v178\n\t"
        "v_mad_u32_u16 v235, v249, 32, %14 op_sel:[1,0,0,0]\n\t"
        "s_and_b64 exec, exec, s[52:53]\n\t"
        "v_cmpx_ne_u32 s[88:89], -1, v250\n\t"
        "v_bfe_u32 v200, v251, 21, 3\n\t"
        "v_lshl_add_u32 v212, v200, 4, %20\n\t"
        "ds_add_rtn_u32 v212, v212, v191\n\t"
        "v_perm_b32 v224, v251, v250, v179\n\t"
        "v_mad_u32_u16 v236, v250, 32, %14 op_sel:[1,0,0,0]\n\t"
        "s_or_b64 s[54:55], s[54:55], s[60:61]\n\t"
        "22:\n\t"
        "s_mov_b64 s[90:91], 0\n\t"
        "s_mov_b64 exec, s[70:71]\n\t"
        "s_cbranch_execz 23f\n\t"
        "v_cmpx_ne_u32 s[90:91], -1, v252\n\t"
        "v_bfe_u32 v201, v255, 5, 3\n\t"
        "v_lshl_add_u32 v213, v201, 4, %20\n\t"
        "ds_add_rtn_u32 v213, v213, v191\n\t"
        "v_perm_b32 v225, v255, v252, v177\n\t"
        "v_mad_u32_u16 v237, v252, 32, %15 op_sel:[1,0,0,0]\n\t"
        "v_cmpx_ne_u32 s[92:93], -1, v253\n\t"
        "v_bfe_u32 v202, v255, 13, 3\n\t"
        "v_lshl_add_u32 v214, v202, 4, %20\n\t"
        "ds_add_rtn_u32 v214, v214, v191\n\t"
        "v_perm_b32 v226, v255, v253, v178\n\t"
        "v_mad_u32_u16 v238, v253, 32, %15 op_sel:[1,0,0,0]\n\t"
        "s_and_b64 exec, exec, s[52:53]\n\t"
        "v_cmpx_ne_u32 s[94:95], -1, v254\n\t"
        "v_bfe_u32 v203, v255, 21, 3\n\t"
        "v_lshl_add_u32 v215, v203, 4, %20\n\t"
        "ds_add_rtn_u32 v215, v215, v191\n\t"
        "v_perm_b32 v227, v255, v254, v179\n\t"
        "v_mad_u32_u16 v239, v254, 32, %15 op_sel:[1,0,0,0]\n\t"
        "s_or_b64 s[54:55], s[54:55], s[62:63]\n\t"
        "23:\n\t"
        "s_cmp_eq_u64 s[54:55], 0\n\t"
        "s_cbranch_scc1 59f\n\t"
        "s_mov_b64 exec, s[56:57]\n\t"
        "s_cbranch_execz 40f\n\t"
        "v_bfe_u32 v188, %4, 7, 8\n\t"
        "v_lshrrev_b32 v189, 23, %4\n\t"
        "v_add_u32 v189, 1, v189\n\t"
        "ds_write_b8 v188, v189 offset:256\n\t"
        "v_add_u32 %4, s96, %4\n\t"
        "v_mov_b32 v190, 0\n\t"
        "v_cmpx_eq_u32 vcc, 2, v189\n\t"
        "ds_write_b32 v190, v191 offset:160\n\t"
        "s_andn2_b64 exec, s[56:57], vcc\n\t"
        "s_cbranch_execz 40f\n\t"
        "s_or_b64 %0, %0, exec\n\t"
        "global_load_dwordx4 v[240:243], %4, %16 sc1\n\t"
        "40:\n\t"
        "s_mov_b64 exec, s[58:59]\n\t"
        "s_cbranch_execz 41f\n\t"
        "v_bfe_u32 v188, %5, 7, 8\n\t"
        "v_lshrrev_b32 v189, 23, %5\n\t"
        "v_add_u32 v189, 1, v189\n\t"
        "ds_write_b8 v188, v189 offset:256\n\t"
        "v_add_u32 %5, s96, %5\n\t"
        "v_mov_b32 v190, 0\n\t"
        "v_cmpx_eq_u32 vcc, 2, v189\n\t"
        "ds_write_b32 v190, v191 offset:160\n\t"
        "s_andn2_b64 exec, s[58:59], vcc\n\t"
        "s_cbranch_execz 41f\n\t"
        "s_or_b64 %1, %1, exec\n\t"
        "global_load_dwordx4 v[244:247], %5, %16 sc1\n\t"
        "41:\n\t"
        "s_mov_b64 exec, s[60:61]\n\t"
        "s_cbranch_execz 42f\n\t"
        "v_bfe_u32 v188, %6, 7, 8\n\t"
        "v_lshrrev_b32 v189, 23, %6\n\t"
        "v_add_u32 v189, 1, v189\n\t"
        "ds_write_b8 v188, v189 offset:256\n\t"
        "v_add_u32 %6, s96, %6\n\t"
        "v_mov_b32 v190, 0\n\t"
        "v_cmpx_eq_u32 vcc, 2, v189\n\t"
        "ds_write_b32 v190, v191 offset:160\n\t"
        "s_andn2_b64 exec, s[60:61], vcc\n\t"
        "s_cbranch_execz 42f\n\t"
        "s_or_b64 %2, %2, exec\n\t"
        "global_load_dwordx4 v[248:251], %6, %16 sc1\n\t"
        "42:\n\t"
        "s_mov_b64 exec, s[62:63]\n\t"
        "s_cbranch_execz 43f\n\t"
        "v_bfe_u32 v188, %7, 7, 8\n\t"
        "v_lshrrev_b32 v189, 23, %7\n\t"
        "v_add_u32 v189, 1, v189\n\t"
        "ds_write_b8 v188, v189 offset:256\n\t"
        "v_add_u32 %7, s96, %7\n\t"
        "v_mov_b32 v190, 0\n\t"
        "v_cmpx_eq_u32 vcc, 2, v189\n\t"
        "ds_write_b32 v190, v191 offset:160\n\t"
        "s_andn2_b64 exec, s[62:63], vcc\n\t"
        "s_cbranch_execz 43f\n\t"
        "s_or_b64 %3, %3, exec\n\t"
        "global_load_dwordx4 v[252:255], %7, %16 sc1\n\t"
        "43:\n\t"
        "59:\n\t"
        "s_waitcnt lgkmcnt(0)\n\t"
        "s_mov_b64 exec, s[72:73]\n\t"
        "s_cbranch_execz 30f\n\t"
        "v_min_u32 v204, v181, v204\n\t"
        "v_min_u32 v205, v181, v205\n\t"
        "v_min_u32 v206, v181, v206\n\t"
        "v_mad_u32_u24 v204, v192, v180, v204\n\t"
        "v_mad_u32_u24 v205, v193, v180, v205\n\t"
        "v_mad_u32_u24 v206, v194, v180, v206\n\t"
        "ds_write2st64_b32 v204, v216, v228 offset1:10\n\t"
        "s_mov_b64 exec, s[74:75]\n\t"
        "ds_write2st64_b32 v205, v217, v229 offset1:10\n\t"
        "s_mov_b64 exec, s[76:77]\n\t"
        "ds_write2st64_b32 v206, v218, v230 offset1:10\n\t"
        "30:\n\t"
        "s_mov_b64 exec, s[78:79]\n\t"
        "s_cbranch_execz 31f\n\t"
        "v_min_u32 v207, v181, v207\n\t"
        "v_min_u32 v208, v181, v208\n\t"
        "v_min_u32 v209, v181, v209\n\t"
        "v_mad_u32_u24 v207, v195, v180, v207\n\t"
        "v_mad_u32_u24 v208, v196, v180, v208\n\t"
        "v_mad_u32_u24 v209, v197, v180, v209\n\t"
        "ds_write2st64_b32 v207, v219, v231 offset1:10\n\t"
        "s_mov_b64 exec, s[80:81]\n\t"
        "ds_write2st64_b32 v208, v220, v232 offset1:10\n\t"
        "s_mov_b64 exec, s[82:83]\n\t"
        "ds_write2st64_b32 v209, v221, v233 offset1:10\n\t"
        "31:\n\t"
        "s_mov_b64 exec, s[84:85]\n\t"
        "s_cbranch_execz 32f\n\t"
        "v_min_u32 v210, v181, v210\n\t"
        "v_min_u32 v211, v181, v211\n\t"
        "v_min_u32 v212, v181, v212\n\t"
        "v_mad_u32_u24 v210, v198, v180, v210\n\t"
        "v_mad_u32_u24 v211, v199, v180, v211\n\t"
        "v_mad_u32_u24 v212, v200, v180, v212\n\t"
        "ds_write2st64_b32 v210, v222, v234 offset1:10\n\t"
        "s_mov_b64 exec, s[86:87]\n\t"
        "ds_write2st64_b32 v211, v223, v235 offset1:10\n\t"
        "s_mov_b64 exec, s[88:89]\n\t"
        "ds_write2st64_b32 v212, v224, v236 offset1:10\n\t"
        "32:\n\t"
        "s_mov_b64 exec, s[90:91]\n\t"
        "s_cbranch_execz 33f\n\t"
        "v_min_u32 v213, v181, v213\n\t"
        "v_min_u32 v214, v181, v214\n\t"
        "v_min_u32 v215, v181, v215\n\t"
        "v_mad_u32_u24 v213, v201, v180, v213\n\t"
        "v_mad_u32_u24 v214, v202, v180, v214\n\t"
        "v_mad_u32_u24 v215, v203, v180, v215\n\t"
        "ds_write2st64_b32 v213, v225, v237 offset1:10\n\t"
        "s_mov_b64 exec, s[92:93]\n\t"
        "ds_write2st64_b32 v214, v226, v238 offset1:10\n\t"
        "s_mov_b64 exec, s[94:95]\n\t"
        "ds_write2st64_b32 v215, v227, v239 offset1:10\n\t"
        "33:\n\t"
        "s_mov_b64 exec, -1\n\t"
        : "+&s"(pend[0]), "+&s"(pend[1]), "+&s"(pend[2]), "+&s"(pend[3]), "+v"(vo[0]), "+v"(vo[1]), "+v"(vo[2]),
          "+v"(vo[3]), "+{v[240:243]}"(cv[0]), "+{v[244:247]}"(cv[1]), "+{v[248:251]}"(cv[2]), "+{v[252:255]}"(cv[3])
        : "v"(eb[0]), "v"(eb[1]), "v"(eb[2]), "v"(eb[3]), "s"(ib), "n"(OFF_L + LARR - 16), "v"(tag[0]), "v"(tag[1]),
          "v"(rq4), "s"(just)
        :
          "memory", "vcc", "scc", "v177", "v178", "v179", "v180", "v181", "v188", "v189", "v190", "v191", "v192",
          "v193", "v194", "v195", "v196", "v197", "v198", "v199", "v200", "v201", "v202", "v203", "v204", "v205",
          "v206", "v207", "v208", "v209", "v210", "v211", "v212", "v213", "v214", "v215", "v216", "v217", "v218",
          "v219", "v220", "v221", "v222", "v223", "v224", "v225", "v226", "v227", "v228", "v229", "v230", "v231",
          "v232", "v233", "v234", "v235", "v236", "v237", "v238", "v239", "s52", "s53", "s54", "s55", "s56", "s57",
          "s58", "s59", "s60", "s61", "s62", "s63", "s64", "s65", "s66", "s67", "s68", "s69", "s70", "s71", "s72",
          "s73", "s74", "s75", "s76", "s77", "s78", "s79", "s80", "s81", "s82", "s83", "s84", "s85", "s86", "s87",
          "s88", "s89", "s90", "s91", "s92", "s93", "s94", "s95", "s96");
    uni();
    just = 0u;
  }
  *(LAS unsigned*)((LAS char*)smem + OFF_CNT + 128 + wave * 4) = early;
  asm volatile("s_waitcnt lgkmcnt(0)\n\ts_barrier" ::: "memory");
  if (wave >= 4) __builtin_amdgcn_s_setprio(1);
  const long long tmA = __builtin_amdgcn_s_memrealtime();
  const u32x4 cw = *(const LAS u32x4*)((LAS char*)smem + OFF_CNT + rid * 16);
  const unsigned ef = *(const LAS unsigned*)((LAS char*)smem + OFF_CNT + 128 + rid * 4);
  unsigned slowf = *(const LAS unsigned*)((LAS char*)smem + OFF_CNT + 160);
  asm volatile("" : "+v"(slowf));
  const int key = (int)((cw[0] + cw[1] + cw[2] + cw[3]) | rid);
  int gb;
  {
    unsigned acc = 0u;
#define RK(k) acc = __builtin_amdgcn_alignbit(acc, (unsigned)(__builtin_amdgcn_update_dpp(0, key, 0x120 + k, 0xf, 0xf, false) - key), 31)
    RK(1); RK(2); RK(3); RK(4); RK(5); RK(6); RK(7);
#undef RK
    const int want = RANKOF(wave);
    gb = __builtin_ctzll(__ballot(__builtin_popcount(acc) == want)) & 7;
  }
  const unsigned ring0 = lds0 + OFF_R0 + gb * RING_SLOT;
  const unsigned la = l0 + gb * LIST_BYTES;
  bf16x8 XAf[2][2];
#pragma unroll
  for (int q = 0; q < 4; ++q) XAf[q >> 1][q & 1] = *(const LAS bf16x8*)(size_t)(ring0 + ringd + q * 1024 + lane * 16);
  int cnt = (int)(__builtin_amdgcn_readlane(key, gb) - 4u * l0 - 24u) >> 4;
  const unsigned cmn = __builtin_amdgcn_readlane(min(min(cw[0], cw[1]), min(cw[2], cw[3])), gb) & ~15u;
  const unsigned cmx = __builtin_amdgcn_readlane(max(max(cw[0], cw[1]), max(cw[2], cw[3])), gb) & ~15u;
  const bool slow = cnt > LCAP || cmx > l0 + LARR - 16 || __builtin_amdgcn_readfirstlane(slowf) != 0u;
  const unsigned cg = (bu * 8 + gb) * 32;
  aLX += gb * LIST_BYTES; aLE += gb * LIST_BYTES; aLS4 += gb * LIST_BYTES; aLS1 += gb * LIST_BYTES;
  auto issue_gather = [&](int tile, int rs) {
    gather5(ld128(aLX + tile * 128), *(const LAS unsigned*)(size_t)(aLE + tile * 128), ring0 + rs * ringd);
  };
  auto pads = [&](int cnt, int lane) __attribute__((always_inline)) {
    const int u = cnt + lane;
    const unsigned c = cg + ((u >> 2) & 7) + 8 * (u & 3), n = nodeof(c);
    const unsigned padA = n | (__builtin_amdgcn_readlane(c, __builtin_ctzll(__ballot((n * Mw + Cw) >> 16 != c))) & 0xffu) << 16;
    const unsigned pa = la + u * 4;
    *(LAS unsigned*)(size_t)pa = padA;
    *(LAS unsigned*)(size_t)(pa + 256) = padA;
    *(LAS unsigned*)(size_t)(pa + LARR) = 0u;
    *(LAS unsigned*)(size_t)(pa + LARR + 256) = 0u;
  };
  constexpr int GEND = 64 + NCH;
  int gs = slow ? 0 : GEND, gp = 0;
  auto gen = [&]() __attribute__((always_inline)) {
    int l2, n = 0;
    asm volatile("v_mbcnt_lo_u32_b32 %0, -1, 0\n\tv_mbcnt_hi_u32_b32 %0, -1, %0" : "=v"(l2));
    const LAS unsigned char* xf = (const LAS unsigned char*)smem + OFF_XF;
    auto put = [&](bool ok, unsigned t, unsigned c, int cc) {
      ok = ok && (int)(c >> 5) == gb;
      const unsigned long long bm = __ballot(ok);
      const unsigned pa = la + 4u * (n + __builtin_amdgcn_mbcnt_hi((unsigned)(bm >> 32), __builtin_amdgcn_mbcnt_lo((unsigned)bm, 0u)));
      if (ok) {
        *(LAS unsigned*)(size_t)pa = (t & 0xffffu) | (c << 16);
        *(LAS unsigned*)(size_t)(pa + LARR) = ((unsigned)(cc * chunk) + (t >> 16)) * 32u;
      }
      n += __builtin_popcountll(bm);
    };
#pragma unroll 1
    while (gs < GEND && n <= LCAP - 192) {
      if (gs < 64) {
        const int cc = (gs & 31) * 8 + (l2 >> 3);
        const unsigned* pp = reinterpret_cast<const unsigned*>(ib + (gs >> 5) * IEXT + (gs & 31) * 1024 + l2 * 16);
        const bool live = gs < 32 || xf[cc] != 0;
        const unsigned m = ldd(pp + 3);
#pragma unroll 1
        for (int j = 0; j < 3; ++j) {
          const unsigned t = ldd(pp + j);
          put(live && t != ~0u && (j < 2 || (l2 & 7) != 7), t, (m >> (8 * j)) & 0xffu, cc);
        }
        ++gs;
      } else {
        const int c2 = gs - 64 + l2;
        const unsigned long long fm = __ballot(c2 < NCH && xf[min(c2, NCH - 1)] == 2);
        if (fm == 0ull) { gs = min(gs + 64, GEND); continue; }
        gs += __builtin_ctzll(fm);
        const int cc = gs - 64;
        const unsigned long long ds = ldq(FL + 512 + bu * NCH + cc);
        const int len = __builtin_amdgcn_readfirstlane((int)(ds >> 32));
        const unsigned long long* tp = TS + (unsigned)ds;
#pragma unroll 1
        for (; gp < len && n <= LCAP - 64; gp += 64) {
          const unsigned long long t = ldq(tp + min(gp + l2, len - 1));
          put(gp + l2 < len, (unsigned)t, (unsigned)(t >> 32), cc);
        }
        if (gp >= len) { gp = 0; ++gs; }
      }
    }
    __builtin_amdgcn_s_waitcnt(0x0F70);
    pads(n, l2);
    return n;
  };
  const bool quick = !slow && cmn >= l0 + 256u && __builtin_amdgcn_readlane(ef, gb) != 0u;
  auto gather1 = [&](unsigned skip) {
    u32x4 sx = ld128(aLX + 128);
    unsigned re = *(const LAS unsigned*)(size_t)(aLE + 128);
    asm volatile("s_cmp_lg_u32 %16, 0\n\t"
                 "s_cbranch_scc1 1f\n\t"
                 "s_mov_b32 m0, %14\n\t"
                 "v_mad_u32_u16 %0, %0, %15, %9\n\t"
                 "v_mad_u32_u16 %1, %1, %15, %10\n\t"
                 "global_load_lds_dwordx4 %0, %12\n\t"
                 "s_addk_i32 m0, 1024\n\t"
                 "v_mad_u32_u16 %2, %2, %15, %9\n\t"
                 "global_load_lds_dwordx4 %1, %12\n\t"
                 "s_addk_i32 m0, 1024\n\t"
                 "v_mad_u32_u16 %3, %3, %15, %10\n\t"
                 "global_load_lds_dwordx4 %2, %12\n\t"
                 "s_addk_i32 m0, 1024\n\t"
                 "v_add_u32 %4, %4, %11\n\t"
                 "global_load_lds_dwordx4 %3, %12\n\t"
                 "s_addk_i32 m0, 1024\n\t"
                 "s_nop 0\n\t"
                 "global_load_lds_dwordx4 %4, %13\n"
                 "1:"
                 : "+v"(sx[0]), "+v"(sx[1]), "+v"(sx[2]), "+v"(sx[3]), "+v"(re), "+v"(XAf[0][0]), "+v"(XAf[0][1]),
                   "+v"(XAf[1][0]), "+v"(XAf[1][1])
                 : "v"(cx0), "v"(cx1), "v"(ce), "s"(x), "s"(ES), "s"(ring0 + ringd), "s"(128u), "s"(skip)
                 : "memory", "m0", "scc");
  };
  const unsigned g1 = __builtin_amdgcn_readfirstlane(quick && wave >= G1W ? 1u : 0u);
  gather1(g1 ^ 1u);
  if (__builtin_expect(!slow, 1)) {
    const unsigned cl = *(const LAS unsigned*)((LAS char*)smem + OFF_CNT + gb * 16 + (lane & 3) * 4);
    unsigned q = cmn + gb * LIST_BYTES;
#pragma unroll 1
    for (unsigned i = cmn; i < cmx; i += 256u) {
      const unsigned pp = i + lane * 4u;
      const unsigned ea = *(const LAS unsigned*)(size_t)(pp + gb * LIST_BYTES);
      const unsigned eb_ = *(const LAS unsigned*)(size_t)(pp + gb * LIST_BYTES + LARR);
      const unsigned long long bm = __ballot(pp < cl);
      const unsigned pa = q + 4u * __builtin_amdgcn_mbcnt_hi((unsigned)(bm >> 32), __builtin_amdgcn_mbcnt_lo((unsigned)bm, 0u));
      if (pp < cl) {
        *(LAS unsigned*)(size_t)pa = ea;
        *(LAS unsigned*)(size_t)(pa + LARR) = eb_;
      }
      q += 4u * __builtin_popcountll(bm);
    }
    pads(cnt, lane);
  }
#pragma unroll
  for (int ct = 0; ct < 2; ++ct)
#pragma unroll
    for (int m = 6; m < 15; ++m)
      if ((HELD >> m) & 1) wr[ct][m] = wl[widx(m, ct) * 64];
#pragma unroll
  for (int ct = 0; ct < 2; ++ct)
#pragma unroll
    for (int m = 6; m < 15; ++m)
      if ((HELD >> m) & 1) asm volatile("" : "+v"(wr[ct][m]));
#pragma unroll
  for (int t = 0; t < 4; ++t) fo[t] += gb * RING_SLOT;
  foe += gb * RING_SLOT;
  cS -= gb * 32 * TPITCH;
  cSt -= gb * 32 * TPITCH;
  asm volatile("s_waitcnt vmcnt(5)\n\ts_cmp_lg_u32 %7, 0\n\ts_cbranch_scc1 1f\n\ts_waitcnt vmcnt(0)\n1:\n\ts_barrier"
               : "+v"(fo[0]), "+v"(fo[1]), "+v"(fo[2]), "+v"(fo[3]), "+v"(foe), "+v"(cS), "+v"(cSt) : "s"(g1) : "memory", "scc");
  bool redo = !quick;
  gather1(__builtin_amdgcn_readfirstlane(quick ? g1 : 1u));
  const int gbin = bk * 8 + wave;
  auto rd128 = [&](unsigned a) { return *(const LAS bf16x8*)(size_t)a; };
  auto trow = [&](unsigned a, unsigned c) {
    unsigned t;
    asm("v_mad_u32_u16 %0, %1, %2, %3 op_sel:[1,0,0,0]" : "=v"(t) : "v"(a), "s"(TPITCH), "v"(c));
    return t;
  };
  auto load_S = [&](const u32x4 ss) {
    const s16x4 a0 = __builtin_amdgcn_ds_read_tr16_b64_v4i16((LAS s16x4*)(size_t)trow(ss[0], cS));
    const s16x4 a1 = __builtin_amdgcn_ds_read_tr16_b64_v4i16((LAS s16x4*)(size_t)trow(ss[1], cS));
    const s16x4 a2 = __builtin_amdgcn_ds_read_tr16_b64_v4i16((LAS s16x4*)(size_t)trow(ss[2], cS));
    const s16x4 a3 = __builtin_amdgcn_ds_read_tr16_b64_v4i16((LAS s16x4*)(size_t)trow(ss[3], cS));
    S[0] = __builtin_shufflevector(a0, a1, 0, 1, 2, 3, 4, 5, 6, 7);
    S[1] = __builtin_shufflevector(a2, a3, 0, 1, 2, 3, 4, 5, 6, 7);
  };
  auto wf = [&](int m, int ct) { return ((HELD >> m) & 1) ? wr[ct][m] : b[(m - 6) % 3]; };

  auto ew = [&](int m, const f32x16& Gc, const f32x16& Vc, const f32x16& Gn, const f32x16& Vp) {
    if (m < 2) {
      const int i = 14 + m;
      pq[i] = mulf(i, Vp[i], rq[i]);
      if (m == 1) pn[1][3] = pack_bf16(pq[14], pq[15]);
    } else if (m == 2) {
      pq[0] = mulf(0, Vc[0], rq[0]);
      pq[1] = mulf(1, Vc[1], rq[1]);
      pn[0][0] = pack_bf16(pq[0], pq[1]);
    } else {
      const int i = m - 1;
      pq[i] = mulf(i, Vc[i], rq[i]);
      if (i & 1) pn[i >> 3][(i >> 1) & 3] = pack_bf16(pq[i - 1], pq[i]);
    }
    if (m == 0) pd[0][0] = pack_bf16(rq[0], rq[1]);
    else if ((m & 1) == 0) { const int i = m + 1; pd[i >> 3][(i >> 1) & 3] = pack_bf16(rq[i - 1], rq[i]); }
    if (m < 14) {
      const int i = m + 2;
      rq[i] = __builtin_amdgcn_rcpf(add1(tq[i]));
    } else {
      rq[0] = __builtin_amdgcn_rcpf(add1(tq[0]));
      rq[1] = __builtin_amdgcn_rcpf(add1(tq[1]));
    }
    if (m < 13) tq[m + 3] = __builtin_amdgcn_exp2f(Gc[m + 3]);
    else if (m == 13) { tq[0] = __builtin_amdgcn_exp2f(Gn[0]); tq[1] = __builtin_amdgcn_exp2f(Gn[1]); }
    else tq[2] = __builtin_amdgcn_exp2f(Gn[2]);
  };

  auto drain = [&]() {
    pq[14] = mulf(0, V1[14], rq[14]);
    pq[15] = mulf(1, V1[15], rq[15]);
    pn[1][3] = pack_bf16(pq[14], pq[15]);
    Y[1] = MFMA32(__builtin_bit_cast(bf16x8, pd[0]), S[0], Y[1]);
    Y[3] = MFMA32(__builtin_bit_cast(bf16x8, pn[0]), S[0], Y[3]);
    Y[1] = MFMA32(__builtin_bit_cast(bf16x8, pd[1]), S[1], Y[1]);
    Y[3] = MFMA32(__builtin_bit_cast(bf16x8, pn[1]), S[1], Y[3]);
  };
  long long tm1 = 0, c1 = 0;
  const long long tmB = __builtin_amdgcn_s_memrealtime();
  while (true) {
    if (__builtin_expect(redo, 0)) {
      asm volatile("s_waitcnt vmcnt(0)" ::: "memory");
      if (slow) cnt = gen();
      issue_gather(0, 0);
      issue_gather(1, 1);
    }
    const int ntiles = (cnt + 31) >> 5;
    {
      const unsigned sl = *(const LAS unsigned short*)(size_t)aLS1;
      bf16x8 w0[15];
#pragma unroll
      for (int m = 6; m < 15; ++m) w0[m] = ((HELD >> m) & 1) ? wr[0][m] : wl[widx(m, 0) * 64];
      u32x4 sx = ld128(aLX + 256);
      unsigned re = *(const LAS unsigned*)(size_t)(aLE + 256);
      St[0] = rd128(sl * TPITCH + cSt);
      St[1] = rd128(sl * TPITCH + cSt + 16);
      G0 = MFMA32(St[0], XAf[0][0], zero16);
      G0 = MFMA32(St[1], XAf[1][0], G0);
      pipe_zero();
#pragma unroll
      for (int m = 6; m < 15; ++m)
        if (!((HELD >> m) & 1)) asm volatile("" : "+v"(w0[m]));
      asm volatile("s_waitcnt vmcnt(5)" : "+v"(sx), "+v"(re), "+v"(G0) :: "memory");
#pragma unroll
      for (int k = 0; k < 4; ++k) as[k] = rd128(fo[k]);
      ae = rd128(foe);
      SB();
#pragma unroll
      for (int k = 0; k < 4; ++k) G0 = MFMA32(as[k], w0[7 + k], G0);
      G0 = MFMA32(ae, w0[6], G0);
      V0 = MFMA32(as[0], w0[11], zero16);
      SB();
      glds16x(x, sx[0], cx0, ring0);
      glds16x(x, sx[1], cx1, ring0 + 1024);
      V0 = MFMA32(as[1], w0[12], V0);
      SB();
      glds16x(x, sx[2], cx0, ring0 + 2048);
      tq[0] = __builtin_amdgcn_exp2f(G0[0]);
      tq[1] = __builtin_amdgcn_exp2f(G0[1]);
      V0 = MFMA32(as[2], w0[13], V0);
      SB();
      glds16x(x, sx[3], cx1, ring0 + 3072);
      tq[2] = __builtin_amdgcn_exp2f(G0[2]);
      V0 = MFMA32(as[3], w0[14], V0);
      SB();
      glds16e(ES, re, ce, ring0 + 4096);
      rq[0] = __builtin_amdgcn_rcpf(add1(tq[0]));
      rq[1] = __builtin_amdgcn_rcpf(add1(tq[1]));
      SB();
    }
    tm1 = __builtin_amdgcn_s_memrealtime(); c1 = __builtin_amdgcn_s_memtime(); __builtin_amdgcn_s_waitcnt(0xC07F);
    unsigned &lS4 = aLS4, &lS1 = aLS1, &lX = aLX, &lE = aLE;
    int adv = 0;
    auto body = [&](const int par) {
      const unsigned lo = par * 128;
      const unsigned ro = (1 - par) * ringd;
      {
        u32x4 ss;
        unsigned sl;
#pragma unroll
        for (int m = 0; m < 15; ++m) {
          if (m == 0) Y[1] = MFMA32(__builtin_bit_cast(bf16x8, pd[0]), S[0], Y[1]);
          else if (m == 1) Y[3] = MFMA32(__builtin_bit_cast(bf16x8, pn[0]), S[0], Y[3]);
          else if (m == 2) Y[1] = MFMA32(__builtin_bit_cast(bf16x8, pd[1]), S[1], Y[1]);
          else if (m == 3) Y[3] = MFMA32(__builtin_bit_cast(bf16x8, pn[1]), S[1], Y[3]);
          else if (m == 4) G1 = MFMA32(St[0], XAf[0][1], zero16);
          else if (m == 5) G1 = MFMA32(St[1], XAf[1][1], G1);
          else if (m == 6) G1 = MFMA32(ae, wf(m, 1), G1);
          else if (m < 11) G1 = MFMA32(as[m - 7], wf(m, 1), G1);
          else if (m == 11) V1 = MFMA32(as[0], wf(m, 1), zero16);
          else V1 = MFMA32(as[m - 11], wf(m, 1), V1);
          SB();
          if (m == 0) { ss = ld128(lS4 + lo); sl = *(const LAS unsigned short*)(size_t)(lS1 + lo + 128); }
          if (m >= 3 && m < 12 && !((HELD >> (m + 3)) & 1)) b[(m - 3) % 3] = wl[widx(m + 3, 1) * 64];
          if (m == 4) load_S(ss);
          if (m == 5) { St[0] = rd128(sl * TPITCH + cSt); St[1] = rd128(sl * TPITCH + cSt + 16); }
          if (m == 6) { asm volatile("s_waitcnt vmcnt(5)" ::: "memory"); ae = rd128(foe + ro); }
          if (m >= 11) as[m - 11] = rd128(fo[m - 11] + ro);
          ew(m, G0, V0, G1, V1);
          SB();
        }
      }
      {
        u32x4 sx;
        unsigned re;
#pragma unroll
        for (int m = 0; m < 15; ++m) {
          if (m == 0) Y[0] = MFMA32(__builtin_bit_cast(bf16x8, pd[0]), S[0], Y[0]);
          else if (m == 1) Y[2] = MFMA32(__builtin_bit_cast(bf16x8, pn[0]), S[0], Y[2]);
          else if (m == 2) Y[0] = MFMA32(__builtin_bit_cast(bf16x8, pd[1]), S[1], Y[0]);
          else if (m == 3) Y[2] = MFMA32(__builtin_bit_cast(bf16x8, pn[1]), S[1], Y[2]);
          else if (m == 4) G0 = MFMA32(St[0], XAf[0][0], zero16);
          else if (m == 5) G0 = MFMA32(St[1], XAf[1][0], G0);
          else if (m == 6) G0 = MFMA32(ae, wf(m, 0), G0);
          else if (m < 11) G0 = MFMA32(as[m - 7], wf(m, 0), G0);
          else if (m == 11) V0 = MFMA32(as[0], wf(m, 0), zero16);
          else V0 = MFMA32(as[m - 11], wf(m, 0), V0);
          SB();
          if (m == 4) { sx = ld128(lX + lo + 3 * 128); re = *(const LAS unsigned*)(size_t)(lE + lo + 3 * 128); }
          if (m >= 3 && m < 12 && !((HELD >> (m + 3)) & 1)) b[(m - 3) % 3] = wl[widx(m + 3, 0) * 64];
          if (m == 7) glds16x(x, sx[0], cx0, ring0 + ro);
          if (m == 8) glds16x(x, sx[1], cx1, ring0 + ro + 1024);
          if (m == 9) glds16x(x, sx[2], cx0, ring0 + ro + 2048);
          if (m == 10) glds16x(x, sx[3], cx1, ring0 + ro + 3072);
          if (m == 11) glds16e(ES, re, ce, ring0 + ro + 4096);
          ew(m, G1, V1, G0, V0);
          SB();
        }
      }
    };
    for (int t = 0; t < ntiles; t += 2) {
      asm volatile("s_cmp_lt_u32 %0, 4\n\ts_cbranch_scc1 1f\n\ts_setprio 1\n1:" :: "s"(wave) : "scc");
      body(0);
      if (t + 1 >= ntiles) break;
      __builtin_amdgcn_s_setprio(0);
      body(1);
      lS4 += 256; lS1 += 256; lX += 256; lE += 256;
      asm volatile("" : "+v"(lS4), "+v"(lS1), "+v"(lX), "+v"(lE));
      adv += 256;
    }
    if (__builtin_expect(gs >= GEND, 1)) break;
    drain();
    lS4 -= adv; lS1 -= adv; lX -= adv; lE -= adv;
    redo = true;
  }
  const long long tm2 = __builtin_amdgcn_s_memrealtime(); const long long c2 = __builtin_amdgcn_s_memtime();

  {
    int lane;
    asm volatile("v_mbcnt_lo_u32_b32 %0, -1, 0\n\tv_mbcnt_hi_u32_b32 %0, -1, %0" : "=v"(lane));
    const int h = lane >> 5;
    const unsigned tu = lds0 + OFF_W + 18 * 1024 + h * 4096 + ((lane >> 2) & 3) * 128 + (lane & 1) * 64 +
                        ((lane >> 4) & 1) * 16 + ((lane >> 1) & 1) * 8;
    bf16x8 wu[8];
#pragma unroll
    for (int k = 0; k < 8; ++k) wu[k] = rawfrag(tu + (k & 3) * 1024 + (k >> 2) * 32);
    SB();
    drain();
    f32x16 X[2], U[2];
#pragma unroll
    for (int i = 0; i < 16; ++i) {
      const unsigned xv = __builtin_bit_cast(u32x4, as[i >> 3])[(i >> 1) & 3];
      X[0][i] = __uint_as_float(i & 1 ? xv & 0xffff0000u : xv << 16);
    }
    SB();
    asm volatile("s_nop 0\n\tv_mfma_f32_32x32x16_bf16 %0, %1, %2, %3" : "=&v"(U[0]) : "v"(wu[0]), "v"(as[0]), "v"(X[0]));
#pragma unroll
    for (int k = 1; k < 4; ++k) U[0] = MFMA32(wu[k], as[k], U[0]);
#pragma unroll
    for (int i = 0; i < 16; ++i) {
      const unsigned xv = __builtin_bit_cast(u32x4, as[2 + (i >> 3)])[(i >> 1) & 3];
      X[1][i] = __uint_as_float(i & 1 ? xv & 0xffff0000u : xv << 16);
    }
    const unsigned nd = nodeof(cg + (lane & 31));
    const bool live = (nd * Mw + Cw) >> 16 == cg + (lane & 31);
    const unsigned lm = (unsigned)__ballot(live);
    const int nodes = __builtin_popcount(lm);
    const unsigned n0 = __builtin_amdgcn_readlane(nd, __builtin_ctz(lm | 0x80000000u));
    const int r = live ? nd - n0 : 31;
    SB();
    asm volatile("s_nop 0\n\tv_mfma_f32_32x32x16_bf16 %0, %1, %2, %3" : "=&v"(U[1]) : "v"(wu[4]), "v"(as[0]), "v"(X[1]));
#pragma unroll
    for (int k = 1; k < 4; ++k) U[1] = MFMA32(wu[4 + k], as[k], U[1]);
#pragma unroll
    for (int i = 0; i < 16; ++i) Y[0][i] = __builtin_amdgcn_rcpf(Y[0][i]);
    SB();
#pragma unroll
    for (int ct = 0; ct < 2; ++ct) {
#pragma unroll
      for (int q = 0; q < 2; ++q) {
        u32x4 ov;
#pragma unroll
        for (int j = 0; j < 4; ++j) {
          const int i = 8 * q + 2 * j;
          if (ct == 0) { Y[1][i] = __builtin_amdgcn_rcpf(Y[1][i]); Y[1][i + 1] = __builtin_amdgcn_rcpf(Y[1][i + 1]); }
          const f32x2 z = __builtin_elementwise_fma(f32x2{Y[2 + ct][i], Y[2 + ct][i + 1]}, f32x2{Y[ct][i], Y[ct][i + 1]},
                                                    f32x2{U[ct][i], U[ct][i + 1]});
          ov[j] = pack_bf16(maxf(z[0], X[ct][i]), maxf(z[1], X[ct][i + 1]));
        }
        *(LAS u32x4*)(size_t)(la + r * 128 + (((4 * h + 2 * ct + q) ^ (r & 7)) * 16)) = ov;
      }
    }
    u32x4 ov[4];
#pragma unroll
    for (int q = 0; q < 4; ++q)
      ov[q] = *(const LAS u32x4*)(size_t)(la + (8 * q + (lane >> 3)) * 128 + (((lane & 7) ^ (lane >> 3)) * 16));
    asm volatile("v_cmpx_lt_i32 vcc, 0, %6\n\t"
                 "global_store_dwordx4 %0, %1, %5 sc1\n\t"
                 "v_cmpx_lt_i32 vcc, 8, %6\n\t"
                 "global_store_dwordx4 %0, %2, %5 offset:1024 sc1\n\t"
                 "v_cmpx_lt_i32 vcc, 16, %6\n\t"
                 "global_store_dwordx4 %0, %3, %5 offset:2048 sc1\n\t"
                 "v_cmpx_lt_i32 vcc, 24, %6\n\t"
                 "global_store_dwordx4 %0, %4, %5 offset:3072 sc1\n\t"
                 "s_mov_b64 exec, -1"
                 :: "v"((unsigned)n0 * 128u + lane * 16u), "v"(ov[0]), "v"(ov[1]), "v"(ov[2]), "v"(ov[3]), "s"(out),
                    "v"(nodes - (lane >> 3))
                 : "memory", "vcc");
  }
  if (bk == 0 && wave == 7) asm volatile("global_store_dwordx2 %0, %1, %2 sc1" :: "v"(0u), "v"(ep), "s"(FL) : "memory");
  asm volatile("s_waitcnt vmcnt(0)" ::: "memory");
  const long long tm3 = __builtin_amdgcn_s_memrealtime();
  if (__builtin_amdgcn_mbcnt_hi(-1, __builtin_amdgcn_mbcnt_lo(-1, 0)) == 0) { const int g = gbin; if (wave == 0) (dbg - (16 << 20))[5000 + bk] = tm0; dbg[g*8+0]=tm0; dbg[g*8+1]=tm1; dbg[g*8+2]=tm2; dbg[g*8+3]=tm3; dbg[g*8+4]=(cnt+31)>>5; dbg[g*8+5]=c2-c1; dbg[g*8+6]=KV; dbg[g*8+7]=tmB-tm0;
  }
}

__global__ void k_dbg(const long long* dbg, int ng) {
  __shared__ long long sh[4][256];
  __shared__ long long vv[2048];
  const int t = threadIdx.x;
  const long long* pd = dbg + (32u << 17);
  const long long* d2 = dbg + 4096 * 8;
  long long pmin = 1ll << 62, sum = 0, mx = 0, cn = 0;
  const int md = QSEL / 100;
  for (int g = t; g < ng; g += 256) {
    const long long* d = dbg + g * 8;
    const long long* p = pd + ((g >> 3) * 16 + (g & 7)) * 8;
    const long long* e = d2 + g * 8;
    pmin = min(pmin, d[0]);
    long long v = 0;
    const int k = QSEL % 100;
    if (k < 7) v = p[k];
    else if (k < 16) v = e[k - 10] + d[0];
    else if (k == 17) v = d[6] + d[0];
    else if (k == 18) v = d[1];
    else if (k == 19) v = d[2];
    else if (k == 20) v = d[3];
    else if (k == 21) v = e[7];
    else if (k == 22) v = d[5] * 100 / (d[2] - d[1]);
    else if (k == 23) v = d[6];
    else if (k == 16) v = d[0];
    const int w = g & 7;
    const bool use = (md == 2 || md == 4) ? w == 7 : md == 3 ? w < 7 : md == 6 ? w >= 4 : md == 7 ? w < 4 : true;
    vv[g] = v;
    if (use) { sum += v; mx = max(mx, v); ++cn; }
  }
  sh[0][t] = pmin; sh[1][t] = sum; sh[2][t] = mx; sh[3][t] = cn;
  __syncthreads();
  __shared__ long long pc;
  if (md == 5 || md == 8) {
    const int want = md == 5 ? ng * 9 / 10 : ng * 97 / 100;
    for (int g = t; g < ng; g += 256) {
      int lo = 0, eq = 0;
      for (int i = 0; i < ng; ++i) { lo += vv[i] < vv[g]; eq += vv[i] == vv[g]; }
      if (lo <= want && want < lo + eq) pc = vv[g];
    }
    __syncthreads();
  }
  if (t == 0) {
    for (int i = 1; i < 256; ++i) { pmin = min(pmin, sh[0][i]); sum += sh[1][i]; mx = max(mx, sh[2][i]); cn += sh[3][i]; }
    long long q = (md == 1 || md == 3 || md == 4) ? mx : (md == 5 || md == 8) ? pc : sum / cn;
    if (QSEL % 100 <= 20) q -= pmin;
    if (QSEL == 99) q = 0;
    q = max(0ll, min(q, 5000ll));
    const long long tend = pmin + 10000 + 4 * q;
    for (int i = 0; i < 1000000; ++i) { if (__builtin_amdgcn_s_memrealtime() >= tend) break; __builtin_amdgcn_s_sleep(1); }
  }
}

extern "C" void kernel_launch(void* const* d_in, const int* in_sizes, int n_in,
                              void* d_out, int out_size, void* d_ws, size_t ws_size,
                              hipStream_t stream) {
  const unsigned short* x = (const unsigned short*)d_in[0];
  const u32x4* eattr = (const u32x4*)d_in[1];
  const int* src = (const int*)d_in[2];
  const int* dst = (const int*)d_in[3];
  const unsigned short* wa = (const unsigned short*)d_in[4];
  const unsigned short* wb = (const unsigned short*)d_in[5];
  const unsigned short* wc = (const unsigned short*)d_in[6];
  const unsigned short* wu = (const unsigned short*)d_in[7];
  const unsigned short* wv = (const unsigned short*)d_in[8];
  unsigned short* out = (unsigned short*)d_out;
  const int N = in_sizes[0] / 64;
  const int E = in_sizes[2];
  const int chunk = (((E + NCH - 1) / NCH) + 3) & ~3;

  const bool skew = N > 4096 && N < 60000;
  const unsigned M = N > 256 ? (unsigned)(4294967295.0 / ((double)N * (skew ? 1.0 + SKEWF : 1.0))) : 0xffffffu;
  const unsigned N1 = (0x7fffffffu / M) + 1u;
  const unsigned M2 = skew ? (0xffffffffu - N1 * M) / ((unsigned)N - N1) : M;

  auto fb = [](double v) { return __builtin_bit_cast(unsigned, (float)v); };
  char* ws = (char*)d_ws;
  size_t off = 0;
  off = 1 << 20;
  unsigned long long* FL = (unsigned long long*)(ws + off);   off += 1 << 20;
  u32x4* ENT = (u32x4*)(ws + off);                       off += (size_t)2 * NBK * IBOX;
  unsigned long long* TS = (unsigned long long*)(ws + off);

  k_main<<<NBK, 512, 0, stream>>>(x, eattr, src, dst, wa, wb, wc, wu, wv, ENT, TS, FL, out, N, E, chunk, M, N1,
                                  M2 - M, fb(65536.0 / M), fb(65536.0 / M2), fb(32768.0 / M + 0.5),
                                  fb((double)N1 + (32768.0 - (double)N1 * M) / M2 + 0.5)
     , (long long*)(ws + (128u<<20))
     );
  k_dbg<<<1, 256, 0, stream>>>((const long long*)(ws + (128u<<20)), 2048);
}
```

```cpp
#define QSEL 17
#define DBGDUMP 1
#define HELD 0x5500
#include <hip/hip_runtime.h>
#include <hip/hip_bf16.h>
#include <stdint.h>

using bf16x8 = __attribute__((ext_vector_type(8))) short;
using s16x4 = __attribute__((ext_vector_type(4))) short;
using f32x16 = __attribute__((ext_vector_type(16))) float;
using f32x2 = __attribute__((ext_vector_type(2))) float;
using u32x4 = __attribute__((ext_vector_type(4))) unsigned;
using u32x2 = __attribute__((ext_vector_type(2))) unsigned;
using i32x4 = __attribute__((ext_vector_type(4))) int;
using u16x2 = __attribute__((ext_vector_type(2))) unsigned short;

#define MFMA32(a, b, c) __builtin_amdgcn_mfma_f32_32x32x16_bf16((a), (b), (c), 0, 0, 0)
#define LAS __attribute__((address_space(3)))
#define RANKOF(w) ((w) < 4 ? 7 - (w) : (w) - 4)
#define SLEEP "s_sleep 2\n\t"
#define NREP 8
#define REPOF(b) ((b) % NREP)
#define POLLAT 2
#define PRE 0
#define PREPOL "sc1"
#define G1W 4
#define DBGWAIT 0
#define XWARM 0
#define XAT 3
#define CPF 32
#define CPFB 18688
#define WEARLY 1
#define FR 0
#define XAP 0
#define TPOLL 0
#define SKEWF 0.015
#define PFAT 1
#define TW 0
#define TWN 256
#define SRCLATE 1
#define XGAT 0
#define SB() __builtin_amdgcn_sched_barrier(0)

constexpr int NCH = 256;
constexpr int NBK = 256;
constexpr int NBIN = NBK * 8;
constexpr int NFRAG = 39;
constexpr int CHMAX = 3200;
constexpr float NLOG2E = -1.44269504088896340736f;
constexpr unsigned TPITCH = 80;

__device__ __forceinline__ unsigned pack_bf16(float lo, float hi) {
  typedef __attribute__((ext_vector_type(2))) float f2;
  typedef __attribute__((ext_vector_type(2))) __bf16 b2;
  f2 v = {lo, hi};
  b2 r = __builtin_convertvector(v, b2);
  return __builtin_bit_cast(unsigned, r);
}

__device__ __forceinline__ int wave_scan(int v) {
  v += __builtin_amdgcn_update_dpp(0, v, 0x111, 0xf, 0xf, false);
  v += __builtin_amdgcn_update_dpp(0, v, 0x112, 0xf, 0xf, false);
  v += __builtin_amdgcn_update_dpp(0, v, 0x114, 0xf, 0xf, false);
  v += __builtin_amdgcn_update_dpp(0, v, 0x118, 0xf, 0xf, false);
  v += __builtin_amdgcn_update_dpp(0, v, 0x142, 0xa, 0xf, false);
  v += __builtin_amdgcn_update_dpp(0, v, 0x143, 0xc, 0xf, false);
  return v;
}

__device__ __forceinline__ void glds16(const void* sbase, unsigned voff, unsigned lds_dst) {
  asm volatile(
      "s_mov_b32 m0, %2\n\t"
      "s_nop 0\n\t"
      "global_load_lds_dwordx4 %0, %1"
      :: "v"(voff), "s"(sbase), "s"(lds_dst) : "memory", "m0");
}
__device__ __forceinline__ void glds16nt(const void* sbase, unsigned voff, unsigned lds_dst) {
  asm volatile(
      "s_mov_b32 m0, %2\n\t"
      "s_nop 0\n\t"
      "global_load_lds_dwordx4 %0, %1 nt"
      :: "v"(voff), "s"(sbase), "s"(lds_dst) : "memory", "m0");
}
__device__ __forceinline__ void glds16x(const void* sbase, unsigned a, unsigned c, unsigned lds_dst) {
  unsigned tmp;
  asm volatile(
      "s_mov_b32 m0, %4\n\t"
      "v_mad_u32_u16 %0, %1, %5, %2\n\t"
      "global_load_lds_dwordx4 %0, %3"
      : "=&v"(tmp) : "v"(a), "v"(c), "s"(sbase), "s"(lds_dst), "s"(128u) : "memory", "m0");
}
__device__ __forceinline__ void glds16e(const void* sbase, unsigned a, unsigned c, unsigned lds_dst) {
  unsigned tmp;
  asm volatile(
      "s_mov_b32 m0, %4\n\t"
      "v_add_u32 %0, %1, %2\n\t"
      "global_load_lds_dwordx4 %0, %3"
      : "=&v"(tmp) : "v"(a), "v"(c), "s"(sbase), "s"(lds_dst) : "memory", "m0");
}
__device__ __forceinline__ int gload32(const void* sbase, unsigned voff) {
  int r;
  asm volatile("global_load_dword %0, %1, %2" : "=v"(r) : "v"(voff), "s"(sbase) : "memory");
  return r;
}
__device__ __forceinline__ u32x4 gload128(const void* sbase, unsigned voff) {
  u32x4 r;
  asm volatile("global_load_dwordx4 %0, %1, %2" : "=v"(r) : "v"(voff), "s"(sbase) : "memory");
  return r;
}

__device__ __forceinline__ u32x4 gload128nt(const void* sbase, unsigned voff) {
  u32x4 r;
  asm volatile("global_load_dwordx4 %0, %1, %2 nt" : "=v"(r) : "v"(voff), "s"(sbase) : "memory");
  return r;
}
__device__ __forceinline__ void gstore128wt(void* sbase, unsigned voff, u32x4 v) {
  asm volatile("global_store_dwordx4 %0, %1, %2 sc1\n\ts_nop 1" :: "v"(voff), "v"(v), "s"(sbase) : "memory");
}

constexpr int IBOX = 32768;
constexpr int IEXT = 256 * IBOX;
constexpr int CPIT = 208;
constexpr unsigned RINC = 87382;
constexpr int KP_HDR = 0;
constexpr int KP_ENT = KP_HDR + NBK * 4;
constexpr int KP_POS = KP_ENT + NBK * CPIT;
constexpr int KP_LDS = KP_POS + NBK * 4;
constexpr int NWL = 26;
constexpr int NBLOB = 31;

__device__ __forceinline__ unsigned mad24(unsigned a, int b, unsigned c) {
  unsigned r;
  asm("v_mad_i32_i24 %0, %1, %2, %3" : "=v"(r) : "v"(a), "s"(b), "v"(c));
  return r;
}
__device__ __forceinline__ unsigned mul24(unsigned a, unsigned b) {
  unsigned r;
  asm("v_mul_u32_u24 %0, %1, %2" : "=v"(r) : "n"(b), "v"(a));
  return r;
}
__device__ __forceinline__ unsigned mul24s(unsigned a, unsigned b) {
  unsigned r;
  asm("v_mul_u32_u24 %0, %1, %2" : "=v"(r) : "s"(b), "v"(a));
  return r;
}
__device__ __forceinline__ unsigned byte3x(unsigned a, unsigned b) {
  unsigned r;
  asm("v_mul_u32_u24_sdwa %0, %1, %2 dst_sel:DWORD dst_unused:UNUSED_PAD src0_sel:DWORD src1_sel:BYTE_3" : "=v"(r) : "s"(b), "v"(a));
  return r;
}
__device__ __forceinline__ unsigned mulhi24(unsigned a, unsigned b) {
  return (unsigned)(((unsigned long long)(a & 0xffffffu) * (unsigned long long)b) >> 32);
}
__device__ __forceinline__ unsigned dot4(unsigned a, unsigned b, unsigned c) {
  return __builtin_amdgcn_udot4(a, b, c, false);
}

constexpr int RING_SLOT = 5 * 1024;
constexpr int LCAP = 512;
constexpr int LARR = 2560;
constexpr int LIST_BYTES = 2 * LARR;
constexpr int OFF_CNT = 0;
constexpr int OFF_XF = 256;
constexpr int OFF_W = 512;
constexpr int OFF_T1 = OFF_W + NWL * 1024;
constexpr int OFF_T2 = OFF_T1 + 32 * TPITCH;
constexpr int OFF_R0 = OFF_W + NFRAG * 1024;
constexpr int OFF_R1 = OFF_R0 + 8 * RING_SLOT;
constexpr int OFF_L = OFF_R1 + 8 * RING_SLOT;
constexpr int MAIN_LDS = OFF_L + 8 * LIST_BYTES;
static_assert(MAIN_LDS <= 160 * 1024, "LDS");
static_assert(KP_LDS <= 16 * RING_SLOT, "phase 1 lives in the ring");

__device__ __forceinline__ unsigned ldd(const unsigned* p) {
  return __hip_atomic_load(p, __ATOMIC_RELAXED, __HIP_MEMORY_SCOPE_AGENT);
}
__device__ __forceinline__ unsigned long long ldq(const unsigned long long* p) {
  return __hip_atomic_load(p, __ATOMIC_RELAXED, __HIP_MEMORY_SCOPE_AGENT);
}
__device__ __forceinline__ bf16x8 rawfrag(unsigned a) {
  const s16x4 a0 = __builtin_amdgcn_ds_read_tr16_b64_v4i16((LAS s16x4*)(size_t)a);
  const s16x4 a1 = __builtin_amdgcn_ds_read_tr16_b64_v4i16((LAS s16x4*)(size_t)(a + 512));
  return __builtin_shufflevector(a0, a1, 0, 1, 2, 3, 4, 5, 6, 7);
}
__device__ __forceinline__ float maxf(float a, float b) {
  float r;
  asm("v_max_f32 %0, %1, %2" : "=v"(r) : "v"(a), "v"(b));
  return r;
}
__device__ __forceinline__ float add1(float t) { return 1.0f + t; }
__device__ __forceinline__ float mulf(int i, float a, float b) { return (i & 1) ? __builtin_fmaf(a, b, 0.0f) : a * b; }

__global__ __launch_bounds__(512, 2) void k_main(const unsigned short* __restrict__ x,
                                                 const u32x4* __restrict__ ES,
                                                 const int* __restrict__ src, const int* __restrict__ dst,
                                                 const unsigned short* __restrict__ wa_g,
                                                 const unsigned short* __restrict__ wb_g,
                                                 const unsigned short* __restrict__ wc_g,
                                                 const unsigned short* __restrict__ wu_g,
                                                 const unsigned short* __restrict__ wv_g,
                                                 u32x4* ENT, unsigned long long* TS, unsigned long long* FL,
                                                 unsigned short* __restrict__ out, int N, int E, int chunk,
                                                 unsigned M, unsigned N1, unsigned DM, unsigned MF0, unsigned MF1,
                                                 unsigned MB0, unsigned MB1
                                                 , long long* dbg
                                                 ) {
  const long long tm0 = __builtin_amdgcn_s_memrealtime(); __builtin_amdgcn_sched_barrier(0);
#define PT(i)
  PT(0);
  __shared__ __attribute__((aligned(16))) char smem[MAIN_LDS];
  const int tid0 = threadIdx.x;
  const int wave = __builtin_amdgcn_readfirstlane(tid0 >> 6);
  const int bk = blockIdx.x;
  const unsigned lds0 = (unsigned)(size_t)(LAS char*)smem;

  LAS char* sm = (LAS char*)smem + OFF_R0;
  const unsigned sm0 = lds0 + OFF_R0;
  const int tid = tid0, lane = tid & 63, r = lane & 31, h = lane >> 5;
  const unsigned pc = tid & 7, bq = tid >> 3;
  unsigned ia = sm0 + KP_ENT + bq * CPIT + pc * 16;
#pragma unroll
  for (int s = 0; s < 4; ++s) *(LAS u32x4*)(size_t)(ia + s * 64 * CPIT) = u32x4{~0u, ~0u, ~0u, ~0u};
  *(LAS unsigned*)(sm + KP_HDR + (tid & 255) * 4) = 0u;
  int q0 = 4 * tid + 3, q1 = 4 * tid + 1795;
  unsigned a0 = tid * 16, a1 = tid * 16 + 7168, z0 = 0u;
  unsigned long long m7 = __ballot(tid == 448);
  int n7 = wave == 7 ? 0 : 0x7fffffff;
  unsigned f0 = lane * 128u, f1 = lane * 128u + 8192u, f2 = min(lane * 128u + 16384u, CPFB - 512u);
  unsigned long long p7 = __ballot((wave | (bk & -CPF)) == 7);
  asm volatile("" : "+v"(f0), "+v"(f1), "+v"(f2), "+s"(p7));
  asm volatile("" : "+v"(ia), "+v"(q0), "+v"(q1), "+v"(a0), "+v"(a1), "+v"(z0), "+s"(m7), "+s"(n7) :: "memory");
  SB();
  asm volatile("" :: "s"(x), "s"(ES), "s"(src), "s"(dst), "s"(wa_g), "s"(wb_g), "s"(wc_g), "s"(wu_g), "s"(wv_g), "s"(ENT),
               "s"(TS), "s"(FL), "s"(out), "s"(N), "s"(E), "s"(chunk), "s"(M), "s"(N1), "s"(DM), "s"(MF0), "s"(MF1), "s"(MB0), "s"(MB1));
  const int base = bk * chunk;
  const int ecnt = max(min(chunk, E - base), 0);
  unsigned long long on[2];
  on[0] = __ballot(q0 < min(min(chunk, E - base), n7));
  on[1] = __ballot(q1 < min(min(chunk, E - base), n7));
  a0 += base * 4u;
  a1 += base * 4u;
  u32x4 d[2], sv[2];
  u32x2 e0;
  asm volatile("s_mov_b64 exec, %11\n\t"
               "global_load_dwordx4 %0, %5, %8 nt\n\t"
               "s_mov_b64 exec, %12\n\t"
               "global_load_dwordx4 %1, %6, %8 nt\n\t"
               "s_mov_b64 exec, %13\n\t"
               "global_load_dwordx2 %2, %7, %9 sc1\n\t"
               "s_getpc_b64 s[96:97]\n\t"
               "s_mov_b32 m0, %17\n\t"
               "s_mov_b64 exec, %18\n\t"
               "global_load_lds_dword %14, s[96:97]\n\t"
               "global_load_lds_dword %15, s[96:97]\n\t"
               "global_load_lds_dword %16, s[96:97]\n\t"
               "s_mov_b64 exec, %11\n\t"
               "s_waitcnt lgkmcnt(0)\n\t"
               "s_barrier\n\t"
               "global_load_dwordx4 %3, %5, %10 nt\n\t"
               "s_mov_b64 exec, %12\n\t"
               "global_load_dwordx4 %4, %6, %10 nt\n\t"
               "s_mov_b64 exec, -1"
               : "=&{v[240:243]}"(d[0]), "=&{v[244:247]}"(d[1]), "=&{v[238:239]}"(e0), "=&{v[248:251]}"(sv[0]),
                 "=&{v[252:255]}"(sv[1])
               : "v"(a0), "v"(a1), "v"(z0), "s"(dst), "s"(FL), "s"(src), "s"(on[0]), "s"(on[1]), "s"(m7)
                 , "v"(f0), "v"(f1), "v"(f2), "n"(OFF_L + 7 * LIST_BYTES + 4096), "s"(p7)
               : "memory", "m0", "s96", "s97");
  SB();
  const int bu = (bk >> 1) | ((bk & 1) << 7);
  const unsigned Mw = bk & 1 ? M + DM : M, Cw = bk & 1 ? 0u - N1 * DM : 0u;
  unsigned MF = bk & 1 ? MF1 : MF0, MB = bk & 1 ? MB1 : MB0;
  asm volatile("" : "+s"(MF), "+s"(MB));
  char* ib = reinterpret_cast<char*>(ENT) + (size_t)bu * IBOX;
  auto nodeof = [&](unsigned c) { return min((unsigned)__builtin_fmaf((float)c, __uint_as_float(MF), __uint_as_float(MB)), (unsigned)N - 1u); };
  unsigned tup[8], val[8], pm[8];
  u32x4 xg[4];
  const unsigned xn0 = __builtin_amdgcn_readfirstlane(nodeof((bu * 8 + wave) * 32));
  unsigned xd[4];
  auto xprep = [&]() {
#pragma unroll
    for (int i = 0; i < 4; ++i)
      xd[i] = min(xn0 + 8 * i + (lane >> 3), N - 1u) * 128u + ((((lane & 7) ^ (lane >> 4)) * 16) ^ (i & 1) * 64) - i * 1024u;
    asm volatile("" : "+v"(xd[0]), "+v"(xd[1]), "+v"(xd[2]), "+v"(xd[3]));
  };
  auto xdma = [&]() {
    asm volatile("s_mov_b32 m0, %5\n\ts_nop 0\n\t"
                 "global_load_lds_dwordx4 %0, %4\n\t"
                 "global_load_lds_dwordx4 %1, %4 offset:1024\n\t"
                 "global_load_lds_dwordx4 %2, %4 offset:2048\n\t"
                 "global_load_lds_dwordx4 %3, %4 offset:3072"
                 :: "v"(xd[0]), "v"(xd[1]), "v"(xd[2]), "v"(xd[3]), "s"(x), "s"(lds0 + OFF_L + wave * LIST_BYTES)
                 : "memory", "m0");
  };
  auto wload = [&](const unsigned short* w, int f) {
    asm volatile("s_mov_b32 m0, %3\n\ts_nop 0\n\t"
                 "global_load_lds_dwordx4 %0, %2\n\t"
                 "global_load_lds_dwordx4 %0, %2 offset:1024\n\t"
                 "global_load_lds_dwordx4 %0, %2 offset:2048\n\t"
                 "global_load_lds_dwordx4 %0, %2 offset:3072\n\t"
                 "s_addk_i32 m0, 4096\n\ts_nop 0\n\t"
                 "global_load_lds_dwordx4 %1, %2\n\t"
                 "global_load_lds_dwordx4 %1, %2 offset:1024\n\t"
                 "global_load_lds_dwordx4 %1, %2 offset:2048\n\t"
                 "global_load_lds_dwordx4 %1, %2 offset:3072"
                 :: "v"(lane * 16u), "v"(lane * 16u + 4096u), "s"(w), "s"(lds0 + OFF_W + f * 1024) : "memory", "m0", "scc");
  };
  auto wall = [&]() {
    wload(wa_g, NBLOB);
    wload(wb_g, 0);
    wload(wv_g, 8);
    glds16(wc_g, lane * 16, lds0 + OFF_W + 16 * 1024);
    glds16(wc_g + 512, lane * 16, lds0 + OFF_W + 17 * 1024);
  };
  int spin = 1 << 20;
  u32x2 ep = {0u, 0u};
  if (wave == 7) {
    wall();
    auto touch = [&](const void* p, unsigned stride) {
      asm volatile("s_mov_b32 m0, %2\n\ts_mov_b64 exec, %3\n\tglobal_load_lds_dword %0, %1 sc1\n\ts_mov_b64 exec, -1"
                   :: "v"(lane * stride), "s"(p), "n"(OFF_L + 7 * LIST_BYTES + 4096), "s"(__ballot(bk < TWN)) : "memory", "m0");
    };
    if (TW & 4) touch(ENT, IEXT / 64);
    if (TW & 2) touch(x, (unsigned)(N / 64) * 128u);
    if (TW & 1) touch(ES, (unsigned)(E / 64) * 32u);
    if (TW & 8) touch(out, (unsigned)(N / 64) * 128u);
    *(LAS u32x4*)((LAS char*)smem + OFF_CNT + r * 16) =
        r < 8 ? u32x4{lds0 + OFF_L, lds0 + OFF_L + 4, lds0 + OFF_L + 8, lds0 + OFF_L + 12} : u32x4{0u, 0u, 0u, 0u};
    {
      const unsigned col = h ? ((r >> 1) & 12u) | ((r << 2) & 16u) | (r & 3u) : r;
      LAS char* tr = (LAS char*)smem + OFF_T1 + lane * TPITCH;
#pragma unroll
      for (int j = 0; j < 4; ++j) *(LAS u32x4*)(tr + j * 16) = u32x4{0u, 0u, 0u, 0u};
      *(LAS unsigned short*)(tr + col * 2) = 0x3F80;
    }
#pragma unroll
    for (int i = 0; i < 8; ++i) { tup[i] = 0u; val[i] = 0u; pm[i] = 0u; }
    asm volatile("s_waitcnt vmcnt(%1)" : "+{v[238:239]}"(e0) : "n"((WEARLY ? 26 : 0) + (PFAT ? 5 : 0) + __builtin_popcount(TW)) : "memory");
    ep[0] = __builtin_amdgcn_readfirstlane(e0[0]) + 0x9E3779B1u;
    ep[1] = __builtin_amdgcn_readfirstlane(e0[1]) + 0x7F4A7C15u;
    *(LAS u32x2*)((LAS char*)smem + OFF_CNT + 168) = u32x2{ep[0], ep[1] >> 24};
    if (__builtin_expect(ecnt & 3, 0)) {
      const int i = (ecnt & ~3) + lane;
      on[0] = __ballot(i < ecnt);
      if (i < ecnt) {
        pm[0] = (unsigned)dst[base + i] * M + (unsigned)max(dst[base + i] - (int)N1, 0) * DM;
        tup[0] = (unsigned)src[base + i] | (unsigned)i << 16;
        val[0] = __hip_atomic_fetch_add((LAS unsigned*)(sm + KP_HDR) + (pm[0] >> 24), RINC, __ATOMIC_RELAXED, __HIP_MEMORY_SCOPE_WORKGROUP);
        const unsigned u = min(val[0] >> 16, 48u);
        *(LAS unsigned*)(sm + KP_ENT + (pm[0] >> 24) * CPIT + u * 4) = tup[0];
        *(LAS unsigned char*)(sm + KP_ENT + (pm[0] >> 24) * CPIT + ((u * 4) | (u & 3) | 12)) = pm[0] >> 16;
      }
      __builtin_amdgcn_s_waitcnt(0x0F70);
    }
    PT(1);
    xprep();
    xdma();
  } else {
    xprep();
    unsigned inc = RINC, t0 = (unsigned)tid << 18;
    asm volatile("s_waitcnt vmcnt(2)" : "+{v[240:243]}"(d[0]), "+{v[244:247]}"(d[1]), "+v"(inc), "+v"(t0) :: "memory");
    PT(1);
    unsigned cb[8];
#pragma unroll
    for (int s = 0; s < 2; ++s) {
      unsigned* rr = val + 4 * s;
#pragma unroll
      for (int k = 0; k < 4; ++k) {
        unsigned t;
        asm("v_sub_u32 %0, %1, %2 clamp" : "=v"(t) : "v"(d[s][k]), "s"(N1));
        asm("v_mad_u32_u24 %0, %1, %2, %3" : "=v"(pm[4 * s + k]) : "v"(t), "s"(DM), "v"(mul24s(d[s][k], M)));
      }
#pragma unroll
      for (int k = 0; k < 4; ++k) rr[k] = byte3x(pm[4 * s + k], 4u);
      asm volatile(
          "s_mov_b64 exec, %5\n\t"
          "ds_add_rtn_u32 %0, %0, %4 offset:%6\n\t"
          "ds_add_rtn_u32 %1, %1, %4 offset:%6\n\t"
          "ds_add_rtn_u32 %2, %2, %4 offset:%6\n\t"
          "ds_add_rtn_u32 %3, %3, %4 offset:%6\n\t"
          "s_mov_b64 exec, -1"
          : "+v"(rr[0]), "+v"(rr[1]), "+v"(rr[2]), "+v"(rr[3])
          : "v"(inc), "s"(on[s]), "n"(OFF_R0 + KP_HDR)
          : "memory");
    }
#pragma unroll
    for (int i = 0; i < 8; ++i) cb[i] = byte3x(pm[i], (unsigned)CPIT);
    asm volatile("s_waitcnt vmcnt(0)"
                 : "+{v[248:251]}"(sv[0]), "+{v[252:255]}"(sv[1]), "+v"(cb[0]), "+v"(cb[1]), "+v"(cb[2]), "+v"(cb[3]), "+v"(cb[4]), "+v"(cb[5]),
                   "+v"(cb[6]), "+v"(cb[7])
                 :: "memory");
    xdma();
#pragma unroll
    for (int i = 0; i < 8; ++i) tup[i] = sv[i >> 2][i & 3] + t0 + (((i >> 2) * 1792u + (i & 3)) << 16);
#pragma unroll
    for (int s = 0; s < 2; ++s) {
      asm volatile("s_waitcnt lgkmcnt(%8)"
                   : "+v"(val[4 * s]), "+v"(val[4 * s + 1]), "+v"(val[4 * s + 2]), "+v"(val[4 * s + 3]), "+v"(tup[4 * s]),
                     "+v"(tup[4 * s + 1]), "+v"(tup[4 * s + 2]), "+v"(tup[4 * s + 3])
                   : "n"(DBGWAIT ? 0 : s ? 8 : 4) : "memory");
      unsigned pa[4], pb[4];
#pragma unroll
      for (int k = 0; k < 4; ++k) {
        const unsigned u = min(val[4 * s + k], 48u << 16);
        asm("v_mad_u32_u16 %0, %1, 4, %2 op_sel:[1,0,0,0]" : "=v"(pa[k]) : "v"(u), "v"(cb[4 * s + k]));
        pb[k] = pa[k] | ((u >> 16) & 3u) | 12u;
      }
      asm volatile(
          "s_mov_b64 exec, %16\n\t"
          "ds_write_b32 %0, %8 offset:%17\n\t"
          "ds_write_b8_d16_hi %4, %12 offset:%17\n\t"
          "ds_write_b32 %1, %9 offset:%17\n\t"
          "ds_write_b8_d16_hi %5, %13 offset:%17\n\t"
          "ds_write_b32 %2, %10 offset:%17\n\t"
          "ds_write_b8_d16_hi %6, %14 offset:%17\n\t"
          "ds_write_b32 %3, %11 offset:%17\n\t"
          "ds_write_b8_d16_hi %7, %15 offset:%17\n\t"
          "s_mov_b64 exec, -1"
          :: "v"(pa[0]), "v"(pa[1]), "v"(pa[2]), "v"(pa[3]), "v"(pb[0]), "v"(pb[1]), "v"(pb[2]), "v"(pb[3]),
             "v"(tup[4 * s]), "v"(tup[4 * s + 1]), "v"(tup[4 * s + 2]), "v"(tup[4 * s + 3]), "v"(pm[4 * s]),
             "v"(pm[4 * s + 1]), "v"(pm[4 * s + 2]), "v"(pm[4 * s + 3]), "s"(on[s]), "n"(OFF_R0 + KP_ENT)
          : "memory");
    }
  }
  PT(2);
  u32x2 tag;
  {
    unsigned ha = sm0 + KP_HDR + bq * 4, hb = sm0 + KP_HDR + lane * 16;
    unsigned go = bq * IBOX + bk * 128 + pc * 16;
    asm volatile("s_waitcnt lgkmcnt(0)\n\ts_barrier" : "+v"(ha), "+v"(hb), "+v"(go) :: "memory");
    PT(3);
    tag = *(const LAS u32x2*)((LAS char*)smem + OFF_CNT + 168);
    const u32x4 call = *(const LAS u32x4*)(size_t)hb;
    unsigned cn[4];
    u32x4 v[4];
#pragma unroll
    for (int s = 0; s < 4; ++s) {
      cn[s] = *(const LAS unsigned*)(size_t)(ha + s * 256);
      v[s] = *(const LAS u32x4*)(size_t)(ia + s * 64 * CPIT);
    }
    asm volatile("" : "+v"(v[0]), "+v"(v[1]), "+v"(v[2]), "+v"(v[3]), "+v"(tag));
    if (__builtin_expect(__ballot(max(max(call[0], call[1]), max(call[2], call[3])) > 36u * RINC) != 0ull, 0)) {
      LAS unsigned* ovc = (LAS unsigned*)((LAS char*)smem + OFF_CNT + 164);
      LAS unsigned* pos = (LAS unsigned*)(sm + KP_POS);
      if (tid < NBK) {
        const unsigned u = *(const LAS unsigned*)(sm + KP_HDR + tid * 4) >> 16, n = u - (u >> 2);
        if (n > 36u) {
          const unsigned p = (unsigned)base + __hip_atomic_fetch_add(ovc, n - 36u, __ATOMIC_RELAXED, __HIP_MEMORY_SCOPE_WORKGROUP);
          pos[tid] = p;
          __hip_atomic_store(FL + 512 + tid * NCH + bk, ((unsigned long long)(n - 36u) << 32) | p, __ATOMIC_RELAXED,
                             __HIP_MEMORY_SCOPE_AGENT);
        }
      }
      asm volatile("s_waitcnt lgkmcnt(0)\n\ts_barrier" ::: "memory");
#pragma unroll
      for (int i = 0; i < 8; ++i) {
        const unsigned u = val[i] >> 16, n = u - (u >> 2);
        if (((on[i >> 2] >> lane) & 1ull) && n >= 36u)
          __hip_atomic_store(TS + pos[pm[i] >> 24] + (n - 36u), ((unsigned long long)((pm[i] >> 16) & 0xffu) << 32) | tup[i],
                             __ATOMIC_RELAXED, __HIP_MEMORY_SCOPE_AGENT);
      }
      __builtin_amdgcn_s_waitcnt(0x0F70);
      asm volatile("s_waitcnt vmcnt(0)\n\ts_barrier" ::: "memory");
    }
    auto seal = [&](u32x4& w, bool more) {
      w[3] = __builtin_amdgcn_perm(tag[1], w[3], 0x04020100u);
      unsigned sm_ = w[0] + w[1] + w[2] + w[3] + tag[0];
      sm_ += __builtin_amdgcn_update_dpp(0u, sm_, 0xB1, 0xf, 0xf, false);
      sm_ += __builtin_amdgcn_update_dpp(0u, sm_, 0x4E, 0xf, 0xf, false);
      sm_ += __builtin_amdgcn_update_dpp(0u, sm_, 0x141, 0xf, 0xf, false);
      w[2] = pc == 7u ? w[2] - sm_ + (more ? 1u : 0u) : w[2];
    };
#pragma unroll
    for (int s = 0; s < 4; ++s) seal(v[s], cn[s] > 23u * RINC);
    asm volatile("global_store_dwordx4 %0, %1, %5 sc1\n\t"
                 "global_store_dwordx4 %0, %2, %6 sc1\n\t"
                 "global_store_dwordx4 %0, %3, %7 sc1\n\t"
                 "global_store_dwordx4 %0, %4, %8 sc1\n\t"
                 "s_nop 1"
                 :: "v"(go), "v"(v[0]), "v"(v[1]), "v"(v[2]), "v"(v[3]), "s"(ENT), "s"(ENT + 4 * IBOX), "s"(ENT + 8 * IBOX),
                    "s"(ENT + 12 * IBOX)
                 : "memory");
    PT(4);
    if (__builtin_expect(__ballot(max(max(cn[0], cn[1]), max(cn[2], cn[3])) > 23u * RINC) != 0ull, 0)) {
#pragma unroll 1
      for (int s = 0; s < 4; ++s) {
        const unsigned c = *(const LAS unsigned*)(size_t)(ha + s * 256);
        u32x4 w = *(const LAS u32x4*)(size_t)(ia + s * 64 * CPIT + (pc < 4u ? 128 : 0));
        if (pc == 7u) w = u32x4{w[2], ~0u, ~0u, w[3] >> 16};
        else {
#pragma unroll
          for (int j = 0; j < 3; ++j) w[j] = pc < 4u && 32u + 4u * pc + j < (c >> 16) ? w[j] : ~0u;
        }
        seal(w, c > 36u * RINC);
        if (c > 23u * RINC) gstore128wt(ENT, go + s * (64 * IBOX) + IEXT, w);
      }
    }
    PT(5);
  }
  u32x4 cv[4];
  unsigned vo[4], eb[4];
  unsigned long long pend[4];
#pragma unroll
  for (int k = 0; k < 4; ++k) {
    vo[k] = wave * 4096u + k * 1024u + lane * 16u;
    eb[k] = (unsigned)((wave * 32 + k * 8 + (lane >> 3)) * chunk) * 32u;
  }
#define POLL0()                                                                                                       \
  asm volatile("global_load_dwordx4 %0, %4, %8 sc1\n\t"                                                               \
               "global_load_dwordx4 %1, %5, %8 sc1\n\t"                                                               \
               "global_load_dwordx4 %2, %6, %8 sc1\n\t"                                                               \
               "global_load_dwordx4 %3, %7, %8 sc1"                                                                   \
               : "=&{v[240:243]}"(cv[0]), "=&{v[244:247]}"(cv[1]), "=&{v[248:251]}"(cv[2]), "=&{v[252:255]}"(cv[3])   \
               : "v"(vo[0]), "v"(vo[1]), "v"(vo[2]), "v"(vo[3]), "s"(ib)                                              \
               : "memory")
  const LAS bf16x8* wl = (const LAS bf16x8*)(smem + OFF_W) + lane;
  const f32x16 zero16 = {0.f, 0.f, 0.f, 0.f, 0.f, 0.f, 0.f, 0.f, 0.f, 0.f, 0.f, 0.f, 0.f, 0.f, 0.f, 0.f};
  auto widx = [&](int m, int ct) { return m == 6 ? 16 + ct : m < 11 ? m - 7 + 4 * ct : 8 + m - 11 + 4 * ct; };
  bf16x8 wr[2][15];
  const unsigned to = lds0 + OFF_W + ((lane >> 2) & 3) * 128 + (lane & 1) * 64 + ((lane >> 4) & 1) * 16 + ((lane >> 1) & 1) * 8;
  auto xa = [&]() {
    bf16x8 wa[8];
#pragma unroll
    for (int k = 0; k < 8; ++k) wa[k] = rawfrag(to + (NBLOB + (k >> 1)) * 1024 + h * 4096 + (k & 1) * 32);
    asm volatile("" : "+v"(wa[0]), "+v"(wa[1]), "+v"(wa[2]), "+v"(wa[3]), "+v"(wa[4]), "+v"(wa[5]), "+v"(wa[6]), "+v"(wa[7]),
                      "+{v[160:163]}"(xg[0]), "+{v[164:167]}"(xg[1]), "+{v[168:171]}"(xg[2]), "+{v[172:175]}"(xg[3]));
    f32x16 XA[2] = {zero16, zero16};
#pragma unroll
    for (int k = 0; k < 4; ++k)
#pragma unroll
      for (int ct = 0; ct < 2; ++ct) XA[ct] = MFMA32(__builtin_bit_cast(bf16x8, xg[k]), wa[k * 2 + ct], XA[ct]);
#pragma unroll
    for (int q = 0; q < 4; ++q) {
      u32x4 xp;
#pragma unroll
      for (int j = 0; j < 4; ++j)
        xp[j] = pack_bf16(XA[q & 1][8 * (q >> 1) + 2 * j] * NLOG2E, XA[q & 1][8 * (q >> 1) + 2 * j + 1] * NLOG2E);
      *(LAS u32x4*)((LAS char*)smem + OFF_R1 + wave * RING_SLOT + q * 1024 + lane * 16) = xp;
    }
  };
  {
    auto prep = [&](unsigned f, unsigned hs, float scale) {
      u32x4 w[2];
#pragma unroll
      for (int ct = 0; ct < 2; ++ct) w[ct] = __builtin_bit_cast(u32x4, rawfrag(to + f + h * hs + ct * 32));
      asm volatile("s_waitcnt lgkmcnt(0)" : "+v"(w[0]), "+v"(w[1]) :: "memory");
#pragma unroll
      for (int ct = 0; ct < 2; ++ct) {
#pragma unroll
        for (int j = 0; j < 4; ++j)
          w[ct][j] = pack_bf16(__uint_as_float(w[ct][j] << 16) * scale, __uint_as_float(w[ct][j] & 0xffff0000u) * scale);
        *(LAS u32x4*)((LAS char*)smem + OFF_W + f + ct * hs + lane * 16) = w[ct];
      }
    };
    {
      const unsigned xr = min(nodeof((bu * 8 + wave) * 32 + r) - xn0, 31u);
      unsigned xa_ = lds0 + OFF_L + wave * LIST_BYTES + xr * 128 + (((4 * h) ^ ((xr >> 1) & 7)) * 16);
      asm volatile("s_waitcnt vmcnt(4)" : "+v"(xa_) :: "memory");
#pragma unroll
      for (int t = 0; t < 4; ++t) xg[t] = *(const LAS u32x4*)(size_t)(xa_ ^ (t * 16));
      asm volatile("" : "+v"(xg[0]), "+v"(xg[1]), "+v"(xg[2]), "+v"(xg[3]) :: "memory");
      *(LAS unsigned*)((LAS char*)smem + OFF_L + wave * LIST_BYTES + h * LARR + r * 4) = ~0u;
    }
    asm volatile("s_waitcnt vmcnt(4) lgkmcnt(0)\n\ts_barrier"
                 : "+v"(vo[0]), "+v"(vo[1]), "+v"(vo[2]), "+v"(vo[3]), "+v"(eb[0]), "+v"(eb[1]), "+v"(eb[2]), "+v"(eb[3])
                 :: "memory");
    xa();
    {
      unsigned f = (wave + (wave & 4)) * 1024, hs = 4096;
      const float scale = wave < 4 ? NLOG2E : 1.0f;
#pragma unroll 1
      while (true) {
        asm volatile("" : "+s"(f), "+s"(hs));
        prep(f, hs, scale);
        if (f != 0u) break;
        f = 16 * 1024;
        hs = 1024;
      }
    }
    PT(6);
    POLL0();
    glds16(wu_g + wave * 512, lane * 16, lds0 + OFF_W + (18 + wave) * 1024);
  }
  const unsigned l0 = lds0 + OFF_L;
  unsigned aLX = l0 + (lane >> 3) * 16;
  unsigned aLE = l0 + LARR + ((((lane >> 1) & 7) << 2) | (lane >> 4)) * 4;
  unsigned aLS4 = l0 + (4 * h + ((lane >> 2) & 3)) * 16;
  unsigned aLS1 = l0 + (((r & 7) << 2) | (r >> 3)) * 4 + 2;
  unsigned cx0 = (unsigned)(((lane & 7) ^ (lane >> 4)) * 16);
  unsigned cx1 = cx0 ^ 64u;
  unsigned ce = (unsigned)((((lane & 1) ^ ((lane >> 4) & 1))) * 16);
  const unsigned ringd = 8 * RING_SLOT;
  unsigned cS = lds0 + OFF_T1 + (((lane >> 4) & 1) * 16 + (lane & 3) * 4) * 2;
  unsigned cSt = lds0 + OFF_T2 + h * 32;
  unsigned fo[4];
#pragma unroll
  for (int t = 0; t < 4; ++t) fo[t] = lds0 + OFF_R0 + (unsigned)(r * 128 + (((4 * h + t) ^ ((r >> 1) & 7)) * 16));
  unsigned foe = lds0 + OFF_R0 + (unsigned)(4096 + r * 32 + ((h ^ ((r >> 3) & 1)) * 16));
  f32x16 Y[4];
#pragma unroll
  for (int q = 0; q < 4; ++q)
#pragma unroll
    for (int i = 0; i < 16; i += 2) {
      f32x2 y;
      if (q < 2) asm volatile("v_mov_b64 %0, %1" : "=v"(y) : "s"(0x358637bd358637bdull));
      else asm volatile("v_mov_b64 %0, 0" : "=v"(y));
      Y[q][i] = y[0];
      Y[q][i + 1] = y[1];
    }
  f32x16 G0, V0, G1, V1;
  bf16x8 S[2], St[2], as[4], ae, b[3];
  u32x4 pd[2], pn[2];
  float tq[16], rq[16], pq[16];
  auto pipe_zero = [&]() {
#pragma unroll
    for (int j = 0; j < 8; ++j) { S[0][j] = 0; S[1][j] = 0; }
#pragma unroll
    for (int j = 0; j < 4; ++j) { pd[0][j] = 0; pd[1][j] = 0; pn[0][j] = 0; pn[1][j] = 0; }
#pragma unroll
    for (int i = 0; i < 16; ++i) V1[i] = 0.f;
    rq[14] = 0.f; rq[15] = 0.f;
  };
  unsigned rid = lane & 7;
  asm volatile("" : "+v"(aLX), "+v"(aLE), "+v"(aLS4), "+v"(aLS1), "+v"(cx0), "+v"(cx1), "+v"(ce), "+v"(cS), "+v"(cSt),
                    "+v"(fo[0]), "+v"(fo[1]), "+v"(fo[2]), "+v"(fo[3]), "+v"(foe), "+v"(rid));
  asm volatile("" : "+v"(Y[0]), "+v"(Y[1]), "+v"(Y[2]), "+v"(Y[3]));
  auto ld128 = [&](unsigned a) { return *(const LAS u32x4*)(size_t)a; };
  auto gather5 = [&](const u32x4 sx, unsigned re, unsigned dstb) {
    glds16x(x, sx[0], cx0, dstb);
    glds16x(x, sx[1], cx1, dstb + 1024);
    glds16x(x, sx[2], cx0, dstb + 2048);
    glds16x(x, sx[3], cx1, dstb + 3072);
    glds16e(ES, re, ce, dstb + 4096);
  };
  unsigned rq4 = ((lane >> 3) & 3) * 4;
  unsigned early, just;
  asm volatile(
        "s_mov_b32 s96, 0x800000\n\t"
        "v_mov_b32 v181, %18\n\t"
        "v_mov_b32 v180, 5120\n\t"
        "v_mov_b32 v177, 0x0c040100\n\t"
        "v_mov_b32 v178, 0x0c050100\n\t"
        "v_mov_b32 v179, 0x0c060100\n\t"
        "s_mov_b32 s52, 0x7f7f7f7f\n\t"
        "s_mov_b32 s53, 0x7f7f7f7f\n\t"
        "s_mov_b64 s[54:55], 0\n\t"
        "s_mov_b32 %12, 0\n\t"
        "v_mov_b32 v191, 16\n\t"
        "s_waitcnt vmcnt(%30)\n\t"
        "v_add3_u32 v228, v240, v241, v242\n\t"
        "v_add3_u32 v231, v244, v245, v246\n\t"
        "v_add3_u32 v228, v228, v243, %19\n\t"
        "v_add3_u32 v231, v231, v247, %19\n\t"
        "v_cmp_ne_u32_sdwa s[72:73], v243, %20 src0_sel:BYTE_3 src1_sel:DWORD\n\t"
        "v_add_u32_dpp v228, v228, v228 quad_perm:[1,0,3,2] row_mask:0xf bank_mask:0xf\n\t"
        "v_add_u32_dpp v231, v231, v231 quad_perm:[1,0,3,2] row_mask:0xf bank_mask:0xf\n\t"
        "v_cmp_ne_u32_sdwa s[78:79], v247, %20 src0_sel:BYTE_3 src1_sel:DWORD\n\t"
        "v_add_u32_dpp v228, v228, v228 quad_perm:[2,3,0,1] row_mask:0xf bank_mask:0xf\n\t"
        "v_add_u32_dpp v231, v231, v231 quad_perm:[2,3,0,1] row_mask:0xf bank_mask:0xf\n\t"
        "s_nop 0\n\t"
        "v_add_u32_dpp v228, v228, v228 row_half_mirror row_mask:0xf bank_mask:0xf\n\t"
        "v_add_u32_dpp v231, v231, v231 row_half_mirror row_mask:0xf bank_mask:0xf\n\t"
        "v_cmp_gt_u32 s[64:65], 2, v228\n\t"
        "v_cmp_eq_u32 s[56:57], 1, v228\n\t"
        "v_cmp_gt_u32 s[66:67], 2, v231\n\t"
        "v_cmp_eq_u32 s[58:59], 1, v231\n\t"
        "s_and_b64 s[72:73], s[72:73], s[64:65]\n\t"
        "s_cselect_b64 s[64:65], 0, s[64:65]\n\t"
        "s_and_b64 s[56:57], s[56:57], s[64:65]\n\t"
        "s_not_b64 %0, s[64:65]\n\t"
        "s_or_b64 s[54:55], s[54:55], s[56:57]\n\t"
        "s_or_b64 s[54:55], s[54:55], %0\n\t"
        "s_and_b64 s[78:79], s[78:79], s[66:67]\n\t"
        "s_cselect_b64 s[66:67], 0, s[66:67]\n\t"
        "s_and_b64 s[58:59], s[58:59], s[66:67]\n\t"
        "s_not_b64 %1, s[66:67]\n\t"
        "s_or_b64 s[54:55], s[54:55], s[58:59]\n\t"
        "s_or_b64 s[54:55], s[54:55], %1\n\t"
        "s_mov_b64 exec, s[64:65]\n\t"
        "v_cmpx_ne_u32 s[72:73], -1, v240\n\t"
        "v_bfe_u32 v192, v243, 5, 3\n\t"
        "v_lshl_add_u32 v204, v192, 4, %21\n\t"
        "ds_add_rtn_u32 v204, v204, v191\n\t"
        "v_perm_b32 v216, v243, v240, v177\n\t"
        "v_mad_u32_u16 v228, v240, 32, %13 op_sel:[1,0,0,0]\n\t"
        "v_cmpx_ne_u32 s[74:75], -1, v241\n\t"
        "v_bfe_u32 v193, v243, 13, 3\n\t"
        "v_lshl_add_u32 v205, v193, 4, %21\n\t"
        "ds_add_rtn_u32 v205, v205, v191\n\t"
        "v_perm_b32 v217, v243, v241, v178\n\t"
        "v_mad_u32_u16 v229, v241, 32, %13 op_sel:[1,0,0,0]\n\t"
        "s_and_b64 exec, exec, s[52:53]\n\t"
        "v_cmpx_ne_u32 s[76:77], -1, v242\n\t"
        "v_bfe_u32 v194, v243, 21, 3\n\t"
        "v_lshl_add_u32 v206, v194, 4, %21\n\t"
        "ds_add_rtn_u32 v206, v206, v191\n\t"
        "v_perm_b32 v218, v243, v242, v179\n\t"
        "v_mad_u32_u16 v230, v242, 32, %13 op_sel:[1,0,0,0]\n\t"
        "s_mov_b64 exec, s[66:67]\n\t"
        "v_cmpx_ne_u32 s[78:79], -1, v244\n\t"
        "v_bfe_u32 v195, v247, 5, 3\n\t"
        "v_lshl_add_u32 v207, v195, 4, %21\n\t"
        "ds_add_rtn_u32 v207, v207, v191\n\t"
        "v_perm_b32 v219, v247, v244, v177\n\t"
        "v_mad_u32_u16 v231, v244, 32, %14 op_sel:[1,0,0,0]\n\t"
        "v_cmpx_ne_u32 s[80:81], -1, v245\n\t"
        "v_bfe_u32 v196, v247, 13, 3\n\t"
        "v_lshl_add_u32 v208, v196, 4, %21\n\t"
        "ds_add_rtn_u32 v208, v208, v191\n\t"
        "v_perm_b32 v220, v247, v245, v178\n\t"
        "v_mad_u32_u16 v232, v245, 32, %14 op_sel:[1,0,0,0]\n\t"
        "s_and_b64 exec, exec, s[52:53]\n\t"
        "v_cmpx_ne_u32 s[82:83], -1, v246\n\t"
        "v_bfe_u32 v197, v247, 21, 3\n\t"
        "v_lshl_add_u32 v209, v197, 4, %21\n\t"
        "ds_add_rtn_u32 v209, v209, v191\n\t"
        "v_perm_b32 v221, v247, v246, v179\n\t"
        "v_mad_u32_u16 v233, v246, 32, %14 op_sel:[1,0,0,0]\n\t"
        "s_mov_b64 exec, -1\n\t"
        "s_waitcnt vmcnt(%31)\n\t"
        "v_add3_u32 v234, v248, v249, v250\n\t"
        "v_add3_u32 v237, v252, v253, v254\n\t"
        "v_add3_u32 v234, v234, v251, %19\n\t"
        "v_add3_u32 v237, v237, v255, %19\n\t"
        "v_cmp_ne_u32_sdwa s[84:85], v251, %20 src0_sel:BYTE_3 src1_sel:DWORD\n\t"
        "v_add_u32_dpp v234, v234, v234 quad_perm:[1,0,3,2] row_mask:0xf bank_mask:0xf\n\t"
        "v_add_u32_dpp v237, v237, v237 quad_perm:[1,0,3,2] row_mask:0xf bank_mask:0xf\n\t"
        "v_cmp_ne_u32_sdwa s[90:91], v255, %20 src0_sel:BYTE_3 src1_sel:DWORD\n\t"
        "v_add_u32_dpp v234, v234, v234 quad_perm:[2,3,0,1] row_mask:0xf bank_mask:0xf\n\t"
        "v_add_u32_dpp v237, v237, v237 quad_perm:[2,3,0,1] row_mask:0xf bank_mask:0xf\n\t"
        "s_nop 0\n\t"
        "v_add_u32_dpp v234, v234, v234 row_half_mirror row_mask:0xf bank_mask:0xf\n\t"
        "v_add_u32_dpp v237, v237, v237 row_half_mirror row_mask:0xf bank_mask:0xf\n\t"
        "v_cmp_gt_u32 s[68:69], 2, v234\n\t"
        "v_cmp_eq_u32 s[60:61], 1, v234\n\t"
        "v_cmp_gt_u32 s[70:71], 2, v237\n\t"
        "v_cmp_eq_u32 s[62:63], 1, v237\n\t"
        "s_and_b64 s[84:85], s[84:85], s[68:69]\n\t"
        "s_cselect_b64 s[68:69], 0, s[68:69]\n\t"
        "s_and_b64 s[60:61], s[60:61], s[68:69]\n\t"
        "s_not_b64 %2, s[68:69]\n\t"
        "s_or_b64 s[54:55], s[54:55], s[60:61]\n\t"
        "s_or_b64 s[54:55], s[54:55], %2\n\t"
        "s_and_b64 s[90:91], s[90:91], s[70:71]\n\t"
        "s_cselect_b64 s[70:71], 0, s[70:71]\n\t"
        "s_and_b64 s[62:63], s[62:63], s[70:71]\n\t"
        "s_not_b64 %3, s[70:71]\n\t"
        "s_or_b64 s[54:55], s[54:55], s[62:63]\n\t"
        "s_or_b64 s[54:55], s[54:55], %3\n\t"
        "s_mov_b64 exec, s[72:73]\n\t"
        "s_waitcnt lgkmcnt(3)\n\t"
        "v_min_u32 v204, v181, v204\n\t"
        "v_min_u32 v205, v181, v205\n\t"
        "v_min_u32 v206, v181, v206\n\t"
        "v_mad_u32_u24 v204, v192, v180, v204\n\t"
        "v_mad_u32_u24 v205, v193, v180, v205\n\t"
        "v_mad_u32_u24 v206, v194, v180, v206\n\t"
        "ds_write2st64_b32 v204, v216, v228 offset1:10\n\t"
        "s_mov_b64 exec, s[74:75]\n\t"
        "ds_write2st64_b32 v205, v217, v229 offset1:10\n\t"
        "s_mov_b64 exec, s[76:77]\n\t"
        "ds_write2st64_b32 v206, v218, v230 offset1:10\n\t"
        "s_mov_b64 exec, s[68:69]\n\t"
        "v_cmpx_ne_u32 s[84:85], -1, v248\n\t"
        "v_bfe_u32 v198, v251, 5, 3\n\t"
        "v_lshl_add_u32 v210, v198, 4, %21\n\t"
        "ds_add_rtn_u32 v210, v210, v191\n\t"
        "v_perm_b32 v222, v251, v248, v177\n\t"
        "v_mad_u32_u16 v234, v248, 32, %15 op_sel:[1,0,0,0]\n\t"
        "v_cmpx_ne_u32 s[86:87], -1, v249\n\t"
        "v_bfe_u32 v199, v251, 13, 3\n\t"
        "v_lshl_add_u32 v211, v199, 4, %21\n\t"
        "ds_add_rtn_u32 v211, v211, v191\n\t"
        "v_perm_b32 v223, v251, v249, v178\n\t"
        "v_mad_u32_u16 v235, v249, 32, %15 op_sel:[1,0,0,0]\n\t"
        "s_and_b64 exec, exec, s[52:53]\n\t"
        "v_cmpx_ne_u32 s[88:89], -1, v250\n\t"
        "v_bfe_u32 v200, v251, 21, 3\n\t"
        "v_lshl_add_u32 v212, v200, 4, %21\n\t"
        "ds_add_rtn_u32 v212, v212, v191\n\t"
        "v_perm_b32 v224, v251, v250, v179\n\t"
        "v_mad_u32_u16 v236, v250, 32, %15 op_sel:[1,0,0,0]\n\t"
        "s_mov_b64 exec, s[78:79]\n\t"
        "s_waitcnt lgkmcnt(6)\n\t"
        "v_min_u32 v207, v181, v207\n\t"
        "v_min_u32 v208, v181, v208\n\t"
        "v_min_u32 v209, v181, v209\n\t"
        "v_mad_u32_u24 v207, v195, v180, v207\n\t"
        "v_mad_u32_u24 v208, v196, v180, v208\n\t"
        "v_mad_u32_u24 v209, v197, v180, v209\n\t"
        "ds_write2st64_b32 v207, v219, v231 offset1:10\n\t"
        "s_mov_b64 exec, s[80:81]\n\t"
        "ds_write2st64_b32 v208, v220, v232 offset1:10\n\t"
        "s_mov_b64 exec, s[82:83]\n\t"
        "ds_write2st64_b32 v209, v221, v233 offset1:10\n\t"
        "s_mov_b64 exec, -1\n\t"
        "ds_read_b128 v[184:187], %22\n\t"
        "ds_read_b32 v183, %23\n\t"
        "s_mov_b64 exec, s[70:71]\n\t"
        "v_cmpx_ne_u32 s[90:91], -1, v252\n\t"
        "v_bfe_u32 v201, v255, 5, 3\n\t"
        "v_lshl_add_u32 v213, v201, 4, %21\n\t"
        "ds_add_rtn_u32 v213, v213, v191\n\t"
        "v_perm_b32 v225, v255, v252, v177\n\t"
        "v_mad_u32_u16 v237, v252, 32, %16 op_sel:[1,0,0,0]\n\t"
        "v_cmpx_ne_u32 s[92:93], -1, v253\n\t"
        "v_bfe_u32 v202, v255, 13, 3\n\t"
        "v_lshl_add_u32 v214, v202, 4, %21\n\t"
        "ds_add_rtn_u32 v214, v214, v191\n\t"
        "v_perm_b32 v226, v255, v253, v178\n\t"
        "v_mad_u32_u16 v238, v253, 32, %16 op_sel:[1,0,0,0]\n\t"
        "s_and_b64 exec, exec, s[52:53]\n\t"
        "v_cmpx_ne_u32 s[94:95], -1, v254\n\t"
        "v_bfe_u32 v203, v255, 21, 3\n\t"
        "v_lshl_add_u32 v215, v203, 4, %21\n\t"
        "ds_add_rtn_u32 v215, v215, v191\n\t"
        "v_perm_b32 v227, v255, v254, v179\n\t"
        "v_mad_u32_u16 v239, v254, 32, %16 op_sel:[1,0,0,0]\n\t"
        "s_cmp_eq_u64 s[54:55], 0\n\t"
        "s_cbranch_scc1 59f\n\t"
        "s_mov_b64 exec, s[56:57]\n\t"
        "s_cbranch_execz 40f\n\t"
        "v_bfe_u32 v188, %4, 7, 8\n\t"
        "v_mov_b32 v189, 1\n\t"
        "ds_write_b8 v188, v189 offset:256\n\t"
        "v_add_u32 %4, s96, %4\n\t"
        "s_or_b64 %0, %0, exec\n\t"
        "40:\n\t"
        "s_mov_b64 exec, %0\n\t"
        "s_cbranch_execz 50f\n\t"
        "global_load_dwordx4 v[240:243], %4, %17 sc1\n\t"
        "50:\n\t"
        "s_mov_b64 exec, s[58:59]\n\t"
        "s_cbranch_execz 41f\n\t"
        "v_bfe_u32 v188, %5, 7, 8\n\t"
        "v_mov_b32 v189, 1\n\t"
        "ds_write_b8 v188, v189 offset:256\n\t"
        "v_add_u32 %5, s96, %5\n\t"
        "s_or_b64 %1, %1, exec\n\t"
        "41:\n\t"
        "s_mov_b64 exec, %1\n\t"
        "s_cbranch_execz 51f\n\t"
        "global_load_dwordx4 v[244:247], %5, %17 sc1\n\t"
        "51:\n\t"
        "s_mov_b64 exec, s[60:61]\n\t"
        "s_cbranch_execz 42f\n\t"
        "v_bfe_u32 v188, %6, 7, 8\n\t"
        "v_mov_b32 v189, 1\n\t"
        "ds_write_b8 v188, v189 offset:256\n\t"
        "v_add_u32 %6, s96, %6\n\t"
        "s_or_b64 %2, %2, exec\n\t"
        "42:\n\t"
        "s_mov_b64 exec, %2\n\t"
        "s_cbranch_execz 52f\n\t"
        "global_load_dwordx4 v[248:251], %6, %17 sc1\n\t"
        "52:\n\t"
        "s_mov_b64 exec, s[62:63]\n\t"
        "s_cbranch_execz 43f\n\t"
        "v_bfe_u32 v188, %7, 7, 8\n\t"
        "v_mov_b32 v189, 1\n\t"
        "ds_write_b8 v188, v189 offset:256\n\t"
        "v_add_u32 %7, s96, %7\n\t"
        "s_or_b64 %3, %3, exec\n\t"
        "43:\n\t"
        "s_mov_b64 exec, %3\n\t"
        "s_cbranch_execz 53f\n\t"
        "global_load_dwordx4 v[252:255], %7, %17 sc1\n\t"
        "53:\n\t"
        "s_waitcnt lgkmcnt(0)\n\t"
        "59:\n\t"
        "s_mov_b64 exec, s[84:85]\n\t"
        "s_waitcnt lgkmcnt(8)\n\t"
        "v_min_u32 v210, v181, v210\n\t"
        "v_min_u32 v211, v181, v211\n\t"
        "v_min_u32 v212, v181, v212\n\t"
        "v_mad_u32_u24 v210, v198, v180, v210\n\t"
        "v_mad_u32_u24 v211, v199, v180, v211\n\t"
        "v_mad_u32_u24 v212, v200, v180, v212\n\t"
        "ds_write2st64_b32 v210, v222, v234 offset1:10\n\t"
        "s_mov_b64 exec, s[86:87]\n\t"
        "ds_write2st64_b32 v211, v223, v235 offset1:10\n\t"
        "s_mov_b64 exec, s[88:89]\n\t"
        "ds_write2st64_b32 v212, v224, v236 offset1:10\n\t"
        "s_mov_b64 exec, -1\n\t"
        "s_movk_i32 s96, 128\n\t"
        "s_mov_b32 m0, %29\n\t"
        "s_waitcnt lgkmcnt(6)\n\t"
        "v_max3_u32 v182, v184, v185, v186\n\t"
        "v_max3_u32 v182, v182, v187, v183\n\t"
        "v_cmp_eq_u32 vcc, -1, v182\n\t"
        "v_mad_u32_u16 v184, v184, s96, %24\n\t"
        "v_mad_u32_u16 v185, v185, s96, %25\n\t"
        "v_mad_u32_u16 v186, v186, s96, %24\n\t"
        "v_mad_u32_u16 v187, v187, s96, %25\n\t"
        "v_add_u32 v183, v183, %26\n\t"
        "s_cbranch_vccnz 60f\n\t"
        "global_load_lds_dwordx4 v184, %27\n\t"
        "s_addk_i32 m0, 1024\n\t"
        "s_nop 0\n\t"
        "global_load_lds_dwordx4 v185, %27\n\t"
        "s_addk_i32 m0, 1024\n\t"
        "s_nop 0\n\t"
        "global_load_lds_dwordx4 v186, %27\n\t"
        "s_addk_i32 m0, 1024\n\t"
        "s_nop 0\n\t"
        "global_load_lds_dwordx4 v187, %27\n\t"
        "s_addk_i32 m0, 1024\n\t"
        "s_nop 0\n\t"
        "global_load_lds_dwordx4 v183, %28\n\t"
        "s_mov_b32 %12, 1\n\t"
        "60:\n\t"
        "s_mov_b64 exec, s[90:91]\n\t"
        "s_waitcnt lgkmcnt(3)\n\t"
        "v_min_u32 v213, v181, v213\n\t"
        "v_min_u32 v214, v181, v214\n\t"
        "v_min_u32 v215, v181, v215\n\t"
        "v_mad_u32_u24 v213, v201, v180, v213\n\t"
        "v_mad_u32_u24 v214, v202, v180, v214\n\t"
        "v_mad_u32_u24 v215, v203, v180, v215\n\t"
        "ds_write2st64_b32 v213, v225, v237 offset1:10\n\t"
        "s_mov_b64 exec, s[92:93]\n\t"
        "ds_write2st64_b32 v214, v226, v238 offset1:10\n\t"
        "s_mov_b64 exec, s[94:95]\n\t"
        "ds_write2st64_b32 v215, v227, v239 offset1:10\n\t"
        "s_mov_b64 exec, -1\n\t"
        : "=&s"(pend[0]), "=&s"(pend[1]), "=&s"(pend[2]), "=&s"(pend[3]), "+v"(vo[0]), "+v"(vo[1]), "+v"(vo[2]),
          "+v"(vo[3]), "+{v[240:243]}"(cv[0]), "+{v[244:247]}"(cv[1]), "+{v[248:251]}"(cv[2]), "+{v[252:255]}"(cv[3]),
          "=&s"(early)
        : "v"(eb[0]), "v"(eb[1]), "v"(eb[2]), "v"(eb[3]), "s"(ib), "n"(OFF_L + LARR - 16), "v"(tag[0]), "v"(tag[1]),
          "v"(rq4), "v"(aLX + wave * LIST_BYTES), "v"(aLE + wave * LIST_BYTES), "v"(cx0), "v"(cx1), "v"(ce), "s"(x),
          "s"(ES), "s"(lds0 + OFF_R0 + wave * RING_SLOT), "n"(3), "n"(1)
        :
          "memory", "vcc", "scc", "m0", "v177", "v178", "v179", "v180", "v181", "v182", "v183", "v184", "v185",
          "v186", "v187", "v188", "v189", "v190", "v191", "v192", "v193", "v194", "v195", "v196", "v197", "v198",
          "v199", "v200", "v201", "v202", "v203", "v204", "v205", "v206", "v207", "v208", "v209", "v210", "v211",
          "v212", "v213", "v214", "v215", "v216", "v217", "v218", "v219", "v220", "v221", "v222", "v223", "v224",
          "v225", "v226", "v227", "v228", "v229", "v230", "v231", "v232", "v233", "v234", "v235", "v236", "v237",
          "v238", "v239", "s52", "s53", "s54", "s55", "s56", "s57", "s58", "s59", "s60", "s61", "s62", "s63", "s64",
          "s65", "s66", "s67", "s68", "s69", "s70", "s71", "s72", "s73", "s74", "s75", "s76", "s77", "s78", "s79",
          "s80", "s81", "s82", "s83", "s84", "s85", "s86", "s87", "s88", "s89", "s90", "s91", "s92", "s93", "s94",
          "s95", "s96");
  auto uni = [&]() {
#pragma unroll
    for (int k = 0; k < 4; ++k)
      pend[k] = ((unsigned long long)(unsigned)__builtin_amdgcn_readfirstlane((unsigned)(pend[k] >> 32)) << 32) |
                (unsigned)__builtin_amdgcn_readfirstlane((unsigned)pend[k]);
  };
  uni();
  early = __builtin_amdgcn_readfirstlane(early);
  just = early;
  if (__builtin_expect((pend[0] | pend[1] | pend[2] | pend[3] | (unsigned long long)(early ^ 1u)) != 0ull, 0))
#pragma clang loop unroll(disable)
  while (true) {
    if (!early) {
      const u32x4 sx = ld128(aLX + wave * LIST_BYTES);
      const unsigned re = *(const LAS unsigned*)(size_t)(aLE + wave * LIST_BYTES);
      if (__ballot(max(max(max(sx[0], sx[1]), max(sx[2], sx[3])), re) == ~0u) == 0ull) {
        gather5(sx, re, lds0 + OFF_R0 + wave * RING_SLOT);
        early = 1u;
        just = 1u;
      }
    }
    if ((pend[0] | pend[1] | pend[2] | pend[3]) == 0ull || --spin < 0) break;
    asm volatile(
        "s_mov_b32 s96, 0x800000\n\t"
        "v_mov_b32 v181, %17\n\t"
        "v_mov_b32 v180, 5120\n\t"
        "v_mov_b32 v177, 0x0c040100\n\t"
        "v_mov_b32 v178, 0x0c050100\n\t"
        "v_mov_b32 v179, 0x0c060100\n\t"
        "s_mov_b32 s52, 0x7f7f7f7f\n\t"
        "s_mov_b32 s53, 0x7f7f7f7f\n\t"
        "v_mov_b32 v191, 16\n\t"
        "s_waitcnt vmcnt(5)\n\t"
        "s_cmp_lg_u32 %21, 0\n\t"
        "s_cbranch_scc1 9f\n\t"
        "s_waitcnt vmcnt(0)\n\t"
        "9:\n\t"
        "v_add3_u32 v228, v240, v241, v242\n\t"
        "v_add3_u32 v229, v244, v245, v246\n\t"
        "v_add3_u32 v230, v248, v249, v250\n\t"
        "v_add3_u32 v231, v252, v253, v254\n\t"
        "v_add3_u32 v228, v228, v243, %18\n\t"
        "v_add3_u32 v229, v229, v247, %18\n\t"
        "v_add3_u32 v230, v230, v251, %18\n\t"
        "v_add3_u32 v231, v231, v255, %18\n\t"
        "v_add_u32_dpp v228, v228, v228 quad_perm:[1,0,3,2] row_mask:0xf bank_mask:0xf\n\t"
        "v_add_u32_dpp v229, v229, v229 quad_perm:[1,0,3,2] row_mask:0xf bank_mask:0xf\n\t"
        "v_add_u32_dpp v230, v230, v230 quad_perm:[1,0,3,2] row_mask:0xf bank_mask:0xf\n\t"
        "v_add_u32_dpp v231, v231, v231 quad_perm:[1,0,3,2] row_mask:0xf bank_mask:0xf\n\t"
        "v_add_u32_dpp v228, v228, v228 quad_perm:[2,3,0,1] row_mask:0xf bank_mask:0xf\n\t"
        "v_add_u32_dpp v229, v229, v229 quad_perm:[2,3,0,1] row_mask:0xf bank_mask:0xf\n\t"
        "v_add_u32_dpp v230, v230, v230 quad_perm:[2,3,0,1] row_mask:0xf bank_mask:0xf\n\t"
        "v_add_u32_dpp v231, v231, v231 quad_perm:[2,3,0,1] row_mask:0xf bank_mask:0xf\n\t"
        "v_add_u32_dpp v228, v228, v228 row_half_mirror row_mask:0xf bank_mask:0xf\n\t"
        "v_add_u32_dpp v229, v229, v229 row_half_mirror row_mask:0xf bank_mask:0xf\n\t"
        "v_add_u32_dpp v230, v230, v230 row_half_mirror row_mask:0xf bank_mask:0xf\n\t"
        "v_add_u32_dpp v231, v231, v231 row_half_mirror row_mask:0xf bank_mask:0xf\n\t"
        "v_cmp_ne_u32_sdwa s[72:73], v243, %19 src0_sel:BYTE_3 src1_sel:DWORD\n\t"
        "v_cmp_gt_u32 s[64:65], 2, v228\n\t"
        "v_cmp_eq_u32 s[56:57], 1, v228\n\t"
        "v_cmp_ne_u32_sdwa s[74:75], v247, %19 src0_sel:BYTE_3 src1_sel:DWORD\n\t"
        "v_cmp_gt_u32 s[66:67], 2, v229\n\t"
        "v_cmp_eq_u32 s[58:59], 1, v229\n\t"
        "v_cmp_ne_u32_sdwa s[76:77], v251, %19 src0_sel:BYTE_3 src1_sel:DWORD\n\t"
        "v_cmp_gt_u32 s[68:69], 2, v230\n\t"
        "v_cmp_eq_u32 s[60:61], 1, v230\n\t"
        "v_cmp_ne_u32_sdwa s[78:79], v255, %19 src0_sel:BYTE_3 src1_sel:DWORD\n\t"
        "v_cmp_gt_u32 s[70:71], 2, v231\n\t"
        "v_cmp_eq_u32 s[62:63], 1, v231\n\t"
        "s_and_b64 s[64:65], s[64:65], %0\n\t"
        "s_and_b64 s[72:73], s[72:73], s[64:65]\n\t"
        "s_cselect_b64 s[64:65], 0, s[64:65]\n\t"
        "s_and_b64 s[56:57], s[56:57], s[64:65]\n\t"
        "s_andn2_b64 %0, %0, s[64:65]\n\t"
        "s_cbranch_scc0 10f\n\t"
        "s_mov_b64 exec, %0\n\t"
        "global_load_dwordx4 v[240:243], %4, %16 sc1\n\t"
        "10:\n\t"
        "s_and_b64 s[66:67], s[66:67], %1\n\t"
        "s_and_b64 s[74:75], s[74:75], s[66:67]\n\t"
        "s_cselect_b64 s[66:67], 0, s[66:67]\n\t"
        "s_and_b64 s[58:59], s[58:59], s[66:67]\n\t"
        "s_andn2_b64 %1, %1, s[66:67]\n\t"
        "s_cbranch_scc0 11f\n\t"
        "s_mov_b64 exec, %1\n\t"
        "global_load_dwordx4 v[244:247], %5, %16 sc1\n\t"
        "11:\n\t"
        "s_and_b64 s[68:69], s[68:69], %2\n\t"
        "s_and_b64 s[76:77], s[76:77], s[68:69]\n\t"
        "s_cselect_b64 s[68:69], 0, s[68:69]\n\t"
        "s_and_b64 s[60:61], s[60:61], s[68:69]\n\t"
        "s_andn2_b64 %2, %2, s[68:69]\n\t"
        "s_cbranch_scc0 12f\n\t"
        "s_mov_b64 exec, %2\n\t"
        "global_load_dwordx4 v[248:251], %6, %16 sc1\n\t"
        "12:\n\t"
        "s_and_b64 s[70:71], s[70:71], %3\n\t"
        "s_and_b64 s[78:79], s[78:79], s[70:71]\n\t"
        "s_cselect_b64 s[70:71], 0, s[70:71]\n\t"
        "s_and_b64 s[62:63], s[62:63], s[70:71]\n\t"
        "s_andn2_b64 %3, %3, s[70:71]\n\t"
        "s_cbranch_scc0 13f\n\t"
        "s_mov_b64 exec, %3\n\t"
        "global_load_dwordx4 v[252:255], %7, %16 sc1\n\t"
        "13:\n\t"
        "s_mov_b64 s[54:55], 0\n\t"
        "s_mov_b64 s[72:73], 0\n\t"
        "s_mov_b64 exec, s[64:65]\n\t"
        "s_cbranch_execz 20f\n\t"
        "v_cmpx_ne_u32 s[72:73], -1, v240\n\t"
        "v_bfe_u32 v192, v243, 5, 3\n\t"
        "v_lshl_add_u32 v204, v192, 4, %20\n\t"
        "ds_add_rtn_u32 v204, v204, v191\n\t"
        "v_perm_b32 v216, v243, v240, v177\n\t"
        "v_mad_u32_u16 v228, v240, 32, %12 op_sel:[1,0,0,0]\n\t"
        "v_cmpx_ne_u32 s[74:75], -1, v241\n\t"
        "v_bfe_u32 v193, v243, 13, 3\n\t"
        "v_lshl_add_u32 v205, v193, 4, %20\n\t"
        "ds_add_rtn_u32 v205, v205, v191\n\t"
        "v_perm_b32 v217, v243, v241, v178\n\t"
        "v_mad_u32_u16 v229, v241, 32, %12 op_sel:[1,0,0,0]\n\t"
        "s_and_b64 exec, exec, s[52:53]\n\t"
        "v_cmpx_ne_u32 s[76:77], -1, v242\n\t"
        "v_bfe_u32 v194, v243, 21, 3\n\t"
        "v_lshl_add_u32 v206, v194, 4, %20\n\t"
        "ds_add_rtn_u32 v206, v206, v191\n\t"
        "v_perm_b32 v218, v243, v242, v179\n\t"
        "v_mad_u32_u16 v230, v242, 32, %12 op_sel:[1,0,0,0]\n\t"
        "s_or_b64 s[54:55], s[54:55], s[56:57]\n\t"
        "20:\n\t"
        "s_mov_b64 s[78:79], 0\n\t"
        "s_mov_b64 exec, s[66:67]\n\t"
        "s_cbranch_execz 21f\n\t"
        "v_cmpx_ne_u32 s[78:79], -1, v244\n\t"
        "v_bfe_u32 v195, v247, 5, 3\n\t"
        "v_lshl_add_u32 v207, v195, 4, %20\n\t"
        "ds_add_rtn_u32 v207, v207, v191\n\t"
        "v_perm_b32 v219, v247, v244, v177\n\t"
        "v_mad_u32_u16 v231, v244, 32, %13 op_sel:[1,0,0,0]\n\t"
        "v_cmpx_ne_u32 s[80:81], -1, v245\n\t"
        "v_bfe_u32 v196, v247, 13, 3\n\t"
        "v_lshl_add_u32 v208, v196, 4, %20\n\t"
        "ds_add_rtn_u32 v208, v208, v191\n\t"
        "v_perm_b32 v220, v247, v245, v178\n\t"
        "v_mad_u32_u16 v232, v245, 32, %13 op_sel:[1,0,0,0]\n\t"
        "s_and_b64 exec, exec, s[52:53]\n\t"
        "v_cmpx_ne_u32 s[82:83], -1, v246\n\t"
        "v_bfe_u32 v197, v247, 21, 3\n\t"
        "v_lshl_add_u32 v209, v197, 4, %20\n\t"
        "ds_add_rtn_u32 v209, v209, v191\n\t"
        "v_perm_b32 v221, v247, v246, v179\n\t"
        "v_mad_u32_u16 v233, v246, 32, %13 op_sel:[1,0,0,0]\n\t"
        "s_or_b64 s[54:55], s[54:55], s[58:59]\n\t"
        "21:\n\t"
        "s_mov_b64 s[84:85], 0\n\t"
        "s_mov_b64 exec, s[68:69]\n\t"
        "s_cbranch_execz 22f\n\t"
        "v_cmpx_ne_u32 s[84:85], -1, v248\n\t"
        "v_bfe_u32 v198, v251, 5, 3\n\t"
        "v_lshl_add_u32 v210, v198, 4, %20\n\t"
        "ds_add_rtn_u32 v210, v210, v191\n\t"
        "v_perm_b32 v222, v251, v248, v177\n\t"
        "v_mad_u32_u16 v234, v248, 32, %14 op_sel:[1,0,0,0]\n\t"
        "v_cmpx_ne_u32 s[86:87], -1, v249\n\t"
        "v_bfe_u32 v199, v251, 13, 3\n\t"
        "v_lshl_add_u32 v211, v199, 4, %20\n\t"
        "ds_add_rtn_u32 v211, v211, v191\n\t"
        "v_perm_b32 v223, v251, v249, v178\n\t"
        "v_mad_u32_u16 v235, v249, 32, %14 op_sel:[1,0,0,0]\n\t"
        "s_and_b64 exec, exec, s[52:53]\n\t"
        "v_cmpx_ne_u32 s[88:89], -1, v250\n\t"
        "v_bfe_u32 v200, v251, 21, 3\n\t"
        "v_lshl_add_u32 v212, v200, 4, %20\n\t"
        "ds_add_rtn_u32 v212, v212, v191\n\t"
        "v_perm_b32 v224, v251, v250, v179\n\t"
        "v_mad_u32_u16 v236, v250, 32, %14 op_sel:[1,0,0,0]\n\t"
        "s_or_b64 s[54:55], s[54:55], s[60:61]\n\t"
        "22:\n\t"
        "s_mov_b64 s[90:91], 0\n\t"
        "s_mov_b64 exec, s[70:71]\n\t"
        "s_cbranch_execz 23f\n\t"
        "v_cmpx_ne_u32 s[90:91], -1, v252\n\t"
        "v_bfe_u32 v201, v255, 5, 3\n\t"
        "v_lshl_add_u32 v213, v201, 4, %20\n\t"
        "ds_add_rtn_u32 v213, v213, v191\n\t"
        "v_perm_b32 v225, v255, v252, v177\n\t"
        "v_mad_u32_u16 v237, v252, 32, %15 op_sel:[1,0,0,0]\n\t"
        "v_cmpx_ne_u32 s[92:93], -1, v253\n\t"
        "v_bfe_u32 v202, v255, 13, 3\n\t"
        "v_lshl_add_u32 v214, v202, 4, %20\n\t"
        "ds_add_rtn_u32 v214, v214, v191\n\t"
        "v_perm_b32 v226, v255, v253, v178\n\t"
        "v_mad_u32_u16 v238, v253, 32, %15 op_sel:[1,0,0,0]\n\t"
        "s_and_b64 exec, exec, s[52:53]\n\t"
        "v_cmpx_ne_u32 s[94:95], -1, v254\n\t"
        "v_bfe_u32 v203, v255, 21, 3\n\t"
        "v_lshl_add_u32 v215, v203, 4, %20\n\t"
        "ds_add_rtn_u32 v215, v215, v191\n\t"
        "v_perm_b32 v227, v255, v254, v179\n\t"
        "v_mad_u32_u16 v239, v254, 32, %15 op_sel:[1,0,0,0]\n\t"
        "s_or_b64 s[54:55], s[54:55], s[62:63]\n\t"
        "23:\n\t"
        "s_cmp_eq_u64 s[54:55], 0\n\t"
        "s_cbranch_scc1 59f\n\t"
        "s_mov_b64 exec, s[56:57]\n\t"
        "s_cbranch_execz 40f\n\t"
        "v_bfe_u32 v188, %4, 7, 8\n\t"
        "v_lshrrev_b32 v189, 23, %4\n\t"
        "v_add_u32 v189, 1, v189\n\t"
        "ds_write_b8 v188, v189 offset:256\n\t"
        "v_add_u32 %4, s96, %4\n\t"
        "v_mov_b32 v190, 0\n\t"
        "v_cmpx_eq_u32 vcc, 2, v189\n\t"
        "ds_write_b32 v190, v191 offset:160\n\t"
        "s_andn2_b64 exec, s[56:57], vcc\n\t"
        "s_cbranch_execz 40f\n\t"
        "s_or_b64 %0, %0, exec\n\t"
        "global_load_dwordx4 v[240:243], %4, %16 sc1\n\t"
        "40:\n\t"
        "s_mov_b64 exec, s[58:59]\n\t"
        "s_cbranch_execz 41f\n\t"
        "v_bfe_u32 v188, %5, 7, 8\n\t"
        "v_lshrrev_b32 v189, 23, %5\n\t"
        "v_add_u32 v189, 1, v189\n\t"
        "ds_write_b8 v188, v189 offset:256\n\t"
        "v_add_u32 %5, s96, %5\n\t"
        "v_mov_b32 v190, 0\n\t"
        "v_cmpx_eq_u32 vcc, 2, v189\n\t"
        "ds_write_b32 v190, v191 offset:160\n\t"
        "s_andn2_b64 exec, s[58:59], vcc\n\t"
        "s_cbranch_execz 41f\n\t"
        "s_or_b64 %1, %1, exec\n\t"
        "global_load_dwordx4 v[244:247], %5, %16 sc1\n\t"
        "41:\n\t"
        "s_mov_b64 exec, s[60:61]\n\t"
        "s_cbranch_execz 42f\n\t"
        "v_bfe_u32 v188, %6, 7, 8\n\t"
        "v_lshrrev_b32 v189, 23, %6\n\t"
        "v_add_u32 v189, 1, v189\n\t"
        "ds_write_b8 v188, v189 offset:256\n\t"
        "v_add_u32 %6, s96, %6\n\t"
        "v_mov_b32 v190, 0\n\t"
        "v_cmpx_eq_u32 vcc, 2, v189\n\t"
        "ds_write_b32 v190, v191 offset:160\n\t"
        "s_andn2_b64 exec, s[60:61], vcc\n\t"
        "s_cbranch_execz 42f\n\t"
        "s_or_b64 %2, %2, exec\n\t"
        "global_load_dwordx4 v[248:251], %6, %16 sc1\n\t"
        "42:\n\t"
        "s_mov_b64 exec, s[62:63]\n\t"
        "s_cbranch_execz 43f\n\t"
        "v_bfe_u32 v188, %7, 7, 8\n\t"
        "v_lshrrev_b32 v189, 23, %7\n\t"
        "v_add_u32 v189, 1, v189\n\t"
        "ds_write_b8 v188, v189 offset:256\n\t"
        "v_add_u32 %7, s96, %7\n\t"
        "v_mov_b32 v190, 0\n\t"
        "v_cmpx_eq_u32 vcc, 2, v189\n\t"
        "ds_write_b32 v190, v191 offset:160\n\t"
        "s_andn2_b64 exec, s[62:63], vcc\n\t"
        "s_cbranch_execz 43f\n\t"
        "s_or_b64 %3, %3, exec\n\t"
        "global_load_dwordx4 v[252:255], %7, %16 sc1\n\t"
        "43:\n\t"
        "59:\n\t"
        "s_waitcnt lgkmcnt(0)\n\t"
        "s_mov_b64 exec, s[72:73]\n\t"
        "s_cbranch_execz 30f\n\t"
        "v_min_u32 v204, v181, v204\n\t"
        "v_min_u32 v205, v181, v205\n\t"
        "v_min_u32 v206, v181, v206\n\t"
        "v_mad_u32_u24 v204, v192, v180, v204\n\t"
        "v_mad_u32_u24 v205, v193, v180, v205\n\t"
        "v_mad_u32_u24 v206, v194, v180, v206\n\t"
        "ds_write2st64_b32 v204, v216, v228 offset1:10\n\t"
        "s_mov_b64 exec, s[74:75]\n\t"
        "ds_write2st64_b32 v205, v217, v229 offset1:10\n\t"
        "s_mov_b64 exec, s[76:77]\n\t"
        "ds_write2st64_b32 v206, v218, v230 offset1:10\n\t"
        "30:\n\t"
        "s_mov_b64 exec, s[78:79]\n\t"
        "s_cbranch_execz 31f\n\t"
        "v_min_u32 v207, v181, v207\n\t"
        "v_min_u32 v208, v181, v208\n\t"
        "v_min_u32 v209, v181, v209\n\t"
        "v_mad_u32_u24 v207, v195, v180, v207\n\t"
        "v_mad_u32_u24 v208, v196, v180, v208\n\t"
        "v_mad_u32_u24 v209, v197, v180, v209\n\t"
        "ds_write2st64_b32 v207, v219, v231 offset1:10\n\t"
        "s_mov_b64 exec, s[80:81]\n\t"
        "ds_write2st64_b32 v208, v220, v232 offset1:10\n\t"
        "s_mov_b64 exec, s[82:83]\n\t"
        "ds_write2st64_b32 v209, v221, v233 offset1:10\n\t"
        "31:\n\t"
        "s_mov_b64 exec, s[84:85]\n\t"
        "s_cbranch_execz 32f\n\t"
        "v_min_u32 v210, v181, v210\n\t"
        "v_min_u32 v211, v181, v211\n\t"
        "v_min_u32 v212, v181, v212\n\t"
        "v_mad_u32_u24 v210, v198, v180, v210\n\t"
        "v_mad_u32_u24 v211, v199, v180, v211\n\t"
        "v_mad_u32_u24 v212, v200, v180, v212\n\t"
        "ds_write2st64_b32 v210, v222, v234 offset1:10\n\t"
        "s_mov_b64 exec, s[86:87]\n\t"
        "ds_write2st64_b32 v211, v223, v235 offset1:10\n\t"
        "s_mov_b64 exec, s[88:89]\n\t"
        "ds_write2st64_b32 v212, v224, v236 offset1:10\n\t"
        "32:\n\t"
        "s_mov_b64 exec, s[90:91]\n\t"
        "s_cbranch_execz 33f\n\t"
        "v_min_u32 v213, v181, v213\n\t"
        "v_min_u32 v214, v181, v214\n\t"
        "v_min_u32 v215, v181, v215\n\t"
        "v_mad_u32_u24 v213, v201, v180, v213\n\t"
        "v_mad_u32_u24 v214, v202, v180, v214\n\t"
        "v_mad_u32_u24 v215, v203, v180, v215\n\t"
        "ds_write2st64_b32 v213, v225, v237 offset1:10\n\t"
        "s_mov_b64 exec, s[92:93]\n\t"
        "ds_write2st64_b32 v214, v226, v238 offset1:10\n\t"
        "s_mov_b64 exec, s[94:95]\n\t"
        "ds_write2st64_b32 v215, v227, v239 offset1:10\n\t"
        "33:\n\t"
        "s_mov_b64 exec, -1\n\t"
        : "+&s"(pend[0]), "+&s"(pend[1]), "+&s"(pend[2]), "+&s"(pend[3]), "+v"(vo[0]), "+v"(vo[1]), "+v"(vo[2]),
          "+v"(vo[3]), "+{v[240:243]}"(cv[0]), "+{v[244:247]}"(cv[1]), "+{v[248:251]}"(cv[2]), "+{v[252:255]}"(cv[3])
        : "v"(eb[0]), "v"(eb[1]), "v"(eb[2]), "v"(eb[3]), "s"(ib), "n"(OFF_L + LARR - 16), "v"(tag[0]), "v"(tag[1]),
          "v"(rq4), "s"(just)
        :
          "memory", "vcc", "scc", "v177", "v178", "v179", "v180", "v181", "v188", "v189", "v190", "v191", "v192",
          "v193", "v194", "v195", "v196", "v197", "v198", "v199", "v200", "v201", "v202", "v203", "v204", "v205",
          "v206", "v207", "v208", "v209", "v210", "v211", "v212", "v213", "v214", "v215", "v216", "v217", "v218",
          "v219", "v220", "v221", "v222", "v223", "v224", "v225", "v226", "v227", "v228", "v229", "v230", "v231",
          "v232", "v233", "v234", "v235", "v236", "v237", "v238", "v239", "s52", "s53", "s54", "s55", "s56", "s57",
          "s58", "s59", "s60", "s61", "s62", "s63", "s64", "s65", "s66", "s67", "s68", "s69", "s70", "s71", "s72",
          "s73", "s74", "s75", "s76", "s77", "s78", "s79", "s80", "s81", "s82", "s83", "s84", "s85", "s86", "s87",
          "s88", "s89", "s90", "s91", "s92", "s93", "s94", "s95", "s96");
    uni();
    just = 0u;
  }
  *(LAS unsigned*)((LAS char*)smem + OFF_CNT + 128 + wave * 4) = early;
  asm volatile("s_waitcnt lgkmcnt(0)\n\ts_barrier" ::: "memory");
  if (wave >= 4) __builtin_amdgcn_s_setprio(1);
  const long long tmA = __builtin_amdgcn_s_memrealtime();
  const u32x4 cw = *(const LAS u32x4*)((LAS char*)smem + OFF_CNT + rid * 16);
  const unsigned ef = *(const LAS unsigned*)((LAS char*)smem + OFF_CNT + 128 + rid * 4);
  unsigned slowf = *(const LAS unsigned*)((LAS char*)smem + OFF_CNT + 160);
  asm volatile("" : "+v"(slowf));
  const int key = (int)((cw[0] + cw[1] + cw[2] + cw[3]) | rid);
  int gb;
  {
    unsigned acc = 0u;
#define RK(k) acc = __builtin_amdgcn_alignbit(acc, (unsigned)(__builtin_amdgcn_update_dpp(0, key, 0x120 + k, 0xf, 0xf, false) - key), 31)
    RK(1); RK(2); RK(3); RK(4); RK(5); RK(6); RK(7);
#undef RK
    const int want = RANKOF(wave);
    gb = __builtin_ctzll(__ballot(__builtin_popcount(acc) == want)) & 7;
  }
  const unsigned ring0 = lds0 + OFF_R0 + gb * RING_SLOT;
  const unsigned la = l0 + gb * LIST_BYTES;
  bf16x8 XAf[2][2];
#pragma unroll
  for (int q = 0; q < 4; ++q) XAf[q >> 1][q & 1] = *(const LAS bf16x8*)(size_t)(ring0 + ringd + q * 1024 + lane * 16);
  int cnt = (int)(__builtin_amdgcn_readlane(key, gb) - 4u * l0 - 24u) >> 4;
  const unsigned cmn = __builtin_amdgcn_readlane(min(min(cw[0], cw[1]), min(cw[2], cw[3])), gb) & ~15u;
  const unsigned cmx = __builtin_amdgcn_readlane(max(max(cw[0], cw[1]), max(cw[2], cw[3])), gb) & ~15u;
  const bool slow = cnt > LCAP || cmx > l0 + LARR - 16 || __builtin_amdgcn_readfirstlane(slowf) != 0u;
  const unsigned cg = (bu * 8 + gb) * 32;
  aLX += gb * LIST_BYTES; aLE += gb * LIST_BYTES; aLS4 += gb * LIST_BYTES; aLS1 += gb * LIST_BYTES;
  auto issue_gather = [&](int tile, int rs) {
    gather5(ld128(aLX + tile * 128), *(const LAS unsigned*)(size_t)(aLE + tile * 128), ring0 + rs * ringd);
  };
  auto pads = [&](int cnt, int lane) __attribute__((always_inline)) {
    const int u = cnt + lane;
    const unsigned c = cg + ((u >> 2) & 7) + 8 * (u & 3), n = nodeof(c);
    const unsigned padA = n | (__builtin_amdgcn_readlane(c, __builtin_ctzll(__ballot((n * Mw + Cw) >> 16 != c))) & 0xffu) << 16;
    const unsigned pa = la + u * 4;
    *(LAS unsigned*)(size_t)pa = padA;
    *(LAS unsigned*)(size_t)(pa + 256) = padA;
    *(LAS unsigned*)(size_t)(pa + LARR) = 0u;
    *(LAS unsigned*)(size_t)(pa + LARR + 256) = 0u;
  };
  constexpr int GEND = 64 + NCH;
  int gs = slow ? 0 : GEND, gp = 0;
  auto gen = [&]() __attribute__((always_inline)) {
    int l2, n = 0;
    asm volatile("v_mbcnt_lo_u32_b32 %0, -1, 0\n\tv_mbcnt_hi_u32_b32 %0, -1, %0" : "=v"(l2));
    const LAS unsigned char* xf = (const LAS unsigned char*)smem + OFF_XF;
    auto put = [&](bool ok, unsigned t, unsigned c, int cc) {
      ok = ok && (int)(c >> 5) == gb;
      const unsigned long long bm = __ballot(ok);
      const unsigned pa = la + 4u * (n + __builtin_amdgcn_mbcnt_hi((unsigned)(bm >> 32), __builtin_amdgcn_mbcnt_lo((unsigned)bm, 0u)));
      if (ok) {
        *(LAS unsigned*)(size_t)pa = (t & 0xffffu) | (c << 16);
        *(LAS unsigned*)(size_t)(pa + LARR) = ((unsigned)(cc * chunk) + (t >> 16)) * 32u;
      }
      n += __builtin_popcountll(bm);
    };
#pragma unroll 1
    while (gs < GEND && n <= LCAP - 192) {
      if (gs < 64) {
        const int cc = (gs & 31) * 8 + (l2 >> 3);
        const unsigned* pp = reinterpret_cast<const unsigned*>(ib + (gs >> 5) * IEXT + (gs & 31) * 1024 + l2 * 16);
        const bool live = gs < 32 || xf[cc] != 0;
        const unsigned m = ldd(pp + 3);
#pragma unroll 1
        for (int j = 0; j < 3; ++j) {
          const unsigned t = ldd(pp + j);
          put(live && t != ~0u && (j < 2 || (l2 & 7) != 7), t, (m >> (8 * j)) & 0xffu, cc);
        }
        ++gs;
      } else {
        const int c2 = gs - 64 + l2;
        const unsigned long long fm = __ballot(c2 < NCH && xf[min(c2, NCH - 1)] == 2);
        if (fm == 0ull) { gs = min(gs + 64, GEND); continue; }
        gs += __builtin_ctzll(fm);
        const int cc = gs - 64;
        const unsigned long long ds = ldq(FL + 512 + bu * NCH + cc);
        const int len = __builtin_amdgcn_readfirstlane((int)(ds >> 32));
        const unsigned long long* tp = TS + (unsigned)ds;
#pragma unroll 1
        for (; gp < len && n <= LCAP - 64; gp += 64) {
          const unsigned long long t = ldq(tp + min(gp + l2, len - 1));
          put(gp + l2 < len, (unsigned)t, (unsigned)(t >> 32), cc);
        }
        if (gp >= len) { gp = 0; ++gs; }
      }
    }
    __builtin_amdgcn_s_waitcnt(0x0F70);
    pads(n, l2);
    return n;
  };
  const bool quick = !slow && cmn >= l0 + 256u && __builtin_amdgcn_readlane(ef, gb) != 0u;
  auto gather1 = [&](unsigned skip) {
    u32x4 sx = ld128(aLX + 128);
    unsigned re = *(const LAS unsigned*)(size_t)(aLE + 128);
    asm volatile("s_cmp_lg_u32 %16, 0\n\t"
                 "s_cbranch_scc1 1f\n\t"
                 "s_mov_b32 m0, %14\n\t"
                 "v_mad_u32_u16 %0, %0, %15, %9\n\t"
                 "v_mad_u32_u16 %1, %1, %15, %10\n\t"
                 "global_load_lds_dwordx4 %0, %12\n\t"
                 "s_addk_i32 m0, 1024\n\t"
                 "v_mad_u32_u16 %2, %2, %15, %9\n\t"
                 "global_load_lds_dwordx4 %1, %12\n\t"
                 "s_addk_i32 m0, 1024\n\t"
                 "v_mad_u32_u16 %3, %3, %15, %10\n\t"
                 "global_load_lds_dwordx4 %2, %12\n\t"
                 "s_addk_i32 m0, 1024\n\t"
                 "v_add_u32 %4, %4, %11\n\t"
                 "global_load_lds_dwordx4 %3, %12\n\t"
                 "s_addk_i32 m0, 1024\n\t"
                 "s_nop 0\n\t"
                 "global_load_lds_dwordx4 %4, %13\n"
                 "1:"
                 : "+v"(sx[0]), "+v"(sx[1]), "+v"(sx[2]), "+v"(sx[3]), "+v"(re), "+v"(XAf[0][0]), "+v"(XAf[0][1]),
                   "+v"(XAf[1][0]), "+v"(XAf[1][1])
                 : "v"(cx0), "v"(cx1), "v"(ce), "s"(x), "s"(ES), "s"(ring0 + ringd), "s"(128u), "s"(skip)
                 : "memory", "m0", "scc");
  };
  const unsigned g1 = __builtin_amdgcn_readfirstlane(quick && wave >= G1W ? 1u : 0u);
  gather1(g1 ^ 1u);
  if (__builtin_expect(!slow, 1)) {
    const unsigned cl = *(const LAS unsigned*)((LAS char*)smem + OFF_CNT + gb * 16 + (lane & 3) * 4);
    unsigned q = cmn + gb * LIST_BYTES;
#pragma unroll 1
    for (unsigned i = cmn; i < cmx; i += 256u) {
      const unsigned pp = i + lane * 4u;
      const unsigned ea = *(const LAS unsigned*)(size_t)(pp + gb * LIST_BYTES);
      const unsigned eb_ = *(const LAS unsigned*)(size_t)(pp + gb * LIST_BYTES + LARR);
      const unsigned long long bm = __ballot(pp < cl);
      const unsigned pa = q + 4u * __builtin_amdgcn_mbcnt_hi((unsigned)(bm >> 32), __builtin_amdgcn_mbcnt_lo((unsigned)bm, 0u));
      if (pp < cl) {
        *(LAS unsigned*)(size_t)pa = ea;
        *(LAS unsigned*)(size_t)(pa + LARR) = eb_;
      }
      q += 4u * __builtin_popcountll(bm);
    }
    pads(cnt, lane);
  }
#pragma unroll
  for (int ct = 0; ct < 2; ++ct)
#pragma unroll
    for (int m = 6; m < 15; ++m)
      if ((HELD >> m) & 1) wr[ct][m] = wl[widx(m, ct) * 64];
#pragma unroll
  for (int ct = 0; ct < 2; ++ct)
#pragma unroll
    for (int m = 6; m < 15; ++m)
      if ((HELD >> m) & 1) asm volatile("" : "+v"(wr[ct][m]));
#pragma unroll
  for (int t = 0; t < 4; ++t) fo[t] += gb * RING_SLOT;
  foe += gb * RING_SLOT;
  cS -= gb * 32 * TPITCH;
  cSt -= gb * 32 * TPITCH;
  asm volatile("s_waitcnt vmcnt(5)\n\ts_cmp_lg_u32 %7, 0\n\ts_cbranch_scc1 1f\n\ts_waitcnt vmcnt(0)\n1:\n\ts_barrier"
               : "+v"(fo[0]), "+v"(fo[1]), "+v"(fo[2]), "+v"(fo[3]), "+v"(foe), "+v"(cS), "+v"(cSt) : "s"(g1) : "memory", "scc");
  bool redo = !quick;
  gather1(__builtin_amdgcn_readfirstlane(quick ? g1 : 1u));
  const int gbin = bk * 8 + wave;
  auto rd128 = [&](unsigned a) { return *(const LAS bf16x8*)(size_t)a; };
  auto trow = [&](unsigned a, unsigned c) {
    unsigned t;
    asm("v_mad_u32_u16 %0, %1, %2, %3 op_sel:[1,0,0,0]" : "=v"(t) : "v"(a), "s"(TPITCH), "v"(c));
    return t;
  };
  auto load_S = [&](const u32x4 ss) {
    const s16x4 a0 = __builtin_amdgcn_ds_read_tr16_b64_v4i16((LAS s16x4*)(size_t)trow(ss[0], cS));
    const s16x4 a1 = __builtin_amdgcn_ds_read_tr16_b64_v4i16((LAS s16x4*)(size_t)trow(ss[1], cS));
    const s16x4 a2 = __builtin_amdgcn_ds_read_tr16_b64_v4i16((LAS s16x4*)(size_t)trow(ss[2], cS));
    const s16x4 a3 = __builtin_amdgcn_ds_read_tr16_b64_v4i16((LAS s16x4*)(size_t)trow(ss[3], cS));
    S[0] = __builtin_shufflevector(a0, a1, 0, 1, 2, 3, 4, 5, 6, 7);
    S[1] = __builtin_shufflevector(a2, a3, 0, 1, 2, 3, 4, 5, 6, 7);
  };
  auto wf = [&](int m, int ct) { return ((HELD >> m) & 1) ? wr[ct][m] : b[(m - 6) % 3]; };

  auto ew = [&](int m, const f32x16& Gc, const f32x16& Vc, const f32x16& Gn, const f32x16& Vp) {
    if (m < 2) {
      const int i = 14 + m;
      pq[i] = mulf(i, Vp[i], rq[i]);
      if (m == 1) pn[1][3] = pack_bf16(pq[14], pq[15]);
    } else if (m == 2) {
      pq[0] = mulf(0, Vc[0], rq[0]);
      pq[1] = mulf(1, Vc[1], rq[1]);
      pn[0][0] = pack_bf16(pq[0], pq[1]);
    } else {
      const int i = m - 1;
      pq[i] = mulf(i, Vc[i], rq[i]);
      if (i & 1) pn[i >> 3][(i >> 1) & 3] = pack_bf16(pq[i - 1], pq[i]);
    }
    if (m == 0) pd[0][0] = pack_bf16(rq[0], rq[1]);
    else if ((m & 1) == 0) { const int i = m + 1; pd[i >> 3][(i >> 1) & 3] = pack_bf16(rq[i - 1], rq[i]); }
    if (m < 14) {
      const int i = m + 2;
      rq[i] = __builtin_amdgcn_rcpf(add1(tq[i]));
    } else {
      rq[0] = __builtin_amdgcn_rcpf(add1(tq[0]));
      rq[1] = __builtin_amdgcn_rcpf(add1(tq[1]));
    }
    if (m < 13) tq[m + 3] = __builtin_amdgcn_exp2f(Gc[m + 3]);
    else if (m == 13) { tq[0] = __builtin_amdgcn_exp2f(Gn[0]); tq[1] = __builtin_amdgcn_exp2f(Gn[1]); }
    else tq[2] = __builtin_amdgcn_exp2f(Gn[2]);
  };

  auto drain = [&]() {
    pq[14] = mulf(0, V1[14], rq[14]);
    pq[15] = mulf(1, V1[15], rq[15]);
    pn[1][3] = pack_bf16(pq[14], pq[15]);
    Y[1] = MFMA32(__builtin_bit_cast(bf16x8, pd[0]), S[0], Y[1]);
    Y[3] = MFMA32(__builtin_bit_cast(bf16x8, pn[0]), S[0], Y[3]);
    Y[1] = MFMA32(__builtin_bit_cast(bf16x8, pd[1]), S[1], Y[1]);
    Y[3] = MFMA32(__builtin_bit_cast(bf16x8, pn[1]), S[1], Y[3]);
  };
  long long tm1 = 0, c1 = 0;
  const long long tmB = __builtin_amdgcn_s_memrealtime();
  while (true) {
    if (__builtin_expect(redo, 0)) {
      asm volatile("s_waitcnt vmcnt(0)" ::: "memory");
      if (slow) cnt = gen();
      issue_gather(0, 0);
      issue_gather(1, 1);
    }
    const int ntiles = (cnt + 31) >> 5;
    {
      const unsigned sl = *(const LAS unsigned short*)(size_t)aLS1;
      bf16x8 w0[15];
#pragma unroll
      for (int m = 6; m < 15; ++m) w0[m] = ((HELD >> m) & 1) ? wr[0][m] : wl[widx(m, 0) * 64];
      u32x4 sx = ld128(aLX + 256);
      unsigned re = *(const LAS unsigned*)(size_t)(aLE + 256);
      St[0] = rd128(sl * TPITCH + cSt);
      St[1] = rd128(sl * TPITCH + cSt + 16);
      G0 = MFMA32(St[0], XAf[0][0], zero16);
      G0 = MFMA32(St[1], XAf[1][0], G0);
      pipe_zero();
#pragma unroll
      for (int m = 6; m < 15; ++m)
        if (!((HELD >> m) & 1)) asm volatile("" : "+v"(w0[m]));
      asm volatile("s_waitcnt vmcnt(5)" : "+v"(sx), "+v"(re), "+v"(G0) :: "memory");
#pragma unroll
      for (int k = 0; k < 4; ++k) as[k] = rd128(fo[k]);
      ae = rd128(foe);
      SB();
#pragma unroll
      for (int k = 0; k < 4; ++k) G0 = MFMA32(as[k], w0[7 + k], G0);
      G0 = MFMA32(ae, w0[6], G0);
      V0 = MFMA32(as[0], w0[11], zero16);
      SB();
      glds16x(x, sx[0], cx0, ring0);
      glds16x(x, sx[1], cx1, ring0 + 1024);
      V0 = MFMA32(as[1], w0[12], V0);
      SB();
      glds16x(x, sx[2], cx0, ring0 + 2048);
      tq[0] = __builtin_amdgcn_exp2f(G0[0]);
      tq[1] = __builtin_amdgcn_exp2f(G0[1]);
      V0 = MFMA32(as[2], w0[13], V0);
      SB();
      glds16x(x, sx[3], cx1, ring0 + 3072);
      tq[2] = __builtin_amdgcn_exp2f(G0[2]);
      V0 = MFMA32(as[3], w0[14], V0);
      SB();
      glds16e(ES, re, ce, ring0 + 4096);
      rq[0] = __builtin_amdgcn_rcpf(add1(tq[0]));
      rq[1] = __builtin_amdgcn_rcpf(add1(tq[1]));
      SB();
    }
    tm1 = __builtin_amdgcn_s_memrealtime(); c1 = __builtin_amdgcn_s_memtime(); __builtin_amdgcn_s_waitcnt(0xC07F);
    unsigned &lS4 = aLS4, &lS1 = aLS1, &lX = aLX, &lE = aLE;
    int adv = 0;
    auto body = [&](const int par) {
      const unsigned lo = par * 128;
      const unsigned ro = (1 - par) * ringd;
      {
        u32x4 ss;
        unsigned sl;
#pragma unroll
        for (int m = 0; m < 15; ++m) {
          if (m == 0) Y[1] = MFMA32(__builtin_bit_cast(bf16x8, pd[0]), S[0], Y[1]);
          else if (m == 1) Y[3] = MFMA32(__builtin_bit_cast(bf16x8, pn[0]), S[0], Y[3]);
          else if (m == 2) Y[1] = MFMA32(__builtin_bit_cast(bf16x8, pd[1]), S[1], Y[1]);
          else if (m == 3) Y[3] = MFMA32(__builtin_bit_cast(bf16x8, pn[1]), S[1], Y[3]);
          else if (m == 4) G1 = MFMA32(St[0], XAf[0][1], zero16);
          else if (m == 5) G1 = MFMA32(St[1], XAf[1][1], G1);
          else if (m == 6) G1 = MFMA32(ae, wf(m, 1), G1);
          else if (m < 11) G1 = MFMA32(as[m - 7], wf(m, 1), G1);
          else if (m == 11) V1 = MFMA32(as[0], wf(m, 1), zero16);
          else V1 = MFMA32(as[m - 11], wf(m, 1), V1);
          SB();
          if (m == 0) { ss = ld128(lS4 + lo); sl = *(const LAS unsigned short*)(size_t)(lS1 + lo + 128); }
          if (m >= 3 && m < 12 && !((HELD >> (m + 3)) & 1)) b[(m - 3) % 3] = wl[widx(m + 3, 1) * 64];
          if (m == 4) load_S(ss);
          if (m == 5) { St[0] = rd128(sl * TPITCH + cSt); St[1] = rd128(sl * TPITCH + cSt + 16); }
          if (m == 6) { asm volatile("s_waitcnt vmcnt(5)" ::: "memory"); ae = rd128(foe + ro); }
          if (m >= 11) as[m - 11] = rd128(fo[m - 11] + ro);
          ew(m, G0, V0, G1, V1);
          SB();
        }
      }
      {
        u32x4 sx;
        unsigned re;
#pragma unroll
        for (int m = 0; m < 15; ++m) {
          if (m == 0) Y[0] = MFMA32(__builtin_bit_cast(bf16x8, pd[0]), S[0], Y[0]);
          else if (m == 1) Y[2] = MFMA32(__builtin_bit_cast(bf16x8, pn[0]), S[0], Y[2]);
          else if (m == 2) Y[0] = MFMA32(__builtin_bit_cast(bf16x8, pd[1]), S[1], Y[0]);
          else if (m == 3) Y[2] = MFMA32(__builtin_bit_cast(bf16x8, pn[1]), S[1], Y[2]);
          else if (m == 4) G0 = MFMA32(St[0], XAf[0][0], zero16);
          else if (m == 5) G0 = MFMA32(St[1], XAf[1][0], G0);
          else if (m == 6) G0 = MFMA32(ae, wf(m, 0), G0);
          else if (m < 11) G0 = MFMA32(as[m - 7], wf(m, 0), G0);
          else if (m == 11) V0 = MFMA32(as[0], wf(m, 0), zero16);
          else V0 = MFMA32(as[m - 11], wf(m, 0), V0);
          SB();
          if (m == 4) { sx = ld128(lX + lo + 3 * 128); re = *(const LAS unsigned*)(size_t)(lE + lo + 3 * 128); }
          if (m >= 3 && m < 12 && !((HELD >> (m + 3)) & 1)) b[(m - 3) % 3] = wl[widx(m + 3, 0) * 64];
          if (m == 7) glds16x(x, sx[0], cx0, ring0 + ro);
          if (m == 8) glds16x(x, sx[1], cx1, ring0 + ro + 1024);
          if (m == 9) glds16x(x, sx[2], cx0, ring0 + ro + 2048);
          if (m == 10) glds16x(x, sx[3], cx1, ring0 + ro + 3072);
          if (m == 11) glds16e(ES, re, ce, ring0 + ro + 4096);
          ew(m, G1, V1, G0, V0);
          SB();
        }
      }
    };
    for (int t = 0; t < ntiles; t += 2) {
      asm volatile("s_cmp_lt_u32 %0, 4\n\ts_cbranch_scc1 1f\n\ts_setprio 1\n1:" :: "s"(wave) : "scc");
      body(0);
      if (t + 1 >= ntiles) break;
      __builtin_amdgcn_s_setprio(0);
      body(1);
      lS4 += 256; lS1 += 256; lX += 256; lE += 256;
      asm volatile("" : "+v"(lS4), "+v"(lS1), "+v"(lX), "+v"(lE));
      adv += 256;
    }
    if (__builtin_expect(gs >= GEND, 1)) break;
    drain();
    lS4 -= adv; lS1 -= adv; lX -= adv; lE -= adv;
    redo = true;
  }
  const long long tm2 = __builtin_amdgcn_s_memrealtime(); const long long c2 = __builtin_amdgcn_s_memtime();

  {
    int lane;
    asm volatile("v_mbcnt_lo_u32_b32 %0, -1, 0\n\tv_mbcnt_hi_u32_b32 %0, -1, %0" : "=v"(lane));
    const int h = lane >> 5;
    const unsigned tu = lds0 + OFF_W + 18 * 1024 + h * 4096 + ((lane >> 2) & 3) * 128 + (lane & 1) * 64 +
                        ((lane >> 4) & 1) * 16 + ((lane >> 1) & 1) * 8;
    bf16x8 wu[8];
#pragma unroll
    for (int k = 0; k < 8; ++k) wu[k] = rawfrag(tu + (k & 3) * 1024 + (k >> 2) * 32);
    SB();
    drain();
    f32x16 X[2], U[2];
#pragma unroll
    for (int i = 0; i < 16; ++i) {
      const unsigned xv = __builtin_bit_cast(u32x4, as[i >> 3])[(i >> 1) & 3];
      X[0][i] = __uint_as_float(i & 1 ? xv & 0xffff0000u : xv << 16);
    }
    SB();
    asm volatile("s_nop 0\n\tv_mfma_f32_32x32x16_bf16 %0, %1, %2, %3" : "=&v"(U[0]) : "v"(wu[0]), "v"(as[0]), "v"(X[0]));
#pragma unroll
    for (int k = 1; k < 4; ++k) U[0] = MFMA32(wu[k], as[k], U[0]);
#pragma unroll
    for (int i = 0; i < 16; ++i) {
      const unsigned xv = __builtin_bit_cast(u32x4, as[2 + (i >> 3)])[(i >> 1) & 3];
      X[1][i] = __uint_as_float(i & 1 ? xv & 0xffff0000u : xv << 16);
    }
    const unsigned nd = nodeof(cg + (lane & 31));
    const bool live = (nd * Mw + Cw) >> 16 == cg + (lane & 31);
    const unsigned lm = (unsigned)__ballot(live);
    const int nodes = __builtin_popcount(lm);
    const unsigned n0 = __builtin_amdgcn_readlane(nd, __builtin_ctz(lm | 0x80000000u));
    const int r = live ? nd - n0 : 31;
    SB();
    asm volatile("s_nop 0\n\tv_mfma_f32_32x32x16_bf16 %0, %1, %2, %3" : "=&v"(U[1]) : "v"(wu[4]), "v"(as[0]), "v"(X[1]));
#pragma unroll
    for (int k = 1; k < 4; ++k) U[1] = MFMA32(wu[4 + k], as[k], U[1]);
#pragma unroll
    for (int i = 0; i < 16; ++i) Y[0][i] = __builtin_amdgcn_rcpf(Y[0][i]);
    SB();
#pragma unroll
    for (int ct = 0; ct < 2; ++ct) {
#pragma unroll
      for (int q = 0; q < 2; ++q) {
        u32x4 ov;
#pragma unroll
        for (int j = 0; j < 4; ++j) {
          const int i = 8 * q + 2 * j;
          if (ct == 0) { Y[1][i] = __builtin_amdgcn_rcpf(Y[1][i]); Y[1][i + 1] = __builtin_amdgcn_rcpf(Y[1][i + 1]); }
          const f32x2 z = __builtin_elementwise_fma(f32x2{Y[2 + ct][i], Y[2 + ct][i + 1]}, f32x2{Y[ct][i], Y[ct][i + 1]},
                                                    f32x2{U[ct][i], U[ct][i + 1]});
          ov[j] = pack_bf16(maxf(z[0], X[ct][i]), maxf(z[1], X[ct][i + 1]));
        }
        *(LAS u32x4*)(size_t)(la + r * 128 + (((4 * h + 2 * ct + q) ^ (r & 7)) * 16)) = ov;
      }
    }
    u32x4 ov[4];
#pragma unroll
    for (int q = 0; q < 4; ++q)
      ov[q] = *(const LAS u32x4*)(size_t)(la + (8 * q + (lane >> 3)) * 128 + (((lane & 7) ^ (lane >> 3)) * 16));
    asm volatile("v_cmpx_lt_i32 vcc, 0, %6\n\t"
                 "global_store_dwordx4 %0, %1, %5 sc1\n\t"
                 "v_cmpx_lt_i32 vcc, 8, %6\n\t"
                 "global_store_dwordx4 %0, %2, %5 offset:1024 sc1\n\t"
                 "v_cmpx_lt_i32 vcc, 16, %6\n\t"
                 "global_store_dwordx4 %0, %3, %5 offset:2048 sc1\n\t"
                 "v_cmpx_lt_i32 vcc, 24, %6\n\t"
                 "global_store_dwordx4 %0, %4, %5 offset:3072 sc1\n\t"
                 "s_mov_b64 exec, -1"
                 :: "v"((unsigned)n0 * 128u + lane * 16u), "v"(ov[0]), "v"(ov[1]), "v"(ov[2]), "v"(ov[3]), "s"(out),
                    "v"(nodes - (lane >> 3))
                 : "memory", "vcc");
  }
  if (bk == 0 && wave == 7) asm volatile("global_store_dwordx2 %0, %1, %2 sc1" :: "v"(0u), "v"(ep), "s"(FL) : "memory");
  asm volatile("s_waitcnt vmcnt(0)" ::: "memory");
  const long long tm3 = __builtin_amdgcn_s_memrealtime();
  if (__builtin_amdgcn_mbcnt_hi(-1, __builtin_amdgcn_mbcnt_lo(-1, 0)) == 0) { const int g = gbin; if (wave == 0) (dbg - (16 << 20))[5000 + bk] = tm0; dbg[g*8+0]=tm0; dbg[g*8+1]=tm1; dbg[g*8+2]=tm2; dbg[g*8+3]=tm3; dbg[g*8+4]=(cnt+31)>>5; dbg[g*8+5]=c2-c1; dbg[g*8+6]=tmA-tm0; dbg[g*8+7]=tmB-tm0;
  }
}

__global__ void k_dbg(const long long* dbg, int ng) {
  __shared__ long long sh[4][256];
  __shared__ long long vv[2048];
  const int t = threadIdx.x;
  const long long* pd = dbg + (32u << 17);
  const long long* d2 = dbg + 4096 * 8;
  long long pmin = 1ll << 62, sum = 0, mx = 0, cn = 0;
  const int md = QSEL / 100;
  for (int g = t; g < ng; g += 256) {
    const long long* d = dbg + g * 8;
    const long long* p = pd + ((g >> 3) * 16 + (g & 7)) * 8;
    const long long* e = d2 + g * 8;
    pmin = min(pmin, d[0]);
    long long v = 0;
    const int k = QSEL % 100;
    if (k < 7) v = p[k];
    else if (k < 16) v = e[k - 10] + d[0];
    else if (k == 17) v = d[6] + d[0];
    else if (k == 18) v = d[1];
    else if (k == 19) v = d[2];
    else if (k == 20) v = d[3];
    else if (k == 21) v = e[7];
    else if (k == 22) v = d[5] * 100 / (d[2] - d[1]);
    else if (k == 23) v = d[6];
    else if (k == 16) v = d[0];
    const int w = g & 7;
    const bool use = (md == 2 || md == 4) ? w == 7 : md == 3 ? w < 7 : md == 6 ? w >= 4 : md == 7 ? w < 4 : true;
    vv[g] = v;
    if (use) { sum += v; mx = max(mx, v); ++cn; }
  }
  sh[0][t] = pmin; sh[1][t] = sum; sh[2][t] = mx; sh[3][t] = cn;
  __syncthreads();
  __shared__ long long pc;
  if (md == 5 || md == 8) {
    const int want = md == 5 ? ng * 9 / 10 : ng * 97 / 100;
    for (int g = t; g < ng; g += 256) {
      int lo = 0, eq = 0;
      for (int i = 0; i < ng; ++i) { lo += vv[i] < vv[g]; eq += vv[i] == vv[g]; }
      if (lo <= want && want < lo + eq) pc = vv[g];
    }
    __syncthreads();
  }
  if (t == 0) {
    for (int i = 1; i < 256; ++i) { pmin = min(pmin, sh[0][i]); sum += sh[1][i]; mx = max(mx, sh[2][i]); cn += sh[3][i]; }
    long long q = (md == 1 || md == 3 || md == 4) ? mx : (md == 5 || md == 8) ? pc : sum / cn;
    if (QSEL % 100 <= 20) q -= pmin;
    if (QSEL == 99) q = 0;
    q = max(0ll, min(q, 5000ll));
    const long long tend = pmin + 10000 + 4 * q;
    for (int i = 0; i < 1000000; ++i) { if (__builtin_amdgcn_s_memrealtime() >= tend) break; __builtin_amdgcn_s_sleep(1); }
  }
}

extern "C" void kernel_launch(void* const* d_in, const int* in_sizes, int n_in,
                              void* d_out, int out_size, void* d_ws, size_t ws_size,
                              hipStream_t stream) {
  const unsigned short* x = (const unsigned short*)d_in[0];
  const u32x4* eattr = (const u32x4*)d_in[1];
  const int* src = (const int*)d_in[2];
  const int* dst = (const int*)d_in[3];
  const unsigned short* wa = (const unsigned short*)d_in[4];
  const unsigned short* wb = (const unsigned short*)d_in[5];
  const unsigned short* wc = (const unsigned short*)d_in[6];
  const unsigned short* wu = (const unsigned short*)d_in[7];
  const unsigned short* wv = (const unsigned short*)d_in[8];
  unsigned short* out = (unsigned short*)d_out;
  const int N = in_sizes[0] / 64;
  const int E = in_sizes[2];
  const int chunk = (((E + NCH - 1) / NCH) + 3) & ~3;

  const bool skew = N > 4096 && N < 60000;
  const unsigned M = N > 256 ? (unsigned)(4294967295.0 / ((double)N * (skew ? 1.0 + SKEWF : 1.0))) : 0xffffffu;
  const unsigned N1 = (0x7fffffffu / M) + 1u;
  const unsigned M2 = skew ? (0xffffffffu - N1 * M) / ((unsigned)N - N1) : M;

  auto fb = [](double v) { return __builtin_bit_cast(unsigned, (float)v); };
  char* ws = (char*)d_ws;
  size_t off = 0;
  off = 1 << 20;
  unsigned long long* FL = (unsigned long long*)(ws + off);   off += 1 << 20;
  u32x4* ENT = (u32x4*)(ws + off);                       off += (size_t)2 * NBK * IBOX;
  unsigned long long* TS = (unsigned long long*)(ws + off);

  k_main<<<NBK, 512, 0, stream>>>(x, eattr, src, dst, wa, wb, wc, wu, wv, ENT, TS, FL, out, N, E, chunk, M, N1,
                                  M2 - M, fb(65536.0 / M), fb(65536.0 / M2), fb(32768.0 / M + 0.5),
                                  fb((double)N1 + (32768.0 - (double)N1 * M) / M2 + 0.5)
     , (long long*)(ws + (128u<<20))
     );
  k_dbg<<<1, 256, 0, stream>>>((const long long*)(ws + (128u<<20)), 2048);
}
```

```cpp
#include <hip/hip_runtime.h>
#include <hip/hip_bf16.h>
#include <stdint.h>

using bf16x8 = __attribute__((ext_vector_type(8))) short;
using s16x4 = __attribute__((ext_vector_type(4))) short;
using f32x16 = __attribute__((ext_vector_type(16))) float;
using f32x2 = __attribute__((ext_vector_type(2))) float;
using u32x4 = __attribute__((ext_vector_type(4))) unsigned;
using u32x2 = __attribute__((ext_vector_type(2))) unsigned;
using i32x4 = __attribute__((ext_vector_type(4))) int;
using u16x2 = __attribute__((ext_vector_type(2))) unsigned short;

#define MFMA32(a, b, c) __builtin_amdgcn_mfma_f32_32x32x16_bf16((a), (b), (c), 0, 0, 0)
#define LAS __attribute__((address_space(3)))
#define RANKOF(w) ((w) < 4 ? 7 - (w) : (w) - 4)
#define SLEEP "s_sleep 2\n\t"
#define NREP 8
#define REPOF(b) ((b) % NREP)
#define POLLAT 2
#define PRE 0
#define PREPOL "sc1"
#define G1W 4
#define DBGWAIT 0
#define XWARM 0
#define XAT 3
#define CPF 32
#define CPFB 18688
#define WEARLY 1
#define FR 0
#define XAP 0
#define TPOLL 0
#define SKEWF 0.015
#define PFAT 1
#define TW 0
#define TWN 256
#define SRCLATE 1
#define XGAT 0
#define SB() __builtin_amdgcn_sched_barrier(0)

constexpr int NCH = 256;
constexpr int NBK = 256;
constexpr int NBIN = NBK * 8;
constexpr int NFRAG = 39;
constexpr int CHMAX = 3200;
constexpr float NLOG2E = -1.44269504088896340736f;
constexpr unsigned TPITCH = 80;

__device__ __forceinline__ unsigned pack_bf16(float lo, float hi) {
  typedef __attribute__((ext_vector_type(2))) float f2;
  typedef __attribute__((ext_vector_type(2))) __bf16 b2;
  f2 v = {lo, hi};
  b2 r = __builtin_convertvector(v, b2);
  return __builtin_bit_cast(unsigned, r);
}

__device__ __forceinline__ int wave_scan(int v) {
  v += __builtin_amdgcn_update_dpp(0, v, 0x111, 0xf, 0xf, false);
  v += __builtin_amdgcn_update_dpp(0, v, 0x112, 0xf, 0xf, false);
  v += __builtin_amdgcn_update_dpp(0, v, 0x114, 0xf, 0xf, false);
  v += __builtin_amdgcn_update_dpp(0, v, 0x118, 0xf, 0xf, false);
  v += __builtin_amdgcn_update_dpp(0, v, 0x142, 0xa, 0xf, false);
  v += __builtin_amdgcn_update_dpp(0, v, 0x143, 0xc, 0xf, false);
  return v;
}

__device__ __forceinline__ void glds16(const void* sbase, unsigned voff, unsigned lds_dst) {
  asm volatile(
      "s_mov_b32 m0, %2\n\t"
      "s_nop 0\n\t"
      "global_load_lds_dwordx4 %0, %1"
      :: "v"(voff), "s"(sbase), "s"(lds_dst) : "memory", "m0");
}
__device__ __forceinline__ void glds16nt(const void* sbase, unsigned voff, unsigned lds_dst) {
  asm volatile(
      "s_mov_b32 m0, %2\n\t"
      "s_nop 0\n\t"
      "global_load_lds_dwordx4 %0, %1 nt"
      :: "v"(voff), "s"(sbase), "s"(lds_dst) : "memory", "m0");
}
__device__ __forceinline__ void glds16x(const void* sbase, unsigned a, unsigned c, unsigned lds_dst) {
  unsigned tmp;
  asm volatile(
      "s_mov_b32 m0, %4\n\t"
      "v_mad_u32_u16 %0, %1, %5, %2\n\t"
      "global_load_lds_dwordx4 %0, %3"
      : "=&v"(tmp) : "v"(a), "v"(c), "s"(sbase), "s"(lds_dst), "s"(128u) : "memory", "m0");
}
__device__ __forceinline__ void glds16e(const void* sbase, unsigned a, unsigned c, unsigned lds_dst) {
  unsigned tmp;
  asm volatile(
      "s_mov_b32 m0, %4\n\t"
      "v_add_u32 %0, %1, %2\n\t"
      "global_load_lds_dwordx4 %0, %3"
      : "=&v"(tmp) : "v"(a), "v"(c), "s"(sbase), "s"(lds_dst) : "memory", "m0");
}
__device__ __forceinline__ int gload32(const void* sbase, unsigned voff) {
  int r;
  asm volatile("global_load_dword %0, %1, %2" : "=v"(r) : "v"(voff), "s"(sbase) : "memory");
  return r;
}
__device__ __forceinline__ u32x4 gload128(const void* sbase, unsigned voff) {
  u32x4 r;
  asm volatile("global_load_dwordx4 %0, %1, %2" : "=v"(r) : "v"(voff), "s"(sbase) : "memory");
  return r;
}

__device__ __forceinline__ u32x4 gload128nt(const void* sbase, unsigned voff) {
  u32x4 r;
  asm volatile("global_load_dwordx4 %0, %1, %2 nt" : "=v"(r) : "v"(voff), "s"(sbase) : "memory");
  return r;
}
__device__ __forceinline__ void gstore128wt(void* sbase, unsigned voff, u32x4 v) {
  asm volatile("global_store_dwordx4 %0, %1, %2 sc1\n\ts_nop 1" :: "v"(voff), "v"(v), "s"(sbase) : "memory");
}

constexpr int IBOX = 32768;
constexpr int IEXT = 256 * IBOX;
constexpr int CPIT = 208;
constexpr unsigned RINC = 87382;
constexpr int KP_HDR = 0;
constexpr int KP_ENT = KP_HDR + NBK * 4;
constexpr int KP_POS = KP_ENT + NBK * CPIT;
constexpr int KP_LDS = KP_POS + NBK * 4;
constexpr int NWL = 26;
constexpr int NBLOB = 31;

__device__ __forceinline__ unsigned mad24(unsigned a, int b, unsigned c) {
  unsigned r;
  asm("v_mad_i32_i24 %0, %1, %2, %3" : "=v"(r) : "v"(a), "s"(b), "v"(c));
  return r;
}
__device__ __forceinline__ unsigned mul24(unsigned a, unsigned b) {
  unsigned r;
  asm("v_mul_u32_u24 %0, %1, %2" : "=v"(r) : "n"(b), "v"(a));
  return r;
}
__device__ __forceinline__ unsigned mul24s(unsigned a, unsigned b) {
  unsigned r;
  asm("v_mul_u32_u24 %0, %1, %2" : "=v"(r) : "s"(b), "v"(a));
  return r;
}
__device__ __forceinline__ unsigned byte3x(unsigned a, unsigned b) {
  unsigned r;
  asm("v_mul_u32_u24_sdwa %0, %1, %2 dst_sel:DWORD dst_unused:UNUSED_PAD src0_sel:DWORD src1_sel:BYTE_3" : "=v"(r) : "s"(b), "v"(a));
  return r;
}
__device__ __forceinline__ unsigned mulhi24(unsigned a, unsigned b) {
  return (unsigned)(((unsigned long long)(a & 0xffffffu) * (unsigned long long)b) >> 32);
}
__device__ __forceinline__ unsigned dot4(unsigned a, unsigned b, unsigned c) {
  return __builtin_amdgcn_udot4(a, b, c, false);
}

constexpr int RING_SLOT = 5 * 1024;
constexpr int LCAP = 512;
constexpr int LARR = 2560;
constexpr int LIST_BYTES = 2 * LARR;
constexpr int OFF_CNT = 0;
constexpr int OFF_XF = 256;
constexpr int OFF_W = 512;
constexpr int OFF_T1 = OFF_W + NWL * 1024;
constexpr int OFF_T2 = OFF_T1 + 32 * TPITCH;
constexpr int OFF_R0 = OFF_W + NFRAG * 1024;
constexpr int OFF_R1 = OFF_R0 + 8 * RING_SLOT;
constexpr int OFF_L = OFF_R1 + 8 * RING_SLOT;
constexpr int MAIN_LDS = OFF_L + 8 * LIST_BYTES;
static_assert(MAIN_LDS <= 160 * 1024, "LDS");
static_assert(KP_LDS <= 16 * RING_SLOT, "phase 1 lives in the ring");

__device__ __forceinline__ unsigned ldd(const unsigned* p) {
  return __hip_atomic_load(p, __ATOMIC_RELAXED, __HIP_MEMORY_SCOPE_AGENT);
}
__device__ __forceinline__ unsigned long long ldq(const unsigned long long* p) {
  return __hip_atomic_load(p, __ATOMIC_RELAXED, __HIP_MEMORY_SCOPE_AGENT);
}
__device__ __forceinline__ bf16x8 rawfrag(unsigned a) {
  const s16x4 a0 = __builtin_amdgcn_ds_read_tr16_b64_v4i16((LAS s16x4*)(size_t)a);
  const s16x4 a1 = __builtin_amdgcn_ds_read_tr16_b64_v4i16((LAS s16x4*)(size_t)(a + 512));
  return __builtin_shufflevector(a0, a1, 0, 1, 2, 3, 4, 5, 6, 7);
}
__device__ __forceinline__ float maxf(float a, float b) {
  float r;
  asm("v_max_f32 %0, %1, %2" : "=v"(r) : "v"(a), "v"(b));
  return r;
}
__device__ __forceinline__ float add1(float t) { return 1.0f + t; }
__device__ __forceinline__ float mulf(int i, float a, float b) { return (i & 1) ? __builtin_fmaf(a, b, 0.0f) : a * b; }

__global__ __launch_bounds__(512, 2) void k_main(const unsigned short* __restrict__ x,
                                                 const u32x4* __restrict__ ES,
                                                 const int* __restrict__ src, const int* __restrict__ dst,
                                                 const unsigned short* __restrict__ wa_g,
                                                 const unsigned short* __restrict__ wb_g,
                                                 const unsigned short* __restrict__ wc_g,
                                                 const unsigned short* __restrict__ wu_g,
                                                 const unsigned short* __restrict__ wv_g,
                                                 u32x4* ENT, unsigned long long* TS, unsigned long long* FL,
                                                 unsigned short* __restrict__ out, int N, int E, int chunk,
                                                 unsigned M, unsigned N1, unsigned DM, unsigned MF0, unsigned MF1,
                                                 unsigned MB0, unsigned MB1
                                                 ) {
#define PT(i)
  PT(0);
  __shared__ __attribute__((aligned(16))) char smem[MAIN_LDS];
  const int tid0 = threadIdx.x;
  const int wave = __builtin_amdgcn_readfirstlane(tid0 >> 6);
  const int bk = blockIdx.x;
  const unsigned lds0 = (unsigned)(size_t)(LAS char*)smem;

  LAS char* sm = (LAS char*)smem + OFF_R0;
  const unsigned sm0 = lds0 + OFF_R0;
  const int tid = tid0, lane = tid & 63, r = lane & 31, h = lane >> 5;
  const unsigned pc = tid & 7, bq = tid >> 3;
  unsigned ia = sm0 + KP_ENT + bq * CPIT + pc * 16;
#pragma unroll
  for (int s = 0; s < 4; ++s) *(LAS u32x4*)(size_t)(ia + s * 64 * CPIT) = u32x4{~0u, ~0u, ~0u, ~0u};
  *(LAS unsigned*)(sm + KP_HDR + (tid & 255) * 4) = 0u;
  int q0 = 4 * tid + 3, q1 = 4 * tid + 1795;
  unsigned a0 = tid * 16, a1 = tid * 16 + 7168, z0 = 0u;
  unsigned long long m7 = __ballot(tid == 448);
  int n7 = wave == 7 ? 0 : 0x7fffffff;
  unsigned f0 = lane * 128u, f1 = lane * 128u + 8192u, f2 = min(lane * 128u + 16384u, CPFB - 512u);
  unsigned long long p7 = __ballot((wave | (bk & -CPF)) == 7);
  asm volatile("" : "+v"(f0), "+v"(f1), "+v"(f2), "+s"(p7));
  asm volatile("" : "+v"(ia), "+v"(q0), "+v"(q1), "+v"(a0), "+v"(a1), "+v"(z0), "+s"(m7), "+s"(n7) :: "memory");
  SB();
  asm volatile("" :: "s"(x), "s"(ES), "s"(src), "s"(dst), "s"(wa_g), "s"(wb_g), "s"(wc_g), "s"(wu_g), "s"(wv_g), "s"(ENT),
               "s"(TS), "s"(FL), "s"(out), "s"(N), "s"(E), "s"(chunk), "s"(M), "s"(N1), "s"(DM), "s"(MF0), "s"(MF1), "s"(MB0), "s"(MB1));
  const int base = bk * chunk;
  const int ecnt = max(min(chunk, E - base), 0);
  unsigned long long on[2];
  on[0] = __ballot(q0 < min(min(chunk, E - base), n7));
  on[1] = __ballot(q1 < min(min(chunk, E - base), n7));
  a0 += base * 4u;
  a1 += base * 4u;
  u32x4 d[2], sv[2];
  u32x2 e0;
  asm volatile("s_mov_b64 exec, %11\n\t"
               "global_load_dwordx4 %0, %5, %8 nt\n\t"
               "s_mov_b64 exec, %12\n\t"
               "global_load_dwordx4 %1, %6, %8 nt\n\t"
               "s_mov_b64 exec, %13\n\t"
               "global_load_dwordx2 %2, %7, %9 sc1\n\t"
               "s_getpc_b64 s[96:97]\n\t"
               "s_mov_b32 m0, %17\n\t"
               "s_mov_b64 exec, %18\n\t"
               "global_load_lds_dword %14, s[96:97]\n\t"
               "global_load_lds_dword %15, s[96:97]\n\t"
               "global_load_lds_dword %16, s[96:97]\n\t"
               "s_mov_b64 exec, %11\n\t"
               "s_waitcnt lgkmcnt(0)\n\t"
               "s_barrier\n\t"
               "global_load_dwordx4 %3, %5, %10 nt\n\t"
               "s_mov_b64 exec, %12\n\t"
               "global_load_dwordx4 %4, %6, %10 nt\n\t"
               "s_mov_b64 exec, -1"
               : "=&{v[240:243]}"(d[0]), "=&{v[244:247]}"(d[1]), "=&{v[238:239]}"(e0), "=&{v[248:251]}"(sv[0]),
                 "=&{v[252:255]}"(sv[1])
               : "v"(a0), "v"(a1), "v"(z0), "s"(dst), "s"(FL), "s"(src), "s"(on[0]), "s"(on[1]), "s"(m7)
                 , "v"(f0), "v"(f1), "v"(f2), "n"(OFF_L + 7 * LIST_BYTES + 4096), "s"(p7)
               : "memory", "m0", "s96", "s97");
  SB();
  const int bu = (bk >> 1) | ((bk & 1) << 7);
  const unsigned Mw = bk & 1 ? M + DM : M, Cw = bk & 1 ? 0u - N1 * DM : 0u;
  unsigned MF = bk & 1 ? MF1 : MF0, MB = bk & 1 ? MB1 : MB0;
  asm volatile("" : "+s"(MF), "+s"(MB));
  char* ib = reinterpret_cast<char*>(ENT) + (size_t)bu * IBOX;
  auto nodeof = [&](unsigned c) { return min((unsigned)__builtin_fmaf((float)c, __uint_as_float(MF), __uint_as_float(MB)), (unsigned)N - 1u); };
  unsigned tup[8], val[8], pm[8];
  u32x4 xg[4];
  const unsigned xn0 = __builtin_amdgcn_readfirstlane(nodeof((bu * 8 + wave) * 32));
  unsigned xd[4];
  auto xprep = [&]() {
#pragma unroll
    for (int i = 0; i < 4; ++i)
      xd[i] = min(xn0 + 8 * i + (lane >> 3), N - 1u) * 128u + ((((lane & 7) ^ (lane >> 4)) * 16) ^ (i & 1) * 64) - i * 1024u;
    asm volatile("" : "+v"(xd[0]), "+v"(xd[1]), "+v"(xd[2]), "+v"(xd[3]));
  };
  auto xdma = [&]() {
    asm volatile("s_mov_b32 m0, %5\n\ts_nop 0\n\t"
                 "global_load_lds_dwordx4 %0, %4\n\t"
                 "global_load_lds_dwordx4 %1, %4 offset:1024\n\t"
                 "global_load_lds_dwordx4 %2, %4 offset:2048\n\t"
                 "global_load_lds_dwordx4 %3, %4 offset:3072"
                 :: "v"(xd[0]), "v"(xd[1]), "v"(xd[2]), "v"(xd[3]), "s"(x), "s"(lds0 + OFF_L + wave * LIST_BYTES)
                 : "memory", "m0");
  };
  auto wload = [&](const unsigned short* w, int f) {
    asm volatile("s_mov_b32 m0, %3\n\ts_nop 0\n\t"
                 "global_load_lds_dwordx4 %0, %2\n\t"
                 "global_load_lds_dwordx4 %0, %2 offset:1024\n\t"
                 "global_load_lds_dwordx4 %0, %2 offset:2048\n\t"
                 "global_load_lds_dwordx4 %0, %2 offset:3072\n\t"
                 "s_addk_i32 m0, 4096\n\ts_nop 0\n\t"
                 "global_load_lds_dwordx4 %1, %2\n\t"
                 "global_load_lds_dwordx4 %1, %2 offset:1024\n\t"
                 "global_load_lds_dwordx4 %1, %2 offset:2048\n\t"
                 "global_load_lds_dwordx4 %1, %2 offset:3072"
                 :: "v"(lane * 16u), "v"(lane * 16u + 4096u), "s"(w), "s"(lds0 + OFF_W + f * 1024) : "memory", "m0", "scc");
  };
  auto wall = [&]() {
    wload(wa_g, NBLOB);
    wload(wb_g, 0);
    wload(wv_g, 8);
    glds16(wc_g, lane * 16, lds0 + OFF_W + 16 * 1024);
    glds16(wc_g + 512, lane * 16, lds0 + OFF_W + 17 * 1024);
  };
  int spin = 1 << 20;
  u32x2 ep = {0u, 0u};
  if (wave == 7) {
    wall();
    auto touch = [&](const void* p, unsigned stride) {
      asm volatile("s_mov_b32 m0, %2\n\ts_mov_b64 exec, %3\n\tglobal_load_lds_dword %0, %1 sc1\n\ts_mov_b64 exec, -1"
                   :: "v"(lane * stride), "s"(p), "n"(OFF_L + 7 * LIST_BYTES + 4096), "s"(__ballot(bk < TWN)) : "memory", "m0");
    };
    if (TW & 4) touch(ENT, IEXT / 64);
    if (TW & 2) touch(x, (unsigned)(N / 64) * 128u);
    if (TW & 1) touch(ES, (unsigned)(E / 64) * 32u);
    if (TW & 8) touch(out, (unsigned)(N / 64) * 128u);
    *(LAS u32x4*)((LAS char*)smem + OFF_CNT + r * 16) =
        r < 8 ? u32x4{lds0 + OFF_L, lds0 + OFF_L + 4, lds0 + OFF_L + 8, lds0 + OFF_L + 12} : u32x4{0u, 0u, 0u, 0u};
    {
      const unsigned col = h ? ((r >> 1) & 12u) | ((r << 2) & 16u) | (r & 3u) : r;
      LAS char* tr = (LAS char*)smem + OFF_T1 + lane * TPITCH;
#pragma unroll
      for (int j = 0; j < 4; ++j) *(LAS u32x4*)(tr + j * 16) = u32x4{0u, 0u, 0u, 0u};
      *(LAS unsigned short*)(tr + col * 2) = 0x3F80;
    }
#pragma unroll
    for (int i = 0; i < 8; ++i) { tup[i] = 0u; val[i] = 0u; pm[i] = 0u; }
    asm volatile("s_waitcnt vmcnt(%1)" : "+{v[238:239]}"(e0) : "n"((WEARLY ? 26 : 0) + (PFAT ? 5 : 0) + __builtin_popcount(TW)) : "memory");
    ep[0] = __builtin_amdgcn_readfirstlane(e0[0]) + 0x9E3779B1u;
    ep[1] = __builtin_amdgcn_readfirstlane(e0[1]) + 0x7F4A7C15u;
    *(LAS u32x2*)((LAS char*)smem + OFF_CNT + 168) = u32x2{ep[0], ep[1] >> 24};
    if (__builtin_expect(ecnt & 3, 0)) {
      const int i = (ecnt & ~3) + lane;
      on[0] = __ballot(i < ecnt);
      if (i < ecnt) {
        pm[0] = (unsigned)dst[base + i] * M + (unsigned)max(dst[base + i] - (int)N1, 0) * DM;
        tup[0] = (unsigned)src[base + i] | (unsigned)i << 16;
        val[0] = __hip_atomic_fetch_add((LAS unsigned*)(sm + KP_HDR) + (pm[0] >> 24), RINC, __ATOMIC_RELAXED, __HIP_MEMORY_SCOPE_WORKGROUP);
        const unsigned u = min(val[0] >> 16, 48u);
        *(LAS unsigned*)(sm + KP_ENT + (pm[0] >> 24) * CPIT + u * 4) = tup[0];
        *(LAS unsigned char*)(sm + KP_ENT + (pm[0] >> 24) * CPIT + ((u * 4) | (u & 3) | 12)) = pm[0] >> 16;
      }
      __builtin_amdgcn_s_waitcnt(0x0F70);
    }
    PT(1);
    xprep();
    xdma();
  } else {
    xprep();
    unsigned inc = RINC, t0 = (unsigned)tid << 18;
    asm volatile("s_waitcnt vmcnt(2)" : "+{v[240:243]}"(d[0]), "+{v[244:247]}"(d[1]), "+v"(inc), "+v"(t0) :: "memory");
    PT(1);
    unsigned cb[8];
#pragma unroll
    for (int s = 0; s < 2; ++s) {
      unsigned* rr = val + 4 * s;
#pragma unroll
      for (int k = 0; k < 4; ++k) {
        unsigned t;
        asm("v_sub_u32 %0, %1, %2 clamp" : "=v"(t) : "v"(d[s][k]), "s"(N1));
        asm("v_mad_u32_u24 %0, %1, %2, %3" : "=v"(pm[4 * s + k]) : "v"(t), "s"(DM), "v"(mul24s(d[s][k], M)));
      }
#pragma unroll
      for (int k = 0; k < 4; ++k) rr[k] = byte3x(pm[4 * s + k], 4u);
      asm volatile(
          "s_mov_b64 exec, %5\n\t"
          "ds_add_rtn_u32 %0, %0, %4 offset:%6\n\t"
          "ds_add_rtn_u32 %1, %1, %4 offset:%6\n\t"
          "ds_add_rtn_u32 %2, %2, %4 offset:%6\n\t"
          "ds_add_rtn_u32 %3, %3, %4 offset:%6\n\t"
          "s_mov_b64 exec, -1"
          : "+v"(rr[0]), "+v"(rr[1]), "+v"(rr[2]), "+v"(rr[3])
          : "v"(inc), "s"(on[s]), "n"(OFF_R0 + KP_HDR)
          : "memory");
    }
#pragma unroll
    for (int i = 0; i < 8; ++i) cb[i] = byte3x(pm[i], (unsigned)CPIT);
    asm volatile("s_waitcnt vmcnt(0)"
                 : "+{v[248:251]}"(sv[0]), "+{v[252:255]}"(sv[1]), "+v"(cb[0]), "+v"(cb[1]), "+v"(cb[2]), "+v"(cb[3]), "+v"(cb[4]), "+v"(cb[5]),
                   "+v"(cb[6]), "+v"(cb[7])
                 :: "memory");
    xdma();
#pragma unroll
    for (int i = 0; i < 8; ++i) tup[i] = sv[i >> 2][i & 3] + t0 + (((i >> 2) * 1792u + (i & 3)) << 16);
#pragma unroll
    for (int s = 0; s < 2; ++s) {
      asm volatile("s_waitcnt lgkmcnt(%8)"
                   : "+v"(val[4 * s]), "+v"(val[4 * s + 1]), "+v"(val[4 * s + 2]), "+v"(val[4 * s + 3]), "+v"(tup[4 * s]),
                     "+v"(tup[4 * s + 1]), "+v"(tup[4 * s + 2]), "+v"(tup[4 * s + 3])
                   : "n"(DBGWAIT ? 0 : s ? 8 : 4) : "memory");
      unsigned pa[4], pb[4];
#pragma unroll
      for (int k = 0; k < 4; ++k) {
        const unsigned u = min(val[4 * s + k], 48u << 16);
        asm("v_mad_u32_u16 %0, %1, 4, %2 op_sel:[1,0,0,0]" : "=v"(pa[k]) : "v"(u), "v"(cb[4 * s + k]));
        pb[k] = pa[k] | ((u >> 16) & 3u) | 12u;
      }
      asm volatile(
          "s_mov_b64 exec, %16\n\t"
          "ds_write_b32 %0, %8 offset:%17\n\t"
          "ds_write_b8_d16_hi %4, %12 offset:%17\n\t"
          "ds_write_b32 %1, %9 offset:%17\n\t"
          "ds_write_b8_d16_hi %5, %13 offset:%17\n\t"
          "ds_write_b32 %2, %10 offset:%17\n\t"
          "ds_write_b8_d16_hi %6, %14 offset:%17\n\t"
          "ds_write_b32 %3, %11 offset:%17\n\t"
          "ds_write_b8_d16_hi %7, %15 offset:%17\n\t"
          "s_mov_b64 exec, -1"
          :: "v"(pa[0]), "v"(pa[1]), "v"(pa[2]), "v"(pa[3]), "v"(pb[0]), "v"(pb[1]), "v"(pb[2]), "v"(pb[3]),
             "v"(tup[4 * s]), "v"(tup[4 * s + 1]), "v"(tup[4 * s + 2]), "v"(tup[4 * s + 3]), "v"(pm[4 * s]),
             "v"(pm[4 * s + 1]), "v"(pm[4 * s + 2]), "v"(pm[4 * s + 3]), "s"(on[s]), "n"(OFF_R0 + KP_ENT)
          : "memory");
    }
  }
  PT(2);
  u32x2 tag;
  {
    unsigned ha = sm0 + KP_HDR + bq * 4, hb = sm0 + KP_HDR + lane * 16;
    unsigned go = bq * IBOX + bk * 128 + pc * 16;
    asm volatile("s_waitcnt lgkmcnt(0)\n\ts_barrier" : "+v"(ha), "+v"(hb), "+v"(go) :: "memory");
    PT(3);
    tag = *(const LAS u32x2*)((LAS char*)smem + OFF_CNT + 168);
    const u32x4 call = *(const LAS u32x4*)(size_t)hb;
    unsigned cn[4];
    u32x4 v[4];
#pragma unroll
    for (int s = 0; s < 4; ++s) {
      cn[s] = *(const LAS unsigned*)(size_t)(ha + s * 256);
      v[s] = *(const LAS u32x4*)(size_t)(ia + s * 64 * CPIT);
    }
    asm volatile("" : "+v"(v[0]), "+v"(v[1]), "+v"(v[2]), "+v"(v[3]), "+v"(tag));
    if (__builtin_expect(__ballot(max(max(call[0], call[1]), max(call[2], call[3])) > 36u * RINC) != 0ull, 0)) {
      LAS unsigned* ovc = (LAS unsigned*)((LAS char*)smem + OFF_CNT + 164);
      LAS unsigned* pos = (LAS unsigned*)(sm + KP_POS);
      if (tid < NBK) {
        const unsigned u = *(const LAS unsigned*)(sm + KP_HDR + tid * 4) >> 16, n = u - (u >> 2);
        if (n > 36u) {
          const unsigned p = (unsigned)base + __hip_atomic_fetch_add(ovc, n - 36u, __ATOMIC_RELAXED, __HIP_MEMORY_SCOPE_WORKGROUP);
          pos[tid] = p;
          __hip_atomic_store(FL + 512 + tid * NCH + bk, ((unsigned long long)(n - 36u) << 32) | p, __ATOMIC_RELAXED,
                             __HIP_MEMORY_SCOPE_AGENT);
        }
      }
      asm volatile("s_waitcnt lgkmcnt(0)\n\ts_barrier" ::: "memory");
#pragma unroll
      for (int i = 0; i < 8; ++i) {
        const unsigned u = val[i] >> 16, n = u - (u >> 2);
        if (((on[i >> 2] >> lane) & 1ull) && n >= 36u)
          __hip_atomic_store(TS + pos[pm[i] >> 24] + (n - 36u), ((unsigned long long)((pm[i] >> 16) & 0xffu) << 32) | tup[i],
                             __ATOMIC_RELAXED, __HIP_MEMORY_SCOPE_AGENT);
      }
      __builtin_amdgcn_s_waitcnt(0x0F70);
      asm volatile("s_waitcnt vmcnt(0)\n\ts_barrier" ::: "memory");
    }
    auto seal = [&](u32x4& w, bool more) {
      w[3] = __builtin_amdgcn_perm(tag[1], w[3], 0x04020100u);
      unsigned sm_ = w[0] + w[1] + w[2] + w[3] + tag[0];
      sm_ += __builtin_amdgcn_update_dpp(0u, sm_, 0xB1, 0xf, 0xf, false);
      sm_ += __builtin_amdgcn_update_dpp(0u, sm_, 0x4E, 0xf, 0xf, false);
      sm_ += __builtin_amdgcn_update_dpp(0u, sm_, 0x141, 0xf, 0xf, false);
      w[2] = pc == 7u ? w[2] - sm_ + (more ? 1u : 0u) : w[2];
    };
#pragma unroll
    for (int s = 0; s < 4; ++s) seal(v[s], cn[s] > 23u * RINC);
    asm volatile("global_store_dwordx4 %0, %1, %5 sc1\n\t"
                 "global_store_dwordx4 %0, %2, %6 sc1\n\t"
                 "global_store_dwordx4 %0, %3, %7 sc1\n\t"
                 "global_store_dwordx4 %0, %4, %8 sc1\n\t"
                 "s_nop 1"
                 :: "v"(go), "v"(v[0]), "v"(v[1]), "v"(v[2]), "v"(v[3]), "s"(ENT), "s"(ENT + 4 * IBOX), "s"(ENT + 8 * IBOX),
                    "s"(ENT + 12 * IBOX)
                 : "memory");
    PT(4);
    if (__builtin_expect(__ballot(max(max(cn[0], cn[1]), max(cn[2], cn[3])) > 23u * RINC) != 0ull, 0)) {
#pragma unroll 1
      for (int s = 0; s < 4; ++s) {
        const unsigned c = *(const LAS unsigned*)(size_t)(ha + s * 256);
        u32x4 w = *(const LAS u32x4*)(size_t)(ia + s * 64 * CPIT + (pc < 4u ? 128 : 0));
        if (pc == 7u) w = u32x4{w[2], ~0u, ~0u, w[3] >> 16};
        else {
#pragma unroll
          for (int j = 0; j < 3; ++j) w[j] = pc < 4u && 32u + 4u * pc + j < (c >> 16) ? w[j] : ~0u;
        }
        seal(w, c > 36u * RINC);
        if (c > 23u * RINC) gstore128wt(ENT, go + s * (64 * IBOX) + IEXT, w);
      }
    }
    PT(5);
  }
  u32x4 cv[4];
  unsigned vo[4], eb[4];
  unsigned long long pend[4];
#pragma unroll
  for (int k = 0; k < 4; ++k) {
    vo[k] = wave * 4096u + k * 1024u + lane * 16u;
    eb[k] = (unsigned)((wave * 32 + k * 8 + (lane >> 3)) * chunk) * 32u;
  }
#define POLL0()                                                                                                       \
  asm volatile("global_load_dwordx4 %0, %4, %8 sc1\n\t"                                                               \
               "global_load_dwordx4 %1, %5, %8 sc1\n\t"                                                               \
               "global_load_dwordx4 %2, %6, %8 sc1\n\t"                                                               \
               "global_load_dwordx4 %3, %7, %8 sc1"                                                                   \
               : "=&{v[240:243]}"(cv[0]), "=&{v[244:247]}"(cv[1]), "=&{v[248:251]}"(cv[2]), "=&{v[252:255]}"(cv[3])   \
               : "v"(vo[0]), "v"(vo[1]), "v"(vo[2]), "v"(vo[3]), "s"(ib)                                              \
               : "memory")
  const LAS bf16x8* wl = (const LAS bf16x8*)(smem + OFF_W) + lane;
  const f32x16 zero16 = {0.f, 0.f, 0.f, 0.f, 0.f, 0.f, 0.f, 0.f, 0.f, 0.f, 0.f, 0.f, 0.f, 0.f, 0.f, 0.f};
  auto widx = [&](int m, int ct) { return m == 6 ? 16 + ct : m < 11 ? m - 7 + 4 * ct : 8 + m - 11 + 4 * ct; };
#define HELD ((1 << 6) | (1 << 8) | (1 << 10) | (1 << 12) | (1 << 14))
  bf16x8 wr[2][15];
  const unsigned to = lds0 + OFF_W + ((lane >> 2) & 3) * 128 + (lane & 1) * 64 + ((lane >> 4) & 1) * 16 + ((lane >> 1) & 1) * 8;
  auto xa = [&]() {
    bf16x8 wa[8];
#pragma unroll
    for (int k = 0; k < 8; ++k) wa[k] = rawfrag(to + (NBLOB + (k >> 1)) * 1024 + h * 4096 + (k & 1) * 32);
    asm volatile("" : "+v"(wa[0]), "+v"(wa[1]), "+v"(wa[2]), "+v"(wa[3]), "+v"(wa[4]), "+v"(wa[5]), "+v"(wa[6]), "+v"(wa[7]),
                      "+{v[160:163]}"(xg[0]), "+{v[164:167]}"(xg[1]), "+{v[168:171]}"(xg[2]), "+{v[172:175]}"(xg[3]));
    f32x16 XA[2] = {zero16, zero16};
#pragma unroll
    for (int k = 0; k < 4; ++k)
#pragma unroll
      for (int ct = 0; ct < 2; ++ct) XA[ct] = MFMA32(__builtin_bit_cast(bf16x8, xg[k]), wa[k * 2 + ct], XA[ct]);
#pragma unroll
    for (int q = 0; q < 4; ++q) {
      u32x4 xp;
#pragma unroll
      for (int j = 0; j < 4; ++j)
        xp[j] = pack_bf16(XA[q & 1][8 * (q >> 1) + 2 * j] * NLOG2E, XA[q & 1][8 * (q >> 1) + 2 * j + 1] * NLOG2E);
      *(LAS u32x4*)((LAS char*)smem + OFF_R1 + wave * RING_SLOT + q * 1024 + lane * 16) = xp;
    }
  };
  {
    auto prep = [&](unsigned f, unsigned hs, float scale) {
      u32x4 w[2];
#pragma unroll
      for (int ct = 0; ct < 2; ++ct) w[ct] = __builtin_bit_cast(u32x4, rawfrag(to + f + h * hs + ct * 32));
      asm volatile("s_waitcnt lgkmcnt(0)" : "+v"(w[0]), "+v"(w[1]) :: "memory");
#pragma unroll
      for (int ct = 0; ct < 2; ++ct) {
#pragma unroll
        for (int j = 0; j < 4; ++j)
          w[ct][j] = pack_bf16(__uint_as_float(w[ct][j] << 16) * scale, __uint_as_float(w[ct][j] & 0xffff0000u) * scale);
        *(LAS u32x4*)((LAS char*)smem + OFF_W + f + ct * hs + lane * 16) = w[ct];
      }
    };
    {
      const unsigned xr = min(nodeof((bu * 8 + wave) * 32 + r) - xn0, 31u);
      unsigned xa_ = lds0 + OFF_L + wave * LIST_BYTES + xr * 128 + (((4 * h) ^ ((xr >> 1) & 7)) * 16);
      asm volatile("s_waitcnt vmcnt(4)" : "+v"(xa_) :: "memory");
#pragma unroll
      for (int t = 0; t < 4; ++t) xg[t] = *(const LAS u32x4*)(size_t)(xa_ ^ (t * 16));
      asm volatile("" : "+v"(xg[0]), "+v"(xg[1]), "+v"(xg[2]), "+v"(xg[3]) :: "memory");
      *(LAS unsigned*)((LAS char*)smem + OFF_L + wave * LIST_BYTES + h * LARR + r * 4) = ~0u;
    }
    asm volatile("s_waitcnt vmcnt(4) lgkmcnt(0)\n\ts_barrier"
                 : "+v"(vo[0]), "+v"(vo[1]), "+v"(vo[2]), "+v"(vo[3]), "+v"(eb[0]), "+v"(eb[1]), "+v"(eb[2]), "+v"(eb[3])
                 :: "memory");
    xa();
    {
      unsigned f = (wave + (wave & 4)) * 1024, hs = 4096;
      const float scale = wave < 4 ? NLOG2E : 1.0f;
#pragma unroll 1
      while (true) {
        asm volatile("" : "+s"(f), "+s"(hs));
        prep(f, hs, scale);
        if (f != 0u) break;
        f = 16 * 1024;
        hs = 1024;
      }
    }
    PT(6);
    POLL0();
    glds16(wu_g + wave * 512, lane * 16, lds0 + OFF_W + (18 + wave) * 1024);
  }
  const unsigned l0 = lds0 + OFF_L;
  unsigned aLX = l0 + (lane >> 3) * 16;
  unsigned aLE = l0 + LARR + ((((lane >> 1) & 7) << 2) | (lane >> 4)) * 4;
  unsigned aLS4 = l0 + (4 * h + ((lane >> 2) & 3)) * 16;
  unsigned aLS1 = l0 + (((r & 7) << 2) | (r >> 3)) * 4 + 2;
  unsigned cx0 = (unsigned)(((lane & 7) ^ (lane >> 4)) * 16);
  unsigned cx1 = cx0 ^ 64u;
  unsigned ce = (unsigned)((((lane & 1) ^ ((lane >> 4) & 1))) * 16);
  const unsigned ringd = 8 * RING_SLOT;
  unsigned cS = lds0 + OFF_T1 + (((lane >> 4) & 1) * 16 + (lane & 3) * 4) * 2;
  unsigned cSt = lds0 + OFF_T2 + h * 32;
  unsigned fo[4];
#pragma unroll
  for (int t = 0; t < 4; ++t) fo[t] = lds0 + OFF_R0 + (unsigned)(r * 128 + (((4 * h + t) ^ ((r >> 1) & 7)) * 16));
  unsigned foe = lds0 + OFF_R0 + (unsigned)(4096 + r * 32 + ((h ^ ((r >> 3) & 1)) * 16));
  f32x16 Y[4];
#pragma unroll
  for (int q = 0; q < 4; ++q)
#pragma unroll
    for (int i = 0; i < 16; i += 2) {
      f32x2 y;
      if (q < 2) asm volatile("v_mov_b64 %0, %1" : "=v"(y) : "s"(0x358637bd358637bdull));
      else asm volatile("v_mov_b64 %0, 0" : "=v"(y));
      Y[q][i] = y[0];
      Y[q][i + 1] = y[1];
    }
  f32x16 G0, V0, G1, V1;
  bf16x8 S[2], St[2], as[4], ae, b[3];
  u32x4 pd[2], pn[2];
  float tq[16], rq[16], pq[16];
  auto pipe_zero = [&]() {
#pragma unroll
    for (int j = 0; j < 8; ++j) { S[0][j] = 0; S[1][j] = 0; }
#pragma unroll
    for (int j = 0; j < 4; ++j) { pd[0][j] = 0; pd[1][j] = 0; pn[0][j] = 0; pn[1][j] = 0; }
#pragma unroll
    for (int i = 0; i < 16; ++i) V1[i] = 0.f;
    rq[14] = 0.f; rq[15] = 0.f;
  };
  unsigned rid = lane & 7;
  asm volatile("" : "+v"(aLX), "+v"(aLE), "+v"(aLS4), "+v"(aLS1), "+v"(cx0), "+v"(cx1), "+v"(ce), "+v"(cS), "+v"(cSt),
                    "+v"(fo[0]), "+v"(fo[1]), "+v"(fo[2]), "+v"(fo[3]), "+v"(foe), "+v"(rid));
  asm volatile("" : "+v"(Y[0]), "+v"(Y[1]), "+v"(Y[2]), "+v"(Y[3]));
  auto ld128 = [&](unsigned a) { return *(const LAS u32x4*)(size_t)a; };
  auto gather5 = [&](const u32x4 sx, unsigned re, unsigned dstb) {
    glds16x(x, sx[0], cx0, dstb);
    glds16x(x, sx[1], cx1, dstb + 1024);
    glds16x(x, sx[2], cx0, dstb + 2048);
    glds16x(x, sx[3], cx1, dstb + 3072);
    glds16e(ES, re, ce, dstb + 4096);
  };
  unsigned rq4 = ((lane >> 3) & 3) * 4;
  unsigned early, just;
  asm volatile(
        "s_mov_b32 s96, 0x800000\n\t"
        "v_mov_b32 v181, %18\n\t"
        "v_mov_b32 v180, 5120\n\t"
        "v_mov_b32 v177, 0x0c040100\n\t"
        "v_mov_b32 v178, 0x0c050100\n\t"
        "v_mov_b32 v179, 0x0c060100\n\t"
        "s_mov_b32 s52, 0x7f7f7f7f\n\t"
        "s_mov_b32 s53, 0x7f7f7f7f\n\t"
        "s_mov_b64 s[54:55], 0\n\t"
        "s_mov_b32 %12, 0\n\t"
        "v_mov_b32 v191, 16\n\t"
        "s_waitcnt vmcnt(%30)\n\t"
        "v_add3_u32 v228, v240, v241, v242\n\t"
        "v_add3_u32 v231, v244, v245, v246\n\t"
        "v_add3_u32 v228, v228, v243, %19\n\t"
        "v_add3_u32 v231, v231, v247, %19\n\t"
        "v_cmp_ne_u32_sdwa s[72:73], v243, %20 src0_sel:BYTE_3 src1_sel:DWORD\n\t"
        "v_add_u32_dpp v228, v228, v228 quad_perm:[1,0,3,2] row_mask:0xf bank_mask:0xf\n\t"
        "v_add_u32_dpp v231, v231, v231 quad_perm:[1,0,3,2] row_mask:0xf bank_mask:0xf\n\t"
        "v_cmp_ne_u32_sdwa s[78:79], v247, %20 src0_sel:BYTE_3 src1_sel:DWORD\n\t"
        "v_add_u32_dpp v228, v228, v228 quad_perm:[2,3,0,1] row_mask:0xf bank_mask:0xf\n\t"
        "v_add_u32_dpp v231, v231, v231 quad_perm:[2,3,0,1] row_mask:0xf bank_mask:0xf\n\t"
        "s_nop 0\n\t"
        "v_add_u32_dpp v228, v228, v228 row_half_mirror row_mask:0xf bank_mask:0xf\n\t"
        "v_add_u32_dpp v231, v231, v231 row_half_mirror row_mask:0xf bank_mask:0xf\n\t"
        "v_cmp_gt_u32 s[64:65], 2, v228\n\t"
        "v_cmp_eq_u32 s[56:57], 1, v228\n\t"
        "v_cmp_gt_u32 s[66:67], 2, v231\n\t"
        "v_cmp_eq_u32 s[58:59], 1, v231\n\t"
        "s_and_b64 s[72:73], s[72:73], s[64:65]\n\t"
        "s_cselect_b64 s[64:65], 0, s[64:65]\n\t"
        "s_and_b64 s[56:57], s[56:57], s[64:65]\n\t"
        "s_not_b64 %0, s[64:65]\n\t"
        "s_or_b64 s[54:55], s[54:55], s[56:57]\n\t"
        "s_or_b64 s[54:55], s[54:55], %0\n\t"
        "s_and_b64 s[78:79], s[78:79], s[66:67]\n\t"
        "s_cselect_b64 s[66:67], 0, s[66:67]\n\t"
        "s_and_b64 s[58:59], s[58:59], s[66:67]\n\t"
        "s_not_b64 %1, s[66:67]\n\t"
        "s_or_b64 s[54:55], s[54:55], s[58:59]\n\t"
        "s_or_b64 s[54:55], s[54:55], %1\n\t"
        "s_mov_b64 exec, s[64:65]\n\t"
        "v_cmpx_ne_u32 s[72:73], -1, v240\n\t"
        "v_bfe_u32 v192, v243, 5, 3\n\t"
        "v_lshl_add_u32 v204, v192, 4, %21\n\t"
        "ds_add_rtn_u32 v204, v204, v191\n\t"
        "v_perm_b32 v216, v243, v240, v177\n\t"
        "v_mad_u32_u16 v228, v240, 32, %13 op_sel:[1,0,0,0]\n\t"
        "v_cmpx_ne_u32 s[74:75], -1, v241\n\t"
        "v_bfe_u32 v193, v243, 13, 3\n\t"
        "v_lshl_add_u32 v205, v193, 4, %21\n\t"
        "ds_add_rtn_u32 v205, v205, v191\n\t"
        "v_perm_b32 v217, v243, v241, v178\n\t"
        "v_mad_u32_u16 v229, v241, 32, %13 op_sel:[1,0,0,0]\n\t"
        "s_and_b64 exec, exec, s[52:53]\n\t"
        "v_cmpx_ne_u32 s[76:77], -1, v242\n\t"
        "v_bfe_u32 v194, v243, 21, 3\n\t"
        "v_lshl_add_u32 v206, v194, 4, %21\n\t"
        "ds_add_rtn_u32 v206, v206, v191\n\t"
        "v_perm_b32 v218, v243, v242, v179\n\t"
        "v_mad_u32_u16 v230, v242, 32, %13 op_sel:[1,0,0,0]\n\t"
        "s_mov_b64 exec, s[66:67]\n\t"
        "v_cmpx_ne_u32 s[78:79], -1, v244\n\t"
        "v_bfe_u32 v195, v247, 5, 3\n\t"
        "v_lshl_add_u32 v207, v195, 4, %21\n\t"
        "ds_add_rtn_u32 v207, v207, v191\n\t"
        "v_perm_b32 v219, v247, v244, v177\n\t"
        "v_mad_u32_u16 v231, v244, 32, %14 op_sel:[1,0,0,0]\n\t"
        "v_cmpx_ne_u32 s[80:81], -1, v245\n\t"
        "v_bfe_u32 v196, v247, 13, 3\n\t"
        "v_lshl_add_u32 v208, v196, 4, %21\n\t"
        "ds_add_rtn_u32 v208, v208, v191\n\t"
        "v_perm_b32 v220, v247, v245, v178\n\t"
        "v_mad_u32_u16 v232, v245, 32, %14 op_sel:[1,0,0,0]\n\t"
        "s_and_b64 exec, exec, s[52:53]\n\t"
        "v_cmpx_ne_u32 s[82:83], -1, v246\n\t"
        "v_bfe_u32 v197, v247, 21, 3\n\t"
        "v_lshl_add_u32 v209, v197, 4, %21\n\t"
        "ds_add_rtn_u32 v209, v209, v191\n\t"
        "v_perm_b32 v221, v247, v246, v179\n\t"
        "v_mad_u32_u16 v233, v246, 32, %14 op_sel:[1,0,0,0]\n\t"
        "s_mov_b64 exec, -1\n\t"
        "s_waitcnt vmcnt(%31)\n\t"
        "v_add3_u32 v234, v248, v249, v250\n\t"
        "v_add3_u32 v237, v252, v253, v254\n\t"
        "v_add3_u32 v234, v234, v251, %19\n\t"
        "v_add3_u32 v237, v237, v255, %19\n\t"
        "v_cmp_ne_u32_sdwa s[84:85], v251, %20 src0_sel:BYTE_3 src1_sel:DWORD\n\t"
        "v_add_u32_dpp v234, v234, v234 quad_perm:[1,0,3,2] row_mask:0xf bank_mask:0xf\n\t"
        "v_add_u32_dpp v237, v237, v237 quad_perm:[1,0,3,2] row_mask:0xf bank_mask:0xf\n\t"
        "v_cmp_ne_u32_sdwa s[90:91], v255, %20 src0_sel:BYTE_3 src1_sel:DWORD\n\t"
        "v_add_u32_dpp v234, v234, v234 quad_perm:[2,3,0,1] row_mask:0xf bank_mask:0xf\n\t"
        "v_add_u32_dpp v237, v237, v237 quad_perm:[2,3,0,1] row_mask:0xf bank_mask:0xf\n\t"
        "s_nop 0\n\t"
        "v_add_u32_dpp v234, v234, v234 row_half_mirror row_mask:0xf bank_mask:0xf\n\t"
        "v_add_u32_dpp v237, v237, v237 row_half_mirror row_mask:0xf bank_mask:0xf\n\t"
        "v_cmp_gt_u32 s[68:69], 2, v234\n\t"
        "v_cmp_eq_u32 s[60:61], 1, v234\n\t"
        "v_cmp_gt_u32 s[70:71], 2, v237\n\t"
        "v_cmp_eq_u32 s[62:63], 1, v237\n\t"
        "s_and_b64 s[84:85], s[84:85], s[68:69]\n\t"
        "s_cselect_b64 s[68:69], 0, s[68:69]\n\t"
        "s_and_b64 s[60:61], s[60:61], s[68:69]\n\t"
        "s_not_b64 %2, s[68:69]\n\t"
        "s_or_b64 s[54:55], s[54:55], s[60:61]\n\t"
        "s_or_b64 s[54:55], s[54:55], %2\n\t"
        "s_and_b64 s[90:91], s[90:91], s[70:71]\n\t"
        "s_cselect_b64 s[70:71], 0, s[70:71]\n\t"
        "s_and_b64 s[62:63], s[62:63], s[70:71]\n\t"
        "s_not_b64 %3, s[70:71]\n\t"
        "s_or_b64 s[54:55], s[54:55], s[62:63]\n\t"
        "s_or_b64 s[54:55], s[54:55], %3\n\t"
        "s_mov_b64 exec, s[72:73]\n\t"
        "s_waitcnt lgkmcnt(3)\n\t"
        "v_min_u32 v204, v181, v204\n\t"
        "v_min_u32 v205, v181, v205\n\t"
        "v_min_u32 v206, v181, v206\n\t"
        "v_mad_u32_u24 v204, v192, v180, v204\n\t"
        "v_mad_u32_u24 v205, v193, v180, v205\n\t"
        "v_mad_u32_u24 v206, v194, v180, v206\n\t"
        "ds_write2st64_b32 v204, v216, v228 offset1:10\n\t"
        "s_mov_b64 exec, s[74:75]\n\t"
        "ds_write2st64_b32 v205, v217, v229 offset1:10\n\t"
        "s_mov_b64 exec, s[76:77]\n\t"
        "ds_write2st64_b32 v206, v218, v230 offset1:10\n\t"
        "s_mov_b64 exec, s[68:69]\n\t"
        "v_cmpx_ne_u32 s[84:85], -1, v248\n\t"
        "v_bfe_u32 v198, v251, 5, 3\n\t"
        "v_lshl_add_u32 v210, v198, 4, %21\n\t"
        "ds_add_rtn_u32 v210, v210, v191\n\t"
        "v_perm_b32 v222, v251, v248, v177\n\t"
        "v_mad_u32_u16 v234, v248, 32, %15 op_sel:[1,0,0,0]\n\t"
        "v_cmpx_ne_u32 s[86:87], -1, v249\n\t"
        "v_bfe_u32 v199, v251, 13, 3\n\t"
        "v_lshl_add_u32 v211, v199, 4, %21\n\t"
        "ds_add_rtn_u32 v211, v211, v191\n\t"
        "v_perm_b32 v223, v251, v249, v178\n\t"
        "v_mad_u32_u16 v235, v249, 32, %15 op_sel:[1,0,0,0]\n\t"
        "s_and_b64 exec, exec, s[52:53]\n\t"
        "v_cmpx_ne_u32 s[88:89], -1, v250\n\t"
        "v_bfe_u32 v200, v251, 21, 3\n\t"
        "v_lshl_add_u32 v212, v200, 4, %21\n\t"
        "ds_add_rtn_u32 v212, v212, v191\n\t"
        "v_perm_b32 v224, v251, v250, v179\n\t"
        "v_mad_u32_u16 v236, v250, 32, %15 op_sel:[1,0,0,0]\n\t"
        "s_mov_b64 exec, s[78:79]\n\t"
        "s_waitcnt lgkmcnt(6)\n\t"
        "v_min_u32 v207, v181, v207\n\t"
        "v_min_u32 v208, v181, v208\n\t"
        "v_min_u32 v209, v181, v209\n\t"
        "v_mad_u32_u24 v207, v195, v180, v207\n\t"
        "v_mad_u32_u24 v208, v196, v180, v208\n\t"
        "v_mad_u32_u24 v209, v197, v180, v209\n\t"
        "ds_write2st64_b32 v207, v219, v231 offset1:10\n\t"
        "s_mov_b64 exec, s[80:81]\n\t"
        "ds_write2st64_b32 v208, v220, v232 offset1:10\n\t"
        "s_mov_b64 exec, s[82:83]\n\t"
        "ds_write2st64_b32 v209, v221, v233 offset1:10\n\t"
        "s_mov_b64 exec, -1\n\t"
        "ds_read_b128 v[184:187], %22\n\t"
        "ds_read_b32 v183, %23\n\t"
        "s_mov_b64 exec, s[70:71]\n\t"
        "v_cmpx_ne_u32 s[90:91], -1, v252\n\t"
        "v_bfe_u32 v201, v255, 5, 3\n\t"
        "v_lshl_add_u32 v213, v201, 4, %21\n\t"
        "ds_add_rtn_u32 v213, v213, v191\n\t"
        "v_perm_b32 v225, v255, v252, v177\n\t"
        "v_mad_u32_u16 v237, v252, 32, %16 op_sel:[1,0,0,0]\n\t"
        "v_cmpx_ne_u32 s[92:93], -1, v253\n\t"
        "v_bfe_u32 v202, v255, 13, 3\n\t"
        "v_lshl_add_u32 v214, v202, 4, %21\n\t"
        "ds_add_rtn_u32 v214, v214, v191\n\t"
        "v_perm_b32 v226, v255, v253, v178\n\t"
        "v_mad_u32_u16 v238, v253, 32, %16 op_sel:[1,0,0,0]\n\t"
        "s_and_b64 exec, exec, s[52:53]\n\t"
        "v_cmpx_ne_u32 s[94:95], -1, v254\n\t"
        "v_bfe_u32 v203, v255, 21, 3\n\t"
        "v_lshl_add_u32 v215, v203, 4, %21\n\t"
        "ds_add_rtn_u32 v215, v215, v191\n\t"
        "v_perm_b32 v227, v255, v254, v179\n\t"
        "v_mad_u32_u16 v239, v254, 32, %16 op_sel:[1,0,0,0]\n\t"
        "s_cmp_eq_u64 s[54:55], 0\n\t"
        "s_cbranch_scc1 59f\n\t"
        "s_mov_b64 exec, s[56:57]\n\t"
        "s_cbranch_execz 40f\n\t"
        "v_bfe_u32 v188, %4, 7, 8\n\t"
        "v_mov_b32 v189, 1\n\t"
        "ds_write_b8 v188, v189 offset:256\n\t"
        "v_add_u32 %4, s96, %4\n\t"
        "s_or_b64 %0, %0, exec\n\t"
        "40:\n\t"
        "s_mov_b64 exec, %0\n\t"
        "s_cbranch_execz 50f\n\t"
        "global_load_dwordx4 v[240:243], %4, %17 sc1\n\t"
        "50:\n\t"
        "s_mov_b64 exec, s[58:59]\n\t"
        "s_cbranch_execz 41f\n\t"
        "v_bfe_u32 v188, %5, 7, 8\n\t"
        "v_mov_b32 v189, 1\n\t"
        "ds_write_b8 v188, v189 offset:256\n\t"
        "v_add_u32 %5, s96, %5\n\t"
        "s_or_b64 %1, %1, exec\n\t"
        "41:\n\t"
        "s_mov_b64 exec, %1\n\t"
        "s_cbranch_execz 51f\n\t"
        "global_load_dwordx4 v[244:247], %5, %17 sc1\n\t"
        "51:\n\t"
        "s_mov_b64 exec, s[60:61]\n\t"
        "s_cbranch_execz 42f\n\t"
        "v_bfe_u32 v188, %6, 7, 8\n\t"
        "v_mov_b32 v189, 1\n\t"
        "ds_write_b8 v188, v189 offset:256\n\t"
        "v_add_u32 %6, s96, %6\n\t"
        "s_or_b64 %2, %2, exec\n\t"
        "42:\n\t"
        "s_mov_b64 exec, %2\n\t"
        "s_cbranch_execz 52f\n\t"
        "global_load_dwordx4 v[248:251], %6, %17 sc1\n\t"
        "52:\n\t"
        "s_mov_b64 exec, s[62:63]\n\t"
        "s_cbranch_execz 43f\n\t"
        "v_bfe_u32 v188, %7, 7, 8\n\t"
        "v_mov_b32 v189, 1\n\t"
        "ds_write_b8 v188, v189 offset:256\n\t"
        "v_add_u32 %7, s96, %7\n\t"
        "s_or_b64 %3, %3, exec\n\t"
        "43:\n\t"
        "s_mov_b64 exec, %3\n\t"
        "s_cbranch_execz 53f\n\t"
        "global_load_dwordx4 v[252:255], %7, %17 sc1\n\t"
        "53:\n\t"
        "s_waitcnt lgkmcnt(0)\n\t"
        "59:\n\t"
        "s_mov_b64 exec, s[84:85]\n\t"
        "s_waitcnt lgkmcnt(8)\n\t"
        "v_min_u32 v210, v181, v210\n\t"
        "v_min_u32 v211, v181, v211\n\t"
        "v_min_u32 v212, v181, v212\n\t"
        "v_mad_u32_u24 v210, v198, v180, v210\n\t"
        "v_mad_u32_u24 v211, v199, v180, v211\n\t"
        "v_mad_u32_u24 v212, v200, v180, v212\n\t"
        "ds_write2st64_b32 v210, v222, v234 offset1:10\n\t"
        "s_mov_b64 exec, s[86:87]\n\t"
        "ds_write2st64_b32 v211, v223, v235 offset1:10\n\t"
        "s_mov_b64 exec, s[88:89]\n\t"
        "ds_write2st64_b32 v212, v224, v236 offset1:10\n\t"
        "s_mov_b64 exec, -1\n\t"
        "s_movk_i32 s96, 128\n\t"
        "s_mov_b32 m0, %29\n\t"
        "s_waitcnt lgkmcnt(6)\n\t"
        "v_max3_u32 v182, v184, v185, v186\n\t"
        "v_max3_u32 v182, v182, v187, v183\n\t"
        "v_cmp_eq_u32 vcc, -1, v182\n\t"
        "v_mad_u32_u16 v184, v184, s96, %24\n\t"
        "v_mad_u32_u16 v185, v185, s96, %25\n\t"
        "v_mad_u32_u16 v186, v186, s96, %24\n\t"
        "v_mad_u32_u16 v187, v187, s96, %25\n\t"
        "v_add_u32 v183, v183, %26\n\t"
        "s_cbranch_vccnz 60f\n\t"
        "global_load_lds_dwordx4 v184, %27\n\t"
        "s_addk_i32 m0, 1024\n\t"
        "s_nop 0\n\t"
        "global_load_lds_dwordx4 v185, %27\n\t"
        "s_addk_i32 m0, 1024\n\t"
        "s_nop 0\n\t"
        "global_load_lds_dwordx4 v186, %27\n\t"
        "s_addk_i32 m0, 1024\n\t"
        "s_nop 0\n\t"
        "global_load_lds_dwordx4 v187, %27\n\t"
        "s_addk_i32 m0, 1024\n\t"
        "s_nop 0\n\t"
        "global_load_lds_dwordx4 v183, %28\n\t"
        "s_mov_b32 %12, 1\n\t"
        "60:\n\t"
        "s_mov_b64 exec, s[90:91]\n\t"
        "s_waitcnt lgkmcnt(3)\n\t"
        "v_min_u32 v213, v181, v213\n\t"
        "v_min_u32 v214, v181, v214\n\t"
        "v_min_u32 v215, v181, v215\n\t"
        "v_mad_u32_u24 v213, v201, v180, v213\n\t"
        "v_mad_u32_u24 v214, v202, v180, v214\n\t"
        "v_mad_u32_u24 v215, v203, v180, v215\n\t"
        "ds_write2st64_b32 v213, v225, v237 offset1:10\n\t"
        "s_mov_b64 exec, s[92:93]\n\t"
        "ds_write2st64_b32 v214, v226, v238 offset1:10\n\t"
        "s_mov_b64 exec, s[94:95]\n\t"
        "ds_write2st64_b32 v215, v227, v239 offset1:10\n\t"
        "s_mov_b64 exec, -1\n\t"
        : "=&s"(pend[0]), "=&s"(pend[1]), "=&s"(pend[2]), "=&s"(pend[3]), "+v"(vo[0]), "+v"(vo[1]), "+v"(vo[2]),
          "+v"(vo[3]), "+{v[240:243]}"(cv[0]), "+{v[244:247]}"(cv[1]), "+{v[248:251]}"(cv[2]), "+{v[252:255]}"(cv[3]),
          "=&s"(early)
        : "v"(eb[0]), "v"(eb[1]), "v"(eb[2]), "v"(eb[3]), "s"(ib), "n"(OFF_L + LARR - 16), "v"(tag[0]), "v"(tag[1]),
          "v"(rq4), "v"(aLX + wave * LIST_BYTES), "v"(aLE + wave * LIST_BYTES), "v"(cx0), "v"(cx1), "v"(ce), "s"(x),
          "s"(ES), "s"(lds0 + OFF_R0 + wave * RING_SLOT), "n"(3), "n"(1)
        :
          "memory", "vcc", "scc", "m0", "v177", "v178", "v179", "v180", "v181", "v182", "v183", "v184", "v185",
          "v186", "v187", "v188", "v189", "v190", "v191", "v192", "v193", "v194", "v195", "v196", "v197", "v198",
          "v199", "v200", "v201", "v202", "v203", "v204", "v205", "v206", "v207", "v208", "v209", "v210", "v211",
          "v212", "v213", "v214", "v215", "v216", "v217", "v218", "v219", "v220", "v221", "v222", "v223", "v224",
          "v225", "v226", "v227", "v228", "v229", "v230", "v231", "v232", "v233", "v234", "v235", "v236", "v237",
          "v238", "v239", "s52", "s53", "s54", "s55", "s56", "s57", "s58", "s59", "s60", "s61", "s62", "s63", "s64",
          "s65", "s66", "s67", "s68", "s69", "s70", "s71", "s72", "s73", "s74", "s75", "s76", "s77", "s78", "s79",
          "s80", "s81", "s82", "s83", "s84", "s85", "s86", "s87", "s88", "s89", "s90", "s91", "s92", "s93", "s94",
          "s95", "s96");
  auto uni = [&]() {
#pragma unroll
    for (int k = 0; k < 4; ++k)
      pend[k] = ((unsigned long long)(unsigned)__builtin_amdgcn_readfirstlane((unsigned)(pend[k] >> 32)) << 32) |
                (unsigned)__builtin_amdgcn_readfirstlane((unsigned)pend[k]);
  };
  uni();
  early = __builtin_amdgcn_readfirstlane(early);
  just = early;
  if (__builtin_expect((pend[0] | pend[1] | pend[2] | pend[3] | (unsigned long long)(early ^ 1u)) != 0ull, 0))
#pragma clang loop unroll(disable)
  while (true) {
    if (!early) {
      const u32x4 sx = ld128(aLX + wave * LIST_BYTES);
      const unsigned re = *(const LAS unsigned*)(size_t)(aLE + wave * LIST_BYTES);
      if (__ballot(max(max(max(sx[0], sx[1]), max(sx[2], sx[3])), re) == ~0u) == 0ull) {
        gather5(sx, re, lds0 + OFF_R0 + wave * RING_SLOT);
        early = 1u;
        just = 1u;
      }
    }
    if ((pend[0] | pend[1] | pend[2] | pend[3]) == 0ull || --spin < 0) break;
    asm volatile(
        "s_mov_b32 s96, 0x800000\n\t"
        "v_mov_b32 v181, %17\n\t"
        "v_mov_b32 v180, 5120\n\t"
        "v_mov_b32 v177, 0x0c040100\n\t"
        "v_mov_b32 v178, 0x0c050100\n\t"
        "v_mov_b32 v179, 0x0c060100\n\t"
        "s_mov_b32 s52, 0x7f7f7f7f\n\t"
        "s_mov_b32 s53, 0x7f7f7f7f\n\t"
        "v_mov_b32 v191, 16\n\t"
        "s_waitcnt vmcnt(5)\n\t"
        "s_cmp_lg_u32 %21, 0\n\t"
        "s_cbranch_scc1 9f\n\t"
        "s_waitcnt vmcnt(0)\n\t"
        "9:\n\t"
        "v_add3_u32 v228, v240, v241, v242\n\t"
        "v_add3_u32 v229, v244, v245, v246\n\t"
        "v_add3_u32 v230, v248, v249, v250\n\t"
        "v_add3_u32 v231, v252, v253, v254\n\t"
        "v_add3_u32 v228, v228, v243, %18\n\t"
        "v_add3_u32 v229, v229, v247, %18\n\t"
        "v_add3_u32 v230, v230, v251, %18\n\t"
        "v_add3_u32 v231, v231, v255, %18\n\t"
        "v_add_u32_dpp v228, v228, v228 quad_perm:[1,0,3,2] row_mask:0xf bank_mask:0xf\n\t"
        "v_add_u32_dpp v229, v229, v229 quad_perm:[1,0,3,2] row_mask:0xf bank_mask:0xf\n\t"
        "v_add_u32_dpp v230, v230, v230 quad_perm:[1,0,3,2] row_mask:0xf bank_mask:0xf\n\t"
        "v_add_u32_dpp v231, v231, v231 quad_perm:[1,0,3,2] row_mask:0xf bank_mask:0xf\n\t"
        "v_add_u32_dpp v228, v228, v228 quad_perm:[2,3,0,1] row_mask:0xf bank_mask:0xf\n\t"
        "v_add_u32_dpp v229, v229, v229 quad_perm:[2,3,0,1] row_mask:0xf bank_mask:0xf\n\t"
        "v_add_u32_dpp v230, v230, v230 quad_perm:[2,3,0,1] row_mask:0xf bank_mask:0xf\n\t"
        "v_add_u32_dpp v231, v231, v231 quad_perm:[2,3,0,1] row_mask:0xf bank_mask:0xf\n\t"
        "v_add_u32_dpp v228, v228, v228 row_half_mirror row_mask:0xf bank_mask:0xf\n\t"
        "v_add_u32_dpp v229, v229, v229 row_half_mirror row_mask:0xf bank_mask:0xf\n\t"
        "v_add_u32_dpp v230, v230, v230 row_half_mirror row_mask:0xf bank_mask:0xf\n\t"
        "v_add_u32_dpp v231, v231, v231 row_half_mirror row_mask:0xf bank_mask:0xf\n\t"
        "v_cmp_ne_u32_sdwa s[72:73], v243, %19 src0_sel:BYTE_3 src1_sel:DWORD\n\t"
        "v_cmp_gt_u32 s[64:65], 2, v228\n\t"
        "v_cmp_eq_u32 s[56:57], 1, v228\n\t"
        "v_cmp_ne_u32_sdwa s[74:75], v247, %19 src0_sel:BYTE_3 src1_sel:DWORD\n\t"
        "v_cmp_gt_u32 s[66:67], 2, v229\n\t"
        "v_cmp_eq_u32 s[58:59], 1, v229\n\t"
        "v_cmp_ne_u32_sdwa s[76:77], v251, %19 src0_sel:BYTE_3 src1_sel:DWORD\n\t"
        "v_cmp_gt_u32 s[68:69], 2, v230\n\t"
        "v_cmp_eq_u32 s[60:61], 1, v230\n\t"
        "v_cmp_ne_u32_sdwa s[78:79], v255, %19 src0_sel:BYTE_3 src1_sel:DWORD\n\t"
        "v_cmp_gt_u32 s[70:71], 2, v231\n\t"
        "v_cmp_eq_u32 s[62:63], 1, v231\n\t"
        "s_and_b64 s[64:65], s[64:65], %0\n\t"
        "s_and_b64 s[72:73], s[72:73], s[64:65]\n\t"
        "s_cselect_b64 s[64:65], 0, s[64:65]\n\t"
        "s_and_b64 s[56:57], s[56:57], s[64:65]\n\t"
        "s_andn2_b64 %0, %0, s[64:65]\n\t"
        "s_cbranch_scc0 10f\n\t"
        "s_mov_b64 exec, %0\n\t"
        "global_load_dwordx4 v[240:243], %4, %16 sc1\n\t"
        "10:\n\t"
        "s_and_b64 s[66:67], s[66:67], %1\n\t"
        "s_and_b64 s[74:75], s[74:75], s[66:67]\n\t"
        "s_cselect_b64 s[66:67], 0, s[66:67]\n\t"
        "s_and_b64 s[58:59], s[58:59], s[66:67]\n\t"
        "s_andn2_b64 %1, %1, s[66:67]\n\t"
        "s_cbranch_scc0 11f\n\t"
        "s_mov_b64 exec, %1\n\t"
        "global_load_dwordx4 v[244:247], %5, %16 sc1\n\t"
        "11:\n\t"
        "s_and_b64 s[68:69], s[68:69], %2\n\t"
        "s_and_b64 s[76:77], s[76:77], s[68:69]\n\t"
        "s_cselect_b64 s[68:69], 0, s[68:69]\n\t"
        "s_and_b64 s[60:61], s[60:61], s[68:69]\n\t"
        "s_andn2_b64 %2, %2, s[68:69]\n\t"
        "s_cbranch_scc0 12f\n\t"
        "s_mov_b64 exec, %2\n\t"
        "global_load_dwordx4 v[248:251], %6, %16 sc1\n\t"
        "12:\n\t"
        "s_and_b64 s[70:71], s[70:71], %3\n\t"
        "s_and_b64 s[78:79], s[78:79], s[70:71]\n\t"
        "s_cselect_b64 s[70:71], 0, s[70:71]\n\t"
        "s_and_b64 s[62:63], s[62:63], s[70:71]\n\t"
        "s_andn2_b64 %3, %3, s[70:71]\n\t"
        "s_cbranch_scc0 13f\n\t"
        "s_mov_b64 exec, %3\n\t"
        "global_load_dwordx4 v[252:255], %7, %16 sc1\n\t"
        "13:\n\t"
        "s_mov_b64 s[54:55], 0\n\t"
        "s_mov_b64 s[72:73], 0\n\t"
        "s_mov_b64 exec, s[64:65]\n\t"
        "s_cbranch_execz 20f\n\t"
        "v_cmpx_ne_u32 s[72:73], -1, v240\n\t"
        "v_bfe_u32 v192, v243, 5, 3\n\t"
        "v_lshl_add_u32 v204, v192, 4, %20\n\t"
        "ds_add_rtn_u32 v204, v204, v191\n\t"
        "v_perm_b32 v216, v243, v240, v177\n\t"
        "v_mad_u32_u16 v228, v240, 32, %12 op_sel:[1,0,0,0]\n\t"
        "v_cmpx_ne_u32 s[74:75], -1, v241\n\t"
        "v_bfe_u32 v193, v243, 13, 3\n\t"
        "v_lshl_add_u32 v205, v193, 4, %20\n\t"
        "ds_add_rtn_u32 v205, v205, v191\n\t"
        "v_perm_b32 v217, v243, v241, v178\n\t"
        "v_mad_u32_u16 v229, v241, 32, %12 op_sel:[1,0,0,0]\n\t"
        "s_and_b64 exec, exec, s[52:53]\n\t"
        "v_cmpx_ne_u32 s[76:77], -1, v242\n\t"
        "v_bfe_u32 v194, v243, 21, 3\n\t"
        "v_lshl_add_u32 v206, v194, 4, %20\n\t"
        "ds_add_rtn_u32 v206, v206, v191\n\t"
        "v_perm_b32 v218, v243, v242, v179\n\t"
        "v_mad_u32_u16 v230, v242, 32, %12 op_sel:[1,0,0,0]\n\t"
        "s_or_b64 s[54:55], s[54:55], s[56:57]\n\t"
        "20:\n\t"
        "s_mov_b64 s[78:79], 0\n\t"
        "s_mov_b64 exec, s[66:67]\n\t"
        "s_cbranch_execz 21f\n\t"
        "v_cmpx_ne_u32 s[78:79], -1, v244\n\t"
        "v_bfe_u32 v195, v247, 5, 3\n\t"
        "v_lshl_add_u32 v207, v195, 4, %20\n\t"
        "ds_add_rtn_u32 v207, v207, v191\n\t"
        "v_perm_b32 v219, v247, v244, v177\n\t"
        "v_mad_u32_u16 v231, v244, 32, %13 op_sel:[1,0,0,0]\n\t"
        "v_cmpx_ne_u32 s[80:81], -1, v245\n\t"
        "v_bfe_u32 v196, v247, 13, 3\n\t"
        "v_lshl_add_u32 v208, v196, 4, %20\n\t"
        "ds_add_rtn_u32 v208, v208, v191\n\t"
        "v_perm_b32 v220, v247, v245, v178\n\t"
        "v_mad_u32_u16 v232, v245, 32, %13 op_sel:[1,0,0,0]\n\t"
        "s_and_b64 exec, exec, s[52:53]\n\t"
        "v_cmpx_ne_u32 s[82:83], -1, v246\n\t"
        "v_bfe_u32 v197, v247, 21, 3\n\t"
        "v_lshl_add_u32 v209, v197, 4, %20\n\t"
        "ds_add_rtn_u32 v209, v209, v191\n\t"
        "v_perm_b32 v221, v247, v246, v179\n\t"
        "v_mad_u32_u16 v233, v246, 32, %13 op_sel:[1,0,0,0]\n\t"
        "s_or_b64 s[54:55], s[54:55], s[58:59]\n\t"
        "21:\n\t"
        "s_mov_b64 s[84:85], 0\n\t"
        "s_mov_b64 exec, s[68:69]\n\t"
        "s_cbranch_execz 22f\n\t"
        "v_cmpx_ne_u32 s[84:85], -1, v248\n\t"
        "v_bfe_u32 v198, v251, 5, 3\n\t"
        "v_lshl_add_u32 v210, v198, 4, %20\n\t"
        "ds_add_rtn_u32 v210, v210, v191\n\t"
        "v_perm_b32 v222, v251, v248, v177\n\t"
        "v_mad_u32_u16 v234, v248, 32, %14 op_sel:[1,0,0,0]\n\t"
        "v_cmpx_ne_u32 s[86:87], -1, v249\n\t"
        "v_bfe_u32 v199, v251, 13, 3\n\t"
        "v_lshl_add_u32 v211, v199, 4, %20\n\t"
        "ds_add_rtn_u32 v211, v211, v191\n\t"
        "v_perm_b32 v223, v251, v249, v178\n\t"
        "v_mad_u32_u16 v235, v249, 32, %14 op_sel:[1,0,0,0]\n\t"
        "s_and_b64 exec, exec, s[52:53]\n\t"
        "v_cmpx_ne_u32 s[88:89], -1, v250\n\t"
        "v_bfe_u32 v200, v251, 21, 3\n\t"
        "v_lshl_add_u32 v212, v200, 4, %20\n\t"
        "ds_add_rtn_u32 v212, v212, v191\n\t"
        "v_perm_b32 v224, v251, v250, v179\n\t"
        "v_mad_u32_u16 v236, v250, 32, %14 op_sel:[1,0,0,0]\n\t"
        "s_or_b64 s[54:55], s[54:55], s[60:61]\n\t"
        "22:\n\t"
        "s_mov_b64 s[90:91], 0\n\t"
        "s_mov_b64 exec, s[70:71]\n\t"
        "s_cbranch_execz 23f\n\t"
        "v_cmpx_ne_u32 s[90:91], -1, v252\n\t"
        "v_bfe_u32 v201, v255, 5, 3\n\t"
        "v_lshl_add_u32 v213, v201, 4, %20\n\t"
        "ds_add_rtn_u32 v213, v213, v191\n\t"
        "v_perm_b32 v225, v255, v252, v177\n\t"
        "v_mad_u32_u16 v237, v252, 32, %15 op_sel:[1,0,0,0]\n\t"
        "v_cmpx_ne_u32 s[92:93], -1, v253\n\t"
        "v_bfe_u32 v202, v255, 13, 3\n\t"
        "v_lshl_add_u32 v214, v202, 4, %20\n\t"
        "ds_add_rtn_u32 v214, v214, v191\n\t"
        "v_perm_b32 v226, v255, v253, v178\n\t"
        "v_mad_u32_u16 v238, v253, 32, %15 op_sel:[1,0,0,0]\n\t"
        "s_and_b64 exec, exec, s[52:53]\n\t"
        "v_cmpx_ne_u32 s[94:95], -1, v254\n\t"
        "v_bfe_u32 v203, v255, 21, 3\n\t"
        "v_lshl_add_u32 v215, v203, 4, %20\n\t"
        "ds_add_rtn_u32 v215, v215, v191\n\t"
        "v_perm_b32 v227, v255, v254, v179\n\t"
        "v_mad_u32_u16 v239, v254, 32, %15 op_sel:[1,0,0,0]\n\t"
        "s_or_b64 s[54:55], s[54:55], s[62:63]\n\t"
        "23:\n\t"
        "s_cmp_eq_u64 s[54:55], 0\n\t"
        "s_cbranch_scc1 59f\n\t"
        "s_mov_b64 exec, s[56:57]\n\t"
        "s_cbranch_execz 40f\n\t"
        "v_bfe_u32 v188, %4, 7, 8\n\t"
        "v_lshrrev_b32 v189, 23, %4\n\t"
        "v_add_u32 v189, 1, v189\n\t"
        "ds_write_b8 v188, v189 offset:256\n\t"
        "v_add_u32 %4, s96, %4\n\t"
        "v_mov_b32 v190, 0\n\t"
        "v_cmpx_eq_u32 vcc, 2, v189\n\t"
        "ds_write_b32 v190, v191 offset:160\n\t"
        "s_andn2_b64 exec, s[56:57], vcc\n\t"
        "s_cbranch_execz 40f\n\t"
        "s_or_b64 %0, %0, exec\n\t"
        "global_load_dwordx4 v[240:243], %4, %16 sc1\n\t"
        "40:\n\t"
        "s_mov_b64 exec, s[58:59]\n\t"
        "s_cbranch_execz 41f\n\t"
        "v_bfe_u32 v188, %5, 7, 8\n\t"
        "v_lshrrev_b32 v189, 23, %5\n\t"
        "v_add_u32 v189, 1, v189\n\t"
        "ds_write_b8 v188, v189 offset:256\n\t"
        "v_add_u32 %5, s96, %5\n\t"
        "v_mov_b32 v190, 0\n\t"
        "v_cmpx_eq_u32 vcc, 2, v189\n\t"
        "ds_write_b32 v190, v191 offset:160\n\t"
        "s_andn2_b64 exec, s[58:59], vcc\n\t"
        "s_cbranch_execz 41f\n\t"
        "s_or_b64 %1, %1, exec\n\t"
        "global_load_dwordx4 v[244:247], %5, %16 sc1\n\t"
        "41:\n\t"
        "s_mov_b64 exec, s[60:61]\n\t"
        "s_cbranch_execz 42f\n\t"
        "v_bfe_u32 v188, %6, 7, 8\n\t"
        "v_lshrrev_b32 v189, 23, %6\n\t"
        "v_add_u32 v189, 1, v189\n\t"
        "ds_write_b8 v188, v189 offset:256\n\t"
        "v_add_u32 %6, s96, %6\n\t"
        "v_mov_b32 v190, 0\n\t"
        "v_cmpx_eq_u32 vcc, 2, v189\n\t"
        "ds_write_b32 v190, v191 offset:160\n\t"
        "s_andn2_b64 exec, s[60:61], vcc\n\t"
        "s_cbranch_execz 42f\n\t"
        "s_or_b64 %2, %2, exec\n\t"
        "global_load_dwordx4 v[248:251], %6, %16 sc1\n\t"
        "42:\n\t"
        "s_mov_b64 exec, s[62:63]\n\t"
        "s_cbranch_execz 43f\n\t"
        "v_bfe_u32 v188, %7, 7, 8\n\t"
        "v_lshrrev_b32 v189, 23, %7\n\t"
        "v_add_u32 v189, 1, v189\n\t"
        "ds_write_b8 v188, v189 offset:256\n\t"
        "v_add_u32 %7, s96, %7\n\t"
        "v_mov_b32 v190, 0\n\t"
        "v_cmpx_eq_u32 vcc, 2, v189\n\t"
        "ds_write_b32 v190, v191 offset:160\n\t"
        "s_andn2_b64 exec, s[62:63], vcc\n\t"
        "s_cbranch_execz 43f\n\t"
        "s_or_b64 %3, %3, exec\n\t"
        "global_load_dwordx4 v[252:255], %7, %16 sc1\n\t"
        "43:\n\t"
        "59:\n\t"
        "s_waitcnt lgkmcnt(0)\n\t"
        "s_mov_b64 exec, s[72:73]\n\t"
        "s_cbranch_execz 30f\n\t"
        "v_min_u32 v204, v181, v204\n\t"
        "v_min_u32 v205, v181, v205\n\t"
        "v_min_u32 v206, v181, v206\n\t"
        "v_mad_u32_u24 v204, v192, v180, v204\n\t"
        "v_mad_u32_u24 v205, v193, v180, v205\n\t"
        "v_mad_u32_u24 v206, v194, v180, v206\n\t"
        "ds_write2st64_b32 v204, v216, v228 offset1:10\n\t"
        "s_mov_b64 exec, s[74:75]\n\t"
        "ds_write2st64_b32 v205, v217, v229 offset1:10\n\t"
        "s_mov_b64 exec, s[76:77]\n\t"
        "ds_write2st64_b32 v206, v218, v230 offset1:10\n\t"
        "30:\n\t"
        "s_mov_b64 exec, s[78:79]\n\t"
        "s_cbranch_execz 31f\n\t"
        "v_min_u32 v207, v181, v207\n\t"
        "v_min_u32 v208, v181, v208\n\t"
        "v_min_u32 v209, v181, v209\n\t"
        "v_mad_u32_u24 v207, v195, v180, v207\n\t"
        "v_mad_u32_u24 v208, v196, v180, v208\n\t"
        "v_mad_u32_u24 v209, v197, v180, v209\n\t"
        "ds_write2st64_b32 v207, v219, v231 offset1:10\n\t"
        "s_mov_b64 exec, s[80:81]\n\t"
        "ds_write2st64_b32 v208, v220, v232 offset1:10\n\t"
        "s_mov_b64 exec, s[82:83]\n\t"
        "ds_write2st64_b32 v209, v221, v233 offset1:10\n\t"
        "31:\n\t"
        "s_mov_b64 exec, s[84:85]\n\t"
        "s_cbranch_execz 32f\n\t"
        "v_min_u32 v210, v181, v210\n\t"
        "v_min_u32 v211, v181, v211\n\t"
        "v_min_u32 v212, v181, v212\n\t"
        "v_mad_u32_u24 v210, v198, v180, v210\n\t"
        "v_mad_u32_u24 v211, v199, v180, v211\n\t"
        "v_mad_u32_u24 v212, v200, v180, v212\n\t"
        "ds_write2st64_b32 v210, v222, v234 offset1:10\n\t"
        "s_mov_b64 exec, s[86:87]\n\t"
        "ds_write2st64_b32 v211, v223, v235 offset1:10\n\t"
        "s_mov_b64 exec, s[88:89]\n\t"
        "ds_write2st64_b32 v212, v224, v236 offset1:10\n\t"
        "32:\n\t"
        "s_mov_b64 exec, s[90:91]\n\t"
        "s_cbranch_execz 33f\n\t"
        "v_min_u32 v213, v181, v213\n\t"
        "v_min_u32 v214, v181, v214\n\t"
        "v_min_u32 v215, v181, v215\n\t"
        "v_mad_u32_u24 v213, v201, v180, v213\n\t"
        "v_mad_u32_u24 v214, v202, v180, v214\n\t"
        "v_mad_u32_u24 v215, v203, v180, v215\n\t"
        "ds_write2st64_b32 v213, v225, v237 offset1:10\n\t"
        "s_mov_b64 exec, s[92:93]\n\t"
        "ds_write2st64_b32 v214, v226, v238 offset1:10\n\t"
        "s_mov_b64 exec, s[94:95]\n\t"
        "ds_write2st64_b32 v215, v227, v239 offset1:10\n\t"
        "33:\n\t"
        "s_mov_b64 exec, -1\n\t"
        : "+&s"(pend[0]), "+&s"(pend[1]), "+&s"(pend[2]), "+&s"(pend[3]), "+v"(vo[0]), "+v"(vo[1]), "+v"(vo[2]),
          "+v"(vo[3]), "+{v[240:243]}"(cv[0]), "+{v[244:247]}"(cv[1]), "+{v[248:251]}"(cv[2]), "+{v[252:255]}"(cv[3])
        : "v"(eb[0]), "v"(eb[1]), "v"(eb[2]), "v"(eb[3]), "s"(ib), "n"(OFF_L + LARR - 16), "v"(tag[0]), "v"(tag[1]),
          "v"(rq4), "s"(just)
        :
          "memory", "vcc", "scc", "v177", "v178", "v179", "v180", "v181", "v188", "v189", "v190", "v191", "v192",
          "v193", "v194", "v195", "v196", "v197", "v198", "v199", "v200", "v201", "v202", "v203", "v204", "v205",
          "v206", "v207", "v208", "v209", "v210", "v211", "v212", "v213", "v214", "v215", "v216", "v217", "v218",
          "v219", "v220", "v221", "v222", "v223", "v224", "v225", "v226", "v227", "v228", "v229", "v230", "v231",
          "v232", "v233", "v234", "v235", "v236", "v237", "v238", "v239", "s52", "s53", "s54", "s55", "s56", "s57",
          "s58", "s59", "s60", "s61", "s62", "s63", "s64", "s65", "s66", "s67", "s68", "s69", "s70", "s71", "s72",
          "s73", "s74", "s75", "s76", "s77", "s78", "s79", "s80", "s81", "s82", "s83", "s84", "s85", "s86", "s87",
          "s88", "s89", "s90", "s91", "s92", "s93", "s94", "s95", "s96");
    uni();
    just = 0u;
  }
  *(LAS unsigned*)((LAS char*)smem + OFF_CNT + 128 + wave * 4) = early;
  asm volatile("s_waitcnt lgkmcnt(0)\n\ts_barrier" ::: "memory");
  if (wave >= 4) __builtin_amdgcn_s_setprio(1);
  const u32x4 cw = *(const LAS u32x4*)((LAS char*)smem + OFF_CNT + rid * 16);
  const unsigned ef = *(const LAS unsigned*)((LAS char*)smem + OFF_CNT + 128 + rid * 4);
  unsigned slowf = *(const LAS unsigned*)((LAS char*)smem + OFF_CNT + 160);
  asm volatile("" : "+v"(slowf));
  const int key = (int)((cw[0] + cw[1] + cw[2] + cw[3]) | rid);
  int gb;
  {
    unsigned acc = 0u;
#define RK(k) acc = __builtin_amdgcn_alignbit(acc, (unsigned)(__builtin_amdgcn_update_dpp(0, key, 0x120 + k, 0xf, 0xf, false) - key), 31)
    RK(1); RK(2); RK(3); RK(4); RK(5); RK(6); RK(7);
#undef RK
    const int want = RANKOF(wave);
    gb = __builtin_ctzll(__ballot(__builtin_popcount(acc) == want)) & 7;
  }
  const unsigned ring0 = lds0 + OFF_R0 + gb * RING_SLOT;
  const unsigned la = l0 + gb * LIST_BYTES;
  bf16x8 XAf[2][2];
#pragma unroll
  for (int q = 0; q < 4; ++q) XAf[q >> 1][q & 1] = *(const LAS bf16x8*)(size_t)(ring0 + ringd + q * 1024 + lane * 16);
  int cnt = (int)(__builtin_amdgcn_readlane(key, gb) - 4u * l0 - 24u) >> 4;
  const unsigned cmn = __builtin_amdgcn_readlane(min(min(cw[0], cw[1]), min(cw[2], cw[3])), gb) & ~15u;
  const unsigned cmx = __builtin_amdgcn_readlane(max(max(cw[0], cw[1]), max(cw[2], cw[3])), gb) & ~15u;
  const bool slow = cnt > LCAP || cmx > l0 + LARR - 16 || __builtin_amdgcn_readfirstlane(slowf) != 0u;
  const unsigned cg = (bu * 8 + gb) * 32;
  aLX += gb * LIST_BYTES; aLE += gb * LIST_BYTES; aLS4 += gb * LIST_BYTES; aLS1 += gb * LIST_BYTES;
  auto issue_gather = [&](int tile, int rs) {
    gather5(ld128(aLX + tile * 128), *(const LAS unsigned*)(size_t)(aLE + tile * 128), ring0 + rs * ringd);
  };
  auto pads = [&](int cnt, int lane) __attribute__((always_inline)) {
    const int u = cnt + lane;
    const unsigned c = cg + ((u >> 2) & 7) + 8 * (u & 3), n = nodeof(c);
    const unsigned padA = n | (__builtin_amdgcn_readlane(c, __builtin_ctzll(__ballot((n * Mw + Cw) >> 16 != c))) & 0xffu) << 16;
    const unsigned pa = la + u * 4;
    *(LAS unsigned*)(size_t)pa = padA;
    *(LAS unsigned*)(size_t)(pa + 256) = padA;
    *(LAS unsigned*)(size_t)(pa + LARR) = 0u;
    *(LAS unsigned*)(size_t)(pa + LARR + 256) = 0u;
  };
  constexpr int GEND = 64 + NCH;
  int gs = slow ? 0 : GEND, gp = 0;
  auto gen = [&]() __attribute__((always_inline)) {
    int l2, n = 0;
    asm volatile("v_mbcnt_lo_u32_b32 %0, -1, 0\n\tv_mbcnt_hi_u32_b32 %0, -1, %0" : "=v"(l2));
    const LAS unsigned char* xf = (const LAS unsigned char*)smem + OFF_XF;
    auto put = [&](bool ok, unsigned t, unsigned c, int cc) {
      ok = ok && (int)(c >> 5) == gb;
      const unsigned long long bm = __ballot(ok);
      const unsigned pa = la + 4u * (n + __builtin_amdgcn_mbcnt_hi((unsigned)(bm >> 32), __builtin_amdgcn_mbcnt_lo((unsigned)bm, 0u)));
      if (ok) {
        *(LAS unsigned*)(size_t)pa = (t & 0xffffu) | (c << 16);
        *(LAS unsigned*)(size_t)(pa + LARR) = ((unsigned)(cc * chunk) + (t >> 16)) * 32u;
      }
      n += __builtin_popcountll(bm);
    };
#pragma unroll 1
    while (gs < GEND && n <= LCAP - 192) {
      if (gs < 64) {
        const int cc = (gs & 31) * 8 + (l2 >> 3);
        const unsigned* pp = reinterpret_cast<const unsigned*>(ib + (gs >> 5) * IEXT + (gs & 31) * 1024 + l2 * 16);
        const bool live = gs < 32 || xf[cc] != 0;
        const unsigned m = ldd(pp + 3);
#pragma unroll 1
        for (int j = 0; j < 3; ++j) {
          const unsigned t = ldd(pp + j);
          put(live && t != ~0u && (j < 2 || (l2 & 7) != 7), t, (m >> (8 * j)) & 0xffu, cc);
        }
        ++gs;
      } else {
        const int c2 = gs - 64 + l2;
        const unsigned long long fm = __ballot(c2 < NCH && xf[min(c2, NCH - 1)] == 2);
        if (fm == 0ull) { gs = min(gs + 64, GEND); continue; }
        gs += __builtin_ctzll(fm);
        const int cc = gs - 64;
        const unsigned long long ds = ldq(FL + 512 + bu * NCH + cc);
        const int len = __builtin_amdgcn_readfirstlane((int)(ds >> 32));
        const unsigned long long* tp = TS + (unsigned)ds;
#pragma unroll 1
        for (; gp < len && n <= LCAP - 64; gp += 64) {
          const unsigned long long t = ldq(tp + min(gp + l2, len - 1));
          put(gp + l2 < len, (unsigned)t, (unsigned)(t >> 32), cc);
        }
        if (gp >= len) { gp = 0; ++gs; }
      }
    }
    __builtin_amdgcn_s_waitcnt(0x0F70);
    pads(n, l2);
    return n;
  };
  const bool quick = !slow && cmn >= l0 + 256u && __builtin_amdgcn_readlane(ef, gb) != 0u;
  auto gather1 = [&](unsigned skip) {
    u32x4 sx = ld128(aLX + 128);
    unsigned re = *(const LAS unsigned*)(size_t)(aLE + 128);
    asm volatile("s_cmp_lg_u32 %16, 0\n\t"
                 "s_cbranch_scc1 1f\n\t"
                 "s_mov_b32 m0, %14\n\t"
                 "v_mad_u32_u16 %0, %0, %15, %9\n\t"
                 "v_mad_u32_u16 %1, %1, %15, %10\n\t"
                 "global_load_lds_dwordx4 %0, %12\n\t"
                 "s_addk_i32 m0, 1024\n\t"
                 "v_mad_u32_u16 %2, %2, %15, %9\n\t"
                 "global_load_lds_dwordx4 %1, %12\n\t"
                 "s_addk_i32 m0, 1024\n\t"
                 "v_mad_u32_u16 %3, %3, %15, %10\n\t"
                 "global_load_lds_dwordx4 %2, %12\n\t"
                 "s_addk_i32 m0, 1024\n\t"
                 "v_add_u32 %4, %4, %11\n\t"
                 "global_load_lds_dwordx4 %3, %12\n\t"
                 "s_addk_i32 m0, 1024\n\t"
                 "s_nop 0\n\t"
                 "global_load_lds_dwordx4 %4, %13\n"
                 "1:"
                 : "+v"(sx[0]), "+v"(sx[1]), "+v"(sx[2]), "+v"(sx[3]), "+v"(re), "+v"(XAf[0][0]), "+v"(XAf[0][1]),
                   "+v"(XAf[1][0]), "+v"(XAf[1][1])
                 : "v"(cx0), "v"(cx1), "v"(ce), "s"(x), "s"(ES), "s"(ring0 + ringd), "s"(128u), "s"(skip)
                 : "memory", "m0", "scc");
  };
  const unsigned g1 = __builtin_amdgcn_readfirstlane(quick && wave >= G1W ? 1u : 0u);
  gather1(g1 ^ 1u);
  if (__builtin_expect(!slow, 1)) {
    const unsigned cl = *(const LAS unsigned*)((LAS char*)smem + OFF_CNT + gb * 16 + (lane & 3) * 4);
    unsigned q = cmn + gb * LIST_BYTES;
#pragma unroll 1
    for (unsigned i = cmn; i < cmx; i += 256u) {
      const unsigned pp = i + lane * 4u;
      const unsigned ea = *(const LAS unsigned*)(size_t)(pp + gb * LIST_BYTES);
      const unsigned eb_ = *(const LAS unsigned*)(size_t)(pp + gb * LIST_BYTES + LARR);
      const unsigned long long bm = __ballot(pp < cl);
      const unsigned pa = q + 4u * __builtin_amdgcn_mbcnt_hi((unsigned)(bm >> 32), __builtin_amdgcn_mbcnt_lo((unsigned)bm, 0u));
      if (pp < cl) {
        *(LAS unsigned*)(size_t)pa = ea;
        *(LAS unsigned*)(size_t)(pa + LARR) = eb_;
      }
      q += 4u * __builtin_popcountll(bm);
    }
    pads(cnt, lane);
  }
#pragma unroll
  for (int ct = 0; ct < 2; ++ct)
#pragma unroll
    for (int m = 6; m < 15; ++m)
      if ((HELD >> m) & 1) wr[ct][m] = wl[widx(m, ct) * 64];
#pragma unroll
  for (int ct = 0; ct < 2; ++ct)
#pragma unroll
    for (int m = 6; m < 15; ++m)
      if ((HELD >> m) & 1) asm volatile("" : "+v"(wr[ct][m]));
#pragma unroll
  for (int t = 0; t < 4; ++t) fo[t] += gb * RING_SLOT;
  foe += gb * RING_SLOT;
  cS -= gb * 32 * TPITCH;
  cSt -= gb * 32 * TPITCH;
  asm volatile("s_waitcnt vmcnt(5)\n\ts_cmp_lg_u32 %7, 0\n\ts_cbranch_scc1 1f\n\ts_waitcnt vmcnt(0)\n1:\n\ts_barrier"
               : "+v"(fo[0]), "+v"(fo[1]), "+v"(fo[2]), "+v"(fo[3]), "+v"(foe), "+v"(cS), "+v"(cSt) : "s"(g1) : "memory", "scc");
  bool redo = !quick;
  gather1(__builtin_amdgcn_readfirstlane(quick ? g1 : 1u));
  const int gbin = bk * 8 + wave;
  auto rd128 = [&](unsigned a) { return *(const LAS bf16x8*)(size_t)a; };
  auto trow = [&](unsigned a, unsigned c) {
    unsigned t;
    asm("v_mad_u32_u16 %0, %1, %2, %3 op_sel:[1,0,0,0]" : "=v"(t) : "v"(a), "s"(TPITCH), "v"(c));
    return t;
  };
  auto load_S = [&](const u32x4 ss) {
    const s16x4 a0 = __builtin_amdgcn_ds_read_tr16_b64_v4i16((LAS s16x4*)(size_t)trow(ss[0], cS));
    const s16x4 a1 = __builtin_amdgcn_ds_read_tr16_b64_v4i16((LAS s16x4*)(size_t)trow(ss[1], cS));
    const s16x4 a2 = __builtin_amdgcn_ds_read_tr16_b64_v4i16((LAS s16x4*)(size_t)trow(ss[2], cS));
    const s16x4 a3 = __builtin_amdgcn_ds_read_tr16_b64_v4i16((LAS s16x4*)(size_t)trow(ss[3], cS));
    S[0] = __builtin_shufflevector(a0, a1, 0, 1, 2, 3, 4, 5, 6, 7);
    S[1] = __builtin_shufflevector(a2, a3, 0, 1, 2, 3, 4, 5, 6, 7);
  };
  auto wf = [&](int m, int ct) { return ((HELD >> m) & 1) ? wr[ct][m] : b[(m - 6) % 3]; };

  auto ew = [&](int m, const f32x16& Gc, const f32x16& Vc, const f32x16& Gn, const f32x16& Vp) {
    if (m < 2) {
      const int i = 14 + m;
      pq[i] = mulf(i, Vp[i], rq[i]);
      if (m == 1) pn[1][3] = pack_bf16(pq[14], pq[15]);
    } else if (m == 2) {
      pq[0] = mulf(0, Vc[0], rq[0]);
      pq[1] = mulf(1, Vc[1], rq[1]);
      pn[0][0] = pack_bf16(pq[0], pq[1]);
    } else {
      const int i = m - 1;
      pq[i] = mulf(i, Vc[i], rq[i]);
      if (i & 1) pn[i >> 3][(i >> 1) & 3] = pack_bf16(pq[i - 1], pq[i]);
    }
    if (m == 0) pd[0][0] = pack_bf16(rq[0], rq[1]);
    else if ((m & 1) == 0) { const int i = m + 1; pd[i >> 3][(i >> 1) & 3] = pack_bf16(rq[i - 1], rq[i]); }
    if (m < 14) {
      const int i = m + 2;
      rq[i] = __builtin_amdgcn_rcpf(add1(tq[i]));
    } else {
      rq[0] = __builtin_amdgcn_rcpf(add1(tq[0]));
      rq[1] = __builtin_amdgcn_rcpf(add1(tq[1]));
    }
    if (m < 13) tq[m + 3] = __builtin_amdgcn_exp2f(Gc[m + 3]);
    else if (m == 13) { tq[0] = __builtin_amdgcn_exp2f(Gn[0]); tq[1] = __builtin_amdgcn_exp2f(Gn[1]); }
    else tq[2] = __builtin_amdgcn_exp2f(Gn[2]);
  };

  auto drain = [&]() {
    pq[14] = mulf(0, V1[14], rq[14]);
    pq[15] = mulf(1, V1[15], rq[15]);
    pn[1][3] = pack_bf16(pq[14], pq[15]);
    Y[1] = MFMA32(__builtin_bit_cast(bf16x8, pd[0]), S[0], Y[1]);
    Y[3] = MFMA32(__builtin_bit_cast(bf16x8, pn[0]), S[0], Y[3]);
    Y[1] = MFMA32(__builtin_bit_cast(bf16x8, pd[1]), S[1], Y[1]);
    Y[3] = MFMA32(__builtin_bit_cast(bf16x8, pn[1]), S[1], Y[3]);
  };
  while (true) {
    if (__builtin_expect(redo, 0)) {
      asm volatile("s_waitcnt vmcnt(0)" ::: "memory");
      if (slow) cnt = gen();
      issue_gather(0, 0);
      issue_gather(1, 1);
    }
    const int ntiles = (cnt + 31) >> 5;
    {
      const unsigned sl = *(const LAS unsigned short*)(size_t)aLS1;
      bf16x8 w0[15];
#pragma unroll
      for (int m = 6; m < 15; ++m) w0[m] = ((HELD >> m) & 1) ? wr[0][m] : wl[widx(m, 0) * 64];
      u32x4 sx = ld128(aLX + 256);
      unsigned re = *(const LAS unsigned*)(size_t)(aLE + 256);
      St[0] = rd128(sl * TPITCH + cSt);
      St[1] = rd128(sl * TPITCH + cSt + 16);
      G0 = MFMA32(St[0], XAf[0][0], zero16);
      G0 = MFMA32(St[1], XAf[1][0], G0);
      pipe_zero();
#pragma unroll
      for (int m = 6; m < 15; ++m)
        if (!((HELD >> m) & 1)) asm volatile("" : "+v"(w0[m]));
      asm volatile("s_waitcnt vmcnt(5)" : "+v"(sx), "+v"(re), "+v"(G0) :: "memory");
#pragma unroll
      for (int k = 0; k < 4; ++k) as[k] = rd128(fo[k]);
      ae = rd128(foe);
      SB();
#pragma unroll
      for (int k = 0; k < 4; ++k) G0 = MFMA32(as[k], w0[7 + k], G0);
      G0 = MFMA32(ae, w0[6], G0);
      V0 = MFMA32(as[0], w0[11], zero16);
      SB();
      glds16x(x, sx[0], cx0, ring0);
      glds16x(x, sx[1], cx1, ring0 + 1024);
      V0 = MFMA32(as[1], w0[12], V0);
      SB();
      glds16x(x, sx[2], cx0, ring0 + 2048);
      tq[0] = __builtin_amdgcn_exp2f(G0[0]);
      tq[1] = __builtin_amdgcn_exp2f(G0[1]);
      V0 = MFMA32(as[2], w0[13], V0);
      SB();
      glds16x(x, sx[3], cx1, ring0 + 3072);
      tq[2] = __builtin_amdgcn_exp2f(G0[2]);
      V0 = MFMA32(as[3], w0[14], V0);
      SB();
      glds16e(ES, re, ce, ring0 + 4096);
      rq[0] = __builtin_amdgcn_rcpf(add1(tq[0]));
      rq[1] = __builtin_amdgcn_rcpf(add1(tq[1]));
      SB();
    }
    unsigned &lS4 = aLS4, &lS1 = aLS1, &lX = aLX, &lE = aLE;
    int adv = 0;
    auto body = [&](const int par) {
      const unsigned lo = par * 128;
      const unsigned ro = (1 - par) * ringd;
      {
        u32x4 ss;
        unsigned sl;
#pragma unroll
        for (int m = 0; m < 15; ++m) {
          if (m == 0) Y[1] = MFMA32(__builtin_bit_cast(bf16x8, pd[0]), S[0], Y[1]);
          else if (m == 1) Y[3] = MFMA32(__builtin_bit_cast(bf16x8, pn[0]), S[0], Y[3]);
          else if (m == 2) Y[1] = MFMA32(__builtin_bit_cast(bf16x8, pd[1]), S[1], Y[1]);
          else if (m == 3) Y[3] = MFMA32(__builtin_bit_cast(bf16x8, pn[1]), S[1], Y[3]);
          else if (m == 4) G1 = MFMA32(St[0], XAf[0][1], zero16);
          else if (m == 5) G1 = MFMA32(St[1], XAf[1][1], G1);
          else if (m == 6) G1 = MFMA32(ae, wf(m, 1), G1);
          else if (m < 11) G1 = MFMA32(as[m - 7], wf(m, 1), G1);
          else if (m == 11) V1 = MFMA32(as[0], wf(m, 1), zero16);
          else V1 = MFMA32(as[m - 11], wf(m, 1), V1);
          SB();
          if (m == 0) { ss = ld128(lS4 + lo); sl = *(const LAS unsigned short*)(size_t)(lS1 + lo + 128); }
          if (m >= 3 && m < 12 && !((HELD >> (m + 3)) & 1)) b[(m - 3) % 3] = wl[widx(m + 3, 1) * 64];
          if (m == 4) load_S(ss);
          if (m == 5) { St[0] = rd128(sl * TPITCH + cSt); St[1] = rd128(sl * TPITCH + cSt + 16); }
          if (m == 6) { asm volatile("s_waitcnt vmcnt(5)" ::: "memory"); ae = rd128(foe + ro); }
          if (m >= 11) as[m - 11] = rd128(fo[m - 11] + ro);
          ew(m, G0, V0, G1, V1);
          SB();
        }
      }
      {
        u32x4 sx;
        unsigned re;
#pragma unroll
        for (int m = 0; m < 15; ++m) {
          if (m == 0) Y[0] = MFMA32(__builtin_bit_cast(bf16x8, pd[0]), S[0], Y[0]);
          else if (m == 1) Y[2] = MFMA32(__builtin_bit_cast(bf16x8, pn[0]), S[0], Y[2]);
          else if (m == 2) Y[0] = MFMA32(__builtin_bit_cast(bf16x8, pd[1]), S[1], Y[0]);
          else if (m == 3) Y[2] = MFMA32(__builtin_bit_cast(bf16x8, pn[1]), S[1], Y[2]);
          else if (m == 4) G0 = MFMA32(St[0], XAf[0][0], zero16);
          else if (m == 5) G0 = MFMA32(St[1], XAf[1][0], G0);
          else if (m == 6) G0 = MFMA32(ae, wf(m, 0), G0);
          else if (m < 11) G0 = MFMA32(as[m - 7], wf(m, 0), G0);
          else if (m == 11) V0 = MFMA32(as[0], wf(m, 0), zero16);
          else V0 = MFMA32(as[m - 11], wf(m, 0), V0);
          SB();
          if (m == 4) { sx = ld128(lX + lo + 3 * 128); re = *(const LAS unsigned*)(size_t)(lE + lo + 3 * 128); }
          if (m >= 3 && m < 12 && !((HELD >> (m + 3)) & 1)) b[(m - 3) % 3] = wl[widx(m + 3, 0) * 64];
          if (m == 7) glds16x(x, sx[0], cx0, ring0 + ro);
          if (m == 8) glds16x(x, sx[1], cx1, ring0 + ro + 1024);
          if (m == 9) glds16x(x, sx[2], cx0, ring0 + ro + 2048);
          if (m == 10) glds16x(x, sx[3], cx1, ring0 + ro + 3072);
          if (m == 11) glds16e(ES, re, ce, ring0 + ro + 4096);
          ew(m, G1, V1, G0, V0);
          SB();
        }
      }
    };
    for (int t = 0; t < ntiles; t += 2) {
      asm volatile("s_cmp_lt_u32 %0, 4\n\ts_cbranch_scc1 1f\n\ts_setprio 1\n1:" :: "s"(wave) : "scc");
      body(0);
      if (t + 1 >= ntiles) break;
      __builtin_amdgcn_s_setprio(0);
      body(1);
      lS4 += 256; lS1 += 256; lX += 256; lE += 256;
      asm volatile("" : "+v"(lS4), "+v"(lS1), "+v"(lX), "+v"(lE));
      adv += 256;
    }
    if (__builtin_expect(gs >= GEND, 1)) break;
    drain();
    lS4 -= adv; lS1 -= adv; lX -= adv; lE -= adv;
    redo = true;
  }

  {
    int lane;
    asm volatile("v_mbcnt_lo_u32_b32 %0, -1, 0\n\tv_mbcnt_hi_u32_b32 %0, -1, %0" : "=v"(lane));
    const int h = lane >> 5;
    const unsigned tu = lds0 + OFF_W + 18 * 1024 + h * 4096 + ((lane >> 2) & 3) * 128 + (lane & 1) * 64 +
                        ((lane >> 4) & 1) * 16 + ((lane >> 1) & 1) * 8;
    bf16x8 wu[8];
#pragma unroll
    for (int k = 0; k < 8; ++k) wu[k] = rawfrag(tu + (k & 3) * 1024 + (k >> 2) * 32);
    SB();
    drain();
    f32x16 X[2], U[2];
#pragma unroll
    for (int i = 0; i < 16; ++i) {
      const unsigned xv = __builtin_bit_cast(u32x4, as[i >> 3])[(i >> 1) & 3];
      X[0][i] = __uint_as_float(i & 1 ? xv & 0xffff0000u : xv << 16);
    }
    SB();
    asm volatile("s_nop 0\n\tv_mfma_f32_32x32x16_bf16 %0, %1, %2, %3" : "=&v"(U[0]) : "v"(wu[0]), "v"(as[0]), "v"(X[0]));
#pragma unroll
    for (int k = 1; k < 4; ++k) U[0] = MFMA32(wu[k], as[k], U[0]);
#pragma unroll
    for (int i = 0; i < 16; ++i) {
      const unsigned xv = __builtin_bit_cast(u32x4, as[2 + (i >> 3)])[(i >> 1) & 3];
      X[1][i] = __uint_as_float(i & 1 ? xv & 0xffff0000u : xv << 16);
    }
    const unsigned nd = nodeof(cg + (lane & 31));
    const bool live = (nd * Mw + Cw) >> 16 == cg + (lane & 31);
    const unsigned lm = (unsigned)__ballot(live);
    const int nodes = __builtin_popcount(lm);
    const unsigned n0 = __builtin_amdgcn_readlane(nd, __builtin_ctz(lm | 0x80000000u));
    const int r = live ? nd - n0 : 31;
    SB();
    asm volatile("s_nop 0\n\tv_mfma_f32_32x32x16_bf16 %0, %1, %2, %3" : "=&v"(U[1]) : "v"(wu[4]), "v"(as[0]), "v"(X[1]));
#pragma unroll
    for (int k = 1; k < 4; ++k) U[1] = MFMA32(wu[4 + k], as[k], U[1]);
#pragma unroll
    for (int i = 0; i < 16; ++i) Y[0][i] = __builtin_amdgcn_rcpf(Y[0][i]);
    SB();
#pragma unroll
    for (int ct = 0; ct < 2; ++ct) {
#pragma unroll
      for (int q = 0; q < 2; ++q) {
        u32x4 ov;
#pragma unroll
        for (int j = 0; j < 4; ++j) {
          const int i = 8 * q + 2 * j;
          if (ct == 0) { Y[1][i] = __builtin_amdgcn_rcpf(Y[1][i]); Y[1][i + 1] = __builtin_amdgcn_rcpf(Y[1][i + 1]); }
          const f32x2 z = __builtin_elementwise_fma(f32x2{Y[2 + ct][i], Y[2 + ct][i + 1]}, f32x2{Y[ct][i], Y[ct][i + 1]},
                                                    f32x2{U[ct][i], U[ct][i + 1]});
          ov[j] = pack_bf16(maxf(z[0], X[ct][i]), maxf(z[1], X[ct][i + 1]));
        }
        *(LAS u32x4*)(size_t)(la + r * 128 + (((4 * h + 2 * ct + q) ^ (r & 7)) * 16)) = ov;
      }
    }
    u32x4 ov[4];
#pragma unroll
    for (int q = 0; q < 4; ++q)
      ov[q] = *(const LAS u32x4*)(size_t)(la + (8 * q + (lane >> 3)) * 128 + (((lane & 7) ^ (lane >> 3)) * 16));
    asm volatile("v_cmpx_lt_i32 vcc, 0, %6\n\t"
                 "global_store_dwordx4 %0, %1, %5 sc1\n\t"
                 "v_cmpx_lt_i32 vcc, 8, %6\n\t"
                 "global_store_dwordx4 %0, %2, %5 offset:1024 sc1\n\t"
                 "v_cmpx_lt_i32 vcc, 16, %6\n\t"
                 "global_store_dwordx4 %0, %3, %5 offset:2048 sc1\n\t"
                 "v_cmpx_lt_i32 vcc, 24, %6\n\t"
                 "global_store_dwordx4 %0, %4, %5 offset:3072 sc1\n\t"
                 "s_mov_b64 exec, -1"
                 :: "v"((unsigned)n0 * 128u + lane * 16u), "v"(ov[0]), "v"(ov[1]), "v"(ov[2]), "v"(ov[3]), "s"(out),
                    "v"(nodes - (lane >> 3))
                 : "memory", "vcc");
  }
  if (bk == 0 && wave == 7) asm volatile("global_store_dwordx2 %0, %1, %2 sc1" :: "v"(0u), "v"(ep), "s"(FL) : "memory");
  asm volatile("s_waitcnt vmcnt(0)" ::: "memory");
}

extern "C" void kernel_launch(void* const* d_in, const int* in_sizes, int n_in,
                              void* d_out, int out_size, void* d_ws, size_t ws_size,
                              hipStream_t stream) {
  const unsigned short* x = (const unsigned short*)d_in[0];
  const u32x4* eattr = (const u32x4*)d_in[1];
  const int* src = (const int*)d_in[2];
  const int* dst = (const int*)d_in[3];
  const unsigned short* wa = (const unsigned short*)d_in[4];
  const unsigned short* wb = (const unsigned short*)d_in[5];
  const unsigned short* wc = (const unsigned short*)d_in[6];
  const unsigned short* wu = (const unsigned short*)d_in[7];
  const unsigned short* wv = (const unsigned short*)d_in[8];
  unsigned short* out = (unsigned short*)d_out;
  const int N = in_sizes[0] / 64;
  const int E = in_sizes[2];
  const int chunk = (((E + NCH - 1) / NCH) + 3) & ~3;

  const bool skew = N > 4096 && N < 60000;
  const unsigned M = N > 256 ? (unsigned)(4294967295.0 / ((double)N * (skew ? 1.0 + SKEWF : 1.0))) : 0xffffffu;
  const unsigned N1 = (0x7fffffffu / M) + 1u;
  const unsigned M2 = skew ? (0xffffffffu - N1 * M) / ((unsigned)N - N1) : M;

  auto fb = [](double v) { return __builtin_bit_cast(unsigned, (float)v); };
  char* ws = (char*)d_ws;
  size_t off = 0;
  off = 1 << 20;
  unsigned long long* FL = (unsigned long long*)(ws + off);   off += 1 << 20;
  u32x4* ENT = (u32x4*)(ws + off);                       off += (size_t)2 * NBK * IBOX;
  unsigned long long* TS = (unsigned long long*)(ws + off);

  k_main<<<NBK, 512, 0, stream>>>(x, eattr, src, dst, wa, wb, wc, wu, wv, ENT, TS, FL, out, N, E, chunk, M, N1,
                                  M2 - M, fb(65536.0 / M), fb(65536.0 / M2), fb(32768.0 / M + 0.5),
                                  fb((double)N1 + (32768.0 - (double)N1 * M) / M2 + 0.5)
     );
}
```

```cpp
#include <hip/hip_runtime.h>
#include <hip/hip_bf16.h>
#include <stdint.h>

using bf16x8 = __attribute__((ext_vector_type(8))) short;
using s16x4 = __attribute__((ext_vector_type(4))) short;
using f32x16 = __attribute__((ext_vector_type(16))) float;
using f32x2 = __attribute__((ext_vector_type(2))) float;
using u32x4 = __attribute__((ext_vector_type(4))) unsigned;
using u32x2 = __attribute__((ext_vector_type(2))) unsigned;
using i32x4 = __attribute__((ext_vector_type(4))) int;
using u16x2 = __attribute__((ext_vector_type(2))) unsigned short;

#define MFMA32(a, b, c) __builtin_amdgcn_mfma_f32_32x32x16_bf16((a), (b), (c), 0, 0, 0)
using f16x2 = __attribute__((ext_vector_type(2))) _Float16;
using f16x8 = __attribute__((ext_vector_type(8))) _Float16;
#define MFMAH(a, b, c) __builtin_amdgcn_mfma_f32_32x32x16_f16(__builtin_bit_cast(f16x8, a), __builtin_bit_cast(f16x8, b), (c), 0, 0, 0)
#define EPOL " nt"
#define EPOL0 ""
#define XPF 0
#define XPFAT 0
#define RA 14
#define VK 4
#define LAS __attribute__((address_space(3)))
#define RANKOF(w) ((w) < 4 ? 7 - (w) : (w) - 4)
#define SLEEP "s_sleep 2\n\t"
#define NREP 8
#define REPOF(b) ((b) % NREP)
#define POLLAT 2
#define PRE 0
#define PREPOL "sc1"
#define G1W 4
#define DBGWAIT 0
#define XWARM 0
#define XAT 3
#define CPF 32
#define CPFB 18688
#define WEARLY 1
#define FR 0
#define XAP 0
#define TPOLL 0
#define SKEWF 0.015
#define PFAT 1
#define TW 0
#define TWN 256
#define SRCLATE 1
#define XGAT 0
#define SB() __builtin_amdgcn_sched_barrier(0)

constexpr int NCH = 256;
constexpr int NBK = 256;
constexpr int NBIN = NBK * 8;
constexpr int NFRAG = 39;
constexpr int CHMAX = 3200;
constexpr float NLOG2E = -1.44269504088896340736f;
constexpr float VSC = 1.0f / (1 << VK);
constexpr unsigned TPITCH = 80;

__device__ __forceinline__ unsigned pack_bf16(float lo, float hi) {
  typedef __attribute__((ext_vector_type(2))) float f2;
  typedef __attribute__((ext_vector_type(2))) __bf16 b2;
  f2 v = {lo, hi};
  b2 r = __builtin_convertvector(v, b2);
  return __builtin_bit_cast(unsigned, r);
}

__device__ __forceinline__ int wave_scan(int v) {
  v += __builtin_amdgcn_update_dpp(0, v, 0x111, 0xf, 0xf, false);
  v += __builtin_amdgcn_update_dpp(0, v, 0x112, 0xf, 0xf, false);
  v += __builtin_amdgcn_update_dpp(0, v, 0x114, 0xf, 0xf, false);
  v += __builtin_amdgcn_update_dpp(0, v, 0x118, 0xf, 0xf, false);
  v += __builtin_amdgcn_update_dpp(0, v, 0x142, 0xa, 0xf, false);
  v += __builtin_amdgcn_update_dpp(0, v, 0x143, 0xc, 0xf, false);
  return v;
}

__device__ __forceinline__ void glds16(const void* sbase, unsigned voff, unsigned lds_dst) {
  asm volatile(
      "s_mov_b32 m0, %2\n\t"
      "s_nop 0\n\t"
      "global_load_lds_dwordx4 %0, %1"
      :: "v"(voff), "s"(sbase), "s"(lds_dst) : "memory", "m0");
}
__device__ __forceinline__ void glds16nt(const void* sbase, unsigned voff, unsigned lds_dst) {
  asm volatile(
      "s_mov_b32 m0, %2\n\t"
      "s_nop 0\n\t"
      "global_load_lds_dwordx4 %0, %1 nt"
      :: "v"(voff), "s"(sbase), "s"(lds_dst) : "memory", "m0");
}
__device__ __forceinline__ void glds16x(const void* sbase, unsigned a, unsigned c, unsigned lds_dst) {
  unsigned tmp;
  asm volatile(
      "s_mov_b32 m0, %4\n\t"
      "v_mad_u32_u16 %0, %1, %5, %2\n\t"
      "global_load_lds_dwordx4 %0, %3"
      : "=&v"(tmp) : "v"(a), "v"(c), "s"(sbase), "s"(lds_dst), "s"(128u) : "memory", "m0");
}
__device__ __forceinline__ void glds16e(const void* sbase, unsigned a, unsigned c, unsigned lds_dst) {
  unsigned tmp;
  asm volatile(
      "s_mov_b32 m0, %4\n\t"
      "v_add_u32 %0, %1, %2\n\t"
      "global_load_lds_dwordx4 %0, %3" EPOL
      : "=&v"(tmp) : "v"(a), "v"(c), "s"(sbase), "s"(lds_dst) : "memory", "m0");
}
__device__ __forceinline__ int gload32(const void* sbase, unsigned voff) {
  int r;
  asm volatile("global_load_dword %0, %1, %2" : "=v"(r) : "v"(voff), "s"(sbase) : "memory");
  return r;
}
__device__ __forceinline__ u32x4 gload128(const void* sbase, unsigned voff) {
  u32x4 r;
  asm volatile("global_load_dwordx4 %0, %1, %2" : "=v"(r) : "v"(voff), "s"(sbase) : "memory");
  return r;
}

__device__ __forceinline__ u32x4 gload128nt(const void* sbase, unsigned voff) {
  u32x4 r;
  asm volatile("global_load_dwordx4 %0, %1, %2 nt" : "=v"(r) : "v"(voff), "s"(sbase) : "memory");
  return r;
}
__device__ __forceinline__ void gstore128wt(void* sbase, unsigned voff, u32x4 v) {
  asm volatile("global_store_dwordx4 %0, %1, %2 sc1\n\ts_nop 1" :: "v"(voff), "v"(v), "s"(sbase) : "memory");
}

constexpr int IBOX = 32768;
constexpr int IEXT = 256 * IBOX;
constexpr int CPIT = 208;
constexpr unsigned RINC = 87382;
constexpr int KP_HDR = 0;
constexpr int KP_ENT = KP_HDR + NBK * 4;
constexpr int KP_POS = KP_ENT + NBK * CPIT;
constexpr int KP_LDS = KP_POS + NBK * 4;
constexpr int NWL = 26;
constexpr int NBLOB = 31;

__device__ __forceinline__ unsigned mad24(unsigned a, int b, unsigned c) {
  unsigned r;
  asm("v_mad_i32_i24 %0, %1, %2, %3" : "=v"(r) : "v"(a), "s"(b), "v"(c));
  return r;
}
__device__ __forceinline__ unsigned mul24(unsigned a, unsigned b) {
  unsigned r;
  asm("v_mul_u32_u24 %0, %1, %2" : "=v"(r) : "n"(b), "v"(a));
  return r;
}
__device__ __forceinline__ unsigned mul24s(unsigned a, unsigned b) {
  unsigned r;
  asm("v_mul_u32_u24 %0, %1, %2" : "=v"(r) : "s"(b), "v"(a));
  return r;
}
__device__ __forceinline__ unsigned byte3x(unsigned a, unsigned b) {
  unsigned r;
  asm("v_mul_u32_u24_sdwa %0, %1, %2 dst_sel:DWORD dst_unused:UNUSED_PAD src0_sel:DWORD src1_sel:BYTE_3" : "=v"(r) : "s"(b), "v"(a));
  return r;
}
__device__ __forceinline__ unsigned mulhi24(unsigned a, unsigned b) {
  return (unsigned)(((unsigned long long)(a & 0xffffffu) * (unsigned long long)b) >> 32);
}
__device__ __forceinline__ unsigned dot4(unsigned a, unsigned b, unsigned c) {
  return __builtin_amdgcn_udot4(a, b, c, false);
}

constexpr int RING_SLOT = 5 * 1024;
constexpr int LCAP = 512;
constexpr int LARR = 2560;
constexpr int LIST_BYTES = 2 * LARR;
constexpr int OFF_CNT = 0;
constexpr int OFF_XF = 256;
constexpr int OFF_W = 512;
constexpr int OFF_T1 = OFF_W + NWL * 1024;
constexpr int OFF_T2 = OFF_T1 + 32 * TPITCH;
constexpr int OFF_R0 = OFF_W + NFRAG * 1024;
constexpr int OFF_R1 = OFF_R0 + 8 * RING_SLOT;
constexpr int OFF_L = OFF_R1 + 8 * RING_SLOT;
constexpr int MAIN_LDS = OFF_L + 8 * LIST_BYTES;
static_assert(MAIN_LDS <= 160 * 1024, "LDS");
static_assert(KP_LDS <= 16 * RING_SLOT, "phase 1 lives in the ring");

__device__ __forceinline__ unsigned ldd(const unsigned* p) {
  return __hip_atomic_load(p, __ATOMIC_RELAXED, __HIP_MEMORY_SCOPE_AGENT);
}
__device__ __forceinline__ unsigned long long ldq(const unsigned long long* p) {
  return __hip_atomic_load(p, __ATOMIC_RELAXED, __HIP_MEMORY_SCOPE_AGENT);
}
__device__ __forceinline__ bf16x8 rawfrag(unsigned a) {
  const s16x4 a0 = __builtin_amdgcn_ds_read_tr16_b64_v4i16((LAS s16x4*)(size_t)a);
  const s16x4 a1 = __builtin_amdgcn_ds_read_tr16_b64_v4i16((LAS s16x4*)(size_t)(a + 512));
  return __builtin_shufflevector(a0, a1, 0, 1, 2, 3, 4, 5, 6, 7);
}
__device__ __forceinline__ float maxf(float a, float b) {
  float r;
  asm("v_max_f32 %0, %1, %2" : "=v"(r) : "v"(a), "v"(b));
  return r;
}
__device__ __forceinline__ float add1(float t) { return 1.0f + t; }
__device__ __forceinline__ float mulf(int i, float a, float b) { return (i & 1) ? __builtin_fmaf(a, b, 0.0f) : a * b; }

__global__ __launch_bounds__(512, 2) void k_main(const unsigned short* __restrict__ x,
                                                 const u32x4* __restrict__ ES,
                                                 const int* __restrict__ src, const int* __restrict__ dst,
                                                 const unsigned short* __restrict__ wa_g,
                                                 const unsigned short* __restrict__ wb_g,
                                                 const unsigned short* __restrict__ wc_g,
                                                 const unsigned short* __restrict__ wu_g,
                                                 const unsigned short* __restrict__ wv_g,
                                                 u32x4* ENT, unsigned long long* TS, unsigned long long* FL,
                                                 unsigned short* __restrict__ out, int N, int E, int chunk,
                                                 unsigned M, unsigned N1, unsigned DM, unsigned MF0, unsigned MF1,
                                                 unsigned MB0, unsigned MB1
                                                 ) {
#define PT(i)
  PT(0);
  __shared__ __attribute__((aligned(16))) char smem[MAIN_LDS];
  const int tid0 = threadIdx.x;
  const int wave = __builtin_amdgcn_readfirstlane(tid0 >> 6);
  const int bk = blockIdx.x;
  const unsigned lds0 = (unsigned)(size_t)(LAS char*)smem;

  LAS char* sm = (LAS char*)smem + OFF_R0;
  const unsigned sm0 = lds0 + OFF_R0;
  const int tid = tid0, lane = tid & 63, r = lane & 31, h = lane >> 5;
  const unsigned pc = tid & 7, bq = tid >> 3;
  unsigned ia = sm0 + KP_ENT + bq * CPIT + pc * 16;
#pragma unroll
  for (int s = 0; s < 4; ++s) *(LAS u32x4*)(size_t)(ia + s * 64 * CPIT) = u32x4{~0u, ~0u, ~0u, ~0u};
  *(LAS unsigned*)(sm + KP_HDR + (tid & 255) * 4) = 0u;
  int q0 = 4 * tid + 3, q1 = 4 * tid + 1795;
  unsigned a0 = tid * 16, a1 = tid * 16 + 7168, z0 = 0u;
  unsigned long long m7 = __ballot(tid == 448);
  int n7 = wave == 7 ? 0 : 0x7fffffff;
  unsigned f0 = lane * 128u, f1 = lane * 128u + 8192u, f2 = min(lane * 128u + 16384u, CPFB - 512u);
  unsigned long long p7 = __ballot((wave | (bk & -CPF)) == 7);
  asm volatile("" : "+v"(f0), "+v"(f1), "+v"(f2), "+s"(p7));
  asm volatile("" : "+v"(ia), "+v"(q0), "+v"(q1), "+v"(a0), "+v"(a1), "+v"(z0), "+s"(m7), "+s"(n7) :: "memory");
  SB();
  asm volatile("" :: "s"(x), "s"(ES), "s"(src), "s"(dst), "s"(wa_g), "s"(wb_g), "s"(wc_g), "s"(wu_g), "s"(wv_g), "s"(ENT),
               "s"(TS), "s"(FL), "s"(out), "s"(N), "s"(E), "s"(chunk), "s"(M), "s"(N1), "s"(DM), "s"(MF0), "s"(MF1), "s"(MB0), "s"(MB1));
  const int base = bk * chunk;
  const int ecnt = max(min(chunk, E - base), 0);
  unsigned long long on[2];
  on[0] = __ballot(q0 < min(min(chunk, E - base), n7));
  on[1] = __ballot(q1 < min(min(chunk, E - base), n7));
  a0 += base * 4u;
  a1 += base * 4u;
  u32x4 d[2], sv[2];
  u32x2 e0;
  asm volatile("s_mov_b64 exec, %11\n\t"
               "global_load_dwordx4 %0, %5, %8 nt\n\t"
               "s_mov_b64 exec, %12\n\t"
               "global_load_dwordx4 %1, %6, %8 nt\n\t"
               "s_mov_b64 exec, %13\n\t"
               "global_load_dwordx2 %2, %7, %9 sc1\n\t"
               "s_getpc_b64 s[96:97]\n\t"
               "s_mov_b32 m0, %17\n\t"
               "s_mov_b64 exec, %18\n\t"
               "global_load_lds_dword %14, s[96:97]\n\t"
               "global_load_lds_dword %15, s[96:97]\n\t"
               "global_load_lds_dword %16, s[96:97]\n\t"
               "s_mov_b64 exec, %11\n\t"
               "s_waitcnt lgkmcnt(0)\n\t"
               "s_barrier\n\t"
               "global_load_dwordx4 %3, %5, %10 nt\n\t"
               "s_mov_b64 exec, %12\n\t"
               "global_load_dwordx4 %4, %6, %10 nt\n\t"
               "s_mov_b64 exec, -1"
               : "=&{v[240:243]}"(d[0]), "=&{v[244:247]}"(d[1]), "=&{v[238:239]}"(e0), "=&{v[248:251]}"(sv[0]),
                 "=&{v[252:255]}"(sv[1])
               : "v"(a0), "v"(a1), "v"(z0), "s"(dst), "s"(FL), "s"(src), "s"(on[0]), "s"(on[1]), "s"(m7)
                 , "v"(f0), "v"(f1), "v"(f2), "n"(OFF_L + 7 * LIST_BYTES + 4096), "s"(p7)
               : "memory", "m0", "s96", "s97");
  SB();
  const int bu = (bk >> 1) | ((bk & 1) << 7);
  const unsigned Mw = bk & 1 ? M + DM : M, Cw = bk & 1 ? 0u - N1 * DM : 0u;
  unsigned MF = bk & 1 ? MF1 : MF0, MB = bk & 1 ? MB1 : MB0;
  asm volatile("" : "+s"(MF), "+s"(MB));
  char* ib = reinterpret_cast<char*>(ENT) + (size_t)bu * IBOX;
  auto nodeof = [&](unsigned c) { return min((unsigned)__builtin_fmaf((float)c, __uint_as_float(MF), __uint_as_float(MB)), (unsigned)N - 1u); };
  unsigned tup[8], val[8], pm[8];
  u32x4 xg[4];
  const unsigned xn0 = __builtin_amdgcn_readfirstlane(nodeof((bu * 8 + wave) * 32));
  unsigned xd[4];
  auto xprep = [&]() {
#pragma unroll
    for (int i = 0; i < 4; ++i)
      xd[i] = min(xn0 + 8 * i + (lane >> 3), N - 1u) * 128u + ((((lane & 7) ^ (lane >> 4)) * 16) ^ (i & 1) * 64) - i * 1024u;
    asm volatile("" : "+v"(xd[0]), "+v"(xd[1]), "+v"(xd[2]), "+v"(xd[3]));
  };
  auto xdma = [&]() {
    asm volatile("s_mov_b32 m0, %5\n\ts_nop 0\n\t"
                 "global_load_lds_dwordx4 %0, %4\n\t"
                 "global_load_lds_dwordx4 %1, %4 offset:1024\n\t"
                 "global_load_lds_dwordx4 %2, %4 offset:2048\n\t"
                 "global_load_lds_dwordx4 %3, %4 offset:3072"
                 :: "v"(xd[0]), "v"(xd[1]), "v"(xd[2]), "v"(xd[3]), "s"(x), "s"(lds0 + OFF_L + wave * LIST_BYTES)
                 : "memory", "m0");
  };
  auto wload = [&](const unsigned short* w, int f) {
    asm volatile("s_mov_b32 m0, %3\n\ts_nop 0\n\t"
                 "global_load_lds_dwordx4 %0, %2\n\t"
                 "global_load_lds_dwordx4 %0, %2 offset:1024\n\t"
                 "global_load_lds_dwordx4 %0, %2 offset:2048\n\t"
                 "global_load_lds_dwordx4 %0, %2 offset:3072\n\t"
                 "s_addk_i32 m0, 4096\n\ts_nop 0\n\t"
                 "global_load_lds_dwordx4 %1, %2\n\t"
                 "global_load_lds_dwordx4 %1, %2 offset:1024\n\t"
                 "global_load_lds_dwordx4 %1, %2 offset:2048\n\t"
                 "global_load_lds_dwordx4 %1, %2 offset:3072"
                 :: "v"(lane * 16u), "v"(lane * 16u + 4096u), "s"(w), "s"(lds0 + OFF_W + f * 1024) : "memory", "m0", "scc");
  };
  auto wall = [&]() {
    wload(wa_g, NBLOB);
    wload(wb_g, 0);
    wload(wv_g, 8);
    glds16(wc_g, lane * 16, lds0 + OFF_W + 16 * 1024);
    glds16(wc_g + 512, lane * 16, lds0 + OFF_W + 17 * 1024);
  };
  auto xpf = [&](int n, unsigned first) {
    unsigned a = min(((unsigned)(bk >> 3) * (XPF * 64u) + first + lane) * 128u, (unsigned)(N - 1) * 128u);
    asm volatile("s_mov_b32 m0, %0" :: "n"(OFF_L + 7 * LIST_BYTES + 4096) : "m0");
#pragma unroll
    for (int i = 0; i < n; ++i) {
      asm volatile("global_load_lds_dword %0, %1" :: "v"(a), "s"(x) : "memory");
      a = min(a + 8192u, (unsigned)(N - 1) * 128u);
    }
  };
  int spin = 1 << 20;
  u32x2 ep = {0u, 0u};
  if (wave == 7) {
    wall();
    auto touch = [&](const void* p, unsigned stride) {
      asm volatile("s_mov_b32 m0, %2\n\ts_mov_b64 exec, %3\n\tglobal_load_lds_dword %0, %1 sc1\n\ts_mov_b64 exec, -1"
                   :: "v"(lane * stride), "s"(p), "n"(OFF_L + 7 * LIST_BYTES + 4096), "s"(__ballot(bk < TWN)) : "memory", "m0");
    };
    if (TW & 4) touch(ENT, IEXT / 64);
    if (TW & 2) touch(x, (unsigned)(N / 64) * 128u);
    if (TW & 1) touch(ES, (unsigned)(E / 64) * 32u);
    if (TW & 8) touch(out, (unsigned)(N / 64) * 128u);
    *(LAS u32x4*)((LAS char*)smem + OFF_CNT + r * 16) =
        r < 8 ? u32x4{lds0 + OFF_L, lds0 + OFF_L + 4, lds0 + OFF_L + 8, lds0 + OFF_L + 12} : u32x4{0u, 0u, 0u, 0u};
    {
      const unsigned col = h ? ((r >> 1) & 12u) | ((r << 2) & 16u) | (r & 3u) : r;
      LAS char* tr = (LAS char*)smem + OFF_T1 + lane * TPITCH;
#pragma unroll
      for (int j = 0; j < 4; ++j) *(LAS u32x4*)(tr + j * 16) = u32x4{0u, 0u, 0u, 0u};
      *(LAS unsigned short*)(tr + col * 2) = h ? 0x3F80 : 0x3C00;
    }
#pragma unroll
    for (int i = 0; i < 8; ++i) { tup[i] = 0u; val[i] = 0u; pm[i] = 0u; }
    asm volatile("s_waitcnt vmcnt(%1)" : "+{v[238:239]}"(e0) : "n"((WEARLY ? 26 : 0) + (PFAT ? 5 : 0) + __builtin_popcount(TW)) : "memory");
    ep[0] = __builtin_amdgcn_readfirstlane(e0[0]) + 0x9E3779B1u;
    ep[1] = __builtin_amdgcn_readfirstlane(e0[1]) + 0x7F4A7C15u;
    *(LAS u32x2*)((LAS char*)smem + OFF_CNT + 168) = u32x2{ep[0], ep[1] >> 24};
    if (__builtin_expect(ecnt & 3, 0)) {
      const int i = (ecnt & ~3) + lane;
      on[0] = __ballot(i < ecnt);
      if (i < ecnt) {
        pm[0] = (unsigned)dst[base + i] * M + (unsigned)max(dst[base + i] - (int)N1, 0) * DM;
        tup[0] = (unsigned)src[base + i] | (unsigned)i << 16;
        val[0] = __hip_atomic_fetch_add((LAS unsigned*)(sm + KP_HDR) + (pm[0] >> 24), RINC, __ATOMIC_RELAXED, __HIP_MEMORY_SCOPE_WORKGROUP);
        const unsigned u = min(val[0] >> 16, 48u);
        *(LAS unsigned*)(sm + KP_ENT + (pm[0] >> 24) * CPIT + u * 4) = tup[0];
        *(LAS unsigned char*)(sm + KP_ENT + (pm[0] >> 24) * CPIT + ((u * 4) | (u & 3) | 12)) = pm[0] >> 16;
      }
      __builtin_amdgcn_s_waitcnt(0x0F70);
    }
    PT(1);
    xprep();
    xdma();
    if (XPFAT == 0) xpf(XPF, 0u);
  } else {
    xprep();
    unsigned inc = RINC, t0 = (unsigned)tid << 18;
    asm volatile("s_waitcnt vmcnt(2)" : "+{v[240:243]}"(d[0]), "+{v[244:247]}"(d[1]), "+v"(inc), "+v"(t0) :: "memory");
    PT(1);
    unsigned cb[8];
#pragma unroll
    for (int s = 0; s < 2; ++s) {
      unsigned* rr = val + 4 * s;
#pragma unroll
      for (int k = 0; k < 4; ++k) {
        unsigned t;
        asm("v_sub_u32 %0, %1, %2 clamp" : "=v"(t) : "v"(d[s][k]), "s"(N1));
        asm("v_mad_u32_u24 %0, %1, %2, %3" : "=v"(pm[4 * s + k]) : "v"(t), "s"(DM), "v"(mul24s(d[s][k], M)));
      }
#pragma unroll
      for (int k = 0; k < 4; ++k) rr[k] = byte3x(pm[4 * s + k], 4u);
      asm volatile(
          "s_mov_b64 exec, %5\n\t"
          "ds_add_rtn_u32 %0, %0, %4 offset:%6\n\t"
          "ds_add_rtn_u32 %1, %1, %4 offset:%6\n\t"
          "ds_add_rtn_u32 %2, %2, %4 offset:%6\n\t"
          "ds_add_rtn_u32 %3, %3, %4 offset:%6\n\t"
          "s_mov_b64 exec, -1"
          : "+v"(rr[0]), "+v"(rr[1]), "+v"(rr[2]), "+v"(rr[3])
          : "v"(inc), "s"(on[s]), "n"(OFF_R0 + KP_HDR)
          : "memory");
    }
#pragma unroll
    for (int i = 0; i < 8; ++i) cb[i] = byte3x(pm[i], (unsigned)CPIT);
    asm volatile("s_waitcnt vmcnt(0)"
                 : "+{v[248:251]}"(sv[0]), "+{v[252:255]}"(sv[1]), "+v"(cb[0]), "+v"(cb[1]), "+v"(cb[2]), "+v"(cb[3]), "+v"(cb[4]), "+v"(cb[5]),
                   "+v"(cb[6]), "+v"(cb[7])
                 :: "memory");
    xdma();
#pragma unroll
    for (int i = 0; i < 8; ++i) tup[i] = sv[i >> 2][i & 3] + t0 + (((i >> 2) * 1792u + (i & 3)) << 16);
#pragma unroll
    for (int s = 0; s < 2; ++s) {
      asm volatile("s_waitcnt lgkmcnt(%8)"
                   : "+v"(val[4 * s]), "+v"(val[4 * s + 1]), "+v"(val[4 * s + 2]), "+v"(val[4 * s + 3]), "+v"(tup[4 * s]),
                     "+v"(tup[4 * s + 1]), "+v"(tup[4 * s + 2]), "+v"(tup[4 * s + 3])
                   : "n"(DBGWAIT ? 0 : s ? 8 : 4) : "memory");
      unsigned pa[4], pb[4];
#pragma unroll
      for (int k = 0; k < 4; ++k) {
        const unsigned u = min(val[4 * s + k], 48u << 16);
        asm("v_mad_u32_u16 %0, %1, 4, %2 op_sel:[1,0,0,0]" : "=v"(pa[k]) : "v"(u), "v"(cb[4 * s + k]));
        pb[k] = pa[k] | ((u >> 16) & 3u) | 12u;
      }
      asm volatile(
          "s_mov_b64 exec, %16\n\t"
          "ds_write_b32 %0, %8 offset:%17\n\t"
          "ds_write_b8_d16_hi %4, %12 offset:%17\n\t"
          "ds_write_b32 %1, %9 offset:%17\n\t"
          "ds_write_b8_d16_hi %5, %13 offset:%17\n\t"
          "ds_write_b32 %2, %10 offset:%17\n\t"
          "ds_write_b8_d16_hi %6, %14 offset:%17\n\t"
          "ds_write_b32 %3, %11 offset:%17\n\t"
          "ds_write_b8_d16_hi %7, %15 offset:%17\n\t"
          "s_mov_b64 exec, -1"
          :: "v"(pa[0]), "v"(pa[1]), "v"(pa[2]), "v"(pa[3]), "v"(pb[0]), "v"(pb[1]), "v"(pb[2]), "v"(pb[3]),
             "v"(tup[4 * s]), "v"(tup[4 * s + 1]), "v"(tup[4 * s + 2]), "v"(tup[4 * s + 3]), "v"(pm[4 * s]),
             "v"(pm[4 * s + 1]), "v"(pm[4 * s + 2]), "v"(pm[4 * s + 3]), "s"(on[s]), "n"(OFF_R0 + KP_ENT)
          : "memory");
    }
  }
  PT(2);
  u32x2 tag;
  {
    unsigned ha = sm0 + KP_HDR + bq * 4, hb = sm0 + KP_HDR + lane * 16;
    unsigned go = bq * IBOX + bk * 128 + pc * 16;
    asm volatile("s_waitcnt lgkmcnt(0)\n\ts_barrier" : "+v"(ha), "+v"(hb), "+v"(go) :: "memory");
    PT(3);
    tag = *(const LAS u32x2*)((LAS char*)smem + OFF_CNT + 168);
    const u32x4 call = *(const LAS u32x4*)(size_t)hb;
    unsigned cn[4];
    u32x4 v[4];
#pragma unroll
    for (int s = 0; s < 4; ++s) {
      cn[s] = *(const LAS unsigned*)(size_t)(ha + s * 256);
      v[s] = *(const LAS u32x4*)(size_t)(ia + s * 64 * CPIT);
    }
    asm volatile("" : "+v"(v[0]), "+v"(v[1]), "+v"(v[2]), "+v"(v[3]), "+v"(tag));
    if (__builtin_expect(__ballot(max(max(call[0], call[1]), max(call[2], call[3])) > 36u * RINC) != 0ull, 0)) {
      LAS unsigned* ovc = (LAS unsigned*)((LAS char*)smem + OFF_CNT + 164);
      LAS unsigned* pos = (LAS unsigned*)(sm + KP_POS);
      if (tid < NBK) {
        const unsigned u = *(const LAS unsigned*)(sm + KP_HDR + tid * 4) >> 16, n = u - (u >> 2);
        if (n > 36u) {
          const unsigned p = (unsigned)base + __hip_atomic_fetch_add(ovc, n - 36u, __ATOMIC_RELAXED, __HIP_MEMORY_SCOPE_WORKGROUP);
          pos[tid] = p;
          __hip_atomic_store(FL + 512 + tid * NCH + bk, ((unsigned long long)(n - 36u) << 32) | p, __ATOMIC_RELAXED,
                             __HIP_MEMORY_SCOPE_AGENT);
        }
      }
      asm volatile("s_waitcnt lgkmcnt(0)\n\ts_barrier" ::: "memory");
#pragma unroll
      for (int i = 0; i < 8; ++i) {
        const unsigned u = val[i] >> 16, n = u - (u >> 2);
        if (((on[i >> 2] >> lane) & 1ull) && n >= 36u)
          __hip_atomic_store(TS + pos[pm[i] >> 24] + (n - 36u), ((unsigned long long)((pm[i] >> 16) & 0xffu) << 32) | tup[i],
                             __ATOMIC_RELAXED, __HIP_MEMORY_SCOPE_AGENT);
      }
      __builtin_amdgcn_s_waitcnt(0x0F70);
      asm volatile("s_waitcnt vmcnt(0)\n\ts_barrier" ::: "memory");
    }
    auto seal = [&](u32x4& w, bool more) {
      w[3] = __builtin_amdgcn_perm(tag[1], w[3], 0x04020100u);
      unsigned sm_ = w[0] + w[1] + w[2] + w[3] + tag[0];
      sm_ += __builtin_amdgcn_update_dpp(0u, sm_, 0xB1, 0xf, 0xf, false);
      sm_ += __builtin_amdgcn_update_dpp(0u, sm_, 0x4E, 0xf, 0xf, false);
      sm_ += __builtin_amdgcn_update_dpp(0u, sm_, 0x141, 0xf, 0xf, false);
      w[2] = pc == 7u ? w[2] - sm_ + (more ? 1u : 0u) : w[2];
    };
#pragma unroll
    for (int s = 0; s < 4; ++s) seal(v[s], cn[s] > 23u * RINC);
    asm volatile("global_store_dwordx4 %0, %1, %5 sc1\n\t"
                 "global_store_dwordx4 %0, %2, %6 sc1\n\t"
                 "global_store_dwordx4 %0, %3, %7 sc1\n\t"
                 "global_store_dwordx4 %0, %4, %8 sc1\n\t"
                 "s_nop 1"
                 :: "v"(go), "v"(v[0]), "v"(v[1]), "v"(v[2]), "v"(v[3]), "s"(ENT), "s"(ENT + 4 * IBOX), "s"(ENT + 8 * IBOX),
                    "s"(ENT + 12 * IBOX)
                 : "memory");
    PT(4);
    if (__builtin_expect(__ballot(max(max(cn[0], cn[1]), max(cn[2], cn[3])) > 23u * RINC) != 0ull, 0)) {
#pragma unroll 1
      for (int s = 0; s < 4; ++s) {
        const unsigned c = *(const LAS unsigned*)(size_t)(ha + s * 256);
        u32x4 w = *(const LAS u32x4*)(size_t)(ia + s * 64 * CPIT + (pc < 4u ? 128 : 0));
        if (pc == 7u) w = u32x4{w[2], ~0u, ~0u, w[3] >> 16};
        else {
#pragma unroll
          for (int j = 0; j < 3; ++j) w[j] = pc < 4u && 32u + 4u * pc + j < (c >> 16) ? w[j] : ~0u;
        }
        seal(w, c > 36u * RINC);
        if (c > 23u * RINC) gstore128wt(ENT, go + s * (64 * IBOX) + IEXT, w);
      }
    }
    PT(5);
  }
  u32x4 cv[4];
  unsigned vo[4], eb[4];
  unsigned long long pend[4];
#pragma unroll
  for (int k = 0; k < 4; ++k) {
    vo[k] = wave * 4096u + k * 1024u + lane * 16u;
    eb[k] = (unsigned)((wave * 32 + k * 8 + (lane >> 3)) * chunk) * 32u;
  }
#define POLL0()                                                                                                       \
  asm volatile("global_load_dwordx4 %0, %4, %8 sc1\n\t"                                                               \
               "global_load_dwordx4 %1, %5, %8 sc1\n\t"                                                               \
               "global_load_dwordx4 %2, %6, %8 sc1\n\t"                                                               \
               "global_load_dwordx4 %3, %7, %8 sc1"                                                                   \
               : "=&{v[240:243]}"(cv[0]), "=&{v[244:247]}"(cv[1]), "=&{v[248:251]}"(cv[2]), "=&{v[252:255]}"(cv[3])   \
               : "v"(vo[0]), "v"(vo[1]), "v"(vo[2]), "v"(vo[3]), "s"(ib)                                              \
               : "memory")
  const LAS bf16x8* wl = (const LAS bf16x8*)(smem + OFF_W) + lane;
  const f32x16 zero16 = {0.f, 0.f, 0.f, 0.f, 0.f, 0.f, 0.f, 0.f, 0.f, 0.f, 0.f, 0.f, 0.f, 0.f, 0.f, 0.f};
  auto widx = [&](int m, int ct) { return m == 6 ? 16 + ct : m < 11 ? m - 7 + 4 * ct : 8 + m - 11 + 4 * ct; };
#define HELD ((1 << 6) | (1 << 8) | (1 << 10) | (1 << 12) | (1 << 14))
  bf16x8 wr[2][15];
  const unsigned to = lds0 + OFF_W + ((lane >> 2) & 3) * 128 + (lane & 1) * 64 + ((lane >> 4) & 1) * 16 + ((lane >> 1) & 1) * 8;
  auto xa = [&]() {
    bf16x8 wa[8];
#pragma unroll
    for (int k = 0; k < 8; ++k) wa[k] = rawfrag(to + (NBLOB + (k >> 1)) * 1024 + h * 4096 + (k & 1) * 32);
    asm volatile("" : "+v"(wa[0]), "+v"(wa[1]), "+v"(wa[2]), "+v"(wa[3]), "+v"(wa[4]), "+v"(wa[5]), "+v"(wa[6]), "+v"(wa[7]),
                      "+{v[160:163]}"(xg[0]), "+{v[164:167]}"(xg[1]), "+{v[168:171]}"(xg[2]), "+{v[172:175]}"(xg[3]));
    f32x16 XA[2] = {zero16, zero16};
#pragma unroll
    for (int k = 0; k < 4; ++k)
#pragma unroll
      for (int ct = 0; ct < 2; ++ct) XA[ct] = MFMA32(__builtin_bit_cast(bf16x8, xg[k]), wa[k * 2 + ct], XA[ct]);
#pragma unroll
    for (int q = 0; q < 4; ++q) {
      u32x4 xp;
#pragma unroll
      for (int j = 0; j < 4; ++j)
        xp[j] = pack_bf16(XA[q & 1][8 * (q >> 1) + 2 * j] * NLOG2E - RA, XA[q & 1][8 * (q >> 1) + 2 * j + 1] * NLOG2E - RA);
      *(LAS u32x4*)((LAS char*)smem + OFF_R1 + wave * RING_SLOT + q * 1024 + lane * 16) = xp;
    }
  };
  {
    auto prep = [&](unsigned f, unsigned hs, float scale) {
      u32x4 w[2];
#pragma unroll
      for (int ct = 0; ct < 2; ++ct) w[ct] = __builtin_bit_cast(u32x4, rawfrag(to + f + h * hs + ct * 32));
      asm volatile("s_waitcnt lgkmcnt(0)" : "+v"(w[0]), "+v"(w[1]) :: "memory");
#pragma unroll
      for (int ct = 0; ct < 2; ++ct) {
#pragma unroll
        for (int j = 0; j < 4; ++j)
          w[ct][j] = pack_bf16(__uint_as_float(w[ct][j] << 16) * scale, __uint_as_float(w[ct][j] & 0xffff0000u) * scale);
        *(LAS u32x4*)((LAS char*)smem + OFF_W + f + ct * hs + lane * 16) = w[ct];
      }
    };
    {
      const unsigned xr = min(nodeof((bu * 8 + wave) * 32 + r) - xn0, 31u);
      unsigned xa_ = lds0 + OFF_L + wave * LIST_BYTES + xr * 128 + (((4 * h) ^ ((xr >> 1) & 7)) * 16);
      asm volatile("s_waitcnt vmcnt(4)" : "+v"(xa_) :: "memory");
#pragma unroll
      for (int t = 0; t < 4; ++t) xg[t] = *(const LAS u32x4*)(size_t)(xa_ ^ (t * 16));
      asm volatile("" : "+v"(xg[0]), "+v"(xg[1]), "+v"(xg[2]), "+v"(xg[3]) :: "memory");
      *(LAS unsigned*)((LAS char*)smem + OFF_L + wave * LIST_BYTES + h * LARR + r * 4) = ~0u;
    }
    asm volatile("s_waitcnt vmcnt(4) lgkmcnt(0)\n\ts_barrier"
                 : "+v"(vo[0]), "+v"(vo[1]), "+v"(vo[2]), "+v"(vo[3]), "+v"(eb[0]), "+v"(eb[1]), "+v"(eb[2]), "+v"(eb[3])
                 :: "memory");
    xa();
    {
      unsigned f = (wave + (wave & 4)) * 1024, hs = 4096;
      const float scale = wave < 4 ? NLOG2E : VSC;
#pragma unroll 1
      while (true) {
        asm volatile("" : "+s"(f), "+s"(hs));
        prep(f, hs, scale);
        if (f != 0u) break;
        f = 16 * 1024;
        hs = 1024;
      }
    }
    PT(6);
    POLL0();
    glds16(wu_g + wave * 512, lane * 16, lds0 + OFF_W + (18 + wave) * 1024);
  }
  const unsigned l0 = lds0 + OFF_L;
  unsigned aLX = l0 + (lane >> 3) * 16;
  unsigned aLE = l0 + LARR + ((((lane >> 1) & 7) << 2) | (lane >> 4)) * 4;
  unsigned aLS4 = l0 + (4 * h + ((lane >> 2) & 3)) * 16;
  unsigned aLS1 = l0 + (((r & 7) << 2) | (r >> 3)) * 4 + 2;
  unsigned cx0 = (unsigned)(((lane & 7) ^ (lane >> 4)) * 16);
  unsigned cx1 = cx0 ^ 64u;
  unsigned ce = (unsigned)((((lane & 1) ^ ((lane >> 4) & 1))) * 16);
  const unsigned ringd = 8 * RING_SLOT;
  unsigned cS = lds0 + OFF_T1 + (((lane >> 4) & 1) * 16 + (lane & 3) * 4) * 2;
  unsigned cSt = lds0 + OFF_T2 + h * 32;
  unsigned fo[4];
#pragma unroll
  for (int t = 0; t < 4; ++t) fo[t] = lds0 + OFF_R0 + (unsigned)(r * 128 + (((4 * h + t) ^ ((r >> 1) & 7)) * 16));
  unsigned foe = lds0 + OFF_R0 + (unsigned)(4096 + r * 32 + ((h ^ ((r >> 3) & 1)) * 16));
  f32x16 Y[4];
#pragma unroll
  for (int q = 0; q < 4; ++q)
#pragma unroll
    for (int i = 0; i < 16; i += 2) {
      f32x2 y;
      if (q < 2) asm volatile("v_mov_b64 %0, %1" : "=v"(y) : "s"(0x358637bd358637bdull + (RA * 0x0080000000800000ull)));
      else asm volatile("v_mov_b64 %0, 0" : "=v"(y));
      Y[q][i] = y[0];
      Y[q][i + 1] = y[1];
    }
  f32x16 G0, V0, G1, V1;
  bf16x8 S[2], St[2], as[4], ae, b[3];
  unsigned pd[8], pn[8], u0;
  constexpr unsigned cu = RA == 14 ? 0x04000400u : 0x02000200u;
  float tq[16];
  auto pipe_zero = [&]() {
#pragma unroll
    for (int j = 0; j < 8; ++j) { S[0][j] = 0; S[1][j] = 0; }
#pragma unroll
    for (int j = 0; j < 8; ++j) { pd[j] = j < 7 ? 0u : cu; pn[j] = 0u; }
#pragma unroll
    for (int i = 0; i < 16; ++i) V1[i] = 0.f;
  };
  unsigned rid = lane & 7;
  asm volatile("" : "+v"(aLX), "+v"(aLE), "+v"(aLS4), "+v"(aLS1), "+v"(cx0), "+v"(cx1), "+v"(ce), "+v"(cS), "+v"(cSt),
                    "+v"(fo[0]), "+v"(fo[1]), "+v"(fo[2]), "+v"(fo[3]), "+v"(foe), "+v"(rid));
  asm volatile("" : "+v"(Y[0]), "+v"(Y[1]), "+v"(Y[2]), "+v"(Y[3]));
  auto ld128 = [&](unsigned a) { return *(const LAS u32x4*)(size_t)a; };
  auto gather5 = [&](const u32x4 sx, unsigned re, unsigned dstb) {
    glds16x(x, sx[0], cx0, dstb);
    glds16x(x, sx[1], cx1, dstb + 1024);
    glds16x(x, sx[2], cx0, dstb + 2048);
    glds16x(x, sx[3], cx1, dstb + 3072);
    glds16e(ES, re, ce, dstb + 4096);
  };
  unsigned rq4 = ((lane >> 3) & 3) * 4;
  unsigned early, just;
  asm volatile(
        "s_mov_b32 s96, 0x800000\n\t"
        "v_mov_b32 v181, %18\n\t"
        "v_mov_b32 v180, 5120\n\t"
        "v_mov_b32 v177, 0x0c040100\n\t"
        "v_mov_b32 v178, 0x0c050100\n\t"
        "v_mov_b32 v179, 0x0c060100\n\t"
        "s_mov_b32 s52, 0x7f7f7f7f\n\t"
        "s_mov_b32 s53, 0x7f7f7f7f\n\t"
        "s_mov_b64 s[54:55], 0\n\t"
        "s_mov_b32 %12, 0\n\t"
        "v_mov_b32 v191, 16\n\t"
        "s_waitcnt vmcnt(%30)\n\t"
        "v_add3_u32 v228, v240, v241, v242\n\t"
        "v_add3_u32 v231, v244, v245, v246\n\t"
        "v_add3_u32 v228, v228, v243, %19\n\t"
        "v_add3_u32 v231, v231, v247, %19\n\t"
        "v_cmp_ne_u32_sdwa s[72:73], v243, %20 src0_sel:BYTE_3 src1_sel:DWORD\n\t"
        "v_add_u32_dpp v228, v228, v228 quad_perm:[1,0,3,2] row_mask:0xf bank_mask:0xf\n\t"
        "v_add_u32_dpp v231, v231, v231 quad_perm:[1,0,3,2] row_mask:0xf bank_mask:0xf\n\t"
        "v_cmp_ne_u32_sdwa s[78:79], v247, %20 src0_sel:BYTE_3 src1_sel:DWORD\n\t"
        "v_add_u32_dpp v228, v228, v228 quad_perm:[2,3,0,1] row_mask:0xf bank_mask:0xf\n\t"
        "v_add_u32_dpp v231, v231, v231 quad_perm:[2,3,0,1] row_mask:0xf bank_mask:0xf\n\t"
        "s_nop 0\n\t"
        "v_add_u32_dpp v228, v228, v228 row_half_mirror row_mask:0xf bank_mask:0xf\n\t"
        "v_add_u32_dpp v231, v231, v231 row_half_mirror row_mask:0xf bank_mask:0xf\n\t"
        "v_cmp_gt_u32 s[64:65], 2, v228\n\t"
        "v_cmp_eq_u32 s[56:57], 1, v228\n\t"
        "v_cmp_gt_u32 s[66:67], 2, v231\n\t"
        "v_cmp_eq_u32 s[58:59], 1, v231\n\t"
        "s_and_b64 s[72:73], s[72:73], s[64:65]\n\t"
        "s_cselect_b64 s[64:65], 0, s[64:65]\n\t"
        "s_and_b64 s[56:57], s[56:57], s[64:65]\n\t"
        "s_not_b64 %0, s[64:65]\n\t"
        "s_or_b64 s[54:55], s[54:55], s[56:57]\n\t"
        "s_or_b64 s[54:55], s[54:55], %0\n\t"
        "s_and_b64 s[78:79], s[78:79], s[66:67]\n\t"
        "s_cselect_b64 s[66:67], 0, s[66:67]\n\t"
        "s_and_b64 s[58:59], s[58:59], s[66:67]\n\t"
        "s_not_b64 %1, s[66:67]\n\t"
        "s_or_b64 s[54:55], s[54:55], s[58:59]\n\t"
        "s_or_b64 s[54:55], s[54:55], %1\n\t"
        "s_mov_b64 exec, s[64:65]\n\t"
        "v_cmpx_ne_u32 s[72:73], -1, v240\n\t"
        "v_bfe_u32 v192, v243, 5, 3\n\t"
        "v_lshl_add_u32 v204, v192, 4, %21\n\t"
        "ds_add_rtn_u32 v204, v204, v191\n\t"
        "v_perm_b32 v216, v243, v240, v177\n\t"
        "v_mad_u32_u16 v228, v240, 32, %13 op_sel:[1,0,0,0]\n\t"
        "v_cmpx_ne_u32 s[74:75], -1, v241\n\t"
        "v_bfe_u32 v193, v243, 13, 3\n\t"
        "v_lshl_add_u32 v205, v193, 4, %21\n\t"
        "ds_add_rtn_u32 v205, v205, v191\n\t"
        "v_perm_b32 v217, v243, v241, v178\n\t"
        "v_mad_u32_u16 v229, v241, 32, %13 op_sel:[1,0,0,0]\n\t"
        "s_and_b64 exec, exec, s[52:53]\n\t"
        "v_cmpx_ne_u32 s[76:77], -1, v242\n\t"
        "v_bfe_u32 v194, v243, 21, 3\n\t"
        "v_lshl_add_u32 v206, v194, 4, %21\n\t"
        "ds_add_rtn_u32 v206, v206, v191\n\t"
        "v_perm_b32 v218, v243, v242, v179\n\t"
        "v_mad_u32_u16 v230, v242, 32, %13 op_sel:[1,0,0,0]\n\t"
        "s_mov_b64 exec, s[66:67]\n\t"
        "v_cmpx_ne_u32 s[78:79], -1, v244\n\t"
        "v_bfe_u32 v195, v247, 5, 3\n\t"
        "v_lshl_add_u32 v207, v195, 4, %21\n\t"
        "ds_add_rtn_u32 v207, v207, v191\n\t"
        "v_perm_b32 v219, v247, v244, v177\n\t"
        "v_mad_u32_u16 v231, v244, 32, %14 op_sel:[1,0,0,0]\n\t"
        "v_cmpx_ne_u32 s[80:81], -1, v245\n\t"
        "v_bfe_u32 v196, v247, 13, 3\n\t"
        "v_lshl_add_u32 v208, v196, 4, %21\n\t"
        "ds_add_rtn_u32 v208, v208, v191\n\t"
        "v_perm_b32 v220, v247, v245, v178\n\t"
        "v_mad_u32_u16 v232, v245, 32, %14 op_sel:[1,0,0,0]\n\t"
        "s_and_b64 exec, exec, s[52:53]\n\t"
        "v_cmpx_ne_u32 s[82:83], -1, v246\n\t"
        "v_bfe_u32 v197, v247, 21, 3\n\t"
        "v_lshl_add_u32 v209, v197, 4, %21\n\t"
        "ds_add_rtn_u32 v209, v209, v191\n\t"
        "v_perm_b32 v221, v247, v246, v179\n\t"
        "v_mad_u32_u16 v233, v246, 32, %14 op_sel:[1,0,0,0]\n\t"
        "s_mov_b64 exec, -1\n\t"
        "s_waitcnt vmcnt(%31)\n\t"
        "v_add3_u32 v234, v248, v249, v250\n\t"
        "v_add3_u32 v237, v252, v253, v254\n\t"
        "v_add3_u32 v234, v234, v251, %19\n\t"
        "v_add3_u32 v237, v237, v255, %19\n\t"
        "v_cmp_ne_u32_sdwa s[84:85], v251, %20 src0_sel:BYTE_3 src1_sel:DWORD\n\t"
        "v_add_u32_dpp v234, v234, v234 quad_perm:[1,0,3,2] row_mask:0xf bank_mask:0xf\n\t"
        "v_add_u32_dpp v237, v237, v237 quad_perm:[1,0,3,2] row_mask:0xf bank_mask:0xf\n\t"
        "v_cmp_ne_u32_sdwa s[90:91], v255, %20 src0_sel:BYTE_3 src1_sel:DWORD\n\t"
        "v_add_u32_dpp v234, v234, v234 quad_perm:[2,3,0,1] row_mask:0xf bank_mask:0xf\n\t"
        "v_add_u32_dpp v237, v237, v237 quad_perm:[2,3,0,1] row_mask:0xf bank_mask:0xf\n\t"
        "s_nop 0\n\t"
        "v_add_u32_dpp v234, v234, v234 row_half_mirror row_mask:0xf bank_mask:0xf\n\t"
        "v_add_u32_dpp v237, v237, v237 row_half_mirror row_mask:0xf bank_mask:0xf\n\t"
        "v_cmp_gt_u32 s[68:69], 2, v234\n\t"
        "v_cmp_eq_u32 s[60:61], 1, v234\n\t"
        "v_cmp_gt_u32 s[70:71], 2, v237\n\t"
        "v_cmp_eq_u32 s[62:63], 1, v237\n\t"
        "s_and_b64 s[84:85], s[84:85], s[68:69]\n\t"
        "s_cselect_b64 s[68:69], 0, s[68:69]\n\t"
        "s_and_b64 s[60:61], s[60:61], s[68:69]\n\t"
        "s_not_b64 %2, s[68:69]\n\t"
        "s_or_b64 s[54:55], s[54:55], s[60:61]\n\t"
        "s_or_b64 s[54:55], s[54:55], %2\n\t"
        "s_and_b64 s[90:91], s[90:91], s[70:71]\n\t"
        "s_cselect_b64 s[70:71], 0, s[70:71]\n\t"
        "s_and_b64 s[62:63], s[62:63], s[70:71]\n\t"
        "s_not_b64 %3, s[70:71]\n\t"
        "s_or_b64 s[54:55], s[54:55], s[62:63]\n\t"
        "s_or_b64 s[54:55], s[54:55], %3\n\t"
        "s_mov_b64 exec, s[72:73]\n\t"
        "s_waitcnt lgkmcnt(3)\n\t"
        "v_min_u32 v204, v181, v204\n\t"
        "v_min_u32 v205, v181, v205\n\t"
        "v_min_u32 v206, v181, v206\n\t"
        "v_mad_u32_u24 v204, v192, v180, v204\n\t"
        "v_mad_u32_u24 v205, v193, v180, v205\n\t"
        "v_mad_u32_u24 v206, v194, v180, v206\n\t"
        "ds_write2st64_b32 v204, v216, v228 offset1:10\n\t"
        "s_mov_b64 exec, s[74:75]\n\t"
        "ds_write2st64_b32 v205, v217, v229 offset1:10\n\t"
        "s_mov_b64 exec, s[76:77]\n\t"
        "ds_write2st64_b32 v206, v218, v230 offset1:10\n\t"
        "s_mov_b64 exec, s[68:69]\n\t"
        "v_cmpx_ne_u32 s[84:85], -1, v248\n\t"
        "v_bfe_u32 v198, v251, 5, 3\n\t"
        "v_lshl_add_u32 v210, v198, 4, %21\n\t"
        "ds_add_rtn_u32 v210, v210, v191\n\t"
        "v_perm_b32 v222, v251, v248, v177\n\t"
        "v_mad_u32_u16 v234, v248, 32, %15 op_sel:[1,0,0,0]\n\t"
        "v_cmpx_ne_u32 s[86:87], -1, v249\n\t"
        "v_bfe_u32 v199, v251, 13, 3\n\t"
        "v_lshl_add_u32 v211, v199, 4, %21\n\t"
        "ds_add_rtn_u32 v211, v211, v191\n\t"
        "v_perm_b32 v223, v251, v249, v178\n\t"
        "v_mad_u32_u16 v235, v249, 32, %15 op_sel:[1,0,0,0]\n\t"
        "s_and_b64 exec, exec, s[52:53]\n\t"
        "v_cmpx_ne_u32 s[88:89], -1, v250\n\t"
        "v_bfe_u32 v200, v251, 21, 3\n\t"
        "v_lshl_add_u32 v212, v200, 4, %21\n\t"
        "ds_add_rtn_u32 v212, v212, v191\n\t"
        "v_perm_b32 v224, v251, v250, v179\n\t"
        "v_mad_u32_u16 v236, v250, 32, %15 op_sel:[1,0,0,0]\n\t"
        "s_mov_b64 exec, s[78:79]\n\t"
        "s_waitcnt lgkmcnt(6)\n\t"
        "v_min_u32 v207, v181, v207\n\t"
        "v_min_u32 v208, v181, v208\n\t"
        "v_min_u32 v209, v181, v209\n\t"
        "v_mad_u32_u24 v207, v195, v180, v207\n\t"
        "v_mad_u32_u24 v208, v196, v180, v208\n\t"
        "v_mad_u32_u24 v209, v197, v180, v209\n\t"
        "ds_write2st64_b32 v207, v219, v231 offset1:10\n\t"
        "s_mov_b64 exec, s[80:81]\n\t"
        "ds_write2st64_b32 v208, v220, v232 offset1:10\n\t"
        "s_mov_b64 exec, s[82:83]\n\t"
        "ds_write2st64_b32 v209, v221, v233 offset1:10\n\t"
        "s_mov_b64 exec, -1\n\t"
        "ds_read_b128 v[184:187], %22\n\t"
        "ds_read_b32 v183, %23\n\t"
        "s_mov_b64 exec, s[70:71]\n\t"
        "v_cmpx_ne_u32 s[90:91], -1, v252\n\t"
        "v_bfe_u32 v201, v255, 5, 3\n\t"
        "v_lshl_add_u32 v213, v201, 4, %21\n\t"
        "ds_add_rtn_u32 v213, v213, v191\n\t"
        "v_perm_b32 v225, v255, v252, v177\n\t"
        "v_mad_u32_u16 v237, v252, 32, %16 op_sel:[1,0,0,0]\n\t"
        "v_cmpx_ne_u32 s[92:93], -1, v253\n\t"
        "v_bfe_u32 v202, v255, 13, 3\n\t"
        "v_lshl_add_u32 v214, v202, 4, %21\n\t"
        "ds_add_rtn_u32 v214, v214, v191\n\t"
        "v_perm_b32 v226, v255, v253, v178\n\t"
        "v_mad_u32_u16 v238, v253, 32, %16 op_sel:[1,0,0,0]\n\t"
        "s_and_b64 exec, exec, s[52:53]\n\t"
        "v_cmpx_ne_u32 s[94:95], -1, v254\n\t"
        "v_bfe_u32 v203, v255, 21, 3\n\t"
        "v_lshl_add_u32 v215, v203, 4, %21\n\t"
        "ds_add_rtn_u32 v215, v215, v191\n\t"
        "v_perm_b32 v227, v255, v254, v179\n\t"
        "v_mad_u32_u16 v239, v254, 32, %16 op_sel:[1,0,0,0]\n\t"
        "s_cmp_eq_u64 s[54:55], 0\n\t"
        "s_cbranch_scc1 59f\n\t"
        "s_mov_b64 exec, s[56:57]\n\t"
        "s_cbranch_execz 40f\n\t"
        "v_bfe_u32 v188, %4, 7, 8\n\t"
        "v_mov_b32 v189, 1\n\t"
        "ds_write_b8 v188, v189 offset:256\n\t"
        "v_add_u32 %4, s96, %4\n\t"
        "s_or_b64 %0, %0, exec\n\t"
        "40:\n\t"
        "s_mov_b64 exec, %0\n\t"
        "s_cbranch_execz 50f\n\t"
        "global_load_dwordx4 v[240:243], %4, %17 sc1\n\t"
        "50:\n\t"
        "s_mov_b64 exec, s[58:59]\n\t"
        "s_cbranch_execz 41f\n\t"
        "v_bfe_u32 v188, %5, 7, 8\n\t"
        "v_mov_b32 v189, 1\n\t"
        "ds_write_b8 v188, v189 offset:256\n\t"
        "v_add_u32 %5, s96, %5\n\t"
        "s_or_b64 %1, %1, exec\n\t"
        "41:\n\t"
        "s_mov_b64 exec, %1\n\t"
        "s_cbranch_execz 51f\n\t"
        "global_load_dwordx4 v[244:247], %5, %17 sc1\n\t"
        "51:\n\t"
        "s_mov_b64 exec, s[60:61]\n\t"
        "s_cbranch_execz 42f\n\t"
        "v_bfe_u32 v188, %6, 7, 8\n\t"
        "v_mov_b32 v189, 1\n\t"
        "ds_write_b8 v188, v189 offset:256\n\t"
        "v_add_u32 %6, s96, %6\n\t"
        "s_or_b64 %2, %2, exec\n\t"
        "42:\n\t"
        "s_mov_b64 exec, %2\n\t"
        "s_cbranch_execz 52f\n\t"
        "global_load_dwordx4 v[248:251], %6, %17 sc1\n\t"
        "52:\n\t"
        "s_mov_b64 exec, s[62:63]\n\t"
        "s_cbranch_execz 43f\n\t"
        "v_bfe_u32 v188, %7, 7, 8\n\t"
        "v_mov_b32 v189, 1\n\t"
        "ds_write_b8 v188, v189 offset:256\n\t"
        "v_add_u32 %7, s96, %7\n\t"
        "s_or_b64 %3, %3, exec\n\t"
        "43:\n\t"
        "s_mov_b64 exec, %3\n\t"
        "s_cbranch_execz 53f\n\t"
        "global_load_dwordx4 v[252:255], %7, %17 sc1\n\t"
        "53:\n\t"
        "s_waitcnt lgkmcnt(0)\n\t"
        "59:\n\t"
        "s_mov_b64 exec, s[84:85]\n\t"
        "s_waitcnt lgkmcnt(8)\n\t"
        "v_min_u32 v210, v181, v210\n\t"
        "v_min_u32 v211, v181, v211\n\t"
        "v_min_u32 v212, v181, v212\n\t"
        "v_mad_u32_u24 v210, v198, v180, v210\n\t"
        "v_mad_u32_u24 v211, v199, v180, v211\n\t"
        "v_mad_u32_u24 v212, v200, v180, v212\n\t"
        "ds_write2st64_b32 v210, v222, v234 offset1:10\n\t"
        "s_mov_b64 exec, s[86:87]\n\t"
        "ds_write2st64_b32 v211, v223, v235 offset1:10\n\t"
        "s_mov_b64 exec, s[88:89]\n\t"
        "ds_write2st64_b32 v212, v224, v236 offset1:10\n\t"
        "s_mov_b64 exec, -1\n\t"
        "s_movk_i32 s96, 128\n\t"
        "s_mov_b32 m0, %29\n\t"
        "s_waitcnt lgkmcnt(6)\n\t"
        "v_max3_u32 v182, v184, v185, v186\n\t"
        "v_max3_u32 v182, v182, v187, v183\n\t"
        "v_cmp_eq_u32 vcc, -1, v182\n\t"
        "v_mad_u32_u16 v184, v184, s96, %24\n\t"
        "v_mad_u32_u16 v185, v185, s96, %25\n\t"
        "v_mad_u32_u16 v186, v186, s96, %24\n\t"
        "v_mad_u32_u16 v187, v187, s96, %25\n\t"
        "v_add_u32 v183, v183, %26\n\t"
        "s_cbranch_vccnz 60f\n\t"
        "global_load_lds_dwordx4 v184, %27\n\t"
        "s_addk_i32 m0, 1024\n\t"
        "s_nop 0\n\t"
        "global_load_lds_dwordx4 v185, %27\n\t"
        "s_addk_i32 m0, 1024\n\t"
        "s_nop 0\n\t"
        "global_load_lds_dwordx4 v186, %27\n\t"
        "s_addk_i32 m0, 1024\n\t"
        "s_nop 0\n\t"
        "global_load_lds_dwordx4 v187, %27\n\t"
        "s_addk_i32 m0, 1024\n\t"
        "s_nop 0\n\t"
        "global_load_lds_dwordx4 v183, %28" EPOL0 "\n\t"
        "s_mov_b32 %12, 1\n\t"
        "60:\n\t"
        "s_mov_b64 exec, s[90:91]\n\t"
        "s_waitcnt lgkmcnt(3)\n\t"
        "v_min_u32 v213, v181, v213\n\t"
        "v_min_u32 v214, v181, v214\n\t"
        "v_min_u32 v215, v181, v215\n\t"
        "v_mad_u32_u24 v213, v201, v180, v213\n\t"
        "v_mad_u32_u24 v214, v202, v180, v214\n\t"
        "v_mad_u32_u24 v215, v203, v180, v215\n\t"
        "ds_write2st64_b32 v213, v225, v237 offset1:10\n\t"
        "s_mov_b64 exec, s[92:93]\n\t"
        "ds_write2st64_b32 v214, v226, v238 offset1:10\n\t"
        "s_mov_b64 exec, s[94:95]\n\t"
        "ds_write2st64_b32 v215, v227, v239 offset1:10\n\t"
        "s_mov_b64 exec, -1\n\t"
        : "=&s"(pend[0]), "=&s"(pend[1]), "=&s"(pend[2]), "=&s"(pend[3]), "+v"(vo[0]), "+v"(vo[1]), "+v"(vo[2]),
          "+v"(vo[3]), "+{v[240:243]}"(cv[0]), "+{v[244:247]}"(cv[1]), "+{v[248:251]}"(cv[2]), "+{v[252:255]}"(cv[3]),
          "=&s"(early)
        : "v"(eb[0]), "v"(eb[1]), "v"(eb[2]), "v"(eb[3]), "s"(ib), "n"(OFF_L + LARR - 16), "v"(tag[0]), "v"(tag[1]),
          "v"(rq4), "v"(aLX + wave * LIST_BYTES), "v"(aLE + wave * LIST_BYTES), "v"(cx0), "v"(cx1), "v"(ce), "s"(x),
          "s"(ES), "s"(lds0 + OFF_R0 + wave * RING_SLOT), "n"(3), "n"(1)
        :
          "memory", "vcc", "scc", "m0", "v177", "v178", "v179", "v180", "v181", "v182", "v183", "v184", "v185",
          "v186", "v187", "v188", "v189", "v190", "v191", "v192", "v193", "v194", "v195", "v196", "v197", "v198",
          "v199", "v200", "v201", "v202", "v203", "v204", "v205", "v206", "v207", "v208", "v209", "v210", "v211",
          "v212", "v213", "v214", "v215", "v216", "v217", "v218", "v219", "v220", "v221", "v222", "v223", "v224",
          "v225", "v226", "v227", "v228", "v229", "v230", "v231", "v232", "v233", "v234", "v235", "v236", "v237",
          "v238", "v239", "s52", "s53", "s54", "s55", "s56", "s57", "s58", "s59", "s60", "s61", "s62", "s63", "s64",
          "s65", "s66", "s67", "s68", "s69", "s70", "s71", "s72", "s73", "s74", "s75", "s76", "s77", "s78", "s79",
          "s80", "s81", "s82", "s83", "s84", "s85", "s86", "s87", "s88", "s89", "s90", "s91", "s92", "s93", "s94",
          "s95", "s96");
  auto uni = [&]() {
#pragma unroll
    for (int k = 0; k < 4; ++k)
      pend[k] = ((unsigned long long)(unsigned)__builtin_amdgcn_readfirstlane((unsigned)(pend[k] >> 32)) << 32) |
                (unsigned)__builtin_amdgcn_readfirstlane((unsigned)pend[k]);
  };
  uni();
  early = __builtin_amdgcn_readfirstlane(early);
  just = early;
  if (__builtin_expect((pend[0] | pend[1] | pend[2] | pend[3] | (unsigned long long)(early ^ 1u)) != 0ull, 0))
#pragma clang loop unroll(disable)
  while (true) {
    if (!early) {
      const u32x4 sx = ld128(aLX + wave * LIST_BYTES);
      const unsigned re = *(const LAS unsigned*)(size_t)(aLE + wave * LIST_BYTES);
      if (__ballot(max(max(max(sx[0], sx[1]), max(sx[2], sx[3])), re) == ~0u) == 0ull) {
        gather5(sx, re, lds0 + OFF_R0 + wave * RING_SLOT);
        early = 1u;
        just = 1u;
      }
    }
    if ((pend[0] | pend[1] | pend[2] | pend[3]) == 0ull || --spin < 0) break;
    asm volatile(
        "s_mov_b32 s96, 0x800000\n\t"
        "v_mov_b32 v181, %17\n\t"
        "v_mov_b32 v180, 5120\n\t"
        "v_mov_b32 v177, 0x0c040100\n\t"
        "v_mov_b32 v178, 0x0c050100\n\t"
        "v_mov_b32 v179, 0x0c060100\n\t"
        "s_mov_b32 s52, 0x7f7f7f7f\n\t"
        "s_mov_b32 s53, 0x7f7f7f7f\n\t"
        "v_mov_b32 v191, 16\n\t"
        "s_waitcnt vmcnt(5)\n\t"
        "s_cmp_lg_u32 %21, 0\n\t"
        "s_cbranch_scc1 9f\n\t"
        "s_waitcnt vmcnt(0)\n\t"
        "9:\n\t"
        "v_add3_u32 v228, v240, v241, v242\n\t"
        "v_add3_u32 v229, v244, v245, v246\n\t"
        "v_add3_u32 v230, v248, v249, v250\n\t"
        "v_add3_u32 v231, v252, v253, v254\n\t"
        "v_add3_u32 v228, v228, v243, %18\n\t"
        "v_add3_u32 v229, v229, v247, %18\n\t"
        "v_add3_u32 v230, v230, v251, %18\n\t"
        "v_add3_u32 v231, v231, v255, %18\n\t"
        "v_add_u32_dpp v228, v228, v228 quad_perm:[1,0,3,2] row_mask:0xf bank_mask:0xf\n\t"
        "v_add_u32_dpp v229, v229, v229 quad_perm:[1,0,3,2] row_mask:0xf bank_mask:0xf\n\t"
        "v_add_u32_dpp v230, v230, v230 quad_perm:[1,0,3,2] row_mask:0xf bank_mask:0xf\n\t"
        "v_add_u32_dpp v231, v231, v231 quad_perm:[1,0,3,2] row_mask:0xf bank_mask:0xf\n\t"
        "v_add_u32_dpp v228, v228, v228 quad_perm:[2,3,0,1] row_mask:0xf bank_mask:0xf\n\t"
        "v_add_u32_dpp v229, v229, v229 quad_perm:[2,3,0,1] row_mask:0xf bank_mask:0xf\n\t"
        "v_add_u32_dpp v230, v230, v230 quad_perm:[2,3,0,1] row_mask:0xf bank_mask:0xf\n\t"
        "v_add_u32_dpp v231, v231, v231 quad_perm:[2,3,0,1] row_mask:0xf bank_mask:0xf\n\t"
        "v_add_u32_dpp v228, v228, v228 row_half_mirror row_mask:0xf bank_mask:0xf\n\t"
        "v_add_u32_dpp v229, v229, v229 row_half_mirror row_mask:0xf bank_mask:0xf\n\t"
        "v_add_u32_dpp v230, v230, v230 row_half_mirror row_mask:0xf bank_mask:0xf\n\t"
        "v_add_u32_dpp v231, v231, v231 row_half_mirror row_mask:0xf bank_mask:0xf\n\t"
        "v_cmp_ne_u32_sdwa s[72:73], v243, %19 src0_sel:BYTE_3 src1_sel:DWORD\n\t"
        "v_cmp_gt_u32 s[64:65], 2, v228\n\t"
        "v_cmp_eq_u32 s[56:57], 1, v228\n\t"
        "v_cmp_ne_u32_sdwa s[74:75], v247, %19 src0_sel:BYTE_3 src1_sel:DWORD\n\t"
        "v_cmp_gt_u32 s[66:67], 2, v229\n\t"
        "v_cmp_eq_u32 s[58:59], 1, v229\n\t"
        "v_cmp_ne_u32_sdwa s[76:77], v251, %19 src0_sel:BYTE_3 src1_sel:DWORD\n\t"
        "v_cmp_gt_u32 s[68:69], 2, v230\n\t"
        "v_cmp_eq_u32 s[60:61], 1, v230\n\t"
        "v_cmp_ne_u32_sdwa s[78:79], v255, %19 src0_sel:BYTE_3 src1_sel:DWORD\n\t"
        "v_cmp_gt_u32 s[70:71], 2, v231\n\t"
        "v_cmp_eq_u32 s[62:63], 1, v231\n\t"
        "s_and_b64 s[64:65], s[64:65], %0\n\t"
        "s_and_b64 s[72:73], s[72:73], s[64:65]\n\t"
        "s_cselect_b64 s[64:65], 0, s[64:65]\n\t"
        "s_and_b64 s[56:57], s[56:57], s[64:65]\n\t"
        "s_andn2_b64 %0, %0, s[64:65]\n\t"
        "s_cbranch_scc0 10f\n\t"
        "s_mov_b64 exec, %0\n\t"
        "global_load_dwordx4 v[240:243], %4, %16 sc1\n\t"
        "10:\n\t"
        "s_and_b64 s[66:67], s[66:67], %1\n\t"
        "s_and_b64 s[74:75], s[74:75], s[66:67]\n\t"
        "s_cselect_b64 s[66:67], 0, s[66:67]\n\t"
        "s_and_b64 s[58:59], s[58:59], s[66:67]\n\t"
        "s_andn2_b64 %1, %1, s[66:67]\n\t"
        "s_cbranch_scc0 11f\n\t"
        "s_mov_b64 exec, %1\n\t"
        "global_load_dwordx4 v[244:247], %5, %16 sc1\n\t"
        "11:\n\t"
        "s_and_b64 s[68:69], s[68:69], %2\n\t"
        "s_and_b64 s[76:77], s[76:77], s[68:69]\n\t"
        "s_cselect_b64 s[68:69], 0, s[68:69]\n\t"
        "s_and_b64 s[60:61], s[60:61], s[68:69]\n\t"
        "s_andn2_b64 %2, %2, s[68:69]\n\t"
        "s_cbranch_scc0 12f\n\t"
        "s_mov_b64 exec, %2\n\t"
        "global_load_dwordx4 v[248:251], %6, %16 sc1\n\t"
        "12:\n\t"
        "s_and_b64 s[70:71], s[70:71], %3\n\t"
        "s_and_b64 s[78:79], s[78:79], s[70:71]\n\t"
        "s_cselect_b64 s[70:71], 0, s[70:71]\n\t"
        "s_and_b64 s[62:63], s[62:63], s[70:71]\n\t"
        "s_andn2_b64 %3, %3, s[70:71]\n\t"
        "s_cbranch_scc0 13f\n\t"
        "s_mov_b64 exec, %3\n\t"
        "global_load_dwordx4 v[252:255], %7, %16 sc1\n\t"
        "13:\n\t"
        "s_mov_b64 s[54:55], 0\n\t"
        "s_mov_b64 s[72:73], 0\n\t"
        "s_mov_b64 exec, s[64:65]\n\t"
        "s_cbranch_execz 20f\n\t"
        "v_cmpx_ne_u32 s[72:73], -1, v240\n\t"
        "v_bfe_u32 v192, v243, 5, 3\n\t"
        "v_lshl_add_u32 v204, v192, 4, %20\n\t"
        "ds_add_rtn_u32 v204, v204, v191\n\t"
        "v_perm_b32 v216, v243, v240, v177\n\t"
        "v_mad_u32_u16 v228, v240, 32, %12 op_sel:[1,0,0,0]\n\t"
        "v_cmpx_ne_u32 s[74:75], -1, v241\n\t"
        "v_bfe_u32 v193, v243, 13, 3\n\t"
        "v_lshl_add_u32 v205, v193, 4, %20\n\t"
        "ds_add_rtn_u32 v205, v205, v191\n\t"
        "v_perm_b32 v217, v243, v241, v178\n\t"
        "v_mad_u32_u16 v229, v241, 32, %12 op_sel:[1,0,0,0]\n\t"
        "s_and_b64 exec, exec, s[52:53]\n\t"
        "v_cmpx_ne_u32 s[76:77], -1, v242\n\t"
        "v_bfe_u32 v194, v243, 21, 3\n\t"
        "v_lshl_add_u32 v206, v194, 4, %20\n\t"
        "ds_add_rtn_u32 v206, v206, v191\n\t"
        "v_perm_b32 v218, v243, v242, v179\n\t"
        "v_mad_u32_u16 v230, v242, 32, %12 op_sel:[1,0,0,0]\n\t"
        "s_or_b64 s[54:55], s[54:55], s[56:57]\n\t"
        "20:\n\t"
        "s_mov_b64 s[78:79], 0\n\t"
        "s_mov_b64 exec, s[66:67]\n\t"
        "s_cbranch_execz 21f\n\t"
        "v_cmpx_ne_u32 s[78:79], -1, v244\n\t"
        "v_bfe_u32 v195, v247, 5, 3\n\t"
        "v_lshl_add_u32 v207, v195, 4, %20\n\t"
        "ds_add_rtn_u32 v207, v207, v191\n\t"
        "v_perm_b32 v219, v247, v244, v177\n\t"
        "v_mad_u32_u16 v231, v244, 32, %13 op_sel:[1,0,0,0]\n\t"
        "v_cmpx_ne_u32 s[80:81], -1, v245\n\t"
        "v_bfe_u32 v196, v247, 13, 3\n\t"
        "v_lshl_add_u32 v208, v196, 4, %20\n\t"
        "ds_add_rtn_u32 v208, v208, v191\n\t"
        "v_perm_b32 v220, v247, v245, v178\n\t"
        "v_mad_u32_u16 v232, v245, 32, %13 op_sel:[1,0,0,0]\n\t"
        "s_and_b64 exec, exec, s[52:53]\n\t"
        "v_cmpx_ne_u32 s[82:83], -1, v246\n\t"
        "v_bfe_u32 v197, v247, 21, 3\n\t"
        "v_lshl_add_u32 v209, v197, 4, %20\n\t"
        "ds_add_rtn_u32 v209, v209, v191\n\t"
        "v_perm_b32 v221, v247, v246, v179\n\t"
        "v_mad_u32_u16 v233, v246, 32, %13 op_sel:[1,0,0,0]\n\t"
        "s_or_b64 s[54:55], s[54:55], s[58:59]\n\t"
        "21:\n\t"
        "s_mov_b64 s[84:85], 0\n\t"
        "s_mov_b64 exec, s[68:69]\n\t"
        "s_cbranch_execz 22f\n\t"
        "v_cmpx_ne_u32 s[84:85], -1, v248\n\t"
        "v_bfe_u32 v198, v251, 5, 3\n\t"
        "v_lshl_add_u32 v210, v198, 4, %20\n\t"
        "ds_add_rtn_u32 v210, v210, v191\n\t"
        "v_perm_b32 v222, v251, v248, v177\n\t"
        "v_mad_u32_u16 v234, v248, 32, %14 op_sel:[1,0,0,0]\n\t"
        "v_cmpx_ne_u32 s[86:87], -1, v249\n\t"
        "v_bfe_u32 v199, v251, 13, 3\n\t"
        "v_lshl_add_u32 v211, v199, 4, %20\n\t"
        "ds_add_rtn_u32 v211, v211, v191\n\t"
        "v_perm_b32 v223, v251, v249, v178\n\t"
        "v_mad_u32_u16 v235, v249, 32, %14 op_sel:[1,0,0,0]\n\t"
        "s_and_b64 exec, exec, s[52:53]\n\t"
        "v_cmpx_ne_u32 s[88:89], -1, v250\n\t"
        "v_bfe_u32 v200, v251, 21, 3\n\t"
        "v_lshl_add_u32 v212, v200, 4, %20\n\t"
        "ds_add_rtn_u32 v212, v212, v191\n\t"
        "v_perm_b32 v224, v251, v250, v179\n\t"
        "v_mad_u32_u16 v236, v250, 32, %14 op_sel:[1,0,0,0]\n\t"
        "s_or_b64 s[54:55], s[54:55], s[60:61]\n\t"
        "22:\n\t"
        "s_mov_b64 s[90:91], 0\n\t"
        "s_mov_b64 exec, s[70:71]\n\t"
        "s_cbranch_execz 23f\n\t"
        "v_cmpx_ne_u32 s[90:91], -1, v252\n\t"
        "v_bfe_u32 v201, v255, 5, 3\n\t"
        "v_lshl_add_u32 v213, v201, 4, %20\n\t"
        "ds_add_rtn_u32 v213, v213, v191\n\t"
        "v_perm_b32 v225, v255, v252, v177\n\t"
        "v_mad_u32_u16 v237, v252, 32, %15 op_sel:[1,0,0,0]\n\t"
        "v_cmpx_ne_u32 s[92:93], -1, v253\n\t"
        "v_bfe_u32 v202, v255, 13, 3\n\t"
        "v_lshl_add_u32 v214, v202, 4, %20\n\t"
        "ds_add_rtn_u32 v214, v214, v191\n\t"
        "v_perm_b32 v226, v255, v253, v178\n\t"
        "v_mad_u32_u16 v238, v253, 32, %15 op_sel:[1,0,0,0]\n\t"
        "s_and_b64 exec, exec, s[52:53]\n\t"
        "v_cmpx_ne_u32 s[94:95], -1, v254\n\t"
        "v_bfe_u32 v203, v255, 21, 3\n\t"
        "v_lshl_add_u32 v215, v203, 4, %20\n\t"
        "ds_add_rtn_u32 v215, v215, v191\n\t"
        "v_perm_b32 v227, v255, v254, v179\n\t"
        "v_mad_u32_u16 v239, v254, 32, %15 op_sel:[1,0,0,0]\n\t"
        "s_or_b64 s[54:55], s[54:55], s[62:63]\n\t"
        "23:\n\t"
        "s_cmp_eq_u64 s[54:55], 0\n\t"
        "s_cbranch_scc1 59f\n\t"
        "s_mov_b64 exec, s[56:57]\n\t"
        "s_cbranch_execz 40f\n\t"
        "v_bfe_u32 v188, %4, 7, 8\n\t"
        "v_lshrrev_b32 v189, 23, %4\n\t"
        "v_add_u32 v189, 1, v189\n\t"
        "ds_write_b8 v188, v189 offset:256\n\t"
        "v_add_u32 %4, s96, %4\n\t"
        "v_mov_b32 v190, 0\n\t"
        "v_cmpx_eq_u32 vcc, 2, v189\n\t"
        "ds_write_b32 v190, v191 offset:160\n\t"
        "s_andn2_b64 exec, s[56:57], vcc\n\t"
        "s_cbranch_execz 40f\n\t"
        "s_or_b64 %0, %0, exec\n\t"
        "global_load_dwordx4 v[240:243], %4, %16 sc1\n\t"
        "40:\n\t"
        "s_mov_b64 exec, s[58:59]\n\t"
        "s_cbranch_execz 41f\n\t"
        "v_bfe_u32 v188, %5, 7, 8\n\t"
        "v_lshrrev_b32 v189, 23, %5\n\t"
        "v_add_u32 v189, 1, v189\n\t"
        "ds_write_b8 v188, v189 offset:256\n\t"
        "v_add_u32 %5, s96, %5\n\t"
        "v_mov_b32 v190, 0\n\t"
        "v_cmpx_eq_u32 vcc, 2, v189\n\t"
        "ds_write_b32 v190, v191 offset:160\n\t"
        "s_andn2_b64 exec, s[58:59], vcc\n\t"
        "s_cbranch_execz 41f\n\t"
        "s_or_b64 %1, %1, exec\n\t"
        "global_load_dwordx4 v[244:247], %5, %16 sc1\n\t"
        "41:\n\t"
        "s_mov_b64 exec, s[60:61]\n\t"
        "s_cbranch_execz 42f\n\t"
        "v_bfe_u32 v188, %6, 7, 8\n\t"
        "v_lshrrev_b32 v189, 23, %6\n\t"
        "v_add_u32 v189, 1, v189\n\t"
        "ds_write_b8 v188, v189 offset:256\n\t"
        "v_add_u32 %6, s96, %6\n\t"
        "v_mov_b32 v190, 0\n\t"
        "v_cmpx_eq_u32 vcc, 2, v189\n\t"
        "ds_write_b32 v190, v191 offset:160\n\t"
        "s_andn2_b64 exec, s[60:61], vcc\n\t"
        "s_cbranch_execz 42f\n\t"
        "s_or_b64 %2, %2, exec\n\t"
        "global_load_dwordx4 v[248:251], %6, %16 sc1\n\t"
        "42:\n\t"
        "s_mov_b64 exec, s[62:63]\n\t"
        "s_cbranch_execz 43f\n\t"
        "v_bfe_u32 v188, %7, 7, 8\n\t"
        "v_lshrrev_b32 v189, 23, %7\n\t"
        "v_add_u32 v189, 1, v189\n\t"
        "ds_write_b8 v188, v189 offset:256\n\t"
        "v_add_u32 %7, s96, %7\n\t"
        "v_mov_b32 v190, 0\n\t"
        "v_cmpx_eq_u32 vcc, 2, v189\n\t"
        "ds_write_b32 v190, v191 offset:160\n\t"
        "s_andn2_b64 exec, s[62:63], vcc\n\t"
        "s_cbranch_execz 43f\n\t"
        "s_or_b64 %3, %3, exec\n\t"
        "global_load_dwordx4 v[252:255], %7, %16 sc1\n\t"
        "43:\n\t"
        "59:\n\t"
        "s_waitcnt lgkmcnt(0)\n\t"
        "s_mov_b64 exec, s[72:73]\n\t"
        "s_cbranch_execz 30f\n\t"
        "v_min_u32 v204, v181, v204\n\t"
        "v_min_u32 v205, v181, v205\n\t"
        "v_min_u32 v206, v181, v206\n\t"
        "v_mad_u32_u24 v204, v192, v180, v204\n\t"
        "v_mad_u32_u24 v205, v193, v180, v205\n\t"
        "v_mad_u32_u24 v206, v194, v180, v206\n\t"
        "ds_write2st64_b32 v204, v216, v228 offset1:10\n\t"
        "s_mov_b64 exec, s[74:75]\n\t"
        "ds_write2st64_b32 v205, v217, v229 offset1:10\n\t"
        "s_mov_b64 exec, s[76:77]\n\t"
        "ds_write2st64_b32 v206, v218, v230 offset1:10\n\t"
        "30:\n\t"
        "s_mov_b64 exec, s[78:79]\n\t"
        "s_cbranch_execz 31f\n\t"
        "v_min_u32 v207, v181, v207\n\t"
        "v_min_u32 v208, v181, v208\n\t"
        "v_min_u32 v209, v181, v209\n\t"
        "v_mad_u32_u24 v207, v195, v180, v207\n\t"
        "v_mad_u32_u24 v208, v196, v180, v208\n\t"
        "v_mad_u32_u24 v209, v197, v180, v209\n\t"
        "ds_write2st64_b32 v207, v219, v231 offset1:10\n\t"
        "s_mov_b64 exec, s[80:81]\n\t"
        "ds_write2st64_b32 v208, v220, v232 offset1:10\n\t"
        "s_mov_b64 exec, s[82:83]\n\t"
        "ds_write2st64_b32 v209, v221, v233 offset1:10\n\t"
        "31:\n\t"
        "s_mov_b64 exec, s[84:85]\n\t"
        "s_cbranch_execz 32f\n\t"
        "v_min_u32 v210, v181, v210\n\t"
        "v_min_u32 v211, v181, v211\n\t"
        "v_min_u32 v212, v181, v212\n\t"
        "v_mad_u32_u24 v210, v198, v180, v210\n\t"
        "v_mad_u32_u24 v211, v199, v180, v211\n\t"
        "v_mad_u32_u24 v212, v200, v180, v212\n\t"
        "ds_write2st64_b32 v210, v222, v234 offset1:10\n\t"
        "s_mov_b64 exec, s[86:87]\n\t"
        "ds_write2st64_b32 v211, v223, v235 offset1:10\n\t"
        "s_mov_b64 exec, s[88:89]\n\t"
        "ds_write2st64_b32 v212, v224, v236 offset1:10\n\t"
        "32:\n\t"
        "s_mov_b64 exec, s[90:91]\n\t"
        "s_cbranch_execz 33f\n\t"
        "v_min_u32 v213, v181, v213\n\t"
        "v_min_u32 v214, v181, v214\n\t"
        "v_min_u32 v215, v181, v215\n\t"
        "v_mad_u32_u24 v213, v201, v180, v213\n\t"
        "v_mad_u32_u24 v214, v202, v180, v214\n\t"
        "v_mad_u32_u24 v215, v203, v180, v215\n\t"
        "ds_write2st64_b32 v213, v225, v237 offset1:10\n\t"
        "s_mov_b64 exec, s[92:93]\n\t"
        "ds_write2st64_b32 v214, v226, v238 offset1:10\n\t"
        "s_mov_b64 exec, s[94:95]\n\t"
        "ds_write2st64_b32 v215, v227, v239 offset1:10\n\t"
        "33:\n\t"
        "s_mov_b64 exec, -1\n\t"
        : "+&s"(pend[0]), "+&s"(pend[1]), "+&s"(pend[2]), "+&s"(pend[3]), "+v"(vo[0]), "+v"(vo[1]), "+v"(vo[2]),
          "+v"(vo[3]), "+{v[240:243]}"(cv[0]), "+{v[244:247]}"(cv[1]), "+{v[248:251]}"(cv[2]), "+{v[252:255]}"(cv[3])
        : "v"(eb[0]), "v"(eb[1]), "v"(eb[2]), "v"(eb[3]), "s"(ib), "n"(OFF_L + LARR - 16), "v"(tag[0]), "v"(tag[1]),
          "v"(rq4), "s"(just)
        :
          "memory", "vcc", "scc", "v177", "v178", "v179", "v180", "v181", "v188", "v189", "v190", "v191", "v192",
          "v193", "v194", "v195", "v196", "v197", "v198", "v199", "v200", "v201", "v202", "v203", "v204", "v205",
          "v206", "v207", "v208", "v209", "v210", "v211", "v212", "v213", "v214", "v215", "v216", "v217", "v218",
          "v219", "v220", "v221", "v222", "v223", "v224", "v225", "v226", "v227", "v228", "v229", "v230", "v231",
          "v232", "v233", "v234", "v235", "v236", "v237", "v238", "v239", "s52", "s53", "s54", "s55", "s56", "s57",
          "s58", "s59", "s60", "s61", "s62", "s63", "s64", "s65", "s66", "s67", "s68", "s69", "s70", "s71", "s72",
          "s73", "s74", "s75", "s76", "s77", "s78", "s79", "s80", "s81", "s82", "s83", "s84", "s85", "s86", "s87",
          "s88", "s89", "s90", "s91", "s92", "s93", "s94", "s95", "s96");
    uni();
    just = 0u;
  }
  *(LAS unsigned*)((LAS char*)smem + OFF_CNT + 128 + wave * 4) = early;
  asm volatile("s_waitcnt lgkmcnt(0)\n\ts_barrier" ::: "memory");
  if (wave >= 4) __builtin_amdgcn_s_setprio(1);
  const u32x4 cw = *(const LAS u32x4*)((LAS char*)smem + OFF_CNT + rid * 16);
  const unsigned ef = *(const LAS unsigned*)((LAS char*)smem + OFF_CNT + 128 + rid * 4);
  unsigned slowf = *(const LAS unsigned*)((LAS char*)smem + OFF_CNT + 160);
  asm volatile("" : "+v"(slowf));
  const int key = (int)((cw[0] + cw[1] + cw[2] + cw[3]) | rid);
  int gb;
  {
    unsigned acc = 0u;
#define RK(k) acc = __builtin_amdgcn_alignbit(acc, (unsigned)(__builtin_amdgcn_update_dpp(0, key, 0x120 + k, 0xf, 0xf, false) - key), 31)
    RK(1); RK(2); RK(3); RK(4); RK(5); RK(6); RK(7);
#undef RK
    const int want = RANKOF(wave);
    gb = __builtin_ctzll(__ballot(__builtin_popcount(acc) == want)) & 7;
  }
  const unsigned ring0 = lds0 + OFF_R0 + gb * RING_SLOT;
  const unsigned la = l0 + gb * LIST_BYTES;
  bf16x8 XAf[2][2];
#pragma unroll
  for (int q = 0; q < 4; ++q) XAf[q >> 1][q & 1] = *(const LAS bf16x8*)(size_t)(ring0 + ringd + q * 1024 + lane * 16);
  int cnt = (int)(__builtin_amdgcn_readlane(key, gb) - 4u * l0 - 24u) >> 4;
  const unsigned cmn = __builtin_amdgcn_readlane(min(min(cw[0], cw[1]), min(cw[2], cw[3])), gb) & ~15u;
  const unsigned cmx = __builtin_amdgcn_readlane(max(max(cw[0], cw[1]), max(cw[2], cw[3])), gb) & ~15u;
  const bool slow = cnt > LCAP || cmx > l0 + LARR - 16 || __builtin_amdgcn_readfirstlane(slowf) != 0u;
  const unsigned cg = (bu * 8 + gb) * 32;
  aLX += gb * LIST_BYTES; aLE += gb * LIST_BYTES; aLS4 += gb * LIST_BYTES; aLS1 += gb * LIST_BYTES;
  auto issue_gather = [&](int tile, int rs) {
    gather5(ld128(aLX + tile * 128), *(const LAS unsigned*)(size_t)(aLE + tile * 128), ring0 + rs * ringd);
  };
  auto pads = [&](int cnt, int lane) __attribute__((always_inline)) {
    const int u = cnt + lane;
    const unsigned c = cg + ((u >> 2) & 7) + 8 * (u & 3), n = nodeof(c);
    const unsigned padA = n | (__builtin_amdgcn_readlane(c, __builtin_ctzll(__ballot((n * Mw + Cw) >> 16 != c))) & 0xffu) << 16;
    const unsigned pa = la + u * 4;
    *(LAS unsigned*)(size_t)pa = padA;
    *(LAS unsigned*)(size_t)(pa + 256) = padA;
    *(LAS unsigned*)(size_t)(pa + LARR) = 0u;
    *(LAS unsigned*)(size_t)(pa + LARR + 256) = 0u;
  };
  constexpr int GEND = 64 + NCH;
  int gs = slow ? 0 : GEND, gp = 0;
  auto gen = [&]() __attribute__((always_inline)) {
    int l2, n = 0;
    asm volatile("v_mbcnt_lo_u32_b32 %0, -1, 0\n\tv_mbcnt_hi_u32_b32 %0, -1, %0" : "=v"(l2));
    const LAS unsigned char* xf = (const LAS unsigned char*)smem + OFF_XF;
    auto put = [&](bool ok, unsigned t, unsigned c, int cc) {
      ok = ok && (int)(c >> 5) == gb;
      const unsigned long long bm = __ballot(ok);
      const unsigned pa = la + 4u * (n + __builtin_amdgcn_mbcnt_hi((unsigned)(bm >> 32), __builtin_amdgcn_mbcnt_lo((unsigned)bm, 0u)));
      if (ok) {
        *(LAS unsigned*)(size_t)pa = (t & 0xffffu) | (c << 16);
        *(LAS unsigned*)(size_t)(pa + LARR) = ((unsigned)(cc * chunk) + (t >> 16)) * 32u;
      }
      n += __builtin_popcountll(bm);
    };
#pragma unroll 1
    while (gs < GEND && n <= LCAP - 192) {
      if (gs < 64) {
        const int cc = (gs & 31) * 8 + (l2 >> 3);
        const unsigned* pp = reinterpret_cast<const unsigned*>(ib + (gs >> 5) * IEXT + (gs & 31) * 1024 + l2 * 16);
        const bool live = gs < 32 || xf[cc] != 0;
        const unsigned m = ldd(pp + 3);
#pragma unroll 1
        for (int j = 0; j < 3; ++j) {
          const unsigned t = ldd(pp + j);
          put(live && t != ~0u && (j < 2 || (l2 & 7) != 7), t, (m >> (8 * j)) & 0xffu, cc);
        }
        ++gs;
      } else {
        const int c2 = gs - 64 + l2;
        const unsigned long long fm = __ballot(c2 < NCH && xf[min(c2, NCH - 1)] == 2);
        if (fm == 0ull) { gs = min(gs + 64, GEND); continue; }
        gs += __builtin_ctzll(fm);
        const int cc = gs - 64;
        const unsigned long long ds = ldq(FL + 512 + bu * NCH + cc);
        const int len = __builtin_amdgcn_readfirstlane((int)(ds >> 32));
        const unsigned long long* tp = TS + (unsigned)ds;
#pragma unroll 1
        for (; gp < len && n <= LCAP - 64; gp += 64) {
          const unsigned long long t = ldq(tp + min(gp + l2, len - 1));
          put(gp + l2 < len, (unsigned)t, (unsigned)(t >> 32), cc);
        }
        if (gp >= len) { gp = 0; ++gs; }
      }
    }
    __builtin_amdgcn_s_waitcnt(0x0F70);
    pads(n, l2);
    return n;
  };
  const bool quick = !slow && cmn >= l0 + 256u && __builtin_amdgcn_readlane(ef, gb) != 0u;
  auto gather1 = [&](unsigned skip) {
    u32x4 sx = ld128(aLX + 128);
    unsigned re = *(const LAS unsigned*)(size_t)(aLE + 128);
    asm volatile("s_cmp_lg_u32 %16, 0\n\t"
                 "s_cbranch_scc1 1f\n\t"
                 "s_mov_b32 m0, %14\n\t"
                 "v_mad_u32_u16 %0, %0, %15, %9\n\t"
                 "v_mad_u32_u16 %1, %1, %15, %10\n\t"
                 "global_load_lds_dwordx4 %0, %12\n\t"
                 "s_addk_i32 m0, 1024\n\t"
                 "v_mad_u32_u16 %2, %2, %15, %9\n\t"
                 "global_load_lds_dwordx4 %1, %12\n\t"
                 "s_addk_i32 m0, 1024\n\t"
                 "v_mad_u32_u16 %3, %3, %15, %10\n\t"
                 "global_load_lds_dwordx4 %2, %12\n\t"
                 "s_addk_i32 m0, 1024\n\t"
                 "v_add_u32 %4, %4, %11\n\t"
                 "global_load_lds_dwordx4 %3, %12\n\t"
                 "s_addk_i32 m0, 1024\n\t"
                 "s_nop 0\n\t"
                 "global_load_lds_dwordx4 %4, %13" EPOL0 "\n"
                 "1:"
                 : "+v"(sx[0]), "+v"(sx[1]), "+v"(sx[2]), "+v"(sx[3]), "+v"(re), "+v"(XAf[0][0]), "+v"(XAf[0][1]),
                   "+v"(XAf[1][0]), "+v"(XAf[1][1])
                 : "v"(cx0), "v"(cx1), "v"(ce), "s"(x), "s"(ES), "s"(ring0 + ringd), "s"(128u), "s"(skip)
                 : "memory", "m0", "scc");
  };
  const unsigned g1 = __builtin_amdgcn_readfirstlane(quick && wave >= G1W ? 1u : 0u);
  gather1(g1 ^ 1u);
  if (__builtin_expect(!slow, 1)) {
    const unsigned cl = *(const LAS unsigned*)((LAS char*)smem + OFF_CNT + gb * 16 + (lane & 3) * 4);
    unsigned q = cmn + gb * LIST_BYTES;
#pragma unroll 1
    for (unsigned i = cmn; i < cmx; i += 256u) {
      const unsigned pp = i + lane * 4u;
      const unsigned ea = *(const LAS unsigned*)(size_t)(pp + gb * LIST_BYTES);
      const unsigned eb_ = *(const LAS unsigned*)(size_t)(pp + gb * LIST_BYTES + LARR);
      const unsigned long long bm = __ballot(pp < cl);
      const unsigned pa = q + 4u * __builtin_amdgcn_mbcnt_hi((unsigned)(bm >> 32), __builtin_amdgcn_mbcnt_lo((unsigned)bm, 0u));
      if (pp < cl) {
        *(LAS unsigned*)(size_t)pa = ea;
        *(LAS unsigned*)(size_t)(pa + LARR) = eb_;
      }
      q += 4u * __builtin_popcountll(bm);
    }
    pads(cnt, lane);
  }
#pragma unroll
  for (int ct = 0; ct < 2; ++ct)
#pragma unroll
    for (int m = 6; m < 15; ++m)
      if ((HELD >> m) & 1) wr[ct][m] = wl[widx(m, ct) * 64];
#pragma unroll
  for (int ct = 0; ct < 2; ++ct)
#pragma unroll
    for (int m = 6; m < 15; ++m)
      if ((HELD >> m) & 1) asm volatile("" : "+v"(wr[ct][m]));
#pragma unroll
  for (int t = 0; t < 4; ++t) fo[t] += gb * RING_SLOT;
  foe += gb * RING_SLOT;
  cS -= gb * 32 * TPITCH;
  cSt -= gb * 32 * TPITCH;
  asm volatile("s_waitcnt vmcnt(5)\n\ts_cmp_lg_u32 %7, 0\n\ts_cbranch_scc1 1f\n\ts_waitcnt vmcnt(0)\n1:\n\ts_barrier"
               : "+v"(fo[0]), "+v"(fo[1]), "+v"(fo[2]), "+v"(fo[3]), "+v"(foe), "+v"(cS), "+v"(cSt) : "s"(g1) : "memory", "scc");
  bool redo = !quick;
  gather1(__builtin_amdgcn_readfirstlane(quick ? g1 : 1u));
  const int gbin = bk * 8 + wave;
  auto rd128 = [&](unsigned a) { return *(const LAS bf16x8*)(size_t)a; };
  auto trow = [&](unsigned a, unsigned c) {
    unsigned t;
    asm("v_mad_u32_u16 %0, %1, %2, %3 op_sel:[1,0,0,0]" : "=v"(t) : "v"(a), "s"(TPITCH), "v"(c));
    return t;
  };
  auto load_S = [&](const u32x4 ss) {
    const s16x4 a0 = __builtin_amdgcn_ds_read_tr16_b64_v4i16((LAS s16x4*)(size_t)trow(ss[0], cS));
    const s16x4 a1 = __builtin_amdgcn_ds_read_tr16_b64_v4i16((LAS s16x4*)(size_t)trow(ss[1], cS));
    const s16x4 a2 = __builtin_amdgcn_ds_read_tr16_b64_v4i16((LAS s16x4*)(size_t)trow(ss[2], cS));
    const s16x4 a3 = __builtin_amdgcn_ds_read_tr16_b64_v4i16((LAS s16x4*)(size_t)trow(ss[3], cS));
    S[0] = __builtin_shufflevector(a0, a1, 0, 1, 2, 3, 4, 5, 6, 7);
    S[1] = __builtin_shufflevector(a2, a3, 0, 1, 2, 3, 4, 5, 6, 7);
  };
  auto wf = [&](int m, int ct) { return ((HELD >> m) & 1) ? wr[ct][m] : b[(m - 6) % 3]; };

#define RLO " dst_sel:WORD_0 dst_unused:UNUSED_PRESERVE src0_sel:WORD_0\n\t"
#define RHI " dst_sel:WORD_1 dst_unused:UNUSED_PRESERVE src0_sel:WORD_1\n\t"
  auto fE = [&](int i, const f32x16& G) { SB(); tq[i] = __builtin_amdgcn_exp2f(G[i]); SB(); };
  auto fC0 = [&]() { asm("v_cvt_pk_f16_f32 %0, %1, %2" : "=v"(u0) : "v"(tq[0]), "v"(tq[1])); };
  auto PD = [&](int q) { return u32x4{pd[4 * q], pd[4 * q + 1], pd[4 * q + 2], pd[4 * q + 3]}; };
  auto PN = [&](int q) { return u32x4{pn[4 * q], pn[4 * q + 1], pn[4 * q + 2], pn[4 * q + 3]}; };
  auto ew = [&](int m, const f32x16& Gc, const f32x16& Vc, const f32x16& Gn, const f32x16& Vp) {
    if (m == 0) {
      asm("v_rcp_f16_sdwa %0, %0" RHI "v_cvt_pk_f16_f32 %1, %2, %3" : "+v"(pd[7]), "=v"(pn[7]) : "v"(Vp[14]), "v"(Vp[15]));
      SB();
      asm("v_pk_add_f16 %0, %1, %2" : "=v"(pd[0]) : "v"(u0), "s"(cu));
      fE(3, Gc);
      asm("v_rcp_f16_sdwa %0, %0" RLO : "+v"(pd[0]));
    } else if (m & 1) {
      const int j = (m + 1) / 2;
      if (m == 1) { asm("v_pk_mul_f16 %0, %0, %1" : "+v"(pn[7]) : "v"(pd[7])); SB(); }
      asm("v_cvt_pk_f16_f32 %0, %2, %3\n\tv_rcp_f16_sdwa %1, %1" RHI "v_pk_add_f16 %0, %0, %4"
          : "=v"(pd[j]), "+v"(pd[j - 1]) : "v"(tq[2 * j]), "v"(tq[2 * j + 1]), "s"(cu));
      if (m < 13) fE(m + 3, Gc); else { fE(0, Gn); fE(1, Gn); }
    } else {
      const int j = m / 2 - 1;
      asm("v_cvt_pk_f16_f32 %0, %2, %3\n\tv_rcp_f16_sdwa %1, %1" RLO "v_pk_mul_f16 %0, %0, %4"
          : "=&v"(pn[j]), "+v"(pd[j + 1]) : "v"(Vc[2 * j]), "v"(Vc[2 * j + 1]), "v"(pd[j]));
      if (m < 14) fE(m + 3, Gc);
      else { fE(2, Gn); fC0(); }
    }
  };

  auto drain = [&]() {
    asm("v_rcp_f16_sdwa %0, %0" RHI "v_cvt_pk_f16_f32 %1, %2, %3\n\tv_pk_mul_f16 %1, %1, %0"
        : "+v"(pd[7]), "=v"(pn[7]) : "v"(V1[14]), "v"(V1[15]));
    SB();
    Y[1] = MFMAH(PD(0), S[0], Y[1]);
    Y[3] = MFMAH(PN(0), S[0], Y[3]);
    Y[1] = MFMAH(PD(1), S[1], Y[1]);
    Y[3] = MFMAH(PN(1), S[1], Y[3]);
  };
  while (true) {
    if (__builtin_expect(redo, 0)) {
      asm volatile("s_waitcnt vmcnt(0)" ::: "memory");
      if (slow) cnt = gen();
      issue_gather(0, 0);
      issue_gather(1, 1);
    }
    const int ntiles = (cnt + 31) >> 5;
    {
      const unsigned sl = *(const LAS unsigned short*)(size_t)aLS1;
      bf16x8 w0[15];
#pragma unroll
      for (int m = 6; m < 15; ++m) w0[m] = ((HELD >> m) & 1) ? wr[0][m] : wl[widx(m, 0) * 64];
      u32x4 sx = ld128(aLX + 256);
      unsigned re = *(const LAS unsigned*)(size_t)(aLE + 256);
      St[0] = rd128(sl * TPITCH + cSt);
      St[1] = rd128(sl * TPITCH + cSt + 16);
      G0 = MFMA32(St[0], XAf[0][0], zero16);
      G0 = MFMA32(St[1], XAf[1][0], G0);
      pipe_zero();
#pragma unroll
      for (int m = 6; m < 15; ++m)
        if (!((HELD >> m) & 1)) asm volatile("" : "+v"(w0[m]));
      asm volatile("s_waitcnt vmcnt(5)" : "+v"(sx), "+v"(re), "+v"(G0) :: "memory");
#pragma unroll
      for (int k = 0; k < 4; ++k) as[k] = rd128(fo[k]);
      ae = rd128(foe);
      SB();
#pragma unroll
      for (int k = 0; k < 4; ++k) G0 = MFMA32(as[k], w0[7 + k], G0);
      G0 = MFMA32(ae, w0[6], G0);
      V0 = MFMA32(as[0], w0[11], zero16);
      SB();
      glds16x(x, sx[0], cx0, ring0);
      glds16x(x, sx[1], cx1, ring0 + 1024);
      V0 = MFMA32(as[1], w0[12], V0);
      SB();
      glds16x(x, sx[2], cx0, ring0 + 2048);
      tq[0] = __builtin_amdgcn_exp2f(G0[0]);
      tq[1] = __builtin_amdgcn_exp2f(G0[1]);
      V0 = MFMA32(as[2], w0[13], V0);
      SB();
      glds16x(x, sx[3], cx1, ring0 + 3072);
      tq[2] = __builtin_amdgcn_exp2f(G0[2]);
      V0 = MFMA32(as[3], w0[14], V0);
      SB();
      glds16e(ES, re, ce, ring0 + 4096);
      fC0();
      SB();
    }
    unsigned &lS4 = aLS4, &lS1 = aLS1, &lX = aLX, &lE = aLE;
    int adv = 0;
    auto body = [&](const int par) {
      const unsigned lo = par * 128;
      const unsigned ro = (1 - par) * ringd;
      {
        u32x4 ss;
        unsigned sl;
#pragma unroll
        for (int m = 0; m < 15; ++m) {
          if (m == 0) Y[1] = MFMAH(PD(0), S[0], Y[1]);
          else if (m == 1) Y[3] = MFMAH(PN(0), S[0], Y[3]);
          else if (m == 2) Y[1] = MFMAH(PD(1), S[1], Y[1]);
          else if (m == 3) Y[3] = MFMAH(PN(1), S[1], Y[3]);
          else if (m == 4) G1 = MFMA32(St[0], XAf[0][1], zero16);
          else if (m == 5) G1 = MFMA32(St[1], XAf[1][1], G1);
          else if (m == 6) G1 = MFMA32(ae, wf(m, 1), G1);
          else if (m < 11) G1 = MFMA32(as[m - 7], wf(m, 1), G1);
          else if (m == 11) V1 = MFMA32(as[0], wf(m, 1), zero16);
          else V1 = MFMA32(as[m - 11], wf(m, 1), V1);
          SB();
          if (m == 0) { ss = ld128(lS4 + lo); sl = *(const LAS unsigned short*)(size_t)(lS1 + lo + 128); }
          if (m >= 3 && m < 12 && !((HELD >> (m + 3)) & 1)) b[(m - 3) % 3] = wl[widx(m + 3, 1) * 64];
          if (m == 4) load_S(ss);
          if (m == 5) { St[0] = rd128(sl * TPITCH + cSt); St[1] = rd128(sl * TPITCH + cSt + 16); }
          if (m == 6) { asm volatile("s_waitcnt vmcnt(5)" ::: "memory"); ae = rd128(foe + ro); }
          if (m >= 11) as[m - 11] = rd128(fo[m - 11] + ro);
          ew(m, G0, V0, G1, V1);
          SB();
        }
      }
      {
        u32x4 sx;
        unsigned re;
#pragma unroll
        for (int m = 0; m < 15; ++m) {
          if (m == 0) Y[0] = MFMAH(PD(0), S[0], Y[0]);
          else if (m == 1) Y[2] = MFMAH(PN(0), S[0], Y[2]);
          else if (m == 2) Y[0] = MFMAH(PD(1), S[1], Y[0]);
          else if (m == 3) Y[2] = MFMAH(PN(1), S[1], Y[2]);
          else if (m == 4) G0 = MFMA32(St[0], XAf[0][0], zero16);
          else if (m == 5) G0 = MFMA32(St[1], XAf[1][0], G0);
          else if (m == 6) G0 = MFMA32(ae, wf(m, 0), G0);
          else if (m < 11) G0 = MFMA32(as[m - 7], wf(m, 0), G0);
          else if (m == 11) V0 = MFMA32(as[0], wf(m, 0), zero16);
          else V0 = MFMA32(as[m - 11], wf(m, 0), V0);
          SB();
          if (m == 4) { sx = ld128(lX + lo + 3 * 128); re = *(const LAS unsigned*)(size_t)(lE + lo + 3 * 128); }
          if (m >= 3 && m < 12 && !((HELD >> (m + 3)) & 1)) b[(m - 3) % 3] = wl[widx(m + 3, 0) * 64];
          if (m == 7) glds16x(x, sx[0], cx0, ring0 + ro);
          if (m == 8) glds16x(x, sx[1], cx1, ring0 + ro + 1024);
          if (m == 9) glds16x(x, sx[2], cx0, ring0 + ro + 2048);
          if (m == 10) glds16x(x, sx[3], cx1, ring0 + ro + 3072);
          if (m == 11) glds16e(ES, re, ce, ring0 + ro + 4096);
          ew(m, G1, V1, G0, V0);
          SB();
        }
      }
    };
    for (int t = 0; t < ntiles; t += 2) {
      asm volatile("s_cmp_lt_u32 %0, 4\n\ts_cbranch_scc1 1f\n\ts_setprio 1\n1:" :: "s"(wave) : "scc");
      body(0);
      if (t + 1 >= ntiles) break;
      __builtin_amdgcn_s_setprio(0);
      body(1);
      lS4 += 256; lS1 += 256; lX += 256; lE += 256;
      asm volatile("" : "+v"(lS4), "+v"(lS1), "+v"(lX), "+v"(lE));
      adv += 256;
    }
    if (__builtin_expect(gs >= GEND, 1)) break;
    drain();
    lS4 -= adv; lS1 -= adv; lX -= adv; lE -= adv;
    redo = true;
  }

  {
    int lane;
    asm volatile("v_mbcnt_lo_u32_b32 %0, -1, 0\n\tv_mbcnt_hi_u32_b32 %0, -1, %0" : "=v"(lane));
    const int h = lane >> 5;
    const unsigned tu = lds0 + OFF_W + 18 * 1024 + h * 4096 + ((lane >> 2) & 3) * 128 + (lane & 1) * 64 +
                        ((lane >> 4) & 1) * 16 + ((lane >> 1) & 1) * 8;
    bf16x8 wu[8];
#pragma unroll
    for (int k = 0; k < 8; ++k) wu[k] = rawfrag(tu + (k & 3) * 1024 + (k >> 2) * 32);
    SB();
    drain();
    f32x16 X[2], U[2];
#pragma unroll
    for (int i = 0; i < 16; ++i) {
      const unsigned xv = __builtin_bit_cast(u32x4, as[i >> 3])[(i >> 1) & 3];
      X[0][i] = __uint_as_float(i & 1 ? xv & 0xffff0000u : xv << 16);
    }
    SB();
    asm volatile("s_nop 0\n\tv_mfma_f32_32x32x16_bf16 %0, %1, %2, %3" : "=&v"(U[0]) : "v"(wu[0]), "v"(as[0]), "v"(X[0]));
#pragma unroll
    for (int k = 1; k < 4; ++k) U[0] = MFMA32(wu[k], as[k], U[0]);
#pragma unroll
    for (int i = 0; i < 16; ++i) {
      const unsigned xv = __builtin_bit_cast(u32x4, as[2 + (i >> 3)])[(i >> 1) & 3];
      X[1][i] = __uint_as_float(i & 1 ? xv & 0xffff0000u : xv << 16);
    }
    const unsigned nd = nodeof(cg + (lane & 31));
    const bool live = (nd * Mw + Cw) >> 16 == cg + (lane & 31);
    const unsigned lm = (unsigned)__ballot(live);
    const int nodes = __builtin_popcount(lm);
    const unsigned n0 = __builtin_amdgcn_readlane(nd, __builtin_ctz(lm | 0x80000000u));
    const int r = live ? nd - n0 : 31;
    SB();
    asm volatile("s_nop 0\n\tv_mfma_f32_32x32x16_bf16 %0, %1, %2, %3" : "=&v"(U[1]) : "v"(wu[4]), "v"(as[0]), "v"(X[1]));
#pragma unroll
    for (int k = 1; k < 4; ++k) U[1] = MFMA32(wu[4 + k], as[k], U[1]);
#pragma unroll
    for (int i = 0; i < 16; ++i) Y[0][i] = __builtin_amdgcn_rcpf(Y[0][i] * VSC);
    SB();
#pragma unroll
    for (int ct = 0; ct < 2; ++ct) {
#pragma unroll
      for (int q = 0; q < 2; ++q) {
        u32x4 ov;
#pragma unroll
        for (int j = 0; j < 4; ++j) {
          const int i = 8 * q + 2 * j;
          if (ct == 0) { Y[1][i] = __builtin_amdgcn_rcpf(Y[1][i] * VSC); Y[1][i + 1] = __builtin_amdgcn_rcpf(Y[1][i + 1] * VSC); }
          const f32x2 z = __builtin_elementwise_fma(f32x2{Y[2 + ct][i], Y[2 + ct][i + 1]}, f32x2{Y[ct][i], Y[ct][i + 1]},
                                                    f32x2{U[ct][i], U[ct][i + 1]});
          ov[j] = pack_bf16(maxf(z[0], X[ct][i]), maxf(z[1], X[ct][i + 1]));
        }
        *(LAS u32x4*)(size_t)(la + r * 128 + (((4 * h + 2 * ct + q) ^ (r & 7)) * 16)) = ov;
      }
    }
    u32x4 ov[4];
#pragma unroll
    for (int q = 0; q < 4; ++q)
      ov[q] = *(const LAS u32x4*)(size_t)(la + (8 * q + (lane >> 3)) * 128 + (((lane & 7) ^ (lane >> 3)) * 16));
    asm volatile("v_cmpx_lt_i32 vcc, 0, %6\n\t"
                 "global_store_dwordx4 %0, %1, %5 sc1\n\t"
                 "v_cmpx_lt_i32 vcc, 8, %6\n\t"
                 "global_store_dwordx4 %0, %2, %5 offset:1024 sc1\n\t"
                 "v_cmpx_lt_i32 vcc, 16, %6\n\t"
                 "global_store_dwordx4 %0, %3, %5 offset:2048 sc1\n\t"
                 "v_cmpx_lt_i32 vcc, 24, %6\n\t"
                 "global_store_dwordx4 %0, %4, %5 offset:3072 sc1\n\t"
                 "s_mov_b64 exec, -1"
                 :: "v"((unsigned)n0 * 128u + lane * 16u), "v"(ov[0]), "v"(ov[1]), "v"(ov[2]), "v"(ov[3]), "s"(out),
                    "v"(nodes - (lane >> 3))
                 : "memory", "vcc");
  }
  if (bk == 0 && wave == 7) asm volatile("global_store_dwordx2 %0, %1, %2 sc1" :: "v"(0u), "v"(ep), "s"(FL) : "memory");
  asm volatile("s_waitcnt vmcnt(0)" ::: "memory");
}

extern "C" void kernel_launch(void* const* d_in, const int* in_sizes, int n_in,
                              void* d_out, int out_size, void* d_ws, size_t ws_size,
                              hipStream_t stream) {
  const unsigned short* x = (const unsigned short*)d_in[0];
  const u32x4* eattr = (const u32x4*)d_in[1];
  const int* src = (const int*)d_in[2];
  const int* dst = (const int*)d_in[3];
  const unsigned short* wa = (const unsigned short*)d_in[4];
  const unsigned short* wb = (const unsigned short*)d_in[5];
  const unsigned short* wc = (const unsigned short*)d_in[6];
  const unsigned short* wu = (const unsigned short*)d_in[7];
  const unsigned short* wv = (const unsigned short*)d_in[8];
  unsigned short* out = (unsigned short*)d_out;
  const int N = in_sizes[0] / 64;
  const int E = in_sizes[2];
  const int chunk = (((E + NCH - 1) / NCH) + 3) & ~3;

  const bool skew = N > 4096 && N < 60000;
  const unsigned M = N > 256 ? (unsigned)(4294967295.0 / ((double)N * (skew ? 1.0 + SKEWF : 1.0))) : 0xffffffu;
  const unsigned N1 = (0x7fffffffu / M) + 1u;
  const unsigned M2 = skew ? (0xffffffffu - N1 * M) / ((unsigned)N - N1) : M;

  auto fb = [](double v) { return __builtin_bit_cast(unsigned, (float)v); };
  char* ws = (char*)d_ws;
  size_t off = 0;
  off = 1 << 20;
  unsigned long long* FL = (unsigned long long*)(ws + off);   off += 1 << 20;
  u32x4* ENT = (u32x4*)(ws + off);                       off += (size_t)2 * NBK * IBOX;
  unsigned long long* TS = (unsigned long long*)(ws + off);

  k_main<<<NBK, 512, 0, stream>>>(x, eattr, src, dst, wa, wb, wc, wu, wv, ENT, TS, FL, out, N, E, chunk, M, N1,
                                  M2 - M, fb(65536.0 / M), fb(65536.0 / M2), fb(32768.0 / M + 0.5),
                                  fb((double)N1 + (32768.0 - (double)N1 * M) / M2 + 0.5)
     );
}
```

```cpp
#include <hip/hip_runtime.h>
#include <hip/hip_bf16.h>
#include <stdint.h>

using bf16x8 = __attribute__((ext_vector_type(8))) short;
using s16x4 = __attribute__((ext_vector_type(4))) short;
using f32x16 = __attribute__((ext_vector_type(16))) float;
using f32x2 = __attribute__((ext_vector_type(2))) float;
using u32x4 = __attribute__((ext_vector_type(4))) unsigned;
using u32x2 = __attribute__((ext_vector_type(2))) unsigned;

#define MFMA32(a, b, c) __builtin_amdgcn_mfma_f32_32x32x16_bf16((a), (b), (c), 0, 0, 0)
#define LAS __attribute__((address_space(3)))
#define RANKOF(w) ((w) < 4 ? 7 - (w) : (w) - 4)
#define G1W 4
#define CPF 32
#define CPFB 18688
#define SKEWF 0.015
#define SB() __builtin_amdgcn_sched_barrier(0)

constexpr int NCH = 256;
constexpr int NBK = 256;
constexpr int NFRAG = 39;
constexpr float NLOG2E = -1.44269504088896340736f;
constexpr unsigned TPITCH = 80;

__device__ __forceinline__ unsigned pack_bf16(float lo, float hi) {
  typedef __attribute__((ext_vector_type(2))) float f2;
  typedef __attribute__((ext_vector_type(2))) __bf16 b2;
  f2 v = {lo, hi};
  b2 r = __builtin_convertvector(v, b2);
  return __builtin_bit_cast(unsigned, r);
}

__device__ __forceinline__ void glds16(const void* sbase, unsigned voff, unsigned lds_dst) {
  asm volatile(
      "s_mov_b32 m0, %2\n\t"
      "s_nop 0\n\t"
      "global_load_lds_dwordx4 %0, %1"
      :: "v"(voff), "s"(sbase), "s"(lds_dst) : "memory", "m0");
}
__device__ __forceinline__ void glds16x(const void* sbase, unsigned a, unsigned c, unsigned lds_dst) {
  unsigned tmp;
  asm volatile(
      "s_mov_b32 m0, %4\n\t"
      "v_mad_u32_u16 %0, %1, %5, %2\n\t"
      "global_load_lds_dwordx4 %0, %3"
      : "=&v"(tmp) : "v"(a), "v"(c), "s"(sbase), "s"(lds_dst), "s"(128u) : "memory", "m0");
}
__device__ __forceinline__ void glds16e(const void* sbase, unsigned a, unsigned c, unsigned lds_dst) {
  unsigned tmp;
  asm volatile(
      "s_mov_b32 m0, %4\n\t"
      "v_add_u32 %0, %1, %2\n\t"
      "global_load_lds_dwordx4 %0, %3"
      : "=&v"(tmp) : "v"(a), "v"(c), "s"(sbase), "s"(lds_dst) : "memory", "m0");
}
__device__ __forceinline__ void gstore128wt(void* sbase, unsigned voff, u32x4 v) {
  asm volatile("global_store_dwordx4 %0, %1, %2 sc1\n\ts_nop 1" :: "v"(voff), "v"(v), "s"(sbase) : "memory");
}

constexpr int IBOX = 32768;
constexpr int IEXT = 256 * IBOX;
constexpr int CPIT = 208;
constexpr unsigned RINC = 87382;
constexpr int KP_HDR = 0;
constexpr int KP_ENT = KP_HDR + NBK * 4;
constexpr int KP_POS = KP_ENT + NBK * CPIT;
constexpr int KP_LDS = KP_POS + NBK * 4;
constexpr int NWL = 26;
constexpr int NBLOB = 31;

__device__ __forceinline__ unsigned mul24s(unsigned a, unsigned b) {
  unsigned r;
  asm("v_mul_u32_u24 %0, %1, %2" : "=v"(r) : "s"(b), "v"(a));
  return r;
}
__device__ __forceinline__ unsigned byte3x(unsigned a, unsigned b) {
  unsigned r;
  asm("v_mul_u32_u24_sdwa %0, %1, %2 dst_sel:DWORD dst_unused:UNUSED_PAD src0_sel:DWORD src1_sel:BYTE_3" : "=v"(r) : "s"(b), "v"(a));
  return r;
}
constexpr int RING_SLOT = 5 * 1024;
constexpr int LCAP = 512;
constexpr int LARR = 2560;
constexpr int LIST_BYTES = 2 * LARR;
constexpr int OFF_CNT = 0;
constexpr int SUBL = LARR / 4;
constexpr int OFF_XF = 256;
constexpr int OFF_W = 512;
constexpr int OFF_T1 = OFF_W + NWL * 1024;
constexpr int OFF_T2 = OFF_T1 + 32 * TPITCH;
constexpr int OFF_R0 = OFF_W + NFRAG * 1024;
constexpr int OFF_R1 = OFF_R0 + 8 * RING_SLOT;
constexpr int OFF_L = OFF_R1 + 8 * RING_SLOT;
constexpr int MAIN_LDS = OFF_L + 8 * LIST_BYTES;
static_assert(MAIN_LDS <= 160 * 1024, "LDS");
static_assert(KP_LDS <= 16 * RING_SLOT, "phase 1 lives in the ring");

__device__ __forceinline__ unsigned ldd(const unsigned* p) {
  return __hip_atomic_load(p, __ATOMIC_RELAXED, __HIP_MEMORY_SCOPE_AGENT);
}
__device__ __forceinline__ unsigned long long ldq(const unsigned long long* p) {
  return __hip_atomic_load(p, __ATOMIC_RELAXED, __HIP_MEMORY_SCOPE_AGENT);
}
__device__ __forceinline__ bf16x8 rawfrag(unsigned a) {
  const s16x4 a0 = __builtin_amdgcn_ds_read_tr16_b64_v4i16((LAS s16x4*)(size_t)a);
  const s16x4 a1 = __builtin_amdgcn_ds_read_tr16_b64_v4i16((LAS s16x4*)(size_t)(a + 512));
  return __builtin_shufflevector(a0, a1, 0, 1, 2, 3, 4, 5, 6, 7);
}
__device__ __forceinline__ float maxf(float a, float b) {
  float r;
  asm("v_max_f32 %0, %1, %2" : "=v"(r) : "v"(a), "v"(b));
  return r;
}
__device__ __forceinline__ float add1(float t) { return 1.0f + t; }
__device__ __forceinline__ float mulf(int i, float a, float b) { return (i & 1) ? __builtin_fmaf(a, b, 0.0f) : a * b; }

__global__ __launch_bounds__(512, 2) void k_main(const unsigned short* __restrict__ x,
                                                 const u32x4* __restrict__ ES,
                                                 const int* __restrict__ src, const int* __restrict__ dst,
                                                 const unsigned short* __restrict__ wa_g,
                                                 const unsigned short* __restrict__ wb_g,
                                                 const unsigned short* __restrict__ wc_g,
                                                 const unsigned short* __restrict__ wu_g,
                                                 const unsigned short* __restrict__ wv_g,
                                                 u32x4* ENT, unsigned long long* TS, unsigned long long* FL,
                                                 unsigned short* __restrict__ out, int N, int E, int chunk,
                                                 unsigned M, unsigned N1, unsigned DM, unsigned MF0, unsigned MF1,
                                                 unsigned MB0, unsigned MB1
                                                 ) {
  __shared__ __attribute__((aligned(16))) char smem[MAIN_LDS];
  const int tid0 = threadIdx.x;
  const int wave = __builtin_amdgcn_readfirstlane(tid0 >> 6);
  const int bk = blockIdx.x;
  const unsigned lds0 = (unsigned)(size_t)(LAS char*)smem;

  LAS char* sm = (LAS char*)smem + OFF_R0;
  const unsigned sm0 = lds0 + OFF_R0;
  const int tid = tid0, lane = tid & 63, r = lane & 31, h = lane >> 5;
  const unsigned pc = tid & 7, bq = tid >> 3;
  unsigned ia = sm0 + KP_ENT + bq * CPIT + pc * 16;
#pragma unroll
  for (int s = 0; s < 4; ++s) *(LAS u32x4*)(size_t)(ia + s * 64 * CPIT) = u32x4{~0u, ~0u, ~0u, ~0u};
  *(LAS unsigned*)(sm + KP_HDR + (tid & 255) * 4) = 0u;
  int q0 = 4 * tid + 3, q1 = 4 * tid + 1795;
  unsigned a0 = tid * 16, a1 = tid * 16 + 7168, z0 = 0u;
  unsigned long long m7 = __ballot(tid == 448);
  int n7 = wave == 7 ? 0 : 0x7fffffff;
  unsigned f0 = lane * 128u, f1 = lane * 128u + 8192u, f2 = min(lane * 128u + 16384u, CPFB - 512u);
  unsigned long long p7 = __ballot((wave | (bk & -CPF)) == 7);
  asm volatile("" : "+v"(f0), "+v"(f1), "+v"(f2), "+s"(p7));
  asm volatile("" : "+v"(ia), "+v"(q0), "+v"(q1), "+v"(a0), "+v"(a1), "+v"(z0), "+s"(m7), "+s"(n7) :: "memory");
  SB();
  asm volatile("" :: "s"(x), "s"(ES), "s"(src), "s"(dst), "s"(wa_g), "s"(wb_g), "s"(wc_g), "s"(wu_g), "s"(wv_g), "s"(ENT),
               "s"(TS), "s"(FL), "s"(out), "s"(N), "s"(E), "s"(chunk), "s"(M), "s"(N1), "s"(DM), "s"(MF0), "s"(MF1), "s"(MB0), "s"(MB1));
  const int base = bk * chunk;
  const int ecnt = max(min(chunk, E - base), 0);
  unsigned long long on[2];
  on[0] = __ballot(q0 < min(min(chunk, E - base), n7));
  on[1] = __ballot(q1 < min(min(chunk, E - base), n7));
  a0 += base * 4u;
  a1 += base * 4u;
  u32x4 d[2], sv[2];
  u32x2 e0;
  asm volatile("s_mov_b64 exec, %11\n\t"
               "global_load_dwordx4 %0, %5, %8 nt\n\t"
               "s_mov_b64 exec, %12\n\t"
               "global_load_dwordx4 %1, %6, %8 nt\n\t"
               "s_mov_b64 exec, %13\n\t"
               "global_load_dwordx2 %2, %7, %9 sc1\n\t"
               "s_getpc_b64 s[96:97]\n\t"
               "s_mov_b32 m0, %17\n\t"
               "s_mov_b64 exec, %18\n\t"
               "global_load_lds_dword %14, s[96:97]\n\t"
               "global_load_lds_dword %15, s[96:97]\n\t"
               "global_load_lds_dword %16, s[96:97]\n\t"
               "s_mov_b64 exec, %11\n\t"
               "s_waitcnt lgkmcnt(0)\n\t"
               "s_barrier\n\t"
               "global_load_dwordx4 %3, %5, %10 nt\n\t"
               "s_mov_b64 exec, %12\n\t"
               "global_load_dwordx4 %4, %6, %10 nt\n\t"
               "s_mov_b64 exec, -1"
               : "=&{v[240:243]}"(d[0]), "=&{v[244:247]}"(d[1]), "=&{v[238:239]}"(e0), "=&{v[248:251]}"(sv[0]),
                 "=&{v[252:255]}"(sv[1])
               : "v"(a0), "v"(a1), "v"(z0), "s"(dst), "s"(FL), "s"(src), "s"(on[0]), "s"(on[1]), "s"(m7)
                 , "v"(f0), "v"(f1), "v"(f2), "n"(OFF_L + 7 * LIST_BYTES + 4096), "s"(p7)
               : "memory", "m0", "s96", "s97");
  SB();
  const int bu = (bk >> 1) | ((bk & 1) << 7);
  const unsigned Mw = bk & 1 ? M + DM : M, Cw = bk & 1 ? 0u - N1 * DM : 0u;
  unsigned MF = bk & 1 ? MF1 : MF0, MB = bk & 1 ? MB1 : MB0;
  asm volatile("" : "+s"(MF), "+s"(MB));
  char* ib = reinterpret_cast<char*>(ENT) + (size_t)bu * IBOX;
  auto nodeof = [&](unsigned c) { return min((unsigned)__builtin_fmaf((float)c, __uint_as_float(MF), __uint_as_float(MB)), (unsigned)N - 1u); };
  unsigned tup[8], val[8], pm[8];
  u32x4 xg[4];
  const unsigned xn0 = __builtin_amdgcn_readfirstlane(nodeof((bu * 8 + wave) * 32));
  unsigned xd[4];
  auto xprep = [&]() {
#pragma unroll
    for (int i = 0; i < 4; ++i)
      xd[i] = min(xn0 + 8 * i + (lane >> 3), N - 1u) * 128u + ((((lane & 7) ^ (lane >> 4)) * 16) ^ (i & 1) * 64) - i * 1024u;
    asm volatile("" : "+v"(xd[0]), "+v"(xd[1]), "+v"(xd[2]), "+v"(xd[3]));
  };
  auto xdma = [&]() {
    asm volatile("s_mov_b32 m0, %5\n\ts_nop 0\n\t"
                 "global_load_lds_dwordx4 %0, %4\n\t"
                 "global_load_lds_dwordx4 %1, %4 offset:1024\n\t"
                 "global_load_lds_dwordx4 %2, %4 offset:2048\n\t"
                 "global_load_lds_dwordx4 %3, %4 offset:3072"
                 :: "v"(xd[0]), "v"(xd[1]), "v"(xd[2]), "v"(xd[3]), "s"(x), "s"(lds0 + OFF_L + wave * LIST_BYTES)
                 : "memory", "m0");
  };
  auto wload = [&](const unsigned short* w, int f) {
    asm volatile("s_mov_b32 m0, %3\n\ts_nop 0\n\t"
                 "global_load_lds_dwordx4 %0, %2\n\t"
                 "global_load_lds_dwordx4 %0, %2 offset:1024\n\t"
                 "global_load_lds_dwordx4 %0, %2 offset:2048\n\t"
                 "global_load_lds_dwordx4 %0, %2 offset:3072\n\t"
                 "s_addk_i32 m0, 4096\n\ts_nop 0\n\t"
                 "global_load_lds_dwordx4 %1, %2\n\t"
                 "global_load_lds_dwordx4 %1, %2 offset:1024\n\t"
                 "global_load_lds_dwordx4 %1, %2 offset:2048\n\t"
                 "global_load_lds_dwordx4 %1, %2 offset:3072"
                 :: "v"(lane * 16u), "v"(lane * 16u + 4096u), "s"(w), "s"(lds0 + OFF_W + f * 1024) : "memory", "m0", "scc");
  };
  auto wall = [&]() {
    wload(wa_g, NBLOB);
    wload(wb_g, 0);
    wload(wv_g, 8);
    glds16(wc_g, lane * 16, lds0 + OFF_W + 16 * 1024);
    glds16(wc_g + 512, lane * 16, lds0 + OFF_W + 17 * 1024);
  };
  int spin = 1 << 20;
  u32x2 ep = {0u, 0u};
  if (wave == 7) {
    wall();
    *(LAS u32x4*)((LAS char*)smem + OFF_CNT + r * 16) =
        (r & 17) == 0 ? u32x4{lds0 + OFF_L, lds0 + OFF_L + SUBL, lds0 + OFF_L + 2 * SUBL, lds0 + OFF_L + 3 * SUBL}
                      : u32x4{0u, 0u, 0u, 0u};
    {
      const unsigned col = h ? ((r >> 1) & 12u) | ((r << 2) & 16u) | (r & 3u) : r;
      LAS char* tr = (LAS char*)smem + OFF_T1 + lane * TPITCH;
#pragma unroll
      for (int j = 0; j < 4; ++j) *(LAS u32x4*)(tr + j * 16) = u32x4{0u, 0u, 0u, 0u};
      *(LAS unsigned short*)(tr + col * 2) = 0x3F80;
    }
#pragma unroll
    for (int i = 0; i < 8; ++i) { tup[i] = 0u; val[i] = 0u; pm[i] = 0u; }
    asm volatile("s_waitcnt vmcnt(%1)" : "+{v[238:239]}"(e0) : "n"(31) : "memory");
    ep[0] = __builtin_amdgcn_readfirstlane(e0[0]) + 0x9E3779B1u;
    ep[1] = __builtin_amdgcn_readfirstlane(e0[1]) + 0x7F4A7C15u;
    *(LAS u32x2*)((LAS char*)smem + OFF_CNT + 56) = u32x2{ep[0], ep[1] >> 24};
    if (__builtin_expect(ecnt & 3, 0)) {
      const int i = (ecnt & ~3) + lane;
      on[0] = __ballot(i < ecnt);
      if (i < ecnt) {
        pm[0] = (unsigned)dst[base + i] * M + (unsigned)max(dst[base + i] - (int)N1, 0) * DM;
        tup[0] = (unsigned)src[base + i] | (unsigned)i << 16;
        val[0] = __hip_atomic_fetch_add((LAS unsigned*)(sm + KP_HDR) + (pm[0] >> 24), RINC, __ATOMIC_RELAXED, __HIP_MEMORY_SCOPE_WORKGROUP);
        const unsigned u = min(val[0] >> 16, 48u);
        *(LAS unsigned*)(sm + KP_ENT + (pm[0] >> 24) * CPIT + u * 4) = tup[0];
        *(LAS unsigned char*)(sm + KP_ENT + (pm[0] >> 24) * CPIT + ((u * 4) | (u & 3) | 12)) = pm[0] >> 16;
      }
      __builtin_amdgcn_s_waitcnt(0x0F70);
    }
    xprep();
    xdma();
  } else {
    xprep();
    unsigned inc = RINC, t0 = (unsigned)tid << 18;
    asm volatile("s_waitcnt vmcnt(2)" : "+{v[240:243]}"(d[0]), "+{v[244:247]}"(d[1]), "+v"(inc), "+v"(t0) :: "memory");
    unsigned cb[8];
#pragma unroll
    for (int s = 0; s < 2; ++s) {
      unsigned* rr = val + 4 * s;
#pragma unroll
      for (int k = 0; k < 4; ++k) {
        unsigned t;
        asm("v_sub_u32 %0, %1, %2 clamp" : "=v"(t) : "v"(d[s][k]), "s"(N1));
        asm("v_mad_u32_u24 %0, %1, %2, %3" : "=v"(pm[4 * s + k]) : "v"(t), "s"(DM), "v"(mul24s(d[s][k], M)));
      }
#pragma unroll
      for (int k = 0; k < 4; ++k) rr[k] = byte3x(pm[4 * s + k], 4u);
      asm volatile(
          "s_mov_b64 exec, %5\n\t"
          "ds_add_rtn_u32 %0, %0, %4 offset:%6\n\t"
          "ds_add_rtn_u32 %1, %1, %4 offset:%6\n\t"
          "ds_add_rtn_u32 %2, %2, %4 offset:%6\n\t"
          "ds_add_rtn_u32 %3, %3, %4 offset:%6\n\t"
          "s_mov_b64 exec, -1"
          : "+v"(rr[0]), "+v"(rr[1]), "+v"(rr[2]), "+v"(rr[3])
          : "v"(inc), "s"(on[s]), "n"(OFF_R0 + KP_HDR)
          : "memory");
    }
#pragma unroll
    for (int i = 0; i < 8; ++i) cb[i] = byte3x(pm[i], (unsigned)CPIT);
    asm volatile("s_waitcnt vmcnt(0)"
                 : "+{v[248:251]}"(sv[0]), "+{v[252:255]}"(sv[1]), "+v"(cb[0]), "+v"(cb[1]), "+v"(cb[2]), "+v"(cb[3]), "+v"(cb[4]), "+v"(cb[5]),
                   "+v"(cb[6]), "+v"(cb[7])
                 :: "memory");
    xdma();
#pragma unroll
    for (int i = 0; i < 8; ++i) tup[i] = sv[i >> 2][i & 3] + t0 + (((i >> 2) * 1792u + (i & 3)) << 16);
#pragma unroll
    for (int s = 0; s < 2; ++s) {
      asm volatile("s_waitcnt lgkmcnt(%8)"
                   : "+v"(val[4 * s]), "+v"(val[4 * s + 1]), "+v"(val[4 * s + 2]), "+v"(val[4 * s + 3]), "+v"(tup[4 * s]),
                     "+v"(tup[4 * s + 1]), "+v"(tup[4 * s + 2]), "+v"(tup[4 * s + 3])
                   : "n"(s ? 8 : 4) : "memory");
      unsigned pa[4], pb[4];
#pragma unroll
      for (int k = 0; k < 4; ++k) {
        const unsigned u = min(val[4 * s + k], 48u << 16);
        asm("v_mad_u32_u16 %0, %1, 4, %2 op_sel:[1,0,0,0]" : "=v"(pa[k]) : "v"(u), "v"(cb[4 * s + k]));
        pb[k] = pa[k] | ((u >> 16) & 3u) | 12u;
      }
      asm volatile(
          "s_mov_b64 exec, %16\n\t"
          "ds_write_b32 %0, %8 offset:%17\n\t"
          "ds_write_b8_d16_hi %4, %12 offset:%17\n\t"
          "ds_write_b32 %1, %9 offset:%17\n\t"
          "ds_write_b8_d16_hi %5, %13 offset:%17\n\t"
          "ds_write_b32 %2, %10 offset:%17\n\t"
          "ds_write_b8_d16_hi %6, %14 offset:%17\n\t"
          "ds_write_b32 %3, %11 offset:%17\n\t"
          "ds_write_b8_d16_hi %7, %15 offset:%17\n\t"
          "s_mov_b64 exec, -1"
          :: "v"(pa[0]), "v"(pa[1]), "v"(pa[2]), "v"(pa[3]), "v"(pb[0]), "v"(pb[1]), "v"(pb[2]), "v"(pb[3]),
             "v"(tup[4 * s]), "v"(tup[4 * s + 1]), "v"(tup[4 * s + 2]), "v"(tup[4 * s + 3]), "v"(pm[4 * s]),
             "v"(pm[4 * s + 1]), "v"(pm[4 * s + 2]), "v"(pm[4 * s + 3]), "s"(on[s]), "n"(OFF_R0 + KP_ENT)
          : "memory");
    }
  }
  u32x2 tag;
  {
    unsigned ha = sm0 + KP_HDR + bq * 4, hb = sm0 + KP_HDR + lane * 16;
    unsigned go = bq * IBOX + bk * 128 + pc * 16;
    asm volatile("s_waitcnt lgkmcnt(0)\n\ts_barrier" : "+v"(ha), "+v"(hb), "+v"(go) :: "memory");
    tag = *(const LAS u32x2*)((LAS char*)smem + OFF_CNT + 56);
    const u32x4 call = *(const LAS u32x4*)(size_t)hb;
    unsigned cn[4];
    u32x4 v[4];
#pragma unroll
    for (int s = 0; s < 4; ++s) {
      cn[s] = *(const LAS unsigned*)(size_t)(ha + s * 256);
      v[s] = *(const LAS u32x4*)(size_t)(ia + s * 64 * CPIT);
    }
    asm volatile("" : "+v"(v[0]), "+v"(v[1]), "+v"(v[2]), "+v"(v[3]), "+v"(tag));
    if (__builtin_expect(__ballot(max(max(call[0], call[1]), max(call[2], call[3])) > 36u * RINC) != 0ull, 0)) {
      LAS unsigned* ovc = (LAS unsigned*)((LAS char*)smem + OFF_CNT + 24);
      LAS unsigned* pos = (LAS unsigned*)(sm + KP_POS);
      if (tid < NBK) {
        const unsigned u = *(const LAS unsigned*)(sm + KP_HDR + tid * 4) >> 16, n = u - (u >> 2);
        if (n > 36u) {
          const unsigned p = (unsigned)base + __hip_atomic_fetch_add(ovc, n - 36u, __ATOMIC_RELAXED, __HIP_MEMORY_SCOPE_WORKGROUP);
          pos[tid] = p;
          __hip_atomic_store(FL + 512 + tid * NCH + bk, ((unsigned long long)(n - 36u) << 32) | p, __ATOMIC_RELAXED,
                             __HIP_MEMORY_SCOPE_AGENT);
        }
      }
      asm volatile("s_waitcnt lgkmcnt(0)\n\ts_barrier" ::: "memory");
#pragma unroll
      for (int i = 0; i < 8; ++i) {
        const unsigned u = val[i] >> 16, n = u - (u >> 2);
        if (((on[i >> 2] >> lane) & 1ull) && n >= 36u)
          __hip_atomic_store(TS + pos[pm[i] >> 24] + (n - 36u), ((unsigned long long)((pm[i] >> 16) & 0xffu) << 32) | tup[i],
                             __ATOMIC_RELAXED, __HIP_MEMORY_SCOPE_AGENT);
      }
      __builtin_amdgcn_s_waitcnt(0x0F70);
      asm volatile("s_waitcnt vmcnt(0)\n\ts_barrier" ::: "memory");
    }
    auto seal = [&](u32x4& w, bool more) {
      w[3] = __builtin_amdgcn_perm(tag[1], w[3], 0x04020100u);
      unsigned sm_ = w[0] + w[1] + w[2] + w[3] + tag[0];
      sm_ += __builtin_amdgcn_update_dpp(0u, sm_, 0xB1, 0xf, 0xf, false);
      sm_ += __builtin_amdgcn_update_dpp(0u, sm_, 0x4E, 0xf, 0xf, false);
      sm_ += __builtin_amdgcn_update_dpp(0u, sm_, 0x141, 0xf, 0xf, false);
      w[2] = pc == 7u ? w[2] - sm_ + (more ? 1u : 0u) : w[2];
    };
#pragma unroll
    for (int s = 0; s < 4; ++s) seal(v[s], cn[s] > 23u * RINC);
    asm volatile("global_store_dwordx4 %0, %1, %5 sc1\n\t"
                 "global_store_dwordx4 %0, %2, %6 sc1\n\t"
                 "global_store_dwordx4 %0, %3, %7 sc1\n\t"
                 "global_store_dwordx4 %0, %4, %8 sc1\n\t"
                 "s_nop 1"
                 :: "v"(go), "v"(v[0]), "v"(v[1]), "v"(v[2]), "v"(v[3]), "s"(ENT), "s"(ENT + 4 * IBOX), "s"(ENT + 8 * IBOX),
                    "s"(ENT + 12 * IBOX)
                 : "memory");
    if (__builtin_expect(__ballot(max(max(cn[0], cn[1]), max(cn[2], cn[3])) > 23u * RINC) != 0ull, 0)) {
#pragma unroll 1
      for (int s = 0; s < 4; ++s) {
        const unsigned c = *(const LAS unsigned*)(size_t)(ha + s * 256);
        u32x4 w = *(const LAS u32x4*)(size_t)(ia + s * 64 * CPIT + (pc < 4u ? 128 : 0));
        if (pc == 7u) w = u32x4{w[2], ~0u, ~0u, w[3] >> 16};
        else {
#pragma unroll
          for (int j = 0; j < 3; ++j) w[j] = pc < 4u && 32u + 4u * pc + j < (c >> 16) ? w[j] : ~0u;
        }
        seal(w, c > 36u * RINC);
        if (c > 23u * RINC) gstore128wt(ENT, go + s * (64 * IBOX) + IEXT, w);
      }
    }
  }
  u32x4 cv[4];
  unsigned vo[4], eb[4];
  unsigned long long pend[4];
#pragma unroll
  for (int k = 0; k < 4; ++k) {
    vo[k] = wave * 4096u + k * 1024u + lane * 16u;
    eb[k] = (unsigned)((wave * 32 + k * 8 + (lane >> 3)) * chunk) * 32u;
  }
#define POLL0()                                                                                                       \
  asm volatile("global_load_dwordx4 %0, %4, %8 sc1\n\t"                                                               \
               "global_load_dwordx4 %1, %5, %8 sc1\n\t"                                                               \
               "global_load_dwordx4 %2, %6, %8 sc1\n\t"                                                               \
               "global_load_dwordx4 %3, %7, %8 sc1"                                                                   \
               : "=&{v[240:243]}"(cv[0]), "=&{v[244:247]}"(cv[1]), "=&{v[248:251]}"(cv[2]), "=&{v[252:255]}"(cv[3])   \
               : "v"(vo[0]), "v"(vo[1]), "v"(vo[2]), "v"(vo[3]), "s"(ib)                                              \
               : "memory")
  const LAS bf16x8* wl = (const LAS bf16x8*)(smem + OFF_W) + lane;
  const f32x16 zero16 = {0.f, 0.f, 0.f, 0.f, 0.f, 0.f, 0.f, 0.f, 0.f, 0.f, 0.f, 0.f, 0.f, 0.f, 0.f, 0.f};
  auto widx = [&](int m, int ct) { return m == 6 ? 16 + ct : m < 11 ? m - 7 + 4 * ct : 8 + m - 11 + 4 * ct; };
#define HELD ((1 << 6) | (1 << 8) | (1 << 10) | (1 << 12) | (1 << 14))
  bf16x8 wr[2][15];
  const unsigned to = lds0 + OFF_W + ((lane >> 2) & 3) * 128 + (lane & 1) * 64 + ((lane >> 4) & 1) * 16 + ((lane >> 1) & 1) * 8;
  auto xa = [&]() {
    bf16x8 wa[8];
#pragma unroll
    for (int k = 0; k < 8; ++k) wa[k] = rawfrag(to + (NBLOB + (k >> 1)) * 1024 + h * 4096 + (k & 1) * 32);
    asm volatile("" : "+v"(wa[0]), "+v"(wa[1]), "+v"(wa[2]), "+v"(wa[3]), "+v"(wa[4]), "+v"(wa[5]), "+v"(wa[6]), "+v"(wa[7]),
                      "+{v[160:163]}"(xg[0]), "+{v[164:167]}"(xg[1]), "+{v[168:171]}"(xg[2]), "+{v[172:175]}"(xg[3]));
    f32x16 XA[2] = {zero16, zero16};
#pragma unroll
    for (int k = 0; k < 4; ++k)
#pragma unroll
      for (int ct = 0; ct < 2; ++ct) XA[ct] = MFMA32(__builtin_bit_cast(bf16x8, xg[k]), wa[k * 2 + ct], XA[ct]);
#pragma unroll
    for (int q = 0; q < 4; ++q) {
      u32x4 xp;
#pragma unroll
      for (int j = 0; j < 4; ++j)
        xp[j] = pack_bf16(XA[q & 1][8 * (q >> 1) + 2 * j] * NLOG2E, XA[q & 1][8 * (q >> 1) + 2 * j + 1] * NLOG2E);
      *(LAS u32x4*)((LAS char*)smem + OFF_R1 + wave * RING_SLOT + q * 1024 + lane * 16) = xp;
    }
  };
  {
    auto prep = [&](unsigned f, unsigned hs, float scale) {
      u32x4 w[2];
#pragma unroll
      for (int ct = 0; ct < 2; ++ct) w[ct] = __builtin_bit_cast(u32x4, rawfrag(to + f + h * hs + ct * 32));
      asm volatile("s_waitcnt lgkmcnt(0)" : "+v"(w[0]), "+v"(w[1]) :: "memory");
#pragma unroll
      for (int ct = 0; ct < 2; ++ct) {
#pragma unroll
        for (int j = 0; j < 4; ++j)
          w[ct][j] = pack_bf16(__uint_as_float(w[ct][j] << 16) * scale, __uint_as_float(w[ct][j] & 0xffff0000u) * scale);
        *(LAS u32x4*)((LAS char*)smem + OFF_W + f + ct * hs + lane * 16) = w[ct];
      }
    };
    {
      const unsigned xr = min(nodeof((bu * 8 + wave) * 32 + r) - xn0, 31u);
      unsigned xa_ = lds0 + OFF_L + wave * LIST_BYTES + xr * 128 + (((4 * h) ^ ((xr >> 1) & 7)) * 16);
      asm volatile("s_waitcnt vmcnt(4)" : "+v"(xa_) :: "memory");
#pragma unroll
      for (int t = 0; t < 4; ++t) xg[t] = *(const LAS u32x4*)(size_t)(xa_ ^ (t * 16));
      asm volatile("" : "+v"(xg[0]), "+v"(xg[1]), "+v"(xg[2]), "+v"(xg[3]) :: "memory");
      *(LAS unsigned*)((LAS char*)smem + OFF_L + wave * LIST_BYTES + h * LARR + r * 4) = ~0u;
    }
    asm volatile("s_waitcnt vmcnt(4) lgkmcnt(0)\n\ts_barrier"
                 : "+v"(vo[0]), "+v"(vo[1]), "+v"(vo[2]), "+v"(vo[3]), "+v"(eb[0]), "+v"(eb[1]), "+v"(eb[2]), "+v"(eb[3])
                 :: "memory");
    xa();
    {
      unsigned f = (wave + (wave & 4)) * 1024, hs = 4096;
      const float scale = wave < 4 ? NLOG2E : 1.0f;
#pragma unroll 1
      while (true) {
        asm volatile("" : "+s"(f), "+s"(hs));
        prep(f, hs, scale);
        if (f != 0u) break;
        f = 16 * 1024;
        hs = 1024;
      }
    }
    POLL0();
    glds16(wu_g + wave * 512, lane * 16, lds0 + OFF_W + (18 + wave) * 1024);
  }
  const unsigned l0 = lds0 + OFF_L;
  unsigned aLX = l0 + (lane >> 3) * 16;
  unsigned aLE = l0 + LARR + ((((lane >> 1) & 7) << 2) | (lane >> 4)) * 4;
  unsigned aLS4 = l0 + (4 * h + ((lane >> 2) & 3)) * 16;
  unsigned aLS1 = l0 + (((r & 7) << 2) | (r >> 3)) * 4 + 2;
  unsigned cx0 = (unsigned)(((lane & 7) ^ (lane >> 4)) * 16);
  unsigned cx1 = cx0 ^ 64u;
  unsigned ce = (unsigned)((((lane & 1) ^ ((lane >> 4) & 1))) * 16);
  const unsigned ringd = 8 * RING_SLOT;
  unsigned cS = lds0 + OFF_T1 + (((lane >> 4) & 1) * 16 + (lane & 3) * 4) * 2;
  unsigned cSt = lds0 + OFF_T2 + h * 32;
  unsigned fo[4];
#pragma unroll
  for (int t = 0; t < 4; ++t) fo[t] = lds0 + OFF_R0 + (unsigned)(r * 128 + (((4 * h + t) ^ ((r >> 1) & 7)) * 16));
  unsigned foe = lds0 + OFF_R0 + (unsigned)(4096 + r * 32 + ((h ^ ((r >> 3) & 1)) * 16));
  f32x16 Y[4];
#pragma unroll
  for (int q = 0; q < 4; ++q)
#pragma unroll
    for (int i = 0; i < 16; i += 2) {
      f32x2 y;
      if (q < 2) asm volatile("v_mov_b64 %0, %1" : "=v"(y) : "s"(0x358637bd358637bdull));
      else asm volatile("v_mov_b64 %0, 0" : "=v"(y));
      Y[q][i] = y[0];
      Y[q][i + 1] = y[1];
    }
  f32x16 G0, V0, G1, V1;
  bf16x8 S[2], St[2], as[4], ae, b[3];
  u32x4 pd[2], pn[2];
  float tq[16], rq[16], pq[16];
  auto pipe_zero = [&]() {
#pragma unroll
    for (int j = 0; j < 8; ++j) { S[0][j] = 0; S[1][j] = 0; }
#pragma unroll
    for (int j = 0; j < 4; ++j) { pd[0][j] = 0; pd[1][j] = 0; pn[0][j] = 0; pn[1][j] = 0; }
#pragma unroll
    for (int i = 0; i < 16; ++i) V1[i] = 0.f;
    rq[14] = 0.f; rq[15] = 0.f;
  };
  unsigned rid = lane & 7;
  asm volatile("" : "+v"(aLX), "+v"(aLE), "+v"(aLS4), "+v"(aLS1), "+v"(cx0), "+v"(cx1), "+v"(ce), "+v"(cS), "+v"(cSt),
                    "+v"(fo[0]), "+v"(fo[1]), "+v"(fo[2]), "+v"(fo[3]), "+v"(foe), "+v"(rid));
  asm volatile("" : "+v"(Y[0]), "+v"(Y[1]), "+v"(Y[2]), "+v"(Y[3]));
  auto ld128 = [&](unsigned a) { return *(const LAS u32x4*)(size_t)a; };
  auto gather5 = [&](const u32x4 sx, unsigned re, unsigned dstb) {
    glds16x(x, sx[0], cx0, dstb);
    glds16x(x, sx[1], cx1, dstb + 1024);
    glds16x(x, sx[2], cx0, dstb + 2048);
    glds16x(x, sx[3], cx1, dstb + 3072);
    glds16e(ES, re, ce, dstb + 4096);
  };
  const unsigned sq = min(4u * M, 0xffffffu);
  unsigned early, just;
  asm volatile(
        "s_mov_b32 s96, 0x800000\n\t"
        "v_mov_b32 v181, %18\n\t"
        "v_mov_b32 v180, 160\n\t"
        "v_mov_b32 v182, 0xe0\n\t"
        "v_mov_b32 v177, 0x0c040100\n\t"
        "v_mov_b32 v178, 0x0c050100\n\t"
        "v_mov_b32 v179, 0x0c060100\n\t"
        "s_mov_b32 s52, 0x7f7f7f7f\n\t"
        "s_mov_b32 s53, 0x7f7f7f7f\n\t"
        "s_mov_b64 s[54:55], 0\n\t"
        "s_mov_b32 %12, 0\n\t"
        "v_mov_b32 v191, 4\n\t"
        "s_waitcnt vmcnt(%30)\n\t"
        "v_add3_u32 v228, v240, v241, v242\n\t"
        "v_add3_u32 v231, v244, v245, v246\n\t"
        "v_add3_u32 v228, v228, v243, %19\n\t"
        "v_add3_u32 v231, v231, v247, %19\n\t"
        "v_cmp_ne_u32_sdwa s[72:73], v243, %20 src0_sel:BYTE_3 src1_sel:DWORD\n\t"
        "v_add_u32_dpp v228, v228, v228 quad_perm:[1,0,3,2] row_mask:0xf bank_mask:0xf\n\t"
        "v_add_u32_dpp v231, v231, v231 quad_perm:[1,0,3,2] row_mask:0xf bank_mask:0xf\n\t"
        "v_cmp_ne_u32_sdwa s[78:79], v247, %20 src0_sel:BYTE_3 src1_sel:DWORD\n\t"
        "v_add_u32_dpp v228, v228, v228 quad_perm:[2,3,0,1] row_mask:0xf bank_mask:0xf\n\t"
        "v_add_u32_dpp v231, v231, v231 quad_perm:[2,3,0,1] row_mask:0xf bank_mask:0xf\n\t"
        "s_nop 0\n\t"
        "v_add_u32_dpp v228, v228, v228 row_half_mirror row_mask:0xf bank_mask:0xf\n\t"
        "v_add_u32_dpp v231, v231, v231 row_half_mirror row_mask:0xf bank_mask:0xf\n\t"
        "v_cmp_gt_u32 s[64:65], 2, v228\n\t"
        "v_cmp_eq_u32 s[56:57], 1, v228\n\t"
        "v_cmp_gt_u32 s[66:67], 2, v231\n\t"
        "v_cmp_eq_u32 s[58:59], 1, v231\n\t"
        "s_and_b64 s[72:73], s[72:73], s[64:65]\n\t"
        "s_cselect_b64 s[64:65], 0, s[64:65]\n\t"
        "s_and_b64 s[56:57], s[56:57], s[64:65]\n\t"
        "s_not_b64 %0, s[64:65]\n\t"
        "s_or_b64 s[54:55], s[54:55], s[56:57]\n\t"
        "s_or_b64 s[54:55], s[54:55], %0\n\t"
        "s_and_b64 s[78:79], s[78:79], s[66:67]\n\t"
        "s_cselect_b64 s[66:67], 0, s[66:67]\n\t"
        "s_and_b64 s[58:59], s[58:59], s[66:67]\n\t"
        "s_not_b64 %1, s[66:67]\n\t"
        "s_or_b64 s[54:55], s[54:55], s[58:59]\n\t"
        "s_or_b64 s[54:55], s[54:55], %1\n\t"
        "s_mov_b64 exec, s[64:65]\n\t"
        "v_cmpx_ne_u32 s[72:73], -1, v240\n\t"
        "v_mul_hi_u32_u24_sdwa v204, v240, %21 dst_sel:DWORD dst_unused:UNUSED_PAD src0_sel:WORD_0 src1_sel:DWORD\n\t"
        "v_and_b32_sdwa v192, v243, v182 dst_sel:DWORD dst_unused:UNUSED_PAD src0_sel:BYTE_0 src1_sel:DWORD\n\t"
        "v_lshl_add_u32 v204, v204, 2, v192\n\t"
        "ds_add_rtn_u32 v204, v204, v191\n\t"
        "v_perm_b32 v216, v243, v240, v177\n\t"
        "v_mad_u32_u16 v228, v240, 32, %13 op_sel:[1,0,0,0]\n\t"
        "v_cmpx_ne_u32 s[74:75], -1, v241\n\t"
        "v_mul_hi_u32_u24_sdwa v205, v241, %21 dst_sel:DWORD dst_unused:UNUSED_PAD src0_sel:WORD_0 src1_sel:DWORD\n\t"
        "v_and_b32_sdwa v193, v243, v182 dst_sel:DWORD dst_unused:UNUSED_PAD src0_sel:BYTE_1 src1_sel:DWORD\n\t"
        "v_lshl_add_u32 v205, v205, 2, v193\n\t"
        "ds_add_rtn_u32 v205, v205, v191\n\t"
        "v_perm_b32 v217, v243, v241, v178\n\t"
        "v_mad_u32_u16 v229, v241, 32, %13 op_sel:[1,0,0,0]\n\t"
        "s_and_b64 exec, exec, s[52:53]\n\t"
        "v_cmpx_ne_u32 s[76:77], -1, v242\n\t"
        "v_mul_hi_u32_u24_sdwa v206, v242, %21 dst_sel:DWORD dst_unused:UNUSED_PAD src0_sel:WORD_0 src1_sel:DWORD\n\t"
        "v_and_b32_sdwa v194, v243, v182 dst_sel:DWORD dst_unused:UNUSED_PAD src0_sel:BYTE_2 src1_sel:DWORD\n\t"
        "v_lshl_add_u32 v206, v206, 2, v194\n\t"
        "ds_add_rtn_u32 v206, v206, v191\n\t"
        "v_perm_b32 v218, v243, v242, v179\n\t"
        "v_mad_u32_u16 v230, v242, 32, %13 op_sel:[1,0,0,0]\n\t"
        "s_mov_b64 exec, s[66:67]\n\t"
        "v_cmpx_ne_u32 s[78:79], -1, v244\n\t"
        "v_mul_hi_u32_u24_sdwa v207, v244, %21 dst_sel:DWORD dst_unused:UNUSED_PAD src0_sel:WORD_0 src1_sel:DWORD\n\t"
        "v_and_b32_sdwa v195, v247, v182 dst_sel:DWORD dst_unused:UNUSED_PAD src0_sel:BYTE_0 src1_sel:DWORD\n\t"
        "v_lshl_add_u32 v207, v207, 2, v195\n\t"
        "ds_add_rtn_u32 v207, v207, v191\n\t"
        "v_perm_b32 v219, v247, v244, v177\n\t"
        "v_mad_u32_u16 v231, v244, 32, %14 op_sel:[1,0,0,0]\n\t"
        "v_cmpx_ne_u32 s[80:81], -1, v245\n\t"
        "v_mul_hi_u32_u24_sdwa v208, v245, %21 dst_sel:DWORD dst_unused:UNUSED_PAD src0_sel:WORD_0 src1_sel:DWORD\n\t"
        "v_and_b32_sdwa v196, v247, v182 dst_sel:DWORD dst_unused:UNUSED_PAD src0_sel:BYTE_1 src1_sel:DWORD\n\t"
        "v_lshl_add_u32 v208, v208, 2, v196\n\t"
        "ds_add_rtn_u32 v208, v208, v191\n\t"
        "v_perm_b32 v220, v247, v245, v178\n\t"
        "v_mad_u32_u16 v232, v245, 32, %14 op_sel:[1,0,0,0]\n\t"
        "s_and_b64 exec, exec, s[52:53]\n\t"
        "v_cmpx_ne_u32 s[82:83], -1, v246\n\t"
        "v_mul_hi_u32_u24_sdwa v209, v246, %21 dst_sel:DWORD dst_unused:UNUSED_PAD src0_sel:WORD_0 src1_sel:DWORD\n\t"
        "v_and_b32_sdwa v197, v247, v182 dst_sel:DWORD dst_unused:UNUSED_PAD src0_sel:BYTE_2 src1_sel:DWORD\n\t"
        "v_lshl_add_u32 v209, v209, 2, v197\n\t"
        "ds_add_rtn_u32 v209, v209, v191\n\t"
        "v_perm_b32 v221, v247, v246, v179\n\t"
        "v_mad_u32_u16 v233, v246, 32, %14 op_sel:[1,0,0,0]\n\t"
        "s_mov_b64 exec, -1\n\t"
        "s_waitcnt vmcnt(%31)\n\t"
        "v_add3_u32 v234, v248, v249, v250\n\t"
        "v_add3_u32 v237, v252, v253, v254\n\t"
        "v_add3_u32 v234, v234, v251, %19\n\t"
        "v_add3_u32 v237, v237, v255, %19\n\t"
        "v_cmp_ne_u32_sdwa s[84:85], v251, %20 src0_sel:BYTE_3 src1_sel:DWORD\n\t"
        "v_add_u32_dpp v234, v234, v234 quad_perm:[1,0,3,2] row_mask:0xf bank_mask:0xf\n\t"
        "v_add_u32_dpp v237, v237, v237 quad_perm:[1,0,3,2] row_mask:0xf bank_mask:0xf\n\t"
        "v_cmp_ne_u32_sdwa s[90:91], v255, %20 src0_sel:BYTE_3 src1_sel:DWORD\n\t"
        "v_add_u32_dpp v234, v234, v234 quad_perm:[2,3,0,1] row_mask:0xf bank_mask:0xf\n\t"
        "v_add_u32_dpp v237, v237, v237 quad_perm:[2,3,0,1] row_mask:0xf bank_mask:0xf\n\t"
        "s_nop 0\n\t"
        "v_add_u32_dpp v234, v234, v234 row_half_mirror row_mask:0xf bank_mask:0xf\n\t"
        "v_add_u32_dpp v237, v237, v237 row_half_mirror row_mask:0xf bank_mask:0xf\n\t"
        "v_cmp_gt_u32 s[68:69], 2, v234\n\t"
        "v_cmp_eq_u32 s[60:61], 1, v234\n\t"
        "v_cmp_gt_u32 s[70:71], 2, v237\n\t"
        "v_cmp_eq_u32 s[62:63], 1, v237\n\t"
        "s_and_b64 s[84:85], s[84:85], s[68:69]\n\t"
        "s_cselect_b64 s[68:69], 0, s[68:69]\n\t"
        "s_and_b64 s[60:61], s[60:61], s[68:69]\n\t"
        "s_not_b64 %2, s[68:69]\n\t"
        "s_or_b64 s[54:55], s[54:55], s[60:61]\n\t"
        "s_or_b64 s[54:55], s[54:55], %2\n\t"
        "s_and_b64 s[90:91], s[90:91], s[70:71]\n\t"
        "s_cselect_b64 s[70:71], 0, s[70:71]\n\t"
        "s_and_b64 s[62:63], s[62:63], s[70:71]\n\t"
        "s_not_b64 %3, s[70:71]\n\t"
        "s_or_b64 s[54:55], s[54:55], s[62:63]\n\t"
        "s_or_b64 s[54:55], s[54:55], %3\n\t"
        "s_mov_b64 exec, s[72:73]\n\t"
        "s_waitcnt lgkmcnt(3)\n\t"
        "v_min_u32 v204, v181, v204\n\t"
        "v_min_u32 v205, v181, v205\n\t"
        "v_min_u32 v206, v181, v206\n\t"
        "v_mad_u32_u24 v204, v192, v180, v204\n\t"
        "v_mad_u32_u24 v205, v193, v180, v205\n\t"
        "v_mad_u32_u24 v206, v194, v180, v206\n\t"
        "ds_write2st64_b32 v204, v216, v228 offset1:10\n\t"
        "s_mov_b64 exec, s[74:75]\n\t"
        "ds_write2st64_b32 v205, v217, v229 offset1:10\n\t"
        "s_mov_b64 exec, s[76:77]\n\t"
        "ds_write2st64_b32 v206, v218, v230 offset1:10\n\t"
        "s_mov_b64 exec, s[68:69]\n\t"
        "v_cmpx_ne_u32 s[84:85], -1, v248\n\t"
        "v_mul_hi_u32_u24_sdwa v210, v248, %21 dst_sel:DWORD dst_unused:UNUSED_PAD src0_sel:WORD_0 src1_sel:DWORD\n\t"
        "v_and_b32_sdwa v198, v251, v182 dst_sel:DWORD dst_unused:UNUSED_PAD src0_sel:BYTE_0 src1_sel:DWORD\n\t"
        "v_lshl_add_u32 v210, v210, 2, v198\n\t"
        "ds_add_rtn_u32 v210, v210, v191\n\t"
        "v_perm_b32 v222, v251, v248, v177\n\t"
        "v_mad_u32_u16 v234, v248, 32, %15 op_sel:[1,0,0,0]\n\t"
        "v_cmpx_ne_u32 s[86:87], -1, v249\n\t"
        "v_mul_hi_u32_u24_sdwa v211, v249, %21 dst_sel:DWORD dst_unused:UNUSED_PAD src0_sel:WORD_0 src1_sel:DWORD\n\t"
        "v_and_b32_sdwa v199, v251, v182 dst_sel:DWORD dst_unused:UNUSED_PAD src0_sel:BYTE_1 src1_sel:DWORD\n\t"
        "v_lshl_add_u32 v211, v211, 2, v199\n\t"
        "ds_add_rtn_u32 v211, v211, v191\n\t"
        "v_perm_b32 v223, v251, v249, v178\n\t"
        "v_mad_u32_u16 v235, v249, 32, %15 op_sel:[1,0,0,0]\n\t"
        "s_and_b64 exec, exec, s[52:53]\n\t"
        "v_cmpx_ne_u32 s[88:89], -1, v250\n\t"
        "v_mul_hi_u32_u24_sdwa v212, v250, %21 dst_sel:DWORD dst_unused:UNUSED_PAD src0_sel:WORD_0 src1_sel:DWORD\n\t"
        "v_and_b32_sdwa v200, v251, v182 dst_sel:DWORD dst_unused:UNUSED_PAD src0_sel:BYTE_2 src1_sel:DWORD\n\t"
        "v_lshl_add_u32 v212, v212, 2, v200\n\t"
        "ds_add_rtn_u32 v212, v212, v191\n\t"
        "v_perm_b32 v224, v251, v250, v179\n\t"
        "v_mad_u32_u16 v236, v250, 32, %15 op_sel:[1,0,0,0]\n\t"
        "s_mov_b64 exec, s[78:79]\n\t"
        "s_waitcnt lgkmcnt(6)\n\t"
        "v_min_u32 v207, v181, v207\n\t"
        "v_min_u32 v208, v181, v208\n\t"
        "v_min_u32 v209, v181, v209\n\t"
        "v_mad_u32_u24 v207, v195, v180, v207\n\t"
        "v_mad_u32_u24 v208, v196, v180, v208\n\t"
        "v_mad_u32_u24 v209, v197, v180, v209\n\t"
        "ds_write2st64_b32 v207, v219, v231 offset1:10\n\t"
        "s_mov_b64 exec, s[80:81]\n\t"
        "ds_write2st64_b32 v208, v220, v232 offset1:10\n\t"
        "s_mov_b64 exec, s[82:83]\n\t"
        "ds_write2st64_b32 v209, v221, v233 offset1:10\n\t"
        "s_mov_b64 exec, -1\n\t"
        "ds_read_b128 v[184:187], %22\n\t"
        "ds_read_b32 v183, %23\n\t"
        "s_mov_b64 exec, s[70:71]\n\t"
        "v_cmpx_ne_u32 s[90:91], -1, v252\n\t"
        "v_mul_hi_u32_u24_sdwa v213, v252, %21 dst_sel:DWORD dst_unused:UNUSED_PAD src0_sel:WORD_0 src1_sel:DWORD\n\t"
        "v_and_b32_sdwa v201, v255, v182 dst_sel:DWORD dst_unused:UNUSED_PAD src0_sel:BYTE_0 src1_sel:DWORD\n\t"
        "v_lshl_add_u32 v213, v213, 2, v201\n\t"
        "ds_add_rtn_u32 v213, v213, v191\n\t"
        "v_perm_b32 v225, v255, v252, v177\n\t"
        "v_mad_u32_u16 v237, v252, 32, %16 op_sel:[1,0,0,0]\n\t"
        "v_cmpx_ne_u32 s[92:93], -1, v253\n\t"
        "v_mul_hi_u32_u24_sdwa v214, v253, %21 dst_sel:DWORD dst_unused:UNUSED_PAD src0_sel:WORD_0 src1_sel:DWORD\n\t"
        "v_and_b32_sdwa v202, v255, v182 dst_sel:DWORD dst_unused:UNUSED_PAD src0_sel:BYTE_1 src1_sel:DWORD\n\t"
        "v_lshl_add_u32 v214, v214, 2, v202\n\t"
        "ds_add_rtn_u32 v214, v214, v191\n\t"
        "v_perm_b32 v226, v255, v253, v178\n\t"
        "v_mad_u32_u16 v238, v253, 32, %16 op_sel:[1,0,0,0]\n\t"
        "s_and_b64 exec, exec, s[52:53]\n\t"
        "v_cmpx_ne_u32 s[94:95], -1, v254\n\t"
        "v_mul_hi_u32_u24_sdwa v215, v254, %21 dst_sel:DWORD dst_unused:UNUSED_PAD src0_sel:WORD_0 src1_sel:DWORD\n\t"
        "v_and_b32_sdwa v203, v255, v182 dst_sel:DWORD dst_unused:UNUSED_PAD src0_sel:BYTE_2 src1_sel:DWORD\n\t"
        "v_lshl_add_u32 v215, v215, 2, v203\n\t"
        "ds_add_rtn_u32 v215, v215, v191\n\t"
        "v_perm_b32 v227, v255, v254, v179\n\t"
        "v_mad_u32_u16 v239, v254, 32, %16 op_sel:[1,0,0,0]\n\t"
        "s_cmp_eq_u64 s[54:55], 0\n\t"
        "s_cbranch_scc1 59f\n\t"
        "s_mov_b64 exec, s[56:57]\n\t"
        "s_cbranch_execz 40f\n\t"
        "v_bfe_u32 v188, %4, 7, 8\n\t"
        "v_mov_b32 v189, 1\n\t"
        "ds_write_b8 v188, v189 offset:256\n\t"
        "v_add_u32 %4, s96, %4\n\t"
        "s_or_b64 %0, %0, exec\n\t"
        "40:\n\t"
        "s_mov_b64 exec, %0\n\t"
        "s_cbranch_execz 50f\n\t"
        "global_load_dwordx4 v[240:243], %4, %17 sc1\n\t"
        "50:\n\t"
        "s_mov_b64 exec, s[58:59]\n\t"
        "s_cbranch_execz 41f\n\t"
        "v_bfe_u32 v188, %5, 7, 8\n\t"
        "v_mov_b32 v189, 1\n\t"
        "ds_write_b8 v188, v189 offset:256\n\t"
        "v_add_u32 %5, s96, %5\n\t"
        "s_or_b64 %1, %1, exec\n\t"
        "41:\n\t"
        "s_mov_b64 exec, %1\n\t"
        "s_cbranch_execz 51f\n\t"
        "global_load_dwordx4 v[244:247], %5, %17 sc1\n\t"
        "51:\n\t"
        "s_mov_b64 exec, s[60:61]\n\t"
        "s_cbranch_execz 42f\n\t"
        "v_bfe_u32 v188, %6, 7, 8\n\t"
        "v_mov_b32 v189, 1\n\t"
        "ds_write_b8 v188, v189 offset:256\n\t"
        "v_add_u32 %6, s96, %6\n\t"
        "s_or_b64 %2, %2, exec\n\t"
        "42:\n\t"
        "s_mov_b64 exec, %2\n\t"
        "s_cbranch_execz 52f\n\t"
        "global_load_dwordx4 v[248:251], %6, %17 sc1\n\t"
        "52:\n\t"
        "s_mov_b64 exec, s[62:63]\n\t"
        "s_cbranch_execz 43f\n\t"
        "v_bfe_u32 v188, %7, 7, 8\n\t"
        "v_mov_b32 v189, 1\n\t"
        "ds_write_b8 v188, v189 offset:256\n\t"
        "v_add_u32 %7, s96, %7\n\t"
        "s_or_b64 %3, %3, exec\n\t"
        "43:\n\t"
        "s_mov_b64 exec, %3\n\t"
        "s_cbranch_execz 53f\n\t"
        "global_load_dwordx4 v[252:255], %7, %17 sc1\n\t"
        "53:\n\t"
        "s_waitcnt lgkmcnt(0)\n\t"
        "59:\n\t"
        "s_mov_b64 exec, s[84:85]\n\t"
        "s_waitcnt lgkmcnt(8)\n\t"
        "v_min_u32 v210, v181, v210\n\t"
        "v_min_u32 v211, v181, v211\n\t"
        "v_min_u32 v212, v181, v212\n\t"
        "v_mad_u32_u24 v210, v198, v180, v210\n\t"
        "v_mad_u32_u24 v211, v199, v180, v211\n\t"
        "v_mad_u32_u24 v212, v200, v180, v212\n\t"
        "ds_write2st64_b32 v210, v222, v234 offset1:10\n\t"
        "s_mov_b64 exec, s[86:87]\n\t"
        "ds_write2st64_b32 v211, v223, v235 offset1:10\n\t"
        "s_mov_b64 exec, s[88:89]\n\t"
        "ds_write2st64_b32 v212, v224, v236 offset1:10\n\t"
        "s_mov_b64 exec, -1\n\t"
        "s_movk_i32 s96, 128\n\t"
        "s_mov_b32 m0, %29\n\t"
        "s_waitcnt lgkmcnt(6)\n\t"
        "v_max3_u32 v182, v184, v185, v186\n\t"
        "v_max3_u32 v182, v182, v187, v183\n\t"
        "v_cmp_eq_u32 vcc, -1, v182\n\t"
        "v_mad_u32_u16 v184, v184, s96, %24\n\t"
        "v_mad_u32_u16 v185, v185, s96, %25\n\t"
        "v_mad_u32_u16 v186, v186, s96, %24\n\t"
        "v_mad_u32_u16 v187, v187, s96, %25\n\t"
        "v_add_u32 v183, v183, %26\n\t"
        "s_cbranch_vccnz 60f\n\t"
        "global_load_lds_dwordx4 v184, %27\n\t"
        "s_addk_i32 m0, 1024\n\t"
        "s_nop 0\n\t"
        "global_load_lds_dwordx4 v185, %27\n\t"
        "s_addk_i32 m0, 1024\n\t"
        "s_nop 0\n\t"
        "global_load_lds_dwordx4 v186, %27\n\t"
        "s_addk_i32 m0, 1024\n\t"
        "s_nop 0\n\t"
        "global_load_lds_dwordx4 v187, %27\n\t"
        "s_addk_i32 m0, 1024\n\t"
        "s_nop 0\n\t"
        "global_load_lds_dwordx4 v183, %28\n\t"
        "s_mov_b32 %12, 1\n\t"
        "60:\n\t"
        "s_mov_b64 exec, s[90:91]\n\t"
        "s_waitcnt lgkmcnt(3)\n\t"
        "v_min_u32 v213, v181, v213\n\t"
        "v_min_u32 v214, v181, v214\n\t"
        "v_min_u32 v215, v181, v215\n\t"
        "v_mad_u32_u24 v213, v201, v180, v213\n\t"
        "v_mad_u32_u24 v214, v202, v180, v214\n\t"
        "v_mad_u32_u24 v215, v203, v180, v215\n\t"
        "ds_write2st64_b32 v213, v225, v237 offset1:10\n\t"
        "s_mov_b64 exec, s[92:93]\n\t"
        "ds_write2st64_b32 v214, v226, v238 offset1:10\n\t"
        "s_mov_b64 exec, s[94:95]\n\t"
        "ds_write2st64_b32 v215, v227, v239 offset1:10\n\t"
        "s_mov_b64 exec, -1\n\t"
        : "=&s"(pend[0]), "=&s"(pend[1]), "=&s"(pend[2]), "=&s"(pend[3]), "+v"(vo[0]), "+v"(vo[1]), "+v"(vo[2]),
          "+v"(vo[3]), "+{v[240:243]}"(cv[0]), "+{v[244:247]}"(cv[1]), "+{v[248:251]}"(cv[2]), "+{v[252:255]}"(cv[3]),
          "=&s"(early)
        : "v"(eb[0]), "v"(eb[1]), "v"(eb[2]), "v"(eb[3]), "s"(ib), "n"(OFF_L + LARR - 4), "v"(tag[0]), "v"(tag[1]),
          "s"(sq), "v"(aLX + wave * LIST_BYTES), "v"(aLE + wave * LIST_BYTES), "v"(cx0), "v"(cx1), "v"(ce), "s"(x),
          "s"(ES), "s"(lds0 + OFF_R0 + wave * RING_SLOT), "n"(3), "n"(1)
        :
          "memory", "vcc", "scc", "m0", "v177", "v178", "v179", "v180", "v181", "v182", "v183", "v184", "v185",
          "v186", "v187", "v188", "v189", "v190", "v191", "v192", "v193", "v194", "v195", "v196", "v197", "v198",
          "v199", "v200", "v201", "v202", "v203", "v204", "v205", "v206", "v207", "v208", "v209", "v210", "v211",
          "v212", "v213", "v214", "v215", "v216", "v217", "v218", "v219", "v220", "v221", "v222", "v223", "v224",
          "v225", "v226", "v227", "v228", "v229", "v230", "v231", "v232", "v233", "v234", "v235", "v236", "v237",
          "v238", "v239", "s52", "s53", "s54", "s55", "s56", "s57", "s58", "s59", "s60", "s61", "s62", "s63", "s64",
          "s65", "s66", "s67", "s68", "s69", "s70", "s71", "s72", "s73", "s74", "s75", "s76", "s77", "s78", "s79",
          "s80", "s81", "s82", "s83", "s84", "s85", "s86", "s87", "s88", "s89", "s90", "s91", "s92", "s93", "s94",
          "s95", "s96");
  auto uni = [&]() {
#pragma unroll
    for (int k = 0; k < 4; ++k)
      pend[k] = ((unsigned long long)(unsigned)__builtin_amdgcn_readfirstlane((unsigned)(pend[k] >> 32)) << 32) |
                (unsigned)__builtin_amdgcn_readfirstlane((unsigned)pend[k]);
  };
  uni();
  early = __builtin_amdgcn_readfirstlane(early);
  just = early;
  if (__builtin_expect((pend[0] | pend[1] | pend[2] | pend[3] | (unsigned long long)(early ^ 1u)) != 0ull, 0))
#pragma clang loop unroll(disable)
  while (true) {
    if (!early) {
      const u32x4 sx = ld128(aLX + wave * LIST_BYTES);
      const unsigned re = *(const LAS unsigned*)(size_t)(aLE + wave * LIST_BYTES);
      if (__ballot(max(max(max(sx[0], sx[1]), max(sx[2], sx[3])), re) == ~0u) == 0ull) {
        gather5(sx, re, lds0 + OFF_R0 + wave * RING_SLOT);
        early = 1u;
        just = 1u;
      }
    }
    if ((pend[0] | pend[1] | pend[2] | pend[3]) == 0ull || --spin < 0) break;
    asm volatile(
        "s_mov_b32 s96, 0x800000\n\t"
        "v_mov_b32 v181, %17\n\t"
        "v_mov_b32 v180, 160\n\t"
        "v_mov_b32 v182, 0xe0\n\t"
        "v_mov_b32 v177, 0x0c040100\n\t"
        "v_mov_b32 v178, 0x0c050100\n\t"
        "v_mov_b32 v179, 0x0c060100\n\t"
        "s_mov_b32 s52, 0x7f7f7f7f\n\t"
        "s_mov_b32 s53, 0x7f7f7f7f\n\t"
        "v_mov_b32 v191, 4\n\t"
        "s_waitcnt vmcnt(5)\n\t"
        "s_cmp_lg_u32 %21, 0\n\t"
        "s_cbranch_scc1 9f\n\t"
        "s_waitcnt vmcnt(0)\n\t"
        "9:\n\t"
        "v_add3_u32 v228, v240, v241, v242\n\t"
        "v_add3_u32 v229, v244, v245, v246\n\t"
        "v_add3_u32 v230, v248, v249, v250\n\t"
        "v_add3_u32 v231, v252, v253, v254\n\t"
        "v_add3_u32 v228, v228, v243, %18\n\t"
        "v_add3_u32 v229, v229, v247, %18\n\t"
        "v_add3_u32 v230, v230, v251, %18\n\t"
        "v_add3_u32 v231, v231, v255, %18\n\t"
        "v_add_u32_dpp v228, v228, v228 quad_perm:[1,0,3,2] row_mask:0xf bank_mask:0xf\n\t"
        "v_add_u32_dpp v229, v229, v229 quad_perm:[1,0,3,2] row_mask:0xf bank_mask:0xf\n\t"
        "v_add_u32_dpp v230, v230, v230 quad_perm:[1,0,3,2] row_mask:0xf bank_mask:0xf\n\t"
        "v_add_u32_dpp v231, v231, v231 quad_perm:[1,0,3,2] row_mask:0xf bank_mask:0xf\n\t"
        "v_add_u32_dpp v228, v228, v228 quad_perm:[2,3,0,1] row_mask:0xf bank_mask:0xf\n\t"
        "v_add_u32_dpp v229, v229, v229 quad_perm:[2,3,0,1] row_mask:0xf bank_mask:0xf\n\t"
        "v_add_u32_dpp v230, v230, v230 quad_perm:[2,3,0,1] row_mask:0xf bank_mask:0xf\n\t"
        "v_add_u32_dpp v231, v231, v231 quad_perm:[2,3,0,1] row_mask:0xf bank_mask:0xf\n\t"
        "v_add_u32_dpp v228, v228, v228 row_half_mirror row_mask:0xf bank_mask:0xf\n\t"
        "v_add_u32_dpp v229, v229, v229 row_half_mirror row_mask:0xf bank_mask:0xf\n\t"
        "v_add_u32_dpp v230, v230, v230 row_half_mirror row_mask:0xf bank_mask:0xf\n\t"
        "v_add_u32_dpp v231, v231, v231 row_half_mirror row_mask:0xf bank_mask:0xf\n\t"
        "v_cmp_ne_u32_sdwa s[72:73], v243, %19 src0_sel:BYTE_3 src1_sel:DWORD\n\t"
        "v_cmp_gt_u32 s[64:65], 2, v228\n\t"
        "v_cmp_eq_u32 s[56:57], 1, v228\n\t"
        "v_cmp_ne_u32_sdwa s[74:75], v247, %19 src0_sel:BYTE_3 src1_sel:DWORD\n\t"
        "v_cmp_gt_u32 s[66:67], 2, v229\n\t"
        "v_cmp_eq_u32 s[58:59], 1, v229\n\t"
        "v_cmp_ne_u32_sdwa s[76:77], v251, %19 src0_sel:BYTE_3 src1_sel:DWORD\n\t"
        "v_cmp_gt_u32 s[68:69], 2, v230\n\t"
        "v_cmp_eq_u32 s[60:61], 1, v230\n\t"
        "v_cmp_ne_u32_sdwa s[78:79], v255, %19 src0_sel:BYTE_3 src1_sel:DWORD\n\t"
        "v_cmp_gt_u32 s[70:71], 2, v231\n\t"
        "v_cmp_eq_u32 s[62:63], 1, v231\n\t"
        "s_and_b64 s[64:65], s[64:65], %0\n\t"
        "s_and_b64 s[72:73], s[72:73], s[64:65]\n\t"
        "s_cselect_b64 s[64:65], 0, s[64:65]\n\t"
        "s_and_b64 s[56:57], s[56:57], s[64:65]\n\t"
        "s_andn2_b64 %0, %0, s[64:65]\n\t"
        "s_cbranch_scc0 10f\n\t"
        "s_mov_b64 exec, %0\n\t"
        "global_load_dwordx4 v[240:243], %4, %16 sc1\n\t"
        "10:\n\t"
        "s_and_b64 s[66:67], s[66:67], %1\n\t"
        "s_and_b64 s[74:75], s[74:75], s[66:67]\n\t"
        "s_cselect_b64 s[66:67], 0, s[66:67]\n\t"
        "s_and_b64 s[58:59], s[58:59], s[66:67]\n\t"
        "s_andn2_b64 %1, %1, s[66:67]\n\t"
        "s_cbranch_scc0 11f\n\t"
        "s_mov_b64 exec, %1\n\t"
        "global_load_dwordx4 v[244:247], %5, %16 sc1\n\t"
        "11:\n\t"
        "s_and_b64 s[68:69], s[68:69], %2\n\t"
        "s_and_b64 s[76:77], s[76:77], s[68:69]\n\t"
        "s_cselect_b64 s[68:69], 0, s[68:69]\n\t"
        "s_and_b64 s[60:61], s[60:61], s[68:69]\n\t"
        "s_andn2_b64 %2, %2, s[68:69]\n\t"
        "s_cbranch_scc0 12f\n\t"
        "s_mov_b64 exec, %2\n\t"
        "global_load_dwordx4 v[248:251], %6, %16 sc1\n\t"
        "12:\n\t"
        "s_and_b64 s[70:71], s[70:71], %3\n\t"
        "s_and_b64 s[78:79], s[78:79], s[70:71]\n\t"
        "s_cselect_b64 s[70:71], 0, s[70:71]\n\t"
        "s_and_b64 s[62:63], s[62:63], s[70:71]\n\t"
        "s_andn2_b64 %3, %3, s[70:71]\n\t"
        "s_cbranch_scc0 13f\n\t"
        "s_mov_b64 exec, %3\n\t"
        "global_load_dwordx4 v[252:255], %7, %16 sc1\n\t"
        "13:\n\t"
        "s_mov_b64 s[54:55], 0\n\t"
        "s_mov_b64 s[72:73], 0\n\t"
        "s_mov_b64 exec, s[64:65]\n\t"
        "s_cbranch_execz 20f\n\t"
        "v_cmpx_ne_u32 s[72:73], -1, v240\n\t"
        "v_mul_hi_u32_u24_sdwa v204, v240, %20 dst_sel:DWORD dst_unused:UNUSED_PAD src0_sel:WORD_0 src1_sel:DWORD\n\t"
        "v_and_b32_sdwa v192, v243, v182 dst_sel:DWORD dst_unused:UNUSED_PAD src0_sel:BYTE_0 src1_sel:DWORD\n\t"
        "v_lshl_add_u32 v204, v204, 2, v192\n\t"
        "ds_add_rtn_u32 v204, v204, v191\n\t"
        "v_perm_b32 v216, v243, v240, v177\n\t"
        "v_mad_u32_u16 v228, v240, 32, %12 op_sel:[1,0,0,0]\n\t"
        "v_cmpx_ne_u32 s[74:75], -1, v241\n\t"
        "v_mul_hi_u32_u24_sdwa v205, v241, %20 dst_sel:DWORD dst_unused:UNUSED_PAD src0_sel:WORD_0 src1_sel:DWORD\n\t"
        "v_and_b32_sdwa v193, v243, v182 dst_sel:DWORD dst_unused:UNUSED_PAD src0_sel:BYTE_1 src1_sel:DWORD\n\t"
        "v_lshl_add_u32 v205, v205, 2, v193\n\t"
        "ds_add_rtn_u32 v205, v205, v191\n\t"
        "v_perm_b32 v217, v243, v241, v178\n\t"
        "v_mad_u32_u16 v229, v241, 32, %12 op_sel:[1,0,0,0]\n\t"
        "s_and_b64 exec, exec, s[52:53]\n\t"
        "v_cmpx_ne_u32 s[76:77], -1, v242\n\t"
        "v_mul_hi_u32_u24_sdwa v206, v242, %20 dst_sel:DWORD dst_unused:UNUSED_PAD src0_sel:WORD_0 src1_sel:DWORD\n\t"
        "v_and_b32_sdwa v194, v243, v182 dst_sel:DWORD dst_unused:UNUSED_PAD src0_sel:BYTE_2 src1_sel:DWORD\n\t"
        "v_lshl_add_u32 v206, v206, 2, v194\n\t"
        "ds_add_rtn_u32 v206, v206, v191\n\t"
        "v_perm_b32 v218, v243, v242, v179\n\t"
        "v_mad_u32_u16 v230, v242, 32, %12 op_sel:[1,0,0,0]\n\t"
        "s_or_b64 s[54:55], s[54:55], s[56:57]\n\t"
        "20:\n\t"
        "s_mov_b64 s[78:79], 0\n\t"
        "s_mov_b64 exec, s[66:67]\n\t"
        "s_cbranch_execz 21f\n\t"
        "v_cmpx_ne_u32 s[78:79], -1, v244\n\t"
        "v_mul_hi_u32_u24_sdwa v207, v244, %20 dst_sel:DWORD dst_unused:UNUSED_PAD src0_sel:WORD_0 src1_sel:DWORD\n\t"
        "v_and_b32_sdwa v195, v247, v182 dst_sel:DWORD dst_unused:UNUSED_PAD src0_sel:BYTE_0 src1_sel:DWORD\n\t"
        "v_lshl_add_u32 v207, v207, 2, v195\n\t"
        "ds_add_rtn_u32 v207, v207, v191\n\t"
        "v_perm_b32 v219, v247, v244, v177\n\t"
        "v_mad_u32_u16 v231, v244, 32, %13 op_sel:[1,0,0,0]\n\t"
        "v_cmpx_ne_u32 s[80:81], -1, v245\n\t"
        "v_mul_hi_u32_u24_sdwa v208, v245, %20 dst_sel:DWORD dst_unused:UNUSED_PAD src0_sel:WORD_0 src1_sel:DWORD\n\t"
        "v_and_b32_sdwa v196, v247, v182 dst_sel:DWORD dst_unused:UNUSED_PAD src0_sel:BYTE_1 src1_sel:DWORD\n\t"
        "v_lshl_add_u32 v208, v208, 2, v196\n\t"
        "ds_add_rtn_u32 v208, v208, v191\n\t"
        "v_perm_b32 v220, v247, v245, v178\n\t"
        "v_mad_u32_u16 v232, v245, 32, %13 op_sel:[1,0,0,0]\n\t"
        "s_and_b64 exec, exec, s[52:53]\n\t"
        "v_cmpx_ne_u32 s[82:83], -1, v246\n\t"
        "v_mul_hi_u32_u24_sdwa v209, v246, %20 dst_sel:DWORD dst_unused:UNUSED_PAD src0_sel:WORD_0 src1_sel:DWORD\n\t"
        "v_and_b32_sdwa v197, v247, v182 dst_sel:DWORD dst_unused:UNUSED_PAD src0_sel:BYTE_2 src1_sel:DWORD\n\t"
        "v_lshl_add_u32 v209, v209, 2, v197\n\t"
        "ds_add_rtn_u32 v209, v209, v191\n\t"
        "v_perm_b32 v221, v247, v246, v179\n\t"
        "v_mad_u32_u16 v233, v246, 32, %13 op_sel:[1,0,0,0]\n\t"
        "s_or_b64 s[54:55], s[54:55], s[58:59]\n\t"
        "21:\n\t"
        "s_mov_b64 s[84:85], 0\n\t"
        "s_mov_b64 exec, s[68:69]\n\t"
        "s_cbranch_execz 22f\n\t"
        "v_cmpx_ne_u32 s[84:85], -1, v248\n\t"
        "v_mul_hi_u32_u24_sdwa v210, v248, %20 dst_sel:DWORD dst_unused:UNUSED_PAD src0_sel:WORD_0 src1_sel:DWORD\n\t"
        "v_and_b32_sdwa v198, v251, v182 dst_sel:DWORD dst_unused:UNUSED_PAD src0_sel:BYTE_0 src1_sel:DWORD\n\t"
        "v_lshl_add_u32 v210, v210, 2, v198\n\t"
        "ds_add_rtn_u32 v210, v210, v191\n\t"
        "v_perm_b32 v222, v251, v248, v177\n\t"
        "v_mad_u32_u16 v234, v248, 32, %14 op_sel:[1,0,0,0]\n\t"
        "v_cmpx_ne_u32 s[86:87], -1, v249\n\t"
        "v_mul_hi_u32_u24_sdwa v211, v249, %20 dst_sel:DWORD dst_unused:UNUSED_PAD src0_sel:WORD_0 src1_sel:DWORD\n\t"
        "v_and_b32_sdwa v199, v251, v182 dst_sel:DWORD dst_unused:UNUSED_PAD src0_sel:BYTE_1 src1_sel:DWORD\n\t"
        "v_lshl_add_u32 v211, v211, 2, v199\n\t"
        "ds_add_rtn_u32 v211, v211, v191\n\t"
        "v_perm_b32 v223, v251, v249, v178\n\t"
        "v_mad_u32_u16 v235, v249, 32, %14 op_sel:[1,0,0,0]\n\t"
        "s_and_b64 exec, exec, s[52:53]\n\t"
        "v_cmpx_ne_u32 s[88:89], -1, v250\n\t"
        "v_mul_hi_u32_u24_sdwa v212, v250, %20 dst_sel:DWORD dst_unused:UNUSED_PAD src0_sel:WORD_0 src1_sel:DWORD\n\t"
        "v_and_b32_sdwa v200, v251, v182 dst_sel:DWORD dst_unused:UNUSED_PAD src0_sel:BYTE_2 src1_sel:DWORD\n\t"
        "v_lshl_add_u32 v212, v212, 2, v200\n\t"
        "ds_add_rtn_u32 v212, v212, v191\n\t"
        "v_perm_b32 v224, v251, v250, v179\n\t"
        "v_mad_u32_u16 v236, v250, 32, %14 op_sel:[1,0,0,0]\n\t"
        "s_or_b64 s[54:55], s[54:55], s[60:61]\n\t"
        "22:\n\t"
        "s_mov_b64 s[90:91], 0\n\t"
        "s_mov_b64 exec, s[70:71]\n\t"
        "s_cbranch_execz 23f\n\t"
        "v_cmpx_ne_u32 s[90:91], -1, v252\n\t"
        "v_mul_hi_u32_u24_sdwa v213, v252, %20 dst_sel:DWORD dst_unused:UNUSED_PAD src0_sel:WORD_0 src1_sel:DWORD\n\t"
        "v_and_b32_sdwa v201, v255, v182 dst_sel:DWORD dst_unused:UNUSED_PAD src0_sel:BYTE_0 src1_sel:DWORD\n\t"
        "v_lshl_add_u32 v213, v213, 2, v201\n\t"
        "ds_add_rtn_u32 v213, v213, v191\n\t"
        "v_perm_b32 v225, v255, v252, v177\n\t"
        "v_mad_u32_u16 v237, v252, 32, %15 op_sel:[1,0,0,0]\n\t"
        "v_cmpx_ne_u32 s[92:93], -1, v253\n\t"
        "v_mul_hi_u32_u24_sdwa v214, v253, %20 dst_sel:DWORD dst_unused:UNUSED_PAD src0_sel:WORD_0 src1_sel:DWORD\n\t"
        "v_and_b32_sdwa v202, v255, v182 dst_sel:DWORD dst_unused:UNUSED_PAD src0_sel:BYTE_1 src1_sel:DWORD\n\t"
        "v_lshl_add_u32 v214, v214, 2, v202\n\t"
        "ds_add_rtn_u32 v214, v214, v191\n\t"
        "v_perm_b32 v226, v255, v253, v178\n\t"
        "v_mad_u32_u16 v238, v253, 32, %15 op_sel:[1,0,0,0]\n\t"
        "s_and_b64 exec, exec, s[52:53]\n\t"
        "v_cmpx_ne_u32 s[94:95], -1, v254\n\t"
        "v_mul_hi_u32_u24_sdwa v215, v254, %20 dst_sel:DWORD dst_unused:UNUSED_PAD src0_sel:WORD_0 src1_sel:DWORD\n\t"
        "v_and_b32_sdwa v203, v255, v182 dst_sel:DWORD dst_unused:UNUSED_PAD src0_sel:BYTE_2 src1_sel:DWORD\n\t"
        "v_lshl_add_u32 v215, v215, 2, v203\n\t"
        "ds_add_rtn_u32 v215, v215, v191\n\t"
        "v_perm_b32 v227, v255, v254, v179\n\t"
        "v_mad_u32_u16 v239, v254, 32, %15 op_sel:[1,0,0,0]\n\t"
        "s_or_b64 s[54:55], s[54:55], s[62:63]\n\t"
        "23:\n\t"
        "s_cmp_eq_u64 s[54:55], 0\n\t"
        "s_cbranch_scc1 59f\n\t"
        "s_mov_b64 exec, s[56:57]\n\t"
        "s_cbranch_execz 40f\n\t"
        "v_bfe_u32 v188, %4, 7, 8\n\t"
        "v_lshrrev_b32 v189, 23, %4\n\t"
        "v_add_u32 v189, 1, v189\n\t"
        "ds_write_b8 v188, v189 offset:256\n\t"
        "v_add_u32 %4, s96, %4\n\t"
        "v_mov_b32 v190, 0\n\t"
        "v_cmpx_eq_u32 vcc, 2, v189\n\t"
        "ds_write_b32 v190, v191 offset:20\n\t"
        "s_andn2_b64 exec, s[56:57], vcc\n\t"
        "s_cbranch_execz 40f\n\t"
        "s_or_b64 %0, %0, exec\n\t"
        "global_load_dwordx4 v[240:243], %4, %16 sc1\n\t"
        "40:\n\t"
        "s_mov_b64 exec, s[58:59]\n\t"
        "s_cbranch_execz 41f\n\t"
        "v_bfe_u32 v188, %5, 7, 8\n\t"
        "v_lshrrev_b32 v189, 23, %5\n\t"
        "v_add_u32 v189, 1, v189\n\t"
        "ds_write_b8 v188, v189 offset:256\n\t"
        "v_add_u32 %5, s96, %5\n\t"
        "v_mov_b32 v190, 0\n\t"
        "v_cmpx_eq_u32 vcc, 2, v189\n\t"
        "ds_write_b32 v190, v191 offset:20\n\t"
        "s_andn2_b64 exec, s[58:59], vcc\n\t"
        "s_cbranch_execz 41f\n\t"
        "s_or_b64 %1, %1, exec\n\t"
        "global_load_dwordx4 v[244:247], %5, %16 sc1\n\t"
        "41:\n\t"
        "s_mov_b64 exec, s[60:61]\n\t"
        "s_cbranch_execz 42f\n\t"
        "v_bfe_u32 v188, %6, 7, 8\n\t"
        "v_lshrrev_b32 v189, 23, %6\n\t"
        "v_add_u32 v189, 1, v189\n\t"
        "ds_write_b8 v188, v189 offset:256\n\t"
        "v_add_u32 %6, s96, %6\n\t"
        "v_mov_b32 v190, 0\n\t"
        "v_cmpx_eq_u32 vcc, 2, v189\n\t"
        "ds_write_b32 v190, v191 offset:20\n\t"
        "s_andn2_b64 exec, s[60:61], vcc\n\t"
        "s_cbranch_execz 42f\n\t"
        "s_or_b64 %2, %2, exec\n\t"
        "global_load_dwordx4 v[248:251], %6, %16 sc1\n\t"
        "42:\n\t"
        "s_mov_b64 exec, s[62:63]\n\t"
        "s_cbranch_execz 43f\n\t"
        "v_bfe_u32 v188, %7, 7, 8\n\t"
        "v_lshrrev_b32 v189, 23, %7\n\t"
        "v_add_u32 v189, 1, v189\n\t"
        "ds_write_b8 v188, v189 offset:256\n\t"
        "v_add_u32 %7, s96, %7\n\t"
        "v_mov_b32 v190, 0\n\t"
        "v_cmpx_eq_u32 vcc, 2, v189\n\t"
        "ds_write_b32 v190, v191 offset:20\n\t"
        "s_andn2_b64 exec, s[62:63], vcc\n\t"
        "s_cbranch_execz 43f\n\t"
        "s_or_b64 %3, %3, exec\n\t"
        "global_load_dwordx4 v[252:255], %7, %16 sc1\n\t"
        "43:\n\t"
        "59:\n\t"
        "s_waitcnt lgkmcnt(0)\n\t"
        "s_mov_b64 exec, s[72:73]\n\t"
        "s_cbranch_execz 30f\n\t"
        "v_min_u32 v204, v181, v204\n\t"
        "v_min_u32 v205, v181, v205\n\t"
        "v_min_u32 v206, v181, v206\n\t"
        "v_mad_u32_u24 v204, v192, v180, v204\n\t"
        "v_mad_u32_u24 v205, v193, v180, v205\n\t"
        "v_mad_u32_u24 v206, v194, v180, v206\n\t"
        "ds_write2st64_b32 v204, v216, v228 offset1:10\n\t"
        "s_mov_b64 exec, s[74:75]\n\t"
        "ds_write2st64_b32 v205, v217, v229 offset1:10\n\t"
        "s_mov_b64 exec, s[76:77]\n\t"
        "ds_write2st64_b32 v206, v218, v230 offset1:10\n\t"
        "30:\n\t"
        "s_mov_b64 exec, s[78:79]\n\t"
        "s_cbranch_execz 31f\n\t"
        "v_min_u32 v207, v181, v207\n\t"
        "v_min_u32 v208, v181, v208\n\t"
        "v_min_u32 v209, v181, v209\n\t"
        "v_mad_u32_u24 v207, v195, v180, v207\n\t"
        "v_mad_u32_u24 v208, v196, v180, v208\n\t"
        "v_mad_u32_u24 v209, v197, v180, v209\n\t"
        "ds_write2st64_b32 v207, v219, v231 offset1:10\n\t"
        "s_mov_b64 exec, s[80:81]\n\t"
        "ds_write2st64_b32 v208, v220, v232 offset1:10\n\t"
        "s_mov_b64 exec, s[82:83]\n\t"
        "ds_write2st64_b32 v209, v221, v233 offset1:10\n\t"
        "31:\n\t"
        "s_mov_b64 exec, s[84:85]\n\t"
        "s_cbranch_execz 32f\n\t"
        "v_min_u32 v210, v181, v210\n\t"
        "v_min_u32 v211, v181, v211\n\t"
        "v_min_u32 v212, v181, v212\n\t"
        "v_mad_u32_u24 v210, v198, v180, v210\n\t"
        "v_mad_u32_u24 v211, v199, v180, v211\n\t"
        "v_mad_u32_u24 v212, v200, v180, v212\n\t"
        "ds_write2st64_b32 v210, v222, v234 offset1:10\n\t"
        "s_mov_b64 exec, s[86:87]\n\t"
        "ds_write2st64_b32 v211, v223, v235 offset1:10\n\t"
        "s_mov_b64 exec, s[88:89]\n\t"
        "ds_write2st64_b32 v212, v224, v236 offset1:10\n\t"
        "32:\n\t"
        "s_mov_b64 exec, s[90:91]\n\t"
        "s_cbranch_execz 33f\n\t"
        "v_min_u32 v213, v181, v213\n\t"
        "v_min_u32 v214, v181, v214\n\t"
        "v_min_u32 v215, v181, v215\n\t"
        "v_mad_u32_u24 v213, v201, v180, v213\n\t"
        "v_mad_u32_u24 v214, v202, v180, v214\n\t"
        "v_mad_u32_u24 v215, v203, v180, v215\n\t"
        "ds_write2st64_b32 v213, v225, v237 offset1:10\n\t"
        "s_mov_b64 exec, s[92:93]\n\t"
        "ds_write2st64_b32 v214, v226, v238 offset1:10\n\t"
        "s_mov_b64 exec, s[94:95]\n\t"
        "ds_write2st64_b32 v215, v227, v239 offset1:10\n\t"
        "33:\n\t"
        "s_mov_b64 exec, -1\n\t"
        : "+&s"(pend[0]), "+&s"(pend[1]), "+&s"(pend[2]), "+&s"(pend[3]), "+v"(vo[0]), "+v"(vo[1]), "+v"(vo[2]),
          "+v"(vo[3]), "+{v[240:243]}"(cv[0]), "+{v[244:247]}"(cv[1]), "+{v[248:251]}"(cv[2]), "+{v[252:255]}"(cv[3])
        : "v"(eb[0]), "v"(eb[1]), "v"(eb[2]), "v"(eb[3]), "s"(ib), "n"(OFF_L + LARR - 4), "v"(tag[0]), "v"(tag[1]),
          "s"(sq), "s"(just)
        :
          "memory", "vcc", "scc", "v177", "v178", "v179", "v180", "v181", "v182", "v188", "v189", "v190", "v191", "v192",
          "v193", "v194", "v195", "v196", "v197", "v198", "v199", "v200", "v201", "v202", "v203", "v204", "v205",
          "v206", "v207", "v208", "v209", "v210", "v211", "v212", "v213", "v214", "v215", "v216", "v217", "v218",
          "v219", "v220", "v221", "v222", "v223", "v224", "v225", "v226", "v227", "v228", "v229", "v230", "v231",
          "v232", "v233", "v234", "v235", "v236", "v237", "v238", "v239", "s52", "s53", "s54", "s55", "s56", "s57",
          "s58", "s59", "s60", "s61", "s62", "s63", "s64", "s65", "s66", "s67", "s68", "s69", "s70", "s71", "s72",
          "s73", "s74", "s75", "s76", "s77", "s78", "s79", "s80", "s81", "s82", "s83", "s84", "s85", "s86", "s87",
          "s88", "s89", "s90", "s91", "s92", "s93", "s94", "s95", "s96");
    uni();
    just = 0u;
  }
  *(LAS unsigned*)((LAS char*)smem + OFF_CNT + 16 + wave * 32) = early;
  asm volatile("s_waitcnt lgkmcnt(0)\n\ts_barrier" ::: "memory");
  if (wave >= 4) __builtin_amdgcn_s_setprio(1);
  const u32x4 cw = *(const LAS u32x4*)((LAS char*)smem + OFF_CNT + rid * 32);
  const unsigned ef = *(const LAS unsigned*)((LAS char*)smem + OFF_CNT + 16 + rid * 32);
  unsigned slowf = *(const LAS unsigned*)((LAS char*)smem + OFF_CNT + 20);
  asm volatile("" : "+v"(slowf));
  const int key = (int)((cw[0] + cw[1] + cw[2] + cw[3]) * 2u + rid);
  int gb;
  {
    unsigned acc = 0u;
#define RK(k) acc = __builtin_amdgcn_alignbit(acc, (unsigned)(__builtin_amdgcn_update_dpp(0, key, 0x120 + k, 0xf, 0xf, false) - key), 31)
    RK(1); RK(2); RK(3); RK(4); RK(5); RK(6); RK(7);
#undef RK
    const int want = RANKOF(wave);
    gb = __builtin_ctzll(__ballot(__builtin_popcount(acc) == want)) & 7;
  }
  const unsigned ring0 = lds0 + OFF_R0 + gb * RING_SLOT;
  const unsigned la = l0 + gb * LIST_BYTES;
  bf16x8 XAf[2][2];
#pragma unroll
  for (int q = 0; q < 4; ++q) XAf[q >> 1][q & 1] = *(const LAS bf16x8*)(size_t)(ring0 + ringd + q * 1024 + lane * 16);
  int cnt = (int)(__builtin_amdgcn_readlane(key, gb) - 8u * l0 - 12u * SUBL) >> 3;
  unsigned ce_[4];
#pragma unroll
  for (int q = 0; q < 4; ++q) ce_[q] = __builtin_amdgcn_readlane(cw[q], gb) - (l0 + q * SUBL);
  const bool slow = cnt > LCAP || max(max(ce_[0], ce_[1]), max(ce_[2], ce_[3])) > SUBL - 4u ||
                    __builtin_amdgcn_readfirstlane(slowf) != 0u;
  const unsigned cg = (bu * 8 + gb) * 32;
  aLX += gb * LIST_BYTES; aLE += gb * LIST_BYTES; aLS4 += gb * LIST_BYTES; aLS1 += gb * LIST_BYTES;
  auto issue_gather = [&](int tile, int rs) {
    gather5(ld128(aLX + tile * 128), *(const LAS unsigned*)(size_t)(aLE + tile * 128), ring0 + rs * ringd);
  };
  auto pads = [&](int cnt, int lane) __attribute__((always_inline)) {
    const int u = cnt + lane;
    const unsigned c = cg + ((u >> 2) & 7) + 8 * (u & 3), n = nodeof(c);
    const unsigned padA = n | (__builtin_amdgcn_readlane(c, __builtin_ctzll(__ballot((n * Mw + Cw) >> 16 != c))) & 0xffu) << 16;
    const unsigned pa = la + u * 4;
    *(LAS unsigned*)(size_t)pa = padA;
    *(LAS unsigned*)(size_t)(pa + 256) = padA;
    *(LAS unsigned*)(size_t)(pa + LARR) = 0u;
    *(LAS unsigned*)(size_t)(pa + LARR + 256) = 0u;
  };
  constexpr int GEND = 64 + NCH;
  int gs = slow ? 0 : GEND, gp = 0;
  auto gen = [&]() __attribute__((always_inline)) {
    int l2, n = 0;
    asm volatile("v_mbcnt_lo_u32_b32 %0, -1, 0\n\tv_mbcnt_hi_u32_b32 %0, -1, %0" : "=v"(l2));
    const LAS unsigned char* xf = (const LAS unsigned char*)smem + OFF_XF;
    auto put = [&](bool ok, unsigned t, unsigned c, int cc) {
      ok = ok && (int)(c >> 5) == gb;
      const unsigned long long bm = __ballot(ok);
      const unsigned pa = la + 4u * (n + __builtin_amdgcn_mbcnt_hi((unsigned)(bm >> 32), __builtin_amdgcn_mbcnt_lo((unsigned)bm, 0u)));
      if (ok) {
        *(LAS unsigned*)(size_t)pa = (t & 0xffffu) | (c << 16);
        *(LAS unsigned*)(size_t)(pa + LARR) = ((unsigned)(cc * chunk) + (t >> 16)) * 32u;
      }
      n += __builtin_popcountll(bm);
    };
#pragma unroll 1
    while (gs < GEND && n <= LCAP - 192) {
      if (gs < 64) {
        const int cc = (gs & 31) * 8 + (l2 >> 3);
        const unsigned* pp = reinterpret_cast<const unsigned*>(ib + (gs >> 5) * IEXT + (gs & 31) * 1024 + l2 * 16);
        const bool live = gs < 32 || xf[cc] != 0;
        const unsigned m = ldd(pp + 3);
#pragma unroll 1
        for (int j = 0; j < 3; ++j) {
          const unsigned t = ldd(pp + j);
          put(live && t != ~0u && (j < 2 || (l2 & 7) != 7), t, (m >> (8 * j)) & 0xffu, cc);
        }
        ++gs;
      } else {
        const int c2 = gs - 64 + l2;
        const unsigned long long fm = __ballot(c2 < NCH && xf[min(c2, NCH - 1)] == 2);
        if (fm == 0ull) { gs = min(gs + 64, GEND); continue; }
        gs += __builtin_ctzll(fm);
        const int cc = gs - 64;
        const unsigned long long ds = ldq(FL + 512 + bu * NCH + cc);
        const int len = __builtin_amdgcn_readfirstlane((int)(ds >> 32));
        const unsigned long long* tp = TS + (unsigned)ds;
#pragma unroll 1
        for (; gp < len && n <= LCAP - 64; gp += 64) {
          const unsigned long long t = ldq(tp + min(gp + l2, len - 1));
          put(gp + l2 < len, (unsigned)t, (unsigned)(t >> 32), cc);
        }
        if (gp >= len) { gp = 0; ++gs; }
      }
    }
    __builtin_amdgcn_s_waitcnt(0x0F70);
    pads(n, l2);
    return n;
  };
  const bool quick = !slow && ce_[0] >= 256u && __builtin_amdgcn_readlane(ef, gb) != 0u;
  auto gather1 = [&](unsigned skip) {
    u32x4 sx = ld128(aLX + 128);
    unsigned re = *(const LAS unsigned*)(size_t)(aLE + 128);
    asm volatile("s_cmp_lg_u32 %16, 0\n\t"
                 "s_cbranch_scc1 1f\n\t"
                 "s_mov_b32 m0, %14\n\t"
                 "v_mad_u32_u16 %0, %0, %15, %9\n\t"
                 "v_mad_u32_u16 %1, %1, %15, %10\n\t"
                 "global_load_lds_dwordx4 %0, %12\n\t"
                 "s_addk_i32 m0, 1024\n\t"
                 "v_mad_u32_u16 %2, %2, %15, %9\n\t"
                 "global_load_lds_dwordx4 %1, %12\n\t"
                 "s_addk_i32 m0, 1024\n\t"
                 "v_mad_u32_u16 %3, %3, %15, %10\n\t"
                 "global_load_lds_dwordx4 %2, %12\n\t"
                 "s_addk_i32 m0, 1024\n\t"
                 "v_add_u32 %4, %4, %11\n\t"
                 "global_load_lds_dwordx4 %3, %12\n\t"
                 "s_addk_i32 m0, 1024\n\t"
                 "s_nop 0\n\t"
                 "global_load_lds_dwordx4 %4, %13\n"
                 "1:"
                 : "+v"(sx[0]), "+v"(sx[1]), "+v"(sx[2]), "+v"(sx[3]), "+v"(re), "+v"(XAf[0][0]), "+v"(XAf[0][1]),
                   "+v"(XAf[1][0]), "+v"(XAf[1][1])
                 : "v"(cx0), "v"(cx1), "v"(ce), "s"(x), "s"(ES), "s"(ring0 + ringd), "s"(128u), "s"(skip)
                 : "memory", "m0", "scc");
  };
  const unsigned g1 = __builtin_amdgcn_readfirstlane(quick && wave >= G1W ? 1u : 0u);
  gather1(g1 ^ 1u);
  if (__builtin_expect(!slow, 1)) {
    const unsigned o2 = la + ce_[0] + ce_[1], o3 = o2 + ce_[2], oe = o3 + ce_[3];
    const unsigned d1 = SUBL - ce_[0], d2 = la + 2 * SUBL - o2, d3 = la + 3 * SUBL - o3;
#pragma unroll 1
    for (unsigned i = la + ce_[0]; i < oe; i += 6 * 256u) {
      const unsigned pp = i + lane * 4u;
      unsigned t[6];
#pragma unroll
      for (int k = 0; k < 6; ++k) t[k] = pp + (pp < o2 - k * 256u ? d1 : pp < o3 - k * 256u ? d2 : d3);
      asm volatile(
          "ds_read2st64_b32 v[228:229], %0 offset0:0 offset1:10\n\t"
          "ds_read2st64_b32 v[230:231], %1 offset0:1 offset1:11\n\t"
          "ds_read2st64_b32 v[232:233], %2 offset0:2 offset1:12\n\t"
          "ds_read2st64_b32 v[234:235], %3 offset0:3 offset1:13\n\t"
          "ds_read2st64_b32 v[236:237], %4 offset0:4 offset1:14\n\t"
          "ds_read2st64_b32 v[238:239], %5 offset0:5 offset1:15\n\t"
          "v_cmpx_lt_i32 vcc, 0, %7\n\t"
          "s_waitcnt lgkmcnt(5)\n\t"
          "ds_write2st64_b32 %6, v228, v229 offset0:0 offset1:10\n\t"
          "v_cmpx_lt_i32 vcc, 256, %7\n\t"
          "s_waitcnt lgkmcnt(5)\n\t"
          "ds_write2st64_b32 %6, v230, v231 offset0:1 offset1:11\n\t"
          "v_cmpx_lt_i32 vcc, 512, %7\n\t"
          "s_waitcnt lgkmcnt(5)\n\t"
          "ds_write2st64_b32 %6, v232, v233 offset0:2 offset1:12\n\t"
          "v_cmpx_lt_i32 vcc, 768, %7\n\t"
          "s_waitcnt lgkmcnt(5)\n\t"
          "ds_write2st64_b32 %6, v234, v235 offset0:3 offset1:13\n\t"
          "v_cmpx_lt_i32 vcc, 1024, %7\n\t"
          "s_waitcnt lgkmcnt(5)\n\t"
          "ds_write2st64_b32 %6, v236, v237 offset0:4 offset1:14\n\t"
          "v_cmpx_lt_i32 vcc, 1280, %7\n\t"
          "s_waitcnt lgkmcnt(5)\n\t"
          "ds_write2st64_b32 %6, v238, v239 offset0:5 offset1:15\n\t"
          "s_mov_b64 exec, -1"
          :: "v"(t[0]), "v"(t[1]), "v"(t[2]), "v"(t[3]), "v"(t[4]), "v"(t[5]), "v"(pp), "v"(oe - pp)
          : "memory", "vcc", "v228", "v229", "v230", "v231", "v232", "v233", "v234", "v235", "v236", "v237", "v238", "v239");
    }
    pads(cnt, lane);
  }
#pragma unroll
  for (int ct = 0; ct < 2; ++ct)
#pragma unroll
    for (int m = 6; m < 15; ++m)
      if ((HELD >> m) & 1) wr[ct][m] = wl[widx(m, ct) * 64];
#pragma unroll
  for (int ct = 0; ct < 2; ++ct)
#pragma unroll
    for (int m = 6; m < 15; ++m)
      if ((HELD >> m) & 1) asm volatile("" : "+v"(wr[ct][m]));
#pragma unroll
  for (int t = 0; t < 4; ++t) fo[t] += gb * RING_SLOT;
  foe += gb * RING_SLOT;
  cS -= gb * 32 * TPITCH;
  cSt -= gb * 32 * TPITCH;
  asm volatile("s_waitcnt vmcnt(5)\n\ts_cmp_lg_u32 %7, 0\n\ts_cbranch_scc1 1f\n\ts_waitcnt vmcnt(0)\n1:\n\ts_barrier"
               : "+v"(fo[0]), "+v"(fo[1]), "+v"(fo[2]), "+v"(fo[3]), "+v"(foe), "+v"(cS), "+v"(cSt) : "s"(g1) : "memory", "scc");
  bool redo = !quick;
  gather1(__builtin_amdgcn_readfirstlane(quick ? g1 : 1u));
  auto rd128 = [&](unsigned a) { return *(const LAS bf16x8*)(size_t)a; };
  auto trow = [&](unsigned a, unsigned c) {
    unsigned t;
    asm("v_mad_u32_u16 %0, %1, %2, %3 op_sel:[1,0,0,0]" : "=v"(t) : "v"(a), "s"(TPITCH), "v"(c));
    return t;
  };
  auto load_S = [&](const u32x4 ss) {
    const s16x4 a0 = __builtin_amdgcn_ds_read_tr16_b64_v4i16((LAS s16x4*)(size_t)trow(ss[0], cS));
    const s16x4 a1 = __builtin_amdgcn_ds_read_tr16_b64_v4i16((LAS s16x4*)(size_t)trow(ss[1], cS));
    const s16x4 a2 = __builtin_amdgcn_ds_read_tr16_b64_v4i16((LAS s16x4*)(size_t)trow(ss[2], cS));
    const s16x4 a3 = __builtin_amdgcn_ds_read_tr16_b64_v4i16((LAS s16x4*)(size_t)trow(ss[3], cS));
    S[0] = __builtin_shufflevector(a0, a1, 0, 1, 2, 3, 4, 5, 6, 7);
    S[1] = __builtin_shufflevector(a2, a3, 0, 1, 2, 3, 4, 5, 6, 7);
  };
  auto wf = [&](int m, int ct) { return ((HELD >> m) & 1) ? wr[ct][m] : b[(m - 6) % 3]; };

  auto ew = [&](int m, const f32x16& Gc, const f32x16& Vc, const f32x16& Gn, const f32x16& Vp) {
    if (m < 2) {
      const int i = 14 + m;
      pq[i] = mulf(i, Vp[i], rq[i]);
      if (m == 1) pn[1][3] = pack_bf16(pq[14], pq[15]);
    } else if (m == 2) {
      pq[0] = mulf(0, Vc[0], rq[0]);
      pq[1] = mulf(1, Vc[1], rq[1]);
      pn[0][0] = pack_bf16(pq[0], pq[1]);
    } else {
      const int i = m - 1;
      pq[i] = mulf(i, Vc[i], rq[i]);
      if (i & 1) pn[i >> 3][(i >> 1) & 3] = pack_bf16(pq[i - 1], pq[i]);
    }
    if (m == 0) pd[0][0] = pack_bf16(rq[0], rq[1]);
    else if ((m & 1) == 0) { const int i = m + 1; pd[i >> 3][(i >> 1) & 3] = pack_bf16(rq[i - 1], rq[i]); }
    if (m < 14) {
      const int i = m + 2;
      rq[i] = __builtin_amdgcn_rcpf(add1(tq[i]));
    } else {
      rq[0] = __builtin_amdgcn_rcpf(add1(tq[0]));
      rq[1] = __builtin_amdgcn_rcpf(add1(tq[1]));
    }
    if (m < 13) tq[m + 3] = __builtin_amdgcn_exp2f(Gc[m + 3]);
    else if (m == 13) { tq[0] = __builtin_amdgcn_exp2f(Gn[0]); tq[1] = __builtin_amdgcn_exp2f(Gn[1]); }
    else tq[2] = __builtin_amdgcn_exp2f(Gn[2]);
  };

  auto drain = [&]() {
    pq[14] = mulf(0, V1[14], rq[14]);
    pq[15] = mulf(1, V1[15], rq[15]);
    pn[1][3] = pack_bf16(pq[14], pq[15]);
    Y[1] = MFMA32(__builtin_bit_cast(bf16x8, pd[0]), S[0], Y[1]);
    Y[3] = MFMA32(__builtin_bit_cast(bf16x8, pn[0]), S[0], Y[3]);
    Y[1] = MFMA32(__builtin_bit_cast(bf16x8, pd[1]), S[1], Y[1]);
    Y[3] = MFMA32(__builtin_bit_cast(bf16x8, pn[1]), S[1], Y[3]);
  };
  while (true) {
    if (__builtin_expect(redo, 0)) {
      asm volatile("s_waitcnt vmcnt(0)" ::: "memory");
      if (slow) cnt = gen();
      issue_gather(0, 0);
      issue_gather(1, 1);
    }
    const int ntiles = (cnt + 31) >> 5;
    {
      const unsigned sl = *(const LAS unsigned short*)(size_t)aLS1;
      bf16x8 w0[15];
#pragma unroll
      for (int m = 6; m < 15; ++m) w0[m] = ((HELD >> m) & 1) ? wr[0][m] : wl[widx(m, 0) * 64];
      u32x4 sx = ld128(aLX + 256);
      unsigned re = *(const LAS unsigned*)(size_t)(aLE + 256);
      St[0] = rd128(sl * TPITCH + cSt);
      St[1] = rd128(sl * TPITCH + cSt + 16);
      G0 = MFMA32(St[0], XAf[0][0], zero16);
      G0 = MFMA32(St[1], XAf[1][0], G0);
      pipe_zero();
#pragma unroll
      for (int m = 6; m < 15; ++m)
        if (!((HELD >> m) & 1)) asm volatile("" : "+v"(w0[m]));
      asm volatile("s_waitcnt vmcnt(5)" : "+v"(sx), "+v"(re), "+v"(G0) :: "memory");
#pragma unroll
      for (int k = 0; k < 4; ++k) as[k] = rd128(fo[k]);
      ae = rd128(foe);
      SB();
#pragma unroll
      for (int k = 0; k < 4; ++k) G0 = MFMA32(as[k], w0[7 + k], G0);
      G0 = MFMA32(ae, w0[6], G0);
      V0 = MFMA32(as[0], w0[11], zero16);
      SB();
      glds16x(x, sx[0], cx0, ring0);
      glds16x(x, sx[1], cx1, ring0 + 1024);
      V0 = MFMA32(as[1], w0[12], V0);
      SB();
      glds16x(x, sx[2], cx0, ring0 + 2048);
      tq[0] = __builtin_amdgcn_exp2f(G0[0]);
      tq[1] = __builtin_amdgcn_exp2f(G0[1]);
      V0 = MFMA32(as[2], w0[13], V0);
      SB();
      glds16x(x, sx[3], cx1, ring0 + 3072);
      tq[2] = __builtin_amdgcn_exp2f(G0[2]);
      V0 = MFMA32(as[3], w0[14], V0);
      SB();
      glds16e(ES, re, ce, ring0 + 4096);
      rq[0] = __builtin_amdgcn_rcpf(add1(tq[0]));
      rq[1] = __builtin_amdgcn_rcpf(add1(tq[1]));
      SB();
    }
    unsigned &lS4 = aLS4, &lS1 = aLS1, &lX = aLX, &lE = aLE;
    int adv = 0;
    auto body = [&](const int par) {
      const unsigned lo = par * 128;
      const unsigned ro = (1 - par) * ringd;
      {
        u32x4 ss;
        unsigned sl;
#pragma unroll
        for (int m = 0; m < 15; ++m) {
          if (m == 0) Y[1] = MFMA32(__builtin_bit_cast(bf16x8, pd[0]), S[0], Y[1]);
          else if (m == 1) Y[3] = MFMA32(__builtin_bit_cast(bf16x8, pn[0]), S[0], Y[3]);
          else if (m == 2) Y[1] = MFMA32(__builtin_bit_cast(bf16x8, pd[1]), S[1], Y[1]);
          else if (m == 3) Y[3] = MFMA32(__builtin_bit_cast(bf16x8, pn[1]), S[1], Y[3]);
          else if (m == 4) G1 = MFMA32(St[0], XAf[0][1], zero16);
          else if (m == 5) G1 = MFMA32(St[1], XAf[1][1], G1);
          else if (m == 6) G1 = MFMA32(ae, wf(m, 1), G1);
          else if (m < 11) G1 = MFMA32(as[m - 7], wf(m, 1), G1);
          else if (m == 11) V1 = MFMA32(as[0], wf(m, 1), zero16);
          else V1 = MFMA32(as[m - 11], wf(m, 1), V1);
          SB();
          if (m == 0) { ss = ld128(lS4 + lo); sl = *(const LAS unsigned short*)(size_t)(lS1 + lo + 128); }
          if (m >= 3 && m < 12 && !((HELD >> (m + 3)) & 1)) b[(m - 3) % 3] = wl[widx(m + 3, 1) * 64];
          if (m == 4) load_S(ss);
          if (m == 5) { St[0] = rd128(sl * TPITCH + cSt); St[1] = rd128(sl * TPITCH + cSt + 16); }
          if (m == 6) { asm volatile("s_waitcnt vmcnt(5)" ::: "memory"); ae = rd128(foe + ro); }
          if (m >= 11) as[m - 11] = rd128(fo[m - 11] + ro);
          ew(m, G0, V0, G1, V1);
          SB();
        }
      }
      {
        u32x4 sx;
        unsigned re;
#pragma unroll
        for (int m = 0; m < 15; ++m) {
          if (m == 0) Y[0] = MFMA32(__builtin_bit_cast(bf16x8, pd[0]), S[0], Y[0]);
          else if (m == 1) Y[2] = MFMA32(__builtin_bit_cast(bf16x8, pn[0]), S[0], Y[2]);
          else if (m == 2) Y[0] = MFMA32(__builtin_bit_cast(bf16x8, pd[1]), S[1], Y[0]);
          else if (m == 3) Y[2] = MFMA32(__builtin_bit_cast(bf16x8, pn[1]), S[1], Y[2]);
          else if (m == 4) G0 = MFMA32(St[0], XAf[0][0], zero16);
          else if (m == 5) G0 = MFMA32(St[1], XAf[1][0], G0);
          else if (m == 6) G0 = MFMA32(ae, wf(m, 0), G0);
          else if (m < 11) G0 = MFMA32(as[m - 7], wf(m, 0), G0);
          else if (m == 11) V0 = MFMA32(as[0], wf(m, 0), zero16);
          else V0 = MFMA32(as[m - 11], wf(m, 0), V0);
          SB();
          if (m == 4) { sx = ld128(lX + lo + 3 * 128); re = *(const LAS unsigned*)(size_t)(lE + lo + 3 * 128); }
          if (m >= 3 && m < 12 && !((HELD >> (m + 3)) & 1)) b[(m - 3) % 3] = wl[widx(m + 3, 0) * 64];
          if (m == 7) glds16x(x, sx[0], cx0, ring0 + ro);
          if (m == 8) glds16x(x, sx[1], cx1, ring0 + ro + 1024);
          if (m == 9) glds16x(x, sx[2], cx0, ring0 + ro + 2048);
          if (m == 10) glds16x(x, sx[3], cx1, ring0 + ro + 3072);
          if (m == 11) glds16e(ES, re, ce, ring0 + ro + 4096);
          ew(m, G1, V1, G0, V0);
          SB();
        }
      }
    };
    for (int t = 0; t < ntiles; t += 2) {
      asm volatile("s_cmp_lt_u32 %0, 4\n\ts_cbranch_scc1 1f\n\ts_setprio 1\n1:" :: "s"(wave) : "scc");
      body(0);
      if (t + 1 >= ntiles) break;
      __builtin_amdgcn_s_setprio(0);
      body(1);
      lS4 += 256; lS1 += 256; lX += 256; lE += 256;
      asm volatile("" : "+v"(lS4), "+v"(lS1), "+v"(lX), "+v"(lE));
      adv += 256;
    }
    if (__builtin_expect(gs >= GEND, 1)) break;
    drain();
    lS4 -= adv; lS1 -= adv; lX -= adv; lE -= adv;
    redo = true;
  }

  {
    int lane;
    asm volatile("v_mbcnt_lo_u32_b32 %0, -1, 0\n\tv_mbcnt_hi_u32_b32 %0, -1, %0" : "=v"(lane));
    const int h = lane >> 5;
    const unsigned tu = lds0 + OFF_W + 18 * 1024 + h * 4096 + ((lane >> 2) & 3) * 128 + (lane & 1) * 64 +
                        ((lane >> 4) & 1) * 16 + ((lane >> 1) & 1) * 8;
    bf16x8 wu[8];
#pragma unroll
    for (int k = 0; k < 8; ++k) wu[k] = rawfrag(tu + (k & 3) * 1024 + (k >> 2) * 32);
    SB();
    drain();
    f32x16 X[2], U[2];
#pragma unroll
    for (int i = 0; i < 16; ++i) {
      const unsigned xv = __builtin_bit_cast(u32x4, as[i >> 3])[(i >> 1) & 3];
      X[0][i] = __uint_as_float(i & 1 ? xv & 0xffff0000u : xv << 16);
    }
    SB();
    asm volatile("s_nop 0\n\tv_mfma_f32_32x32x16_bf16 %0, %1, %2, %3" : "=&v"(U[0]) : "v"(wu[0]), "v"(as[0]), "v"(X[0]));
#pragma unroll
    for (int k = 1; k < 4; ++k) U[0] = MFMA32(wu[k], as[k], U[0]);
#pragma unroll
    for (int i = 0; i < 16; ++i) {
      const unsigned xv = __builtin_bit_cast(u32x4, as[2 + (i >> 3)])[(i >> 1) & 3];
      X[1][i] = __uint_as_float(i & 1 ? xv & 0xffff0000u : xv << 16);
    }
    const unsigned nd = nodeof(cg + (lane & 31));
    const bool live = (nd * Mw + Cw) >> 16 == cg + (lane & 31);
    const unsigned lm = (unsigned)__ballot(live);
    const int nodes = __builtin_popcount(lm);
    const unsigned n0 = __builtin_amdgcn_readlane(nd, __builtin_ctz(lm | 0x80000000u));
    const int r = live ? nd - n0 : 31;
    SB();
    asm volatile("s_nop 0\n\tv_mfma_f32_32x32x16_bf16 %0, %1, %2, %3" : "=&v"(U[1]) : "v"(wu[4]), "v"(as[0]), "v"(X[1]));
#pragma unroll
    for (int k = 1; k < 4; ++k) U[1] = MFMA32(wu[4 + k], as[k], U[1]);
#pragma unroll
    for (int i = 0; i < 16; ++i) Y[0][i] = __builtin_amdgcn_rcpf(Y[0][i]);
    SB();
#pragma unroll
    for (int ct = 0; ct < 2; ++ct) {
#pragma unroll
      for (int q = 0; q < 2; ++q) {
        u32x4 ov;
#pragma unroll
        for (int j = 0; j < 4; ++j) {
          const int i = 8 * q + 2 * j;
          if (ct == 0) { Y[1][i] = __builtin_amdgcn_rcpf(Y[1][i]); Y[1][i + 1] = __builtin_amdgcn_rcpf(Y[1][i + 1]); }
          const f32x2 z = __builtin_elementwise_fma(f32x2{Y[2 + ct][i], Y[2 + ct][i + 1]}, f32x2{Y[ct][i], Y[ct][i + 1]},
                                                    f32x2{U[ct][i], U[ct][i + 1]});
          ov[j] = pack_bf16(maxf(z[0], X[ct][i]), maxf(z[1], X[ct][i + 1]));
        }
        *(LAS u32x4*)(size_t)(la + r * 128 + (((4 * h + 2 * ct + q) ^ (r & 7)) * 16)) = ov;
      }
    }
    u32x4 ov[4];
#pragma unroll
    for (int q = 0; q < 4; ++q)
      ov[q] = *(const LAS u32x4*)(size_t)(la + (8 * q + (lane >> 3)) * 128 + (((lane & 7) ^ (lane >> 3)) * 16));
    asm volatile("v_cmpx_lt_i32 vcc, 0, %6\n\t"
                 "global_store_dwordx4 %0, %1, %5 sc1\n\t"
                 "v_cmpx_lt_i32 vcc, 8, %6\n\t"
                 "global_store_dwordx4 %0, %2, %5 offset:1024 sc1\n\t"
                 "v_cmpx_lt_i32 vcc, 16, %6\n\t"
                 "global_store_dwordx4 %0, %3, %5 offset:2048 sc1\n\t"
                 "v_cmpx_lt_i32 vcc, 24, %6\n\t"
                 "global_store_dwordx4 %0, %4, %5 offset:3072 sc1\n\t"
                 "s_mov_b64 exec, -1"
                 :: "v"((unsigned)n0 * 128u + lane * 16u), "v"(ov[0]), "v"(ov[1]), "v"(ov[2]), "v"(ov[3]), "s"(out),
                    "v"(nodes - (lane >> 3))
                 : "memory", "vcc");
  }
  if (bk == 0 && wave == 7) asm volatile("global_store_dwordx2 %0, %1, %2 sc1" :: "v"(0u), "v"(ep), "s"(FL) : "memory");
  asm volatile("s_waitcnt vmcnt(0)" ::: "memory");
}

extern "C" void kernel_launch(void* const* d_in, const int* in_sizes, int n_in,
                              void* d_out, int out_size, void* d_ws, size_t ws_size,
                              hipStream_t stream) {
  const unsigned short* x = (const unsigned short*)d_in[0];
  const u32x4* eattr = (const u32x4*)d_in[1];
  const int* src = (const int*)d_in[2];
  const int* dst = (const int*)d_in[3];
  const unsigned short* wa = (const unsigned short*)d_in[4];
  const unsigned short* wb = (const unsigned short*)d_in[5];
  const unsigned short* wc = (const unsigned short*)d_in[6];
  const unsigned short* wu = (const unsigned short*)d_in[7];
  const unsigned short* wv = (const unsigned short*)d_in[8];
  unsigned short* out = (unsigned short*)d_out;
  const int N = in_sizes[0] / 64;
  const int E = in_sizes[2];
  const int chunk = (((E + NCH - 1) / NCH) + 3) & ~3;

  const bool skew = N > 4096 && N < 60000;
  const unsigned M = N > 256 ? (unsigned)(4294967295.0 / ((double)N * (skew ? 1.0 + SKEWF : 1.0))) : 0xffffffu;
  const unsigned N1 = (0x7fffffffu / M) + 1u;
  const unsigned M2 = skew ? (0xffffffffu - N1 * M) / ((unsigned)N - N1) : M;

  auto fb = [](double v) { return __builtin_bit_cast(unsigned, (float)v); };
  char* ws = (char*)d_ws;
  size_t off = 0;
  off = 1 << 20;
  unsigned long long* FL = (unsigned long long*)(ws + off);   off += 1 << 20;
  u32x4* ENT = (u32x4*)(ws + off);                       off += (size_t)2 * NBK * IBOX;
  unsigned long long* TS = (unsigned long long*)(ws + off);

  k_main<<<NBK, 512, 0, stream>>>(x, eattr, src, dst, wa, wb, wc, wu, wv, ENT, TS, FL, out, N, E, chunk, M, N1,
                                  M2 - M, fb(65536.0 / M), fb(65536.0 / M2), fb(32768.0 / M + 0.5),
                                  fb((double)N1 + (32768.0 - (double)N1 * M) / M2 + 0.5)
     );
}
```

```cpp
#include <hip/hip_runtime.h>
#include <hip/hip_bf16.h>
#include <stdint.h>

using bf16x8 = __attribute__((ext_vector_type(8))) short;
using s16x4 = __attribute__((ext_vector_type(4))) short;
using f32x16 = __attribute__((ext_vector_type(16))) float;
using f32x2 = __attribute__((ext_vector_type(2))) float;
using u32x4 = __attribute__((ext_vector_type(4))) unsigned;
using u32x16 = __attribute__((ext_vector_type(16))) unsigned;
using u32x2 = __attribute__((ext_vector_type(2))) unsigned;
using f16x2 = __attribute__((ext_vector_type(2))) _Float16;

#define MFMA32(a, b, c) __builtin_amdgcn_mfma_f32_32x32x16_bf16((a), (b), (c), 0, 0, 0)
#define RA 14
#define VK 4
#define LAS __attribute__((address_space(3)))
#define RANKOF(w) ((w) < 4 ? 7 - (w) : (w) - 4)
#define G1W 4
#define CPF 32
#define CPFB 19072
#define SKEWF 0.015
#define SB() __builtin_amdgcn_sched_barrier(0)

constexpr int NCH = 256;
constexpr int NBK = 256;
constexpr int NFRAG = 39;
constexpr float NLOG2E = -1.44269504088896340736f;
constexpr float VSC = 1.0f / (1 << VK);
constexpr unsigned TPITCH = 80;

__device__ __forceinline__ unsigned pack_bf16(float lo, float hi) {
  typedef __attribute__((ext_vector_type(2))) float f2;
  typedef __attribute__((ext_vector_type(2))) __bf16 b2;
  f2 v = {lo, hi};
  b2 r = __builtin_convertvector(v, b2);
  return __builtin_bit_cast(unsigned, r);
}

__device__ __forceinline__ unsigned pack_f16(float lo, float hi) {
  unsigned r;
  asm("v_cvt_pk_f16_f32 %0, %1, %2" : "=v"(r) : "v"(lo), "v"(hi));
  return r;
}

__device__ __forceinline__ void glds16(const void* sbase, unsigned voff, unsigned lds_dst) {
  asm volatile(
      "s_mov_b32 m0, %2\n\t"
      "s_nop 0\n\t"
      "global_load_lds_dwordx4 %0, %1"
      :: "v"(voff), "s"(sbase), "s"(lds_dst) : "memory", "m0");
}
__device__ __forceinline__ void glds16x(const void* sbase, unsigned a, unsigned c, unsigned lds_dst) {
  unsigned tmp;
  asm volatile(
      "s_mov_b32 m0, %4\n\t"
      "v_mad_u32_u16 %0, %1, %5, %2\n\t"
      "global_load_lds_dwordx4 %0, %3"
      : "=&v"(tmp) : "v"(a), "v"(c), "s"(sbase), "s"(lds_dst), "s"(128u) : "memory", "m0");
}
__device__ __forceinline__ void glds16e(const void* sbase, unsigned a, unsigned c, unsigned lds_dst) {
  unsigned tmp;
  asm volatile(
      "s_mov_b32 m0, %4\n\t"
      "v_add_u32 %0, %1, %2\n\t"
      "global_load_lds_dwordx4 %0, %3"
      : "=&v"(tmp) : "v"(a), "v"(c), "s"(sbase), "s"(lds_dst) : "memory", "m0");
}
__device__ __forceinline__ void gstore128wt(void* sbase, unsigned voff, u32x4 v) {
  asm volatile("global_store_dwordx4 %0, %1, %2 sc1\n\ts_nop 1" :: "v"(voff), "v"(v), "s"(sbase) : "memory");
}

constexpr int IBOX = 32768;
constexpr int IEXT = 256 * IBOX;
constexpr int CPIT = 208;
constexpr unsigned RINC = 87382;
constexpr int KP_HDR = 0;
constexpr int KP_ENT = KP_HDR + NBK * 4;
constexpr int KP_POS = KP_ENT + NBK * CPIT;
constexpr int KP_LDS = KP_POS + NBK * 4;
constexpr int NWL = 26;
constexpr int NBLOB = 31;

__device__ __forceinline__ unsigned mul24s(unsigned a, unsigned b) {
  unsigned r;
  asm("v_mul_u32_u24 %0, %1, %2" : "=v"(r) : "s"(b), "v"(a));
  return r;
}
__device__ __forceinline__ unsigned byte3x(unsigned a, unsigned b) {
  unsigned r;
  asm("v_mul_u32_u24_sdwa %0, %1, %2 dst_sel:DWORD dst_unused:UNUSED_PAD src0_sel:DWORD src1_sel:BYTE_3" : "=v"(r) : "s"(b), "v"(a));
  return r;
}
constexpr int RING_SLOT = 5 * 1024;
constexpr int LCAP = 512;
constexpr int LARR = 2560;
constexpr int LIST_BYTES = 2 * LARR;
constexpr int OFF_CNT = 0;
constexpr int SUBL = LARR / 4;
constexpr int OFF_XF = 256;
constexpr int OFF_W = 512;
constexpr int OFF_T1 = OFF_W + NWL * 1024;
constexpr int OFF_T2 = OFF_T1 + 32 * TPITCH;
constexpr int OFF_R0 = OFF_W + NFRAG * 1024;
constexpr int OFF_R1 = OFF_R0 + 8 * RING_SLOT;
constexpr int OFF_L = OFF_R1 + 8 * RING_SLOT;
constexpr int MAIN_LDS = OFF_L + 8 * LIST_BYTES;
static_assert(MAIN_LDS <= 160 * 1024, "LDS");
static_assert(KP_LDS <= 16 * RING_SLOT, "phase 1 lives in the ring");

__device__ __forceinline__ unsigned ldd(const unsigned* p) {
  return __hip_atomic_load(p, __ATOMIC_RELAXED, __HIP_MEMORY_SCOPE_AGENT);
}
__device__ __forceinline__ unsigned long long ldq(const unsigned long long* p) {
  return __hip_atomic_load(p, __ATOMIC_RELAXED, __HIP_MEMORY_SCOPE_AGENT);
}
__device__ __forceinline__ bf16x8 rawfrag(unsigned a) {
  const s16x4 a0 = __builtin_amdgcn_ds_read_tr16_b64_v4i16((LAS s16x4*)(size_t)a);
  const s16x4 a1 = __builtin_amdgcn_ds_read_tr16_b64_v4i16((LAS s16x4*)(size_t)(a + 512));
  return __builtin_shufflevector(a0, a1, 0, 1, 2, 3, 4, 5, 6, 7);
}
#define MAXN(a, b) __builtin_elementwise_maximum(a, b)

__global__ __launch_bounds__(512, 2) void k_main(const unsigned short* __restrict__ x,
                                                 const u32x4* __restrict__ ES,
                                                 const int* __restrict__ src, const int* __restrict__ dst,
                                                 const unsigned short* __restrict__ wa_g,
                                                 const unsigned short* __restrict__ wb_g,
                                                 const unsigned short* __restrict__ wc_g,
                                                 const unsigned short* __restrict__ wu_g,
                                                 const unsigned short* __restrict__ wv_g,
                                                 u32x4* ENT, unsigned long long* TS, unsigned long long* FL,
                                                 unsigned short* __restrict__ out, int N, int E, int chunk,
                                                 unsigned M, unsigned N1, unsigned DM, unsigned MF0, unsigned MF1,
                                                 unsigned MB0, unsigned MB1
                                                 ) {
  __shared__ __attribute__((aligned(16))) char smem[MAIN_LDS];
  const int tid0 = threadIdx.x;
  const int wave = __builtin_amdgcn_readfirstlane(tid0 >> 6);
  const int bk = blockIdx.x;
  const unsigned lds0 = (unsigned)(size_t)(LAS char*)smem;

  LAS char* sm = (LAS char*)smem + OFF_R0;
  const unsigned sm0 = lds0 + OFF_R0;
  const int tid = tid0, lane = tid & 63, r = lane & 31, h = lane >> 5;
  const unsigned pc = tid & 7, bq = tid >> 3;
  unsigned ia = sm0 + KP_ENT + bq * CPIT + pc * 16;
#pragma unroll
  for (int s = 0; s < 4; ++s) *(LAS u32x4*)(size_t)(ia + s * 64 * CPIT) = u32x4{~0u, ~0u, ~0u, ~0u};
  *(LAS unsigned*)(sm + KP_HDR + (tid & 255) * 4) = 0u;
  int q0 = 4 * tid + 3, q1 = 4 * tid + 1795;
  unsigned a0 = tid * 16, a1 = tid * 16 + 7168, z0 = 0u;
  unsigned long long m7 = __ballot(tid == 448);
  int n7 = wave == 7 ? 0 : 0x7fffffff;
  unsigned f0 = lane * 128u, f1 = lane * 128u + 8192u, f2 = min(lane * 128u + 16384u, CPFB - 512u);
  unsigned long long p7 = __ballot((wave | (bk & -CPF)) == 7);
  asm volatile("" : "+v"(f0), "+v"(f1), "+v"(f2), "+s"(p7));
  asm volatile("" : "+v"(ia), "+v"(q0), "+v"(q1), "+v"(a0), "+v"(a1), "+v"(z0), "+s"(m7), "+s"(n7) :: "memory");
  SB();
  asm volatile("" :: "s"(x), "s"(ES), "s"(src), "s"(dst), "s"(wa_g), "s"(wb_g), "s"(wc_g), "s"(wu_g), "s"(wv_g), "s"(ENT),
               "s"(TS), "s"(FL), "s"(out), "s"(N), "s"(E), "s"(chunk), "s"(M), "s"(N1), "s"(DM), "s"(MF0), "s"(MF1), "s"(MB0), "s"(MB1));
  const int base = bk * chunk;
  const int ecnt = max(min(chunk, E - base), 0);
  unsigned long long on[2];
  on[0] = __ballot(q0 < min(min(chunk, E - base), n7));
  on[1] = __ballot(q1 < min(min(chunk, E - base), n7));
  a0 += base * 4u;
  a1 += base * 4u;
  u32x4 d[2], sv[2];
  u32x2 e0;
  asm volatile("s_mov_b64 exec, %11\n\t"
               "global_load_dwordx4 %0, %5, %8 nt\n\t"
               "s_mov_b64 exec, %12\n\t"
               "global_load_dwordx4 %1, %6, %8 nt\n\t"
               "s_mov_b64 exec, %13\n\t"
               "global_load_dwordx2 %2, %7, %9 sc1\n\t"
               "s_getpc_b64 s[96:97]\n\t"
               "s_mov_b32 m0, %17\n\t"
               "s_mov_b64 exec, %18\n\t"
               "global_load_lds_dword %14, s[96:97]\n\t"
               "global_load_lds_dword %15, s[96:97]\n\t"
               "global_load_lds_dword %16, s[96:97]\n\t"
               "s_mov_b64 exec, %11\n\t"
               "s_waitcnt lgkmcnt(0)\n\t"
               "s_barrier\n\t"
               "global_load_dwordx4 %3, %5, %10 nt\n\t"
               "s_mov_b64 exec, %12\n\t"
               "global_load_dwordx4 %4, %6, %10 nt\n\t"
               "s_mov_b64 exec, -1"
               : "=&{v[240:243]}"(d[0]), "=&{v[244:247]}"(d[1]), "=&{v[238:239]}"(e0), "=&{v[248:251]}"(sv[0]),
                 "=&{v[252:255]}"(sv[1])
               : "v"(a0), "v"(a1), "v"(z0), "s"(dst), "s"(FL), "s"(src), "s"(on[0]), "s"(on[1]), "s"(m7)
                 , "v"(f0), "v"(f1), "v"(f2), "n"(OFF_L + 7 * LIST_BYTES + 4096), "s"(p7)
               : "memory", "m0", "s96", "s97");
  SB();
  const int bu = (bk >> 1) | ((bk & 1) << 7);
  const unsigned Mw = bk & 1 ? M + DM : M, Cw = bk & 1 ? 0u - N1 * DM : 0u;
  unsigned MF = bk & 1 ? MF1 : MF0, MB = bk & 1 ? MB1 : MB0;
  asm volatile("" : "+s"(MF), "+s"(MB));
  char* ib = reinterpret_cast<char*>(ENT) + (size_t)bu * IBOX;
  auto nodeof = [&](unsigned c) { return min((unsigned)__builtin_fmaf((float)c, __uint_as_float(MF), __uint_as_float(MB)), (unsigned)N - 1u); };
  unsigned tup[8], val[8], pm[8];
  u32x4 xg[4];
  const unsigned xn0 = __builtin_amdgcn_readfirstlane(nodeof((bu * 8 + wave) * 32));
  unsigned xd[4];
  auto xprep = [&]() {
#pragma unroll
    for (int i = 0; i < 4; ++i)
      xd[i] = min(xn0 + 8 * i + (lane >> 3), N - 1u) * 128u + ((((lane & 7) ^ (lane >> 4)) * 16) ^ (i & 1) * 64) - i * 1024u;
    asm volatile("" : "+v"(xd[0]), "+v"(xd[1]), "+v"(xd[2]), "+v"(xd[3]));
  };
  auto xdma = [&]() {
    asm volatile("s_mov_b32 m0, %5\n\ts_nop 0\n\t"
                 "global_load_lds_dwordx4 %0, %4\n\t"
                 "global_load_lds_dwordx4 %1, %4 offset:1024\n\t"
                 "global_load_lds_dwordx4 %2, %4 offset:2048\n\t"
                 "global_load_lds_dwordx4 %3, %4 offset:3072"
                 :: "v"(xd[0]), "v"(xd[1]), "v"(xd[2]), "v"(xd[3]), "s"(x), "s"(lds0 + OFF_L + wave * LIST_BYTES)
                 : "memory", "m0");
  };
  auto wload = [&](const unsigned short* w, int f) {
    asm volatile("s_mov_b32 m0, %3\n\ts_nop 0\n\t"
                 "global_load_lds_dwordx4 %0, %2\n\t"
                 "global_load_lds_dwordx4 %0, %2 offset:1024\n\t"
                 "global_load_lds_dwordx4 %0, %2 offset:2048\n\t"
                 "global_load_lds_dwordx4 %0, %2 offset:3072\n\t"
                 "s_addk_i32 m0, 4096\n\ts_nop 0\n\t"
                 "global_load_lds_dwordx4 %1, %2\n\t"
                 "global_load_lds_dwordx4 %1, %2 offset:1024\n\t"
                 "global_load_lds_dwordx4 %1, %2 offset:2048\n\t"
                 "global_load_lds_dwordx4 %1, %2 offset:3072"
                 :: "v"(lane * 16u), "v"(lane * 16u + 4096u), "s"(w), "s"(lds0 + OFF_W + f * 1024) : "memory", "m0", "scc");
  };
  auto wall = [&]() {
    wload(wa_g, NBLOB);
    wload(wb_g, 0);
    wload(wv_g, 8);
    glds16(wc_g, lane * 16, lds0 + OFF_W + 16 * 1024);
    glds16(wc_g + 512, lane * 16, lds0 + OFF_W + 17 * 1024);
  };
  int spin = 1 << 20;
  u32x2 ep = {0u, 0u};
  if (wave == 7) {
    wall();
    *(LAS u32x4*)((LAS char*)smem + OFF_CNT + r * 16) =
        (r & 17) == 0 ? u32x4{lds0 + OFF_L, lds0 + OFF_L + SUBL, lds0 + OFF_L + 2 * SUBL, lds0 + OFF_L + 3 * SUBL}
                      : u32x4{0u, 0u, 0u, 0u};
    {
      const unsigned col = h ? ((r >> 1) & 12u) | ((r << 2) & 16u) | (r & 3u) : r;
      LAS char* tr = (LAS char*)smem + OFF_T1 + lane * TPITCH;
#pragma unroll
      for (int j = 0; j < 4; ++j) *(LAS u32x4*)(tr + j * 16) = u32x4{0u, 0u, 0u, 0u};
      *(LAS unsigned short*)(tr + col * 2) = 0x3C00;
    }
#pragma unroll
    for (int i = 0; i < 8; ++i) { tup[i] = 0u; val[i] = 0u; pm[i] = 0u; }
    asm volatile("s_waitcnt vmcnt(%1)" : "+{v[238:239]}"(e0) : "n"(31) : "memory");
    ep[0] = __builtin_amdgcn_readfirstlane(e0[0]) + 0x9E3779B1u;
    ep[1] = __builtin_amdgcn_readfirstlane(e0[1]) + 0x7F4A7C15u;
    *(LAS u32x2*)((LAS char*)smem + OFF_CNT + 56) = u32x2{ep[0], ep[1] >> 24};
    if (__builtin_expect(ecnt & 3, 0)) {
      const int i = (ecnt & ~3) + lane;
      on[0] = __ballot(i < ecnt);
      if (i < ecnt) {
        pm[0] = (unsigned)dst[base + i] * M + (unsigned)max(dst[base + i] - (int)N1, 0) * DM;
        tup[0] = (unsigned)src[base + i] | (unsigned)i << 16;
        val[0] = __hip_atomic_fetch_add((LAS unsigned*)(sm + KP_HDR) + (pm[0] >> 24), RINC, __ATOMIC_RELAXED, __HIP_MEMORY_SCOPE_WORKGROUP);
        const unsigned u = min(val[0] >> 16, 48u);
        *(LAS unsigned*)(sm + KP_ENT + (pm[0] >> 24) * CPIT + u * 4) = tup[0];
        *(LAS unsigned char*)(sm + KP_ENT + (pm[0] >> 24) * CPIT + ((u * 4) | (u & 3) | 12)) = pm[0] >> 16;
      }
      __builtin_amdgcn_s_waitcnt(0x0F70);
    }
    xprep();
    xdma();
  } else {
    xprep();
    unsigned inc = RINC, t0 = (unsigned)tid << 18;
    asm volatile("s_waitcnt vmcnt(2)" : "+{v[240:243]}"(d[0]), "+{v[244:247]}"(d[1]), "+v"(inc), "+v"(t0) :: "memory");
    unsigned cb[8];
#pragma unroll
    for (int s = 0; s < 2; ++s) {
      unsigned* rr = val + 4 * s;
#pragma unroll
      for (int k = 0; k < 4; ++k) {
        unsigned t;
        asm("v_sub_u32 %0, %1, %2 clamp" : "=v"(t) : "v"(d[s][k]), "s"(N1));
        asm("v_mad_u32_u24 %0, %1, %2, %3" : "=v"(pm[4 * s + k]) : "v"(t), "s"(DM), "v"(mul24s(d[s][k], M)));
      }
#pragma unroll
      for (int k = 0; k < 4; ++k) rr[k] = byte3x(pm[4 * s + k], 4u);
      asm volatile(
          "s_mov_b64 exec, %5\n\t"
          "ds_add_rtn_u32 %0, %0, %4 offset:%6\n\t"
          "ds_add_rtn_u32 %1, %1, %4 offset:%6\n\t"
          "ds_add_rtn_u32 %2, %2, %4 offset:%6\n\t"
          "ds_add_rtn_u32 %3, %3, %4 offset:%6\n\t"
          "s_mov_b64 exec, -1"
          : "+v"(rr[0]), "+v"(rr[1]), "+v"(rr[2]), "+v"(rr[3])
          : "v"(inc), "s"(on[s]), "n"(OFF_R0 + KP_HDR)
          : "memory");
    }
#pragma unroll
    for (int i = 0; i < 8; ++i) cb[i] = byte3x(pm[i], (unsigned)CPIT);
    asm volatile("s_waitcnt vmcnt(0)"
                 : "+{v[248:251]}"(sv[0]), "+{v[252:255]}"(sv[1]), "+v"(cb[0]), "+v"(cb[1]), "+v"(cb[2]), "+v"(cb[3]), "+v"(cb[4]), "+v"(cb[5]),
                   "+v"(cb[6]), "+v"(cb[7])
                 :: "memory");
    xdma();
#pragma unroll
    for (int i = 0; i < 8; ++i) tup[i] = sv[i >> 2][i & 3] + t0 + (((i >> 2) * 1792u + (i & 3)) << 16);
#pragma unroll
    for (int s = 0; s < 2; ++s) {
      asm volatile("s_waitcnt lgkmcnt(%8)"
                   : "+v"(val[4 * s]), "+v"(val[4 * s + 1]), "+v"(val[4 * s + 2]), "+v"(val[4 * s + 3]), "+v"(tup[4 * s]),
                     "+v"(tup[4 * s + 1]), "+v"(tup[4 * s + 2]), "+v"(tup[4 * s + 3])
                   : "n"(s ? 8 : 4) : "memory");
      unsigned pa[4], pb[4];
#pragma unroll
      for (int k = 0; k < 4; ++k) {
        const unsigned u = min(val[4 * s + k], 48u << 16);
        asm("v_mad_u32_u16 %0, %1, 4, %2 op_sel:[1,0,0,0]" : "=v"(pa[k]) : "v"(u), "v"(cb[4 * s + k]));
        pb[k] = pa[k] | ((u >> 16) & 3u) | 12u;
      }
      asm volatile(
          "s_mov_b64 exec, %16\n\t"
          "ds_write_b32 %0, %8 offset:%17\n\t"
          "ds_write_b8_d16_hi %4, %12 offset:%17\n\t"
          "ds_write_b32 %1, %9 offset:%17\n\t"
          "ds_write_b8_d16_hi %5, %13 offset:%17\n\t"
          "ds_write_b32 %2, %10 offset:%17\n\t"
          "ds_write_b8_d16_hi %6, %14 offset:%17\n\t"
          "ds_write_b32 %3, %11 offset:%17\n\t"
          "ds_write_b8_d16_hi %7, %15 offset:%17\n\t"
          "s_mov_b64 exec, -1"
          :: "v"(pa[0]), "v"(pa[1]), "v"(pa[2]), "v"(pa[3]), "v"(pb[0]), "v"(pb[1]), "v"(pb[2]), "v"(pb[3]),
             "v"(tup[4 * s]), "v"(tup[4 * s + 1]), "v"(tup[4 * s + 2]), "v"(tup[4 * s + 3]), "v"(pm[4 * s]),
             "v"(pm[4 * s + 1]), "v"(pm[4 * s + 2]), "v"(pm[4 * s + 3]), "s"(on[s]), "n"(OFF_R0 + KP_ENT)
          : "memory");
    }
  }
  u32x2 tag;
  {
    unsigned ha = sm0 + KP_HDR + bq * 4, hb = sm0 + KP_HDR + lane * 16;
    unsigned go = bq * IBOX + bk * 128 + pc * 16;
    asm volatile("s_waitcnt lgkmcnt(0)\n\ts_barrier" : "+v"(ha), "+v"(hb), "+v"(go) :: "memory");
    tag = *(const LAS u32x2*)((LAS char*)smem + OFF_CNT + 56);
    const u32x4 call = *(const LAS u32x4*)(size_t)hb;
    unsigned cn[4];
    u32x4 v[4];
#pragma unroll
    for (int s = 0; s < 4; ++s) {
      cn[s] = *(const LAS unsigned*)(size_t)(ha + s * 256);
      v[s] = *(const LAS u32x4*)(size_t)(ia + s * 64 * CPIT);
    }
    asm volatile("" : "+v"(v[0]), "+v"(v[1]), "+v"(v[2]), "+v"(v[3]), "+v"(tag));
    if (__builtin_expect(__ballot(max(max(call[0], call[1]), max(call[2], call[3])) > 36u * RINC) != 0ull, 0)) {
      LAS unsigned* ovc = (LAS unsigned*)((LAS char*)smem + OFF_CNT + 24);
      LAS unsigned* pos = (LAS unsigned*)(sm + KP_POS);
      if (tid < NBK) {
        const unsigned u = *(const LAS unsigned*)(sm + KP_HDR + tid * 4) >> 16, n = u - (u >> 2);
        if (n > 36u) {
          const unsigned p = (unsigned)base + __hip_atomic_fetch_add(ovc, n - 36u, __ATOMIC_RELAXED, __HIP_MEMORY_SCOPE_WORKGROUP);
          pos[tid] = p;
          __hip_atomic_store(FL + 512 + tid * NCH + bk, ((unsigned long long)(n - 36u) << 32) | p, __ATOMIC_RELAXED,
                             __HIP_MEMORY_SCOPE_AGENT);
        }
      }
      asm volatile("s_waitcnt lgkmcnt(0)\n\ts_barrier" ::: "memory");
#pragma unroll
      for (int i = 0; i < 8; ++i) {
        const unsigned u = val[i] >> 16, n = u - (u >> 2);
        if (((on[i >> 2] >> lane) & 1ull) && n >= 36u)
          __hip_atomic_store(TS + pos[pm[i] >> 24] + (n - 36u), ((unsigned long long)((pm[i] >> 16) & 0xffu) << 32) | tup[i],
                             __ATOMIC_RELAXED, __HIP_MEMORY_SCOPE_AGENT);
      }
      __builtin_amdgcn_s_waitcnt(0x0F70);
      asm volatile("s_waitcnt vmcnt(0)\n\ts_barrier" ::: "memory");
    }
    auto seal = [&](u32x4& w, bool more) {
      w[3] = __builtin_amdgcn_perm(tag[1], w[3], 0x04020100u);
      unsigned sm_ = w[0] + w[1] + w[2] + w[3] + tag[0];
      sm_ += __builtin_amdgcn_update_dpp(0u, sm_, 0xB1, 0xf, 0xf, false);
      sm_ += __builtin_amdgcn_update_dpp(0u, sm_, 0x4E, 0xf, 0xf, false);
      sm_ += __builtin_amdgcn_update_dpp(0u, sm_, 0x141, 0xf, 0xf, false);
      w[2] = pc == 7u ? w[2] - sm_ + (more ? 1u : 0u) : w[2];
    };
#pragma unroll
    for (int s = 0; s < 4; ++s) seal(v[s], cn[s] > 23u * RINC);
    asm volatile("global_store_dwordx4 %0, %1, %5 sc1\n\t"
                 "global_store_dwordx4 %0, %2, %6 sc1\n\t"
                 "global_store_dwordx4 %0, %3, %7 sc1\n\t"
                 "global_store_dwordx4 %0, %4, %8 sc1\n\t"
                 "s_nop 1"
                 :: "v"(go), "v"(v[0]), "v"(v[1]), "v"(v[2]), "v"(v[3]), "s"(ENT), "s"(ENT + 4 * IBOX), "s"(ENT + 8 * IBOX),
                    "s"(ENT + 12 * IBOX)
                 : "memory");
    if (__builtin_expect(__ballot(max(max(cn[0], cn[1]), max(cn[2], cn[3])) > 23u * RINC) != 0ull, 0)) {
#pragma unroll 1
      for (int s = 0; s < 4; ++s) {
        const unsigned c = *(const LAS unsigned*)(size_t)(ha + s * 256);
        u32x4 w = *(const LAS u32x4*)(size_t)(ia + s * 64 * CPIT + (pc < 4u ? 128 : 0));
        if (pc == 7u) w = u32x4{w[2], ~0u, ~0u, w[3] >> 16};
        else {
#pragma unroll
          for (int j = 0; j < 3; ++j) w[j] = pc < 4u && 32u + 4u * pc + j < (c >> 16) ? w[j] : ~0u;
        }
        seal(w, c > 36u * RINC);
        if (c > 23u * RINC) gstore128wt(ENT, go + s * (64 * IBOX) + IEXT, w);
      }
    }
  }
  u32x4 cv[4];
  unsigned vo[4], eb[4];
  unsigned long long pend[4];
#pragma unroll
  for (int k = 0; k < 4; ++k) {
    vo[k] = wave * 4096u + k * 1024u + lane * 16u;
    eb[k] = (unsigned)((wave * 32 + k * 8 + (lane >> 3)) * chunk) * 32u;
  }
#define POLL0()                                                                                                       \
  asm volatile("global_load_dwordx4 %0, %4, %8 sc1\n\t"                                                               \
               "global_load_dwordx4 %1, %5, %8 sc1\n\t"                                                               \
               "global_load_dwordx4 %2, %6, %8 sc1\n\t"                                                               \
               "global_load_dwordx4 %3, %7, %8 sc1"                                                                   \
               : "=&{v[240:243]}"(cv[0]), "=&{v[244:247]}"(cv[1]), "=&{v[248:251]}"(cv[2]), "=&{v[252:255]}"(cv[3])   \
               : "v"(vo[0]), "v"(vo[1]), "v"(vo[2]), "v"(vo[3]), "s"(ib)                                              \
               : "memory")
  const f32x16 zero16 = {0.f, 0.f, 0.f, 0.f, 0.f, 0.f, 0.f, 0.f, 0.f, 0.f, 0.f, 0.f, 0.f, 0.f, 0.f, 0.f};
  const unsigned to = lds0 + OFF_W + ((lane >> 2) & 3) * 128 + (lane & 1) * 64 + ((lane >> 4) & 1) * 16 + ((lane >> 1) & 1) * 8;
  auto xa = [&]() {
    bf16x8 wa[8];
#pragma unroll
    for (int k = 0; k < 8; ++k) wa[k] = rawfrag(to + (NBLOB + (k >> 1)) * 1024 + h * 4096 + (k & 1) * 32);
    asm volatile("" : "+v"(wa[0]), "+v"(wa[1]), "+v"(wa[2]), "+v"(wa[3]), "+v"(wa[4]), "+v"(wa[5]), "+v"(wa[6]), "+v"(wa[7]),
                      "+{v[160:163]}"(xg[0]), "+{v[164:167]}"(xg[1]), "+{v[168:171]}"(xg[2]), "+{v[172:175]}"(xg[3]));
    f32x16 XA[2] = {zero16, zero16};
#pragma unroll
    for (int k = 0; k < 4; ++k)
#pragma unroll
      for (int ct = 0; ct < 2; ++ct) XA[ct] = MFMA32(__builtin_bit_cast(bf16x8, xg[k]), wa[k * 2 + ct], XA[ct]);
#pragma unroll
    for (int q = 0; q < 4; ++q) {
      u32x4 xp;
#pragma unroll
      for (int j = 0; j < 4; ++j)
        xp[j] = pack_f16(XA[q & 1][8 * (q >> 1) + 2 * j] * NLOG2E - RA, XA[q & 1][8 * (q >> 1) + 2 * j + 1] * NLOG2E - RA);
      *(LAS u32x4*)((LAS char*)smem + OFF_R1 + wave * RING_SLOT + q * 1024 + lane * 16) = xp;
    }
  };
  {
    auto prep = [&](unsigned f, unsigned hs, float scale) {
      u32x4 w[2];
#pragma unroll
      for (int ct = 0; ct < 2; ++ct) w[ct] = __builtin_bit_cast(u32x4, rawfrag(to + f + h * hs + ct * 32));
      asm volatile("s_waitcnt lgkmcnt(0)" : "+v"(w[0]), "+v"(w[1]) :: "memory");
#pragma unroll
      for (int ct = 0; ct < 2; ++ct) {
#pragma unroll
        for (int j = 0; j < 4; ++j)
          w[ct][j] = pack_bf16(__uint_as_float(w[ct][j] << 16) * scale, __uint_as_float(w[ct][j] & 0xffff0000u) * scale);
        *(LAS u32x4*)((LAS char*)smem + OFF_W + f + ct * hs + lane * 16) = w[ct];
      }
    };
    {
      const unsigned xr = min(nodeof((bu * 8 + wave) * 32 + r) - xn0, 31u);
      unsigned xa_ = lds0 + OFF_L + wave * LIST_BYTES + xr * 128 + (((4 * h) ^ ((xr >> 1) & 7)) * 16);
      asm volatile("s_waitcnt vmcnt(4)" : "+v"(xa_) :: "memory");
#pragma unroll
      for (int t = 0; t < 4; ++t) xg[t] = *(const LAS u32x4*)(size_t)(xa_ ^ (t * 16));
      asm volatile("" : "+v"(xg[0]), "+v"(xg[1]), "+v"(xg[2]), "+v"(xg[3]) :: "memory");
      *(LAS unsigned*)((LAS char*)smem + OFF_L + wave * LIST_BYTES + h * LARR + r * 4) = ~0u;
    }
    asm volatile("s_waitcnt vmcnt(4) lgkmcnt(0)\n\ts_barrier"
                 : "+v"(vo[0]), "+v"(vo[1]), "+v"(vo[2]), "+v"(vo[3]), "+v"(eb[0]), "+v"(eb[1]), "+v"(eb[2]), "+v"(eb[3])
                 :: "memory");
    xa();
    {
      unsigned f = (wave + (wave & 4)) * 1024, hs = 4096;
      const float scale = wave < 4 ? NLOG2E : VSC;
#pragma unroll 1
      while (true) {
        asm volatile("" : "+s"(f), "+s"(hs));
        prep(f, hs, scale);
        if (f != 0u) break;
        f = 16 * 1024;
        hs = 1024;
      }
    }
    POLL0();
    glds16(wu_g + wave * 512, lane * 16, lds0 + OFF_W + (18 + wave) * 1024);
  }
  const unsigned l0 = lds0 + OFF_L;
  unsigned aLX = l0 + (lane >> 3) * 16;
  unsigned aLE = l0 + LARR + ((((lane >> 1) & 7) << 2) | (lane >> 4)) * 4;
  unsigned aLS4 = l0 + (4 * h + ((lane >> 2) & 3)) * 16;
  unsigned aLS1 = l0 + (((r & 7) << 2) | (r >> 3)) * 4 + 2;
  unsigned cx0 = (unsigned)(((lane & 7) ^ (lane >> 4)) * 16);
  unsigned cx1 = cx0 ^ 64u;
  unsigned ce = (unsigned)((((lane & 1) ^ ((lane >> 4) & 1))) * 16);
  const unsigned ringd = 8 * RING_SLOT;
  unsigned cS = lds0 + OFF_T1 + (((lane >> 4) & 1) * 16 + (lane & 3) * 4) * 2;
  unsigned cSt = lds0 + OFF_T2 + h * 32;
  unsigned fo[4];
#pragma unroll
  for (int t = 0; t < 4; ++t) fo[t] = lds0 + OFF_R0 + (unsigned)(r * 128 + (((4 * h + t) ^ ((r >> 1) & 7)) * 16));
  unsigned foe = lds0 + OFF_R0 + (unsigned)(4096 + r * 32 + ((h ^ ((r >> 3) & 1)) * 16));
  f32x16 Y[4];
#pragma unroll
  for (int q = 0; q < 4; ++q)
#pragma unroll
    for (int i = 0; i < 16; i += 2) {
      f32x2 y;
      if (q < 2) asm volatile("v_mov_b64 %0, %1" : "=v"(y) : "s"(0x358637bd358637bdull + (RA * 0x0080000000800000ull)));
      else asm volatile("v_mov_b64 %0, 0" : "=v"(y));
      Y[q][i] = y[0];
      Y[q][i + 1] = y[1];
    }
  constexpr unsigned cu = RA == 14 ? 0x04000400u : 0x02000200u;
  unsigned rid = lane & 7;
  asm volatile("" : "+v"(aLX), "+v"(aLE), "+v"(aLS4), "+v"(aLS1), "+v"(cx0), "+v"(cx1), "+v"(ce), "+v"(cS), "+v"(cSt),
                    "+v"(fo[0]), "+v"(fo[1]), "+v"(fo[2]), "+v"(fo[3]), "+v"(foe), "+v"(rid));
  asm volatile("" : "+{v[0:15]}"(Y[0]), "+{v[16:31]}"(Y[1]), "+{v[32:47]}"(Y[2]), "+{v[48:63]}"(Y[3]));
  auto ld128 = [&](unsigned a) { return *(const LAS u32x4*)(size_t)a; };
  auto gather5 = [&](const u32x4 sx, unsigned re, unsigned dstb) {
    glds16e(ES, re, ce, dstb + 4096);
    glds16x(x, sx[0], cx0, dstb);
    glds16x(x, sx[1], cx1, dstb + 1024);
    glds16x(x, sx[2], cx0, dstb + 2048);
    glds16x(x, sx[3], cx1, dstb + 3072);
  };
  const unsigned sq = min(4u * M, 0xffffffu);
  unsigned early, just;
  asm volatile(
        "s_mov_b32 s96, 0x800000\n\t"
        "v_mov_b32 v181, %18\n\t"
        "v_mov_b32 v180, 160\n\t"
        "v_mov_b32 v176, 0xe0\n\t"
        "v_mov_b32 v177, 0x0c040100\n\t"
        "v_mov_b32 v178, 0x0c050100\n\t"
        "v_mov_b32 v179, 0x0c060100\n\t"
        "s_mov_b32 s52, 0x7f7f7f7f\n\t"
        "s_mov_b32 s53, 0x7f7f7f7f\n\t"
        "s_mov_b64 s[54:55], 0\n\t"
        "s_mov_b32 %12, 0\n\t"
        "v_mov_b32 v191, 4\n\t"
        "s_waitcnt vmcnt(%30)\n\t"
        "v_add3_u32 v228, v240, v241, v242\n\t"
        "v_add3_u32 v231, v244, v245, v246\n\t"
        "v_add3_u32 v228, v228, v243, %19\n\t"
        "v_add3_u32 v231, v231, v247, %19\n\t"
        "v_cmp_ne_u32_sdwa s[72:73], v243, %20 src0_sel:BYTE_3 src1_sel:DWORD\n\t"
        "v_add_u32_dpp v228, v228, v228 quad_perm:[1,0,3,2] row_mask:0xf bank_mask:0xf\n\t"
        "v_add_u32_dpp v231, v231, v231 quad_perm:[1,0,3,2] row_mask:0xf bank_mask:0xf\n\t"
        "v_cmp_ne_u32_sdwa s[78:79], v247, %20 src0_sel:BYTE_3 src1_sel:DWORD\n\t"
        "v_add_u32_dpp v228, v228, v228 quad_perm:[2,3,0,1] row_mask:0xf bank_mask:0xf\n\t"
        "v_add_u32_dpp v231, v231, v231 quad_perm:[2,3,0,1] row_mask:0xf bank_mask:0xf\n\t"
        "s_nop 0\n\t"
        "v_add_u32_dpp v228, v228, v228 row_half_mirror row_mask:0xf bank_mask:0xf\n\t"
        "v_add_u32_dpp v231, v231, v231 row_half_mirror row_mask:0xf bank_mask:0xf\n\t"
        "v_cmp_gt_u32 s[64:65], 2, v228\n\t"
        "v_cmp_eq_u32 s[56:57], 1, v228\n\t"
        "v_cmp_gt_u32 s[66:67], 2, v231\n\t"
        "v_cmp_eq_u32 s[58:59], 1, v231\n\t"
        "s_and_b64 s[72:73], s[72:73], s[64:65]\n\t"
        "s_cselect_b64 s[64:65], 0, s[64:65]\n\t"
        "s_and_b64 s[56:57], s[56:57], s[64:65]\n\t"
        "s_not_b64 %0, s[64:65]\n\t"
        "s_or_b64 s[54:55], s[54:55], s[56:57]\n\t"
        "s_or_b64 s[54:55], s[54:55], %0\n\t"
        "s_and_b64 s[78:79], s[78:79], s[66:67]\n\t"
        "s_cselect_b64 s[66:67], 0, s[66:67]\n\t"
        "s_and_b64 s[58:59], s[58:59], s[66:67]\n\t"
        "s_not_b64 %1, s[66:67]\n\t"
        "s_or_b64 s[54:55], s[54:55], s[58:59]\n\t"
        "s_or_b64 s[54:55], s[54:55], %1\n\t"
        "s_mov_b64 exec, s[64:65]\n\t"
        "v_cmpx_ne_u32 s[72:73], -1, v240\n\t"
        "v_mul_hi_u32_u24_sdwa v204, v240, %21 dst_sel:DWORD dst_unused:UNUSED_PAD src0_sel:WORD_0 src1_sel:DWORD\n\t"
        "v_and_b32_sdwa v192, v243, v176 dst_sel:DWORD dst_unused:UNUSED_PAD src0_sel:BYTE_0 src1_sel:DWORD\n\t"
        "v_lshl_add_u32 v204, v204, 2, v192\n\t"
        "ds_add_rtn_u32 v204, v204, v191\n\t"
        "v_perm_b32 v216, v243, v240, v177\n\t"
        "v_mad_u32_u16 v228, v240, 32, %13 op_sel:[1,0,0,0]\n\t"
        "v_cmpx_ne_u32 s[74:75], -1, v241\n\t"
        "v_mul_hi_u32_u24_sdwa v205, v241, %21 dst_sel:DWORD dst_unused:UNUSED_PAD src0_sel:WORD_0 src1_sel:DWORD\n\t"
        "v_and_b32_sdwa v193, v243, v176 dst_sel:DWORD dst_unused:UNUSED_PAD src0_sel:BYTE_1 src1_sel:DWORD\n\t"
        "v_lshl_add_u32 v205, v205, 2, v193\n\t"
        "ds_add_rtn_u32 v205, v205, v191\n\t"
        "v_perm_b32 v217, v243, v241, v178\n\t"
        "v_mad_u32_u16 v229, v241, 32, %13 op_sel:[1,0,0,0]\n\t"
        "s_and_b64 exec, exec, s[52:53]\n\t"
        "v_cmpx_ne_u32 s[76:77], -1, v242\n\t"
        "v_mul_hi_u32_u24_sdwa v206, v242, %21 dst_sel:DWORD dst_unused:UNUSED_PAD src0_sel:WORD_0 src1_sel:DWORD\n\t"
        "v_and_b32_sdwa v194, v243, v176 dst_sel:DWORD dst_unused:UNUSED_PAD src0_sel:BYTE_2 src1_sel:DWORD\n\t"
        "v_lshl_add_u32 v206, v206, 2, v194\n\t"
        "ds_add_rtn_u32 v206, v206, v191\n\t"
        "v_perm_b32 v218, v243, v242, v179\n\t"
        "v_mad_u32_u16 v230, v242, 32, %13 op_sel:[1,0,0,0]\n\t"
        "s_mov_b64 exec, s[66:67]\n\t"
        "v_cmpx_ne_u32 s[78:79], -1, v244\n\t"
        "v_mul_hi_u32_u24_sdwa v207, v244, %21 dst_sel:DWORD dst_unused:UNUSED_PAD src0_sel:WORD_0 src1_sel:DWORD\n\t"
        "v_and_b32_sdwa v195, v247, v176 dst_sel:DWORD dst_unused:UNUSED_PAD src0_sel:BYTE_0 src1_sel:DWORD\n\t"
        "v_lshl_add_u32 v207, v207, 2, v195\n\t"
        "ds_add_rtn_u32 v207, v207, v191\n\t"
        "v_perm_b32 v219, v247, v244, v177\n\t"
        "v_mad_u32_u16 v231, v244, 32, %14 op_sel:[1,0,0,0]\n\t"
        "v_cmpx_ne_u32 s[80:81], -1, v245\n\t"
        "v_mul_hi_u32_u24_sdwa v208, v245, %21 dst_sel:DWORD dst_unused:UNUSED_PAD src0_sel:WORD_0 src1_sel:DWORD\n\t"
        "v_and_b32_sdwa v196, v247, v176 dst_sel:DWORD dst_unused:UNUSED_PAD src0_sel:BYTE_1 src1_sel:DWORD\n\t"
        "v_lshl_add_u32 v208, v208, 2, v196\n\t"
        "ds_add_rtn_u32 v208, v208, v191\n\t"
        "v_perm_b32 v220, v247, v245, v178\n\t"
        "v_mad_u32_u16 v232, v245, 32, %14 op_sel:[1,0,0,0]\n\t"
        "s_and_b64 exec, exec, s[52:53]\n\t"
        "v_cmpx_ne_u32 s[82:83], -1, v246\n\t"
        "v_mul_hi_u32_u24_sdwa v209, v246, %21 dst_sel:DWORD dst_unused:UNUSED_PAD src0_sel:WORD_0 src1_sel:DWORD\n\t"
        "v_and_b32_sdwa v197, v247, v176 dst_sel:DWORD dst_unused:UNUSED_PAD src0_sel:BYTE_2 src1_sel:DWORD\n\t"
        "v_lshl_add_u32 v209, v209, 2, v197\n\t"
        "ds_add_rtn_u32 v209, v209, v191\n\t"
        "v_perm_b32 v221, v247, v246, v179\n\t"
        "v_mad_u32_u16 v233, v246, 32, %14 op_sel:[1,0,0,0]\n\t"
        "s_mov_b64 exec, -1\n\t"
        "s_waitcnt vmcnt(%31)\n\t"
        "v_add3_u32 v234, v248, v249, v250\n\t"
        "v_add3_u32 v237, v252, v253, v254\n\t"
        "v_add3_u32 v234, v234, v251, %19\n\t"
        "v_add3_u32 v237, v237, v255, %19\n\t"
        "v_cmp_ne_u32_sdwa s[84:85], v251, %20 src0_sel:BYTE_3 src1_sel:DWORD\n\t"
        "v_add_u32_dpp v234, v234, v234 quad_perm:[1,0,3,2] row_mask:0xf bank_mask:0xf\n\t"
        "v_add_u32_dpp v237, v237, v237 quad_perm:[1,0,3,2] row_mask:0xf bank_mask:0xf\n\t"
        "v_cmp_ne_u32_sdwa s[90:91], v255, %20 src0_sel:BYTE_3 src1_sel:DWORD\n\t"
        "v_add_u32_dpp v234, v234, v234 quad_perm:[2,3,0,1] row_mask:0xf bank_mask:0xf\n\t"
        "v_add_u32_dpp v237, v237, v237 quad_perm:[2,3,0,1] row_mask:0xf bank_mask:0xf\n\t"
        "s_nop 0\n\t"
        "v_add_u32_dpp v234, v234, v234 row_half_mirror row_mask:0xf bank_mask:0xf\n\t"
        "v_add_u32_dpp v237, v237, v237 row_half_mirror row_mask:0xf bank_mask:0xf\n\t"
        "v_cmp_gt_u32 s[68:69], 2, v234\n\t"
        "v_cmp_eq_u32 s[60:61], 1, v234\n\t"
        "v_cmp_gt_u32 s[70:71], 2, v237\n\t"
        "v_cmp_eq_u32 s[62:63], 1, v237\n\t"
        "s_and_b64 s[84:85], s[84:85], s[68:69]\n\t"
        "s_cselect_b64 s[68:69], 0, s[68:69]\n\t"
        "s_and_b64 s[60:61], s[60:61], s[68:69]\n\t"
        "s_not_b64 %2, s[68:69]\n\t"
        "s_or_b64 s[54:55], s[54:55], s[60:61]\n\t"
        "s_or_b64 s[54:55], s[54:55], %2\n\t"
        "s_and_b64 s[90:91], s[90:91], s[70:71]\n\t"
        "s_cselect_b64 s[70:71], 0, s[70:71]\n\t"
        "s_and_b64 s[62:63], s[62:63], s[70:71]\n\t"
        "s_not_b64 %3, s[70:71]\n\t"
        "s_or_b64 s[54:55], s[54:55], s[62:63]\n\t"
        "s_or_b64 s[54:55], s[54:55], %3\n\t"
        "s_mov_b64 exec, s[72:73]\n\t"
        "s_waitcnt lgkmcnt(3)\n\t"
        "v_min_u32 v204, v181, v204\n\t"
        "v_min_u32 v205, v181, v205\n\t"
        "v_min_u32 v206, v181, v206\n\t"
        "v_mad_u32_u24 v204, v192, v180, v204\n\t"
        "v_mad_u32_u24 v205, v193, v180, v205\n\t"
        "v_mad_u32_u24 v206, v194, v180, v206\n\t"
        "ds_write2st64_b32 v204, v216, v228 offset1:10\n\t"
        "s_mov_b64 exec, s[74:75]\n\t"
        "ds_write2st64_b32 v205, v217, v229 offset1:10\n\t"
        "s_mov_b64 exec, s[76:77]\n\t"
        "ds_write2st64_b32 v206, v218, v230 offset1:10\n\t"
        "s_mov_b64 exec, s[68:69]\n\t"
        "v_cmpx_ne_u32 s[84:85], -1, v248\n\t"
        "v_mul_hi_u32_u24_sdwa v210, v248, %21 dst_sel:DWORD dst_unused:UNUSED_PAD src0_sel:WORD_0 src1_sel:DWORD\n\t"
        "v_and_b32_sdwa v198, v251, v176 dst_sel:DWORD dst_unused:UNUSED_PAD src0_sel:BYTE_0 src1_sel:DWORD\n\t"
        "v_lshl_add_u32 v210, v210, 2, v198\n\t"
        "ds_add_rtn_u32 v210, v210, v191\n\t"
        "v_perm_b32 v222, v251, v248, v177\n\t"
        "v_mad_u32_u16 v234, v248, 32, %15 op_sel:[1,0,0,0]\n\t"
        "v_cmpx_ne_u32 s[86:87], -1, v249\n\t"
        "v_mul_hi_u32_u24_sdwa v211, v249, %21 dst_sel:DWORD dst_unused:UNUSED_PAD src0_sel:WORD_0 src1_sel:DWORD\n\t"
        "v_and_b32_sdwa v199, v251, v176 dst_sel:DWORD dst_unused:UNUSED_PAD src0_sel:BYTE_1 src1_sel:DWORD\n\t"
        "v_lshl_add_u32 v211, v211, 2, v199\n\t"
        "ds_add_rtn_u32 v211, v211, v191\n\t"
        "v_perm_b32 v223, v251, v249, v178\n\t"
        "v_mad_u32_u16 v235, v249, 32, %15 op_sel:[1,0,0,0]\n\t"
        "s_and_b64 exec, exec, s[52:53]\n\t"
        "v_cmpx_ne_u32 s[88:89], -1, v250\n\t"
        "v_mul_hi_u32_u24_sdwa v212, v250, %21 dst_sel:DWORD dst_unused:UNUSED_PAD src0_sel:WORD_0 src1_sel:DWORD\n\t"
        "v_and_b32_sdwa v200, v251, v176 dst_sel:DWORD dst_unused:UNUSED_PAD src0_sel:BYTE_2 src1_sel:DWORD\n\t"
        "v_lshl_add_u32 v212, v212, 2, v200\n\t"
        "ds_add_rtn_u32 v212, v212, v191\n\t"
        "v_perm_b32 v224, v251, v250, v179\n\t"
        "v_mad_u32_u16 v236, v250, 32, %15 op_sel:[1,0,0,0]\n\t"
        "s_mov_b64 exec, s[78:79]\n\t"
        "s_waitcnt lgkmcnt(6)\n\t"
        "v_min_u32 v207, v181, v207\n\t"
        "v_min_u32 v208, v181, v208\n\t"
        "v_min_u32 v209, v181, v209\n\t"
        "v_mad_u32_u24 v207, v195, v180, v207\n\t"
        "v_mad_u32_u24 v208, v196, v180, v208\n\t"
        "v_mad_u32_u24 v209, v197, v180, v209\n\t"
        "ds_write2st64_b32 v207, v219, v231 offset1:10\n\t"
        "s_mov_b64 exec, s[80:81]\n\t"
        "ds_write2st64_b32 v208, v220, v232 offset1:10\n\t"
        "s_mov_b64 exec, s[82:83]\n\t"
        "ds_write2st64_b32 v209, v221, v233 offset1:10\n\t"
        "s_mov_b64 exec, -1\n\t"
        "ds_read_b128 v[184:187], %22\n\t"
        "ds_read_b32 v183, %23\n\t"
        "s_mov_b64 exec, s[70:71]\n\t"
        "v_cmpx_ne_u32 s[90:91], -1, v252\n\t"
        "v_mul_hi_u32_u24_sdwa v213, v252, %21 dst_sel:DWORD dst_unused:UNUSED_PAD src0_sel:WORD_0 src1_sel:DWORD\n\t"
        "v_and_b32_sdwa v201, v255, v176 dst_sel:DWORD dst_unused:UNUSED_PAD src0_sel:BYTE_0 src1_sel:DWORD\n\t"
        "v_lshl_add_u32 v213, v213, 2, v201\n\t"
        "ds_add_rtn_u32 v213, v213, v191\n\t"
        "v_perm_b32 v225, v255, v252, v177\n\t"
        "v_mad_u32_u16 v237, v252, 32, %16 op_sel:[1,0,0,0]\n\t"
        "v_cmpx_ne_u32 s[92:93], -1, v253\n\t"
        "v_mul_hi_u32_u24_sdwa v214, v253, %21 dst_sel:DWORD dst_unused:UNUSED_PAD src0_sel:WORD_0 src1_sel:DWORD\n\t"
        "v_and_b32_sdwa v202, v255, v176 dst_sel:DWORD dst_unused:UNUSED_PAD src0_sel:BYTE_1 src1_sel:DWORD\n\t"
        "v_lshl_add_u32 v214, v214, 2, v202\n\t"
        "ds_add_rtn_u32 v214, v214, v191\n\t"
        "v_perm_b32 v226, v255, v253, v178\n\t"
        "v_mad_u32_u16 v238, v253, 32, %16 op_sel:[1,0,0,0]\n\t"
        "s_and_b64 exec, exec, s[52:53]\n\t"
        "v_cmpx_ne_u32 s[94:95], -1, v254\n\t"
        "v_mul_hi_u32_u24_sdwa v215, v254, %21 dst_sel:DWORD dst_unused:UNUSED_PAD src0_sel:WORD_0 src1_sel:DWORD\n\t"
        "v_and_b32_sdwa v203, v255, v176 dst_sel:DWORD dst_unused:UNUSED_PAD src0_sel:BYTE_2 src1_sel:DWORD\n\t"
        "v_lshl_add_u32 v215, v215, 2, v203\n\t"
        "ds_add_rtn_u32 v215, v215, v191\n\t"
        "v_perm_b32 v227, v255, v254, v179\n\t"
        "v_mad_u32_u16 v239, v254, 32, %16 op_sel:[1,0,0,0]\n\t"
        "s_cmp_eq_u64 s[54:55], 0\n\t"
        "s_cbranch_scc1 59f\n\t"
        "s_mov_b64 exec, s[56:57]\n\t"
        "s_cbranch_execz 40f\n\t"
        "v_bfe_u32 v188, %4, 7, 8\n\t"
        "v_mov_b32 v189, 1\n\t"
        "ds_write_b8 v188, v189 offset:256\n\t"
        "v_add_u32 %4, s96, %4\n\t"
        "s_or_b64 %0, %0, exec\n\t"
        "40:\n\t"
        "s_mov_b64 exec, %0\n\t"
        "s_cbranch_execz 50f\n\t"
        "global_load_dwordx4 v[240:243], %4, %17 sc1\n\t"
        "50:\n\t"
        "s_mov_b64 exec, s[58:59]\n\t"
        "s_cbranch_execz 41f\n\t"
        "v_bfe_u32 v188, %5, 7, 8\n\t"
        "v_mov_b32 v189, 1\n\t"
        "ds_write_b8 v188, v189 offset:256\n\t"
        "v_add_u32 %5, s96, %5\n\t"
        "s_or_b64 %1, %1, exec\n\t"
        "41:\n\t"
        "s_mov_b64 exec, %1\n\t"
        "s_cbranch_execz 51f\n\t"
        "global_load_dwordx4 v[244:247], %5, %17 sc1\n\t"
        "51:\n\t"
        "s_mov_b64 exec, s[60:61]\n\t"
        "s_cbranch_execz 42f\n\t"
        "v_bfe_u32 v188, %6, 7, 8\n\t"
        "v_mov_b32 v189, 1\n\t"
        "ds_write_b8 v188, v189 offset:256\n\t"
        "v_add_u32 %6, s96, %6\n\t"
        "s_or_b64 %2, %2, exec\n\t"
        "42:\n\t"
        "s_mov_b64 exec, %2\n\t"
        "s_cbranch_execz 52f\n\t"
        "global_load_dwordx4 v[248:251], %6, %17 sc1\n\t"
        "52:\n\t"
        "s_mov_b64 exec, s[62:63]\n\t"
        "s_cbranch_execz 43f\n\t"
        "v_bfe_u32 v188, %7, 7, 8\n\t"
        "v_mov_b32 v189, 1\n\t"
        "ds_write_b8 v188, v189 offset:256\n\t"
        "v_add_u32 %7, s96, %7\n\t"
        "s_or_b64 %3, %3, exec\n\t"
        "43:\n\t"
        "s_mov_b64 exec, %3\n\t"
        "s_cbranch_execz 53f\n\t"
        "global_load_dwordx4 v[252:255], %7, %17 sc1\n\t"
        "53:\n\t"
        "s_waitcnt lgkmcnt(0)\n\t"
        "59:\n\t"
        "s_mov_b64 exec, s[84:85]\n\t"
        "s_waitcnt lgkmcnt(8)\n\t"
        "v_min_u32 v210, v181, v210\n\t"
        "v_min_u32 v211, v181, v211\n\t"
        "v_min_u32 v212, v181, v212\n\t"
        "v_mad_u32_u24 v210, v198, v180, v210\n\t"
        "v_mad_u32_u24 v211, v199, v180, v211\n\t"
        "v_mad_u32_u24 v212, v200, v180, v212\n\t"
        "ds_write2st64_b32 v210, v222, v234 offset1:10\n\t"
        "s_mov_b64 exec, s[86:87]\n\t"
        "ds_write2st64_b32 v211, v223, v235 offset1:10\n\t"
        "s_mov_b64 exec, s[88:89]\n\t"
        "ds_write2st64_b32 v212, v224, v236 offset1:10\n\t"
        "s_mov_b64 exec, -1\n\t"
        "s_movk_i32 s96, 128\n\t"
        "s_mov_b32 m0, %29\n\t"
        "s_waitcnt lgkmcnt(6)\n\t"
        "v_max3_u32 v182, v184, v185, v186\n\t"
        "v_max3_u32 v182, v182, v187, v183\n\t"
        "v_cmp_eq_u32 vcc, -1, v182\n\t"
        "v_mad_u32_u16 v184, v184, s96, %24\n\t"
        "v_mad_u32_u16 v185, v185, s96, %25\n\t"
        "v_mad_u32_u16 v186, v186, s96, %24\n\t"
        "v_mad_u32_u16 v187, v187, s96, %25\n\t"
        "v_add_u32 v183, v183, %26\n\t"
        "s_cbranch_vccnz 60f\n\t"
        "global_load_lds_dwordx4 v184, %27\n\t"
        "s_addk_i32 m0, 1024\n\t"
        "s_nop 0\n\t"
        "global_load_lds_dwordx4 v185, %27\n\t"
        "s_addk_i32 m0, 1024\n\t"
        "s_nop 0\n\t"
        "global_load_lds_dwordx4 v186, %27\n\t"
        "s_addk_i32 m0, 1024\n\t"
        "s_nop 0\n\t"
        "global_load_lds_dwordx4 v187, %27\n\t"
        "s_addk_i32 m0, 1024\n\t"
        "s_nop 0\n\t"
        "global_load_lds_dwordx4 v183, %28\n\t"
        "s_mov_b32 %12, 1\n\t"
        "60:\n\t"
        "s_mov_b64 exec, s[90:91]\n\t"
        "s_waitcnt lgkmcnt(3)\n\t"
        "v_min_u32 v213, v181, v213\n\t"
        "v_min_u32 v214, v181, v214\n\t"
        "v_min_u32 v215, v181, v215\n\t"
        "v_mad_u32_u24 v213, v201, v180, v213\n\t"
        "v_mad_u32_u24 v214, v202, v180, v214\n\t"
        "v_mad_u32_u24 v215, v203, v180, v215\n\t"
        "ds_write2st64_b32 v213, v225, v237 offset1:10\n\t"
        "s_mov_b64 exec, s[92:93]\n\t"
        "ds_write2st64_b32 v214, v226, v238 offset1:10\n\t"
        "s_mov_b64 exec, s[94:95]\n\t"
        "ds_write2st64_b32 v215, v227, v239 offset1:10\n\t"
        "s_or_b32 s54, s54, s55\n\t"
        "s_or_b32 s54, s54, %12\n\t"
        "s_cbranch_scc1 60f\n\t"
        "s_movk_i32 s54, 12\n\t"
        "61:\n\t"
        "s_sub_u32 s54, s54, 1\n\t"
        "s_cbranch_scc1 60f\n\t"
        "s_mov_b64 exec, -1\n\t"
        "ds_read_b128 v[184:187], %22\n\t"
        "ds_read_b32 v183, %23\n\t"
        "s_mov_b64 exec, -1\n\t"
        "s_movk_i32 s96, 128\n\t"
        "s_mov_b32 m0, %29\n\t"
        "s_waitcnt lgkmcnt(0)\n\t"
        "v_max3_u32 v182, v184, v185, v186\n\t"
        "v_max3_u32 v182, v182, v187, v183\n\t"
        "v_cmp_eq_u32 vcc, -1, v182\n\t"
        "v_mad_u32_u16 v184, v184, s96, %24\n\t"
        "v_mad_u32_u16 v185, v185, s96, %25\n\t"
        "v_mad_u32_u16 v186, v186, s96, %24\n\t"
        "v_mad_u32_u16 v187, v187, s96, %25\n\t"
        "v_add_u32 v183, v183, %26\n\t"
        "s_cbranch_vccnz 61b\n\t"
        "global_load_lds_dwordx4 v184, %27\n\t"
        "s_addk_i32 m0, 1024\n\t"
        "s_nop 0\n\t"
        "global_load_lds_dwordx4 v185, %27\n\t"
        "s_addk_i32 m0, 1024\n\t"
        "s_nop 0\n\t"
        "global_load_lds_dwordx4 v186, %27\n\t"
        "s_addk_i32 m0, 1024\n\t"
        "s_nop 0\n\t"
        "global_load_lds_dwordx4 v187, %27\n\t"
        "s_addk_i32 m0, 1024\n\t"
        "s_nop 0\n\t"
        "global_load_lds_dwordx4 v183, %28\n\t"
        "s_mov_b32 %12, 1\n\t"
        "60:\n\t"
        "s_mov_b64 exec, -1\n\t"
        : "=&s"(pend[0]), "=&s"(pend[1]), "=&s"(pend[2]), "=&s"(pend[3]), "+v"(vo[0]), "+v"(vo[1]), "+v"(vo[2]),
          "+v"(vo[3]), "+{v[240:243]}"(cv[0]), "+{v[244:247]}"(cv[1]), "+{v[248:251]}"(cv[2]), "+{v[252:255]}"(cv[3]),
          "=&s"(early)
        : "v"(eb[0]), "v"(eb[1]), "v"(eb[2]), "v"(eb[3]), "s"(ib), "n"(OFF_L + LARR - 4), "v"(tag[0]), "v"(tag[1]),
          "s"(sq), "v"(aLX + wave * LIST_BYTES), "v"(aLE + wave * LIST_BYTES), "v"(cx0), "v"(cx1), "v"(ce), "s"(x),
          "s"(ES), "s"(lds0 + OFF_R0 + wave * RING_SLOT), "n"(3), "n"(1)
        :
          "memory", "vcc", "scc", "m0", "v176", "v177", "v178", "v179", "v180", "v181", "v182", "v183", "v184",
          "v185", "v186", "v187", "v188", "v189", "v190", "v191", "v192", "v193", "v194", "v195", "v196", "v197",
          "v198", "v199", "v200", "v201", "v202", "v203", "v204", "v205", "v206", "v207", "v208", "v209", "v210",
          "v211", "v212", "v213", "v214", "v215", "v216", "v217", "v218", "v219", "v220", "v221", "v222", "v223",
          "v224", "v225", "v226", "v227", "v228", "v229", "v230", "v231", "v232", "v233", "v234", "v235", "v236",
          "v237", "v238", "v239", "s52", "s53", "s54", "s55", "s56", "s57", "s58", "s59", "s60", "s61", "s62", "s63",
          "s64", "s65", "s66", "s67", "s68", "s69", "s70", "s71", "s72", "s73", "s74", "s75", "s76", "s77", "s78",
          "s79", "s80", "s81", "s82", "s83", "s84", "s85", "s86", "s87", "s88", "s89", "s90", "s91", "s92", "s93",
          "s94", "s95", "s96");
  auto uni = [&]() {
#pragma unroll
    for (int k = 0; k < 4; ++k)
      pend[k] = ((unsigned long long)(unsigned)__builtin_amdgcn_readfirstlane((unsigned)(pend[k] >> 32)) << 32) |
                (unsigned)__builtin_amdgcn_readfirstlane((unsigned)pend[k]);
  };
  uni();
  early = __builtin_amdgcn_readfirstlane(early);
  just = early;
  if (__builtin_expect((pend[0] | pend[1] | pend[2] | pend[3] | (unsigned long long)(early ^ 1u)) != 0ull, 0))
#pragma clang loop unroll(disable)
  while (true) {
    if (!early) {
      const u32x4 sx = ld128(aLX + wave * LIST_BYTES);
      const unsigned re = *(const LAS unsigned*)(size_t)(aLE + wave * LIST_BYTES);
      if (__ballot(max(max(max(sx[0], sx[1]), max(sx[2], sx[3])), re) == ~0u) == 0ull) {
        gather5(sx, re, lds0 + OFF_R0 + wave * RING_SLOT);
        early = 1u;
        just = 1u;
      }
    }
    if ((pend[0] | pend[1] | pend[2] | pend[3]) == 0ull || --spin < 0) break;
    asm volatile(
        "s_mov_b32 s96, 0x800000\n\t"
        "v_mov_b32 v181, %17\n\t"
        "v_mov_b32 v180, 160\n\t"
        "v_mov_b32 v176, 0xe0\n\t"
        "v_mov_b32 v177, 0x0c040100\n\t"
        "v_mov_b32 v178, 0x0c050100\n\t"
        "v_mov_b32 v179, 0x0c060100\n\t"
        "s_mov_b32 s52, 0x7f7f7f7f\n\t"
        "s_mov_b32 s53, 0x7f7f7f7f\n\t"
        "v_mov_b32 v191, 4\n\t"
        "s_waitcnt vmcnt(5)\n\t"
        "s_cmp_lg_u32 %21, 0\n\t"
        "s_cbranch_scc1 9f\n\t"
        "s_waitcnt vmcnt(0)\n\t"
        "9:\n\t"
        "v_add3_u32 v228, v240, v241, v242\n\t"
        "v_add3_u32 v229, v244, v245, v246\n\t"
        "v_add3_u32 v230, v248, v249, v250\n\t"
        "v_add3_u32 v231, v252, v253, v254\n\t"
        "v_add3_u32 v228, v228, v243, %18\n\t"
        "v_add3_u32 v229, v229, v247, %18\n\t"
        "v_add3_u32 v230, v230, v251, %18\n\t"
        "v_add3_u32 v231, v231, v255, %18\n\t"
        "v_add_u32_dpp v228, v228, v228 quad_perm:[1,0,3,2] row_mask:0xf bank_mask:0xf\n\t"
        "v_add_u32_dpp v229, v229, v229 quad_perm:[1,0,3,2] row_mask:0xf bank_mask:0xf\n\t"
        "v_add_u32_dpp v230, v230, v230 quad_perm:[1,0,3,2] row_mask:0xf bank_mask:0xf\n\t"
        "v_add_u32_dpp v231, v231, v231 quad_perm:[1,0,3,2] row_mask:0xf bank_mask:0xf\n\t"
        "v_add_u32_dpp v228, v228, v228 quad_perm:[2,3,0,1] row_mask:0xf bank_mask:0xf\n\t"
        "v_add_u32_dpp v229, v229, v229 quad_perm:[2,3,0,1] row_mask:0xf bank_mask:0xf\n\t"
        "v_add_u32_dpp v230, v230, v230 quad_perm:[2,3,0,1] row_mask:0xf bank_mask:0xf\n\t"
        "v_add_u32_dpp v231, v231, v231 quad_perm:[2,3,0,1] row_mask:0xf bank_mask:0xf\n\t"
        "v_add_u32_dpp v228, v228, v228 row_half_mirror row_mask:0xf bank_mask:0xf\n\t"
        "v_add_u32_dpp v229, v229, v229 row_half_mirror row_mask:0xf bank_mask:0xf\n\t"
        "v_add_u32_dpp v230, v230, v230 row_half_mirror row_mask:0xf bank_mask:0xf\n\t"
        "v_add_u32_dpp v231, v231, v231 row_half_mirror row_mask:0xf bank_mask:0xf\n\t"
        "v_cmp_ne_u32_sdwa s[72:73], v243, %19 src0_sel:BYTE_3 src1_sel:DWORD\n\t"
        "v_cmp_gt_u32 s[64:65], 2, v228\n\t"
        "v_cmp_eq_u32 s[56:57], 1, v228\n\t"
        "v_cmp_ne_u32_sdwa s[74:75], v247, %19 src0_sel:BYTE_3 src1_sel:DWORD\n\t"
        "v_cmp_gt_u32 s[66:67], 2, v229\n\t"
        "v_cmp_eq_u32 s[58:59], 1, v229\n\t"
        "v_cmp_ne_u32_sdwa s[76:77], v251, %19 src0_sel:BYTE_3 src1_sel:DWORD\n\t"
        "v_cmp_gt_u32 s[68:69], 2, v230\n\t"
        "v_cmp_eq_u32 s[60:61], 1, v230\n\t"
        "v_cmp_ne_u32_sdwa s[78:79], v255, %19 src0_sel:BYTE_3 src1_sel:DWORD\n\t"
        "v_cmp_gt_u32 s[70:71], 2, v231\n\t"
        "v_cmp_eq_u32 s[62:63], 1, v231\n\t"
        "s_and_b64 s[64:65], s[64:65], %0\n\t"
        "s_and_b64 s[72:73], s[72:73], s[64:65]\n\t"
        "s_cselect_b64 s[64:65], 0, s[64:65]\n\t"
        "s_and_b64 s[56:57], s[56:57], s[64:65]\n\t"
        "s_andn2_b64 %0, %0, s[64:65]\n\t"
        "s_cbranch_scc0 10f\n\t"
        "s_mov_b64 exec, %0\n\t"
        "global_load_dwordx4 v[240:243], %4, %16 sc1\n\t"
        "10:\n\t"
        "s_and_b64 s[66:67], s[66:67], %1\n\t"
        "s_and_b64 s[74:75], s[74:75], s[66:67]\n\t"
        "s_cselect_b64 s[66:67], 0, s[66:67]\n\t"
        "s_and_b64 s[58:59], s[58:59], s[66:67]\n\t"
        "s_andn2_b64 %1, %1, s[66:67]\n\t"
        "s_cbranch_scc0 11f\n\t"
        "s_mov_b64 exec, %1\n\t"
        "global_load_dwordx4 v[244:247], %5, %16 sc1\n\t"
        "11:\n\t"
        "s_and_b64 s[68:69], s[68:69], %2\n\t"
        "s_and_b64 s[76:77], s[76:77], s[68:69]\n\t"
        "s_cselect_b64 s[68:69], 0, s[68:69]\n\t"
        "s_and_b64 s[60:61], s[60:61], s[68:69]\n\t"
        "s_andn2_b64 %2, %2, s[68:69]\n\t"
        "s_cbranch_scc0 12f\n\t"
        "s_mov_b64 exec, %2\n\t"
        "global_load_dwordx4 v[248:251], %6, %16 sc1\n\t"
        "12:\n\t"
        "s_and_b64 s[70:71], s[70:71], %3\n\t"
        "s_and_b64 s[78:79], s[78:79], s[70:71]\n\t"
        "s_cselect_b64 s[70:71], 0, s[70:71]\n\t"
        "s_and_b64 s[62:63], s[62:63], s[70:71]\n\t"
        "s_andn2_b64 %3, %3, s[70:71]\n\t"
        "s_cbranch_scc0 13f\n\t"
        "s_mov_b64 exec, %3\n\t"
        "global_load_dwordx4 v[252:255], %7, %16 sc1\n\t"
        "13:\n\t"
        "s_mov_b64 s[54:55], 0\n\t"
        "s_mov_b64 s[72:73], 0\n\t"
        "s_mov_b64 exec, s[64:65]\n\t"
        "s_cbranch_execz 20f\n\t"
        "v_cmpx_ne_u32 s[72:73], -1, v240\n\t"
        "v_mul_hi_u32_u24_sdwa v204, v240, %20 dst_sel:DWORD dst_unused:UNUSED_PAD src0_sel:WORD_0 src1_sel:DWORD\n\t"
        "v_and_b32_sdwa v192, v243, v176 dst_sel:DWORD dst_unused:UNUSED_PAD src0_sel:BYTE_0 src1_sel:DWORD\n\t"
        "v_lshl_add_u32 v204, v204, 2, v192\n\t"
        "ds_add_rtn_u32 v204, v204, v191\n\t"
        "v_perm_b32 v216, v243, v240, v177\n\t"
        "v_mad_u32_u16 v228, v240, 32, %12 op_sel:[1,0,0,0]\n\t"
        "v_cmpx_ne_u32 s[74:75], -1, v241\n\t"
        "v_mul_hi_u32_u24_sdwa v205, v241, %20 dst_sel:DWORD dst_unused:UNUSED_PAD src0_sel:WORD_0 src1_sel:DWORD\n\t"
        "v_and_b32_sdwa v193, v243, v176 dst_sel:DWORD dst_unused:UNUSED_PAD src0_sel:BYTE_1 src1_sel:DWORD\n\t"
        "v_lshl_add_u32 v205, v205, 2, v193\n\t"
        "ds_add_rtn_u32 v205, v205, v191\n\t"
        "v_perm_b32 v217, v243, v241, v178\n\t"
        "v_mad_u32_u16 v229, v241, 32, %12 op_sel:[1,0,0,0]\n\t"
        "s_and_b64 exec, exec, s[52:53]\n\t"
        "v_cmpx_ne_u32 s[76:77], -1, v242\n\t"
        "v_mul_hi_u32_u24_sdwa v206, v242, %20 dst_sel:DWORD dst_unused:UNUSED_PAD src0_sel:WORD_0 src1_sel:DWORD\n\t"
        "v_and_b32_sdwa v194, v243, v176 dst_sel:DWORD dst_unused:UNUSED_PAD src0_sel:BYTE_2 src1_sel:DWORD\n\t"
        "v_lshl_add_u32 v206, v206, 2, v194\n\t"
        "ds_add_rtn_u32 v206, v206, v191\n\t"
        "v_perm_b32 v218, v243, v242, v179\n\t"
        "v_mad_u32_u16 v230, v242, 32, %12 op_sel:[1,0,0,0]\n\t"
        "s_or_b64 s[54:55], s[54:55], s[56:57]\n\t"
        "20:\n\t"
        "s_mov_b64 s[78:79], 0\n\t"
        "s_mov_b64 exec, s[66:67]\n\t"
        "s_cbranch_execz 21f\n\t"
        "v_cmpx_ne_u32 s[78:79], -1, v244\n\t"
        "v_mul_hi_u32_u24_sdwa v207, v244, %20 dst_sel:DWORD dst_unused:UNUSED_PAD src0_sel:WORD_0 src1_sel:DWORD\n\t"
        "v_and_b32_sdwa v195, v247, v176 dst_sel:DWORD dst_unused:UNUSED_PAD src0_sel:BYTE_0 src1_sel:DWORD\n\t"
        "v_lshl_add_u32 v207, v207, 2, v195\n\t"
        "ds_add_rtn_u32 v207, v207, v191\n\t"
        "v_perm_b32 v219, v247, v244, v177\n\t"
        "v_mad_u32_u16 v231, v244, 32, %13 op_sel:[1,0,0,0]\n\t"
        "v_cmpx_ne_u32 s[80:81], -1, v245\n\t"
        "v_mul_hi_u32_u24_sdwa v208, v245, %20 dst_sel:DWORD dst_unused:UNUSED_PAD src0_sel:WORD_0 src1_sel:DWORD\n\t"
        "v_and_b32_sdwa v196, v247, v176 dst_sel:DWORD dst_unused:UNUSED_PAD src0_sel:BYTE_1 src1_sel:DWORD\n\t"
        "v_lshl_add_u32 v208, v208, 2, v196\n\t"
        "ds_add_rtn_u32 v208, v208, v191\n\t"
        "v_perm_b32 v220, v247, v245, v178\n\t"
        "v_mad_u32_u16 v232, v245, 32, %13 op_sel:[1,0,0,0]\n\t"
        "s_and_b64 exec, exec, s[52:53]\n\t"
        "v_cmpx_ne_u32 s[82:83], -1, v246\n\t"
        "v_mul_hi_u32_u24_sdwa v209, v246, %20 dst_sel:DWORD dst_unused:UNUSED_PAD src0_sel:WORD_0 src1_sel:DWORD\n\t"
        "v_and_b32_sdwa v197, v247, v176 dst_sel:DWORD dst_unused:UNUSED_PAD src0_sel:BYTE_2 src1_sel:DWORD\n\t"
        "v_lshl_add_u32 v209, v209, 2, v197\n\t"
        "ds_add_rtn_u32 v209, v209, v191\n\t"
        "v_perm_b32 v221, v247, v246, v179\n\t"
        "v_mad_u32_u16 v233, v246, 32, %13 op_sel:[1,0,0,0]\n\t"
        "s_or_b64 s[54:55], s[54:55], s[58:59]\n\t"
        "21:\n\t"
        "s_mov_b64 s[84:85], 0\n\t"
        "s_mov_b64 exec, s[68:69]\n\t"
        "s_cbranch_execz 22f\n\t"
        "v_cmpx_ne_u32 s[84:85], -1, v248\n\t"
        "v_mul_hi_u32_u24_sdwa v210, v248, %20 dst_sel:DWORD dst_unused:UNUSED_PAD src0_sel:WORD_0 src1_sel:DWORD\n\t"
        "v_and_b32_sdwa v198, v251, v176 dst_sel:DWORD dst_unused:UNUSED_PAD src0_sel:BYTE_0 src1_sel:DWORD\n\t"
        "v_lshl_add_u32 v210, v210, 2, v198\n\t"
        "ds_add_rtn_u32 v210, v210, v191\n\t"
        "v_perm_b32 v222, v251, v248, v177\n\t"
        "v_mad_u32_u16 v234, v248, 32, %14 op_sel:[1,0,0,0]\n\t"
        "v_cmpx_ne_u32 s[86:87], -1, v249\n\t"
        "v_mul_hi_u32_u24_sdwa v211, v249, %20 dst_sel:DWORD dst_unused:UNUSED_PAD src0_sel:WORD_0 src1_sel:DWORD\n\t"
        "v_and_b32_sdwa v199, v251, v176 dst_sel:DWORD dst_unused:UNUSED_PAD src0_sel:BYTE_1 src1_sel:DWORD\n\t"
        "v_lshl_add_u32 v211, v211, 2, v199\n\t"
        "ds_add_rtn_u32 v211, v211, v191\n\t"
        "v_perm_b32 v223, v251, v249, v178\n\t"
        "v_mad_u32_u16 v235, v249, 32, %14 op_sel:[1,0,0,0]\n\t"
        "s_and_b64 exec, exec, s[52:53]\n\t"
        "v_cmpx_ne_u32 s[88:89], -1, v250\n\t"
        "v_mul_hi_u32_u24_sdwa v212, v250, %20 dst_sel:DWORD dst_unused:UNUSED_PAD src0_sel:WORD_0 src1_sel:DWORD\n\t"
        "v_and_b32_sdwa v200, v251, v176 dst_sel:DWORD dst_unused:UNUSED_PAD src0_sel:BYTE_2 src1_sel:DWORD\n\t"
        "v_lshl_add_u32 v212, v212, 2, v200\n\t"
        "ds_add_rtn_u32 v212, v212, v191\n\t"
        "v_perm_b32 v224, v251, v250, v179\n\t"
        "v_mad_u32_u16 v236, v250, 32, %14 op_sel:[1,0,0,0]\n\t"
        "s_or_b64 s[54:55], s[54:55], s[60:61]\n\t"
        "22:\n\t"
        "s_mov_b64 s[90:91], 0\n\t"
        "s_mov_b64 exec, s[70:71]\n\t"
        "s_cbranch_execz 23f\n\t"
        "v_cmpx_ne_u32 s[90:91], -1, v252\n\t"
        "v_mul_hi_u32_u24_sdwa v213, v252, %20 dst_sel:DWORD dst_unused:UNUSED_PAD src0_sel:WORD_0 src1_sel:DWORD\n\t"
        "v_and_b32_sdwa v201, v255, v176 dst_sel:DWORD dst_unused:UNUSED_PAD src0_sel:BYTE_0 src1_sel:DWORD\n\t"
        "v_lshl_add_u32 v213, v213, 2, v201\n\t"
        "ds_add_rtn_u32 v213, v213, v191\n\t"
        "v_perm_b32 v225, v255, v252, v177\n\t"
        "v_mad_u32_u16 v237, v252, 32, %15 op_sel:[1,0,0,0]\n\t"
        "v_cmpx_ne_u32 s[92:93], -1, v253\n\t"
        "v_mul_hi_u32_u24_sdwa v214, v253, %20 dst_sel:DWORD dst_unused:UNUSED_PAD src0_sel:WORD_0 src1_sel:DWORD\n\t"
        "v_and_b32_sdwa v202, v255, v176 dst_sel:DWORD dst_unused:UNUSED_PAD src0_sel:BYTE_1 src1_sel:DWORD\n\t"
        "v_lshl_add_u32 v214, v214, 2, v202\n\t"
        "ds_add_rtn_u32 v214, v214, v191\n\t"
        "v_perm_b32 v226, v255, v253, v178\n\t"
        "v_mad_u32_u16 v238, v253, 32, %15 op_sel:[1,0,0,0]\n\t"
        "s_and_b64 exec, exec, s[52:53]\n\t"
        "v_cmpx_ne_u32 s[94:95], -1, v254\n\t"
        "v_mul_hi_u32_u24_sdwa v215, v254, %20 dst_sel:DWORD dst_unused:UNUSED_PAD src0_sel:WORD_0 src1_sel:DWORD\n\t"
        "v_and_b32_sdwa v203, v255, v176 dst_sel:DWORD dst_unused:UNUSED_PAD src0_sel:BYTE_2 src1_sel:DWORD\n\t"
        "v_lshl_add_u32 v215, v215, 2, v203\n\t"
        "ds_add_rtn_u32 v215, v215, v191\n\t"
        "v_perm_b32 v227, v255, v254, v179\n\t"
        "v_mad_u32_u16 v239, v254, 32, %15 op_sel:[1,0,0,0]\n\t"
        "s_or_b64 s[54:55], s[54:55], s[62:63]\n\t"
        "23:\n\t"
        "s_cmp_eq_u64 s[54:55], 0\n\t"
        "s_cbranch_scc1 59f\n\t"
        "s_mov_b64 exec, s[56:57]\n\t"
        "s_cbranch_execz 40f\n\t"
        "v_bfe_u32 v188, %4, 7, 8\n\t"
        "v_lshrrev_b32 v189, 23, %4\n\t"
        "v_add_u32 v189, 1, v189\n\t"
        "ds_write_b8 v188, v189 offset:256\n\t"
        "v_add_u32 %4, s96, %4\n\t"
        "v_mov_b32 v190, 0\n\t"
        "v_cmpx_eq_u32 vcc, 2, v189\n\t"
        "ds_write_b32 v190, v191 offset:20\n\t"
        "s_andn2_b64 exec, s[56:57], vcc\n\t"
        "s_cbranch_execz 40f\n\t"
        "s_or_b64 %0, %0, exec\n\t"
        "global_load_dwordx4 v[240:243], %4, %16 sc1\n\t"
        "40:\n\t"
        "s_mov_b64 exec, s[58:59]\n\t"
        "s_cbranch_execz 41f\n\t"
        "v_bfe_u32 v188, %5, 7, 8\n\t"
        "v_lshrrev_b32 v189, 23, %5\n\t"
        "v_add_u32 v189, 1, v189\n\t"
        "ds_write_b8 v188, v189 offset:256\n\t"
        "v_add_u32 %5, s96, %5\n\t"
        "v_mov_b32 v190, 0\n\t"
        "v_cmpx_eq_u32 vcc, 2, v189\n\t"
        "ds_write_b32 v190, v191 offset:20\n\t"
        "s_andn2_b64 exec, s[58:59], vcc\n\t"
        "s_cbranch_execz 41f\n\t"
        "s_or_b64 %1, %1, exec\n\t"
        "global_load_dwordx4 v[244:247], %5, %16 sc1\n\t"
        "41:\n\t"
        "s_mov_b64 exec, s[60:61]\n\t"
        "s_cbranch_execz 42f\n\t"
        "v_bfe_u32 v188, %6, 7, 8\n\t"
        "v_lshrrev_b32 v189, 23, %6\n\t"
        "v_add_u32 v189, 1, v189\n\t"
        "ds_write_b8 v188, v189 offset:256\n\t"
        "v_add_u32 %6, s96, %6\n\t"
        "v_mov_b32 v190, 0\n\t"
        "v_cmpx_eq_u32 vcc, 2, v189\n\t"
        "ds_write_b32 v190, v191 offset:20\n\t"
        "s_andn2_b64 exec, s[60:61], vcc\n\t"
        "s_cbranch_execz 42f\n\t"
        "s_or_b64 %2, %2, exec\n\t"
        "global_load_dwordx4 v[248:251], %6, %16 sc1\n\t"
        "42:\n\t"
        "s_mov_b64 exec, s[62:63]\n\t"
        "s_cbranch_execz 43f\n\t"
        "v_bfe_u32 v188, %7, 7, 8\n\t"
        "v_lshrrev_b32 v189, 23, %7\n\t"
        "v_add_u32 v189, 1, v189\n\t"
        "ds_write_b8 v188, v189 offset:256\n\t"
        "v_add_u32 %7, s96, %7\n\t"
        "v_mov_b32 v190, 0\n\t"
        "v_cmpx_eq_u32 vcc, 2, v189\n\t"
        "ds_write_b32 v190, v191 offset:20\n\t"
        "s_andn2_b64 exec, s[62:63], vcc\n\t"
        "s_cbranch_execz 43f\n\t"
        "s_or_b64 %3, %3, exec\n\t"
        "global_load_dwordx4 v[252:255], %7, %16 sc1\n\t"
        "43:\n\t"
        "59:\n\t"
        "s_waitcnt lgkmcnt(0)\n\t"
        "s_mov_b64 exec, s[72:73]\n\t"
        "s_cbranch_execz 30f\n\t"
        "v_min_u32 v204, v181, v204\n\t"
        "v_min_u32 v205, v181, v205\n\t"
        "v_min_u32 v206, v181, v206\n\t"
        "v_mad_u32_u24 v204, v192, v180, v204\n\t"
        "v_mad_u32_u24 v205, v193, v180, v205\n\t"
        "v_mad_u32_u24 v206, v194, v180, v206\n\t"
        "ds_write2st64_b32 v204, v216, v228 offset1:10\n\t"
        "s_mov_b64 exec, s[74:75]\n\t"
        "ds_write2st64_b32 v205, v217, v229 offset1:10\n\t"
        "s_mov_b64 exec, s[76:77]\n\t"
        "ds_write2st64_b32 v206, v218, v230 offset1:10\n\t"
        "30:\n\t"
        "s_mov_b64 exec, s[78:79]\n\t"
        "s_cbranch_execz 31f\n\t"
        "v_min_u32 v207, v181, v207\n\t"
        "v_min_u32 v208, v181, v208\n\t"
        "v_min_u32 v209, v181, v209\n\t"
        "v_mad_u32_u24 v207, v195, v180, v207\n\t"
        "v_mad_u32_u24 v208, v196, v180, v208\n\t"
        "v_mad_u32_u24 v209, v197, v180, v209\n\t"
        "ds_write2st64_b32 v207, v219, v231 offset1:10\n\t"
        "s_mov_b64 exec, s[80:81]\n\t"
        "ds_write2st64_b32 v208, v220, v232 offset1:10\n\t"
        "s_mov_b64 exec, s[82:83]\n\t"
        "ds_write2st64_b32 v209, v221, v233 offset1:10\n\t"
        "31:\n\t"
        "s_mov_b64 exec, s[84:85]\n\t"
        "s_cbranch_execz 32f\n\t"
        "v_min_u32 v210, v181, v210\n\t"
        "v_min_u32 v211, v181, v211\n\t"
        "v_min_u32 v212, v181, v212\n\t"
        "v_mad_u32_u24 v210, v198, v180, v210\n\t"
        "v_mad_u32_u24 v211, v199, v180, v211\n\t"
        "v_mad_u32_u24 v212, v200, v180, v212\n\t"
        "ds_write2st64_b32 v210, v222, v234 offset1:10\n\t"
        "s_mov_b64 exec, s[86:87]\n\t"
        "ds_write2st64_b32 v211, v223, v235 offset1:10\n\t"
        "s_mov_b64 exec, s[88:89]\n\t"
        "ds_write2st64_b32 v212, v224, v236 offset1:10\n\t"
        "32:\n\t"
        "s_mov_b64 exec, s[90:91]\n\t"
        "s_cbranch_execz 33f\n\t"
        "v_min_u32 v213, v181, v213\n\t"
        "v_min_u32 v214, v181, v214\n\t"
        "v_min_u32 v215, v181, v215\n\t"
        "v_mad_u32_u24 v213, v201, v180, v213\n\t"
        "v_mad_u32_u24 v214, v202, v180, v214\n\t"
        "v_mad_u32_u24 v215, v203, v180, v215\n\t"
        "ds_write2st64_b32 v213, v225, v237 offset1:10\n\t"
        "s_mov_b64 exec, s[92:93]\n\t"
        "ds_write2st64_b32 v214, v226, v238 offset1:10\n\t"
        "s_mov_b64 exec, s[94:95]\n\t"
        "ds_write2st64_b32 v215, v227, v239 offset1:10\n\t"
        "33:\n\t"
        "s_mov_b64 exec, -1\n\t"
        : "+&s"(pend[0]), "+&s"(pend[1]), "+&s"(pend[2]), "+&s"(pend[3]), "+v"(vo[0]), "+v"(vo[1]), "+v"(vo[2]),
          "+v"(vo[3]), "+{v[240:243]}"(cv[0]), "+{v[244:247]}"(cv[1]), "+{v[248:251]}"(cv[2]), "+{v[252:255]}"(cv[3])
        : "v"(eb[0]), "v"(eb[1]), "v"(eb[2]), "v"(eb[3]), "s"(ib), "n"(OFF_L + LARR - 4), "v"(tag[0]), "v"(tag[1]),
          "s"(sq), "s"(just)
        :
          "memory", "vcc", "scc", "v176", "v177", "v178", "v179", "v180", "v181", "v188", "v189", "v190", "v191",
          "v192", "v193", "v194", "v195", "v196", "v197", "v198", "v199", "v200", "v201", "v202", "v203", "v204",
          "v205", "v206", "v207", "v208", "v209", "v210", "v211", "v212", "v213", "v214", "v215", "v216", "v217",
          "v218", "v219", "v220", "v221", "v222", "v223", "v224", "v225", "v226", "v227", "v228", "v229", "v230",
          "v231", "v232", "v233", "v234", "v235", "v236", "v237", "v238", "v239", "s52", "s53", "s54", "s55", "s56",
          "s57", "s58", "s59", "s60", "s61", "s62", "s63", "s64", "s65", "s66", "s67", "s68", "s69", "s70", "s71",
          "s72", "s73", "s74", "s75", "s76", "s77", "s78", "s79", "s80", "s81", "s82", "s83", "s84", "s85", "s86",
          "s87", "s88", "s89", "s90", "s91", "s92", "s93", "s94", "s95", "s96");
    uni();
    just = 0u;
  }
  *(LAS unsigned*)((LAS char*)smem + OFF_CNT + 16 + wave * 32) = early;
  asm volatile("s_waitcnt lgkmcnt(0)\n\ts_barrier" ::: "memory");
  if (wave >= 4) __builtin_amdgcn_s_setprio(1);
  const u32x4 cw = *(const LAS u32x4*)((LAS char*)smem + OFF_CNT + rid * 32);
  const unsigned ef = *(const LAS unsigned*)((LAS char*)smem + OFF_CNT + 16 + rid * 32);
  unsigned slowf = *(const LAS unsigned*)((LAS char*)smem + OFF_CNT + 20);
  asm volatile("" : "+v"(slowf));
  const int key = (int)((cw[0] + cw[1] + cw[2] + cw[3]) * 2u + rid);
  int gb;
  {
    unsigned acc = 0u;
#define RK(k) acc = __builtin_amdgcn_alignbit(acc, (unsigned)(__builtin_amdgcn_update_dpp(0, key, 0x120 + k, 0xf, 0xf, false) - key), 31)
    RK(1); RK(2); RK(3); RK(4); RK(5); RK(6); RK(7);
#undef RK
    const int want = RANKOF(wave);
    gb = __builtin_ctzll(__ballot(__builtin_popcount(acc) == want)) & 7;
  }
  const unsigned ring0 = lds0 + OFF_R0 + gb * RING_SLOT;
  const unsigned la = l0 + gb * LIST_BYTES;
  u32x16 XAf;
#pragma unroll
  for (int q = 0; q < 4; ++q) {
    const u32x4 t = ld128(ring0 + ringd + q * 1024 + lane * 16);
#pragma unroll
    for (int j = 0; j < 4; ++j) XAf[4 * q + j] = t[j];
  }
  unsigned sl[4];
#pragma unroll
  for (int q = 0; q < 4; ++q) sl[q] = __builtin_amdgcn_readlane(cw[q], gb) - (l0 + q * SUBL);
  const unsigned o2 = sl[0] + sl[1], o3 = o2 + sl[2];
  const bool slow = o3 + sl[3] > LCAP * 4u || max(max(sl[0], sl[1]), max(sl[2], sl[3])) > SUBL - 4u ||
                    __builtin_amdgcn_readfirstlane(slowf) != 0u;
  const int cnt = slow ? 0 : (int)(o3 + sl[3]) >> 2;
  const unsigned cg = (bu * 8 + gb) * 32;
  u32x16 AD;
  AD[2] = aLX + gb * LIST_BYTES;
  AD[3] = aLE + gb * LIST_BYTES;
  AD[4] = cx0; AD[5] = cx1; AD[6] = ce;
  auto issue_gather = [&](int tile, int rs) {
    const u32x4 sx = ld128(AD[2] + tile * 128);
    glds16e(ES, *(const LAS unsigned*)(size_t)(AD[3] + tile * 128), AD[6], ring0 + rs * ringd + 4096);
#pragma unroll
    for (int q = 0; q < 4; ++q) glds16x(x, sx[q], AD[4 + (q & 1)], ring0 + rs * ringd + q * 1024);
  };
  auto pads = [&](int cnt, int lane) __attribute__((always_inline)) {
    const int u = cnt + lane;
    const unsigned c = cg + ((u >> 2) & 7) + 8 * (u & 3), n = nodeof(c);
    const unsigned padA = n | (__builtin_amdgcn_readlane(c, __builtin_ctzll(__ballot((n * Mw + Cw) >> 16 != c))) & 0xffu) << 16;
    const unsigned pa = la + u * 4;
    *(LAS unsigned*)(size_t)pa = padA;
    *(LAS unsigned*)(size_t)(pa + 256) = padA;
    *(LAS unsigned*)(size_t)(pa + LARR) = 0u;
    *(LAS unsigned*)(size_t)(pa + LARR + 256) = 0u;
  };
  constexpr int GEND = 64 + NCH;
  int gs = 0, gp = 0;
  auto gen = [&]() __attribute__((always_inline)) {
    int l2, n = 0;
    asm volatile("v_mbcnt_lo_u32_b32 %0, -1, 0\n\tv_mbcnt_hi_u32_b32 %0, -1, %0" : "=v"(l2));
    const LAS unsigned char* xf = (const LAS unsigned char*)smem + OFF_XF;
    auto put = [&](bool ok, unsigned t, unsigned c, int cc) {
      ok = ok && (int)(c >> 5) == gb;
      const unsigned long long bm = __ballot(ok);
      const unsigned pa = la + 4u * (n + __builtin_amdgcn_mbcnt_hi((unsigned)(bm >> 32), __builtin_amdgcn_mbcnt_lo((unsigned)bm, 0u)));
      if (ok) {
        *(LAS unsigned*)(size_t)pa = (t & 0xffffu) | (c << 16);
        *(LAS unsigned*)(size_t)(pa + LARR) = ((unsigned)(cc * chunk) + (t >> 16)) * 32u;
      }
      n += __builtin_popcountll(bm);
    };
#pragma unroll 1
    while (gs < GEND && n <= LCAP - 192) {
      if (gs < 64) {
        const int cc = (gs & 31) * 8 + (l2 >> 3);
        const unsigned* pp = reinterpret_cast<const unsigned*>(ib + (gs >> 5) * IEXT + (gs & 31) * 1024 + l2 * 16);
        const bool live = gs < 32 || xf[cc] != 0;
        const unsigned m = ldd(pp + 3);
#pragma unroll 1
        for (int j = 0; j < 3; ++j) {
          const unsigned t = ldd(pp + j);
          put(live && t != ~0u && (j < 2 || (l2 & 7) != 7), t, (m >> (8 * j)) & 0xffu, cc);
        }
        ++gs;
      } else {
        const int c2 = gs - 64 + l2;
        const unsigned long long fm = __ballot(c2 < NCH && xf[min(c2, NCH - 1)] == 2);
        if (fm == 0ull) { gs = min(gs + 64, GEND); continue; }
        gs += __builtin_ctzll(fm);
        const int cc = gs - 64;
        const unsigned long long ds = ldq(FL + 512 + bu * NCH + cc);
        const int len = __builtin_amdgcn_readfirstlane((int)(ds >> 32));
        const unsigned long long* tp = TS + (unsigned)ds;
#pragma unroll 1
        for (; gp < len && n <= LCAP - 64; gp += 64) {
          const unsigned long long t = ldq(tp + min(gp + l2, len - 1));
          put(gp + l2 < len, (unsigned)t, (unsigned)(t >> 32), cc);
        }
        if (gp >= len) { gp = 0; ++gs; }
      }
    }
    __builtin_amdgcn_s_waitcnt(0x0F70);
    pads(n, l2);
    return n;
  };
  const bool quick = !slow && sl[0] >= 256u && __builtin_amdgcn_readlane(ef, gb) != 0u;
  auto gather1 = [&](unsigned skip) {
    u32x4 sx = ld128(AD[2] + 128);
    unsigned re = *(const LAS unsigned*)(size_t)(AD[3] + 128);
    asm volatile("s_cmp_lg_u32 %13, 0\n\t"
                 "s_cbranch_scc1 1f\n\t"
                 "s_add_i32 m0, %11, 4096\n\t"
                 "v_add_u32 %4, %4, %8\n\t"
                 "v_mad_u32_u16 %0, %0, %12, %6\n\t"
                 "global_load_lds_dwordx4 %4, %10\n\t"
                 "s_mov_b32 m0, %11\n\t"
                 "v_mad_u32_u16 %1, %1, %12, %7\n\t"
                 "global_load_lds_dwordx4 %0, %9\n\t"
                 "s_addk_i32 m0, 1024\n\t"
                 "v_mad_u32_u16 %2, %2, %12, %6\n\t"
                 "global_load_lds_dwordx4 %1, %9\n\t"
                 "s_addk_i32 m0, 1024\n\t"
                 "v_mad_u32_u16 %3, %3, %12, %7\n\t"
                 "global_load_lds_dwordx4 %2, %9\n\t"
                 "s_addk_i32 m0, 1024\n\t"
                 "s_nop 0\n\t"
                 "global_load_lds_dwordx4 %3, %9\n"
                 "1:"
                 : "+v"(sx[0]), "+v"(sx[1]), "+v"(sx[2]), "+v"(sx[3]), "+v"(re), "+{v[128:143]}"(XAf)
                 : "v"(cx0), "v"(cx1), "v"(ce), "s"(x), "s"(ES), "s"(ring0 + ringd), "s"(128u), "s"(skip)
                 : "memory", "m0", "scc");
  };
  unsigned mode;
  asm volatile("s_cmp_lg_u64 %1, 0\n\ts_cselect_b32 %0, %2, 0" : "=s"(mode) : "s"(__ballot(quick)), "s"(1 + wave / G1W) : "scc");
  gather1(mode ^ 2u);
  if (__builtin_expect(!slow, 1)) {
    const unsigned p0 = la + lane * 4;
    asm volatile(
        "ds_read2st64_b32 v[222:223], %1 offset0:2 offset1:12\n\t"
        "ds_read2st64_b32 v[224:225], %1 offset0:3 offset1:13\n\t"
        "ds_read2st64_b32 v[226:227], %1 offset0:4 offset1:14\n\t"
        "ds_read2st64_b32 v[228:229], %0 offset0:5 offset1:15\n\t"
        "ds_read2st64_b32 v[230:231], %0 offset0:6 offset1:16\n\t"
        "ds_read2st64_b32 v[232:233], %0 offset0:7 offset1:17\n\t"
        "ds_read2st64_b32 v[234:235], %1 offset0:7 offset1:17\n\t"
        "ds_read2st64_b32 v[236:237], %1 offset0:8 offset1:18\n\t"
        "ds_read2st64_b32 v[238:239], %1 offset0:9 offset1:19\n\t"
        "s_waitcnt lgkmcnt(8)\n\t"
        "ds_write2st64_b32 %2, v222, v223 offset0:0 offset1:10\n\t"
        "s_waitcnt lgkmcnt(8)\n\t"
        "ds_write2st64_b32 %2, v224, v225 offset0:1 offset1:11\n\t"
        "s_waitcnt lgkmcnt(8)\n\t"
        "ds_write2st64_b32 %2, v226, v227 offset0:2 offset1:12\n\t"
        "s_waitcnt lgkmcnt(8)\n\t"
        "ds_write2st64_b32 %3, v228, v229 offset0:0 offset1:10\n\t"
        "s_waitcnt lgkmcnt(8)\n\t"
        "ds_write2st64_b32 %3, v230, v231 offset0:1 offset1:11\n\t"
        "s_waitcnt lgkmcnt(8)\n\t"
        "ds_write2st64_b32 %3, v232, v233 offset0:2 offset1:12\n\t"
        "s_waitcnt lgkmcnt(8)\n\t"
        "ds_write2st64_b32 %4, v234, v235 offset0:0 offset1:10\n\t"
        "s_waitcnt lgkmcnt(8)\n\t"
        "ds_write2st64_b32 %4, v236, v237 offset0:1 offset1:11\n\t"
        "s_mov_b64 exec, 0xffffffff\n\t"
        "s_waitcnt lgkmcnt(8)\n\t"
        "ds_write2st64_b32 %4, v238, v239 offset0:2 offset1:12\n\t"
        "s_mov_b64 exec, -1"
        :: "v"(p0), "v"(p0 + 128u), "v"(p0 + sl[0]), "v"(p0 + o2), "v"(p0 + o3)
        : "memory", "v222", "v223", "v224", "v225", "v226", "v227", "v228", "v229", "v230", "v231", "v232", "v233", "v234",
          "v235", "v236", "v237", "v238", "v239");
  }
  pads(cnt, lane);
  AD[0] = aLS4 + gb * LIST_BYTES;
  AD[1] = aLS1 + gb * LIST_BYTES;
  AD[7] = cS - gb * 32 * TPITCH;
  AD[8] = cSt - gb * 32 * TPITCH;
#pragma unroll
  for (int t = 0; t < 4; ++t) AD[9 + t] = fo[t] + gb * RING_SLOT;
  AD[13] = foe + gb * RING_SLOT;
  AD[14] = lds0 + lane * 16;
  AD[15] = to + 18 * 1024 + h * 4096;
  if (__builtin_expect(mode == 0u, 0)) asm volatile("s_waitcnt vmcnt(0)\n\ts_barrier" ::: "memory");
  f32x16 X[2], U[2];
  if (__builtin_expect(mode == 0u, 0)) {
    issue_gather(0, 0);
    issue_gather(1, 1);
  }
  {
    int nt = __builtin_amdgcn_readfirstlane(max((cnt - 1) >> 5, 0));
    asm volatile(
        "s_movk_i32 s94, 128\n\t"
        "s_movk_i32 s95, 80\n\t"
        "s_mov_b32 s96, %[cu]\n\t"
        "ds_read_u16 v102, v161\n\t"
        "ds_read_b128 v[104:107], v162 offset:128\n\t"
        "ds_read_b32 v101, v163 offset:128\n\t"
        "ds_read_b128 v[96:99], v162 offset:256\n\t"
        "ds_read_b32 v100, v163 offset:256\n\t"
        "ds_read_b128 v[120:123], v160\n\t"
        "ds_read_u16 v124, v161 offset:128\n\t"
        "s_waitcnt lgkmcnt(6)\n\t"
        "v_mad_u32_u24 v102, v102, s95, v168\n\t"
        "ds_read_b128 v[184:187], v102\n\t"
        "ds_read_b128 v[188:191], v102 offset:16\n\t"
        "ds_read_b128 v[112:115], v174 offset:512\n\t"
        "ds_read_b128 v[116:119], v174 offset:2560\n\t"
        "ds_read_b128 v[108:111], v174 offset:8704\n\t"
        "ds_read_b128 v[196:199], v174 offset:16896\n\t"
        "ds_read_b128 v[200:203], v174 offset:1536\n\t"
        "ds_read_b128 v[204:207], v174 offset:3584\n\t"
        "s_waitcnt lgkmcnt(12)\n\t"
        "v_add_u32 v101, v101, v166\n\t"
        "v_mad_u32_u16 v104, v104, s94, v164\n\t"
        "v_mad_u32_u16 v105, v105, s94, v165\n\t"
        "v_mad_u32_u16 v106, v106, s94, v164\n\t"
        "v_mad_u32_u16 v107, v107, s94, v165\n\t"
        "s_waitcnt lgkmcnt(7)\n\t"
        "v_mfma_f32_32x32x16_f16 v[64:79], v[184:187], v[128:131], 0\n\t"
        "ds_read_b128 v[208:211], v174 offset:9728\n\t"
        "ds_read_b128 v[212:215], v174 offset:10752\n\t"
        "ds_read_b128 v[216:219], v174 offset:11776\n\t"
        "ds_read_b128 v[220:223], v174 offset:17920\n\t"
        "ds_read_b128 v[224:227], v174 offset:4608\n\t"
        "ds_read_b128 v[228:231], v174 offset:5632\n\t"
        "s_waitcnt lgkmcnt(12)\n\t"
        "v_mfma_f32_32x32x16_f16 v[64:79], v[188:191], v[136:139], v[64:79]\n\t"
        "ds_read_b128 v[232:235], v174 offset:6656\n\t"
        "ds_read_b128 v[236:239], v174 offset:7680\n\t"
        "ds_read_b128 v[240:243], v174 offset:12800\n\t"
        "ds_read_b128 v[244:247], v174 offset:13824\n\t"
        "ds_read_b128 v[248:251], v174 offset:14848\n\t"
        "ds_read_b128 v[252:255], v174 offset:15872\n\t"
        "s_add_i32 m0, %[ring], 45056\n\t"
        "s_waitcnt vmcnt(5)\n\t"
        "s_cmp_eq_u32 %[flag], 0\n\t"
        "s_cbranch_scc1 2f\n\t"
        "s_cmp_eq_u32 %[flag], 2\n\t"
        "s_cbranch_scc1 3f\n\t"
        "s_waitcnt vmcnt(0)\n\t"
        "3:\n\t"
        "s_barrier\n\t"
        "2:\n\t"
        "ds_read_b128 v[144:147], v169\n\t"
        "ds_read_b128 v[148:151], v170\n\t"
        "ds_read_b128 v[152:155], v171\n\t"
        "ds_read_b128 v[156:159], v172\n\t"
        "ds_read_b128 v[192:195], v173\n\t"
        "s_cbranch_scc1 4f\n\t"
        "global_load_lds_dwordx4 v101, %[es]\n\t"
        "s_add_i32 m0, %[ring], 40960\n\t"
        "s_nop 0\n\t"
        "global_load_lds_dwordx4 v104, %[x]\n\t"
        "s_add_i32 m0, %[ring], 41984\n\t"
        "s_nop 0\n\t"
        "global_load_lds_dwordx4 v105, %[x]\n\t"
        "s_add_i32 m0, %[ring], 43008\n\t"
        "s_nop 0\n\t"
        "global_load_lds_dwordx4 v106, %[x]\n\t"
        "s_add_i32 m0, %[ring], 44032\n\t"
        "s_nop 0\n\t"
        "global_load_lds_dwordx4 v107, %[x]\n\t"
        "4:\n\t"
        "s_waitcnt lgkmcnt(4)\n\t"
        "v_mfma_f32_32x32x16_bf16 v[64:79], v[144:147], v[112:115], v[64:79]\n\t"
        "s_waitcnt lgkmcnt(3)\n\t"
        "v_mfma_f32_32x32x16_bf16 v[64:79], v[148:151], v[200:203], v[64:79]\n\t"
        "s_waitcnt lgkmcnt(2)\n\t"
        "v_mfma_f32_32x32x16_bf16 v[64:79], v[152:155], v[116:119], v[64:79]\n\t"
        "s_waitcnt lgkmcnt(1)\n\t"
        "v_mfma_f32_32x32x16_bf16 v[64:79], v[156:159], v[204:207], v[64:79]\n\t"
        "s_waitcnt lgkmcnt(0)\n\t"
        "v_mfma_f32_32x32x16_bf16 v[64:79], v[192:195], v[196:199], v[64:79]\n\t"
        "s_add_i32 m0, %[ring], 4096\n\t"
        "v_add_u32 v100, v100, v166\n\t"
        "global_load_lds_dwordx4 v100, %[es]\n\t"
        "v_mfma_f32_32x32x16_bf16 v[80:95], v[144:147], v[108:111], 0\n\t"
        "s_mov_b32 m0, %[ring]\n\t"
        "v_mad_u32_u16 v96, v96, s94, v164\n\t"
        "global_load_lds_dwordx4 v96, %[x]\n\t"
        "s_add_i32 m0, %[ring], 1024\n\t"
        "v_mad_u32_u16 v97, v97, s94, v165\n\t"
        "global_load_lds_dwordx4 v97, %[x]\n\t"
        "v_mfma_f32_32x32x16_bf16 v[80:95], v[148:151], v[208:211], v[80:95]\n\t"
        "s_add_i32 m0, %[ring], 2048\n\t"
        "v_mad_u32_u16 v98, v98, s94, v164\n\t"
        "global_load_lds_dwordx4 v98, %[x]\n\t"
        "v_exp_f32 v64, v64\n\t"
        "v_exp_f32 v65, v65\n\t"
        "v_mfma_f32_32x32x16_bf16 v[80:95], v[152:155], v[212:215], v[80:95]\n\t"
        "s_add_i32 m0, %[ring], 3072\n\t"
        "v_mad_u32_u16 v99, v99, s94, v165\n\t"
        "global_load_lds_dwordx4 v99, %[x]\n\t"
        "v_exp_f32 v66, v66\n\t"
        "v_exp_f32 v67, v67\n\t"
        "v_mfma_f32_32x32x16_bf16 v[80:95], v[156:159], v[216:219], v[80:95]\n\t"
        "v_cvt_pk_f16_f32 v64, v64, v65\n\t"
        "v_exp_f32 v68, v68\n\t"
        "v_cvt_pk_f16_f32 v65, v66, v67\n\t"
        "v_pk_add_f16 v64, v64, s96\n\t"
        "v_exp_f32 v69, v69\n\t"
        "v_pk_add_f16 v65, v65, s96\n\t"
        "v_rcp_f16_sdwa v64, v64 dst_sel:WORD_0 dst_unused:UNUSED_PRESERVE src0_sel:WORD_0\n\t"
        "v_exp_f32 v70, v70\n\t"
        "v_cvt_pk_f16_f32 v66, v68, v69\n\t"
        "v_rcp_f16_sdwa v65, v65 dst_sel:WORD_0 dst_unused:UNUSED_PRESERVE src0_sel:WORD_0\n\t"
        "v_pk_add_f16 v66, v66, s96\n\t"
        "v_rcp_f16_sdwa v64, v64 dst_sel:WORD_1 dst_unused:UNUSED_PRESERVE src0_sel:WORD_1\n\t"
        "v_cvt_pk_f16_f32 v80, v80, v81\n\t"
        "v_rcp_f16_sdwa v65, v65 dst_sel:WORD_1 dst_unused:UNUSED_PRESERVE src0_sel:WORD_1\n\t"
        "v_pk_mul_f16 v80, v80, v64\n\t"
        "s_branch .Lr4_%=\n\t"
        ".Lrt_%=:\n\t"
        "v_mfma_f32_32x32x16_f16 v[16:31], v[96:99], v[176:179], v[16:31]\n\t"
        "s_cmp_lt_u32 %[wave], 4\n\t"
        "s_cbranch_scc1 1f\n\t"
        "s_setprio 1\n\t"
        "1:\n\t"
        "ds_read_b128 v[120:123], v160\n\t"
        "ds_read_u16 v124, v161 offset:128\n\t"
        "v_rcp_f16_sdwa v103, v103 dst_sel:WORD_1 dst_unused:UNUSED_PRESERVE src0_sel:WORD_1\n\t"
        "v_cvt_pk_f16_f32 v119, v126, v127\n\t"
        "v_pk_add_f16 v64, v64, s96\n\t"
        "v_exp_f32 v67, v67\n\t"
        "v_rcp_f16_sdwa v64, v64 dst_sel:WORD_0 dst_unused:UNUSED_PRESERVE src0_sel:WORD_0\n\t"
        "v_mfma_f32_32x32x16_f16 v[48:63], v[112:115], v[176:179], v[48:63]\n\t"
        "v_pk_mul_f16 v119, v119, v103\n\t"
        "v_cvt_pk_f16_f32 v65, v66, v67\n\t"
        "v_rcp_f16_sdwa v64, v64 dst_sel:WORD_1 dst_unused:UNUSED_PRESERVE src0_sel:WORD_1\n\t"
        "v_pk_add_f16 v65, v65, s96\n\t"
        "v_exp_f32 v68, v68\n\t"
        "v_mfma_f32_32x32x16_f16 v[16:31], v[100:103], v[180:183], v[16:31]\n\t"
        "v_cvt_pk_f16_f32 v80, v80, v81\n\t"
        "v_rcp_f16_sdwa v65, v65 dst_sel:WORD_0 dst_unused:UNUSED_PRESERVE src0_sel:WORD_0\n\t"
        "v_pk_mul_f16 v80, v80, v64\n\t"
        "v_exp_f32 v69, v69\n\t"
        "v_mfma_f32_32x32x16_f16 v[48:63], v[116:119], v[180:183], v[48:63]\n\t"
        "v_cvt_pk_f16_f32 v66, v68, v69\n\t"
        "v_rcp_f16_sdwa v65, v65 dst_sel:WORD_1 dst_unused:UNUSED_PRESERVE src0_sel:WORD_1\n\t"
        "v_pk_add_f16 v66, v66, s96\n\t"
        "v_exp_f32 v70, v70\n\t"
        ".Lr4_%=:\n\t"
        "v_mfma_f32_32x32x16_f16 v[96:111], v[184:187], v[132:135], 0\n\t"
        "s_waitcnt lgkmcnt(1)\n\t"
        "v_mad_u32_u16 v120, v120, s95, v167 op_sel:[1,0,0,0]\n\t"
        "ds_read_b64_tr_b16 v[176:177], v120\n\t"
        "v_mad_u32_u16 v121, v121, s95, v167 op_sel:[1,0,0,0]\n\t"
        "ds_read_b64_tr_b16 v[178:179], v121\n\t"
        "v_mad_u32_u16 v122, v122, s95, v167 op_sel:[1,0,0,0]\n\t"
        "ds_read_b64_tr_b16 v[180:181], v122\n\t"
        "v_mad_u32_u16 v123, v123, s95, v167 op_sel:[1,0,0,0]\n\t"
        "ds_read_b64_tr_b16 v[182:183], v123\n\t"
        "v_cvt_pk_f16_f32 v81, v82, v83\n\t"
        "v_rcp_f16_sdwa v66, v66 dst_sel:WORD_0 dst_unused:UNUSED_PRESERVE src0_sel:WORD_0\n\t"
        "v_pk_mul_f16 v81, v81, v65\n\t"
        "v_exp_f32 v71, v71\n\t"
        "v_mfma_f32_32x32x16_f16 v[96:111], v[188:191], v[140:143], v[96:111]\n\t"
        "s_waitcnt lgkmcnt(4)\n\t"
        "v_mad_u32_u24 v124, v124, s95, v168\n\t"
        "ds_read_b128 v[184:187], v124\n\t"
        "ds_read_b128 v[188:191], v124 offset:16\n\t"
        "v_cvt_pk_f16_f32 v67, v70, v71\n\t"
        "v_rcp_f16_sdwa v66, v66 dst_sel:WORD_1 dst_unused:UNUSED_PRESERVE src0_sel:WORD_1\n\t"
        "v_pk_add_f16 v67, v67, s96\n\t"
        "v_exp_f32 v72, v72\n\t"
        "v_mfma_f32_32x32x16_bf16 v[96:111], v[192:195], v[220:223], v[96:111]\n\t"
        "s_waitcnt vmcnt(9)\n\t"
        "ds_read_b128 v[192:195], v173 offset:40960\n\t"
        "ds_read_b32 v125, v163 offset:384\n\t"
        "v_cvt_pk_f16_f32 v82, v84, v85\n\t"
        "v_rcp_f16_sdwa v67, v67 dst_sel:WORD_0 dst_unused:UNUSED_PRESERVE src0_sel:WORD_0\n\t"
        "v_pk_mul_f16 v82, v82, v66\n\t"
        "v_exp_f32 v73, v73\n\t"
        "v_mfma_f32_32x32x16_bf16 v[96:111], v[144:147], v[224:227], v[96:111]\n\t"
        "v_cvt_pk_f16_f32 v68, v72, v73\n\t"
        "v_rcp_f16_sdwa v67, v67 dst_sel:WORD_1 dst_unused:UNUSED_PRESERVE src0_sel:WORD_1\n\t"
        "v_pk_add_f16 v68, v68, s96\n\t"
        "v_exp_f32 v74, v74\n\t"
        "v_mfma_f32_32x32x16_bf16 v[96:111], v[148:151], v[228:231], v[96:111]\n\t"
        "v_cvt_pk_f16_f32 v83, v86, v87\n\t"
        "v_rcp_f16_sdwa v68, v68 dst_sel:WORD_0 dst_unused:UNUSED_PRESERVE src0_sel:WORD_0\n\t"
        "v_pk_mul_f16 v83, v83, v67\n\t"
        "v_exp_f32 v75, v75\n\t"
        "v_mfma_f32_32x32x16_bf16 v[96:111], v[152:155], v[232:235], v[96:111]\n\t"
        "s_add_i32 m0, %[ring], 45056\n\t"
        "s_waitcnt lgkmcnt(0)\n\t"
        "v_add_u32 v125, v125, v166\n\t"
        "global_load_lds_dwordx4 v125, %[es]\n\t"
        "v_cvt_pk_f16_f32 v69, v74, v75\n\t"
        "v_rcp_f16_sdwa v68, v68 dst_sel:WORD_1 dst_unused:UNUSED_PRESERVE src0_sel:WORD_1\n\t"
        "v_pk_add_f16 v69, v69, s96\n\t"
        "v_exp_f32 v76, v76\n\t"
        "v_mfma_f32_32x32x16_bf16 v[96:111], v[156:159], v[236:239], v[96:111]\n\t"
        "v_cvt_pk_f16_f32 v84, v88, v89\n\t"
        "v_rcp_f16_sdwa v69, v69 dst_sel:WORD_0 dst_unused:UNUSED_PRESERVE src0_sel:WORD_0\n\t"
        "v_pk_mul_f16 v84, v84, v68\n\t"
        "v_exp_f32 v77, v77\n\t"
        "v_mfma_f32_32x32x16_bf16 v[112:127], v[144:147], v[240:243], 0\n\t"
        "s_waitcnt vmcnt(6)\n\t"
        "ds_read_b128 v[144:147], v169 offset:40960\n\t"
        "v_cvt_pk_f16_f32 v70, v76, v77\n\t"
        "v_rcp_f16_sdwa v69, v69 dst_sel:WORD_1 dst_unused:UNUSED_PRESERVE src0_sel:WORD_1\n\t"
        "v_pk_add_f16 v70, v70, s96\n\t"
        "v_exp_f32 v78, v78\n\t"
        "v_mfma_f32_32x32x16_bf16 v[112:127], v[148:151], v[244:247], v[112:127]\n\t"
        "ds_read_b128 v[148:151], v170 offset:40960\n\t"
        "s_sub_u32 %[nt], %[nt], 1\n\t"
        "v_cvt_pk_f16_f32 v85, v90, v91\n\t"
        "v_rcp_f16_sdwa v70, v70 dst_sel:WORD_0 dst_unused:UNUSED_PRESERVE src0_sel:WORD_0\n\t"
        "v_pk_mul_f16 v85, v85, v69\n\t"
        "v_exp_f32 v79, v79\n\t"
        "v_mfma_f32_32x32x16_bf16 v[112:127], v[152:155], v[248:251], v[112:127]\n\t"
        "ds_read_b128 v[152:155], v171 offset:40960\n\t"
        "ds_read_b128 v[88:91], v162 offset:384\n\t"
        "v_cvt_pk_f16_f32 v71, v78, v79\n\t"
        "v_rcp_f16_sdwa v70, v70 dst_sel:WORD_1 dst_unused:UNUSED_PRESERVE src0_sel:WORD_1\n\t"
        "v_pk_add_f16 v71, v71, s96\n\t"
        "v_exp_f32 v96, v96\n\t"
        "v_exp_f32 v97, v97\n\t"
        "v_mfma_f32_32x32x16_bf16 v[112:127], v[156:159], v[252:255], v[112:127]\n\t"
        "ds_read_b128 v[156:159], v172 offset:40960\n\t"
        "v_cvt_pk_f16_f32 v86, v92, v93\n\t"
        "v_rcp_f16_sdwa v71, v71 dst_sel:WORD_0 dst_unused:UNUSED_PRESERVE src0_sel:WORD_0\n\t"
        "v_pk_mul_f16 v86, v86, v70\n\t"
        "v_exp_f32 v98, v98\n\t"
        "v_cvt_pk_f16_f32 v96, v96, v97\n\t"
        "s_cbranch_scc1 .Lrl_%=\n\t"
        "v_mfma_f32_32x32x16_f16 v[0:15], v[64:67], v[176:179], v[0:15]\n\t"
        "v_rcp_f16_sdwa v71, v71 dst_sel:WORD_1 dst_unused:UNUSED_PRESERVE src0_sel:WORD_1\n\t"
        "v_cvt_pk_f16_f32 v87, v94, v95\n\t"
        "v_pk_add_f16 v96, v96, s96\n\t"
        "v_exp_f32 v99, v99\n\t"
        "v_rcp_f16_sdwa v96, v96 dst_sel:WORD_0 dst_unused:UNUSED_PRESERVE src0_sel:WORD_0\n\t"
        "v_mfma_f32_32x32x16_f16 v[32:47], v[80:83], v[176:179], v[32:47]\n\t"
        "s_add_i32 m0, %[ring], 40960\n\t"
        "s_waitcnt lgkmcnt(1)\n\t"
        "v_mad_u32_u16 v88, v88, s94, v164\n\t"
        "s_waitcnt lgkmcnt(0)\n\t"
        "global_load_lds_dwordx4 v88, %[x]\n\t"
        "v_pk_mul_f16 v87, v87, v71\n\t"
        "v_cvt_pk_f16_f32 v97, v98, v99\n\t"
        "v_rcp_f16_sdwa v96, v96 dst_sel:WORD_1 dst_unused:UNUSED_PRESERVE src0_sel:WORD_1\n\t"
        "v_pk_add_f16 v97, v97, s96\n\t"
        "v_exp_f32 v100, v100\n\t"
        "v_mfma_f32_32x32x16_f16 v[0:15], v[68:71], v[180:183], v[0:15]\n\t"
        "ds_read_b128 v[92:95], v174 offset:512\n\t"
        "s_add_i32 m0, %[ring], 41984\n\t"
        "v_mad_u32_u16 v89, v89, s94, v165\n\t"
        "global_load_lds_dwordx4 v89, %[x]\n\t"
        "v_cvt_pk_f16_f32 v112, v112, v113\n\t"
        "v_rcp_f16_sdwa v97, v97 dst_sel:WORD_0 dst_unused:UNUSED_PRESERVE src0_sel:WORD_0\n\t"
        "v_pk_mul_f16 v112, v112, v96\n\t"
        "v_exp_f32 v101, v101\n\t"
        "v_mfma_f32_32x32x16_f16 v[32:47], v[84:87], v[180:183], v[32:47]\n\t"
        "s_add_i32 m0, %[ring], 43008\n\t"
        "v_mad_u32_u16 v90, v90, s94, v164\n\t"
        "global_load_lds_dwordx4 v90, %[x]\n\t"
        "v_cvt_pk_f16_f32 v98, v100, v101\n\t"
        "v_rcp_f16_sdwa v97, v97 dst_sel:WORD_1 dst_unused:UNUSED_PRESERVE src0_sel:WORD_1\n\t"
        "v_pk_add_f16 v98, v98, s96\n\t"
        "v_exp_f32 v102, v102\n\t"
        "v_mfma_f32_32x32x16_f16 v[64:79], v[184:187], v[128:131], 0\n\t"
        "s_add_i32 m0, %[ring], 44032\n\t"
        "v_mad_u32_u16 v91, v91, s94, v165\n\t"
        "global_load_lds_dwordx4 v91, %[x]\n\t"
        "v_cvt_pk_f16_f32 v113, v114, v115\n\t"
        "v_rcp_f16_sdwa v98, v98 dst_sel:WORD_0 dst_unused:UNUSED_PRESERVE src0_sel:WORD_0\n\t"
        "v_pk_mul_f16 v113, v113, v97\n\t"
        "v_exp_f32 v103, v103\n\t"
        "v_mfma_f32_32x32x16_f16 v[64:79], v[188:191], v[136:139], v[64:79]\n\t"
        "ds_read_b128 v[80:83], v174 offset:2560\n\t"
        "v_cvt_pk_f16_f32 v99, v102, v103\n\t"
        "v_rcp_f16_sdwa v98, v98 dst_sel:WORD_1 dst_unused:UNUSED_PRESERVE src0_sel:WORD_1\n\t"
        "v_pk_add_f16 v99, v99, s96\n\t"
        "v_exp_f32 v104, v104\n\t"
        "v_mfma_f32_32x32x16_bf16 v[64:79], v[192:195], v[196:199], v[64:79]\n\t"
        "v_cvt_pk_f16_f32 v114, v116, v117\n\t"
        "v_rcp_f16_sdwa v99, v99 dst_sel:WORD_0 dst_unused:UNUSED_PRESERVE src0_sel:WORD_0\n\t"
        "v_pk_mul_f16 v114, v114, v98\n\t"
        "v_exp_f32 v105, v105\n\t"
        "s_waitcnt lgkmcnt(1)\n\t"
        "v_mfma_f32_32x32x16_bf16 v[64:79], v[144:147], v[92:95], v[64:79]\n\t"
        "ds_read_b128 v[84:87], v174 offset:8704\n\t"
        "v_cvt_pk_f16_f32 v100, v104, v105\n\t"
        "v_rcp_f16_sdwa v99, v99 dst_sel:WORD_1 dst_unused:UNUSED_PRESERVE src0_sel:WORD_1\n\t"
        "v_pk_add_f16 v100, v100, s96\n\t"
        "v_exp_f32 v106, v106\n\t"
        "v_mfma_f32_32x32x16_bf16 v[64:79], v[148:151], v[200:203], v[64:79]\n\t"
        "v_cvt_pk_f16_f32 v115, v118, v119\n\t"
        "v_rcp_f16_sdwa v100, v100 dst_sel:WORD_0 dst_unused:UNUSED_PRESERVE src0_sel:WORD_0\n\t"
        "v_pk_mul_f16 v115, v115, v99\n\t"
        "v_exp_f32 v107, v107\n\t"
        "s_waitcnt lgkmcnt(1)\n\t"
        "v_mfma_f32_32x32x16_bf16 v[64:79], v[152:155], v[80:83], v[64:79]\n\t"
        "v_cvt_pk_f16_f32 v101, v106, v107\n\t"
        "v_rcp_f16_sdwa v100, v100 dst_sel:WORD_1 dst_unused:UNUSED_PRESERVE src0_sel:WORD_1\n\t"
        "v_pk_add_f16 v101, v101, s96\n\t"
        "v_exp_f32 v108, v108\n\t"
        "v_mfma_f32_32x32x16_bf16 v[64:79], v[156:159], v[204:207], v[64:79]\n\t"
        "v_cvt_pk_f16_f32 v116, v120, v121\n\t"
        "v_rcp_f16_sdwa v101, v101 dst_sel:WORD_0 dst_unused:UNUSED_PRESERVE src0_sel:WORD_0\n\t"
        "v_pk_mul_f16 v116, v116, v100\n\t"
        "v_exp_f32 v109, v109\n\t"
        "s_waitcnt lgkmcnt(0)\n\t"
        "v_mfma_f32_32x32x16_bf16 v[80:95], v[144:147], v[84:87], 0\n\t"
        "v_cvt_pk_f16_f32 v102, v108, v109\n\t"
        "v_rcp_f16_sdwa v101, v101 dst_sel:WORD_1 dst_unused:UNUSED_PRESERVE src0_sel:WORD_1\n\t"
        "v_pk_add_f16 v102, v102, s96\n\t"
        "v_exp_f32 v110, v110\n\t"
        "v_mfma_f32_32x32x16_bf16 v[80:95], v[148:151], v[208:211], v[80:95]\n\t"
        "v_cvt_pk_f16_f32 v117, v122, v123\n\t"
        "v_rcp_f16_sdwa v102, v102 dst_sel:WORD_0 dst_unused:UNUSED_PRESERVE src0_sel:WORD_0\n\t"
        "v_pk_mul_f16 v117, v117, v101\n\t"
        "v_exp_f32 v111, v111\n\t"
        "v_mfma_f32_32x32x16_bf16 v[80:95], v[152:155], v[212:215], v[80:95]\n\t"
        "v_cvt_pk_f16_f32 v103, v110, v111\n\t"
        "v_rcp_f16_sdwa v102, v102 dst_sel:WORD_1 dst_unused:UNUSED_PRESERVE src0_sel:WORD_1\n\t"
        "v_pk_add_f16 v103, v103, s96\n\t"
        "v_exp_f32 v64, v64\n\t"
        "v_exp_f32 v65, v65\n\t"
        "v_mfma_f32_32x32x16_bf16 v[80:95], v[156:159], v[216:219], v[80:95]\n\t"
        "v_cvt_pk_f16_f32 v118, v124, v125\n\t"
        "v_rcp_f16_sdwa v103, v103 dst_sel:WORD_0 dst_unused:UNUSED_PRESERVE src0_sel:WORD_0\n\t"
        "v_pk_mul_f16 v118, v118, v102\n\t"
        "v_exp_f32 v66, v66\n\t"
        "v_cvt_pk_f16_f32 v64, v64, v65\n\t"
        "v_mfma_f32_32x32x16_f16 v[16:31], v[96:99], v[176:179], v[16:31]\n\t"
        "s_setprio 0\n\t"
        "ds_read_b128 v[120:123], v160 offset:128\n\t"
        "ds_read_u16 v124, v161 offset:256\n\t"
        "v_rcp_f16_sdwa v103, v103 dst_sel:WORD_1 dst_unused:UNUSED_PRESERVE src0_sel:WORD_1\n\t"
        "v_cvt_pk_f16_f32 v119, v126, v127\n\t"
        "v_pk_add_f16 v64, v64, s96\n\t"
        "v_exp_f32 v67, v67\n\t"
        "v_rcp_f16_sdwa v64, v64 dst_sel:WORD_0 dst_unused:UNUSED_PRESERVE src0_sel:WORD_0\n\t"
        "v_mfma_f32_32x32x16_f16 v[48:63], v[112:115], v[176:179], v[48:63]\n\t"
        "v_pk_mul_f16 v119, v119, v103\n\t"
        "v_cvt_pk_f16_f32 v65, v66, v67\n\t"
        "v_rcp_f16_sdwa v64, v64 dst_sel:WORD_1 dst_unused:UNUSED_PRESERVE src0_sel:WORD_1\n\t"
        "v_pk_add_f16 v65, v65, s96\n\t"
        "v_exp_f32 v68, v68\n\t"
        "v_mfma_f32_32x32x16_f16 v[16:31], v[100:103], v[180:183], v[16:31]\n\t"
        "v_cvt_pk_f16_f32 v80, v80, v81\n\t"
        "v_rcp_f16_sdwa v65, v65 dst_sel:WORD_0 dst_unused:UNUSED_PRESERVE src0_sel:WORD_0\n\t"
        "v_pk_mul_f16 v80, v80, v64\n\t"
        "v_exp_f32 v69, v69\n\t"
        "v_mfma_f32_32x32x16_f16 v[48:63], v[116:119], v[180:183], v[48:63]\n\t"
        "v_cvt_pk_f16_f32 v66, v68, v69\n\t"
        "v_rcp_f16_sdwa v65, v65 dst_sel:WORD_1 dst_unused:UNUSED_PRESERVE src0_sel:WORD_1\n\t"
        "v_pk_add_f16 v66, v66, s96\n\t"
        "v_exp_f32 v70, v70\n\t"
        "v_mfma_f32_32x32x16_f16 v[96:111], v[184:187], v[132:135], 0\n\t"
        "s_waitcnt lgkmcnt(1)\n\t"
        "v_mad_u32_u16 v120, v120, s95, v167 op_sel:[1,0,0,0]\n\t"
        "ds_read_b64_tr_b16 v[176:177], v120\n\t"
        "v_mad_u32_u16 v121, v121, s95, v167 op_sel:[1,0,0,0]\n\t"
        "ds_read_b64_tr_b16 v[178:179], v121\n\t"
        "v_mad_u32_u16 v122, v122, s95, v167 op_sel:[1,0,0,0]\n\t"
        "ds_read_b64_tr_b16 v[180:181], v122\n\t"
        "v_mad_u32_u16 v123, v123, s95, v167 op_sel:[1,0,0,0]\n\t"
        "ds_read_b64_tr_b16 v[182:183], v123\n\t"
        "v_cvt_pk_f16_f32 v81, v82, v83\n\t"
        "v_rcp_f16_sdwa v66, v66 dst_sel:WORD_0 dst_unused:UNUSED_PRESERVE src0_sel:WORD_0\n\t"
        "v_pk_mul_f16 v81, v81, v65\n\t"
        "v_exp_f32 v71, v71\n\t"
        "v_mfma_f32_32x32x16_f16 v[96:111], v[188:191], v[140:143], v[96:111]\n\t"
        "s_waitcnt lgkmcnt(4)\n\t"
        "v_mad_u32_u24 v124, v124, s95, v168\n\t"
        "ds_read_b128 v[184:187], v124\n\t"
        "ds_read_b128 v[188:191], v124 offset:16\n\t"
        "v_cvt_pk_f16_f32 v67, v70, v71\n\t"
        "v_rcp_f16_sdwa v66, v66 dst_sel:WORD_1 dst_unused:UNUSED_PRESERVE src0_sel:WORD_1\n\t"
        "v_pk_add_f16 v67, v67, s96\n\t"
        "v_exp_f32 v72, v72\n\t"
        "v_mfma_f32_32x32x16_bf16 v[96:111], v[192:195], v[220:223], v[96:111]\n\t"
        "s_waitcnt vmcnt(9)\n\t"
        "ds_read_b128 v[192:195], v173\n\t"
        "ds_read_b32 v125, v163 offset:512\n\t"
        "v_cvt_pk_f16_f32 v82, v84, v85\n\t"
        "v_rcp_f16_sdwa v67, v67 dst_sel:WORD_0 dst_unused:UNUSED_PRESERVE src0_sel:WORD_0\n\t"
        "v_pk_mul_f16 v82, v82, v66\n\t"
        "v_exp_f32 v73, v73\n\t"
        "v_mfma_f32_32x32x16_bf16 v[96:111], v[144:147], v[224:227], v[96:111]\n\t"
        "v_cvt_pk_f16_f32 v68, v72, v73\n\t"
        "v_rcp_f16_sdwa v67, v67 dst_sel:WORD_1 dst_unused:UNUSED_PRESERVE src0_sel:WORD_1\n\t"
        "v_pk_add_f16 v68, v68, s96\n\t"
        "v_exp_f32 v74, v74\n\t"
        "v_mfma_f32_32x32x16_bf16 v[96:111], v[148:151], v[228:231], v[96:111]\n\t"
        "v_cvt_pk_f16_f32 v83, v86, v87\n\t"
        "v_rcp_f16_sdwa v68, v68 dst_sel:WORD_0 dst_unused:UNUSED_PRESERVE src0_sel:WORD_0\n\t"
        "v_pk_mul_f16 v83, v83, v67\n\t"
        "v_exp_f32 v75, v75\n\t"
        "v_mfma_f32_32x32x16_bf16 v[96:111], v[152:155], v[232:235], v[96:111]\n\t"
        "s_add_i32 m0, %[ring], 4096\n\t"
        "s_waitcnt lgkmcnt(0)\n\t"
        "v_add_u32 v125, v125, v166\n\t"
        "global_load_lds_dwordx4 v125, %[es]\n\t"
        "v_cvt_pk_f16_f32 v69, v74, v75\n\t"
        "v_rcp_f16_sdwa v68, v68 dst_sel:WORD_1 dst_unused:UNUSED_PRESERVE src0_sel:WORD_1\n\t"
        "v_pk_add_f16 v69, v69, s96\n\t"
        "v_exp_f32 v76, v76\n\t"
        "v_mfma_f32_32x32x16_bf16 v[96:111], v[156:159], v[236:239], v[96:111]\n\t"
        "v_cvt_pk_f16_f32 v84, v88, v89\n\t"
        "v_rcp_f16_sdwa v69, v69 dst_sel:WORD_0 dst_unused:UNUSED_PRESERVE src0_sel:WORD_0\n\t"
        "v_pk_mul_f16 v84, v84, v68\n\t"
        "v_exp_f32 v77, v77\n\t"
        "v_mfma_f32_32x32x16_bf16 v[112:127], v[144:147], v[240:243], 0\n\t"
        "s_waitcnt vmcnt(6)\n\t"
        "ds_read_b128 v[144:147], v169\n\t"
        "v_cvt_pk_f16_f32 v70, v76, v77\n\t"
        "v_rcp_f16_sdwa v69, v69 dst_sel:WORD_1 dst_unused:UNUSED_PRESERVE src0_sel:WORD_1\n\t"
        "v_pk_add_f16 v70, v70, s96\n\t"
        "v_exp_f32 v78, v78\n\t"
        "v_mfma_f32_32x32x16_bf16 v[112:127], v[148:151], v[244:247], v[112:127]\n\t"
        "ds_read_b128 v[148:151], v170\n\t"
        "s_sub_u32 %[nt], %[nt], 1\n\t"
        "v_cvt_pk_f16_f32 v85, v90, v91\n\t"
        "v_rcp_f16_sdwa v70, v70 dst_sel:WORD_0 dst_unused:UNUSED_PRESERVE src0_sel:WORD_0\n\t"
        "v_pk_mul_f16 v85, v85, v69\n\t"
        "v_exp_f32 v79, v79\n\t"
        "v_mfma_f32_32x32x16_bf16 v[112:127], v[152:155], v[248:251], v[112:127]\n\t"
        "ds_read_b128 v[152:155], v171\n\t"
        "ds_read_b128 v[88:91], v162 offset:512\n\t"
        "v_cvt_pk_f16_f32 v71, v78, v79\n\t"
        "v_rcp_f16_sdwa v70, v70 dst_sel:WORD_1 dst_unused:UNUSED_PRESERVE src0_sel:WORD_1\n\t"
        "v_pk_add_f16 v71, v71, s96\n\t"
        "v_exp_f32 v96, v96\n\t"
        "v_exp_f32 v97, v97\n\t"
        "v_mfma_f32_32x32x16_bf16 v[112:127], v[156:159], v[252:255], v[112:127]\n\t"
        "ds_read_b128 v[156:159], v172\n\t"
        "v_cvt_pk_f16_f32 v86, v92, v93\n\t"
        "v_rcp_f16_sdwa v71, v71 dst_sel:WORD_0 dst_unused:UNUSED_PRESERVE src0_sel:WORD_0\n\t"
        "v_pk_mul_f16 v86, v86, v70\n\t"
        "v_exp_f32 v98, v98\n\t"
        "v_cvt_pk_f16_f32 v96, v96, v97\n\t"
        "s_cbranch_scc1 .Lrl_%=\n\t"
        "v_mfma_f32_32x32x16_f16 v[0:15], v[64:67], v[176:179], v[0:15]\n\t"
        "v_rcp_f16_sdwa v71, v71 dst_sel:WORD_1 dst_unused:UNUSED_PRESERVE src0_sel:WORD_1\n\t"
        "v_cvt_pk_f16_f32 v87, v94, v95\n\t"
        "v_pk_add_f16 v96, v96, s96\n\t"
        "v_exp_f32 v99, v99\n\t"
        "v_rcp_f16_sdwa v96, v96 dst_sel:WORD_0 dst_unused:UNUSED_PRESERVE src0_sel:WORD_0\n\t"
        "v_mfma_f32_32x32x16_f16 v[32:47], v[80:83], v[176:179], v[32:47]\n\t"
        "s_mov_b32 m0, %[ring]\n\t"
        "s_waitcnt lgkmcnt(1)\n\t"
        "v_mad_u32_u16 v88, v88, s94, v164\n\t"
        "s_waitcnt lgkmcnt(0)\n\t"
        "global_load_lds_dwordx4 v88, %[x]\n\t"
        "v_pk_mul_f16 v87, v87, v71\n\t"
        "v_cvt_pk_f16_f32 v97, v98, v99\n\t"
        "v_rcp_f16_sdwa v96, v96 dst_sel:WORD_1 dst_unused:UNUSED_PRESERVE src0_sel:WORD_1\n\t"
        "v_pk_add_f16 v97, v97, s96\n\t"
        "v_exp_f32 v100, v100\n\t"
        "v_mfma_f32_32x32x16_f16 v[0:15], v[68:71], v[180:183], v[0:15]\n\t"
        "ds_read_b128 v[92:95], v174 offset:512\n\t"
        "s_add_i32 m0, %[ring], 1024\n\t"
        "v_mad_u32_u16 v89, v89, s94, v165\n\t"
        "global_load_lds_dwordx4 v89, %[x]\n\t"
        "v_cvt_pk_f16_f32 v112, v112, v113\n\t"
        "v_rcp_f16_sdwa v97, v97 dst_sel:WORD_0 dst_unused:UNUSED_PRESERVE src0_sel:WORD_0\n\t"
        "v_pk_mul_f16 v112, v112, v96\n\t"
        "v_exp_f32 v101, v101\n\t"
        "v_mfma_f32_32x32x16_f16 v[32:47], v[84:87], v[180:183], v[32:47]\n\t"
        "s_add_i32 m0, %[ring], 2048\n\t"
        "v_mad_u32_u16 v90, v90, s94, v164\n\t"
        "global_load_lds_dwordx4 v90, %[x]\n\t"
        "v_cvt_pk_f16_f32 v98, v100, v101\n\t"
        "v_rcp_f16_sdwa v97, v97 dst_sel:WORD_1 dst_unused:UNUSED_PRESERVE src0_sel:WORD_1\n\t"
        "v_pk_add_f16 v98, v98, s96\n\t"
        "v_exp_f32 v102, v102\n\t"
        "v_mfma_f32_32x32x16_f16 v[64:79], v[184:187], v[128:131], 0\n\t"
        "s_add_i32 m0, %[ring], 3072\n\t"
        "v_mad_u32_u16 v91, v91, s94, v165\n\t"
        "global_load_lds_dwordx4 v91, %[x]\n\t"
        "v_cvt_pk_f16_f32 v113, v114, v115\n\t"
        "v_rcp_f16_sdwa v98, v98 dst_sel:WORD_0 dst_unused:UNUSED_PRESERVE src0_sel:WORD_0\n\t"
        "v_pk_mul_f16 v113, v113, v97\n\t"
        "v_exp_f32 v103, v103\n\t"
        "v_mfma_f32_32x32x16_f16 v[64:79], v[188:191], v[136:139], v[64:79]\n\t"
        "ds_read_b128 v[80:83], v174 offset:2560\n\t"
        "v_cvt_pk_f16_f32 v99, v102, v103\n\t"
        "v_rcp_f16_sdwa v98, v98 dst_sel:WORD_1 dst_unused:UNUSED_PRESERVE src0_sel:WORD_1\n\t"
        "v_pk_add_f16 v99, v99, s96\n\t"
        "v_exp_f32 v104, v104\n\t"
        "v_mfma_f32_32x32x16_bf16 v[64:79], v[192:195], v[196:199], v[64:79]\n\t"
        "v_cvt_pk_f16_f32 v114, v116, v117\n\t"
        "v_rcp_f16_sdwa v99, v99 dst_sel:WORD_0 dst_unused:UNUSED_PRESERVE src0_sel:WORD_0\n\t"
        "v_pk_mul_f16 v114, v114, v98\n\t"
        "v_exp_f32 v105, v105\n\t"
        "s_waitcnt lgkmcnt(1)\n\t"
        "v_mfma_f32_32x32x16_bf16 v[64:79], v[144:147], v[92:95], v[64:79]\n\t"
        "ds_read_b128 v[84:87], v174 offset:8704\n\t"
        "v_cvt_pk_f16_f32 v100, v104, v105\n\t"
        "v_rcp_f16_sdwa v99, v99 dst_sel:WORD_1 dst_unused:UNUSED_PRESERVE src0_sel:WORD_1\n\t"
        "v_pk_add_f16 v100, v100, s96\n\t"
        "v_exp_f32 v106, v106\n\t"
        "v_mfma_f32_32x32x16_bf16 v[64:79], v[148:151], v[200:203], v[64:79]\n\t"
        "v_cvt_pk_f16_f32 v115, v118, v119\n\t"
        "v_rcp_f16_sdwa v100, v100 dst_sel:WORD_0 dst_unused:UNUSED_PRESERVE src0_sel:WORD_0\n\t"
        "v_pk_mul_f16 v115, v115, v99\n\t"
        "v_exp_f32 v107, v107\n\t"
        "s_waitcnt lgkmcnt(1)\n\t"
        "v_mfma_f32_32x32x16_bf16 v[64:79], v[152:155], v[80:83], v[64:79]\n\t"
        "v_cvt_pk_f16_f32 v101, v106, v107\n\t"
        "v_rcp_f16_sdwa v100, v100 dst_sel:WORD_1 dst_unused:UNUSED_PRESERVE src0_sel:WORD_1\n\t"
        "v_pk_add_f16 v101, v101, s96\n\t"
        "v_exp_f32 v108, v108\n\t"
        "v_mfma_f32_32x32x16_bf16 v[64:79], v[156:159], v[204:207], v[64:79]\n\t"
        "v_cvt_pk_f16_f32 v116, v120, v121\n\t"
        "v_rcp_f16_sdwa v101, v101 dst_sel:WORD_0 dst_unused:UNUSED_PRESERVE src0_sel:WORD_0\n\t"
        "v_pk_mul_f16 v116, v116, v100\n\t"
        "v_exp_f32 v109, v109\n\t"
        "s_waitcnt lgkmcnt(0)\n\t"
        "v_mfma_f32_32x32x16_bf16 v[80:95], v[144:147], v[84:87], 0\n\t"
        "v_add_u32 v160, 0x100, v160\n\t"
        "v_cvt_pk_f16_f32 v102, v108, v109\n\t"
        "v_rcp_f16_sdwa v101, v101 dst_sel:WORD_1 dst_unused:UNUSED_PRESERVE src0_sel:WORD_1\n\t"
        "v_pk_add_f16 v102, v102, s96\n\t"
        "v_exp_f32 v110, v110\n\t"
        "v_mfma_f32_32x32x16_bf16 v[80:95], v[148:151], v[208:211], v[80:95]\n\t"
        "v_add_u32 v161, 0x100, v161\n\t"
        "v_cvt_pk_f16_f32 v117, v122, v123\n\t"
        "v_rcp_f16_sdwa v102, v102 dst_sel:WORD_0 dst_unused:UNUSED_PRESERVE src0_sel:WORD_0\n\t"
        "v_pk_mul_f16 v117, v117, v101\n\t"
        "v_exp_f32 v111, v111\n\t"
        "v_mfma_f32_32x32x16_bf16 v[80:95], v[152:155], v[212:215], v[80:95]\n\t"
        "v_add_u32 v162, 0x100, v162\n\t"
        "v_cvt_pk_f16_f32 v103, v110, v111\n\t"
        "v_rcp_f16_sdwa v102, v102 dst_sel:WORD_1 dst_unused:UNUSED_PRESERVE src0_sel:WORD_1\n\t"
        "v_pk_add_f16 v103, v103, s96\n\t"
        "v_exp_f32 v64, v64\n\t"
        "v_exp_f32 v65, v65\n\t"
        "v_mfma_f32_32x32x16_bf16 v[80:95], v[156:159], v[216:219], v[80:95]\n\t"
        "v_add_u32 v163, 0x100, v163\n\t"
        "v_cvt_pk_f16_f32 v118, v124, v125\n\t"
        "v_rcp_f16_sdwa v103, v103 dst_sel:WORD_0 dst_unused:UNUSED_PRESERVE src0_sel:WORD_0\n\t"
        "v_pk_mul_f16 v118, v118, v102\n\t"
        "v_exp_f32 v66, v66\n\t"
        "v_cvt_pk_f16_f32 v64, v64, v65\n\t"
        "s_branch .Lrt_%=\n\t"
        ".Lrl_%=:\n\t"
        "v_mfma_f32_32x32x16_f16 v[0:15], v[64:67], v[176:179], v[0:15]\n\t"
        "ds_read_b64_tr_b16 v[184:185], v175\n\t"
        "ds_read_b64_tr_b16 v[186:187], v175 offset:512\n\t"
        "ds_read_b64_tr_b16 v[188:189], v175 offset:1024\n\t"
        "ds_read_b64_tr_b16 v[190:191], v175 offset:1536\n\t"
        "v_rcp_f16_sdwa v71, v71 dst_sel:WORD_1 dst_unused:UNUSED_PRESERVE src0_sel:WORD_1\n\t"
        "v_cvt_pk_f16_f32 v87, v94, v95\n\t"
        "v_pk_add_f16 v96, v96, s96\n\t"
        "v_exp_f32 v99, v99\n\t"
        "v_rcp_f16_sdwa v96, v96 dst_sel:WORD_0 dst_unused:UNUSED_PRESERVE src0_sel:WORD_0\n\t"
        "v_mfma_f32_32x32x16_f16 v[32:47], v[80:83], v[176:179], v[32:47]\n\t"
        "ds_read_b64_tr_b16 v[192:193], v175 offset:2048\n\t"
        "ds_read_b64_tr_b16 v[194:195], v175 offset:2560\n\t"
        "ds_read_b64_tr_b16 v[196:197], v175 offset:3072\n\t"
        "ds_read_b64_tr_b16 v[198:199], v175 offset:3584\n\t"
        "v_pk_mul_f16 v87, v87, v71\n\t"
        "v_cvt_pk_f16_f32 v97, v98, v99\n\t"
        "v_rcp_f16_sdwa v96, v96 dst_sel:WORD_1 dst_unused:UNUSED_PRESERVE src0_sel:WORD_1\n\t"
        "v_pk_add_f16 v97, v97, s96\n\t"
        "v_exp_f32 v100, v100\n\t"
        "v_mfma_f32_32x32x16_f16 v[0:15], v[68:71], v[180:183], v[0:15]\n\t"
        "ds_read_b64_tr_b16 v[200:201], v175 offset:32\n\t"
        "ds_read_b64_tr_b16 v[202:203], v175 offset:544\n\t"
        "ds_read_b64_tr_b16 v[204:205], v175 offset:1056\n\t"
        "ds_read_b64_tr_b16 v[206:207], v175 offset:1568\n\t"
        "s_waitcnt lgkmcnt(14)\n\t"
        "v_lshlrev_b32 v216, 16, v144\n\t"
        "v_and_b32 v217, 0xffff0000, v144\n\t"
        "v_cvt_pk_f16_f32 v112, v112, v113\n\t"
        "v_rcp_f16_sdwa v97, v97 dst_sel:WORD_0 dst_unused:UNUSED_PRESERVE src0_sel:WORD_0\n\t"
        "v_pk_mul_f16 v112, v112, v96\n\t"
        "v_exp_f32 v101, v101\n\t"
        "v_mfma_f32_32x32x16_f16 v[32:47], v[84:87], v[180:183], v[32:47]\n\t"
        "ds_read_b64_tr_b16 v[208:209], v175 offset:2080\n\t"
        "ds_read_b64_tr_b16 v[210:211], v175 offset:2592\n\t"
        "ds_read_b64_tr_b16 v[212:213], v175 offset:3104\n\t"
        "ds_read_b64_tr_b16 v[214:215], v175 offset:3616\n\t"
        "v_lshlrev_b32 v218, 16, v145\n\t"
        "v_and_b32 v219, 0xffff0000, v145\n\t"
        "v_cvt_pk_f16_f32 v98, v100, v101\n\t"
        "v_rcp_f16_sdwa v97, v97 dst_sel:WORD_1 dst_unused:UNUSED_PRESERVE src0_sel:WORD_1\n\t"
        "v_pk_add_f16 v98, v98, s96\n\t"
        "v_exp_f32 v102, v102\n\t"
        "v_lshlrev_b32 v220, 16, v146\n\t"
        "v_and_b32 v221, 0xffff0000, v146\n\t"
        "v_lshlrev_b32 v222, 16, v147\n\t"
        "v_and_b32 v223, 0xffff0000, v147\n\t"
        "s_waitcnt lgkmcnt(14)\n\t"
        "v_lshlrev_b32 v224, 16, v148\n\t"
        "v_and_b32 v225, 0xffff0000, v148\n\t"
        "v_cvt_pk_f16_f32 v113, v114, v115\n\t"
        "v_rcp_f16_sdwa v98, v98 dst_sel:WORD_0 dst_unused:UNUSED_PRESERVE src0_sel:WORD_0\n\t"
        "v_pk_mul_f16 v113, v113, v97\n\t"
        "v_exp_f32 v103, v103\n\t"
        "v_lshlrev_b32 v226, 16, v149\n\t"
        "v_and_b32 v227, 0xffff0000, v149\n\t"
        "v_lshlrev_b32 v228, 16, v150\n\t"
        "v_and_b32 v229, 0xffff0000, v150\n\t"
        "v_lshlrev_b32 v230, 16, v151\n\t"
        "v_and_b32 v231, 0xffff0000, v151\n\t"
        "v_cvt_pk_f16_f32 v99, v102, v103\n\t"
        "v_rcp_f16_sdwa v98, v98 dst_sel:WORD_1 dst_unused:UNUSED_PRESERVE src0_sel:WORD_1\n\t"
        "v_pk_add_f16 v99, v99, s96\n\t"
        "v_exp_f32 v104, v104\n\t"
        "s_waitcnt lgkmcnt(14)\n\t"
        "v_mfma_f32_32x32x16_bf16 v[64:79], v[184:187], v[144:147], v[216:231]\n\t"
        "v_lshlrev_b32 v232, 16, v152\n\t"
        "v_and_b32 v233, 0xffff0000, v152\n\t"
        "v_cvt_pk_f16_f32 v114, v116, v117\n\t"
        "v_rcp_f16_sdwa v99, v99 dst_sel:WORD_0 dst_unused:UNUSED_PRESERVE src0_sel:WORD_0\n\t"
        "v_pk_mul_f16 v114, v114, v98\n\t"
        "v_exp_f32 v105, v105\n\t"
        "s_waitcnt lgkmcnt(12)\n\t"
        "v_mfma_f32_32x32x16_bf16 v[64:79], v[188:191], v[148:151], v[64:79]\n\t"
        "v_lshlrev_b32 v234, 16, v153\n\t"
        "v_and_b32 v235, 0xffff0000, v153\n\t"
        "v_cvt_pk_f16_f32 v100, v104, v105\n\t"
        "v_rcp_f16_sdwa v99, v99 dst_sel:WORD_1 dst_unused:UNUSED_PRESERVE src0_sel:WORD_1\n\t"
        "v_pk_add_f16 v100, v100, s96\n\t"
        "v_exp_f32 v106, v106\n\t"
        "s_waitcnt lgkmcnt(10)\n\t"
        "v_mfma_f32_32x32x16_bf16 v[64:79], v[192:195], v[152:155], v[64:79]\n\t"
        "v_lshlrev_b32 v236, 16, v154\n\t"
        "v_and_b32 v237, 0xffff0000, v154\n\t"
        "v_cvt_pk_f16_f32 v115, v118, v119\n\t"
        "v_rcp_f16_sdwa v100, v100 dst_sel:WORD_0 dst_unused:UNUSED_PRESERVE src0_sel:WORD_0\n\t"
        "v_pk_mul_f16 v115, v115, v99\n\t"
        "v_exp_f32 v107, v107\n\t"
        "s_waitcnt lgkmcnt(8)\n\t"
        "v_mfma_f32_32x32x16_bf16 v[64:79], v[196:199], v[156:159], v[64:79]\n\t"
        "v_lshlrev_b32 v238, 16, v155\n\t"
        "v_and_b32 v239, 0xffff0000, v155\n\t"
        "v_cvt_pk_f16_f32 v101, v106, v107\n\t"
        "v_rcp_f16_sdwa v100, v100 dst_sel:WORD_1 dst_unused:UNUSED_PRESERVE src0_sel:WORD_1\n\t"
        "v_pk_add_f16 v101, v101, s96\n\t"
        "v_exp_f32 v108, v108\n\t"
        "v_lshlrev_b32 v240, 16, v156\n\t"
        "v_and_b32 v241, 0xffff0000, v156\n\t"
        "v_lshlrev_b32 v242, 16, v157\n\t"
        "v_and_b32 v243, 0xffff0000, v157\n\t"
        "v_lshlrev_b32 v244, 16, v158\n\t"
        "v_and_b32 v245, 0xffff0000, v158\n\t"
        "v_lshlrev_b32 v246, 16, v159\n\t"
        "v_and_b32 v247, 0xffff0000, v159\n\t"
        "v_cvt_pk_f16_f32 v116, v120, v121\n\t"
        "v_rcp_f16_sdwa v101, v101 dst_sel:WORD_0 dst_unused:UNUSED_PRESERVE src0_sel:WORD_0\n\t"
        "v_pk_mul_f16 v116, v116, v100\n\t"
        "v_exp_f32 v109, v109\n\t"
        "s_waitcnt lgkmcnt(6)\n\t"
        "v_mfma_f32_32x32x16_bf16 v[80:95], v[200:203], v[144:147], v[232:247]\n\t"
        "v_cvt_pk_f16_f32 v102, v108, v109\n\t"
        "v_rcp_f16_sdwa v101, v101 dst_sel:WORD_1 dst_unused:UNUSED_PRESERVE src0_sel:WORD_1\n\t"
        "v_pk_add_f16 v102, v102, s96\n\t"
        "v_exp_f32 v110, v110\n\t"
        "s_waitcnt lgkmcnt(4)\n\t"
        "v_mfma_f32_32x32x16_bf16 v[80:95], v[204:207], v[148:151], v[80:95]\n\t"
        "v_cvt_pk_f16_f32 v117, v122, v123\n\t"
        "v_rcp_f16_sdwa v102, v102 dst_sel:WORD_0 dst_unused:UNUSED_PRESERVE src0_sel:WORD_0\n\t"
        "v_pk_mul_f16 v117, v117, v101\n\t"
        "v_exp_f32 v111, v111\n\t"
        "s_waitcnt lgkmcnt(2)\n\t"
        "v_mfma_f32_32x32x16_bf16 v[80:95], v[208:211], v[152:155], v[80:95]\n\t"
        "v_cvt_pk_f16_f32 v103, v110, v111\n\t"
        "v_rcp_f16_sdwa v102, v102 dst_sel:WORD_1 dst_unused:UNUSED_PRESERVE src0_sel:WORD_1\n\t"
        "v_pk_add_f16 v103, v103, s96\n\t"
        "s_waitcnt lgkmcnt(0)\n\t"
        "v_mfma_f32_32x32x16_bf16 v[80:95], v[212:215], v[156:159], v[80:95]\n\t"
        "v_cvt_pk_f16_f32 v118, v124, v125\n\t"
        "v_rcp_f16_sdwa v103, v103 dst_sel:WORD_0 dst_unused:UNUSED_PRESERVE src0_sel:WORD_0\n\t"
        "v_pk_mul_f16 v118, v118, v102\n\t"
        "v_mfma_f32_32x32x16_f16 v[16:31], v[96:99], v[176:179], v[16:31]\n\t"
        "v_rcp_f16_sdwa v103, v103 dst_sel:WORD_1 dst_unused:UNUSED_PRESERVE src0_sel:WORD_1\n\t"
        "v_cvt_pk_f16_f32 v119, v126, v127\n\t"
        "v_mfma_f32_32x32x16_f16 v[48:63], v[112:115], v[176:179], v[48:63]\n\t"
        "v_pk_mul_f16 v119, v119, v103\n\t"
        "v_mfma_f32_32x32x16_f16 v[16:31], v[100:103], v[180:183], v[16:31]\n\t"
        "s_nop 0\n\t"
        "v_mfma_f32_32x32x16_f16 v[48:63], v[116:119], v[180:183], v[48:63]\n\t"
        : "+{v[0:15]}"(Y[0]), "+{v[16:31]}"(Y[1]), "+{v[32:47]}"(Y[2]), "+{v[48:63]}"(Y[3]), "={v[64:79]}"(U[0]),
          "={v[80:95]}"(U[1]), "={v[216:231]}"(X[0]), "={v[232:247]}"(X[1]), "+{v[160:175]}"(AD), [nt] "+s"(nt)
        : "{v[128:143]}"(XAf), [x] "s"(x), [es] "s"(ES), [ring] "s"(ring0), [wave] "s"(wave), [flag] "s"(mode),
          [cu] "n"(cu)
        :
          "memory", "scc", "m0", "s94", "s95", "s96", "v96", "v97", "v98", "v99", "v100", "v101", "v102", "v103",
          "v104", "v105", "v106", "v107", "v108", "v109", "v110", "v111", "v112", "v113", "v114", "v115", "v116",
          "v117", "v118", "v119", "v120", "v121", "v122", "v123", "v124", "v125", "v126", "v127", "v144", "v145",
          "v146", "v147", "v148", "v149", "v150", "v151", "v152", "v153", "v154", "v155", "v156", "v157", "v158",
          "v159", "v176", "v177", "v178", "v179", "v180", "v181", "v182", "v183", "v184", "v185", "v186", "v187",
          "v188", "v189", "v190", "v191", "v192", "v193", "v194", "v195", "v196", "v197", "v198", "v199", "v200",
          "v201", "v202", "v203", "v204", "v205", "v206", "v207", "v208", "v209", "v210", "v211", "v212", "v213",
          "v214", "v215", "v248", "v249", "v250", "v251", "v252", "v253", "v254", "v255");
  }
  SB();
  auto wide = [&]() __attribute__((always_inline)) {
    int l2;
    asm volatile("v_mbcnt_lo_u32_b32 %0, -1, 0\n\tv_mbcnt_hi_u32_b32 %0, -1, %0\n\ts_waitcnt vmcnt(0)" : "=v"(l2) :: "memory");
    AD[0] = la + (4 * (l2 >> 5) + ((l2 >> 2) & 3)) * 16;
    AD[1] = la + (((l2 & 7) << 2) | ((l2 >> 3) & 3)) * 4 + 2;
    AD[2] = la + (l2 >> 3) * 16;
    AD[3] = la + LARR + ((((l2 >> 1) & 7) << 2) | (l2 >> 4)) * 4;
    auto rd = [&](unsigned a) { return *(const LAS bf16x8*)(size_t)a; };
    auto one = [&](bf16x8 v) {
      const u32x4 t = __builtin_bit_cast(u32x4, v);
      return __builtin_bit_cast(bf16x8, t | (t >> 3));
    };
    bf16x8 xq[4];
    {
      f32x16 T[2] = {zero16, zero16};
#pragma unroll
      for (int k = 0; k < 4; ++k) {
        u32x4 t;
#pragma unroll
        for (int j = 0; j < 4; ++j) t[j] = pack_bf16(X[k >> 1][8 * (k & 1) + 2 * j], X[k >> 1][8 * (k & 1) + 2 * j + 1]);
#pragma unroll
        for (int ct = 0; ct < 2; ++ct)
          T[ct] = MFMA32(__builtin_bit_cast(bf16x8, t), rawfrag(AD[15] + (NBLOB - 18 + k) * 1024 + ct * 32), T[ct]);
      }
#pragma unroll
      for (int q = 0; q < 4; ++q) {
        u32x4 t;
#pragma unroll
        for (int j = 0; j < 4; ++j)
          t[j] = pack_bf16(T[q & 1][8 * (q >> 1) + 2 * j] * NLOG2E, T[q & 1][8 * (q >> 1) + 2 * j + 1] * NLOG2E);
        xq[q] = __builtin_bit_cast(bf16x8, t);
      }
    }
#pragma unroll
    for (int i = 0; i < 16; ++i) { Y[0][i] = 1e-6f * (1 << RA); Y[1][i] = 1e-6f * (1 << RA); Y[2][i] = 0.f; Y[3][i] = 0.f; }
#pragma unroll 1
    do {
      const int n = slow ? gen() : cnt;
      issue_gather(0, 0);
#pragma unroll 1
      for (int t = 0; t * 32 < n; ++t) {
        const unsigned ro = (t & 1) * ringd;
        asm volatile("s_waitcnt lgkmcnt(0)" ::: "memory");
        issue_gather(t + 1, ~t & 1);
        const u32x4 ss = ld128(AD[0] + t * 128);
        const unsigned st = *(const LAS unsigned short*)(size_t)(AD[1] + t * 128) * TPITCH + AD[8];
        s16x4 a[4];
#pragma unroll
        for (int q = 0; q < 4; ++q)
          a[q] = __builtin_amdgcn_ds_read_tr16_b64_v4i16((LAS s16x4*)(size_t)((ss[q] >> 16) * TPITCH + AD[7]));
        const bf16x8 S0 = one(__builtin_shufflevector(a[0], a[1], 0, 1, 2, 3, 4, 5, 6, 7));
        const bf16x8 S1 = one(__builtin_shufflevector(a[2], a[3], 0, 1, 2, 3, 4, 5, 6, 7));
        const bf16x8 T0 = one(rd(st)), T1 = one(rd(st + 16));
        asm volatile("s_waitcnt vmcnt(5)" ::: "memory");
#pragma unroll
        for (int ct = 0; ct < 2; ++ct) {
          const unsigned w = AD[14] + OFF_W + ct * 4096;
          f32x16 G = MFMA32(T0, xq[ct], zero16), V = zero16;
          G = MFMA32(T1, xq[2 + ct], G);
          G = MFMA32(rd(AD[13] + ro), rd(AD[14] + OFF_W + (16 + ct) * 1024), G);
#pragma unroll
          for (int k = 0; k < 4; ++k) {
            const bf16x8 as = rd(AD[9 + k] + ro);
            G = MFMA32(as, rd(w + k * 1024), G);
            V = MFMA32(as, rd(w + (8 + k) * 1024), V);
          }
          u32x4 pd[2], pn[2];
#pragma unroll
          for (int i = 0; i < 16; i += 2) {
            constexpr float u = 1.0f / (1 << RA);
            const float r0 = __builtin_amdgcn_rcpf(__builtin_fmaf(__builtin_amdgcn_exp2f(G[i]), u, u));
            const float r1 = __builtin_amdgcn_rcpf(__builtin_fmaf(__builtin_amdgcn_exp2f(G[i + 1]), u, u));
            pd[i >> 3][(i >> 1) & 3] = pack_bf16(r0, r1);
            pn[i >> 3][(i >> 1) & 3] = pack_bf16(r0 * V[i], r1 * V[i + 1]);
          }
          Y[ct] = MFMA32(__builtin_bit_cast(bf16x8, pd[0]), S0, Y[ct]);
          Y[ct] = MFMA32(__builtin_bit_cast(bf16x8, pd[1]), S1, Y[ct]);
          Y[2 + ct] = MFMA32(__builtin_bit_cast(bf16x8, pn[0]), S0, Y[2 + ct]);
          Y[2 + ct] = MFMA32(__builtin_bit_cast(bf16x8, pn[1]), S1, Y[2 + ct]);
          SB();
        }
      }
      asm volatile("s_waitcnt vmcnt(0) lgkmcnt(0)" ::: "memory");
    } while (slow && gs < GEND);
  };

  const unsigned ot = ring0 + (max(cnt - 1, 0) & 32 ? 0u : ringd);
  auto finish = [&](const bool check) __attribute__((always_inline)) {
    auto inv = [&](f32x16& y) {
      y *= VSC;
#pragma unroll
      for (int i = 0; i < 16; ++i) y[i] = __builtin_amdgcn_rcpf(y[i]);
    };
    inv(Y[0]);
    SB();
    int lane;
    asm volatile("v_mbcnt_lo_u32_b32 %0, -1, 0\n\tv_mbcnt_hi_u32_b32 %0, -1, %0" : "=v"(lane));
    const int h = lane >> 5;
    const unsigned nd = nodeof(cg + (lane & 31));
    const bool live = (nd * Mw + Cw) >> 16 == cg + (lane & 31);
    const unsigned lm = (unsigned)__ballot(live);
    const int nodes = __builtin_popcount(lm);
    const unsigned n0 = __builtin_amdgcn_readlane(nd, __builtin_ctz(lm | 0x80000000u));
    const int r = live ? nd - n0 : 31;
    SB();
    inv(Y[1]);
    unsigned ov[16];
#pragma unroll
    for (int p = 0; p < 4; ++p) {
      const int ct = p >> 1;
#pragma unroll
      for (int j = 0; j < 4; ++j) {
        const int i = 8 * (p & 1) + 2 * j;
        const f32x2 z = __builtin_elementwise_fma(f32x2{Y[2 + ct][i], Y[2 + ct][i + 1]}, f32x2{Y[ct][i], Y[ct][i + 1]},
                                                  f32x2{U[ct][i], U[ct][i + 1]});
        ov[4 * p + j] = pack_bf16(MAXN(z[0], X[ct][i]), MAXN(z[1], X[ct][i + 1]));
      }
      *(LAS u32x4*)(size_t)(ot + r * 128 + (((4 * h + p) ^ (r & 7)) * 16)) =
          u32x4{ov[4 * p], ov[4 * p + 1], ov[4 * p + 2], ov[4 * p + 3]};
    }
    u32x4 ow[4];
#pragma unroll
    for (int q = 0; q < 4; ++q)
      ow[q] = *(const LAS u32x4*)(size_t)(ot + (8 * q + (lane >> 3)) * 128 + (((lane & 7) ^ (lane >> 3)) * 16));
    asm volatile("v_cmpx_lt_i32 vcc, 0, %6\n\t"
                 "global_store_dwordx4 %0, %1, %5 sc1\n\t"
                 "v_cmpx_lt_i32 vcc, 8, %6\n\t"
                 "global_store_dwordx4 %0, %2, %5 offset:1024 sc1\n\t"
                 "v_cmpx_lt_i32 vcc, 16, %6\n\t"
                 "global_store_dwordx4 %0, %3, %5 offset:2048 sc1\n\t"
                 "v_cmpx_lt_i32 vcc, 24, %6\n\t"
                 "global_store_dwordx4 %0, %4, %5 offset:3072 sc1\n\t"
                 "s_mov_b64 exec, -1"
                 :: "v"((unsigned)n0 * 128u + lane * 16u), "v"(ow[0]), "v"(ow[1]), "v"(ow[2]), "v"(ow[3]), "s"(out),
                    "v"(nodes - (lane >> 3))
                 : "memory", "vcc");
    SB();
    if (!check) return false;
    constexpr float WLIM = 1 << (22 - RA + VK);
    float wm = MAXN(Y[0][0], Y[1][0]);
#pragma unroll
    for (int i = 1; i < 16; ++i) wm = MAXN(MAXN(wm, Y[0][i]), Y[1][i]);
    f16x2 om = __builtin_bit_cast(f16x2, ov[0]);
#pragma unroll
    for (int i = 1; i < 16; ++i) om = MAXN(om, __builtin_bit_cast(f16x2, ov[i]));
    const _Float16 o1 = MAXN(om[0], om[1]);
    const unsigned long long lv = lm * 0x100000001ull;
    unsigned long long bad = __ballot(o1 != o1) | (__ballot(o1 < __builtin_bit_cast(_Float16, (unsigned short)((127 - 9 + VK) << 7))) & lv);
    if (__builtin_expect((__ballot(wm > WLIM / LCAP) & lv) != 0ull, 0)) {
      LAS unsigned* dg = (LAS unsigned*)(size_t)ot;
      dg[lane & 31] = 0u;
#pragma unroll 1
      for (int i = lane; i < cnt; i += 64)
        __hip_atomic_fetch_add(dg + (*(const LAS unsigned short*)(size_t)(la + i * 4 + 2) & 31), 1u, __ATOMIC_RELAXED,
                               __HIP_MEMORY_SCOPE_WORKGROUP);
      bad |= __ballot(wm * (float)dg[lane & 31] > WLIM) & lv;
    }
    return slow || bad != 0ull;
  };
  if (__builtin_expect(finish(true), 0)) {
    wide();
    finish(false);
  }
  if (bk == 0 && wave == 7) asm volatile("global_store_dwordx2 %0, %1, %2 sc1" :: "v"(0u), "v"(ep), "s"(FL) : "memory");
  asm volatile("s_waitcnt vmcnt(0)" ::: "memory");
}

extern "C" void kernel_launch(void* const* d_in, const int* in_sizes, int n_in,
                              void* d_out, int out_size, void* d_ws, size_t ws_size,
                              hipStream_t stream) {
  const unsigned short* x = (const unsigned short*)d_in[0];
  const u32x4* eattr = (const u32x4*)d_in[1];
  const int* src = (const int*)d_in[2];
  const int* dst = (const int*)d_in[3];
  const unsigned short* wa = (const unsigned short*)d_in[4];
  const unsigned short* wb = (const unsigned short*)d_in[5];
  const unsigned short* wc = (const unsigned short*)d_in[6];
  const unsigned short* wu = (const unsigned short*)d_in[7];
  const unsigned short* wv = (const unsigned short*)d_in[8];
  unsigned short* out = (unsigned short*)d_out;
  const int N = in_sizes[0] / 64;
  const int E = in_sizes[2];
  const int chunk = (((E + NCH - 1) / NCH) + 3) & ~3;

  const bool skew = N > 4096 && N < 60000;
  const unsigned M = N > 256 ? (unsigned)(4294967295.0 / ((double)N * (skew ? 1.0 + SKEWF : 1.0))) : 0xffffffu;
  const unsigned N1 = (0x7fffffffu / M) + 1u;
  const unsigned M2 = skew ? (0xffffffffu - N1 * M) / ((unsigned)N - N1) : M;

  auto fb = [](double v) { return __builtin_bit_cast(unsigned, (float)v); };
  char* ws = (char*)d_ws;
  size_t off = 0;
  off = 1 << 20;
  unsigned long long* FL = (unsigned long long*)(ws + off);   off += 1 << 20;
  u32x4* ENT = (u32x4*)(ws + off);                       off += (size_t)2 * NBK * IBOX;
  unsigned long long* TS = (unsigned long long*)(ws + off);

  k_main<<<NBK, 512, 0, stream>>>(x, eattr, src, dst, wa, wb, wc, wu, wv, ENT, TS, FL, out, N, E, chunk, M, N1,
                                  M2 - M, fb(65536.0 / M), fb(65536.0 / M2), fb(32768.0 / M + 0.5),
                                  fb((double)N1 + (32768.0 - (double)N1 * M) / M2 + 0.5)
     );
}
```

```cpp
#include <hip/hip_runtime.h>
#include <hip/hip_bf16.h>
#include <stdint.h>

using bf16x8 = __attribute__((ext_vector_type(8))) short;
using s16x4 = __attribute__((ext_vector_type(4))) short;
using f32x16 = __attribute__((ext_vector_type(16))) float;
using f32x2 = __attribute__((ext_vector_type(2))) float;
using u32x4 = __attribute__((ext_vector_type(4))) unsigned;
using u32x16 = __attribute__((ext_vector_type(16))) unsigned;
using u32x2 = __attribute__((ext_vector_type(2))) unsigned;
using f16x2 = __attribute__((ext_vector_type(2))) _Float16;

#define MFMA32(a, b, c) __builtin_amdgcn_mfma_f32_32x32x16_bf16((a), (b), (c), 0, 0, 0)
#define RA 14
#define VK 4
#define LAS __attribute__((address_space(3)))
#define RANKOF(w) ((w) < 4 ? 7 - (w) : (w) - 4)
#define G1W 4
#define CPF 32
#define CPFB 19712
#define SKEWF 0.015
#define XLIKE(c) __builtin_expect(c, 0)
#define SB() __builtin_amdgcn_sched_barrier(0)

constexpr int NCH = 256;
constexpr int NBK = 256;
constexpr int NFRAG = 39;
constexpr float NLOG2E = -1.44269504088896340736f;
constexpr float VSC = 1.0f / (1 << VK);
constexpr unsigned TPITCH = 80;

__device__ __forceinline__ unsigned pack_bf16(float lo, float hi) {
  typedef __attribute__((ext_vector_type(2))) float f2;
  typedef __attribute__((ext_vector_type(2))) __bf16 b2;
  f2 v = {lo, hi};
  b2 r = __builtin_convertvector(v, b2);
  return __builtin_bit_cast(unsigned, r);
}

__device__ __forceinline__ unsigned pack_f16(float lo, float hi) {
  unsigned r;
  asm("v_cvt_pk_f16_f32 %0, %1, %2" : "=v"(r) : "v"(lo), "v"(hi));
  return r;
}

__device__ __forceinline__ void glds16(const void* sbase, unsigned voff, unsigned lds_dst) {
  asm volatile(
      "s_mov_b32 m0, %2\n\t"
      "s_nop 0\n\t"
      "global_load_lds_dwordx4 %0, %1"
      :: "v"(voff), "s"(sbase), "s"(lds_dst) : "memory", "m0");
}
__device__ __forceinline__ void glds16x(const void* sbase, unsigned a, unsigned c, unsigned lds_dst) {
  unsigned tmp;
  asm volatile(
      "s_mov_b32 m0, %4\n\t"
      "v_mad_u32_u16 %0, %1, %5, %2\n\t"
      "global_load_lds_dwordx4 %0, %3"
      : "=&v"(tmp) : "v"(a), "v"(c), "s"(sbase), "s"(lds_dst), "s"(128u) : "memory", "m0");
}
__device__ __forceinline__ void glds16e(const void* sbase, unsigned a, unsigned c, unsigned lds_dst) {
  unsigned tmp;
  asm volatile(
      "s_mov_b32 m0, %4\n\t"
      "v_add_u32 %0, %1, %2\n\t"
      "global_load_lds_dwordx4 %0, %3"
      : "=&v"(tmp) : "v"(a), "v"(c), "s"(sbase), "s"(lds_dst) : "memory", "m0");
}
__device__ __forceinline__ void gstore128wt(void* sbase, unsigned voff, u32x4 v) {
  asm volatile("global_store_dwordx4 %0, %1, %2 sc1\n\ts_nop 1" :: "v"(voff), "v"(v), "s"(sbase) : "memory");
}

constexpr int IBOX = 40960;
constexpr int IEXT = 1 << 24;
constexpr int CPIT = 208;
constexpr unsigned RINC = 87382;
constexpr int KP_HDR = 0;
constexpr int KP_ENT = KP_HDR + NBK * 4;
constexpr int KP_POS = KP_ENT + NBK * CPIT;
constexpr int KP_LDS = KP_POS + NBK * 4;
constexpr int NWL = 26;
constexpr int NBLOB = 31;

__device__ __forceinline__ unsigned mul24s(unsigned a, unsigned b) {
  unsigned r;
  asm("v_mul_u32_u24 %0, %1, %2" : "=v"(r) : "s"(b), "v"(a));
  return r;
}
__device__ __forceinline__ unsigned byte3x(unsigned a, unsigned b) {
  unsigned r;
  asm("v_mul_u32_u24_sdwa %0, %1, %2 dst_sel:DWORD dst_unused:UNUSED_PAD src0_sel:DWORD src1_sel:BYTE_3" : "=v"(r) : "s"(b), "v"(a));
  return r;
}
constexpr int RING_SLOT = 5 * 1024;
constexpr int LCAP = 512;
constexpr int LARR = 2560;
constexpr int LIST_BYTES = 2 * LARR;
constexpr int OFF_CNT = 0;
constexpr int SUBL = LARR / 4;
constexpr int OFF_XF = 256;
constexpr int OFF_W = 512;
constexpr int OFF_T1 = OFF_W + NWL * 1024;
constexpr int OFF_T2 = OFF_T1 + 32 * TPITCH;
constexpr int OFF_R0 = OFF_W + NFRAG * 1024;
constexpr int OFF_R1 = OFF_R0 + 8 * RING_SLOT;
constexpr int OFF_L = OFF_R1 + 8 * RING_SLOT;
constexpr int MAIN_LDS = OFF_L + 8 * LIST_BYTES;
static_assert(MAIN_LDS <= 160 * 1024, "LDS");
static_assert(KP_LDS <= 16 * RING_SLOT, "phase 1 lives in the ring");

__device__ __forceinline__ unsigned ldd(const unsigned* p) {
  return __hip_atomic_load(p, __ATOMIC_RELAXED, __HIP_MEMORY_SCOPE_AGENT);
}
__device__ __forceinline__ unsigned long long ldq(const unsigned long long* p) {
  return __hip_atomic_load(p, __ATOMIC_RELAXED, __HIP_MEMORY_SCOPE_AGENT);
}
__device__ __forceinline__ bf16x8 rawfrag(unsigned a) {
  const s16x4 a0 = __builtin_amdgcn_ds_read_tr16_b64_v4i16((LAS s16x4*)(size_t)a);
  const s16x4 a1 = __builtin_amdgcn_ds_read_tr16_b64_v4i16((LAS s16x4*)(size_t)(a + 512));
  return __builtin_shufflevector(a0, a1, 0, 1, 2, 3, 4, 5, 6, 7);
}
#define MAXN(a, b) __builtin_elementwise_maximum(a, b)

__global__ __launch_bounds__(512, 2) void k_main(const unsigned short* __restrict__ x,
                                                 const u32x4* __restrict__ ES,
                                                 const int* __restrict__ src, const int* __restrict__ dst,
                                                 const unsigned short* __restrict__ wa_g,
                                                 const unsigned short* __restrict__ wb_g,
                                                 const unsigned short* __restrict__ wc_g,
                                                 const unsigned short* __restrict__ wu_g,
                                                 const unsigned short* __restrict__ wv_g,
                                                 u32x4* ENT, unsigned long long* TS, unsigned long long* FL,
                                                 unsigned short* __restrict__ out, int N, int E, int chunk,
                                                 unsigned M, unsigned N1, unsigned DM, unsigned MF0, unsigned MF1,
                                                 unsigned MB0, unsigned MB1
                                                 ) {
  __shared__ __attribute__((aligned(16))) char smem[MAIN_LDS];
  const int tid0 = threadIdx.x;
  const int wave = __builtin_amdgcn_readfirstlane(tid0 >> 6);
  const int bk = blockIdx.x;
  const unsigned lds0 = (unsigned)(size_t)(LAS char*)smem;

  LAS char* sm = (LAS char*)smem + OFF_R0;
  const unsigned sm0 = lds0 + OFF_R0;
  const int tid = tid0, lane = tid & 63, r = lane & 31, h = lane >> 5;
  const unsigned pc = tid & 7, bq = tid >> 3;
  unsigned ia = sm0 + KP_ENT + bq * CPIT + pc * 16;
#pragma unroll
  for (int s = 0; s < 4; ++s) *(LAS u32x4*)(size_t)(ia + s * 64 * CPIT) = u32x4{~0u, ~0u, ~0u, ~0u};
  *(LAS unsigned*)(sm + KP_HDR + (tid & 255) * 4) = 0u;
  int q0 = 4 * tid + 3, q1 = 4 * tid + 1795;
  unsigned a0 = tid * 16, a1 = tid * 16 + 7168, z0 = 0u;
  unsigned long long m7 = __ballot(tid == 448);
  int n7 = wave == 7 ? 0 : 0x7fffffff;
  unsigned f0 = lane * 128u, f1 = lane * 128u + 8192u, f2 = min(lane * 128u + 16384u, CPFB - 512u);
  unsigned long long p7 = __ballot((wave | (bk & -CPF)) == 7);
  asm volatile("" : "+v"(f0), "+v"(f1), "+v"(f2), "+s"(p7));
  asm volatile("" : "+v"(ia), "+v"(q0), "+v"(q1), "+v"(a0), "+v"(a1), "+v"(z0), "+s"(m7), "+s"(n7) :: "memory");
  SB();
  asm volatile("" :: "s"(x), "s"(ES), "s"(src), "s"(dst), "s"(wa_g), "s"(wb_g), "s"(wc_g), "s"(wu_g), "s"(wv_g), "s"(ENT),
               "s"(TS), "s"(FL), "s"(out), "s"(N), "s"(E), "s"(chunk), "s"(M), "s"(N1), "s"(DM), "s"(MF0), "s"(MF1), "s"(MB0), "s"(MB1));
  const int base = bk * chunk;
  const int ecnt = max(min(chunk, E - base), 0);
  unsigned long long on[2];
  on[0] = __ballot(q0 < min(min(chunk, E - base), n7));
  on[1] = __ballot(q1 < min(min(chunk, E - base), n7));
  a0 += base * 4u;
  a1 += base * 4u;
  u32x4 d[2], sv[2];
  u32x2 e0;
  asm volatile("s_mov_b64 exec, %11\n\t"
               "global_load_dwordx4 %0, %5, %8 nt\n\t"
               "s_mov_b64 exec, %12\n\t"
               "global_load_dwordx4 %1, %6, %8 nt\n\t"
               "s_mov_b64 exec, %13\n\t"
               "global_load_dwordx2 %2, %7, %9 sc1\n\t"
               "s_getpc_b64 s[96:97]\n\t"
               "s_mov_b32 m0, %17\n\t"
               "s_mov_b64 exec, %18\n\t"
               "global_load_lds_dword %14, s[96:97]\n\t"
               "global_load_lds_dword %15, s[96:97]\n\t"
               "global_load_lds_dword %16, s[96:97]\n\t"
               "s_mov_b64 exec, %11\n\t"
               "s_waitcnt lgkmcnt(0)\n\t"
               "s_barrier\n\t"
               "global_load_dwordx4 %3, %5, %10 nt\n\t"
               "s_mov_b64 exec, %12\n\t"
               "global_load_dwordx4 %4, %6, %10 nt\n\t"
               "s_mov_b64 exec, -1"
               : "=&{v[240:243]}"(d[0]), "=&{v[244:247]}"(d[1]), "=&{v[238:239]}"(e0), "=&{v[248:251]}"(sv[0]),
                 "=&{v[252:255]}"(sv[1])
               : "v"(a0), "v"(a1), "v"(z0), "s"(dst), "s"(FL), "s"(src), "s"(on[0]), "s"(on[1]), "s"(m7)
                 , "v"(f0), "v"(f1), "v"(f2), "n"(OFF_L + 7 * LIST_BYTES + 4096), "s"(p7)
               : "memory", "m0", "s96", "s97");
  SB();
  const int bu = (bk >> 1) | ((bk & 1) << 7);
  const unsigned Mw = bk & 1 ? M + DM : M, Cw = bk & 1 ? 0u - N1 * DM : 0u;
  unsigned MF = bk & 1 ? MF1 : MF0, MB = bk & 1 ? MB1 : MB0;
  asm volatile("" : "+s"(MF), "+s"(MB));
  char* ib = reinterpret_cast<char*>(ENT) + (size_t)bu * IBOX;
  auto nodeof = [&](unsigned c) { return min((unsigned)__builtin_fmaf((float)c, __uint_as_float(MF), __uint_as_float(MB)), (unsigned)N - 1u); };
  unsigned tup[8], val[8], pm[8];
  u32x4 xg[4];
  const unsigned xn0 = __builtin_amdgcn_readfirstlane(nodeof((bu * 8 + wave) * 32));
  unsigned xd[4];
  auto xprep = [&]() {
#pragma unroll
    for (int i = 0; i < 4; ++i)
      xd[i] = min(xn0 + 8 * i + (lane >> 3), N - 1u) * 128u + ((((lane & 7) ^ (lane >> 4)) * 16) ^ (i & 1) * 64) - i * 1024u;
    asm volatile("" : "+v"(xd[0]), "+v"(xd[1]), "+v"(xd[2]), "+v"(xd[3]));
  };
  auto xdma = [&]() {
    asm volatile("s_mov_b32 m0, %5\n\ts_nop 0\n\t"
                 "global_load_lds_dwordx4 %0, %4\n\t"
                 "global_load_lds_dwordx4 %1, %4 offset:1024\n\t"
                 "global_load_lds_dwordx4 %2, %4 offset:2048\n\t"
                 "global_load_lds_dwordx4 %3, %4 offset:3072"
                 :: "v"(xd[0]), "v"(xd[1]), "v"(xd[2]), "v"(xd[3]), "s"(x), "s"(lds0 + OFF_L + wave * LIST_BYTES)
                 : "memory", "m0");
  };
  auto wload = [&](const unsigned short* w, int f) {
    asm volatile("s_mov_b32 m0, %3\n\ts_nop 0\n\t"
                 "global_load_lds_dwordx4 %0, %2\n\t"
                 "global_load_lds_dwordx4 %0, %2 offset:1024\n\t"
                 "global_load_lds_dwordx4 %0, %2 offset:2048\n\t"
                 "global_load_lds_dwordx4 %0, %2 offset:3072\n\t"
                 "s_addk_i32 m0, 4096\n\ts_nop 0\n\t"
                 "global_load_lds_dwordx4 %1, %2\n\t"
                 "global_load_lds_dwordx4 %1, %2 offset:1024\n\t"
                 "global_load_lds_dwordx4 %1, %2 offset:2048\n\t"
                 "global_load_lds_dwordx4 %1, %2 offset:3072"
                 :: "v"(lane * 16u), "v"(lane * 16u + 4096u), "s"(w), "s"(lds0 + OFF_W + f * 1024) : "memory", "m0", "scc");
  };
  auto wall = [&]() {
    wload(wa_g, NBLOB);
    wload(wb_g, 0);
    wload(wv_g, 8);
    glds16(wc_g, lane * 16, lds0 + OFF_W + 16 * 1024);
    glds16(wc_g + 512, lane * 16, lds0 + OFF_W + 17 * 1024);
  };
  int spin = 1 << 20;
  u32x2 ep = {0u, 0u};
  if (wave == 7) {
    wall();
    *(LAS u32x4*)((LAS char*)smem + OFF_CNT + r * 16) =
        (r & 17) == 0 ? u32x4{lds0 + OFF_L, lds0 + OFF_L + SUBL, lds0 + OFF_L + 2 * SUBL, lds0 + OFF_L + 3 * SUBL}
                      : u32x4{0u, 0u, 0u, 0u};
    {
      const unsigned col = h ? ((r >> 1) & 12u) | ((r << 2) & 16u) | (r & 3u) : r;
      LAS char* tr = (LAS char*)smem + OFF_T1 + lane * TPITCH;
#pragma unroll
      for (int j = 0; j < 4; ++j) *(LAS u32x4*)(tr + j * 16) = u32x4{0u, 0u, 0u, 0u};
      *(LAS unsigned short*)(tr + col * 2) = 0x3C00;
    }
#pragma unroll
    for (int i = 0; i < 8; ++i) { tup[i] = 0u; val[i] = 0u; pm[i] = 0u; }
    asm volatile("s_waitcnt vmcnt(%1)" : "+{v[238:239]}"(e0) : "n"(31) : "memory");
    ep[0] = __builtin_amdgcn_readfirstlane(e0[0]) + 0x9E3779B1u;
    ep[1] = __builtin_amdgcn_readfirstlane(e0[1]) + 0x7F4A7C15u;
    *(LAS u32x2*)((LAS char*)smem + OFF_CNT + 56) = u32x2{ep[0], ep[1] >> 24};
    if (__builtin_expect(ecnt & 3, 0)) {
      const int i = (ecnt & ~3) + lane;
      on[0] = __ballot(i < ecnt);
      if (i < ecnt) {
        pm[0] = (unsigned)dst[base + i] * M + (unsigned)max(dst[base + i] - (int)N1, 0) * DM;
        tup[0] = (unsigned)src[base + i] | (unsigned)i << 16;
        val[0] = __hip_atomic_fetch_add((LAS unsigned*)(sm + KP_HDR) + (pm[0] >> 24), RINC, __ATOMIC_RELAXED, __HIP_MEMORY_SCOPE_WORKGROUP);
        const unsigned u = min(val[0] >> 16, 48u);
        *(LAS unsigned*)(sm + KP_ENT + (pm[0] >> 24) * CPIT + u * 4) = tup[0];
        *(LAS unsigned char*)(sm + KP_ENT + (pm[0] >> 24) * CPIT + ((u * 4) | (u & 3) | 12)) = pm[0] >> 16;
      }
      __builtin_amdgcn_s_waitcnt(0x0F70);
    }
    xprep();
    xdma();
  } else {
    xprep();
    unsigned inc = RINC, t0 = (unsigned)tid << 18;
    asm volatile("s_waitcnt vmcnt(2)" : "+{v[240:243]}"(d[0]), "+{v[244:247]}"(d[1]), "+v"(inc), "+v"(t0) :: "memory");
    unsigned cb[8];
#pragma unroll
    for (int s = 0; s < 2; ++s) {
      unsigned* rr = val + 4 * s;
#pragma unroll
      for (int k = 0; k < 4; ++k) {
        unsigned t;
        asm("v_sub_u32 %0, %1, %2 clamp" : "=v"(t) : "v"(d[s][k]), "s"(N1));
        asm("v_mad_u32_u24 %0, %1, %2, %3" : "=v"(pm[4 * s + k]) : "v"(t), "s"(DM), "v"(mul24s(d[s][k], M)));
      }
#pragma unroll
      for (int k = 0; k < 4; ++k) rr[k] = byte3x(pm[4 * s + k], 4u);
      asm volatile(
          "s_mov_b64 exec, %5\n\t"
          "ds_add_rtn_u32 %0, %0, %4 offset:%6\n\t"
          "ds_add_rtn_u32 %1, %1, %4 offset:%6\n\t"
          "ds_add_rtn_u32 %2, %2, %4 offset:%6\n\t"
          "ds_add_rtn_u32 %3, %3, %4 offset:%6\n\t"
          "s_mov_b64 exec, -1"
          : "+v"(rr[0]), "+v"(rr[1]), "+v"(rr[2]), "+v"(rr[3])
          : "v"(inc), "s"(on[s]), "n"(OFF_R0 + KP_HDR)
          : "memory");
    }
#pragma unroll
    for (int i = 0; i < 8; ++i) cb[i] = byte3x(pm[i], (unsigned)CPIT);
    asm volatile("s_waitcnt vmcnt(0)"
                 : "+{v[248:251]}"(sv[0]), "+{v[252:255]}"(sv[1]), "+v"(cb[0]), "+v"(cb[1]), "+v"(cb[2]), "+v"(cb[3]), "+v"(cb[4]), "+v"(cb[5]),
                   "+v"(cb[6]), "+v"(cb[7])
                 :: "memory");
    xdma();
#pragma unroll
    for (int i = 0; i < 8; ++i) tup[i] = sv[i >> 2][i & 3] + t0 + (((i >> 2) * 1792u + (i & 3)) << 16);
#pragma unroll
    for (int s = 0; s < 2; ++s) {
      asm volatile("s_waitcnt lgkmcnt(%8)"
                   : "+v"(val[4 * s]), "+v"(val[4 * s + 1]), "+v"(val[4 * s + 2]), "+v"(val[4 * s + 3]), "+v"(tup[4 * s]),
                     "+v"(tup[4 * s + 1]), "+v"(tup[4 * s + 2]), "+v"(tup[4 * s + 3])
                   : "n"(s ? 8 : 4) : "memory");
      unsigned pa[4], pb[4];
#pragma unroll
      for (int k = 0; k < 4; ++k) {
        const unsigned u = min(val[4 * s + k], 48u << 16);
        asm("v_mad_u32_u16 %0, %1, 4, %2 op_sel:[1,0,0,0]" : "=v"(pa[k]) : "v"(u), "v"(cb[4 * s + k]));
        pb[k] = pa[k] | ((u >> 16) & 3u) | 12u;
      }
      asm volatile(
          "s_mov_b64 exec, %16\n\t"
          "ds_write_b32 %0, %8 offset:%17\n\t"
          "ds_write_b8_d16_hi %4, %12 offset:%17\n\t"
          "ds_write_b32 %1, %9 offset:%17\n\t"
          "ds_write_b8_d16_hi %5, %13 offset:%17\n\t"
          "ds_write_b32 %2, %10 offset:%17\n\t"
          "ds_write_b8_d16_hi %6, %14 offset:%17\n\t"
          "ds_write_b32 %3, %11 offset:%17\n\t"
          "ds_write_b8_d16_hi %7, %15 offset:%17\n\t"
          "s_mov_b64 exec, -1"
          :: "v"(pa[0]), "v"(pa[1]), "v"(pa[2]), "v"(pa[3]), "v"(pb[0]), "v"(pb[1]), "v"(pb[2]), "v"(pb[3]),
             "v"(tup[4 * s]), "v"(tup[4 * s + 1]), "v"(tup[4 * s + 2]), "v"(tup[4 * s + 3]), "v"(pm[4 * s]),
             "v"(pm[4 * s + 1]), "v"(pm[4 * s + 2]), "v"(pm[4 * s + 3]), "s"(on[s]), "n"(OFF_R0 + KP_ENT)
          : "memory");
    }
  }
  u32x2 tag;
  int xw = 0;
  {
    unsigned ha = sm0 + KP_HDR + bq * 4, hb = sm0 + KP_HDR + lane * 16;
    unsigned go = bq * IBOX + bk * 128 + pc * 16;
    asm volatile("s_waitcnt lgkmcnt(0)\n\ts_barrier" : "+v"(ha), "+v"(hb), "+v"(go) :: "memory");
    tag = *(const LAS u32x2*)((LAS char*)smem + OFF_CNT + 56);
    const u32x4 call = *(const LAS u32x4*)(size_t)hb;
    unsigned cn[4];
    u32x4 v[4];
#pragma unroll
    for (int s = 0; s < 4; ++s) {
      cn[s] = *(const LAS unsigned*)(size_t)(ha + s * 256);
      v[s] = *(const LAS u32x4*)(size_t)(ia + s * 64 * CPIT);
    }
    asm volatile("" : "+v"(v[0]), "+v"(v[1]), "+v"(v[2]), "+v"(v[3]), "+v"(tag));
    if (__builtin_expect(__ballot(max(max(call[0], call[1]), max(call[2], call[3])) > 36u * RINC) != 0ull, 0)) {
      LAS unsigned* ovc = (LAS unsigned*)((LAS char*)smem + OFF_CNT + 24);
      LAS unsigned* pos = (LAS unsigned*)(sm + KP_POS);
      if (tid < NBK) {
        const unsigned u = *(const LAS unsigned*)(sm + KP_HDR + tid * 4) >> 16, n = u - (u >> 2);
        if (n > 36u) {
          const unsigned p = (unsigned)base + __hip_atomic_fetch_add(ovc, n - 36u, __ATOMIC_RELAXED, __HIP_MEMORY_SCOPE_WORKGROUP);
          pos[tid] = p;
          __hip_atomic_store(FL + 512 + tid * NCH + bk, ((unsigned long long)(n - 36u) << 32) | p, __ATOMIC_RELAXED,
                             __HIP_MEMORY_SCOPE_AGENT);
        }
      }
      asm volatile("s_waitcnt lgkmcnt(0)\n\ts_barrier" ::: "memory");
#pragma unroll
      for (int i = 0; i < 8; ++i) {
        const unsigned u = val[i] >> 16, n = u - (u >> 2);
        if (((on[i >> 2] >> lane) & 1ull) && n >= 36u)
          __hip_atomic_store(TS + pos[pm[i] >> 24] + (n - 36u), ((unsigned long long)((pm[i] >> 16) & 0xffu) << 32) | tup[i],
                             __ATOMIC_RELAXED, __HIP_MEMORY_SCOPE_AGENT);
      }
      __builtin_amdgcn_s_waitcnt(0x0F70);
      asm volatile("s_waitcnt vmcnt(0)\n\ts_barrier" ::: "memory");
    }
    auto seal = [&](u32x4& w, bool more) {
      w[3] = __builtin_amdgcn_perm(tag[1], w[3], 0x04020100u);
      unsigned sm_ = w[0] + w[1] + w[2] + w[3] + tag[0];
      sm_ += __builtin_amdgcn_update_dpp(0u, sm_, 0xB1, 0xf, 0xf, false);
      sm_ += __builtin_amdgcn_update_dpp(0u, sm_, 0x4E, 0xf, 0xf, false);
      sm_ += __builtin_amdgcn_update_dpp(0u, sm_, 0x141, 0xf, 0xf, false);
      w[2] = pc == 7u ? w[2] - sm_ + (more ? 1u : 0u) : w[2];
    };
#pragma unroll
    for (int s = 0; s < 4; ++s) seal(v[s], cn[s] > 23u * RINC);
    asm volatile("global_store_dwordx4 %0, %1, %5 sc1\n\t"
                 "global_store_dwordx4 %0, %2, %6 sc1\n\t"
                 "global_store_dwordx4 %0, %3, %7 sc1\n\t"
                 "global_store_dwordx4 %0, %4, %8 sc1\n\t"
                 "s_nop 1"
                 :: "v"(go), "v"(v[0]), "v"(v[1]), "v"(v[2]), "v"(v[3]), "s"(ENT), "s"(ENT + 4 * IBOX), "s"(ENT + 8 * IBOX),
                    "s"(ENT + 12 * IBOX)
                 : "memory");
    if (XLIKE(__ballot(max(max(cn[0], cn[1]), max(cn[2], cn[3])) > 23u * RINC) != 0ull)) {
      unsigned xm = 0u;
#pragma unroll
      for (int s = 0; s < 4; ++s) xm |= cn[s] > 23u * RINC ? 1u << s : 0u;
#pragma unroll 1
      do {
        const unsigned s = __builtin_ctz(xm | 8u);
        const unsigned c = *(const LAS unsigned*)(size_t)(ha + s * 256);
        u32x4 w = *(const LAS u32x4*)(size_t)(ia + s * (64 * CPIT) + (pc < 4u ? 128 : 0));
        const unsigned lim = pc < 4u ? (c >> 16) - 4u * pc : 0u;
        w[3] = pc == 7u ? w[3] >> 16 : w[3];
        w[0] = pc == 7u ? w[2] : 32u < lim ? w[0] : ~0u;
        w[1] = 33u < lim ? w[1] : ~0u;
        w[2] = 34u < lim ? w[2] : ~0u;
        seal(w, c > 36u * RINC);
        const unsigned ga = go + s * (64 * IBOX);
        if (xm) {
          w[3] += (((bk >> 3) & 3) + 1) << 24;
          gstore128wt(ENT + 2048 + ((bk >> 5) * 8 + (bk & 7) - bk) * 8, ga, w);
          w[3] -= (((bk >> 3) & 3) + 1) << 24;
          gstore128wt(ENT + IEXT / 16, ga, w);
        }
        xm &= xm - 1u;
        ++xw;
      } while (__ballot(xm != 0u) != 0ull);
    }
  }
  u32x4 cv[4];
  unsigned vo[4], eb[4];
  unsigned long long pend[4];
#pragma unroll
  for (int k = 0; k < 4; ++k) {
    vo[k] = wave * 4096u + k * 1024u + lane * 16u;
    eb[k] = (unsigned)((wave * 32 + k * 8 + (lane >> 3)) * chunk) * 32u;
  }
#define POLL0()                                                                                                       \
  asm volatile("global_load_dwordx4 %0, %4, %8 sc1\n\t"                                                               \
               "global_load_dwordx4 %1, %5, %8 sc1\n\t"                                                               \
               "global_load_dwordx4 %2, %6, %8 sc1\n\t"                                                               \
               "global_load_dwordx4 %3, %7, %8 sc1"                                                                   \
               : "=&{v[240:243]}"(cv[0]), "=&{v[244:247]}"(cv[1]), "=&{v[248:251]}"(cv[2]), "=&{v[252:255]}"(cv[3])   \
               : "v"(vo[0]), "v"(vo[1]), "v"(vo[2]), "v"(vo[3]), "s"(ib)                                              \
               : "memory")
  const f32x16 zero16 = {0.f, 0.f, 0.f, 0.f, 0.f, 0.f, 0.f, 0.f, 0.f, 0.f, 0.f, 0.f, 0.f, 0.f, 0.f, 0.f};
  const unsigned to = lds0 + OFF_W + ((lane >> 2) & 3) * 128 + (lane & 1) * 64 + ((lane >> 4) & 1) * 16 + ((lane >> 1) & 1) * 8;
  auto xa = [&]() {
    bf16x8 wa[8];
#pragma unroll
    for (int k = 0; k < 8; ++k) wa[k] = rawfrag(to + (NBLOB + (k >> 1)) * 1024 + h * 4096 + (k & 1) * 32);
    asm volatile("" : "+v"(wa[0]), "+v"(wa[1]), "+v"(wa[2]), "+v"(wa[3]), "+v"(wa[4]), "+v"(wa[5]), "+v"(wa[6]), "+v"(wa[7]),
                      "+{v[160:163]}"(xg[0]), "+{v[164:167]}"(xg[1]), "+{v[168:171]}"(xg[2]), "+{v[172:175]}"(xg[3]));
    f32x16 XA[2] = {zero16, zero16};
#pragma unroll
    for (int k = 0; k < 4; ++k)
#pragma unroll
      for (int ct = 0; ct < 2; ++ct) XA[ct] = MFMA32(__builtin_bit_cast(bf16x8, xg[k]), wa[k * 2 + ct], XA[ct]);
#pragma unroll
    for (int q = 0; q < 4; ++q) {
      u32x4 xp;
#pragma unroll
      for (int j = 0; j < 4; ++j)
        xp[j] = pack_f16(XA[q & 1][8 * (q >> 1) + 2 * j] * NLOG2E - RA, XA[q & 1][8 * (q >> 1) + 2 * j + 1] * NLOG2E - RA);
      *(LAS u32x4*)((LAS char*)smem + OFF_R1 + wave * RING_SLOT + q * 1024 + lane * 16) = xp;
    }
  };
  {
    auto prep = [&](unsigned f, unsigned hs, float scale) {
      u32x4 w[2];
#pragma unroll
      for (int ct = 0; ct < 2; ++ct) w[ct] = __builtin_bit_cast(u32x4, rawfrag(to + f + h * hs + ct * 32));
      asm volatile("s_waitcnt lgkmcnt(0)" : "+v"(w[0]), "+v"(w[1]) :: "memory");
#pragma unroll
      for (int ct = 0; ct < 2; ++ct) {
#pragma unroll
        for (int j = 0; j < 4; ++j)
          w[ct][j] = pack_bf16(__uint_as_float(w[ct][j] << 16) * scale, __uint_as_float(w[ct][j] & 0xffff0000u) * scale);
        *(LAS u32x4*)((LAS char*)smem + OFF_W + f + ct * hs + lane * 16) = w[ct];
      }
    };
    {
      const unsigned xr = min(nodeof((bu * 8 + wave) * 32 + r) - xn0, 31u);
      unsigned xa_ = lds0 + OFF_L + wave * LIST_BYTES + xr * 128 + (((4 * h) ^ ((xr >> 1) & 7)) * 16);
      asm volatile("s_cmp_eq_u32 %1, 1\n\ts_cbranch_scc1 1f\n\ts_waitcnt vmcnt(4)\n1:\n\ts_waitcnt vmcnt(6)"
                   : "+v"(xa_) : "s"(xw) : "memory", "scc");
#pragma unroll
      for (int t = 0; t < 4; ++t) xg[t] = *(const LAS u32x4*)(size_t)(xa_ ^ (t * 16));
      asm volatile("" : "+v"(xg[0]), "+v"(xg[1]), "+v"(xg[2]), "+v"(xg[3]) :: "memory");
      *(LAS unsigned*)((LAS char*)smem + OFF_L + wave * LIST_BYTES + h * LARR + r * 4) = ~0u;
    }
    asm volatile("s_waitcnt lgkmcnt(0)\n\ts_barrier"
                 : "+v"(vo[0]), "+v"(vo[1]), "+v"(vo[2]), "+v"(vo[3]), "+v"(eb[0]), "+v"(eb[1]), "+v"(eb[2]), "+v"(eb[3])
                 :: "memory");
    xa();
    {
      unsigned f = (wave + (wave & 4)) * 1024, hs = 4096;
      const float scale = wave < 4 ? NLOG2E : VSC;
#pragma unroll 1
      while (true) {
        asm volatile("" : "+s"(f), "+s"(hs));
        prep(f, hs, scale);
        if (f != 0u) break;
        f = 16 * 1024;
        hs = 1024;
      }
    }
    POLL0();
    glds16(wu_g + wave * 512, lane * 16, lds0 + OFF_W + (18 + wave) * 1024);
  }
  const unsigned l0 = lds0 + OFF_L;
  unsigned aLX = l0 + (lane >> 3) * 16;
  unsigned aLE = l0 + LARR + ((((lane >> 1) & 7) << 2) | (lane >> 4)) * 4;
  unsigned aLS4 = l0 + (4 * h + ((lane >> 2) & 3)) * 16;
  unsigned aLS1 = l0 + (((r & 7) << 2) | (r >> 3)) * 4 + 2;
  unsigned cx0 = (unsigned)(((lane & 7) ^ (lane >> 4)) * 16);
  unsigned cx1 = cx0 ^ 64u;
  unsigned ce = (unsigned)((((lane & 1) ^ ((lane >> 4) & 1))) * 16);
  const unsigned ringd = 8 * RING_SLOT;
  unsigned cS = lds0 + OFF_T1 + (((lane >> 4) & 1) * 16 + (lane & 3) * 4) * 2;
  unsigned cSt = lds0 + OFF_T2 + h * 32;
  unsigned fo[4];
#pragma unroll
  for (int t = 0; t < 4; ++t) fo[t] = lds0 + OFF_R0 + (unsigned)(r * 128 + (((4 * h + t) ^ ((r >> 1) & 7)) * 16));
  unsigned foe = lds0 + OFF_R0 + (unsigned)(4096 + r * 32 + ((h ^ ((r >> 3) & 1)) * 16));
  f32x16 Y[4];
#pragma unroll
  for (int q = 0; q < 4; ++q)
#pragma unroll
    for (int i = 0; i < 16; i += 2) {
      f32x2 y;
      if (q < 2) asm volatile("v_mov_b64 %0, %1" : "=v"(y) : "s"(0x358637bd358637bdull + (RA * 0x0080000000800000ull)));
      else asm volatile("v_mov_b64 %0, 0" : "=v"(y));
      Y[q][i] = y[0];
      Y[q][i + 1] = y[1];
    }
  constexpr unsigned cu = RA == 14 ? 0x04000400u : 0x02000200u;
  unsigned rid = lane & 7;
  asm volatile("" : "+v"(aLX), "+v"(aLE), "+v"(aLS4), "+v"(aLS1), "+v"(cx0), "+v"(cx1), "+v"(ce), "+v"(cS), "+v"(cSt),
                    "+v"(fo[0]), "+v"(fo[1]), "+v"(fo[2]), "+v"(fo[3]), "+v"(foe), "+v"(rid));
  asm volatile("" : "+{v[0:15]}"(Y[0]), "+{v[16:31]}"(Y[1]), "+{v[32:47]}"(Y[2]), "+{v[48:63]}"(Y[3]));
  auto ld128 = [&](unsigned a) { return *(const LAS u32x4*)(size_t)a; };
  auto gather5 = [&](const u32x4 sx, unsigned re, unsigned dstb) {
    glds16e(ES, re, ce, dstb + 4096);
    glds16x(x, sx[0], cx0, dstb);
    glds16x(x, sx[1], cx1, dstb + 1024);
    glds16x(x, sx[2], cx0, dstb + 2048);
    glds16x(x, sx[3], cx1, dstb + 3072);
  };
  const unsigned sq = min(4u * M, 0xffffffu);
  unsigned early, just;
  asm volatile(
        "v_mov_b32 v181, %18\n\t"
        "v_mov_b32 v180, 160\n\t"
        "v_mov_b32 v176, 0xe0\n\t"
        "v_mov_b32 v177, 0x0c040100\n\t"
        "v_mov_b32 v178, 0x0c050100\n\t"
        "v_mov_b32 v179, 0x0c060100\n\t"
        "s_mov_b32 s52, 0x7f7f7f7f\n\t"
        "s_mov_b32 s53, 0x7f7f7f7f\n\t"
        "s_mov_b64 s[54:55], 0\n\t"
        "s_mov_b32 %12, 0\n\t"
        "v_mov_b32 v191, 4\n\t"
        "s_waitcnt vmcnt(3)\n\t"
        "v_add3_u32 v228, v240, v241, v242\n\t"
        "v_add3_u32 v231, v244, v245, v246\n\t"
        "v_add3_u32 v228, v228, v243, %19\n\t"
        "v_add3_u32 v231, v231, v247, %19\n\t"
        "v_cmp_ne_u32_sdwa s[72:73], v243, %20 src0_sel:BYTE_3 src1_sel:DWORD\n\t"
        "v_add_u32_dpp v228, v228, v228 quad_perm:[1,0,3,2] row_mask:0xf bank_mask:0xf\n\t"
        "v_add_u32_dpp v231, v231, v231 quad_perm:[1,0,3,2] row_mask:0xf bank_mask:0xf\n\t"
        "v_cmp_ne_u32_sdwa s[78:79], v247, %20 src0_sel:BYTE_3 src1_sel:DWORD\n\t"
        "v_add_u32_dpp v228, v228, v228 quad_perm:[2,3,0,1] row_mask:0xf bank_mask:0xf\n\t"
        "v_add_u32_dpp v231, v231, v231 quad_perm:[2,3,0,1] row_mask:0xf bank_mask:0xf\n\t"
        "s_nop 0\n\t"
        "v_add_u32_dpp v228, v228, v228 row_half_mirror row_mask:0xf bank_mask:0xf\n\t"
        "v_add_u32_dpp v231, v231, v231 row_half_mirror row_mask:0xf bank_mask:0xf\n\t"
        "v_cmp_gt_u32 s[64:65], 2, v228\n\t"
        "v_cmp_eq_u32 s[56:57], 1, v228\n\t"
        "v_cmp_gt_u32 s[66:67], 2, v231\n\t"
        "v_cmp_eq_u32 s[58:59], 1, v231\n\t"
        "s_and_b64 s[72:73], s[72:73], s[64:65]\n\t"
        "s_cselect_b64 s[64:65], 0, s[64:65]\n\t"
        "s_and_b64 s[56:57], s[56:57], s[64:65]\n\t"
        "s_not_b64 %0, s[64:65]\n\t"
        "s_or_b64 s[54:55], s[54:55], %0\n\t"
        "s_and_b64 s[78:79], s[78:79], s[66:67]\n\t"
        "s_cselect_b64 s[66:67], 0, s[66:67]\n\t"
        "s_and_b64 s[58:59], s[58:59], s[66:67]\n\t"
        "s_not_b64 %1, s[66:67]\n\t"
        "s_or_b64 s[54:55], s[54:55], %1\n\t"
        "s_mov_b64 exec, s[64:65]\n\t"
        "v_cmpx_ne_u32 s[72:73], -1, v240\n\t"
        "v_mul_hi_u32_u24_sdwa v204, v240, %21 dst_sel:DWORD dst_unused:UNUSED_PAD src0_sel:WORD_0 src1_sel:DWORD\n\t"
        "v_and_b32_sdwa v192, v243, v176 dst_sel:DWORD dst_unused:UNUSED_PAD src0_sel:BYTE_0 src1_sel:DWORD\n\t"
        "v_lshl_add_u32 v204, v204, 2, v192\n\t"
        "ds_add_rtn_u32 v204, v204, v191\n\t"
        "v_perm_b32 v216, v243, v240, v177\n\t"
        "v_mad_u32_u16 v228, v240, 32, %13 op_sel:[1,0,0,0]\n\t"
        "v_cmpx_ne_u32 s[74:75], -1, v241\n\t"
        "v_mul_hi_u32_u24_sdwa v205, v241, %21 dst_sel:DWORD dst_unused:UNUSED_PAD src0_sel:WORD_0 src1_sel:DWORD\n\t"
        "v_and_b32_sdwa v193, v243, v176 dst_sel:DWORD dst_unused:UNUSED_PAD src0_sel:BYTE_1 src1_sel:DWORD\n\t"
        "v_lshl_add_u32 v205, v205, 2, v193\n\t"
        "ds_add_rtn_u32 v205, v205, v191\n\t"
        "v_perm_b32 v217, v243, v241, v178\n\t"
        "v_mad_u32_u16 v229, v241, 32, %13 op_sel:[1,0,0,0]\n\t"
        "s_and_b64 exec, exec, s[52:53]\n\t"
        "v_cmpx_ne_u32 s[76:77], -1, v242\n\t"
        "v_mul_hi_u32_u24_sdwa v206, v242, %21 dst_sel:DWORD dst_unused:UNUSED_PAD src0_sel:WORD_0 src1_sel:DWORD\n\t"
        "v_and_b32_sdwa v194, v243, v176 dst_sel:DWORD dst_unused:UNUSED_PAD src0_sel:BYTE_2 src1_sel:DWORD\n\t"
        "v_lshl_add_u32 v206, v206, 2, v194\n\t"
        "ds_add_rtn_u32 v206, v206, v191\n\t"
        "v_perm_b32 v218, v243, v242, v179\n\t"
        "v_mad_u32_u16 v230, v242, 32, %13 op_sel:[1,0,0,0]\n\t"
        "s_mov_b64 exec, s[66:67]\n\t"
        "v_cmpx_ne_u32 s[78:79], -1, v244\n\t"
        "v_mul_hi_u32_u24_sdwa v207, v244, %21 dst_sel:DWORD dst_unused:UNUSED_PAD src0_sel:WORD_0 src1_sel:DWORD\n\t"
        "v_and_b32_sdwa v195, v247, v176 dst_sel:DWORD dst_unused:UNUSED_PAD src0_sel:BYTE_0 src1_sel:DWORD\n\t"
        "v_lshl_add_u32 v207, v207, 2, v195\n\t"
        "ds_add_rtn_u32 v207, v207, v191\n\t"
        "v_perm_b32 v219, v247, v244, v177\n\t"
        "v_mad_u32_u16 v231, v244, 32, %14 op_sel:[1,0,0,0]\n\t"
        "v_cmpx_ne_u32 s[80:81], -1, v245\n\t"
        "v_mul_hi_u32_u24_sdwa v208, v245, %21 dst_sel:DWORD dst_unused:UNUSED_PAD src0_sel:WORD_0 src1_sel:DWORD\n\t"
        "v_and_b32_sdwa v196, v247, v176 dst_sel:DWORD dst_unused:UNUSED_PAD src0_sel:BYTE_1 src1_sel:DWORD\n\t"
        "v_lshl_add_u32 v208, v208, 2, v196\n\t"
        "ds_add_rtn_u32 v208, v208, v191\n\t"
        "v_perm_b32 v220, v247, v245, v178\n\t"
        "v_mad_u32_u16 v232, v245, 32, %14 op_sel:[1,0,0,0]\n\t"
        "s_and_b64 exec, exec, s[52:53]\n\t"
        "v_cmpx_ne_u32 s[82:83], -1, v246\n\t"
        "v_mul_hi_u32_u24_sdwa v209, v246, %21 dst_sel:DWORD dst_unused:UNUSED_PAD src0_sel:WORD_0 src1_sel:DWORD\n\t"
        "v_and_b32_sdwa v197, v247, v176 dst_sel:DWORD dst_unused:UNUSED_PAD src0_sel:BYTE_2 src1_sel:DWORD\n\t"
        "v_lshl_add_u32 v209, v209, 2, v197\n\t"
        "ds_add_rtn_u32 v209, v209, v191\n\t"
        "v_perm_b32 v221, v247, v246, v179\n\t"
        "v_mad_u32_u16 v233, v246, 32, %14 op_sel:[1,0,0,0]\n\t"
        "s_mov_b64 exec, -1\n\t"
        "s_waitcnt vmcnt(1)\n\t"
        "v_add3_u32 v234, v248, v249, v250\n\t"
        "v_add3_u32 v237, v252, v253, v254\n\t"
        "v_add3_u32 v234, v234, v251, %19\n\t"
        "v_add3_u32 v237, v237, v255, %19\n\t"
        "v_cmp_ne_u32_sdwa s[84:85], v251, %20 src0_sel:BYTE_3 src1_sel:DWORD\n\t"
        "v_add_u32_dpp v234, v234, v234 quad_perm:[1,0,3,2] row_mask:0xf bank_mask:0xf\n\t"
        "v_add_u32_dpp v237, v237, v237 quad_perm:[1,0,3,2] row_mask:0xf bank_mask:0xf\n\t"
        "v_cmp_ne_u32_sdwa s[90:91], v255, %20 src0_sel:BYTE_3 src1_sel:DWORD\n\t"
        "v_add_u32_dpp v234, v234, v234 quad_perm:[2,3,0,1] row_mask:0xf bank_mask:0xf\n\t"
        "v_add_u32_dpp v237, v237, v237 quad_perm:[2,3,0,1] row_mask:0xf bank_mask:0xf\n\t"
        "s_nop 0\n\t"
        "v_add_u32_dpp v234, v234, v234 row_half_mirror row_mask:0xf bank_mask:0xf\n\t"
        "v_add_u32_dpp v237, v237, v237 row_half_mirror row_mask:0xf bank_mask:0xf\n\t"
        "v_cmp_gt_u32 s[68:69], 2, v234\n\t"
        "v_cmp_eq_u32 s[60:61], 1, v234\n\t"
        "v_cmp_gt_u32 s[70:71], 2, v237\n\t"
        "v_cmp_eq_u32 s[62:63], 1, v237\n\t"
        "s_and_b64 s[84:85], s[84:85], s[68:69]\n\t"
        "s_cselect_b64 s[68:69], 0, s[68:69]\n\t"
        "s_and_b64 s[60:61], s[60:61], s[68:69]\n\t"
        "s_not_b64 %2, s[68:69]\n\t"
        "s_or_b64 s[54:55], s[54:55], %2\n\t"
        "s_and_b64 s[90:91], s[90:91], s[70:71]\n\t"
        "s_cselect_b64 s[70:71], 0, s[70:71]\n\t"
        "s_and_b64 s[62:63], s[62:63], s[70:71]\n\t"
        "s_not_b64 %3, s[70:71]\n\t"
        "s_or_b64 s[54:55], s[54:55], %3\n\t"
        "s_or_b64 s[50:51], s[56:57], s[58:59]\n\t"
        "s_or_b64 exec, s[60:61], s[62:63]\n\t"
        "s_or_b64 s[50:51], s[50:51], exec\n\t"
        "s_cbranch_scc0 57f\n\t"
        "s_mov_b64 exec, s[50:51]\n\t"
        "global_load_dwordx4 v[172:175], %30, %17 sc1\n\t"
        "57:\n\t"
        "s_mov_b64 exec, s[72:73]\n\t"
        "s_waitcnt lgkmcnt(3)\n\t"
        "v_min_u32 v204, v181, v204\n\t"
        "v_min_u32 v205, v181, v205\n\t"
        "v_min_u32 v206, v181, v206\n\t"
        "v_mad_u32_u24 v204, v192, v180, v204\n\t"
        "v_mad_u32_u24 v205, v193, v180, v205\n\t"
        "v_mad_u32_u24 v206, v194, v180, v206\n\t"
        "ds_write2st64_b32 v204, v216, v228 offset1:10\n\t"
        "s_mov_b64 exec, s[74:75]\n\t"
        "ds_write2st64_b32 v205, v217, v229 offset1:10\n\t"
        "s_mov_b64 exec, s[76:77]\n\t"
        "ds_write2st64_b32 v206, v218, v230 offset1:10\n\t"
        "s_mov_b64 exec, s[68:69]\n\t"
        "v_cmpx_ne_u32 s[84:85], -1, v248\n\t"
        "v_mul_hi_u32_u24_sdwa v210, v248, %21 dst_sel:DWORD dst_unused:UNUSED_PAD src0_sel:WORD_0 src1_sel:DWORD\n\t"
        "v_and_b32_sdwa v198, v251, v176 dst_sel:DWORD dst_unused:UNUSED_PAD src0_sel:BYTE_0 src1_sel:DWORD\n\t"
        "v_lshl_add_u32 v210, v210, 2, v198\n\t"
        "ds_add_rtn_u32 v210, v210, v191\n\t"
        "v_perm_b32 v222, v251, v248, v177\n\t"
        "v_mad_u32_u16 v234, v248, 32, %15 op_sel:[1,0,0,0]\n\t"
        "v_cmpx_ne_u32 s[86:87], -1, v249\n\t"
        "v_mul_hi_u32_u24_sdwa v211, v249, %21 dst_sel:DWORD dst_unused:UNUSED_PAD src0_sel:WORD_0 src1_sel:DWORD\n\t"
        "v_and_b32_sdwa v199, v251, v176 dst_sel:DWORD dst_unused:UNUSED_PAD src0_sel:BYTE_1 src1_sel:DWORD\n\t"
        "v_lshl_add_u32 v211, v211, 2, v199\n\t"
        "ds_add_rtn_u32 v211, v211, v191\n\t"
        "v_perm_b32 v223, v251, v249, v178\n\t"
        "v_mad_u32_u16 v235, v249, 32, %15 op_sel:[1,0,0,0]\n\t"
        "s_and_b64 exec, exec, s[52:53]\n\t"
        "v_cmpx_ne_u32 s[88:89], -1, v250\n\t"
        "v_mul_hi_u32_u24_sdwa v212, v250, %21 dst_sel:DWORD dst_unused:UNUSED_PAD src0_sel:WORD_0 src1_sel:DWORD\n\t"
        "v_and_b32_sdwa v200, v251, v176 dst_sel:DWORD dst_unused:UNUSED_PAD src0_sel:BYTE_2 src1_sel:DWORD\n\t"
        "v_lshl_add_u32 v212, v212, 2, v200\n\t"
        "ds_add_rtn_u32 v212, v212, v191\n\t"
        "v_perm_b32 v224, v251, v250, v179\n\t"
        "v_mad_u32_u16 v236, v250, 32, %15 op_sel:[1,0,0,0]\n\t"
        "s_mov_b64 exec, s[78:79]\n\t"
        "s_waitcnt lgkmcnt(6)\n\t"
        "v_min_u32 v207, v181, v207\n\t"
        "v_min_u32 v208, v181, v208\n\t"
        "v_min_u32 v209, v181, v209\n\t"
        "v_mad_u32_u24 v207, v195, v180, v207\n\t"
        "v_mad_u32_u24 v208, v196, v180, v208\n\t"
        "v_mad_u32_u24 v209, v197, v180, v209\n\t"
        "ds_write2st64_b32 v207, v219, v231 offset1:10\n\t"
        "s_mov_b64 exec, s[80:81]\n\t"
        "ds_write2st64_b32 v208, v220, v232 offset1:10\n\t"
        "s_mov_b64 exec, s[82:83]\n\t"
        "ds_write2st64_b32 v209, v221, v233 offset1:10\n\t"
        "s_mov_b64 exec, -1\n\t"
        "ds_read_b128 v[184:187], %22\n\t"
        "ds_read_b32 v183, %23\n\t"
        "s_mov_b64 exec, s[70:71]\n\t"
        "v_cmpx_ne_u32 s[90:91], -1, v252\n\t"
        "v_mul_hi_u32_u24_sdwa v213, v252, %21 dst_sel:DWORD dst_unused:UNUSED_PAD src0_sel:WORD_0 src1_sel:DWORD\n\t"
        "v_and_b32_sdwa v201, v255, v176 dst_sel:DWORD dst_unused:UNUSED_PAD src0_sel:BYTE_0 src1_sel:DWORD\n\t"
        "v_lshl_add_u32 v213, v213, 2, v201\n\t"
        "ds_add_rtn_u32 v213, v213, v191\n\t"
        "v_perm_b32 v225, v255, v252, v177\n\t"
        "v_mad_u32_u16 v237, v252, 32, %16 op_sel:[1,0,0,0]\n\t"
        "v_cmpx_ne_u32 s[92:93], -1, v253\n\t"
        "v_mul_hi_u32_u24_sdwa v214, v253, %21 dst_sel:DWORD dst_unused:UNUSED_PAD src0_sel:WORD_0 src1_sel:DWORD\n\t"
        "v_and_b32_sdwa v202, v255, v176 dst_sel:DWORD dst_unused:UNUSED_PAD src0_sel:BYTE_1 src1_sel:DWORD\n\t"
        "v_lshl_add_u32 v214, v214, 2, v202\n\t"
        "ds_add_rtn_u32 v214, v214, v191\n\t"
        "v_perm_b32 v226, v255, v253, v178\n\t"
        "v_mad_u32_u16 v238, v253, 32, %16 op_sel:[1,0,0,0]\n\t"
        "s_and_b64 exec, exec, s[52:53]\n\t"
        "v_cmpx_ne_u32 s[94:95], -1, v254\n\t"
        "v_mul_hi_u32_u24_sdwa v215, v254, %21 dst_sel:DWORD dst_unused:UNUSED_PAD src0_sel:WORD_0 src1_sel:DWORD\n\t"
        "v_and_b32_sdwa v203, v255, v176 dst_sel:DWORD dst_unused:UNUSED_PAD src0_sel:BYTE_2 src1_sel:DWORD\n\t"
        "v_lshl_add_u32 v215, v215, 2, v203\n\t"
        "ds_add_rtn_u32 v215, v215, v191\n\t"
        "v_perm_b32 v227, v255, v254, v179\n\t"
        "v_mad_u32_u16 v239, v254, 32, %16 op_sel:[1,0,0,0]\n\t"
        "s_cmp_eq_u64 s[54:55], 0\n\t"
        "s_cbranch_scc1 59f\n\t"
        "s_mov_b64 exec, %0\n\t"
        "s_cbranch_execz 50f\n\t"
        "global_load_dwordx4 v[240:243], %4, %17 sc1\n\t"
        "50:\n\t"
        "s_mov_b64 exec, %1\n\t"
        "s_cbranch_execz 51f\n\t"
        "global_load_dwordx4 v[244:247], %5, %17 sc1\n\t"
        "51:\n\t"
        "s_mov_b64 exec, %2\n\t"
        "s_cbranch_execz 52f\n\t"
        "global_load_dwordx4 v[248:251], %6, %17 sc1\n\t"
        "52:\n\t"
        "s_mov_b64 exec, %3\n\t"
        "s_cbranch_execz 53f\n\t"
        "global_load_dwordx4 v[252:255], %7, %17 sc1\n\t"
        "53:\n\t"
        "59:\n\t"
        "s_mov_b64 exec, s[84:85]\n\t"
        "s_waitcnt lgkmcnt(8)\n\t"
        "v_min_u32 v210, v181, v210\n\t"
        "v_min_u32 v211, v181, v211\n\t"
        "v_min_u32 v212, v181, v212\n\t"
        "v_mad_u32_u24 v210, v198, v180, v210\n\t"
        "v_mad_u32_u24 v211, v199, v180, v211\n\t"
        "v_mad_u32_u24 v212, v200, v180, v212\n\t"
        "ds_write2st64_b32 v210, v222, v234 offset1:10\n\t"
        "s_mov_b64 exec, s[86:87]\n\t"
        "ds_write2st64_b32 v211, v223, v235 offset1:10\n\t"
        "s_mov_b64 exec, s[88:89]\n\t"
        "ds_write2st64_b32 v212, v224, v236 offset1:10\n\t"
        "s_mov_b64 exec, -1\n\t"
        "s_movk_i32 s96, 128\n\t"
        "s_mov_b32 m0, %29\n\t"
        "s_waitcnt lgkmcnt(6)\n\t"
        "v_max3_u32 v182, v184, v185, v186\n\t"
        "v_max3_u32 v182, v182, v187, v183\n\t"
        "v_cmp_eq_u32 vcc, -1, v182\n\t"
        "v_mad_u32_u16 v184, v184, s96, %24\n\t"
        "v_mad_u32_u16 v185, v185, s96, %25\n\t"
        "v_mad_u32_u16 v186, v186, s96, %24\n\t"
        "v_mad_u32_u16 v187, v187, s96, %25\n\t"
        "v_add_u32 v183, v183, %26\n\t"
        "s_cbranch_vccnz 60f\n\t"
        "global_load_lds_dwordx4 v184, %27\n\t"
        "s_addk_i32 m0, 1024\n\t"
        "s_nop 0\n\t"
        "global_load_lds_dwordx4 v185, %27\n\t"
        "s_addk_i32 m0, 1024\n\t"
        "s_nop 0\n\t"
        "global_load_lds_dwordx4 v186, %27\n\t"
        "s_addk_i32 m0, 1024\n\t"
        "s_nop 0\n\t"
        "global_load_lds_dwordx4 v187, %27\n\t"
        "s_addk_i32 m0, 1024\n\t"
        "s_nop 0\n\t"
        "global_load_lds_dwordx4 v183, %28\n\t"
        "s_mov_b32 %12, 1\n\t"
        "60:\n\t"
        "s_mov_b64 exec, s[90:91]\n\t"
        "s_waitcnt lgkmcnt(3)\n\t"
        "v_min_u32 v213, v181, v213\n\t"
        "v_min_u32 v214, v181, v214\n\t"
        "v_min_u32 v215, v181, v215\n\t"
        "v_mad_u32_u24 v213, v201, v180, v213\n\t"
        "v_mad_u32_u24 v214, v202, v180, v214\n\t"
        "v_mad_u32_u24 v215, v203, v180, v215\n\t"
        "ds_write2st64_b32 v213, v225, v237 offset1:10\n\t"
        "s_mov_b64 exec, s[92:93]\n\t"
        "ds_write2st64_b32 v214, v226, v238 offset1:10\n\t"
        "s_mov_b64 exec, s[94:95]\n\t"
        "ds_write2st64_b32 v215, v227, v239 offset1:10\n\t"
        "s_cmp_eq_u64 s[50:51], 0\n\t"
        "s_cbranch_scc1 49f\n\t"
        "s_mov_b64 exec, s[56:57]\n\t"
        "v_mov_b32 v188, 0xff000000\n\t"
        "v_mov_b32 v189, %13\n\t"
        "v_bfe_u32 v190, %4, 7, 8\n\t"
        "ds_write_b8 v190, v191 offset:256\n\t"
        "s_mov_b64 exec, s[58:59]\n\t"
        "v_mov_b32 v188, 0xfe000000\n\t"
        "v_mov_b32 v189, %14\n\t"
        "v_bfe_u32 v190, %5, 7, 8\n\t"
        "ds_write_b8 v190, v191 offset:256\n\t"
        "s_mov_b64 exec, s[60:61]\n\t"
        "v_mov_b32 v188, 0xfd000000\n\t"
        "v_mov_b32 v189, %15\n\t"
        "v_bfe_u32 v190, %6, 7, 8\n\t"
        "ds_write_b8 v190, v191 offset:256\n\t"
        "s_mov_b64 exec, s[62:63]\n\t"
        "v_mov_b32 v188, 0xfc000000\n\t"
        "v_mov_b32 v189, %16\n\t"
        "v_bfe_u32 v190, %7, 7, 8\n\t"
        "ds_write_b8 v190, v191 offset:256\n\t"
        "s_mov_b64 exec, s[50:51]\n\t"
        "s_waitcnt vmcnt(5)\n\t"
        "s_cmp_lg_u32 %12, 0\n\t"
        "s_cbranch_scc1 47f\n\t"
        "s_waitcnt vmcnt(0)\n\t"
        "47:\n\t"
        "v_add_u32 v175, v175, v188\n\t"
        "v_add3_u32 v228, v172, v173, v174\n\t"
        "v_add3_u32 v228, v228, v175, %19\n\t"
        "v_cmp_ne_u32_sdwa s[72:73], v175, %20 src0_sel:BYTE_3 src1_sel:DWORD\n\t"
        "s_nop 0\n\t"
        "v_add_u32_dpp v228, v228, v228 quad_perm:[1,0,3,2] row_mask:0xf bank_mask:0xf\n\t"
        "s_nop 1\n\t"
        "v_add_u32_dpp v228, v228, v228 quad_perm:[2,3,0,1] row_mask:0xf bank_mask:0xf\n\t"
        "s_nop 1\n\t"
        "v_add_u32_dpp v228, v228, v228 row_half_mirror row_mask:0xf bank_mask:0xf\n\t"
        "v_cmp_eq_u32 s[68:69], 0, v228\n\t"
        "s_and_b64 s[72:73], s[72:73], s[68:69]\n\t"
        "s_cselect_b64 exec, 0, s[68:69]\n\t"
        "s_cbranch_execz 48f\n\t"
        "v_cmpx_ne_u32 s[72:73], -1, v172\n\t"
        "v_mul_hi_u32_u24_sdwa v204, v172, %21 dst_sel:DWORD dst_unused:UNUSED_PAD src0_sel:WORD_0 src1_sel:DWORD\n\t"
        "v_and_b32_sdwa v192, v175, v176 dst_sel:DWORD dst_unused:UNUSED_PAD src0_sel:BYTE_0 src1_sel:DWORD\n\t"
        "v_lshl_add_u32 v204, v204, 2, v192\n\t"
        "ds_add_rtn_u32 v204, v204, v191\n\t"
        "v_perm_b32 v216, v175, v172, v177\n\t"
        "v_mad_u32_u16 v228, v172, 32, v189 op_sel:[1,0,0,0]\n\t"
        "v_cmpx_ne_u32 s[74:75], -1, v173\n\t"
        "v_mul_hi_u32_u24_sdwa v205, v173, %21 dst_sel:DWORD dst_unused:UNUSED_PAD src0_sel:WORD_0 src1_sel:DWORD\n\t"
        "v_and_b32_sdwa v193, v175, v176 dst_sel:DWORD dst_unused:UNUSED_PAD src0_sel:BYTE_1 src1_sel:DWORD\n\t"
        "v_lshl_add_u32 v205, v205, 2, v193\n\t"
        "ds_add_rtn_u32 v205, v205, v191\n\t"
        "v_perm_b32 v217, v175, v173, v178\n\t"
        "v_mad_u32_u16 v229, v173, 32, v189 op_sel:[1,0,0,0]\n\t"
        "s_and_b64 exec, exec, s[52:53]\n\t"
        "v_cmpx_ne_u32 s[76:77], -1, v174\n\t"
        "v_mul_hi_u32_u24_sdwa v206, v174, %21 dst_sel:DWORD dst_unused:UNUSED_PAD src0_sel:WORD_0 src1_sel:DWORD\n\t"
        "v_and_b32_sdwa v194, v175, v176 dst_sel:DWORD dst_unused:UNUSED_PAD src0_sel:BYTE_2 src1_sel:DWORD\n\t"
        "v_lshl_add_u32 v206, v206, 2, v194\n\t"
        "ds_add_rtn_u32 v206, v206, v191\n\t"
        "v_perm_b32 v218, v175, v174, v179\n\t"
        "v_mad_u32_u16 v230, v174, 32, v189 op_sel:[1,0,0,0]\n\t"
        "s_mov_b64 s[50:51], 0\n\t"
        "s_and_b64 s[70:71], s[68:69], s[62:63]\n\t"
        "s_xor_b64 s[62:63], s[62:63], s[70:71]\n\t"
        "s_xor_b64 s[68:69], s[68:69], s[70:71]\n\t"
        "s_or_b64 s[50:51], s[50:51], s[62:63]\n\t"
        "s_and_b64 s[70:71], s[68:69], s[60:61]\n\t"
        "s_xor_b64 s[60:61], s[60:61], s[70:71]\n\t"
        "s_xor_b64 s[68:69], s[68:69], s[70:71]\n\t"
        "s_or_b64 s[50:51], s[50:51], s[60:61]\n\t"
        "s_and_b64 s[70:71], s[68:69], s[58:59]\n\t"
        "s_xor_b64 s[58:59], s[58:59], s[70:71]\n\t"
        "s_xor_b64 s[68:69], s[68:69], s[70:71]\n\t"
        "s_or_b64 s[50:51], s[50:51], s[58:59]\n\t"
        "s_and_b64 s[70:71], s[68:69], s[56:57]\n\t"
        "s_xor_b64 s[56:57], s[56:57], s[70:71]\n\t"
        "s_or_b64 s[50:51], s[50:51], s[56:57]\n\t"
        "s_mov_b64 exec, s[72:73]\n\t"
        "s_waitcnt lgkmcnt(0)\n\t"
        "v_min_u32 v204, v181, v204\n\t"
        "v_min_u32 v205, v181, v205\n\t"
        "v_min_u32 v206, v181, v206\n\t"
        "v_mad_u32_u24 v204, v192, v180, v204\n\t"
        "v_mad_u32_u24 v205, v193, v180, v205\n\t"
        "v_mad_u32_u24 v206, v194, v180, v206\n\t"
        "ds_write2st64_b32 v204, v216, v228 offset1:10\n\t"
        "s_mov_b64 exec, s[74:75]\n\t"
        "ds_write2st64_b32 v205, v217, v229 offset1:10\n\t"
        "s_mov_b64 exec, s[76:77]\n\t"
        "ds_write2st64_b32 v206, v218, v230 offset1:10\n\t"
        "s_cbranch_scc0 49f\n\t"
        "48:\n\t"
        "s_mov_b32 s96, 0x1000000\n\t"
        "s_or_b32 %12, %12, 2\n\t"
        "s_mov_b64 exec, s[56:57]\n\t"
        "s_cbranch_execz 40f\n\t"
        "v_add_u32 %4, s96, %4\n\t"
        "s_or_b64 %0, %0, exec\n\t"
        "global_load_dwordx4 v[240:243], %4, %17 sc1\n\t"
        "40:\n\t"
        "s_mov_b64 exec, s[58:59]\n\t"
        "s_cbranch_execz 41f\n\t"
        "v_add_u32 %5, s96, %5\n\t"
        "s_or_b64 %1, %1, exec\n\t"
        "global_load_dwordx4 v[244:247], %5, %17 sc1\n\t"
        "41:\n\t"
        "s_mov_b64 exec, s[60:61]\n\t"
        "s_cbranch_execz 42f\n\t"
        "v_add_u32 %6, s96, %6\n\t"
        "s_or_b64 %2, %2, exec\n\t"
        "global_load_dwordx4 v[248:251], %6, %17 sc1\n\t"
        "42:\n\t"
        "s_mov_b64 exec, s[62:63]\n\t"
        "s_cbranch_execz 43f\n\t"
        "v_add_u32 %7, s96, %7\n\t"
        "s_or_b64 %3, %3, exec\n\t"
        "global_load_dwordx4 v[252:255], %7, %17 sc1\n\t"
        "43:\n\t"
        "49:\n\t"
        "s_or_b32 s54, s54, s55\n\t"
        "s_or_b32 s54, s54, %12\n\t"
        "s_cbranch_scc1 60f\n\t"
        "s_movk_i32 s54, 12\n\t"
        "61:\n\t"
        "s_sub_u32 s54, s54, 1\n\t"
        "s_cbranch_scc1 60f\n\t"
        "s_mov_b64 exec, -1\n\t"
        "ds_read_b128 v[184:187], %22\n\t"
        "ds_read_b32 v183, %23\n\t"
        "s_mov_b64 exec, -1\n\t"
        "s_movk_i32 s96, 128\n\t"
        "s_mov_b32 m0, %29\n\t"
        "s_waitcnt lgkmcnt(0)\n\t"
        "v_max3_u32 v182, v184, v185, v186\n\t"
        "v_max3_u32 v182, v182, v187, v183\n\t"
        "v_cmp_eq_u32 vcc, -1, v182\n\t"
        "v_mad_u32_u16 v184, v184, s96, %24\n\t"
        "v_mad_u32_u16 v185, v185, s96, %25\n\t"
        "v_mad_u32_u16 v186, v186, s96, %24\n\t"
        "v_mad_u32_u16 v187, v187, s96, %25\n\t"
        "v_add_u32 v183, v183, %26\n\t"
        "s_cbranch_vccnz 61b\n\t"
        "global_load_lds_dwordx4 v184, %27\n\t"
        "s_addk_i32 m0, 1024\n\t"
        "s_nop 0\n\t"
        "global_load_lds_dwordx4 v185, %27\n\t"
        "s_addk_i32 m0, 1024\n\t"
        "s_nop 0\n\t"
        "global_load_lds_dwordx4 v186, %27\n\t"
        "s_addk_i32 m0, 1024\n\t"
        "s_nop 0\n\t"
        "global_load_lds_dwordx4 v187, %27\n\t"
        "s_addk_i32 m0, 1024\n\t"
        "s_nop 0\n\t"
        "global_load_lds_dwordx4 v183, %28\n\t"
        "s_mov_b32 %12, 1\n\t"
        "60:\n\t"
        "s_mov_b64 exec, -1\n\t"
        : "=&s"(pend[0]), "=&s"(pend[1]), "=&s"(pend[2]), "=&s"(pend[3]), "+v"(vo[0]), "+v"(vo[1]), "+v"(vo[2]),
          "+v"(vo[3]), "+{v[240:243]}"(cv[0]), "+{v[244:247]}"(cv[1]), "+{v[248:251]}"(cv[2]), "+{v[252:255]}"(cv[3]),
          "=&s"(early)
        : "v"(eb[0]), "v"(eb[1]), "v"(eb[2]), "v"(eb[3]), "s"(ib), "n"(OFF_L + LARR - 4), "v"(tag[0]), "v"(tag[1]),
          "s"(sq), "v"(aLX + wave * LIST_BYTES), "v"(aLE + wave * LIST_BYTES), "v"(cx0), "v"(cx1), "v"(ce), "s"(x),
          "s"(ES), "s"(lds0 + OFF_R0 + wave * RING_SLOT), "v"(32768u + wave * 1024u + lane * 16u)
        :
          "memory", "vcc", "scc", "m0", "v172", "v173", "v174", "v175", "v176", "v177", "v178", "v179", "v180",
          "v181", "v182", "v183", "v184", "v185", "v186", "v187", "v188", "v189", "v190", "v191", "v192", "v193",
          "v194", "v195", "v196", "v197", "v198", "v199", "v200", "v201", "v202", "v203", "v204", "v205", "v206",
          "v207", "v208", "v209", "v210", "v211", "v212", "v213", "v214", "v215", "v216", "v217", "v218", "v219",
          "v220", "v221", "v222", "v223", "v224", "v225", "v226", "v227", "v228", "v229", "v230", "v231", "v232",
          "v233", "v234", "v235", "v236", "v237", "v238", "v239", "s50", "s51", "s52", "s53", "s54", "s55", "s56",
          "s57", "s58", "s59", "s60", "s61", "s62", "s63", "s64", "s65", "s66", "s67", "s68", "s69", "s70", "s71",
          "s72", "s73", "s74", "s75", "s76", "s77", "s78", "s79", "s80", "s81", "s82", "s83", "s84", "s85", "s86",
          "s87", "s88", "s89", "s90", "s91", "s92", "s93", "s94", "s95", "s96");
  auto uni = [&]() {
#pragma unroll
    for (int k = 0; k < 4; ++k)
      pend[k] = ((unsigned long long)(unsigned)__builtin_amdgcn_readfirstlane((unsigned)(pend[k] >> 32)) << 32) |
                (unsigned)__builtin_amdgcn_readfirstlane((unsigned)pend[k]);
  };
  uni();
  early = __builtin_amdgcn_readfirstlane(early);
  just = early;
  if (__builtin_expect((pend[0] | pend[1] | pend[2] | pend[3] | (unsigned long long)(early ^ 1u)) != 0ull, 0))
#pragma clang loop unroll(disable)
  while (true) {
    early &= 1u;
    if (!early) {
      const u32x4 sx = ld128(aLX + wave * LIST_BYTES);
      const unsigned re = *(const LAS unsigned*)(size_t)(aLE + wave * LIST_BYTES);
      if (__ballot(max(max(max(sx[0], sx[1]), max(sx[2], sx[3])), re) == ~0u) == 0ull) {
        gather5(sx, re, lds0 + OFF_R0 + wave * RING_SLOT);
        early = 1u;
        just = 1u;
      }
    }
    if ((pend[0] | pend[1] | pend[2] | pend[3]) == 0ull || --spin < 0) break;
    asm volatile(
        "s_mov_b32 s96, 0x1000000\n\t"
        "v_mov_b32 v181, %17\n\t"
        "v_mov_b32 v180, 160\n\t"
        "v_mov_b32 v176, 0xe0\n\t"
        "v_mov_b32 v177, 0x0c040100\n\t"
        "v_mov_b32 v178, 0x0c050100\n\t"
        "v_mov_b32 v179, 0x0c060100\n\t"
        "s_mov_b32 s52, 0x7f7f7f7f\n\t"
        "s_mov_b32 s53, 0x7f7f7f7f\n\t"
        "v_mov_b32 v191, 4\n\t"
        "s_waitcnt vmcnt(5)\n\t"
        "s_cmp_eq_u32 %21, 1\n\t"
        "s_cbranch_scc1 9f\n\t"
        "s_waitcnt vmcnt(0)\n\t"
        "9:\n\t"
        "v_add3_u32 v228, v240, v241, v242\n\t"
        "v_add3_u32 v229, v244, v245, v246\n\t"
        "v_add3_u32 v230, v248, v249, v250\n\t"
        "v_add3_u32 v231, v252, v253, v254\n\t"
        "v_add3_u32 v228, v228, v243, %18\n\t"
        "v_add3_u32 v229, v229, v247, %18\n\t"
        "v_add3_u32 v230, v230, v251, %18\n\t"
        "v_add3_u32 v231, v231, v255, %18\n\t"
        "v_add_u32_dpp v228, v228, v228 quad_perm:[1,0,3,2] row_mask:0xf bank_mask:0xf\n\t"
        "v_add_u32_dpp v229, v229, v229 quad_perm:[1,0,3,2] row_mask:0xf bank_mask:0xf\n\t"
        "v_add_u32_dpp v230, v230, v230 quad_perm:[1,0,3,2] row_mask:0xf bank_mask:0xf\n\t"
        "v_add_u32_dpp v231, v231, v231 quad_perm:[1,0,3,2] row_mask:0xf bank_mask:0xf\n\t"
        "v_add_u32_dpp v228, v228, v228 quad_perm:[2,3,0,1] row_mask:0xf bank_mask:0xf\n\t"
        "v_add_u32_dpp v229, v229, v229 quad_perm:[2,3,0,1] row_mask:0xf bank_mask:0xf\n\t"
        "v_add_u32_dpp v230, v230, v230 quad_perm:[2,3,0,1] row_mask:0xf bank_mask:0xf\n\t"
        "v_add_u32_dpp v231, v231, v231 quad_perm:[2,3,0,1] row_mask:0xf bank_mask:0xf\n\t"
        "v_add_u32_dpp v228, v228, v228 row_half_mirror row_mask:0xf bank_mask:0xf\n\t"
        "v_add_u32_dpp v229, v229, v229 row_half_mirror row_mask:0xf bank_mask:0xf\n\t"
        "v_add_u32_dpp v230, v230, v230 row_half_mirror row_mask:0xf bank_mask:0xf\n\t"
        "v_add_u32_dpp v231, v231, v231 row_half_mirror row_mask:0xf bank_mask:0xf\n\t"
        "v_cmp_ne_u32_sdwa s[72:73], v243, %19 src0_sel:BYTE_3 src1_sel:DWORD\n\t"
        "v_cmp_gt_u32 s[64:65], 2, v228\n\t"
        "v_cmp_eq_u32 s[56:57], 1, v228\n\t"
        "v_cmp_ne_u32_sdwa s[74:75], v247, %19 src0_sel:BYTE_3 src1_sel:DWORD\n\t"
        "v_cmp_gt_u32 s[66:67], 2, v229\n\t"
        "v_cmp_eq_u32 s[58:59], 1, v229\n\t"
        "v_cmp_ne_u32_sdwa s[76:77], v251, %19 src0_sel:BYTE_3 src1_sel:DWORD\n\t"
        "v_cmp_gt_u32 s[68:69], 2, v230\n\t"
        "v_cmp_eq_u32 s[60:61], 1, v230\n\t"
        "v_cmp_ne_u32_sdwa s[78:79], v255, %19 src0_sel:BYTE_3 src1_sel:DWORD\n\t"
        "v_cmp_gt_u32 s[70:71], 2, v231\n\t"
        "v_cmp_eq_u32 s[62:63], 1, v231\n\t"
        "s_and_b64 s[64:65], s[64:65], %0\n\t"
        "s_and_b64 s[72:73], s[72:73], s[64:65]\n\t"
        "s_cselect_b64 s[64:65], 0, s[64:65]\n\t"
        "s_and_b64 s[56:57], s[56:57], s[64:65]\n\t"
        "s_andn2_b64 %0, %0, s[64:65]\n\t"
        "s_cbranch_scc0 10f\n\t"
        "s_mov_b64 exec, %0\n\t"
        "global_load_dwordx4 v[240:243], %4, %16 sc1\n\t"
        "10:\n\t"
        "s_and_b64 s[66:67], s[66:67], %1\n\t"
        "s_and_b64 s[74:75], s[74:75], s[66:67]\n\t"
        "s_cselect_b64 s[66:67], 0, s[66:67]\n\t"
        "s_and_b64 s[58:59], s[58:59], s[66:67]\n\t"
        "s_andn2_b64 %1, %1, s[66:67]\n\t"
        "s_cbranch_scc0 11f\n\t"
        "s_mov_b64 exec, %1\n\t"
        "global_load_dwordx4 v[244:247], %5, %16 sc1\n\t"
        "11:\n\t"
        "s_and_b64 s[68:69], s[68:69], %2\n\t"
        "s_and_b64 s[76:77], s[76:77], s[68:69]\n\t"
        "s_cselect_b64 s[68:69], 0, s[68:69]\n\t"
        "s_and_b64 s[60:61], s[60:61], s[68:69]\n\t"
        "s_andn2_b64 %2, %2, s[68:69]\n\t"
        "s_cbranch_scc0 12f\n\t"
        "s_mov_b64 exec, %2\n\t"
        "global_load_dwordx4 v[248:251], %6, %16 sc1\n\t"
        "12:\n\t"
        "s_and_b64 s[70:71], s[70:71], %3\n\t"
        "s_and_b64 s[78:79], s[78:79], s[70:71]\n\t"
        "s_cselect_b64 s[70:71], 0, s[70:71]\n\t"
        "s_and_b64 s[62:63], s[62:63], s[70:71]\n\t"
        "s_andn2_b64 %3, %3, s[70:71]\n\t"
        "s_cbranch_scc0 13f\n\t"
        "s_mov_b64 exec, %3\n\t"
        "global_load_dwordx4 v[252:255], %7, %16 sc1\n\t"
        "13:\n\t"
        "s_mov_b64 s[54:55], 0\n\t"
        "s_mov_b64 s[72:73], 0\n\t"
        "s_mov_b64 exec, s[64:65]\n\t"
        "s_cbranch_execz 20f\n\t"
        "v_cmpx_ne_u32 s[72:73], -1, v240\n\t"
        "v_mul_hi_u32_u24_sdwa v204, v240, %20 dst_sel:DWORD dst_unused:UNUSED_PAD src0_sel:WORD_0 src1_sel:DWORD\n\t"
        "v_and_b32_sdwa v192, v243, v176 dst_sel:DWORD dst_unused:UNUSED_PAD src0_sel:BYTE_0 src1_sel:DWORD\n\t"
        "v_lshl_add_u32 v204, v204, 2, v192\n\t"
        "ds_add_rtn_u32 v204, v204, v191\n\t"
        "v_perm_b32 v216, v243, v240, v177\n\t"
        "v_mad_u32_u16 v228, v240, 32, %12 op_sel:[1,0,0,0]\n\t"
        "v_cmpx_ne_u32 s[74:75], -1, v241\n\t"
        "v_mul_hi_u32_u24_sdwa v205, v241, %20 dst_sel:DWORD dst_unused:UNUSED_PAD src0_sel:WORD_0 src1_sel:DWORD\n\t"
        "v_and_b32_sdwa v193, v243, v176 dst_sel:DWORD dst_unused:UNUSED_PAD src0_sel:BYTE_1 src1_sel:DWORD\n\t"
        "v_lshl_add_u32 v205, v205, 2, v193\n\t"
        "ds_add_rtn_u32 v205, v205, v191\n\t"
        "v_perm_b32 v217, v243, v241, v178\n\t"
        "v_mad_u32_u16 v229, v241, 32, %12 op_sel:[1,0,0,0]\n\t"
        "s_and_b64 exec, exec, s[52:53]\n\t"
        "v_cmpx_ne_u32 s[76:77], -1, v242\n\t"
        "v_mul_hi_u32_u24_sdwa v206, v242, %20 dst_sel:DWORD dst_unused:UNUSED_PAD src0_sel:WORD_0 src1_sel:DWORD\n\t"
        "v_and_b32_sdwa v194, v243, v176 dst_sel:DWORD dst_unused:UNUSED_PAD src0_sel:BYTE_2 src1_sel:DWORD\n\t"
        "v_lshl_add_u32 v206, v206, 2, v194\n\t"
        "ds_add_rtn_u32 v206, v206, v191\n\t"
        "v_perm_b32 v218, v243, v242, v179\n\t"
        "v_mad_u32_u16 v230, v242, 32, %12 op_sel:[1,0,0,0]\n\t"
        "s_or_b64 s[54:55], s[54:55], s[56:57]\n\t"
        "20:\n\t"
        "s_mov_b64 s[78:79], 0\n\t"
        "s_mov_b64 exec, s[66:67]\n\t"
        "s_cbranch_execz 21f\n\t"
        "v_cmpx_ne_u32 s[78:79], -1, v244\n\t"
        "v_mul_hi_u32_u24_sdwa v207, v244, %20 dst_sel:DWORD dst_unused:UNUSED_PAD src0_sel:WORD_0 src1_sel:DWORD\n\t"
        "v_and_b32_sdwa v195, v247, v176 dst_sel:DWORD dst_unused:UNUSED_PAD src0_sel:BYTE_0 src1_sel:DWORD\n\t"
        "v_lshl_add_u32 v207, v207, 2, v195\n\t"
        "ds_add_rtn_u32 v207, v207, v191\n\t"
        "v_perm_b32 v219, v247, v244, v177\n\t"
        "v_mad_u32_u16 v231, v244, 32, %13 op_sel:[1,0,0,0]\n\t"
        "v_cmpx_ne_u32 s[80:81], -1, v245\n\t"
        "v_mul_hi_u32_u24_sdwa v208, v245, %20 dst_sel:DWORD dst_unused:UNUSED_PAD src0_sel:WORD_0 src1_sel:DWORD\n\t"
        "v_and_b32_sdwa v196, v247, v176 dst_sel:DWORD dst_unused:UNUSED_PAD src0_sel:BYTE_1 src1_sel:DWORD\n\t"
        "v_lshl_add_u32 v208, v208, 2, v196\n\t"
        "ds_add_rtn_u32 v208, v208, v191\n\t"
        "v_perm_b32 v220, v247, v245, v178\n\t"
        "v_mad_u32_u16 v232, v245, 32, %13 op_sel:[1,0,0,0]\n\t"
        "s_and_b64 exec, exec, s[52:53]\n\t"
        "v_cmpx_ne_u32 s[82:83], -1, v246\n\t"
        "v_mul_hi_u32_u24_sdwa v209, v246, %20 dst_sel:DWORD dst_unused:UNUSED_PAD src0_sel:WORD_0 src1_sel:DWORD\n\t"
        "v_and_b32_sdwa v197, v247, v176 dst_sel:DWORD dst_unused:UNUSED_PAD src0_sel:BYTE_2 src1_sel:DWORD\n\t"
        "v_lshl_add_u32 v209, v209, 2, v197\n\t"
        "ds_add_rtn_u32 v209, v209, v191\n\t"
        "v_perm_b32 v221, v247, v246, v179\n\t"
        "v_mad_u32_u16 v233, v246, 32, %13 op_sel:[1,0,0,0]\n\t"
        "s_or_b64 s[54:55], s[54:55], s[58:59]\n\t"
        "21:\n\t"
        "s_mov_b64 s[84:85], 0\n\t"
        "s_mov_b64 exec, s[68:69]\n\t"
        "s_cbranch_execz 22f\n\t"
        "v_cmpx_ne_u32 s[84:85], -1, v248\n\t"
        "v_mul_hi_u32_u24_sdwa v210, v248, %20 dst_sel:DWORD dst_unused:UNUSED_PAD src0_sel:WORD_0 src1_sel:DWORD\n\t"
        "v_and_b32_sdwa v198, v251, v176 dst_sel:DWORD dst_unused:UNUSED_PAD src0_sel:BYTE_0 src1_sel:DWORD\n\t"
        "v_lshl_add_u32 v210, v210, 2, v198\n\t"
        "ds_add_rtn_u32 v210, v210, v191\n\t"
        "v_perm_b32 v222, v251, v248, v177\n\t"
        "v_mad_u32_u16 v234, v248, 32, %14 op_sel:[1,0,0,0]\n\t"
        "v_cmpx_ne_u32 s[86:87], -1, v249\n\t"
        "v_mul_hi_u32_u24_sdwa v211, v249, %20 dst_sel:DWORD dst_unused:UNUSED_PAD src0_sel:WORD_0 src1_sel:DWORD\n\t"
        "v_and_b32_sdwa v199, v251, v176 dst_sel:DWORD dst_unused:UNUSED_PAD src0_sel:BYTE_1 src1_sel:DWORD\n\t"
        "v_lshl_add_u32 v211, v211, 2, v199\n\t"
        "ds_add_rtn_u32 v211, v211, v191\n\t"
        "v_perm_b32 v223, v251, v249, v178\n\t"
        "v_mad_u32_u16 v235, v249, 32, %14 op_sel:[1,0,0,0]\n\t"
        "s_and_b64 exec, exec, s[52:53]\n\t"
        "v_cmpx_ne_u32 s[88:89], -1, v250\n\t"
        "v_mul_hi_u32_u24_sdwa v212, v250, %20 dst_sel:DWORD dst_unused:UNUSED_PAD src0_sel:WORD_0 src1_sel:DWORD\n\t"
        "v_and_b32_sdwa v200, v251, v176 dst_sel:DWORD dst_unused:UNUSED_PAD src0_sel:BYTE_2 src1_sel:DWORD\n\t"
        "v_lshl_add_u32 v212, v212, 2, v200\n\t"
        "ds_add_rtn_u32 v212, v212, v191\n\t"
        "v_perm_b32 v224, v251, v250, v179\n\t"
        "v_mad_u32_u16 v236, v250, 32, %14 op_sel:[1,0,0,0]\n\t"
        "s_or_b64 s[54:55], s[54:55], s[60:61]\n\t"
        "22:\n\t"
        "s_mov_b64 s[90:91], 0\n\t"
        "s_mov_b64 exec, s[70:71]\n\t"
        "s_cbranch_execz 23f\n\t"
        "v_cmpx_ne_u32 s[90:91], -1, v252\n\t"
        "v_mul_hi_u32_u24_sdwa v213, v252, %20 dst_sel:DWORD dst_unused:UNUSED_PAD src0_sel:WORD_0 src1_sel:DWORD\n\t"
        "v_and_b32_sdwa v201, v255, v176 dst_sel:DWORD dst_unused:UNUSED_PAD src0_sel:BYTE_0 src1_sel:DWORD\n\t"
        "v_lshl_add_u32 v213, v213, 2, v201\n\t"
        "ds_add_rtn_u32 v213, v213, v191\n\t"
        "v_perm_b32 v225, v255, v252, v177\n\t"
        "v_mad_u32_u16 v237, v252, 32, %15 op_sel:[1,0,0,0]\n\t"
        "v_cmpx_ne_u32 s[92:93], -1, v253\n\t"
        "v_mul_hi_u32_u24_sdwa v214, v253, %20 dst_sel:DWORD dst_unused:UNUSED_PAD src0_sel:WORD_0 src1_sel:DWORD\n\t"
        "v_and_b32_sdwa v202, v255, v176 dst_sel:DWORD dst_unused:UNUSED_PAD src0_sel:BYTE_1 src1_sel:DWORD\n\t"
        "v_lshl_add_u32 v214, v214, 2, v202\n\t"
        "ds_add_rtn_u32 v214, v214, v191\n\t"
        "v_perm_b32 v226, v255, v253, v178\n\t"
        "v_mad_u32_u16 v238, v253, 32, %15 op_sel:[1,0,0,0]\n\t"
        "s_and_b64 exec, exec, s[52:53]\n\t"
        "v_cmpx_ne_u32 s[94:95], -1, v254\n\t"
        "v_mul_hi_u32_u24_sdwa v215, v254, %20 dst_sel:DWORD dst_unused:UNUSED_PAD src0_sel:WORD_0 src1_sel:DWORD\n\t"
        "v_and_b32_sdwa v203, v255, v176 dst_sel:DWORD dst_unused:UNUSED_PAD src0_sel:BYTE_2 src1_sel:DWORD\n\t"
        "v_lshl_add_u32 v215, v215, 2, v203\n\t"
        "ds_add_rtn_u32 v215, v215, v191\n\t"
        "v_perm_b32 v227, v255, v254, v179\n\t"
        "v_mad_u32_u16 v239, v254, 32, %15 op_sel:[1,0,0,0]\n\t"
        "s_or_b64 s[54:55], s[54:55], s[62:63]\n\t"
        "23:\n\t"
        "s_cmp_eq_u64 s[54:55], 0\n\t"
        "s_cbranch_scc1 59f\n\t"
        "s_mov_b64 exec, s[56:57]\n\t"
        "s_cbranch_execz 40f\n\t"
        "v_bfe_u32 v188, %4, 7, 8\n\t"
        "v_lshrrev_b32 v189, 24, %4\n\t"
        "v_add_u32 v189, 1, v189\n\t"
        "ds_write_b8 v188, v189 offset:256\n\t"
        "v_add_u32 %4, s96, %4\n\t"
        "v_mov_b32 v190, 0\n\t"
        "v_cmpx_eq_u32 vcc, 2, v189\n\t"
        "ds_write_b32 v190, v191 offset:20\n\t"
        "s_andn2_b64 exec, s[56:57], vcc\n\t"
        "s_cbranch_execz 40f\n\t"
        "s_or_b64 %0, %0, exec\n\t"
        "global_load_dwordx4 v[240:243], %4, %16 sc1\n\t"
        "40:\n\t"
        "s_mov_b64 exec, s[58:59]\n\t"
        "s_cbranch_execz 41f\n\t"
        "v_bfe_u32 v188, %5, 7, 8\n\t"
        "v_lshrrev_b32 v189, 24, %5\n\t"
        "v_add_u32 v189, 1, v189\n\t"
        "ds_write_b8 v188, v189 offset:256\n\t"
        "v_add_u32 %5, s96, %5\n\t"
        "v_mov_b32 v190, 0\n\t"
        "v_cmpx_eq_u32 vcc, 2, v189\n\t"
        "ds_write_b32 v190, v191 offset:20\n\t"
        "s_andn2_b64 exec, s[58:59], vcc\n\t"
        "s_cbranch_execz 41f\n\t"
        "s_or_b64 %1, %1, exec\n\t"
        "global_load_dwordx4 v[244:247], %5, %16 sc1\n\t"
        "41:\n\t"
        "s_mov_b64 exec, s[60:61]\n\t"
        "s_cbranch_execz 42f\n\t"
        "v_bfe_u32 v188, %6, 7, 8\n\t"
        "v_lshrrev_b32 v189, 24, %6\n\t"
        "v_add_u32 v189, 1, v189\n\t"
        "ds_write_b8 v188, v189 offset:256\n\t"
        "v_add_u32 %6, s96, %6\n\t"
        "v_mov_b32 v190, 0\n\t"
        "v_cmpx_eq_u32 vcc, 2, v189\n\t"
        "ds_write_b32 v190, v191 offset:20\n\t"
        "s_andn2_b64 exec, s[60:61], vcc\n\t"
        "s_cbranch_execz 42f\n\t"
        "s_or_b64 %2, %2, exec\n\t"
        "global_load_dwordx4 v[248:251], %6, %16 sc1\n\t"
        "42:\n\t"
        "s_mov_b64 exec, s[62:63]\n\t"
        "s_cbranch_execz 43f\n\t"
        "v_bfe_u32 v188, %7, 7, 8\n\t"
        "v_lshrrev_b32 v189, 24, %7\n\t"
        "v_add_u32 v189, 1, v189\n\t"
        "ds_write_b8 v188, v189 offset:256\n\t"
        "v_add_u32 %7, s96, %7\n\t"
        "v_mov_b32 v190, 0\n\t"
        "v_cmpx_eq_u32 vcc, 2, v189\n\t"
        "ds_write_b32 v190, v191 offset:20\n\t"
        "s_andn2_b64 exec, s[62:63], vcc\n\t"
        "s_cbranch_execz 43f\n\t"
        "s_or_b64 %3, %3, exec\n\t"
        "global_load_dwordx4 v[252:255], %7, %16 sc1\n\t"
        "43:\n\t"
        "59:\n\t"
        "s_waitcnt lgkmcnt(0)\n\t"
        "s_mov_b64 exec, s[72:73]\n\t"
        "s_cbranch_execz 30f\n\t"
        "v_min_u32 v204, v181, v204\n\t"
        "v_min_u32 v205, v181, v205\n\t"
        "v_min_u32 v206, v181, v206\n\t"
        "v_mad_u32_u24 v204, v192, v180, v204\n\t"
        "v_mad_u32_u24 v205, v193, v180, v205\n\t"
        "v_mad_u32_u24 v206, v194, v180, v206\n\t"
        "ds_write2st64_b32 v204, v216, v228 offset1:10\n\t"
        "s_mov_b64 exec, s[74:75]\n\t"
        "ds_write2st64_b32 v205, v217, v229 offset1:10\n\t"
        "s_mov_b64 exec, s[76:77]\n\t"
        "ds_write2st64_b32 v206, v218, v230 offset1:10\n\t"
        "30:\n\t"
        "s_mov_b64 exec, s[78:79]\n\t"
        "s_cbranch_execz 31f\n\t"
        "v_min_u32 v207, v181, v207\n\t"
        "v_min_u32 v208, v181, v208\n\t"
        "v_min_u32 v209, v181, v209\n\t"
        "v_mad_u32_u24 v207, v195, v180, v207\n\t"
        "v_mad_u32_u24 v208, v196, v180, v208\n\t"
        "v_mad_u32_u24 v209, v197, v180, v209\n\t"
        "ds_write2st64_b32 v207, v219, v231 offset1:10\n\t"
        "s_mov_b64 exec, s[80:81]\n\t"
        "ds_write2st64_b32 v208, v220, v232 offset1:10\n\t"
        "s_mov_b64 exec, s[82:83]\n\t"
        "ds_write2st64_b32 v209, v221, v233 offset1:10\n\t"
        "31:\n\t"
        "s_mov_b64 exec, s[84:85]\n\t"
        "s_cbranch_execz 32f\n\t"
        "v_min_u32 v210, v181, v210\n\t"
        "v_min_u32 v211, v181, v211\n\t"
        "v_min_u32 v212, v181, v212\n\t"
        "v_mad_u32_u24 v210, v198, v180, v210\n\t"
        "v_mad_u32_u24 v211, v199, v180, v211\n\t"
        "v_mad_u32_u24 v212, v200, v180, v212\n\t"
        "ds_write2st64_b32 v210, v222, v234 offset1:10\n\t"
        "s_mov_b64 exec, s[86:87]\n\t"
        "ds_write2st64_b32 v211, v223, v235 offset1:10\n\t"
        "s_mov_b64 exec, s[88:89]\n\t"
        "ds_write2st64_b32 v212, v224, v236 offset1:10\n\t"
        "32:\n\t"
        "s_mov_b64 exec, s[90:91]\n\t"
        "s_cbranch_execz 33f\n\t"
        "v_min_u32 v213, v181, v213\n\t"
        "v_min_u32 v214, v181, v214\n\t"
        "v_min_u32 v215, v181, v215\n\t"
        "v_mad_u32_u24 v213, v201, v180, v213\n\t"
        "v_mad_u32_u24 v214, v202, v180, v214\n\t"
        "v_mad_u32_u24 v215, v203, v180, v215\n\t"
        "ds_write2st64_b32 v213, v225, v237 offset1:10\n\t"
        "s_mov_b64 exec, s[92:93]\n\t"
        "ds_write2st64_b32 v214, v226, v238 offset1:10\n\t"
        "s_mov_b64 exec, s[94:95]\n\t"
        "ds_write2st64_b32 v215, v227, v239 offset1:10\n\t"
        "33:\n\t"
        "s_mov_b64 exec, -1\n\t"
        : "+&s"(pend[0]), "+&s"(pend[1]), "+&s"(pend[2]), "+&s"(pend[3]), "+v"(vo[0]), "+v"(vo[1]), "+v"(vo[2]),
          "+v"(vo[3]), "+{v[240:243]}"(cv[0]), "+{v[244:247]}"(cv[1]), "+{v[248:251]}"(cv[2]), "+{v[252:255]}"(cv[3])
        : "v"(eb[0]), "v"(eb[1]), "v"(eb[2]), "v"(eb[3]), "s"(ib), "n"(OFF_L + LARR - 4), "v"(tag[0]), "v"(tag[1]),
          "s"(sq), "s"(just)
        :
          "memory", "vcc", "scc", "v176", "v177", "v178", "v179", "v180", "v181", "v188", "v189", "v190", "v191",
          "v192", "v193", "v194", "v195", "v196", "v197", "v198", "v199", "v200", "v201", "v202", "v203", "v204",
          "v205", "v206", "v207", "v208", "v209", "v210", "v211", "v212", "v213", "v214", "v215", "v216", "v217",
          "v218", "v219", "v220", "v221", "v222", "v223", "v224", "v225", "v226", "v227", "v228", "v229", "v230",
          "v231", "v232", "v233", "v234", "v235", "v236", "v237", "v238", "v239", "s52", "s53", "s54", "s55", "s56",
          "s57", "s58", "s59", "s60", "s61", "s62", "s63", "s64", "s65", "s66", "s67", "s68", "s69", "s70", "s71",
          "s72", "s73", "s74", "s75", "s76", "s77", "s78", "s79", "s80", "s81", "s82", "s83", "s84", "s85", "s86",
          "s87", "s88", "s89", "s90", "s91", "s92", "s93", "s94", "s95", "s96");
    uni();
    just = 0u;
  }
  *(LAS unsigned*)((LAS char*)smem + OFF_CNT + 16 + wave * 32) = early;
  asm volatile("s_waitcnt lgkmcnt(0)\n\ts_barrier" ::: "memory");
  if (wave >= 4) __builtin_amdgcn_s_setprio(1);
  const u32x4 cw = *(const LAS u32x4*)((LAS char*)smem + OFF_CNT + rid * 32);
  const unsigned ef = *(const LAS unsigned*)((LAS char*)smem + OFF_CNT + 16 + rid * 32);
  unsigned slowf = *(const LAS unsigned*)((LAS char*)smem + OFF_CNT + 20);
  asm volatile("" : "+v"(slowf));
  const int key = (int)((cw[0] + cw[1] + cw[2] + cw[3]) * 2u + rid);
  int gb;
  {
    unsigned acc = 0u;
#define RK(k) acc = __builtin_amdgcn_alignbit(acc, (unsigned)(__builtin_amdgcn_update_dpp(0, key, 0x120 + k, 0xf, 0xf, false) - key), 31)
    RK(1); RK(2); RK(3); RK(4); RK(5); RK(6); RK(7);
#undef RK
    const int want = RANKOF(wave);
    gb = __builtin_ctzll(__ballot(__builtin_popcount(acc) == want)) & 7;
  }
  const unsigned ring0 = lds0 + OFF_R0 + gb * RING_SLOT;
  const unsigned la = l0 + gb * LIST_BYTES;
  u32x16 XAf;
#pragma unroll
  for (int q = 0; q < 4; ++q) {
    const u32x4 t = ld128(ring0 + ringd + q * 1024 + lane * 16);
#pragma unroll
    for (int j = 0; j < 4; ++j) XAf[4 * q + j] = t[j];
  }
  unsigned sl[4];
#pragma unroll
  for (int q = 0; q < 4; ++q) sl[q] = __builtin_amdgcn_readlane(cw[q], gb) - (l0 + q * SUBL);
  const unsigned o2 = sl[0] + sl[1], o3 = o2 + sl[2];
  const bool slow = o3 + sl[3] > LCAP * 4u || max(max(sl[0], sl[1]), max(sl[2], sl[3])) > SUBL - 4u ||
                    __builtin_amdgcn_readfirstlane(slowf) != 0u;
  const int cnt = slow ? 0 : (int)(o3 + sl[3]) >> 2;
  const unsigned cg = (bu * 8 + gb) * 32;
  u32x16 AD;
  AD[2] = aLX + gb * LIST_BYTES;
  AD[3] = aLE + gb * LIST_BYTES;
  AD[4] = cx0; AD[5] = cx1; AD[6] = ce;
  auto issue_gather = [&](int tile, int rs) {
    const u32x4 sx = ld128(AD[2] + tile * 128);
    glds16e(ES, *(const LAS unsigned*)(size_t)(AD[3] + tile * 128), AD[6], ring0 + rs * ringd + 4096);
#pragma unroll
    for (int q = 0; q < 4; ++q) glds16x(x, sx[q], AD[4 + (q & 1)], ring0 + rs * ringd + q * 1024);
  };
  auto pads = [&](int cnt, int lane) __attribute__((always_inline)) {
    const int u = cnt + lane;
    const unsigned c = cg + ((u >> 2) & 7) + 8 * (u & 3), n = nodeof(c);
    const unsigned padA = n | (__builtin_amdgcn_readlane(c, __builtin_ctzll(__ballot((n * Mw + Cw) >> 16 != c))) & 0xffu) << 16;
    const unsigned pa = la + u * 4;
    *(LAS unsigned*)(size_t)pa = padA;
    *(LAS unsigned*)(size_t)(pa + 256) = padA;
    *(LAS unsigned*)(size_t)(pa + LARR) = 0u;
    *(LAS unsigned*)(size_t)(pa + LARR + 256) = 0u;
  };
  constexpr int GEND = 64 + NCH;
  int gs = 0, gp = 0;
  auto gen = [&]() __attribute__((always_inline)) {
    int l2, n = 0;
    asm volatile("v_mbcnt_lo_u32_b32 %0, -1, 0\n\tv_mbcnt_hi_u32_b32 %0, -1, %0" : "=v"(l2));
    const LAS unsigned char* xf = (const LAS unsigned char*)smem + OFF_XF;
    auto put = [&](bool ok, unsigned t, unsigned c, int cc) {
      ok = ok && (int)(c >> 5) == gb;
      const unsigned long long bm = __ballot(ok);
      const unsigned pa = la + 4u * (n + __builtin_amdgcn_mbcnt_hi((unsigned)(bm >> 32), __builtin_amdgcn_mbcnt_lo((unsigned)bm, 0u)));
      if (ok) {
        *(LAS unsigned*)(size_t)pa = (t & 0xffffu) | (c << 16);
        *(LAS unsigned*)(size_t)(pa + LARR) = ((unsigned)(cc * chunk) + (t >> 16)) * 32u;
      }
      n += __builtin_popcountll(bm);
    };
#pragma unroll 1
    while (gs < GEND && n <= LCAP - 192) {
      if (gs < 64) {
        const int cc = (gs & 31) * 8 + (l2 >> 3);
        const unsigned* pp = reinterpret_cast<const unsigned*>(ib + (gs >> 5) * IEXT + (gs & 31) * 1024 + l2 * 16);
        const bool live = gs < 32 || xf[cc] != 0;
        const u32x2 tg = *(const LAS u32x2*)((LAS char*)smem + OFF_CNT + 56);
#pragma unroll 1
        for (int i = 0; i < (1 << 20); ++i) {
          const unsigned m = ldd(pp + 3);
          unsigned sm_ = ldd(pp) + ldd(pp + 1) + ldd(pp + 2) + m + tg[0];
          sm_ += __builtin_amdgcn_update_dpp(0u, sm_, 0xB1, 0xf, 0xf, false);
          sm_ += __builtin_amdgcn_update_dpp(0u, sm_, 0x4E, 0xf, 0xf, false);
          sm_ += __builtin_amdgcn_update_dpp(0u, sm_, 0x141, 0xf, 0xf, false);
          if (__ballot(live && (sm_ > 1u || m >> 24 != tg[1])) == 0ull) break;
        }
        const unsigned m = ldd(pp + 3);
#pragma unroll 1
        for (int j = 0; j < 3; ++j) {
          const unsigned t = ldd(pp + j);
          put(live && t != ~0u && (j < 2 || (l2 & 7) != 7), t, (m >> (8 * j)) & 0xffu, cc);
        }
        ++gs;
      } else {
        const int c2 = gs - 64 + l2;
        const unsigned long long fm = __ballot(c2 < NCH && xf[min(c2, NCH - 1)] == 2);
        if (fm == 0ull) { gs = min(gs + 64, GEND); continue; }
        gs += __builtin_ctzll(fm);
        const int cc = gs - 64;
        const unsigned long long ds = ldq(FL + 512 + bu * NCH + cc);
        const int len = __builtin_amdgcn_readfirstlane((int)(ds >> 32));
        const unsigned long long* tp = TS + (unsigned)ds;
#pragma unroll 1
        for (; gp < len && n <= LCAP - 64; gp += 64) {
          const unsigned long long t = ldq(tp + min(gp + l2, len - 1));
          put(gp + l2 < len, (unsigned)t, (unsigned)(t >> 32), cc);
        }
        if (gp >= len) { gp = 0; ++gs; }
      }
    }
    __builtin_amdgcn_s_waitcnt(0x0F70);
    pads(n, l2);
    return n;
  };
  const bool quick = !slow && sl[0] >= 256u && __builtin_amdgcn_readlane(ef, gb) != 0u;
  auto gather1 = [&](unsigned skip) {
    u32x4 sx = ld128(AD[2] + 128);
    unsigned re = *(const LAS unsigned*)(size_t)(AD[3] + 128);
    asm volatile("s_cmp_lg_u32 %13, 0\n\t"
                 "s_cbranch_scc1 1f\n\t"
                 "s_add_i32 m0, %11, 4096\n\t"
                 "v_add_u32 %4, %4, %8\n\t"
                 "v_mad_u32_u16 %0, %0, %12, %6\n\t"
                 "global_load_lds_dwordx4 %4, %10\n\t"
                 "s_mov_b32 m0, %11\n\t"
                 "v_mad_u32_u16 %1, %1, %12, %7\n\t"
                 "global_load_lds_dwordx4 %0, %9\n\t"
                 "s_addk_i32 m0, 1024\n\t"
                 "v_mad_u32_u16 %2, %2, %12, %6\n\t"
                 "global_load_lds_dwordx4 %1, %9\n\t"
                 "s_addk_i32 m0, 1024\n\t"
                 "v_mad_u32_u16 %3, %3, %12, %7\n\t"
                 "global_load_lds_dwordx4 %2, %9\n\t"
                 "s_addk_i32 m0, 1024\n\t"
                 "s_nop 0\n\t"
                 "global_load_lds_dwordx4 %3, %9\n"
                 "1:"
                 : "+v"(sx[0]), "+v"(sx[1]), "+v"(sx[2]), "+v"(sx[3]), "+v"(re), "+{v[128:143]}"(XAf)
                 : "v"(cx0), "v"(cx1), "v"(ce), "s"(x), "s"(ES), "s"(ring0 + ringd), "s"(128u), "s"(skip)
                 : "memory", "m0", "scc");
  };
  unsigned mode;
  asm volatile("s_cmp_lg_u64 %1, 0\n\ts_cselect_b32 %0, %2, 0" : "=s"(mode) : "s"(__ballot(quick)), "s"(1 + wave / G1W) : "scc");
  gather1(mode ^ 2u);
  if (__builtin_expect(!slow, 1)) {
    const unsigned p0 = la + lane * 4;
    asm volatile(
        "ds_read2st64_b32 v[222:223], %1 offset0:2 offset1:12\n\t"
        "ds_read2st64_b32 v[224:225], %1 offset0:3 offset1:13\n\t"
        "ds_read2st64_b32 v[226:227], %1 offset0:4 offset1:14\n\t"
        "ds_read2st64_b32 v[228:229], %0 offset0:5 offset1:15\n\t"
        "ds_read2st64_b32 v[230:231], %0 offset0:6 offset1:16\n\t"
        "ds_read2st64_b32 v[232:233], %0 offset0:7 offset1:17\n\t"
        "ds_read2st64_b32 v[234:235], %1 offset0:7 offset1:17\n\t"
        "ds_read2st64_b32 v[236:237], %1 offset0:8 offset1:18\n\t"
        "ds_read2st64_b32 v[238:239], %1 offset0:9 offset1:19\n\t"
        "s_waitcnt lgkmcnt(8)\n\t"
        "ds_write2st64_b32 %2, v222, v223 offset0:0 offset1:10\n\t"
        "s_waitcnt lgkmcnt(8)\n\t"
        "ds_write2st64_b32 %2, v224, v225 offset0:1 offset1:11\n\t"
        "s_waitcnt lgkmcnt(8)\n\t"
        "ds_write2st64_b32 %2, v226, v227 offset0:2 offset1:12\n\t"
        "s_waitcnt lgkmcnt(8)\n\t"
        "ds_write2st64_b32 %3, v228, v229 offset0:0 offset1:10\n\t"
        "s_waitcnt lgkmcnt(8)\n\t"
        "ds_write2st64_b32 %3, v230, v231 offset0:1 offset1:11\n\t"
        "s_waitcnt lgkmcnt(8)\n\t"
        "ds_write2st64_b32 %3, v232, v233 offset0:2 offset1:12\n\t"
        "s_waitcnt lgkmcnt(8)\n\t"
        "ds_write2st64_b32 %4, v234, v235 offset0:0 offset1:10\n\t"
        "s_waitcnt lgkmcnt(8)\n\t"
        "ds_write2st64_b32 %4, v236, v237 offset0:1 offset1:11\n\t"
        "s_mov_b64 exec, 0xffffffff\n\t"
        "s_waitcnt lgkmcnt(8)\n\t"
        "ds_write2st64_b32 %4, v238, v239 offset0:2 offset1:12\n\t"
        "s_mov_b64 exec, -1"
        :: "v"(p0), "v"(p0 + 128u), "v"(p0 + sl[0]), "v"(p0 + o2), "v"(p0 + o3)
        : "memory", "v222", "v223", "v224", "v225", "v226", "v227", "v228", "v229", "v230", "v231", "v232", "v233", "v234",
          "v235", "v236", "v237", "v238", "v239");
  }
  pads(cnt, lane);
  AD[0] = aLS4 + gb * LIST_BYTES;
  AD[1] = aLS1 + gb * LIST_BYTES;
  AD[7] = cS - gb * 32 * TPITCH;
  AD[8] = cSt - gb * 32 * TPITCH;
#pragma unroll
  for (int t = 0; t < 4; ++t) AD[9 + t] = fo[t] + gb * RING_SLOT;
  AD[13] = foe + gb * RING_SLOT;
  AD[14] = lds0 + lane * 16;
  AD[15] = to + 18 * 1024 + h * 4096;
  if (__builtin_expect(mode == 0u, 0)) asm volatile("s_waitcnt vmcnt(0)\n\ts_barrier" ::: "memory");
  f32x16 X[2], U[2];
  if (__builtin_expect(mode == 0u, 0)) {
    issue_gather(0, 0);
    issue_gather(1, 1);
  }
  {
    int nt = __builtin_amdgcn_readfirstlane(max((cnt - 1) >> 5, 0));
    asm volatile(
        "s_movk_i32 s94, 128\n\t"
        "s_movk_i32 s95, 80\n\t"
        "s_mov_b32 s96, %[cu]\n\t"
        "ds_read_u16 v102, v161\n\t"
        "ds_read_b128 v[104:107], v162 offset:128\n\t"
        "ds_read_b32 v101, v163 offset:128\n\t"
        "ds_read_b128 v[96:99], v162 offset:256\n\t"
        "ds_read_b32 v100, v163 offset:256\n\t"
        "ds_read_b128 v[120:123], v160\n\t"
        "ds_read_u16 v124, v161 offset:128\n\t"
        "s_waitcnt lgkmcnt(6)\n\t"
        "v_mad_u32_u24 v102, v102, s95, v168\n\t"
        "ds_read_b128 v[184:187], v102\n\t"
        "ds_read_b128 v[188:191], v102 offset:16\n\t"
        "ds_read_b128 v[112:115], v174 offset:512\n\t"
        "ds_read_b128 v[116:119], v174 offset:2560\n\t"
        "ds_read_b128 v[108:111], v174 offset:8704\n\t"
        "ds_read_b128 v[196:199], v174 offset:16896\n\t"
        "ds_read_b128 v[200:203], v174 offset:1536\n\t"
        "ds_read_b128 v[204:207], v174 offset:3584\n\t"
        "s_waitcnt lgkmcnt(12)\n\t"
        "v_add_u32 v101, v101, v166\n\t"
        "v_mad_u32_u16 v104, v104, s94, v164\n\t"
        "v_mad_u32_u16 v105, v105, s94, v165\n\t"
        "v_mad_u32_u16 v106, v106, s94, v164\n\t"
        "v_mad_u32_u16 v107, v107, s94, v165\n\t"
        "s_waitcnt lgkmcnt(7)\n\t"
        "v_mfma_f32_32x32x16_f16 v[64:79], v[184:187], v[128:131], 0\n\t"
        "ds_read_b128 v[208:211], v174 offset:9728\n\t"
        "ds_read_b128 v[212:215], v174 offset:10752\n\t"
        "ds_read_b128 v[216:219], v174 offset:11776\n\t"
        "ds_read_b128 v[220:223], v174 offset:17920\n\t"
        "ds_read_b128 v[224:227], v174 offset:4608\n\t"
        "ds_read_b128 v[228:231], v174 offset:5632\n\t"
        "s_waitcnt lgkmcnt(12)\n\t"
        "v_mfma_f32_32x32x16_f16 v[64:79], v[188:191], v[136:139], v[64:79]\n\t"
        "ds_read_b128 v[232:235], v174 offset:6656\n\t"
        "ds_read_b128 v[236:239], v174 offset:7680\n\t"
        "ds_read_b128 v[240:243], v174 offset:12800\n\t"
        "ds_read_b128 v[244:247], v174 offset:13824\n\t"
        "ds_read_b128 v[248:251], v174 offset:14848\n\t"
        "ds_read_b128 v[252:255], v174 offset:15872\n\t"
        "s_add_i32 m0, %[ring], 45056\n\t"
        "s_waitcnt vmcnt(5)\n\t"
        "s_cmp_eq_u32 %[flag], 0\n\t"
        "s_cbranch_scc1 2f\n\t"
        "s_cmp_eq_u32 %[flag], 2\n\t"
        "s_cbranch_scc1 3f\n\t"
        "s_waitcnt vmcnt(0)\n\t"
        "3:\n\t"
        "s_barrier\n\t"
        "2:\n\t"
        "ds_read_b128 v[144:147], v169\n\t"
        "ds_read_b128 v[148:151], v170\n\t"
        "ds_read_b128 v[152:155], v171\n\t"
        "ds_read_b128 v[156:159], v172\n\t"
        "ds_read_b128 v[192:195], v173\n\t"
        "s_cbranch_scc1 4f\n\t"
        "global_load_lds_dwordx4 v101, %[es]\n\t"
        "s_add_i32 m0, %[ring], 40960\n\t"
        "s_nop 0\n\t"
        "global_load_lds_dwordx4 v104, %[x]\n\t"
        "s_add_i32 m0, %[ring], 41984\n\t"
        "s_nop 0\n\t"
        "global_load_lds_dwordx4 v105, %[x]\n\t"
        "s_add_i32 m0, %[ring], 43008\n\t"
        "s_nop 0\n\t"
        "global_load_lds_dwordx4 v106, %[x]\n\t"
        "s_add_i32 m0, %[ring], 44032\n\t"
        "s_nop 0\n\t"
        "global_load_lds_dwordx4 v107, %[x]\n\t"
        "4:\n\t"
        "s_waitcnt lgkmcnt(4)\n\t"
        "v_mfma_f32_32x32x16_bf16 v[64:79], v[144:147], v[112:115], v[64:79]\n\t"
        "s_waitcnt lgkmcnt(3)\n\t"
        "v_mfma_f32_32x32x16_bf16 v[64:79], v[148:151], v[200:203], v[64:79]\n\t"
        "s_waitcnt lgkmcnt(2)\n\t"
        "v_mfma_f32_32x32x16_bf16 v[64:79], v[152:155], v[116:119], v[64:79]\n\t"
        "s_waitcnt lgkmcnt(1)\n\t"
        "v_mfma_f32_32x32x16_bf16 v[64:79], v[156:159], v[204:207], v[64:79]\n\t"
        "s_waitcnt lgkmcnt(0)\n\t"
        "v_mfma_f32_32x32x16_bf16 v[64:79], v[192:195], v[196:199], v[64:79]\n\t"
        "s_add_i32 m0, %[ring], 4096\n\t"
        "v_add_u32 v100, v100, v166\n\t"
        "global_load_lds_dwordx4 v100, %[es]\n\t"
        "v_mfma_f32_32x32x16_bf16 v[80:95], v[144:147], v[108:111], 0\n\t"
        "s_mov_b32 m0, %[ring]\n\t"
        "v_mad_u32_u16 v96, v96, s94, v164\n\t"
        "global_load_lds_dwordx4 v96, %[x]\n\t"
        "s_add_i32 m0, %[ring], 1024\n\t"
        "v_mad_u32_u16 v97, v97, s94, v165\n\t"
        "global_load_lds_dwordx4 v97, %[x]\n\t"
        "v_mfma_f32_32x32x16_bf16 v[80:95], v[148:151], v[208:211], v[80:95]\n\t"
        "s_add_i32 m0, %[ring], 2048\n\t"
        "v_mad_u32_u16 v98, v98, s94, v164\n\t"
        "global_load_lds_dwordx4 v98, %[x]\n\t"
        "v_exp_f32 v64, v64\n\t"
        "v_exp_f32 v65, v65\n\t"
        "v_mfma_f32_32x32x16_bf16 v[80:95], v[152:155], v[212:215], v[80:95]\n\t"
        "s_add_i32 m0, %[ring], 3072\n\t"
        "v_mad_u32_u16 v99, v99, s94, v165\n\t"
        "global_load_lds_dwordx4 v99, %[x]\n\t"
        "v_exp_f32 v66, v66\n\t"
        "v_exp_f32 v67, v67\n\t"
        "v_mfma_f32_32x32x16_bf16 v[80:95], v[156:159], v[216:219], v[80:95]\n\t"
        "v_cvt_pk_f16_f32 v64, v64, v65\n\t"
        "v_exp_f32 v68, v68\n\t"
        "v_cvt_pk_f16_f32 v65, v66, v67\n\t"
        "v_pk_add_f16 v64, v64, s96\n\t"
        "v_exp_f32 v69, v69\n\t"
        "v_pk_add_f16 v65, v65, s96\n\t"
        "v_rcp_f16_sdwa v64, v64 dst_sel:WORD_0 dst_unused:UNUSED_PRESERVE src0_sel:WORD_0\n\t"
        "v_exp_f32 v70, v70\n\t"
        "v_cvt_pk_f16_f32 v66, v68, v69\n\t"
        "v_rcp_f16_sdwa v65, v65 dst_sel:WORD_0 dst_unused:UNUSED_PRESERVE src0_sel:WORD_0\n\t"
        "v_pk_add_f16 v66, v66, s96\n\t"
        "v_rcp_f16_sdwa v64, v64 dst_sel:WORD_1 dst_unused:UNUSED_PRESERVE src0_sel:WORD_1\n\t"
        "v_cvt_pk_f16_f32 v80, v80, v81\n\t"
        "v_rcp_f16_sdwa v65, v65 dst_sel:WORD_1 dst_unused:UNUSED_PRESERVE src0_sel:WORD_1\n\t"
        "v_pk_mul_f16 v80, v80, v64\n\t"
        "s_branch .Lr4_%=\n\t"
        ".Lrt_%=:\n\t"
        "v_mfma_f32_32x32x16_f16 v[16:31], v[96:99], v[176:179], v[16:31]\n\t"
        "s_cmp_lt_u32 %[wave], 4\n\t"
        "s_cbranch_scc1 1f\n\t"
        "s_setprio 1\n\t"
        "1:\n\t"
        "ds_read_b128 v[120:123], v160\n\t"
        "ds_read_u16 v124, v161 offset:128\n\t"
        "v_rcp_f16_sdwa v103, v103 dst_sel:WORD_1 dst_unused:UNUSED_PRESERVE src0_sel:WORD_1\n\t"
        "v_cvt_pk_f16_f32 v119, v126, v127\n\t"
        "v_pk_add_f16 v64, v64, s96\n\t"
        "v_exp_f32 v67, v67\n\t"
        "v_rcp_f16_sdwa v64, v64 dst_sel:WORD_0 dst_unused:UNUSED_PRESERVE src0_sel:WORD_0\n\t"
        "v_mfma_f32_32x32x16_f16 v[48:63], v[112:115], v[176:179], v[48:63]\n\t"
        "v_pk_mul_f16 v119, v119, v103\n\t"
        "v_cvt_pk_f16_f32 v65, v66, v67\n\t"
        "v_rcp_f16_sdwa v64, v64 dst_sel:WORD_1 dst_unused:UNUSED_PRESERVE src0_sel:WORD_1\n\t"
        "v_pk_add_f16 v65, v65, s96\n\t"
        "v_exp_f32 v68, v68\n\t"
        "v_mfma_f32_32x32x16_f16 v[16:31], v[100:103], v[180:183], v[16:31]\n\t"
        "v_cvt_pk_f16_f32 v80, v80, v81\n\t"
        "v_rcp_f16_sdwa v65, v65 dst_sel:WORD_0 dst_unused:UNUSED_PRESERVE src0_sel:WORD_0\n\t"
        "v_pk_mul_f16 v80, v80, v64\n\t"
        "v_exp_f32 v69, v69\n\t"
        "v_mfma_f32_32x32x16_f16 v[48:63], v[116:119], v[180:183], v[48:63]\n\t"
        "v_cvt_pk_f16_f32 v66, v68, v69\n\t"
        "v_rcp_f16_sdwa v65, v65 dst_sel:WORD_1 dst_unused:UNUSED_PRESERVE src0_sel:WORD_1\n\t"
        "v_pk_add_f16 v66, v66, s96\n\t"
        "v_exp_f32 v70, v70\n\t"
        ".Lr4_%=:\n\t"
        "v_mfma_f32_32x32x16_f16 v[96:111], v[184:187], v[132:135], 0\n\t"
        "s_waitcnt lgkmcnt(1)\n\t"
        "v_mad_u32_u16 v120, v120, s95, v167 op_sel:[1,0,0,0]\n\t"
        "ds_read_b64_tr_b16 v[176:177], v120\n\t"
        "v_mad_u32_u16 v121, v121, s95, v167 op_sel:[1,0,0,0]\n\t"
        "ds_read_b64_tr_b16 v[178:179], v121\n\t"
        "v_mad_u32_u16 v122, v122, s95, v167 op_sel:[1,0,0,0]\n\t"
        "ds_read_b64_tr_b16 v[180:181], v122\n\t"
        "v_mad_u32_u16 v123, v123, s95, v167 op_sel:[1,0,0,0]\n\t"
        "ds_read_b64_tr_b16 v[182:183], v123\n\t"
        "v_cvt_pk_f16_f32 v81, v82, v83\n\t"
        "v_rcp_f16_sdwa v66, v66 dst_sel:WORD_0 dst_unused:UNUSED_PRESERVE src0_sel:WORD_0\n\t"
        "v_pk_mul_f16 v81, v81, v65\n\t"
        "v_exp_f32 v71, v71\n\t"
        "v_mfma_f32_32x32x16_f16 v[96:111], v[188:191], v[140:143], v[96:111]\n\t"
        "s_waitcnt lgkmcnt(4)\n\t"
        "v_mad_u32_u24 v124, v124, s95, v168\n\t"
        "ds_read_b128 v[184:187], v124\n\t"
        "ds_read_b128 v[188:191], v124 offset:16\n\t"
        "v_cvt_pk_f16_f32 v67, v70, v71\n\t"
        "v_rcp_f16_sdwa v66, v66 dst_sel:WORD_1 dst_unused:UNUSED_PRESERVE src0_sel:WORD_1\n\t"
        "v_pk_add_f16 v67, v67, s96\n\t"
        "v_exp_f32 v72, v72\n\t"
        "v_mfma_f32_32x32x16_bf16 v[96:111], v[192:195], v[220:223], v[96:111]\n\t"
        "s_waitcnt vmcnt(9)\n\t"
        "ds_read_b128 v[192:195], v173 offset:40960\n\t"
        "ds_read_b32 v125, v163 offset:384\n\t"
        "v_cvt_pk_f16_f32 v82, v84, v85\n\t"
        "v_rcp_f16_sdwa v67, v67 dst_sel:WORD_0 dst_unused:UNUSED_PRESERVE src0_sel:WORD_0\n\t"
        "v_pk_mul_f16 v82, v82, v66\n\t"
        "v_exp_f32 v73, v73\n\t"
        "v_mfma_f32_32x32x16_bf16 v[96:111], v[144:147], v[224:227], v[96:111]\n\t"
        "v_cvt_pk_f16_f32 v68, v72, v73\n\t"
        "v_rcp_f16_sdwa v67, v67 dst_sel:WORD_1 dst_unused:UNUSED_PRESERVE src0_sel:WORD_1\n\t"
        "v_pk_add_f16 v68, v68, s96\n\t"
        "v_exp_f32 v74, v74\n\t"
        "v_mfma_f32_32x32x16_bf16 v[96:111], v[148:151], v[228:231], v[96:111]\n\t"
        "v_cvt_pk_f16_f32 v83, v86, v87\n\t"
        "v_rcp_f16_sdwa v68, v68 dst_sel:WORD_0 dst_unused:UNUSED_PRESERVE src0_sel:WORD_0\n\t"
        "v_pk_mul_f16 v83, v83, v67\n\t"
        "v_exp_f32 v75, v75\n\t"
        "v_mfma_f32_32x32x16_bf16 v[96:111], v[152:155], v[232:235], v[96:111]\n\t"
        "s_add_i32 m0, %[ring], 45056\n\t"
        "s_waitcnt lgkmcnt(0)\n\t"
        "v_add_u32 v125, v125, v166\n\t"
        "global_load_lds_dwordx4 v125, %[es]\n\t"
        "v_cvt_pk_f16_f32 v69, v74, v75\n\t"
        "v_rcp_f16_sdwa v68, v68 dst_sel:WORD_1 dst_unused:UNUSED_PRESERVE src0_sel:WORD_1\n\t"
        "v_pk_add_f16 v69, v69, s96\n\t"
        "v_exp_f32 v76, v76\n\t"
        "v_mfma_f32_32x32x16_bf16 v[96:111], v[156:159], v[236:239], v[96:111]\n\t"
        "v_cvt_pk_f16_f32 v84, v88, v89\n\t"
        "v_rcp_f16_sdwa v69, v69 dst_sel:WORD_0 dst_unused:UNUSED_PRESERVE src0_sel:WORD_0\n\t"
        "v_pk_mul_f16 v84, v84, v68\n\t"
        "v_exp_f32 v77, v77\n\t"
        "v_mfma_f32_32x32x16_bf16 v[112:127], v[144:147], v[240:243], 0\n\t"
        "s_waitcnt vmcnt(6)\n\t"
        "ds_read_b128 v[144:147], v169 offset:40960\n\t"
        "v_cvt_pk_f16_f32 v70, v76, v77\n\t"
        "v_rcp_f16_sdwa v69, v69 dst_sel:WORD_1 dst_unused:UNUSED_PRESERVE src0_sel:WORD_1\n\t"
        "v_pk_add_f16 v70, v70, s96\n\t"
        "v_exp_f32 v78, v78\n\t"
        "v_mfma_f32_32x32x16_bf16 v[112:127], v[148:151], v[244:247], v[112:127]\n\t"
        "ds_read_b128 v[148:151], v170 offset:40960\n\t"
        "s_sub_u32 %[nt], %[nt], 1\n\t"
        "v_cvt_pk_f16_f32 v85, v90, v91\n\t"
        "v_rcp_f16_sdwa v70, v70 dst_sel:WORD_0 dst_unused:UNUSED_PRESERVE src0_sel:WORD_0\n\t"
        "v_pk_mul_f16 v85, v85, v69\n\t"
        "v_exp_f32 v79, v79\n\t"
        "v_mfma_f32_32x32x16_bf16 v[112:127], v[152:155], v[248:251], v[112:127]\n\t"
        "ds_read_b128 v[152:155], v171 offset:40960\n\t"
        "ds_read_b128 v[88:91], v162 offset:384\n\t"
        "v_cvt_pk_f16_f32 v71, v78, v79\n\t"
        "v_rcp_f16_sdwa v70, v70 dst_sel:WORD_1 dst_unused:UNUSED_PRESERVE src0_sel:WORD_1\n\t"
        "v_pk_add_f16 v71, v71, s96\n\t"
        "v_exp_f32 v96, v96\n\t"
        "v_exp_f32 v97, v97\n\t"
        "v_mfma_f32_32x32x16_bf16 v[112:127], v[156:159], v[252:255], v[112:127]\n\t"
        "ds_read_b128 v[156:159], v172 offset:40960\n\t"
        "v_cvt_pk_f16_f32 v86, v92, v93\n\t"
        "v_rcp_f16_sdwa v71, v71 dst_sel:WORD_0 dst_unused:UNUSED_PRESERVE src0_sel:WORD_0\n\t"
        "v_pk_mul_f16 v86, v86, v70\n\t"
        "v_exp_f32 v98, v98\n\t"
        "v_cvt_pk_f16_f32 v96, v96, v97\n\t"
        "s_cbranch_scc1 .Lrl_%=\n\t"
        "v_mfma_f32_32x32x16_f16 v[0:15], v[64:67], v[176:179], v[0:15]\n\t"
        "v_rcp_f16_sdwa v71, v71 dst_sel:WORD_1 dst_unused:UNUSED_PRESERVE src0_sel:WORD_1\n\t"
        "v_cvt_pk_f16_f32 v87, v94, v95\n\t"
        "v_pk_add_f16 v96, v96, s96\n\t"
        "v_exp_f32 v99, v99\n\t"
        "v_rcp_f16_sdwa v96, v96 dst_sel:WORD_0 dst_unused:UNUSED_PRESERVE src0_sel:WORD_0\n\t"
        "v_mfma_f32_32x32x16_f16 v[32:47], v[80:83], v[176:179], v[32:47]\n\t"
        "s_add_i32 m0, %[ring], 40960\n\t"
        "s_waitcnt lgkmcnt(1)\n\t"
        "v_mad_u32_u16 v88, v88, s94, v164\n\t"
        "s_waitcnt lgkmcnt(0)\n\t"
        "global_load_lds_dwordx4 v88, %[x]\n\t"
        "v_pk_mul_f16 v87, v87, v71\n\t"
        "v_cvt_pk_f16_f32 v97, v98, v99\n\t"
        "v_rcp_f16_sdwa v96, v96 dst_sel:WORD_1 dst_unused:UNUSED_PRESERVE src0_sel:WORD_1\n\t"
        "v_pk_add_f16 v97, v97, s96\n\t"
        "v_exp_f32 v100, v100\n\t"
        "v_mfma_f32_32x32x16_f16 v[0:15], v[68:71], v[180:183], v[0:15]\n\t"
        "ds_read_b128 v[92:95], v174 offset:512\n\t"
        "s_add_i32 m0, %[ring], 41984\n\t"
        "v_mad_u32_u16 v89, v89, s94, v165\n\t"
        "global_load_lds_dwordx4 v89, %[x]\n\t"
        "v_cvt_pk_f16_f32 v112, v112, v113\n\t"
        "v_rcp_f16_sdwa v97, v97 dst_sel:WORD_0 dst_unused:UNUSED_PRESERVE src0_sel:WORD_0\n\t"
        "v_pk_mul_f16 v112, v112, v96\n\t"
        "v_exp_f32 v101, v101\n\t"
        "v_mfma_f32_32x32x16_f16 v[32:47], v[84:87], v[180:183], v[32:47]\n\t"
        "s_add_i32 m0, %[ring], 43008\n\t"
        "v_mad_u32_u16 v90, v90, s94, v164\n\t"
        "global_load_lds_dwordx4 v90, %[x]\n\t"
        "v_cvt_pk_f16_f32 v98, v100, v101\n\t"
        "v_rcp_f16_sdwa v97, v97 dst_sel:WORD_1 dst_unused:UNUSED_PRESERVE src0_sel:WORD_1\n\t"
        "v_pk_add_f16 v98, v98, s96\n\t"
        "v_exp_f32 v102, v102\n\t"
        "v_mfma_f32_32x32x16_f16 v[64:79], v[184:187], v[128:131], 0\n\t"
        "s_add_i32 m0, %[ring], 44032\n\t"
        "v_mad_u32_u16 v91, v91, s94, v165\n\t"
        "global_load_lds_dwordx4 v91, %[x]\n\t"
        "v_cvt_pk_f16_f32 v113, v114, v115\n\t"
        "v_rcp_f16_sdwa v98, v98 dst_sel:WORD_0 dst_unused:UNUSED_PRESERVE src0_sel:WORD_0\n\t"
        "v_pk_mul_f16 v113, v113, v97\n\t"
        "v_exp_f32 v103, v103\n\t"
        "v_mfma_f32_32x32x16_f16 v[64:79], v[188:191], v[136:139], v[64:79]\n\t"
        "ds_read_b128 v[80:83], v174 offset:2560\n\t"
        "v_cvt_pk_f16_f32 v99, v102, v103\n\t"
        "v_rcp_f16_sdwa v98, v98 dst_sel:WORD_1 dst_unused:UNUSED_PRESERVE src0_sel:WORD_1\n\t"
        "v_pk_add_f16 v99, v99, s96\n\t"
        "v_exp_f32 v104, v104\n\t"
        "v_mfma_f32_32x32x16_bf16 v[64:79], v[192:195], v[196:199], v[64:79]\n\t"
        "v_cvt_pk_f16_f32 v114, v116, v117\n\t"
        "v_rcp_f16_sdwa v99, v99 dst_sel:WORD_0 dst_unused:UNUSED_PRESERVE src0_sel:WORD_0\n\t"
        "v_pk_mul_f16 v114, v114, v98\n\t"
        "v_exp_f32 v105, v105\n\t"
        "s_waitcnt lgkmcnt(1)\n\t"
        "v_mfma_f32_32x32x16_bf16 v[64:79], v[144:147], v[92:95], v[64:79]\n\t"
        "ds_read_b128 v[84:87], v174 offset:8704\n\t"
        "v_cvt_pk_f16_f32 v100, v104, v105\n\t"
        "v_rcp_f16_sdwa v99, v99 dst_sel:WORD_1 dst_unused:UNUSED_PRESERVE src0_sel:WORD_1\n\t"
        "v_pk_add_f16 v100, v100, s96\n\t"
        "v_exp_f32 v106, v106\n\t"
        "v_mfma_f32_32x32x16_bf16 v[64:79], v[148:151], v[200:203], v[64:79]\n\t"
        "v_cvt_pk_f16_f32 v115, v118, v119\n\t"
        "v_rcp_f16_sdwa v100, v100 dst_sel:WORD_0 dst_unused:UNUSED_PRESERVE src0_sel:WORD_0\n\t"
        "v_pk_mul_f16 v115, v115, v99\n\t"
        "v_exp_f32 v107, v107\n\t"
        "s_waitcnt lgkmcnt(1)\n\t"
        "v_mfma_f32_32x32x16_bf16 v[64:79], v[152:155], v[80:83], v[64:79]\n\t"
        "v_cvt_pk_f16_f32 v101, v106, v107\n\t"
        "v_rcp_f16_sdwa v100, v100 dst_sel:WORD_1 dst_unused:UNUSED_PRESERVE src0_sel:WORD_1\n\t"
        "v_pk_add_f16 v101, v101, s96\n\t"
        "v_exp_f32 v108, v108\n\t"
        "v_mfma_f32_32x32x16_bf16 v[64:79], v[156:159], v[204:207], v[64:79]\n\t"
        "v_cvt_pk_f16_f32 v116, v120, v121\n\t"
        "v_rcp_f16_sdwa v101, v101 dst_sel:WORD_0 dst_unused:UNUSED_PRESERVE src0_sel:WORD_0\n\t"
        "v_pk_mul_f16 v116, v116, v100\n\t"
        "v_exp_f32 v109, v109\n\t"
        "s_waitcnt lgkmcnt(0)\n\t"
        "v_mfma_f32_32x32x16_bf16 v[80:95], v[144:147], v[84:87], 0\n\t"
        "v_cvt_pk_f16_f32 v102, v108, v109\n\t"
        "v_rcp_f16_sdwa v101, v101 dst_sel:WORD_1 dst_unused:UNUSED_PRESERVE src0_sel:WORD_1\n\t"
        "v_pk_add_f16 v102, v102, s96\n\t"
        "v_exp_f32 v110, v110\n\t"
        "v_mfma_f32_32x32x16_bf16 v[80:95], v[148:151], v[208:211], v[80:95]\n\t"
        "v_cvt_pk_f16_f32 v117, v122, v123\n\t"
        "v_rcp_f16_sdwa v102, v102 dst_sel:WORD_0 dst_unused:UNUSED_PRESERVE src0_sel:WORD_0\n\t"
        "v_pk_mul_f16 v117, v117, v101\n\t"
        "v_exp_f32 v111, v111\n\t"
        "v_mfma_f32_32x32x16_bf16 v[80:95], v[152:155], v[212:215], v[80:95]\n\t"
        "v_cvt_pk_f16_f32 v103, v110, v111\n\t"
        "v_rcp_f16_sdwa v102, v102 dst_sel:WORD_1 dst_unused:UNUSED_PRESERVE src0_sel:WORD_1\n\t"
        "v_pk_add_f16 v103, v103, s96\n\t"
        "v_exp_f32 v64, v64\n\t"
        "v_exp_f32 v65, v65\n\t"
        "v_mfma_f32_32x32x16_bf16 v[80:95], v[156:159], v[216:219], v[80:95]\n\t"
        "v_cvt_pk_f16_f32 v118, v124, v125\n\t"
        "v_rcp_f16_sdwa v103, v103 dst_sel:WORD_0 dst_unused:UNUSED_PRESERVE src0_sel:WORD_0\n\t"
        "v_pk_mul_f16 v118, v118, v102\n\t"
        "v_exp_f32 v66, v66\n\t"
        "v_cvt_pk_f16_f32 v64, v64, v65\n\t"
        "v_mfma_f32_32x32x16_f16 v[16:31], v[96:99], v[176:179], v[16:31]\n\t"
        "s_setprio 0\n\t"
        "ds_read_b128 v[120:123], v160 offset:128\n\t"
        "ds_read_u16 v124, v161 offset:256\n\t"
        "v_rcp_f16_sdwa v103, v103 dst_sel:WORD_1 dst_unused:UNUSED_PRESERVE src0_sel:WORD_1\n\t"
        "v_cvt_pk_f16_f32 v119, v126, v127\n\t"
        "v_pk_add_f16 v64, v64, s96\n\t"
        "v_exp_f32 v67, v67\n\t"
        "v_rcp_f16_sdwa v64, v64 dst_sel:WORD_0 dst_unused:UNUSED_PRESERVE src0_sel:WORD_0\n\t"
        "v_mfma_f32_32x32x16_f16 v[48:63], v[112:115], v[176:179], v[48:63]\n\t"
        "v_pk_mul_f16 v119, v119, v103\n\t"
        "v_cvt_pk_f16_f32 v65, v66, v67\n\t"
        "v_rcp_f16_sdwa v64, v64 dst_sel:WORD_1 dst_unused:UNUSED_PRESERVE src0_sel:WORD_1\n\t"
        "v_pk_add_f16 v65, v65, s96\n\t"
        "v_exp_f32 v68, v68\n\t"
        "v_mfma_f32_32x32x16_f16 v[16:31], v[100:103], v[180:183], v[16:31]\n\t"
        "v_cvt_pk_f16_f32 v80, v80, v81\n\t"
        "v_rcp_f16_sdwa v65, v65 dst_sel:WORD_0 dst_unused:UNUSED_PRESERVE src0_sel:WORD_0\n\t"
        "v_pk_mul_f16 v80, v80, v64\n\t"
        "v_exp_f32 v69, v69\n\t"
        "v_mfma_f32_32x32x16_f16 v[48:63], v[116:119], v[180:183], v[48:63]\n\t"
        "v_cvt_pk_f16_f32 v66, v68, v69\n\t"
        "v_rcp_f16_sdwa v65, v65 dst_sel:WORD_1 dst_unused:UNUSED_PRESERVE src0_sel:WORD_1\n\t"
        "v_pk_add_f16 v66, v66, s96\n\t"
        "v_exp_f32 v70, v70\n\t"
        "v_mfma_f32_32x32x16_f16 v[96:111], v[184:187], v[132:135], 0\n\t"
        "s_waitcnt lgkmcnt(1)\n\t"
        "v_mad_u32_u16 v120, v120, s95, v167 op_sel:[1,0,0,0]\n\t"
        "ds_read_b64_tr_b16 v[176:177], v120\n\t"
        "v_mad_u32_u16 v121, v121, s95, v167 op_sel:[1,0,0,0]\n\t"
        "ds_read_b64_tr_b16 v[178:179], v121\n\t"
        "v_mad_u32_u16 v122, v122, s95, v167 op_sel:[1,0,0,0]\n\t"
        "ds_read_b64_tr_b16 v[180:181], v122\n\t"
        "v_mad_u32_u16 v123, v123, s95, v167 op_sel:[1,0,0,0]\n\t"
        "ds_read_b64_tr_b16 v[182:183], v123\n\t"
        "v_cvt_pk_f16_f32 v81, v82, v83\n\t"
        "v_rcp_f16_sdwa v66, v66 dst_sel:WORD_0 dst_unused:UNUSED_PRESERVE src0_sel:WORD_0\n\t"
        "v_pk_mul_f16 v81, v81, v65\n\t"
        "v_exp_f32 v71, v71\n\t"
        "v_mfma_f32_32x32x16_f16 v[96:111], v[188:191], v[140:143], v[96:111]\n\t"
        "s_waitcnt lgkmcnt(4)\n\t"
        "v_mad_u32_u24 v124, v124, s95, v168\n\t"
        "ds_read_b128 v[184:187], v124\n\t"
        "ds_read_b128 v[188:191], v124 offset:16\n\t"
        "v_cvt_pk_f16_f32 v67, v70, v71\n\t"
        "v_rcp_f16_sdwa v66, v66 dst_sel:WORD_1 dst_unused:UNUSED_PRESERVE src0_sel:WORD_1\n\t"
        "v_pk_add_f16 v67, v67, s96\n\t"
        "v_exp_f32 v72, v72\n\t"
        "v_mfma_f32_32x32x16_bf16 v[96:111], v[192:195], v[220:223], v[96:111]\n\t"
        "s_waitcnt vmcnt(9)\n\t"
        "ds_read_b128 v[192:195], v173\n\t"
        "ds_read_b32 v125, v163 offset:512\n\t"
        "v_cvt_pk_f16_f32 v82, v84, v85\n\t"
        "v_rcp_f16_sdwa v67, v67 dst_sel:WORD_0 dst_unused:UNUSED_PRESERVE src0_sel:WORD_0\n\t"
        "v_pk_mul_f16 v82, v82, v66\n\t"
        "v_exp_f32 v73, v73\n\t"
        "v_mfma_f32_32x32x16_bf16 v[96:111], v[144:147], v[224:227], v[96:111]\n\t"
        "v_cvt_pk_f16_f32 v68, v72, v73\n\t"
        "v_rcp_f16_sdwa v67, v67 dst_sel:WORD_1 dst_unused:UNUSED_PRESERVE src0_sel:WORD_1\n\t"
        "v_pk_add_f16 v68, v68, s96\n\t"
        "v_exp_f32 v74, v74\n\t"
        "v_mfma_f32_32x32x16_bf16 v[96:111], v[148:151], v[228:231], v[96:111]\n\t"
        "v_cvt_pk_f16_f32 v83, v86, v87\n\t"
        "v_rcp_f16_sdwa v68, v68 dst_sel:WORD_0 dst_unused:UNUSED_PRESERVE src0_sel:WORD_0\n\t"
        "v_pk_mul_f16 v83, v83, v67\n\t"
        "v_exp_f32 v75, v75\n\t"
        "v_mfma_f32_32x32x16_bf16 v[96:111], v[152:155], v[232:235], v[96:111]\n\t"
        "s_add_i32 m0, %[ring], 4096\n\t"
        "s_waitcnt lgkmcnt(0)\n\t"
        "v_add_u32 v125, v125, v166\n\t"
        "global_load_lds_dwordx4 v125, %[es]\n\t"
        "v_cvt_pk_f16_f32 v69, v74, v75\n\t"
        "v_rcp_f16_sdwa v68, v68 dst_sel:WORD_1 dst_unused:UNUSED_PRESERVE src0_sel:WORD_1\n\t"
        "v_pk_add_f16 v69, v69, s96\n\t"
        "v_exp_f32 v76, v76\n\t"
        "v_mfma_f32_32x32x16_bf16 v[96:111], v[156:159], v[236:239], v[96:111]\n\t"
        "v_cvt_pk_f16_f32 v84, v88, v89\n\t"
        "v_rcp_f16_sdwa v69, v69 dst_sel:WORD_0 dst_unused:UNUSED_PRESERVE src0_sel:WORD_0\n\t"
        "v_pk_mul_f16 v84, v84, v68\n\t"
        "v_exp_f32 v77, v77\n\t"
        "v_mfma_f32_32x32x16_bf16 v[112:127], v[144:147], v[240:243], 0\n\t"
        "s_waitcnt vmcnt(6)\n\t"
        "ds_read_b128 v[144:147], v169\n\t"
        "v_cvt_pk_f16_f32 v70, v76, v77\n\t"
        "v_rcp_f16_sdwa v69, v69 dst_sel:WORD_1 dst_unused:UNUSED_PRESERVE src0_sel:WORD_1\n\t"
        "v_pk_add_f16 v70, v70, s96\n\t"
        "v_exp_f32 v78, v78\n\t"
        "v_mfma_f32_32x32x16_bf16 v[112:127], v[148:151], v[244:247], v[112:127]\n\t"
        "ds_read_b128 v[148:151], v170\n\t"
        "s_sub_u32 %[nt], %[nt], 1\n\t"
        "v_cvt_pk_f16_f32 v85, v90, v91\n\t"
        "v_rcp_f16_sdwa v70, v70 dst_sel:WORD_0 dst_unused:UNUSED_PRESERVE src0_sel:WORD_0\n\t"
        "v_pk_mul_f16 v85, v85, v69\n\t"
        "v_exp_f32 v79, v79\n\t"
        "v_mfma_f32_32x32x16_bf16 v[112:127], v[152:155], v[248:251], v[112:127]\n\t"
        "ds_read_b128 v[152:155], v171\n\t"
        "ds_read_b128 v[88:91], v162 offset:512\n\t"
        "v_cvt_pk_f16_f32 v71, v78, v79\n\t"
        "v_rcp_f16_sdwa v70, v70 dst_sel:WORD_1 dst_unused:UNUSED_PRESERVE src0_sel:WORD_1\n\t"
        "v_pk_add_f16 v71, v71, s96\n\t"
        "v_exp_f32 v96, v96\n\t"
        "v_exp_f32 v97, v97\n\t"
        "v_mfma_f32_32x32x16_bf16 v[112:127], v[156:159], v[252:255], v[112:127]\n\t"
        "ds_read_b128 v[156:159], v172\n\t"
        "v_cvt_pk_f16_f32 v86, v92, v93\n\t"
        "v_rcp_f16_sdwa v71, v71 dst_sel:WORD_0 dst_unused:UNUSED_PRESERVE src0_sel:WORD_0\n\t"
        "v_pk_mul_f16 v86, v86, v70\n\t"
        "v_exp_f32 v98, v98\n\t"
        "v_cvt_pk_f16_f32 v96, v96, v97\n\t"
        "s_cbranch_scc1 .Lrl_%=\n\t"
        "v_mfma_f32_32x32x16_f16 v[0:15], v[64:67], v[176:179], v[0:15]\n\t"
        "v_rcp_f16_sdwa v71, v71 dst_sel:WORD_1 dst_unused:UNUSED_PRESERVE src0_sel:WORD_1\n\t"
        "v_cvt_pk_f16_f32 v87, v94, v95\n\t"
        "v_pk_add_f16 v96, v96, s96\n\t"
        "v_exp_f32 v99, v99\n\t"
        "v_rcp_f16_sdwa v96, v96 dst_sel:WORD_0 dst_unused:UNUSED_PRESERVE src0_sel:WORD_0\n\t"
        "v_mfma_f32_32x32x16_f16 v[32:47], v[80:83], v[176:179], v[32:47]\n\t"
        "s_mov_b32 m0, %[ring]\n\t"
        "s_waitcnt lgkmcnt(1)\n\t"
        "v_mad_u32_u16 v88, v88, s94, v164\n\t"
        "s_waitcnt lgkmcnt(0)\n\t"
        "global_load_lds_dwordx4 v88, %[x]\n\t"
        "v_pk_mul_f16 v87, v87, v71\n\t"
        "v_cvt_pk_f16_f32 v97, v98, v99\n\t"
        "v_rcp_f16_sdwa v96, v96 dst_sel:WORD_1 dst_unused:UNUSED_PRESERVE src0_sel:WORD_1\n\t"
        "v_pk_add_f16 v97, v97, s96\n\t"
        "v_exp_f32 v100, v100\n\t"
        "v_mfma_f32_32x32x16_f16 v[0:15], v[68:71], v[180:183], v[0:15]\n\t"
        "ds_read_b128 v[92:95], v174 offset:512\n\t"
        "s_add_i32 m0, %[ring], 1024\n\t"
        "v_mad_u32_u16 v89, v89, s94, v165\n\t"
        "global_load_lds_dwordx4 v89, %[x]\n\t"
        "v_cvt_pk_f16_f32 v112, v112, v113\n\t"
        "v_rcp_f16_sdwa v97, v97 dst_sel:WORD_0 dst_unused:UNUSED_PRESERVE src0_sel:WORD_0\n\t"
        "v_pk_mul_f16 v112, v112, v96\n\t"
        "v_exp_f32 v101, v101\n\t"
        "v_mfma_f32_32x32x16_f16 v[32:47], v[84:87], v[180:183], v[32:47]\n\t"
        "s_add_i32 m0, %[ring], 2048\n\t"
        "v_mad_u32_u16 v90, v90, s94, v164\n\t"
        "global_load_lds_dwordx4 v90, %[x]\n\t"
        "v_cvt_pk_f16_f32 v98, v100, v101\n\t"
        "v_rcp_f16_sdwa v97, v97 dst_sel:WORD_1 dst_unused:UNUSED_PRESERVE src0_sel:WORD_1\n\t"
        "v_pk_add_f16 v98, v98, s96\n\t"
        "v_exp_f32 v102, v102\n\t"
        "v_mfma_f32_32x32x16_f16 v[64:79], v[184:187], v[128:131], 0\n\t"
        "s_add_i32 m0, %[ring], 3072\n\t"
        "v_mad_u32_u16 v91, v91, s94, v165\n\t"
        "global_load_lds_dwordx4 v91, %[x]\n\t"
        "v_cvt_pk_f16_f32 v113, v114, v115\n\t"
        "v_rcp_f16_sdwa v98, v98 dst_sel:WORD_0 dst_unused:UNUSED_PRESERVE src0_sel:WORD_0\n\t"
        "v_pk_mul_f16 v113, v113, v97\n\t"
        "v_exp_f32 v103, v103\n\t"
        "v_mfma_f32_32x32x16_f16 v[64:79], v[188:191], v[136:139], v[64:79]\n\t"
        "ds_read_b128 v[80:83], v174 offset:2560\n\t"
        "v_cvt_pk_f16_f32 v99, v102, v103\n\t"
        "v_rcp_f16_sdwa v98, v98 dst_sel:WORD_1 dst_unused:UNUSED_PRESERVE src0_sel:WORD_1\n\t"
        "v_pk_add_f16 v99, v99, s96\n\t"
        "v_exp_f32 v104, v104\n\t"
        "v_mfma_f32_32x32x16_bf16 v[64:79], v[192:195], v[196:199], v[64:79]\n\t"
        "v_cvt_pk_f16_f32 v114, v116, v117\n\t"
        "v_rcp_f16_sdwa v99, v99 dst_sel:WORD_0 dst_unused:UNUSED_PRESERVE src0_sel:WORD_0\n\t"
        "v_pk_mul_f16 v114, v114, v98\n\t"
        "v_exp_f32 v105, v105\n\t"
        "s_waitcnt lgkmcnt(1)\n\t"
        "v_mfma_f32_32x32x16_bf16 v[64:79], v[144:147], v[92:95], v[64:79]\n\t"
        "ds_read_b128 v[84:87], v174 offset:8704\n\t"
        "v_cvt_pk_f16_f32 v100, v104, v105\n\t"
        "v_rcp_f16_sdwa v99, v99 dst_sel:WORD_1 dst_unused:UNUSED_PRESERVE src0_sel:WORD_1\n\t"
        "v_pk_add_f16 v100, v100, s96\n\t"
        "v_exp_f32 v106, v106\n\t"
        "v_mfma_f32_32x32x16_bf16 v[64:79], v[148:151], v[200:203], v[64:79]\n\t"
        "v_cvt_pk_f16_f32 v115, v118, v119\n\t"
        "v_rcp_f16_sdwa v100, v100 dst_sel:WORD_0 dst_unused:UNUSED_PRESERVE src0_sel:WORD_0\n\t"
        "v_pk_mul_f16 v115, v115, v99\n\t"
        "v_exp_f32 v107, v107\n\t"
        "s_waitcnt lgkmcnt(1)\n\t"
        "v_mfma_f32_32x32x16_bf16 v[64:79], v[152:155], v[80:83], v[64:79]\n\t"
        "v_cvt_pk_f16_f32 v101, v106, v107\n\t"
        "v_rcp_f16_sdwa v100, v100 dst_sel:WORD_1 dst_unused:UNUSED_PRESERVE src0_sel:WORD_1\n\t"
        "v_pk_add_f16 v101, v101, s96\n\t"
        "v_exp_f32 v108, v108\n\t"
        "v_mfma_f32_32x32x16_bf16 v[64:79], v[156:159], v[204:207], v[64:79]\n\t"
        "v_cvt_pk_f16_f32 v116, v120, v121\n\t"
        "v_rcp_f16_sdwa v101, v101 dst_sel:WORD_0 dst_unused:UNUSED_PRESERVE src0_sel:WORD_0\n\t"
        "v_pk_mul_f16 v116, v116, v100\n\t"
        "v_exp_f32 v109, v109\n\t"
        "s_waitcnt lgkmcnt(0)\n\t"
        "v_mfma_f32_32x32x16_bf16 v[80:95], v[144:147], v[84:87], 0\n\t"
        "v_add_u32 v160, 0x100, v160\n\t"
        "v_cvt_pk_f16_f32 v102, v108, v109\n\t"
        "v_rcp_f16_sdwa v101, v101 dst_sel:WORD_1 dst_unused:UNUSED_PRESERVE src0_sel:WORD_1\n\t"
        "v_pk_add_f16 v102, v102, s96\n\t"
        "v_exp_f32 v110, v110\n\t"
        "v_mfma_f32_32x32x16_bf16 v[80:95], v[148:151], v[208:211], v[80:95]\n\t"
        "v_add_u32 v161, 0x100, v161\n\t"
        "v_cvt_pk_f16_f32 v117, v122, v123\n\t"
        "v_rcp_f16_sdwa v102, v102 dst_sel:WORD_0 dst_unused:UNUSED_PRESERVE src0_sel:WORD_0\n\t"
        "v_pk_mul_f16 v117, v117, v101\n\t"
        "v_exp_f32 v111, v111\n\t"
        "v_mfma_f32_32x32x16_bf16 v[80:95], v[152:155], v[212:215], v[80:95]\n\t"
        "v_add_u32 v162, 0x100, v162\n\t"
        "v_cvt_pk_f16_f32 v103, v110, v111\n\t"
        "v_rcp_f16_sdwa v102, v102 dst_sel:WORD_1 dst_unused:UNUSED_PRESERVE src0_sel:WORD_1\n\t"
        "v_pk_add_f16 v103, v103, s96\n\t"
        "v_exp_f32 v64, v64\n\t"
        "v_exp_f32 v65, v65\n\t"
        "v_mfma_f32_32x32x16_bf16 v[80:95], v[156:159], v[216:219], v[80:95]\n\t"
        "v_add_u32 v163, 0x100, v163\n\t"
        "v_cvt_pk_f16_f32 v118, v124, v125\n\t"
        "v_rcp_f16_sdwa v103, v103 dst_sel:WORD_0 dst_unused:UNUSED_PRESERVE src0_sel:WORD_0\n\t"
        "v_pk_mul_f16 v118, v118, v102\n\t"
        "v_exp_f32 v66, v66\n\t"
        "v_cvt_pk_f16_f32 v64, v64, v65\n\t"
        "s_branch .Lrt_%=\n\t"
        ".Lrl_%=:\n\t"
        "v_mfma_f32_32x32x16_f16 v[0:15], v[64:67], v[176:179], v[0:15]\n\t"
        "ds_read_b64_tr_b16 v[184:185], v175\n\t"
        "ds_read_b64_tr_b16 v[186:187], v175 offset:512\n\t"
        "ds_read_b64_tr_b16 v[188:189], v175 offset:1024\n\t"
        "ds_read_b64_tr_b16 v[190:191], v175 offset:1536\n\t"
        "v_rcp_f16_sdwa v71, v71 dst_sel:WORD_1 dst_unused:UNUSED_PRESERVE src0_sel:WORD_1\n\t"
        "v_cvt_pk_f16_f32 v87, v94, v95\n\t"
        "v_pk_add_f16 v96, v96, s96\n\t"
        "v_exp_f32 v99, v99\n\t"
        "v_rcp_f16_sdwa v96, v96 dst_sel:WORD_0 dst_unused:UNUSED_PRESERVE src0_sel:WORD_0\n\t"
        "v_mfma_f32_32x32x16_f16 v[32:47], v[80:83], v[176:179], v[32:47]\n\t"
        "ds_read_b64_tr_b16 v[192:193], v175 offset:2048\n\t"
        "ds_read_b64_tr_b16 v[194:195], v175 offset:2560\n\t"
        "ds_read_b64_tr_b16 v[196:197], v175 offset:3072\n\t"
        "ds_read_b64_tr_b16 v[198:199], v175 offset:3584\n\t"
        "v_pk_mul_f16 v87, v87, v71\n\t"
        "v_cvt_pk_f16_f32 v97, v98, v99\n\t"
        "v_rcp_f16_sdwa v96, v96 dst_sel:WORD_1 dst_unused:UNUSED_PRESERVE src0_sel:WORD_1\n\t"
        "v_pk_add_f16 v97, v97, s96\n\t"
        "v_exp_f32 v100, v100\n\t"
        "v_mfma_f32_32x32x16_f16 v[0:15], v[68:71], v[180:183], v[0:15]\n\t"
        "ds_read_b64_tr_b16 v[200:201], v175 offset:32\n\t"
        "ds_read_b64_tr_b16 v[202:203], v175 offset:544\n\t"
        "ds_read_b64_tr_b16 v[204:205], v175 offset:1056\n\t"
        "ds_read_b64_tr_b16 v[206:207], v175 offset:1568\n\t"
        "s_waitcnt lgkmcnt(14)\n\t"
        "v_lshlrev_b32 v216, 16, v144\n\t"
        "v_and_b32 v217, 0xffff0000, v144\n\t"
        "v_cvt_pk_f16_f32 v112, v112, v113\n\t"
        "v_rcp_f16_sdwa v97, v97 dst_sel:WORD_0 dst_unused:UNUSED_PRESERVE src0_sel:WORD_0\n\t"
        "v_pk_mul_f16 v112, v112, v96\n\t"
        "v_exp_f32 v101, v101\n\t"
        "v_mfma_f32_32x32x16_f16 v[32:47], v[84:87], v[180:183], v[32:47]\n\t"
        "ds_read_b64_tr_b16 v[208:209], v175 offset:2080\n\t"
        "ds_read_b64_tr_b16 v[210:211], v175 offset:2592\n\t"
        "ds_read_b64_tr_b16 v[212:213], v175 offset:3104\n\t"
        "ds_read_b64_tr_b16 v[214:215], v175 offset:3616\n\t"
        "v_lshlrev_b32 v218, 16, v145\n\t"
        "v_and_b32 v219, 0xffff0000, v145\n\t"
        "v_cvt_pk_f16_f32 v98, v100, v101\n\t"
        "v_rcp_f16_sdwa v97, v97 dst_sel:WORD_1 dst_unused:UNUSED_PRESERVE src0_sel:WORD_1\n\t"
        "v_pk_add_f16 v98, v98, s96\n\t"
        "v_exp_f32 v102, v102\n\t"
        "v_lshlrev_b32 v220, 16, v146\n\t"
        "v_and_b32 v221, 0xffff0000, v146\n\t"
        "v_lshlrev_b32 v222, 16, v147\n\t"
        "v_and_b32 v223, 0xffff0000, v147\n\t"
        "s_waitcnt lgkmcnt(14)\n\t"
        "v_lshlrev_b32 v224, 16, v148\n\t"
        "v_and_b32 v225, 0xffff0000, v148\n\t"
        "v_cvt_pk_f16_f32 v113, v114, v115\n\t"
        "v_rcp_f16_sdwa v98, v98 dst_sel:WORD_0 dst_unused:UNUSED_PRESERVE src0_sel:WORD_0\n\t"
        "v_pk_mul_f16 v113, v113, v97\n\t"
        "v_exp_f32 v103, v103\n\t"
        "v_lshlrev_b32 v226, 16, v149\n\t"
        "v_and_b32 v227, 0xffff0000, v149\n\t"
        "v_lshlrev_b32 v228, 16, v150\n\t"
        "v_and_b32 v229, 0xffff0000, v150\n\t"
        "v_lshlrev_b32 v230, 16, v151\n\t"
        "v_and_b32 v231, 0xffff0000, v151\n\t"
        "v_cvt_pk_f16_f32 v99, v102, v103\n\t"
        "v_rcp_f16_sdwa v98, v98 dst_sel:WORD_1 dst_unused:UNUSED_PRESERVE src0_sel:WORD_1\n\t"
        "v_pk_add_f16 v99, v99, s96\n\t"
        "v_exp_f32 v104, v104\n\t"
        "s_waitcnt lgkmcnt(14)\n\t"
        "v_mfma_f32_32x32x16_bf16 v[64:79], v[184:187], v[144:147], v[216:231]\n\t"
        "v_lshlrev_b32 v232, 16, v152\n\t"
        "v_and_b32 v233, 0xffff0000, v152\n\t"
        "v_cvt_pk_f16_f32 v114, v116, v117\n\t"
        "v_rcp_f16_sdwa v99, v99 dst_sel:WORD_0 dst_unused:UNUSED_PRESERVE src0_sel:WORD_0\n\t"
        "v_pk_mul_f16 v114, v114, v98\n\t"
        "v_exp_f32 v105, v105\n\t"
        "s_waitcnt lgkmcnt(12)\n\t"
        "v_mfma_f32_32x32x16_bf16 v[64:79], v[188:191], v[148:151], v[64:79]\n\t"
        "v_lshlrev_b32 v234, 16, v153\n\t"
        "v_and_b32 v235, 0xffff0000, v153\n\t"
        "v_cvt_pk_f16_f32 v100, v104, v105\n\t"
        "v_rcp_f16_sdwa v99, v99 dst_sel:WORD_1 dst_unused:UNUSED_PRESERVE src0_sel:WORD_1\n\t"
        "v_pk_add_f16 v100, v100, s96\n\t"
        "v_exp_f32 v106, v106\n\t"
        "s_waitcnt lgkmcnt(10)\n\t"
        "v_mfma_f32_32x32x16_bf16 v[64:79], v[192:195], v[152:155], v[64:79]\n\t"
        "v_lshlrev_b32 v236, 16, v154\n\t"
        "v_and_b32 v237, 0xffff0000, v154\n\t"
        "v_cvt_pk_f16_f32 v115, v118, v119\n\t"
        "v_rcp_f16_sdwa v100, v100 dst_sel:WORD_0 dst_unused:UNUSED_PRESERVE src0_sel:WORD_0\n\t"
        "v_pk_mul_f16 v115, v115, v99\n\t"
        "v_exp_f32 v107, v107\n\t"
        "s_waitcnt lgkmcnt(8)\n\t"
        "v_mfma_f32_32x32x16_bf16 v[64:79], v[196:199], v[156:159], v[64:79]\n\t"
        "v_lshlrev_b32 v238, 16, v155\n\t"
        "v_and_b32 v239, 0xffff0000, v155\n\t"
        "v_cvt_pk_f16_f32 v101, v106, v107\n\t"
        "v_rcp_f16_sdwa v100, v100 dst_sel:WORD_1 dst_unused:UNUSED_PRESERVE src0_sel:WORD_1\n\t"
        "v_pk_add_f16 v101, v101, s96\n\t"
        "v_exp_f32 v108, v108\n\t"
        "v_lshlrev_b32 v240, 16, v156\n\t"
        "v_and_b32 v241, 0xffff0000, v156\n\t"
        "v_lshlrev_b32 v242, 16, v157\n\t"
        "v_and_b32 v243, 0xffff0000, v157\n\t"
        "v_lshlrev_b32 v244, 16, v158\n\t"
        "v_and_b32 v245, 0xffff0000, v158\n\t"
        "v_lshlrev_b32 v246, 16, v159\n\t"
        "v_and_b32 v247, 0xffff0000, v159\n\t"
        "v_cvt_pk_f16_f32 v116, v120, v121\n\t"
        "v_rcp_f16_sdwa v101, v101 dst_sel:WORD_0 dst_unused:UNUSED_PRESERVE src0_sel:WORD_0\n\t"
        "v_pk_mul_f16 v116, v116, v100\n\t"
        "v_exp_f32 v109, v109\n\t"
        "s_waitcnt lgkmcnt(6)\n\t"
        "v_mfma_f32_32x32x16_bf16 v[80:95], v[200:203], v[144:147], v[232:247]\n\t"
        "v_cvt_pk_f16_f32 v102, v108, v109\n\t"
        "v_rcp_f16_sdwa v101, v101 dst_sel:WORD_1 dst_unused:UNUSED_PRESERVE src0_sel:WORD_1\n\t"
        "v_pk_add_f16 v102, v102, s96\n\t"
        "v_exp_f32 v110, v110\n\t"
        "s_waitcnt lgkmcnt(4)\n\t"
        "v_mfma_f32_32x32x16_bf16 v[80:95], v[204:207], v[148:151], v[80:95]\n\t"
        "v_cvt_pk_f16_f32 v117, v122, v123\n\t"
        "v_rcp_f16_sdwa v102, v102 dst_sel:WORD_0 dst_unused:UNUSED_PRESERVE src0_sel:WORD_0\n\t"
        "v_pk_mul_f16 v117, v117, v101\n\t"
        "v_exp_f32 v111, v111\n\t"
        "s_waitcnt lgkmcnt(2)\n\t"
        "v_mfma_f32_32x32x16_bf16 v[80:95], v[208:211], v[152:155], v[80:95]\n\t"
        "v_cvt_pk_f16_f32 v103, v110, v111\n\t"
        "v_rcp_f16_sdwa v102, v102 dst_sel:WORD_1 dst_unused:UNUSED_PRESERVE src0_sel:WORD_1\n\t"
        "v_pk_add_f16 v103, v103, s96\n\t"
        "s_waitcnt lgkmcnt(0)\n\t"
        "v_mfma_f32_32x32x16_bf16 v[80:95], v[212:215], v[156:159], v[80:95]\n\t"
        "v_cvt_pk_f16_f32 v118, v124, v125\n\t"
        "v_rcp_f16_sdwa v103, v103 dst_sel:WORD_0 dst_unused:UNUSED_PRESERVE src0_sel:WORD_0\n\t"
        "v_pk_mul_f16 v118, v118, v102\n\t"
        "v_mfma_f32_32x32x16_f16 v[16:31], v[96:99], v[176:179], v[16:31]\n\t"
        "v_rcp_f16_sdwa v103, v103 dst_sel:WORD_1 dst_unused:UNUSED_PRESERVE src0_sel:WORD_1\n\t"
        "v_cvt_pk_f16_f32 v119, v126, v127\n\t"
        "v_mfma_f32_32x32x16_f16 v[48:63], v[112:115], v[176:179], v[48:63]\n\t"
        "v_pk_mul_f16 v119, v119, v103\n\t"
        "v_mfma_f32_32x32x16_f16 v[16:31], v[100:103], v[180:183], v[16:31]\n\t"
        "s_nop 0\n\t"
        "v_mfma_f32_32x32x16_f16 v[48:63], v[116:119], v[180:183], v[48:63]\n\t"
        : "+{v[0:15]}"(Y[0]), "+{v[16:31]}"(Y[1]), "+{v[32:47]}"(Y[2]), "+{v[48:63]}"(Y[3]), "={v[64:79]}"(U[0]),
          "={v[80:95]}"(U[1]), "={v[216:231]}"(X[0]), "={v[232:247]}"(X[1]), "+{v[160:175]}"(AD), [nt] "+s"(nt)
        : "{v[128:143]}"(XAf), [x] "s"(x), [es] "s"(ES), [ring] "s"(ring0), [wave] "s"(wave), [flag] "s"(mode),
          [cu] "n"(cu)
        :
          "memory", "scc", "m0", "s94", "s95", "s96", "v96", "v97", "v98", "v99", "v100", "v101", "v102", "v103",
          "v104", "v105", "v106", "v107", "v108", "v109", "v110", "v111", "v112", "v113", "v114", "v115", "v116",
          "v117", "v118", "v119", "v120", "v121", "v122", "v123", "v124", "v125", "v126", "v127", "v144", "v145",
          "v146", "v147", "v148", "v149", "v150", "v151", "v152", "v153", "v154", "v155", "v156", "v157", "v158",
          "v159", "v176", "v177", "v178", "v179", "v180", "v181", "v182", "v183", "v184", "v185", "v186", "v187",
          "v188", "v189", "v190", "v191", "v192", "v193", "v194", "v195", "v196", "v197", "v198", "v199", "v200",
          "v201", "v202", "v203", "v204", "v205", "v206", "v207", "v208", "v209", "v210", "v211", "v212", "v213",
          "v214", "v215", "v248", "v249", "v250", "v251", "v252", "v253", "v254", "v255");
  }
  SB();
  auto wide = [&]() __attribute__((always_inline)) {
    int l2;
    asm volatile("v_mbcnt_lo_u32_b32 %0, -1, 0\n\tv_mbcnt_hi_u32_b32 %0, -1, %0\n\ts_waitcnt vmcnt(0)" : "=v"(l2) :: "memory");
    AD[0] = la + (4 * (l2 >> 5) + ((l2 >> 2) & 3)) * 16;
    AD[1] = la + (((l2 & 7) << 2) | ((l2 >> 3) & 3)) * 4 + 2;
    AD[2] = la + (l2 >> 3) * 16;
    AD[3] = la + LARR + ((((l2 >> 1) & 7) << 2) | (l2 >> 4)) * 4;
    auto rd = [&](unsigned a) { return *(const LAS bf16x8*)(size_t)a; };
    auto one = [&](bf16x8 v) {
      const u32x4 t = __builtin_bit_cast(u32x4, v);
      return __builtin_bit_cast(bf16x8, t | (t >> 3));
    };
    bf16x8 xq[4];
    {
      f32x16 T[2] = {zero16, zero16};
#pragma unroll
      for (int k = 0; k < 4; ++k) {
        u32x4 t;
#pragma unroll
        for (int j = 0; j < 4; ++j) t[j] = pack_bf16(X[k >> 1][8 * (k & 1) + 2 * j], X[k >> 1][8 * (k & 1) + 2 * j + 1]);
#pragma unroll
        for (int ct = 0; ct < 2; ++ct)
          T[ct] = MFMA32(__builtin_bit_cast(bf16x8, t), rawfrag(AD[15] + (NBLOB - 18 + k) * 1024 + ct * 32), T[ct]);
      }
#pragma unroll
      for (int q = 0; q < 4; ++q) {
        u32x4 t;
#pragma unroll
        for (int j = 0; j < 4; ++j)
          t[j] = pack_bf16(T[q & 1][8 * (q >> 1) + 2 * j] * NLOG2E, T[q & 1][8 * (q >> 1) + 2 * j + 1] * NLOG2E);
        xq[q] = __builtin_bit_cast(bf16x8, t);
      }
    }
#pragma unroll
    for (int i = 0; i < 16; ++i) { Y[0][i] = 1e-6f * (1 << RA); Y[1][i] = 1e-6f * (1 << RA); Y[2][i] = 0.f; Y[3][i] = 0.f; }
#pragma unroll 1
    do {
      const int n = slow ? gen() : cnt;
      issue_gather(0, 0);
#pragma unroll 1
      for (int t = 0; t * 32 < n; ++t) {
        const unsigned ro = (t & 1) * ringd;
        asm volatile("s_waitcnt lgkmcnt(0)" ::: "memory");
        issue_gather(t + 1, ~t & 1);
        const u32x4 ss = ld128(AD[0] + t * 128);
        const unsigned st = *(const LAS unsigned short*)(size_t)(AD[1] + t * 128) * TPITCH + AD[8];
        s16x4 a[4];
#pragma unroll
        for (int q = 0; q < 4; ++q)
          a[q] = __builtin_amdgcn_ds_read_tr16_b64_v4i16((LAS s16x4*)(size_t)((ss[q] >> 16) * TPITCH + AD[7]));
        const bf16x8 S0 = one(__builtin_shufflevector(a[0], a[1], 0, 1, 2, 3, 4, 5, 6, 7));
        const bf16x8 S1 = one(__builtin_shufflevector(a[2], a[3], 0, 1, 2, 3, 4, 5, 6, 7));
        const bf16x8 T0 = one(rd(st)), T1 = one(rd(st + 16));
        asm volatile("s_waitcnt vmcnt(5)" ::: "memory");
#pragma unroll
        for (int ct = 0; ct < 2; ++ct) {
          const unsigned w = AD[14] + OFF_W + ct * 4096;
          f32x16 G = MFMA32(T0, xq[ct], zero16), V = zero16;
          G = MFMA32(T1, xq[2 + ct], G);
          G = MFMA32(rd(AD[13] + ro), rd(AD[14] + OFF_W + (16 + ct) * 1024), G);
#pragma unroll
          for (int k = 0; k < 4; ++k) {
            const bf16x8 as = rd(AD[9 + k] + ro);
            G = MFMA32(as, rd(w + k * 1024), G);
            V = MFMA32(as, rd(w + (8 + k) * 1024), V);
          }
          u32x4 pd[2], pn[2];
#pragma unroll
          for (int i = 0; i < 16; i += 2) {
            constexpr float u = 1.0f / (1 << RA);
            const float r0 = __builtin_amdgcn_rcpf(__builtin_fmaf(__builtin_amdgcn_exp2f(G[i]), u, u));
            const float r1 = __builtin_amdgcn_rcpf(__builtin_fmaf(__builtin_amdgcn_exp2f(G[i + 1]), u, u));
            pd[i >> 3][(i >> 1) & 3] = pack_bf16(r0, r1);
            pn[i >> 3][(i >> 1) & 3] = pack_bf16(r0 * V[i], r1 * V[i + 1]);
          }
          Y[ct] = MFMA32(__builtin_bit_cast(bf16x8, pd[0]), S0, Y[ct]);
          Y[ct] = MFMA32(__builtin_bit_cast(bf16x8, pd[1]), S1, Y[ct]);
          Y[2 + ct] = MFMA32(__builtin_bit_cast(bf16x8, pn[0]), S0, Y[2 + ct]);
          Y[2 + ct] = MFMA32(__builtin_bit_cast(bf16x8, pn[1]), S1, Y[2 + ct]);
          SB();
        }
      }
      asm volatile("s_waitcnt vmcnt(0) lgkmcnt(0)" ::: "memory");
    } while (slow && gs < GEND);
  };

  const unsigned ot = ring0 + (max(cnt - 1, 0) & 32 ? 0u : ringd);
  auto finish = [&](const bool check) __attribute__((always_inline)) {
    auto inv = [&](f32x16& y) {
      y *= VSC;
#pragma unroll
      for (int i = 0; i < 16; ++i) y[i] = __builtin_amdgcn_rcpf(y[i]);
    };
    inv(Y[0]);
    SB();
    int lane;
    asm volatile("v_mbcnt_lo_u32_b32 %0, -1, 0\n\tv_mbcnt_hi_u32_b32 %0, -1, %0" : "=v"(lane));
    const int h = lane >> 5;
    const unsigned nd = nodeof(cg + (lane & 31));
    const bool live = (nd * Mw + Cw) >> 16 == cg + (lane & 31);
    const unsigned lm = (unsigned)__ballot(live);
    const int nodes = __builtin_popcount(lm);
    const unsigned n0 = __builtin_amdgcn_readlane(nd, __builtin_ctz(lm | 0x80000000u));
    const int r = live ? nd - n0 : 31;
    SB();
    inv(Y[1]);
    unsigned ov[16];
#pragma unroll
    for (int p = 0; p < 4; ++p) {
      const int ct = p >> 1;
#pragma unroll
      for (int j = 0; j < 4; ++j) {
        const int i = 8 * (p & 1) + 2 * j;
        const f32x2 z = __builtin_elementwise_fma(f32x2{Y[2 + ct][i], Y[2 + ct][i + 1]}, f32x2{Y[ct][i], Y[ct][i + 1]},
                                                  f32x2{U[ct][i], U[ct][i + 1]});
        ov[4 * p + j] = pack_bf16(MAXN(z[0], X[ct][i]), MAXN(z[1], X[ct][i + 1]));
      }
      *(LAS u32x4*)(size_t)(ot + r * 128 + (((4 * h + p) ^ (r & 7)) * 16)) =
          u32x4{ov[4 * p], ov[4 * p + 1], ov[4 * p + 2], ov[4 * p + 3]};
    }
    u32x4 ow[4];
#pragma unroll
    for (int q = 0; q < 4; ++q)
      ow[q] = *(const LAS u32x4*)(size_t)(ot + (8 * q + (lane >> 3)) * 128 + (((lane & 7) ^ (lane >> 3)) * 16));
    asm volatile("v_cmpx_lt_i32 vcc, 0, %6\n\t"
                 "global_store_dwordx4 %0, %1, %5 sc1\n\t"
                 "v_cmpx_lt_i32 vcc, 8, %6\n\t"
                 "global_store_dwordx4 %0, %2, %5 offset:1024 sc1\n\t"
                 "v_cmpx_lt_i32 vcc, 16, %6\n\t"
                 "global_store_dwordx4 %0, %3, %5 offset:2048 sc1\n\t"
                 "v_cmpx_lt_i32 vcc, 24, %6\n\t"
                 "global_store_dwordx4 %0, %4, %5 offset:3072 sc1\n\t"
                 "s_mov_b64 exec, -1"
                 :: "v"((unsigned)n0 * 128u + lane * 16u), "v"(ow[0]), "v"(ow[1]), "v"(ow[2]), "v"(ow[3]), "s"(out),
                    "v"(nodes - (lane >> 3))
                 : "memory", "vcc");
    SB();
    if (!check) return false;
    constexpr float WLIM = 1 << (22 - RA + VK);
    float wm = MAXN(Y[0][0], Y[1][0]);
#pragma unroll
    for (int i = 1; i < 16; ++i) wm = MAXN(MAXN(wm, Y[0][i]), Y[1][i]);
    f16x2 om = __builtin_bit_cast(f16x2, ov[0]);
#pragma unroll
    for (int i = 1; i < 16; ++i) om = MAXN(om, __builtin_bit_cast(f16x2, ov[i]));
    const _Float16 o1 = MAXN(om[0], om[1]);
    const unsigned long long lv = lm * 0x100000001ull;
    unsigned long long bad = __ballot(o1 != o1) | (__ballot(o1 < __builtin_bit_cast(_Float16, (unsigned short)((127 - 9 + VK) << 7))) & lv);
    if (__builtin_expect((__ballot(wm > WLIM / LCAP) & lv) != 0ull, 0)) {
      LAS unsigned* dg = (LAS unsigned*)(size_t)ot;
      dg[lane & 31] = 0u;
#pragma unroll 1
      for (int i = lane; i < cnt; i += 64)
        __hip_atomic_fetch_add(dg + (*(const LAS unsigned short*)(size_t)(la + i * 4 + 2) & 31), 1u, __ATOMIC_RELAXED,
                               __HIP_MEMORY_SCOPE_WORKGROUP);
      bad |= __ballot(wm * (float)dg[lane & 31] > WLIM) & lv;
    }
    return slow || bad != 0ull;
  };
  if (__builtin_expect(finish(true), 0)) {
    wide();
    finish(false);
  }
  if (bk == 0 && wave == 7) asm volatile("global_store_dwordx2 %0, %1, %2 sc1" :: "v"(0u), "v"(ep), "s"(FL) : "memory");
  asm volatile("s_waitcnt vmcnt(0)" ::: "memory");
}

extern "C" void kernel_launch(void* const* d_in, const int* in_sizes, int n_in,
                              void* d_out, int out_size, void* d_ws, size_t ws_size,
                              hipStream_t stream) {
  const unsigned short* x = (const unsigned short*)d_in[0];
  const u32x4* eattr = (const u32x4*)d_in[1];
  const int* src = (const int*)d_in[2];
  const int* dst = (const int*)d_in[3];
  const unsigned short* wa = (const unsigned short*)d_in[4];
  const unsigned short* wb = (const unsigned short*)d_in[5];
  const unsigned short* wc = (const unsigned short*)d_in[6];
  const unsigned short* wu = (const unsigned short*)d_in[7];
  const unsigned short* wv = (const unsigned short*)d_in[8];
  unsigned short* out = (unsigned short*)d_out;
  const int N = in_sizes[0] / 64;
  const int E = in_sizes[2];
  const int chunk = (((E + NCH - 1) / NCH) + 3) & ~3;

  const bool skew = N > 4096 && N < 60000;
  const unsigned M = N > 256 ? (unsigned)(4294967295.0 / ((double)N * (skew ? 1.0 + SKEWF : 1.0))) : 0xffffffu;
  const unsigned N1 = (0x7fffffffu / M) + 1u;
  const unsigned M2 = skew ? (0xffffffffu - N1 * M) / ((unsigned)N - N1) : M;

  auto fb = [](double v) { return __builtin_bit_cast(unsigned, (float)v); };
  char* ws = (char*)d_ws;
  size_t off = 0;
  off = 1 << 20;
  unsigned long long* FL = (unsigned long long*)(ws + off);   off += 1 << 20;
  u32x4* ENT = (u32x4*)(ws + off);                       off += (size_t)2 * IEXT;
  unsigned long long* TS = (unsigned long long*)(ws + off);

  k_main<<<NBK, 512, 0, stream>>>(x, eattr, src, dst, wa, wb, wc, wu, wv, ENT, TS, FL, out, N, E, chunk, M, N1,
                                  M2 - M, fb(65536.0 / M), fb(65536.0 / M2), fb(32768.0 / M + 0.5),
                                  fb((double)N1 + (32768.0 - (double)N1 * M) / M2 + 0.5)
     );
}
```

```cpp
#define QSEL 100
#define DBGDUMP 1
#include <hip/hip_runtime.h>
#include <hip/hip_bf16.h>
#include <stdint.h>

using bf16x8 = __attribute__((ext_vector_type(8))) short;
using s16x4 = __attribute__((ext_vector_type(4))) short;
using f32x16 = __attribute__((ext_vector_type(16))) float;
using f32x2 = __attribute__((ext_vector_type(2))) float;
using u32x4 = __attribute__((ext_vector_type(4))) unsigned;
using u32x16 = __attribute__((ext_vector_type(16))) unsigned;
using u32x2 = __attribute__((ext_vector_type(2))) unsigned;
using f16x2 = __attribute__((ext_vector_type(2))) _Float16;

#define MFMA32(a, b, c) __builtin_amdgcn_mfma_f32_32x32x16_bf16((a), (b), (c), 0, 0, 0)
#define RA 14
#define VK 4
#define LAS __attribute__((address_space(3)))
#define RANKOF(w) ((w) < 4 ? 7 - (w) : (w) - 4)
#define G1W 4
#define DBGWAIT 1
#define CPF 32
#define CPFB 19712
#define SKEWF 0.015
#define XLIKE(c) __builtin_expect(c, 0)
#define SB() __builtin_amdgcn_sched_barrier(0)

constexpr int NCH = 256;
constexpr int NBK = 256;
constexpr int NFRAG = 39;
constexpr float NLOG2E = -1.44269504088896340736f;
constexpr float VSC = 1.0f / (1 << VK);
constexpr unsigned TPITCH = 80;

__device__ __forceinline__ unsigned pack_bf16(float lo, float hi) {
  typedef __attribute__((ext_vector_type(2))) float f2;
  typedef __attribute__((ext_vector_type(2))) __bf16 b2;
  f2 v = {lo, hi};
  b2 r = __builtin_convertvector(v, b2);
  return __builtin_bit_cast(unsigned, r);
}

__device__ __forceinline__ unsigned pack_f16(float lo, float hi) {
  unsigned r;
  asm("v_cvt_pk_f16_f32 %0, %1, %2" : "=v"(r) : "v"(lo), "v"(hi));
  return r;
}

__device__ __forceinline__ void glds16(const void* sbase, unsigned voff, unsigned lds_dst) {
  asm volatile(
      "s_mov_b32 m0, %2\n\t"
      "s_nop 0\n\t"
      "global_load_lds_dwordx4 %0, %1"
      :: "v"(voff), "s"(sbase), "s"(lds_dst) : "memory", "m0");
}
__device__ __forceinline__ void glds16x(const void* sbase, unsigned a, unsigned c, unsigned lds_dst) {
  unsigned tmp;
  asm volatile(
      "s_mov_b32 m0, %4\n\t"
      "v_mad_u32_u16 %0, %1, %5, %2\n\t"
      "global_load_lds_dwordx4 %0, %3"
      : "=&v"(tmp) : "v"(a), "v"(c), "s"(sbase), "s"(lds_dst), "s"(128u) : "memory", "m0");
}
__device__ __forceinline__ void glds16e(const void* sbase, unsigned a, unsigned c, unsigned lds_dst) {
  unsigned tmp;
  asm volatile(
      "s_mov_b32 m0, %4\n\t"
      "v_add_u32 %0, %1, %2\n\t"
      "global_load_lds_dwordx4 %0, %3"
      : "=&v"(tmp) : "v"(a), "v"(c), "s"(sbase), "s"(lds_dst) : "memory", "m0");
}
__device__ __forceinline__ void gstore128wt(void* sbase, unsigned voff, u32x4 v) {
  asm volatile("global_store_dwordx4 %0, %1, %2 sc1\n\ts_nop 1" :: "v"(voff), "v"(v), "s"(sbase) : "memory");
}

constexpr int IBOX = 40960;
constexpr int IEXT = 1 << 24;
constexpr int CPIT = 208;
constexpr unsigned RINC = 87382;
constexpr int KP_HDR = 0;
constexpr int KP_ENT = KP_HDR + NBK * 4;
constexpr int KP_POS = KP_ENT + NBK * CPIT;
constexpr int KP_LDS = KP_POS + NBK * 4;
constexpr int NWL = 26;
constexpr int NBLOB = 31;

__device__ __forceinline__ unsigned mul24s(unsigned a, unsigned b) {
  unsigned r;
  asm("v_mul_u32_u24 %0, %1, %2" : "=v"(r) : "s"(b), "v"(a));
  return r;
}
__device__ __forceinline__ unsigned byte3x(unsigned a, unsigned b) {
  unsigned r;
  asm("v_mul_u32_u24_sdwa %0, %1, %2 dst_sel:DWORD dst_unused:UNUSED_PAD src0_sel:DWORD src1_sel:BYTE_3" : "=v"(r) : "s"(b), "v"(a));
  return r;
}
constexpr int RING_SLOT = 5 * 1024;
constexpr int LCAP = 512;
constexpr int LARR = 2560;
constexpr int LIST_BYTES = 2 * LARR;
constexpr int OFF_CNT = 0;
constexpr int SUBL = LARR / 4;
constexpr int OFF_XF = 256;
constexpr int OFF_W = 512;
constexpr int OFF_T1 = OFF_W + NWL * 1024;
constexpr int OFF_T2 = OFF_T1 + 32 * TPITCH;
constexpr int OFF_R0 = OFF_W + NFRAG * 1024;
constexpr int OFF_R1 = OFF_R0 + 8 * RING_SLOT;
constexpr int OFF_L = OFF_R1 + 8 * RING_SLOT;
constexpr int MAIN_LDS = OFF_L + 8 * LIST_BYTES;
static_assert(MAIN_LDS <= 160 * 1024, "LDS");
static_assert(KP_LDS <= 16 * RING_SLOT, "phase 1 lives in the ring");

__device__ __forceinline__ unsigned ldd(const unsigned* p) {
  return __hip_atomic_load(p, __ATOMIC_RELAXED, __HIP_MEMORY_SCOPE_AGENT);
}
__device__ __forceinline__ unsigned long long ldq(const unsigned long long* p) {
  return __hip_atomic_load(p, __ATOMIC_RELAXED, __HIP_MEMORY_SCOPE_AGENT);
}
__device__ __forceinline__ bf16x8 rawfrag(unsigned a) {
  const s16x4 a0 = __builtin_amdgcn_ds_read_tr16_b64_v4i16((LAS s16x4*)(size_t)a);
  const s16x4 a1 = __builtin_amdgcn_ds_read_tr16_b64_v4i16((LAS s16x4*)(size_t)(a + 512));
  return __builtin_shufflevector(a0, a1, 0, 1, 2, 3, 4, 5, 6, 7);
}
#define MAXN(a, b) __builtin_elementwise_maximum(a, b)

__global__ __launch_bounds__(512, 2) void k_main(const unsigned short* __restrict__ x,
                                                 const u32x4* __restrict__ ES,
                                                 const int* __restrict__ src, const int* __restrict__ dst,
                                                 const unsigned short* __restrict__ wa_g,
                                                 const unsigned short* __restrict__ wb_g,
                                                 const unsigned short* __restrict__ wc_g,
                                                 const unsigned short* __restrict__ wu_g,
                                                 const unsigned short* __restrict__ wv_g,
                                                 u32x4* ENT, unsigned long long* TS, unsigned long long* FL,
                                                 unsigned short* __restrict__ out, int N, int E, int chunk,
                                                 unsigned M, unsigned N1, unsigned DM, unsigned MF0, unsigned MF1,
                                                 unsigned MB0, unsigned MB1
                                                 ) {
  unsigned tm0v = (unsigned)__builtin_amdgcn_s_memrealtime(); __builtin_amdgcn_sched_barrier(0);
  unsigned stv = 0;
#define PT(i) asm volatile("s_memrealtime s[92:93]\n\ts_waitcnt lgkmcnt(0)\n\tv_writelane_b32 %0, s92, " #i : "+v"(stv) :: "memory", "s92", "s93")
#define PV(i, v) asm volatile("v_writelane_b32 %0, %1, " #i : "+v"(stv) : "s"(v))

  __shared__ __attribute__((aligned(16))) char smem[MAIN_LDS + 512];
  const int tid0 = threadIdx.x;
  const int wave = __builtin_amdgcn_readfirstlane(tid0 >> 6);
  const int bk = blockIdx.x;
  const unsigned lds0 = (unsigned)(size_t)(LAS char*)smem;

  LAS char* sm = (LAS char*)smem + OFF_R0;
  const unsigned sm0 = lds0 + OFF_R0;
  const int tid = tid0, lane = tid & 63, r = lane & 31, h = lane >> 5;
  const unsigned pc = tid & 7, bq = tid >> 3;
  unsigned ia = sm0 + KP_ENT + bq * CPIT + pc * 16;
#pragma unroll
  for (int s = 0; s < 4; ++s) *(LAS u32x4*)(size_t)(ia + s * 64 * CPIT) = u32x4{~0u, ~0u, ~0u, ~0u};
  *(LAS unsigned*)(sm + KP_HDR + (tid & 255) * 4) = 0u;
  int q0 = 4 * tid + 3, q1 = 4 * tid + 1795;
  unsigned a0 = tid * 16, a1 = tid * 16 + 7168, z0 = 0u;
  unsigned long long m7 = __ballot(tid == 448);
  int n7 = wave == 7 ? 0 : 0x7fffffff;
  unsigned f0 = lane * 128u, f1 = lane * 128u + 8192u, f2 = min(lane * 128u + 16384u, CPFB - 512u);
  unsigned long long p7 = __ballot((wave | (bk & -CPF)) == 7);
  asm volatile("" : "+v"(f0), "+v"(f1), "+v"(f2), "+s"(p7));
  asm volatile("" : "+v"(ia), "+v"(q0), "+v"(q1), "+v"(a0), "+v"(a1), "+v"(z0), "+s"(m7), "+s"(n7) :: "memory");
  SB();
  asm volatile("" :: "s"(x), "s"(ES), "s"(src), "s"(dst), "s"(wa_g), "s"(wb_g), "s"(wc_g), "s"(wu_g), "s"(wv_g), "s"(ENT),
               "s"(TS), "s"(FL), "s"(out), "s"(N), "s"(E), "s"(chunk), "s"(M), "s"(N1), "s"(DM), "s"(MF0), "s"(MF1), "s"(MB0), "s"(MB1));
  const int base = bk * chunk;
  asm volatile("" : "+v"(tm0v));
  const int ecnt = max(min(chunk, E - base), 0);
  unsigned long long on[2];
  on[0] = __ballot(q0 < min(min(chunk, E - base), n7));
  on[1] = __ballot(q1 < min(min(chunk, E - base), n7));
  a0 += base * 4u;
  a1 += base * 4u;
  u32x4 d[2], sv[2];
  u32x2 e0;
  asm volatile("s_mov_b64 exec, %11\n\t"
               "global_load_dwordx4 %0, %5, %8 nt\n\t"
               "s_mov_b64 exec, %12\n\t"
               "global_load_dwordx4 %1, %6, %8 nt\n\t"
               "s_mov_b64 exec, %13\n\t"
               "global_load_dwordx2 %2, %7, %9 sc1\n\t"
               "s_getpc_b64 s[96:97]\n\t"
               "s_mov_b32 m0, %17\n\t"
               "s_mov_b64 exec, %18\n\t"
               "global_load_lds_dword %14, s[96:97]\n\t"
               "global_load_lds_dword %15, s[96:97]\n\t"
               "global_load_lds_dword %16, s[96:97]\n\t"
               "s_mov_b64 exec, %11\n\t"
               "s_waitcnt lgkmcnt(0)\n\t"
               "s_barrier\n\t"
               "global_load_dwordx4 %3, %5, %10 nt\n\t"
               "s_mov_b64 exec, %12\n\t"
               "global_load_dwordx4 %4, %6, %10 nt\n\t"
               "s_mov_b64 exec, -1"
               : "=&{v[240:243]}"(d[0]), "=&{v[244:247]}"(d[1]), "=&{v[238:239]}"(e0), "=&{v[248:251]}"(sv[0]),
                 "=&{v[252:255]}"(sv[1])
               : "v"(a0), "v"(a1), "v"(z0), "s"(dst), "s"(FL), "s"(src), "s"(on[0]), "s"(on[1]), "s"(m7)
                 , "v"(f0), "v"(f1), "v"(f2), "n"(OFF_L + 7 * LIST_BYTES + 4096), "s"(p7)
               : "memory", "m0", "s96", "s97");
  SB();
  const int bu = (bk >> 1) | ((bk & 1) << 7);
  const unsigned Mw = bk & 1 ? M + DM : M, Cw = bk & 1 ? 0u - N1 * DM : 0u;
  unsigned MF = bk & 1 ? MF1 : MF0, MB = bk & 1 ? MB1 : MB0;
  asm volatile("" : "+s"(MF), "+s"(MB));
  char* ib = reinterpret_cast<char*>(ENT) + (size_t)bu * IBOX;
  auto nodeof = [&](unsigned c) { return min((unsigned)__builtin_fmaf((float)c, __uint_as_float(MF), __uint_as_float(MB)), (unsigned)N - 1u); };
  unsigned tup[8], val[8], pm[8];
  u32x4 xg[4];
  const unsigned xn0 = __builtin_amdgcn_readfirstlane(nodeof((bu * 8 + wave) * 32));
  unsigned xd[4];
  auto xprep = [&]() {
#pragma unroll
    for (int i = 0; i < 4; ++i)
      xd[i] = min(xn0 + 8 * i + (lane >> 3), N - 1u) * 128u + ((((lane & 7) ^ (lane >> 4)) * 16) ^ (i & 1) * 64) - i * 1024u;
    asm volatile("" : "+v"(xd[0]), "+v"(xd[1]), "+v"(xd[2]), "+v"(xd[3]));
  };
  auto xdma = [&]() {
    asm volatile("s_mov_b32 m0, %5\n\ts_nop 0\n\t"
                 "global_load_lds_dwordx4 %0, %4\n\t"
                 "global_load_lds_dwordx4 %1, %4 offset:1024\n\t"
                 "global_load_lds_dwordx4 %2, %4 offset:2048\n\t"
                 "global_load_lds_dwordx4 %3, %4 offset:3072"
                 :: "v"(xd[0]), "v"(xd[1]), "v"(xd[2]), "v"(xd[3]), "s"(x), "s"(lds0 + OFF_L + wave * LIST_BYTES)
                 : "memory", "m0");
  };
  auto wload = [&](const unsigned short* w, int f) {
    asm volatile("s_mov_b32 m0, %3\n\ts_nop 0\n\t"
                 "global_load_lds_dwordx4 %0, %2\n\t"
                 "global_load_lds_dwordx4 %0, %2 offset:1024\n\t"
                 "global_load_lds_dwordx4 %0, %2 offset:2048\n\t"
                 "global_load_lds_dwordx4 %0, %2 offset:3072\n\t"
                 "s_addk_i32 m0, 4096\n\ts_nop 0\n\t"
                 "global_load_lds_dwordx4 %1, %2\n\t"
                 "global_load_lds_dwordx4 %1, %2 offset:1024\n\t"
                 "global_load_lds_dwordx4 %1, %2 offset:2048\n\t"
                 "global_load_lds_dwordx4 %1, %2 offset:3072"
                 :: "v"(lane * 16u), "v"(lane * 16u + 4096u), "s"(w), "s"(lds0 + OFF_W + f * 1024) : "memory", "m0", "scc");
  };
  auto wall = [&]() {
    wload(wa_g, NBLOB);
    wload(wb_g, 0);
    wload(wv_g, 8);
    glds16(wc_g, lane * 16, lds0 + OFF_W + 16 * 1024);
    glds16(wc_g + 512, lane * 16, lds0 + OFF_W + 17 * 1024);
  };
  int spin = 1 << 20;
  u32x2 ep = {0u, 0u};
  if (wave == 7) {
    wall();
    *(LAS u32x4*)((LAS char*)smem + OFF_CNT + r * 16) =
        (r & 17) == 0 ? u32x4{lds0 + OFF_L, lds0 + OFF_L + SUBL, lds0 + OFF_L + 2 * SUBL, lds0 + OFF_L + 3 * SUBL}
                      : u32x4{0u, 0u, 0u, 0u};
    {
      const unsigned col = h ? ((r >> 1) & 12u) | ((r << 2) & 16u) | (r & 3u) : r;
      LAS char* tr = (LAS char*)smem + OFF_T1 + lane * TPITCH;
#pragma unroll
      for (int j = 0; j < 4; ++j) *(LAS u32x4*)(tr + j * 16) = u32x4{0u, 0u, 0u, 0u};
      *(LAS unsigned short*)(tr + col * 2) = 0x3C00;
    }
#pragma unroll
    for (int i = 0; i < 8; ++i) { tup[i] = 0u; val[i] = 0u; pm[i] = 0u; }
    asm volatile("s_waitcnt vmcnt(%1)" : "+{v[238:239]}"(e0) : "n"(31) : "memory");
    ep[0] = __builtin_amdgcn_readfirstlane(e0[0]) + 0x9E3779B1u;
    ep[1] = __builtin_amdgcn_readfirstlane(e0[1]) + 0x7F4A7C15u;
    *(LAS u32x2*)((LAS char*)smem + OFF_CNT + 56) = u32x2{ep[0], ep[1] >> 24};
    if (__builtin_expect(ecnt & 3, 0)) {
      const int i = (ecnt & ~3) + lane;
      on[0] = __ballot(i < ecnt);
      if (i < ecnt) {
        pm[0] = (unsigned)dst[base + i] * M + (unsigned)max(dst[base + i] - (int)N1, 0) * DM;
        tup[0] = (unsigned)src[base + i] | (unsigned)i << 16;
        val[0] = __hip_atomic_fetch_add((LAS unsigned*)(sm + KP_HDR) + (pm[0] >> 24), RINC, __ATOMIC_RELAXED, __HIP_MEMORY_SCOPE_WORKGROUP);
        const unsigned u = min(val[0] >> 16, 48u);
        *(LAS unsigned*)(sm + KP_ENT + (pm[0] >> 24) * CPIT + u * 4) = tup[0];
        *(LAS unsigned char*)(sm + KP_ENT + (pm[0] >> 24) * CPIT + ((u * 4) | (u & 3) | 12)) = pm[0] >> 16;
      }
      __builtin_amdgcn_s_waitcnt(0x0F70);
    }
    PT(1);
    xprep();
    xdma();
  } else {
    xprep();
    unsigned inc = RINC, t0 = (unsigned)tid << 18;
    asm volatile("s_waitcnt vmcnt(2)" : "+{v[240:243]}"(d[0]), "+{v[244:247]}"(d[1]), "+v"(inc), "+v"(t0) :: "memory");
    PT(1);
    unsigned cb[8];
#pragma unroll
    for (int s = 0; s < 2; ++s) {
      unsigned* rr = val + 4 * s;
#pragma unroll
      for (int k = 0; k < 4; ++k) {
        unsigned t;
        asm("v_sub_u32 %0, %1, %2 clamp" : "=v"(t) : "v"(d[s][k]), "s"(N1));
        asm("v_mad_u32_u24 %0, %1, %2, %3" : "=v"(pm[4 * s + k]) : "v"(t), "s"(DM), "v"(mul24s(d[s][k], M)));
      }
#pragma unroll
      for (int k = 0; k < 4; ++k) rr[k] = byte3x(pm[4 * s + k], 4u);
      asm volatile(
          "s_mov_b64 exec, %5\n\t"
          "ds_add_rtn_u32 %0, %0, %4 offset:%6\n\t"
          "ds_add_rtn_u32 %1, %1, %4 offset:%6\n\t"
          "ds_add_rtn_u32 %2, %2, %4 offset:%6\n\t"
          "ds_add_rtn_u32 %3, %3, %4 offset:%6\n\t"
          "s_mov_b64 exec, -1"
          : "+v"(rr[0]), "+v"(rr[1]), "+v"(rr[2]), "+v"(rr[3])
          : "v"(inc), "s"(on[s]), "n"(OFF_R0 + KP_HDR)
          : "memory");
    }
#pragma unroll
    for (int i = 0; i < 8; ++i) cb[i] = byte3x(pm[i], (unsigned)CPIT);
    asm volatile("s_waitcnt vmcnt(0)"
                 : "+{v[248:251]}"(sv[0]), "+{v[252:255]}"(sv[1]), "+v"(cb[0]), "+v"(cb[1]), "+v"(cb[2]), "+v"(cb[3]), "+v"(cb[4]), "+v"(cb[5]),
                   "+v"(cb[6]), "+v"(cb[7])
                 :: "memory");
    xdma();
#pragma unroll
    for (int i = 0; i < 8; ++i) tup[i] = sv[i >> 2][i & 3] + t0 + (((i >> 2) * 1792u + (i & 3)) << 16);
#pragma unroll
    for (int s = 0; s < 2; ++s) {
      asm volatile("s_waitcnt lgkmcnt(%8)"
                   : "+v"(val[4 * s]), "+v"(val[4 * s + 1]), "+v"(val[4 * s + 2]), "+v"(val[4 * s + 3]), "+v"(tup[4 * s]),
                     "+v"(tup[4 * s + 1]), "+v"(tup[4 * s + 2]), "+v"(tup[4 * s + 3])
                   : "n"(DBGWAIT ? 0 : s ? 8 : 4) : "memory");
      unsigned pa[4], pb[4];
#pragma unroll
      for (int k = 0; k < 4; ++k) {
        const unsigned u = min(val[4 * s + k], 48u << 16);
        asm("v_mad_u32_u16 %0, %1, 4, %2 op_sel:[1,0,0,0]" : "=v"(pa[k]) : "v"(u), "v"(cb[4 * s + k]));
        pb[k] = pa[k] | ((u >> 16) & 3u) | 12u;
      }
      asm volatile(
          "s_mov_b64 exec, %16\n\t"
          "ds_write_b32 %0, %8 offset:%17\n\t"
          "ds_write_b8_d16_hi %4, %12 offset:%17\n\t"
          "ds_write_b32 %1, %9 offset:%17\n\t"
          "ds_write_b8_d16_hi %5, %13 offset:%17\n\t"
          "ds_write_b32 %2, %10 offset:%17\n\t"
          "ds_write_b8_d16_hi %6, %14 offset:%17\n\t"
          "ds_write_b32 %3, %11 offset:%17\n\t"
          "ds_write_b8_d16_hi %7, %15 offset:%17\n\t"
          "s_mov_b64 exec, -1"
          :: "v"(pa[0]), "v"(pa[1]), "v"(pa[2]), "v"(pa[3]), "v"(pb[0]), "v"(pb[1]), "v"(pb[2]), "v"(pb[3]),
             "v"(tup[4 * s]), "v"(tup[4 * s + 1]), "v"(tup[4 * s + 2]), "v"(tup[4 * s + 3]), "v"(pm[4 * s]),
             "v"(pm[4 * s + 1]), "v"(pm[4 * s + 2]), "v"(pm[4 * s + 3]), "s"(on[s]), "n"(OFF_R0 + KP_ENT)
          : "memory");
    }
  }
  PT(2);
  u32x2 tag;
  int xw = 0;
  {
    unsigned ha = sm0 + KP_HDR + bq * 4, hb = sm0 + KP_HDR + lane * 16;
    unsigned go = bq * IBOX + bk * 128 + pc * 16;
    asm volatile("s_waitcnt lgkmcnt(0)\n\ts_barrier" : "+v"(ha), "+v"(hb), "+v"(go) :: "memory");
    PT(3);
    tag = *(const LAS u32x2*)((LAS char*)smem + OFF_CNT + 56);
    const u32x4 call = *(const LAS u32x4*)(size_t)hb;
    unsigned cn[4];
    u32x4 v[4];
#pragma unroll
    for (int s = 0; s < 4; ++s) {
      cn[s] = *(const LAS unsigned*)(size_t)(ha + s * 256);
      v[s] = *(const LAS u32x4*)(size_t)(ia + s * 64 * CPIT);
    }
    asm volatile("" : "+v"(v[0]), "+v"(v[1]), "+v"(v[2]), "+v"(v[3]), "+v"(tag));
    if (__builtin_expect(__ballot(max(max(call[0], call[1]), max(call[2], call[3])) > 36u * RINC) != 0ull, 0)) {
      LAS unsigned* ovc = (LAS unsigned*)((LAS char*)smem + OFF_CNT + 24);
      LAS unsigned* pos = (LAS unsigned*)(sm + KP_POS);
      if (tid < NBK) {
        const unsigned u = *(const LAS unsigned*)(sm + KP_HDR + tid * 4) >> 16, n = u - (u >> 2);
        if (n > 36u) {
          const unsigned p = (unsigned)base + __hip_atomic_fetch_add(ovc, n - 36u, __ATOMIC_RELAXED, __HIP_MEMORY_SCOPE_WORKGROUP);
          pos[tid] = p;
          __hip_atomic_store(FL + 512 + tid * NCH + bk, ((unsigned long long)(n - 36u) << 32) | p, __ATOMIC_RELAXED,
                             __HIP_MEMORY_SCOPE_AGENT);
        }
      }
      asm volatile("s_waitcnt lgkmcnt(0)\n\ts_barrier" ::: "memory");
#pragma unroll
      for (int i = 0; i < 8; ++i) {
        const unsigned u = val[i] >> 16, n = u - (u >> 2);
        if (((on[i >> 2] >> lane) & 1ull) && n >= 36u)
          __hip_atomic_store(TS + pos[pm[i] >> 24] + (n - 36u), ((unsigned long long)((pm[i] >> 16) & 0xffu) << 32) | tup[i],
                             __ATOMIC_RELAXED, __HIP_MEMORY_SCOPE_AGENT);
      }
      __builtin_amdgcn_s_waitcnt(0x0F70);
      asm volatile("s_waitcnt vmcnt(0)\n\ts_barrier" ::: "memory");
    }
    auto seal = [&](u32x4& w, bool more) {
      w[3] = __builtin_amdgcn_perm(tag[1], w[3], 0x04020100u);
      unsigned sm_ = w[0] + w[1] + w[2] + w[3] + tag[0];
      sm_ += __builtin_amdgcn_update_dpp(0u, sm_, 0xB1, 0xf, 0xf, false);
      sm_ += __builtin_amdgcn_update_dpp(0u, sm_, 0x4E, 0xf, 0xf, false);
      sm_ += __builtin_amdgcn_update_dpp(0u, sm_, 0x141, 0xf, 0xf, false);
      w[2] = pc == 7u ? w[2] - sm_ + (more ? 1u : 0u) : w[2];
    };
#pragma unroll
    for (int s = 0; s < 4; ++s) seal(v[s], cn[s] > 23u * RINC);
    asm volatile("global_store_dwordx4 %0, %1, %5 sc1\n\t"
                 "global_store_dwordx4 %0, %2, %6 sc1\n\t"
                 "global_store_dwordx4 %0, %3, %7 sc1\n\t"
                 "global_store_dwordx4 %0, %4, %8 sc1\n\t"
                 "s_nop 1"
                 :: "v"(go), "v"(v[0]), "v"(v[1]), "v"(v[2]), "v"(v[3]), "s"(ENT), "s"(ENT + 4 * IBOX), "s"(ENT + 8 * IBOX),
                    "s"(ENT + 12 * IBOX)
                 : "memory");
    PT(4);
    if (XLIKE(__ballot(max(max(cn[0], cn[1]), max(cn[2], cn[3])) > 23u * RINC) != 0ull)) {
      unsigned xm = 0u;
#pragma unroll
      for (int s = 0; s < 4; ++s) xm |= cn[s] > 23u * RINC ? 1u << s : 0u;
#pragma unroll 1
      do {
        const unsigned s = __builtin_ctz(xm | 8u);
        const unsigned c = *(const LAS unsigned*)(size_t)(ha + s * 256);
        u32x4 w = *(const LAS u32x4*)(size_t)(ia + s * (64 * CPIT) + (pc < 4u ? 128 : 0));
        const unsigned lim = pc < 4u ? (c >> 16) - 4u * pc : 0u;
        w[3] = pc == 7u ? w[3] >> 16 : w[3];
        w[0] = pc == 7u ? w[2] : 32u < lim ? w[0] : ~0u;
        w[1] = 33u < lim ? w[1] : ~0u;
        w[2] = 34u < lim ? w[2] : ~0u;
        seal(w, c > 36u * RINC);
        const unsigned ga = go + s * (64 * IBOX);
        if (xm) {
          w[3] += (((bk >> 3) & 3) + 1) << 24;
          gstore128wt(ENT + 2048 + ((bk >> 5) * 8 + (bk & 7) - bk) * 8, ga, w);
          w[3] -= (((bk >> 3) & 3) + 1) << 24;
          gstore128wt(ENT + IEXT / 16, ga, w);
        }
        xm &= xm - 1u;
        ++xw;
      } while (__ballot(xm != 0u) != 0ull);
    }
    PT(5);
    PV(0, xw);
  }
  u32x4 cv[4];
  unsigned vo[4], eb[4];
  unsigned long long pend[4];
#pragma unroll
  for (int k = 0; k < 4; ++k) {
    vo[k] = wave * 4096u + k * 1024u + lane * 16u;
    eb[k] = (unsigned)((wave * 32 + k * 8 + (lane >> 3)) * chunk) * 32u;
  }
#define POLL0()                                                                                                       \
  asm volatile("global_load_dwordx4 %0, %4, %8 sc1\n\t"                                                               \
               "global_load_dwordx4 %1, %5, %8 sc1\n\t"                                                               \
               "global_load_dwordx4 %2, %6, %8 sc1\n\t"                                                               \
               "global_load_dwordx4 %3, %7, %8 sc1"                                                                   \
               : "=&{v[240:243]}"(cv[0]), "=&{v[244:247]}"(cv[1]), "=&{v[248:251]}"(cv[2]), "=&{v[252:255]}"(cv[3])   \
               : "v"(vo[0]), "v"(vo[1]), "v"(vo[2]), "v"(vo[3]), "s"(ib)                                              \
               : "memory")
  const f32x16 zero16 = {0.f, 0.f, 0.f, 0.f, 0.f, 0.f, 0.f, 0.f, 0.f, 0.f, 0.f, 0.f, 0.f, 0.f, 0.f, 0.f};
  const unsigned to = lds0 + OFF_W + ((lane >> 2) & 3) * 128 + (lane & 1) * 64 + ((lane >> 4) & 1) * 16 + ((lane >> 1) & 1) * 8;
  auto xa = [&]() {
    bf16x8 wa[8];
#pragma unroll
    for (int k = 0; k < 8; ++k) wa[k] = rawfrag(to + (NBLOB + (k >> 1)) * 1024 + h * 4096 + (k & 1) * 32);
    asm volatile("" : "+v"(wa[0]), "+v"(wa[1]), "+v"(wa[2]), "+v"(wa[3]), "+v"(wa[4]), "+v"(wa[5]), "+v"(wa[6]), "+v"(wa[7]),
                      "+{v[160:163]}"(xg[0]), "+{v[164:167]}"(xg[1]), "+{v[168:171]}"(xg[2]), "+{v[172:175]}"(xg[3]));
    f32x16 XA[2] = {zero16, zero16};
#pragma unroll
    for (int k = 0; k < 4; ++k)
#pragma unroll
      for (int ct = 0; ct < 2; ++ct) XA[ct] = MFMA32(__builtin_bit_cast(bf16x8, xg[k]), wa[k * 2 + ct], XA[ct]);
#pragma unroll
    for (int q = 0; q < 4; ++q) {
      u32x4 xp;
#pragma unroll
      for (int j = 0; j < 4; ++j)
        xp[j] = pack_f16(XA[q & 1][8 * (q >> 1) + 2 * j] * NLOG2E - RA, XA[q & 1][8 * (q >> 1) + 2 * j + 1] * NLOG2E - RA);
      *(LAS u32x4*)((LAS char*)smem + OFF_R1 + wave * RING_SLOT + q * 1024 + lane * 16) = xp;
    }
  };
  {
    auto prep = [&](unsigned f, unsigned hs, float scale) {
      u32x4 w[2];
#pragma unroll
      for (int ct = 0; ct < 2; ++ct) w[ct] = __builtin_bit_cast(u32x4, rawfrag(to + f + h * hs + ct * 32));
      asm volatile("s_waitcnt lgkmcnt(0)" : "+v"(w[0]), "+v"(w[1]) :: "memory");
#pragma unroll
      for (int ct = 0; ct < 2; ++ct) {
#pragma unroll
        for (int j = 0; j < 4; ++j)
          w[ct][j] = pack_bf16(__uint_as_float(w[ct][j] << 16) * scale, __uint_as_float(w[ct][j] & 0xffff0000u) * scale);
        *(LAS u32x4*)((LAS char*)smem + OFF_W + f + ct * hs + lane * 16) = w[ct];
      }
    };
    {
      const unsigned xr = min(nodeof((bu * 8 + wave) * 32 + r) - xn0, 31u);
      unsigned xa_ = lds0 + OFF_L + wave * LIST_BYTES + xr * 128 + (((4 * h) ^ ((xr >> 1) & 7)) * 16);
      asm volatile("s_cmp_eq_u32 %1, 1\n\ts_cbranch_scc1 1f\n\ts_waitcnt vmcnt(4)\n1:\n\ts_waitcnt vmcnt(6)"
                   : "+v"(xa_) : "s"(xw) : "memory", "scc");
#pragma unroll
      for (int t = 0; t < 4; ++t) xg[t] = *(const LAS u32x4*)(size_t)(xa_ ^ (t * 16));
      asm volatile("" : "+v"(xg[0]), "+v"(xg[1]), "+v"(xg[2]), "+v"(xg[3]) :: "memory");
      *(LAS unsigned*)((LAS char*)smem + OFF_L + wave * LIST_BYTES + h * LARR + r * 4) = ~0u;
    }
    asm volatile("s_waitcnt lgkmcnt(0)\n\ts_barrier"
                 : "+v"(vo[0]), "+v"(vo[1]), "+v"(vo[2]), "+v"(vo[3]), "+v"(eb[0]), "+v"(eb[1]), "+v"(eb[2]), "+v"(eb[3])
                 :: "memory");
    xa();
    {
      unsigned f = (wave + (wave & 4)) * 1024, hs = 4096;
      const float scale = wave < 4 ? NLOG2E : VSC;
#pragma unroll 1
      while (true) {
        asm volatile("" : "+s"(f), "+s"(hs));
        prep(f, hs, scale);
        if (f != 0u) break;
        f = 16 * 1024;
        hs = 1024;
      }
    }
    PT(6);
    POLL0();
    glds16(wu_g + wave * 512, lane * 16, lds0 + OFF_W + (18 + wave) * 1024);
  }
  const unsigned l0 = lds0 + OFF_L;
  unsigned aLX = l0 + (lane >> 3) * 16;
  unsigned aLE = l0 + LARR + ((((lane >> 1) & 7) << 2) | (lane >> 4)) * 4;
  unsigned aLS4 = l0 + (4 * h + ((lane >> 2) & 3)) * 16;
  unsigned aLS1 = l0 + (((r & 7) << 2) | (r >> 3)) * 4 + 2;
  unsigned cx0 = (unsigned)(((lane & 7) ^ (lane >> 4)) * 16);
  unsigned cx1 = cx0 ^ 64u;
  unsigned ce = (unsigned)((((lane & 1) ^ ((lane >> 4) & 1))) * 16);
  const unsigned ringd = 8 * RING_SLOT;
  unsigned cS = lds0 + OFF_T1 + (((lane >> 4) & 1) * 16 + (lane & 3) * 4) * 2;
  unsigned cSt = lds0 + OFF_T2 + h * 32;
  unsigned fo[4];
#pragma unroll
  for (int t = 0; t < 4; ++t) fo[t] = lds0 + OFF_R0 + (unsigned)(r * 128 + (((4 * h + t) ^ ((r >> 1) & 7)) * 16));
  unsigned foe = lds0 + OFF_R0 + (unsigned)(4096 + r * 32 + ((h ^ ((r >> 3) & 1)) * 16));
  f32x16 Y[4];
#pragma unroll
  for (int q = 0; q < 4; ++q)
#pragma unroll
    for (int i = 0; i < 16; i += 2) {
      f32x2 y;
      if (q < 2) asm volatile("v_mov_b64 %0, %1" : "=v"(y) : "s"(0x358637bd358637bdull + (RA * 0x0080000000800000ull)));
      else asm volatile("v_mov_b64 %0, 0" : "=v"(y));
      Y[q][i] = y[0];
      Y[q][i + 1] = y[1];
    }
  constexpr unsigned cu = RA == 14 ? 0x04000400u : 0x02000200u;
  unsigned rid = lane & 7;
  asm volatile("" : "+v"(aLX), "+v"(aLE), "+v"(aLS4), "+v"(aLS1), "+v"(cx0), "+v"(cx1), "+v"(ce), "+v"(cS), "+v"(cSt),
                    "+v"(fo[0]), "+v"(fo[1]), "+v"(fo[2]), "+v"(fo[3]), "+v"(foe), "+v"(rid));
  asm volatile("" : "+{v[0:15]}"(Y[0]), "+{v[16:31]}"(Y[1]), "+{v[32:47]}"(Y[2]), "+{v[48:63]}"(Y[3]));
  auto ld128 = [&](unsigned a) { return *(const LAS u32x4*)(size_t)a; };
  auto gather5 = [&](const u32x4 sx, unsigned re, unsigned dstb) {
    glds16e(ES, re, ce, dstb + 4096);
    glds16x(x, sx[0], cx0, dstb);
    glds16x(x, sx[1], cx1, dstb + 1024);
    glds16x(x, sx[2], cx0, dstb + 2048);
    glds16x(x, sx[3], cx1, dstb + 3072);
  };
  const unsigned sq = min(4u * M, 0xffffffu);
  unsigned early, just;
  PT(7);
  asm volatile("s_waitcnt vmcnt(3)" ::: "memory");
  PT(8);
  asm volatile(
        "v_mov_b32 v181, %18\n\t"
        "v_mov_b32 v180, 160\n\t"
        "v_mov_b32 v176, 0xe0\n\t"
        "v_mov_b32 v177, 0x0c040100\n\t"
        "v_mov_b32 v178, 0x0c050100\n\t"
        "v_mov_b32 v179, 0x0c060100\n\t"
        "s_mov_b32 s52, 0x7f7f7f7f\n\t"
        "s_mov_b32 s53, 0x7f7f7f7f\n\t"
        "s_mov_b64 s[54:55], 0\n\t"
        "s_mov_b32 %12, 0\n\t"
        "v_mov_b32 v191, 4\n\t"
        "s_waitcnt vmcnt(3)\n\t"
        "v_add3_u32 v228, v240, v241, v242\n\t"
        "v_add3_u32 v231, v244, v245, v246\n\t"
        "v_add3_u32 v228, v228, v243, %19\n\t"
        "v_add3_u32 v231, v231, v247, %19\n\t"
        "v_cmp_ne_u32_sdwa s[72:73], v243, %20 src0_sel:BYTE_3 src1_sel:DWORD\n\t"
        "v_add_u32_dpp v228, v228, v228 quad_perm:[1,0,3,2] row_mask:0xf bank_mask:0xf\n\t"
        "v_add_u32_dpp v231, v231, v231 quad_perm:[1,0,3,2] row_mask:0xf bank_mask:0xf\n\t"
        "v_cmp_ne_u32_sdwa s[78:79], v247, %20 src0_sel:BYTE_3 src1_sel:DWORD\n\t"
        "v_add_u32_dpp v228, v228, v228 quad_perm:[2,3,0,1] row_mask:0xf bank_mask:0xf\n\t"
        "v_add_u32_dpp v231, v231, v231 quad_perm:[2,3,0,1] row_mask:0xf bank_mask:0xf\n\t"
        "s_nop 0\n\t"
        "v_add_u32_dpp v228, v228, v228 row_half_mirror row_mask:0xf bank_mask:0xf\n\t"
        "v_add_u32_dpp v231, v231, v231 row_half_mirror row_mask:0xf bank_mask:0xf\n\t"
        "v_cmp_gt_u32 s[64:65], 2, v228\n\t"
        "v_cmp_eq_u32 s[56:57], 1, v228\n\t"
        "v_cmp_gt_u32 s[66:67], 2, v231\n\t"
        "v_cmp_eq_u32 s[58:59], 1, v231\n\t"
        "s_and_b64 s[72:73], s[72:73], s[64:65]\n\t"
        "s_cselect_b64 s[64:65], 0, s[64:65]\n\t"
        "s_and_b64 s[56:57], s[56:57], s[64:65]\n\t"
        "s_not_b64 %0, s[64:65]\n\t"
        "s_or_b64 s[54:55], s[54:55], %0\n\t"
        "s_and_b64 s[78:79], s[78:79], s[66:67]\n\t"
        "s_cselect_b64 s[66:67], 0, s[66:67]\n\t"
        "s_and_b64 s[58:59], s[58:59], s[66:67]\n\t"
        "s_not_b64 %1, s[66:67]\n\t"
        "s_or_b64 s[54:55], s[54:55], %1\n\t"
        "s_mov_b64 exec, s[64:65]\n\t"
        "v_cmpx_ne_u32 s[72:73], -1, v240\n\t"
        "v_mul_hi_u32_u24_sdwa v204, v240, %21 dst_sel:DWORD dst_unused:UNUSED_PAD src0_sel:WORD_0 src1_sel:DWORD\n\t"
        "v_and_b32_sdwa v192, v243, v176 dst_sel:DWORD dst_unused:UNUSED_PAD src0_sel:BYTE_0 src1_sel:DWORD\n\t"
        "v_lshl_add_u32 v204, v204, 2, v192\n\t"
        "ds_add_rtn_u32 v204, v204, v191\n\t"
        "v_perm_b32 v216, v243, v240, v177\n\t"
        "v_mad_u32_u16 v228, v240, 32, %13 op_sel:[1,0,0,0]\n\t"
        "v_cmpx_ne_u32 s[74:75], -1, v241\n\t"
        "v_mul_hi_u32_u24_sdwa v205, v241, %21 dst_sel:DWORD dst_unused:UNUSED_PAD src0_sel:WORD_0 src1_sel:DWORD\n\t"
        "v_and_b32_sdwa v193, v243, v176 dst_sel:DWORD dst_unused:UNUSED_PAD src0_sel:BYTE_1 src1_sel:DWORD\n\t"
        "v_lshl_add_u32 v205, v205, 2, v193\n\t"
        "ds_add_rtn_u32 v205, v205, v191\n\t"
        "v_perm_b32 v217, v243, v241, v178\n\t"
        "v_mad_u32_u16 v229, v241, 32, %13 op_sel:[1,0,0,0]\n\t"
        "s_and_b64 exec, exec, s[52:53]\n\t"
        "v_cmpx_ne_u32 s[76:77], -1, v242\n\t"
        "v_mul_hi_u32_u24_sdwa v206, v242, %21 dst_sel:DWORD dst_unused:UNUSED_PAD src0_sel:WORD_0 src1_sel:DWORD\n\t"
        "v_and_b32_sdwa v194, v243, v176 dst_sel:DWORD dst_unused:UNUSED_PAD src0_sel:BYTE_2 src1_sel:DWORD\n\t"
        "v_lshl_add_u32 v206, v206, 2, v194\n\t"
        "ds_add_rtn_u32 v206, v206, v191\n\t"
        "v_perm_b32 v218, v243, v242, v179\n\t"
        "v_mad_u32_u16 v230, v242, 32, %13 op_sel:[1,0,0,0]\n\t"
        "s_mov_b64 exec, s[66:67]\n\t"
        "v_cmpx_ne_u32 s[78:79], -1, v244\n\t"
        "v_mul_hi_u32_u24_sdwa v207, v244, %21 dst_sel:DWORD dst_unused:UNUSED_PAD src0_sel:WORD_0 src1_sel:DWORD\n\t"
        "v_and_b32_sdwa v195, v247, v176 dst_sel:DWORD dst_unused:UNUSED_PAD src0_sel:BYTE_0 src1_sel:DWORD\n\t"
        "v_lshl_add_u32 v207, v207, 2, v195\n\t"
        "ds_add_rtn_u32 v207, v207, v191\n\t"
        "v_perm_b32 v219, v247, v244, v177\n\t"
        "v_mad_u32_u16 v231, v244, 32, %14 op_sel:[1,0,0,0]\n\t"
        "v_cmpx_ne_u32 s[80:81], -1, v245\n\t"
        "v_mul_hi_u32_u24_sdwa v208, v245, %21 dst_sel:DWORD dst_unused:UNUSED_PAD src0_sel:WORD_0 src1_sel:DWORD\n\t"
        "v_and_b32_sdwa v196, v247, v176 dst_sel:DWORD dst_unused:UNUSED_PAD src0_sel:BYTE_1 src1_sel:DWORD\n\t"
        "v_lshl_add_u32 v208, v208, 2, v196\n\t"
        "ds_add_rtn_u32 v208, v208, v191\n\t"
        "v_perm_b32 v220, v247, v245, v178\n\t"
        "v_mad_u32_u16 v232, v245, 32, %14 op_sel:[1,0,0,0]\n\t"
        "s_and_b64 exec, exec, s[52:53]\n\t"
        "v_cmpx_ne_u32 s[82:83], -1, v246\n\t"
        "v_mul_hi_u32_u24_sdwa v209, v246, %21 dst_sel:DWORD dst_unused:UNUSED_PAD src0_sel:WORD_0 src1_sel:DWORD\n\t"
        "v_and_b32_sdwa v197, v247, v176 dst_sel:DWORD dst_unused:UNUSED_PAD src0_sel:BYTE_2 src1_sel:DWORD\n\t"
        "v_lshl_add_u32 v209, v209, 2, v197\n\t"
        "ds_add_rtn_u32 v209, v209, v191\n\t"
        "v_perm_b32 v221, v247, v246, v179\n\t"
        "v_mad_u32_u16 v233, v246, 32, %14 op_sel:[1,0,0,0]\n\t"
        "s_mov_b64 exec, -1\n\t"
        "s_waitcnt vmcnt(1)\n\t"
        "v_add3_u32 v234, v248, v249, v250\n\t"
        "v_add3_u32 v237, v252, v253, v254\n\t"
        "v_add3_u32 v234, v234, v251, %19\n\t"
        "v_add3_u32 v237, v237, v255, %19\n\t"
        "v_cmp_ne_u32_sdwa s[84:85], v251, %20 src0_sel:BYTE_3 src1_sel:DWORD\n\t"
        "v_add_u32_dpp v234, v234, v234 quad_perm:[1,0,3,2] row_mask:0xf bank_mask:0xf\n\t"
        "v_add_u32_dpp v237, v237, v237 quad_perm:[1,0,3,2] row_mask:0xf bank_mask:0xf\n\t"
        "v_cmp_ne_u32_sdwa s[90:91], v255, %20 src0_sel:BYTE_3 src1_sel:DWORD\n\t"
        "v_add_u32_dpp v234, v234, v234 quad_perm:[2,3,0,1] row_mask:0xf bank_mask:0xf\n\t"
        "v_add_u32_dpp v237, v237, v237 quad_perm:[2,3,0,1] row_mask:0xf bank_mask:0xf\n\t"
        "s_nop 0\n\t"
        "v_add_u32_dpp v234, v234, v234 row_half_mirror row_mask:0xf bank_mask:0xf\n\t"
        "v_add_u32_dpp v237, v237, v237 row_half_mirror row_mask:0xf bank_mask:0xf\n\t"
        "v_cmp_gt_u32 s[68:69], 2, v234\n\t"
        "v_cmp_eq_u32 s[60:61], 1, v234\n\t"
        "v_cmp_gt_u32 s[70:71], 2, v237\n\t"
        "v_cmp_eq_u32 s[62:63], 1, v237\n\t"
        "s_and_b64 s[84:85], s[84:85], s[68:69]\n\t"
        "s_cselect_b64 s[68:69], 0, s[68:69]\n\t"
        "s_and_b64 s[60:61], s[60:61], s[68:69]\n\t"
        "s_not_b64 %2, s[68:69]\n\t"
        "s_or_b64 s[54:55], s[54:55], %2\n\t"
        "s_and_b64 s[90:91], s[90:91], s[70:71]\n\t"
        "s_cselect_b64 s[70:71], 0, s[70:71]\n\t"
        "s_and_b64 s[62:63], s[62:63], s[70:71]\n\t"
        "s_not_b64 %3, s[70:71]\n\t"
        "s_or_b64 s[54:55], s[54:55], %3\n\t"
        "s_or_b64 s[50:51], s[56:57], s[58:59]\n\t"
        "s_or_b64 exec, s[60:61], s[62:63]\n\t"
        "s_or_b64 s[50:51], s[50:51], exec\n\t"
        "s_cbranch_scc0 57f\n\t"
        "s_mov_b64 exec, s[50:51]\n\t"
        "global_load_dwordx4 v[172:175], %30, %17 sc1\n\t"
        "57:\n\t"
        "s_mov_b64 exec, s[72:73]\n\t"
        "s_waitcnt lgkmcnt(3)\n\t"
        "v_min_u32 v204, v181, v204\n\t"
        "v_min_u32 v205, v181, v205\n\t"
        "v_min_u32 v206, v181, v206\n\t"
        "v_mad_u32_u24 v204, v192, v180, v204\n\t"
        "v_mad_u32_u24 v205, v193, v180, v205\n\t"
        "v_mad_u32_u24 v206, v194, v180, v206\n\t"
        "ds_write2st64_b32 v204, v216, v228 offset1:10\n\t"
        "s_mov_b64 exec, s[74:75]\n\t"
        "ds_write2st64_b32 v205, v217, v229 offset1:10\n\t"
        "s_mov_b64 exec, s[76:77]\n\t"
        "ds_write2st64_b32 v206, v218, v230 offset1:10\n\t"
        "s_mov_b64 exec, s[68:69]\n\t"
        "v_cmpx_ne_u32 s[84:85], -1, v248\n\t"
        "v_mul_hi_u32_u24_sdwa v210, v248, %21 dst_sel:DWORD dst_unused:UNUSED_PAD src0_sel:WORD_0 src1_sel:DWORD\n\t"
        "v_and_b32_sdwa v198, v251, v176 dst_sel:DWORD dst_unused:UNUSED_PAD src0_sel:BYTE_0 src1_sel:DWORD\n\t"
        "v_lshl_add_u32 v210, v210, 2, v198\n\t"
        "ds_add_rtn_u32 v210, v210, v191\n\t"
        "v_perm_b32 v222, v251, v248, v177\n\t"
        "v_mad_u32_u16 v234, v248, 32, %15 op_sel:[1,0,0,0]\n\t"
        "v_cmpx_ne_u32 s[86:87], -1, v249\n\t"
        "v_mul_hi_u32_u24_sdwa v211, v249, %21 dst_sel:DWORD dst_unused:UNUSED_PAD src0_sel:WORD_0 src1_sel:DWORD\n\t"
        "v_and_b32_sdwa v199, v251, v176 dst_sel:DWORD dst_unused:UNUSED_PAD src0_sel:BYTE_1 src1_sel:DWORD\n\t"
        "v_lshl_add_u32 v211, v211, 2, v199\n\t"
        "ds_add_rtn_u32 v211, v211, v191\n\t"
        "v_perm_b32 v223, v251, v249, v178\n\t"
        "v_mad_u32_u16 v235, v249, 32, %15 op_sel:[1,0,0,0]\n\t"
        "s_and_b64 exec, exec, s[52:53]\n\t"
        "v_cmpx_ne_u32 s[88:89], -1, v250\n\t"
        "v_mul_hi_u32_u24_sdwa v212, v250, %21 dst_sel:DWORD dst_unused:UNUSED_PAD src0_sel:WORD_0 src1_sel:DWORD\n\t"
        "v_and_b32_sdwa v200, v251, v176 dst_sel:DWORD dst_unused:UNUSED_PAD src0_sel:BYTE_2 src1_sel:DWORD\n\t"
        "v_lshl_add_u32 v212, v212, 2, v200\n\t"
        "ds_add_rtn_u32 v212, v212, v191\n\t"
        "v_perm_b32 v224, v251, v250, v179\n\t"
        "v_mad_u32_u16 v236, v250, 32, %15 op_sel:[1,0,0,0]\n\t"
        "s_mov_b64 exec, s[78:79]\n\t"
        "s_waitcnt lgkmcnt(6)\n\t"
        "v_min_u32 v207, v181, v207\n\t"
        "v_min_u32 v208, v181, v208\n\t"
        "v_min_u32 v209, v181, v209\n\t"
        "v_mad_u32_u24 v207, v195, v180, v207\n\t"
        "v_mad_u32_u24 v208, v196, v180, v208\n\t"
        "v_mad_u32_u24 v209, v197, v180, v209\n\t"
        "ds_write2st64_b32 v207, v219, v231 offset1:10\n\t"
        "s_mov_b64 exec, s[80:81]\n\t"
        "ds_write2st64_b32 v208, v220, v232 offset1:10\n\t"
        "s_mov_b64 exec, s[82:83]\n\t"
        "ds_write2st64_b32 v209, v221, v233 offset1:10\n\t"
        "s_mov_b64 exec, -1\n\t"
        "ds_read_b128 v[184:187], %22\n\t"
        "ds_read_b32 v183, %23\n\t"
        "s_mov_b64 exec, s[70:71]\n\t"
        "v_cmpx_ne_u32 s[90:91], -1, v252\n\t"
        "v_mul_hi_u32_u24_sdwa v213, v252, %21 dst_sel:DWORD dst_unused:UNUSED_PAD src0_sel:WORD_0 src1_sel:DWORD\n\t"
        "v_and_b32_sdwa v201, v255, v176 dst_sel:DWORD dst_unused:UNUSED_PAD src0_sel:BYTE_0 src1_sel:DWORD\n\t"
        "v_lshl_add_u32 v213, v213, 2, v201\n\t"
        "ds_add_rtn_u32 v213, v213, v191\n\t"
        "v_perm_b32 v225, v255, v252, v177\n\t"
        "v_mad_u32_u16 v237, v252, 32, %16 op_sel:[1,0,0,0]\n\t"
        "v_cmpx_ne_u32 s[92:93], -1, v253\n\t"
        "v_mul_hi_u32_u24_sdwa v214, v253, %21 dst_sel:DWORD dst_unused:UNUSED_PAD src0_sel:WORD_0 src1_sel:DWORD\n\t"
        "v_and_b32_sdwa v202, v255, v176 dst_sel:DWORD dst_unused:UNUSED_PAD src0_sel:BYTE_1 src1_sel:DWORD\n\t"
        "v_lshl_add_u32 v214, v214, 2, v202\n\t"
        "ds_add_rtn_u32 v214, v214, v191\n\t"
        "v_perm_b32 v226, v255, v253, v178\n\t"
        "v_mad_u32_u16 v238, v253, 32, %16 op_sel:[1,0,0,0]\n\t"
        "s_and_b64 exec, exec, s[52:53]\n\t"
        "v_cmpx_ne_u32 s[94:95], -1, v254\n\t"
        "v_mul_hi_u32_u24_sdwa v215, v254, %21 dst_sel:DWORD dst_unused:UNUSED_PAD src0_sel:WORD_0 src1_sel:DWORD\n\t"
        "v_and_b32_sdwa v203, v255, v176 dst_sel:DWORD dst_unused:UNUSED_PAD src0_sel:BYTE_2 src1_sel:DWORD\n\t"
        "v_lshl_add_u32 v215, v215, 2, v203\n\t"
        "ds_add_rtn_u32 v215, v215, v191\n\t"
        "v_perm_b32 v227, v255, v254, v179\n\t"
        "v_mad_u32_u16 v239, v254, 32, %16 op_sel:[1,0,0,0]\n\t"
        "s_cmp_eq_u64 s[54:55], 0\n\t"
        "s_cbranch_scc1 59f\n\t"
        "s_mov_b64 exec, %0\n\t"
        "s_cbranch_execz 50f\n\t"
        "global_load_dwordx4 v[240:243], %4, %17 sc1\n\t"
        "50:\n\t"
        "s_mov_b64 exec, %1\n\t"
        "s_cbranch_execz 51f\n\t"
        "global_load_dwordx4 v[244:247], %5, %17 sc1\n\t"
        "51:\n\t"
        "s_mov_b64 exec, %2\n\t"
        "s_cbranch_execz 52f\n\t"
        "global_load_dwordx4 v[248:251], %6, %17 sc1\n\t"
        "52:\n\t"
        "s_mov_b64 exec, %3\n\t"
        "s_cbranch_execz 53f\n\t"
        "global_load_dwordx4 v[252:255], %7, %17 sc1\n\t"
        "53:\n\t"
        "59:\n\t"
        "s_mov_b64 exec, s[84:85]\n\t"
        "s_waitcnt lgkmcnt(8)\n\t"
        "v_min_u32 v210, v181, v210\n\t"
        "v_min_u32 v211, v181, v211\n\t"
        "v_min_u32 v212, v181, v212\n\t"
        "v_mad_u32_u24 v210, v198, v180, v210\n\t"
        "v_mad_u32_u24 v211, v199, v180, v211\n\t"
        "v_mad_u32_u24 v212, v200, v180, v212\n\t"
        "ds_write2st64_b32 v210, v222, v234 offset1:10\n\t"
        "s_mov_b64 exec, s[86:87]\n\t"
        "ds_write2st64_b32 v211, v223, v235 offset1:10\n\t"
        "s_mov_b64 exec, s[88:89]\n\t"
        "ds_write2st64_b32 v212, v224, v236 offset1:10\n\t"
        "s_mov_b64 exec, -1\n\t"
        "s_movk_i32 s96, 128\n\t"
        "s_mov_b32 m0, %29\n\t"
        "s_waitcnt lgkmcnt(6)\n\t"
        "v_max3_u32 v182, v184, v185, v186\n\t"
        "v_max3_u32 v182, v182, v187, v183\n\t"
        "v_cmp_eq_u32 vcc, -1, v182\n\t"
        "v_mad_u32_u16 v184, v184, s96, %24\n\t"
        "v_mad_u32_u16 v185, v185, s96, %25\n\t"
        "v_mad_u32_u16 v186, v186, s96, %24\n\t"
        "v_mad_u32_u16 v187, v187, s96, %25\n\t"
        "v_add_u32 v183, v183, %26\n\t"
        "s_cbranch_vccnz 60f\n\t"
        "global_load_lds_dwordx4 v184, %27\n\t"
        "s_addk_i32 m0, 1024\n\t"
        "s_nop 0\n\t"
        "global_load_lds_dwordx4 v185, %27\n\t"
        "s_addk_i32 m0, 1024\n\t"
        "s_nop 0\n\t"
        "global_load_lds_dwordx4 v186, %27\n\t"
        "s_addk_i32 m0, 1024\n\t"
        "s_nop 0\n\t"
        "global_load_lds_dwordx4 v187, %27\n\t"
        "s_addk_i32 m0, 1024\n\t"
        "s_nop 0\n\t"
        "global_load_lds_dwordx4 v183, %28\n\t"
        "s_mov_b32 %12, 1\n\t"
        "60:\n\t"
        "s_mov_b64 exec, s[90:91]\n\t"
        "s_waitcnt lgkmcnt(3)\n\t"
        "v_min_u32 v213, v181, v213\n\t"
        "v_min_u32 v214, v181, v214\n\t"
        "v_min_u32 v215, v181, v215\n\t"
        "v_mad_u32_u24 v213, v201, v180, v213\n\t"
        "v_mad_u32_u24 v214, v202, v180, v214\n\t"
        "v_mad_u32_u24 v215, v203, v180, v215\n\t"
        "ds_write2st64_b32 v213, v225, v237 offset1:10\n\t"
        "s_mov_b64 exec, s[92:93]\n\t"
        "ds_write2st64_b32 v214, v226, v238 offset1:10\n\t"
        "s_mov_b64 exec, s[94:95]\n\t"
        "ds_write2st64_b32 v215, v227, v239 offset1:10\n\t"
        "s_cmp_eq_u64 s[50:51], 0\n\t"
        "s_cbranch_scc1 49f\n\t"
        "s_mov_b64 exec, s[56:57]\n\t"
        "v_mov_b32 v188, 0xff000000\n\t"
        "v_mov_b32 v189, %13\n\t"
        "v_bfe_u32 v190, %4, 7, 8\n\t"
        "ds_write_b8 v190, v191 offset:256\n\t"
        "s_mov_b64 exec, s[58:59]\n\t"
        "v_mov_b32 v188, 0xfe000000\n\t"
        "v_mov_b32 v189, %14\n\t"
        "v_bfe_u32 v190, %5, 7, 8\n\t"
        "ds_write_b8 v190, v191 offset:256\n\t"
        "s_mov_b64 exec, s[60:61]\n\t"
        "v_mov_b32 v188, 0xfd000000\n\t"
        "v_mov_b32 v189, %15\n\t"
        "v_bfe_u32 v190, %6, 7, 8\n\t"
        "ds_write_b8 v190, v191 offset:256\n\t"
        "s_mov_b64 exec, s[62:63]\n\t"
        "v_mov_b32 v188, 0xfc000000\n\t"
        "v_mov_b32 v189, %16\n\t"
        "v_bfe_u32 v190, %7, 7, 8\n\t"
        "ds_write_b8 v190, v191 offset:256\n\t"
        "s_mov_b64 exec, s[50:51]\n\t"
        "s_waitcnt vmcnt(5)\n\t"
        "s_cmp_lg_u32 %12, 0\n\t"
        "s_cbranch_scc1 47f\n\t"
        "s_waitcnt vmcnt(0)\n\t"
        "47:\n\t"
        "v_add_u32 v175, v175, v188\n\t"
        "v_add3_u32 v228, v172, v173, v174\n\t"
        "v_add3_u32 v228, v228, v175, %19\n\t"
        "v_cmp_ne_u32_sdwa s[72:73], v175, %20 src0_sel:BYTE_3 src1_sel:DWORD\n\t"
        "s_nop 0\n\t"
        "v_add_u32_dpp v228, v228, v228 quad_perm:[1,0,3,2] row_mask:0xf bank_mask:0xf\n\t"
        "s_nop 1\n\t"
        "v_add_u32_dpp v228, v228, v228 quad_perm:[2,3,0,1] row_mask:0xf bank_mask:0xf\n\t"
        "s_nop 1\n\t"
        "v_add_u32_dpp v228, v228, v228 row_half_mirror row_mask:0xf bank_mask:0xf\n\t"
        "v_cmp_eq_u32 s[68:69], 0, v228\n\t"
        "s_and_b64 s[72:73], s[72:73], s[68:69]\n\t"
        "s_cselect_b64 exec, 0, s[68:69]\n\t"
        "s_cbranch_execz 48f\n\t"
        "v_cmpx_ne_u32 s[72:73], -1, v172\n\t"
        "v_mul_hi_u32_u24_sdwa v204, v172, %21 dst_sel:DWORD dst_unused:UNUSED_PAD src0_sel:WORD_0 src1_sel:DWORD\n\t"
        "v_and_b32_sdwa v192, v175, v176 dst_sel:DWORD dst_unused:UNUSED_PAD src0_sel:BYTE_0 src1_sel:DWORD\n\t"
        "v_lshl_add_u32 v204, v204, 2, v192\n\t"
        "ds_add_rtn_u32 v204, v204, v191\n\t"
        "v_perm_b32 v216, v175, v172, v177\n\t"
        "v_mad_u32_u16 v228, v172, 32, v189 op_sel:[1,0,0,0]\n\t"
        "v_cmpx_ne_u32 s[74:75], -1, v173\n\t"
        "v_mul_hi_u32_u24_sdwa v205, v173, %21 dst_sel:DWORD dst_unused:UNUSED_PAD src0_sel:WORD_0 src1_sel:DWORD\n\t"
        "v_and_b32_sdwa v193, v175, v176 dst_sel:DWORD dst_unused:UNUSED_PAD src0_sel:BYTE_1 src1_sel:DWORD\n\t"
        "v_lshl_add_u32 v205, v205, 2, v193\n\t"
        "ds_add_rtn_u32 v205, v205, v191\n\t"
        "v_perm_b32 v217, v175, v173, v178\n\t"
        "v_mad_u32_u16 v229, v173, 32, v189 op_sel:[1,0,0,0]\n\t"
        "s_and_b64 exec, exec, s[52:53]\n\t"
        "v_cmpx_ne_u32 s[76:77], -1, v174\n\t"
        "v_mul_hi_u32_u24_sdwa v206, v174, %21 dst_sel:DWORD dst_unused:UNUSED_PAD src0_sel:WORD_0 src1_sel:DWORD\n\t"
        "v_and_b32_sdwa v194, v175, v176 dst_sel:DWORD dst_unused:UNUSED_PAD src0_sel:BYTE_2 src1_sel:DWORD\n\t"
        "v_lshl_add_u32 v206, v206, 2, v194\n\t"
        "ds_add_rtn_u32 v206, v206, v191\n\t"
        "v_perm_b32 v218, v175, v174, v179\n\t"
        "v_mad_u32_u16 v230, v174, 32, v189 op_sel:[1,0,0,0]\n\t"
        "s_mov_b64 s[50:51], 0\n\t"
        "s_and_b64 s[70:71], s[68:69], s[62:63]\n\t"
        "s_xor_b64 s[62:63], s[62:63], s[70:71]\n\t"
        "s_xor_b64 s[68:69], s[68:69], s[70:71]\n\t"
        "s_or_b64 s[50:51], s[50:51], s[62:63]\n\t"
        "s_and_b64 s[70:71], s[68:69], s[60:61]\n\t"
        "s_xor_b64 s[60:61], s[60:61], s[70:71]\n\t"
        "s_xor_b64 s[68:69], s[68:69], s[70:71]\n\t"
        "s_or_b64 s[50:51], s[50:51], s[60:61]\n\t"
        "s_and_b64 s[70:71], s[68:69], s[58:59]\n\t"
        "s_xor_b64 s[58:59], s[58:59], s[70:71]\n\t"
        "s_xor_b64 s[68:69], s[68:69], s[70:71]\n\t"
        "s_or_b64 s[50:51], s[50:51], s[58:59]\n\t"
        "s_and_b64 s[70:71], s[68:69], s[56:57]\n\t"
        "s_xor_b64 s[56:57], s[56:57], s[70:71]\n\t"
        "s_or_b64 s[50:51], s[50:51], s[56:57]\n\t"
        "s_mov_b64 exec, s[72:73]\n\t"
        "s_waitcnt lgkmcnt(0)\n\t"
        "v_min_u32 v204, v181, v204\n\t"
        "v_min_u32 v205, v181, v205\n\t"
        "v_min_u32 v206, v181, v206\n\t"
        "v_mad_u32_u24 v204, v192, v180, v204\n\t"
        "v_mad_u32_u24 v205, v193, v180, v205\n\t"
        "v_mad_u32_u24 v206, v194, v180, v206\n\t"
        "ds_write2st64_b32 v204, v216, v228 offset1:10\n\t"
        "s_mov_b64 exec, s[74:75]\n\t"
        "ds_write2st64_b32 v205, v217, v229 offset1:10\n\t"
        "s_mov_b64 exec, s[76:77]\n\t"
        "ds_write2st64_b32 v206, v218, v230 offset1:10\n\t"
        "s_cbranch_scc0 49f\n\t"
        "48:\n\t"
        "s_mov_b32 s96, 0x1000000\n\t"
        "s_or_b32 %12, %12, 2\n\t"
        "s_mov_b64 exec, s[56:57]\n\t"
        "s_cbranch_execz 40f\n\t"
        "v_add_u32 %4, s96, %4\n\t"
        "s_or_b64 %0, %0, exec\n\t"
        "global_load_dwordx4 v[240:243], %4, %17 sc1\n\t"
        "40:\n\t"
        "s_mov_b64 exec, s[58:59]\n\t"
        "s_cbranch_execz 41f\n\t"
        "v_add_u32 %5, s96, %5\n\t"
        "s_or_b64 %1, %1, exec\n\t"
        "global_load_dwordx4 v[244:247], %5, %17 sc1\n\t"
        "41:\n\t"
        "s_mov_b64 exec, s[60:61]\n\t"
        "s_cbranch_execz 42f\n\t"
        "v_add_u32 %6, s96, %6\n\t"
        "s_or_b64 %2, %2, exec\n\t"
        "global_load_dwordx4 v[248:251], %6, %17 sc1\n\t"
        "42:\n\t"
        "s_mov_b64 exec, s[62:63]\n\t"
        "s_cbranch_execz 43f\n\t"
        "v_add_u32 %7, s96, %7\n\t"
        "s_or_b64 %3, %3, exec\n\t"
        "global_load_dwordx4 v[252:255], %7, %17 sc1\n\t"
        "43:\n\t"
        "49:\n\t"
        "s_or_b32 s54, s54, s55\n\t"
        "s_or_b32 s54, s54, %12\n\t"
        "s_cbranch_scc1 60f\n\t"
        "s_movk_i32 s54, 12\n\t"
        "61:\n\t"
        "s_sub_u32 s54, s54, 1\n\t"
        "s_cbranch_scc1 60f\n\t"
        "s_mov_b64 exec, -1\n\t"
        "ds_read_b128 v[184:187], %22\n\t"
        "ds_read_b32 v183, %23\n\t"
        "s_mov_b64 exec, -1\n\t"
        "s_movk_i32 s96, 128\n\t"
        "s_mov_b32 m0, %29\n\t"
        "s_waitcnt lgkmcnt(0)\n\t"
        "v_max3_u32 v182, v184, v185, v186\n\t"
        "v_max3_u32 v182, v182, v187, v183\n\t"
        "v_cmp_eq_u32 vcc, -1, v182\n\t"
        "v_mad_u32_u16 v184, v184, s96, %24\n\t"
        "v_mad_u32_u16 v185, v185, s96, %25\n\t"
        "v_mad_u32_u16 v186, v186, s96, %24\n\t"
        "v_mad_u32_u16 v187, v187, s96, %25\n\t"
        "v_add_u32 v183, v183, %26\n\t"
        "s_cbranch_vccnz 61b\n\t"
        "global_load_lds_dwordx4 v184, %27\n\t"
        "s_addk_i32 m0, 1024\n\t"
        "s_nop 0\n\t"
        "global_load_lds_dwordx4 v185, %27\n\t"
        "s_addk_i32 m0, 1024\n\t"
        "s_nop 0\n\t"
        "global_load_lds_dwordx4 v186, %27\n\t"
        "s_addk_i32 m0, 1024\n\t"
        "s_nop 0\n\t"
        "global_load_lds_dwordx4 v187, %27\n\t"
        "s_addk_i32 m0, 1024\n\t"
        "s_nop 0\n\t"
        "global_load_lds_dwordx4 v183, %28\n\t"
        "s_mov_b32 %12, 1\n\t"
        "60:\n\t"
        "s_mov_b64 exec, -1\n\t"
        : "=&s"(pend[0]), "=&s"(pend[1]), "=&s"(pend[2]), "=&s"(pend[3]), "+v"(vo[0]), "+v"(vo[1]), "+v"(vo[2]),
          "+v"(vo[3]), "+{v[240:243]}"(cv[0]), "+{v[244:247]}"(cv[1]), "+{v[248:251]}"(cv[2]), "+{v[252:255]}"(cv[3]),
          "=&s"(early)
        : "v"(eb[0]), "v"(eb[1]), "v"(eb[2]), "v"(eb[3]), "s"(ib), "n"(OFF_L + LARR - 4), "v"(tag[0]), "v"(tag[1]),
          "s"(sq), "v"(aLX + wave * LIST_BYTES), "v"(aLE + wave * LIST_BYTES), "v"(cx0), "v"(cx1), "v"(ce), "s"(x),
          "s"(ES), "s"(lds0 + OFF_R0 + wave * RING_SLOT), "v"(32768u + wave * 1024u + lane * 16u)
        :
          "memory", "vcc", "scc", "m0", "v172", "v173", "v174", "v175", "v176", "v177", "v178", "v179", "v180",
          "v181", "v182", "v183", "v184", "v185", "v186", "v187", "v188", "v189", "v190", "v191", "v192", "v193",
          "v194", "v195", "v196", "v197", "v198", "v199", "v200", "v201", "v202", "v203", "v204", "v205", "v206",
          "v207", "v208", "v209", "v210", "v211", "v212", "v213", "v214", "v215", "v216", "v217", "v218", "v219",
          "v220", "v221", "v222", "v223", "v224", "v225", "v226", "v227", "v228", "v229", "v230", "v231", "v232",
          "v233", "v234", "v235", "v236", "v237", "v238", "v239", "s50", "s51", "s52", "s53", "s54", "s55", "s56",
          "s57", "s58", "s59", "s60", "s61", "s62", "s63", "s64", "s65", "s66", "s67", "s68", "s69", "s70", "s71",
          "s72", "s73", "s74", "s75", "s76", "s77", "s78", "s79", "s80", "s81", "s82", "s83", "s84", "s85", "s86",
          "s87", "s88", "s89", "s90", "s91", "s92", "s93", "s94", "s95", "s96");
  auto uni = [&]() {
#pragma unroll
    for (int k = 0; k < 4; ++k)
      pend[k] = ((unsigned long long)(unsigned)__builtin_amdgcn_readfirstlane((unsigned)(pend[k] >> 32)) << 32) |
                (unsigned)__builtin_amdgcn_readfirstlane((unsigned)pend[k]);
  };
  uni();
  early = __builtin_amdgcn_readfirstlane(early);
  just = early;
  PT(9);
  PV(13, early);
  if (__builtin_expect((pend[0] | pend[1] | pend[2] | pend[3] | (unsigned long long)(early ^ 1u)) != 0ull, 0))
#pragma clang loop unroll(disable)
  while (true) {
    early &= 1u;
    if (!early) {
      const u32x4 sx = ld128(aLX + wave * LIST_BYTES);
      const unsigned re = *(const LAS unsigned*)(size_t)(aLE + wave * LIST_BYTES);
      if (__ballot(max(max(max(sx[0], sx[1]), max(sx[2], sx[3])), re) == ~0u) == 0ull) {
        gather5(sx, re, lds0 + OFF_R0 + wave * RING_SLOT);
        early = 1u;
        just = 1u;
      }
    }
    if ((pend[0] | pend[1] | pend[2] | pend[3]) == 0ull || --spin < 0) break;
    asm volatile(
        "s_mov_b32 s96, 0x1000000\n\t"
        "v_mov_b32 v181, %17\n\t"
        "v_mov_b32 v180, 160\n\t"
        "v_mov_b32 v176, 0xe0\n\t"
        "v_mov_b32 v177, 0x0c040100\n\t"
        "v_mov_b32 v178, 0x0c050100\n\t"
        "v_mov_b32 v179, 0x0c060100\n\t"
        "s_mov_b32 s52, 0x7f7f7f7f\n\t"
        "s_mov_b32 s53, 0x7f7f7f7f\n\t"
        "v_mov_b32 v191, 4\n\t"
        "s_waitcnt vmcnt(5)\n\t"
        "s_cmp_eq_u32 %21, 1\n\t"
        "s_cbranch_scc1 9f\n\t"
        "s_waitcnt vmcnt(0)\n\t"
        "9:\n\t"
        "v_add3_u32 v228, v240, v241, v242\n\t"
        "v_add3_u32 v229, v244, v245, v246\n\t"
        "v_add3_u32 v230, v248, v249, v250\n\t"
        "v_add3_u32 v231, v252, v253, v254\n\t"
        "v_add3_u32 v228, v228, v243, %18\n\t"
        "v_add3_u32 v229, v229, v247, %18\n\t"
        "v_add3_u32 v230, v230, v251, %18\n\t"
        "v_add3_u32 v231, v231, v255, %18\n\t"
        "v_add_u32_dpp v228, v228, v228 quad_perm:[1,0,3,2] row_mask:0xf bank_mask:0xf\n\t"
        "v_add_u32_dpp v229, v229, v229 quad_perm:[1,0,3,2] row_mask:0xf bank_mask:0xf\n\t"
        "v_add_u32_dpp v230, v230, v230 quad_perm:[1,0,3,2] row_mask:0xf bank_mask:0xf\n\t"
        "v_add_u32_dpp v231, v231, v231 quad_perm:[1,0,3,2] row_mask:0xf bank_mask:0xf\n\t"
        "v_add_u32_dpp v228, v228, v228 quad_perm:[2,3,0,1] row_mask:0xf bank_mask:0xf\n\t"
        "v_add_u32_dpp v229, v229, v229 quad_perm:[2,3,0,1] row_mask:0xf bank_mask:0xf\n\t"
        "v_add_u32_dpp v230, v230, v230 quad_perm:[2,3,0,1] row_mask:0xf bank_mask:0xf\n\t"
        "v_add_u32_dpp v231, v231, v231 quad_perm:[2,3,0,1] row_mask:0xf bank_mask:0xf\n\t"
        "v_add_u32_dpp v228, v228, v228 row_half_mirror row_mask:0xf bank_mask:0xf\n\t"
        "v_add_u32_dpp v229, v229, v229 row_half_mirror row_mask:0xf bank_mask:0xf\n\t"
        "v_add_u32_dpp v230, v230, v230 row_half_mirror row_mask:0xf bank_mask:0xf\n\t"
        "v_add_u32_dpp v231, v231, v231 row_half_mirror row_mask:0xf bank_mask:0xf\n\t"
        "v_cmp_ne_u32_sdwa s[72:73], v243, %19 src0_sel:BYTE_3 src1_sel:DWORD\n\t"
        "v_cmp_gt_u32 s[64:65], 2, v228\n\t"
        "v_cmp_eq_u32 s[56:57], 1, v228\n\t"
        "v_cmp_ne_u32_sdwa s[74:75], v247, %19 src0_sel:BYTE_3 src1_sel:DWORD\n\t"
        "v_cmp_gt_u32 s[66:67], 2, v229\n\t"
        "v_cmp_eq_u32 s[58:59], 1, v229\n\t"
        "v_cmp_ne_u32_sdwa s[76:77], v251, %19 src0_sel:BYTE_3 src1_sel:DWORD\n\t"
        "v_cmp_gt_u32 s[68:69], 2, v230\n\t"
        "v_cmp_eq_u32 s[60:61], 1, v230\n\t"
        "v_cmp_ne_u32_sdwa s[78:79], v255, %19 src0_sel:BYTE_3 src1_sel:DWORD\n\t"
        "v_cmp_gt_u32 s[70:71], 2, v231\n\t"
        "v_cmp_eq_u32 s[62:63], 1, v231\n\t"
        "s_and_b64 s[64:65], s[64:65], %0\n\t"
        "s_and_b64 s[72:73], s[72:73], s[64:65]\n\t"
        "s_cselect_b64 s[64:65], 0, s[64:65]\n\t"
        "s_and_b64 s[56:57], s[56:57], s[64:65]\n\t"
        "s_andn2_b64 %0, %0, s[64:65]\n\t"
        "s_cbranch_scc0 10f\n\t"
        "s_mov_b64 exec, %0\n\t"
        "global_load_dwordx4 v[240:243], %4, %16 sc1\n\t"
        "10:\n\t"
        "s_and_b64 s[66:67], s[66:67], %1\n\t"
        "s_and_b64 s[74:75], s[74:75], s[66:67]\n\t"
        "s_cselect_b64 s[66:67], 0, s[66:67]\n\t"
        "s_and_b64 s[58:59], s[58:59], s[66:67]\n\t"
        "s_andn2_b64 %1, %1, s[66:67]\n\t"
        "s_cbranch_scc0 11f\n\t"
        "s_mov_b64 exec, %1\n\t"
        "global_load_dwordx4 v[244:247], %5, %16 sc1\n\t"
        "11:\n\t"
        "s_and_b64 s[68:69], s[68:69], %2\n\t"
        "s_and_b64 s[76:77], s[76:77], s[68:69]\n\t"
        "s_cselect_b64 s[68:69], 0, s[68:69]\n\t"
        "s_and_b64 s[60:61], s[60:61], s[68:69]\n\t"
        "s_andn2_b64 %2, %2, s[68:69]\n\t"
        "s_cbranch_scc0 12f\n\t"
        "s_mov_b64 exec, %2\n\t"
        "global_load_dwordx4 v[248:251], %6, %16 sc1\n\t"
        "12:\n\t"
        "s_and_b64 s[70:71], s[70:71], %3\n\t"
        "s_and_b64 s[78:79], s[78:79], s[70:71]\n\t"
        "s_cselect_b64 s[70:71], 0, s[70:71]\n\t"
        "s_and_b64 s[62:63], s[62:63], s[70:71]\n\t"
        "s_andn2_b64 %3, %3, s[70:71]\n\t"
        "s_cbranch_scc0 13f\n\t"
        "s_mov_b64 exec, %3\n\t"
        "global_load_dwordx4 v[252:255], %7, %16 sc1\n\t"
        "13:\n\t"
        "s_mov_b64 s[54:55], 0\n\t"
        "s_mov_b64 s[72:73], 0\n\t"
        "s_mov_b64 exec, s[64:65]\n\t"
        "s_cbranch_execz 20f\n\t"
        "v_cmpx_ne_u32 s[72:73], -1, v240\n\t"
        "v_mul_hi_u32_u24_sdwa v204, v240, %20 dst_sel:DWORD dst_unused:UNUSED_PAD src0_sel:WORD_0 src1_sel:DWORD\n\t"
        "v_and_b32_sdwa v192, v243, v176 dst_sel:DWORD dst_unused:UNUSED_PAD src0_sel:BYTE_0 src1_sel:DWORD\n\t"
        "v_lshl_add_u32 v204, v204, 2, v192\n\t"
        "ds_add_rtn_u32 v204, v204, v191\n\t"
        "v_perm_b32 v216, v243, v240, v177\n\t"
        "v_mad_u32_u16 v228, v240, 32, %12 op_sel:[1,0,0,0]\n\t"
        "v_cmpx_ne_u32 s[74:75], -1, v241\n\t"
        "v_mul_hi_u32_u24_sdwa v205, v241, %20 dst_sel:DWORD dst_unused:UNUSED_PAD src0_sel:WORD_0 src1_sel:DWORD\n\t"
        "v_and_b32_sdwa v193, v243, v176 dst_sel:DWORD dst_unused:UNUSED_PAD src0_sel:BYTE_1 src1_sel:DWORD\n\t"
        "v_lshl_add_u32 v205, v205, 2, v193\n\t"
        "ds_add_rtn_u32 v205, v205, v191\n\t"
        "v_perm_b32 v217, v243, v241, v178\n\t"
        "v_mad_u32_u16 v229, v241, 32, %12 op_sel:[1,0,0,0]\n\t"
        "s_and_b64 exec, exec, s[52:53]\n\t"
        "v_cmpx_ne_u32 s[76:77], -1, v242\n\t"
        "v_mul_hi_u32_u24_sdwa v206, v242, %20 dst_sel:DWORD dst_unused:UNUSED_PAD src0_sel:WORD_0 src1_sel:DWORD\n\t"
        "v_and_b32_sdwa v194, v243, v176 dst_sel:DWORD dst_unused:UNUSED_PAD src0_sel:BYTE_2 src1_sel:DWORD\n\t"
        "v_lshl_add_u32 v206, v206, 2, v194\n\t"
        "ds_add_rtn_u32 v206, v206, v191\n\t"
        "v_perm_b32 v218, v243, v242, v179\n\t"
        "v_mad_u32_u16 v230, v242, 32, %12 op_sel:[1,0,0,0]\n\t"
        "s_or_b64 s[54:55], s[54:55], s[56:57]\n\t"
        "20:\n\t"
        "s_mov_b64 s[78:79], 0\n\t"
        "s_mov_b64 exec, s[66:67]\n\t"
        "s_cbranch_execz 21f\n\t"
        "v_cmpx_ne_u32 s[78:79], -1, v244\n\t"
        "v_mul_hi_u32_u24_sdwa v207, v244, %20 dst_sel:DWORD dst_unused:UNUSED_PAD src0_sel:WORD_0 src1_sel:DWORD\n\t"
        "v_and_b32_sdwa v195, v247, v176 dst_sel:DWORD dst_unused:UNUSED_PAD src0_sel:BYTE_0 src1_sel:DWORD\n\t"
        "v_lshl_add_u32 v207, v207, 2, v195\n\t"
        "ds_add_rtn_u32 v207, v207, v191\n\t"
        "v_perm_b32 v219, v247, v244, v177\n\t"
        "v_mad_u32_u16 v231, v244, 32, %13 op_sel:[1,0,0,0]\n\t"
        "v_cmpx_ne_u32 s[80:81], -1, v245\n\t"
        "v_mul_hi_u32_u24_sdwa v208, v245, %20 dst_sel:DWORD dst_unused:UNUSED_PAD src0_sel:WORD_0 src1_sel:DWORD\n\t"
        "v_and_b32_sdwa v196, v247, v176 dst_sel:DWORD dst_unused:UNUSED_PAD src0_sel:BYTE_1 src1_sel:DWORD\n\t"
        "v_lshl_add_u32 v208, v208, 2, v196\n\t"
        "ds_add_rtn_u32 v208, v208, v191\n\t"
        "v_perm_b32 v220, v247, v245, v178\n\t"
        "v_mad_u32_u16 v232, v245, 32, %13 op_sel:[1,0,0,0]\n\t"
        "s_and_b64 exec, exec, s[52:53]\n\t"
        "v_cmpx_ne_u32 s[82:83], -1, v246\n\t"
        "v_mul_hi_u32_u24_sdwa v209, v246, %20 dst_sel:DWORD dst_unused:UNUSED_PAD src0_sel:WORD_0 src1_sel:DWORD\n\t"
        "v_and_b32_sdwa v197, v247, v176 dst_sel:DWORD dst_unused:UNUSED_PAD src0_sel:BYTE_2 src1_sel:DWORD\n\t"
        "v_lshl_add_u32 v209, v209, 2, v197\n\t"
        "ds_add_rtn_u32 v209, v209, v191\n\t"
        "v_perm_b32 v221, v247, v246, v179\n\t"
        "v_mad_u32_u16 v233, v246, 32, %13 op_sel:[1,0,0,0]\n\t"
        "s_or_b64 s[54:55], s[54:55], s[58:59]\n\t"
        "21:\n\t"
        "s_mov_b64 s[84:85], 0\n\t"
        "s_mov_b64 exec, s[68:69]\n\t"
        "s_cbranch_execz 22f\n\t"
        "v_cmpx_ne_u32 s[84:85], -1, v248\n\t"
        "v_mul_hi_u32_u24_sdwa v210, v248, %20 dst_sel:DWORD dst_unused:UNUSED_PAD src0_sel:WORD_0 src1_sel:DWORD\n\t"
        "v_and_b32_sdwa v198, v251, v176 dst_sel:DWORD dst_unused:UNUSED_PAD src0_sel:BYTE_0 src1_sel:DWORD\n\t"
        "v_lshl_add_u32 v210, v210, 2, v198\n\t"
        "ds_add_rtn_u32 v210, v210, v191\n\t"
        "v_perm_b32 v222, v251, v248, v177\n\t"
        "v_mad_u32_u16 v234, v248, 32, %14 op_sel:[1,0,0,0]\n\t"
        "v_cmpx_ne_u32 s[86:87], -1, v249\n\t"
        "v_mul_hi_u32_u24_sdwa v211, v249, %20 dst_sel:DWORD dst_unused:UNUSED_PAD src0_sel:WORD_0 src1_sel:DWORD\n\t"
        "v_and_b32_sdwa v199, v251, v176 dst_sel:DWORD dst_unused:UNUSED_PAD src0_sel:BYTE_1 src1_sel:DWORD\n\t"
        "v_lshl_add_u32 v211, v211, 2, v199\n\t"
        "ds_add_rtn_u32 v211, v211, v191\n\t"
        "v_perm_b32 v223, v251, v249, v178\n\t"
        "v_mad_u32_u16 v235, v249, 32, %14 op_sel:[1,0,0,0]\n\t"
        "s_and_b64 exec, exec, s[52:53]\n\t"
        "v_cmpx_ne_u32 s[88:89], -1, v250\n\t"
        "v_mul_hi_u32_u24_sdwa v212, v250, %20 dst_sel:DWORD dst_unused:UNUSED_PAD src0_sel:WORD_0 src1_sel:DWORD\n\t"
        "v_and_b32_sdwa v200, v251, v176 dst_sel:DWORD dst_unused:UNUSED_PAD src0_sel:BYTE_2 src1_sel:DWORD\n\t"
        "v_lshl_add_u32 v212, v212, 2, v200\n\t"
        "ds_add_rtn_u32 v212, v212, v191\n\t"
        "v_perm_b32 v224, v251, v250, v179\n\t"
        "v_mad_u32_u16 v236, v250, 32, %14 op_sel:[1,0,0,0]\n\t"
        "s_or_b64 s[54:55], s[54:55], s[60:61]\n\t"
        "22:\n\t"
        "s_mov_b64 s[90:91], 0\n\t"
        "s_mov_b64 exec, s[70:71]\n\t"
        "s_cbranch_execz 23f\n\t"
        "v_cmpx_ne_u32 s[90:91], -1, v252\n\t"
        "v_mul_hi_u32_u24_sdwa v213, v252, %20 dst_sel:DWORD dst_unused:UNUSED_PAD src0_sel:WORD_0 src1_sel:DWORD\n\t"
        "v_and_b32_sdwa v201, v255, v176 dst_sel:DWORD dst_unused:UNUSED_PAD src0_sel:BYTE_0 src1_sel:DWORD\n\t"
        "v_lshl_add_u32 v213, v213, 2, v201\n\t"
        "ds_add_rtn_u32 v213, v213, v191\n\t"
        "v_perm_b32 v225, v255, v252, v177\n\t"
        "v_mad_u32_u16 v237, v252, 32, %15 op_sel:[1,0,0,0]\n\t"
        "v_cmpx_ne_u32 s[92:93], -1, v253\n\t"
        "v_mul_hi_u32_u24_sdwa v214, v253, %20 dst_sel:DWORD dst_unused:UNUSED_PAD src0_sel:WORD_0 src1_sel:DWORD\n\t"
        "v_and_b32_sdwa v202, v255, v176 dst_sel:DWORD dst_unused:UNUSED_PAD src0_sel:BYTE_1 src1_sel:DWORD\n\t"
        "v_lshl_add_u32 v214, v214, 2, v202\n\t"
        "ds_add_rtn_u32 v214, v214, v191\n\t"
        "v_perm_b32 v226, v255, v253, v178\n\t"
        "v_mad_u32_u16 v238, v253, 32, %15 op_sel:[1,0,0,0]\n\t"
        "s_and_b64 exec, exec, s[52:53]\n\t"
        "v_cmpx_ne_u32 s[94:95], -1, v254\n\t"
        "v_mul_hi_u32_u24_sdwa v215, v254, %20 dst_sel:DWORD dst_unused:UNUSED_PAD src0_sel:WORD_0 src1_sel:DWORD\n\t"
        "v_and_b32_sdwa v203, v255, v176 dst_sel:DWORD dst_unused:UNUSED_PAD src0_sel:BYTE_2 src1_sel:DWORD\n\t"
        "v_lshl_add_u32 v215, v215, 2, v203\n\t"
        "ds_add_rtn_u32 v215, v215, v191\n\t"
        "v_perm_b32 v227, v255, v254, v179\n\t"
        "v_mad_u32_u16 v239, v254, 32, %15 op_sel:[1,0,0,0]\n\t"
        "s_or_b64 s[54:55], s[54:55], s[62:63]\n\t"
        "23:\n\t"
        "s_cmp_eq_u64 s[54:55], 0\n\t"
        "s_cbranch_scc1 59f\n\t"
        "s_mov_b64 exec, s[56:57]\n\t"
        "s_cbranch_execz 40f\n\t"
        "v_bfe_u32 v188, %4, 7, 8\n\t"
        "v_lshrrev_b32 v189, 24, %4\n\t"
        "v_add_u32 v189, 1, v189\n\t"
        "ds_write_b8 v188, v189 offset:256\n\t"
        "v_add_u32 %4, s96, %4\n\t"
        "v_mov_b32 v190, 0\n\t"
        "v_cmpx_eq_u32 vcc, 2, v189\n\t"
        "ds_write_b32 v190, v191 offset:20\n\t"
        "s_andn2_b64 exec, s[56:57], vcc\n\t"
        "s_cbranch_execz 40f\n\t"
        "s_or_b64 %0, %0, exec\n\t"
        "global_load_dwordx4 v[240:243], %4, %16 sc1\n\t"
        "40:\n\t"
        "s_mov_b64 exec, s[58:59]\n\t"
        "s_cbranch_execz 41f\n\t"
        "v_bfe_u32 v188, %5, 7, 8\n\t"
        "v_lshrrev_b32 v189, 24, %5\n\t"
        "v_add_u32 v189, 1, v189\n\t"
        "ds_write_b8 v188, v189 offset:256\n\t"
        "v_add_u32 %5, s96, %5\n\t"
        "v_mov_b32 v190, 0\n\t"
        "v_cmpx_eq_u32 vcc, 2, v189\n\t"
        "ds_write_b32 v190, v191 offset:20\n\t"
        "s_andn2_b64 exec, s[58:59], vcc\n\t"
        "s_cbranch_execz 41f\n\t"
        "s_or_b64 %1, %1, exec\n\t"
        "global_load_dwordx4 v[244:247], %5, %16 sc1\n\t"
        "41:\n\t"
        "s_mov_b64 exec, s[60:61]\n\t"
        "s_cbranch_execz 42f\n\t"
        "v_bfe_u32 v188, %6, 7, 8\n\t"
        "v_lshrrev_b32 v189, 24, %6\n\t"
        "v_add_u32 v189, 1, v189\n\t"
        "ds_write_b8 v188, v189 offset:256\n\t"
        "v_add_u32 %6, s96, %6\n\t"
        "v_mov_b32 v190, 0\n\t"
        "v_cmpx_eq_u32 vcc, 2, v189\n\t"
        "ds_write_b32 v190, v191 offset:20\n\t"
        "s_andn2_b64 exec, s[60:61], vcc\n\t"
        "s_cbranch_execz 42f\n\t"
        "s_or_b64 %2, %2, exec\n\t"
        "global_load_dwordx4 v[248:251], %6, %16 sc1\n\t"
        "42:\n\t"
        "s_mov_b64 exec, s[62:63]\n\t"
        "s_cbranch_execz 43f\n\t"
        "v_bfe_u32 v188, %7, 7, 8\n\t"
        "v_lshrrev_b32 v189, 24, %7\n\t"
        "v_add_u32 v189, 1, v189\n\t"
        "ds_write_b8 v188, v189 offset:256\n\t"
        "v_add_u32 %7, s96, %7\n\t"
        "v_mov_b32 v190, 0\n\t"
        "v_cmpx_eq_u32 vcc, 2, v189\n\t"
        "ds_write_b32 v190, v191 offset:20\n\t"
        "s_andn2_b64 exec, s[62:63], vcc\n\t"
        "s_cbranch_execz 43f\n\t"
        "s_or_b64 %3, %3, exec\n\t"
        "global_load_dwordx4 v[252:255], %7, %16 sc1\n\t"
        "43:\n\t"
        "59:\n\t"
        "s_waitcnt lgkmcnt(0)\n\t"
        "s_mov_b64 exec, s[72:73]\n\t"
        "s_cbranch_execz 30f\n\t"
        "v_min_u32 v204, v181, v204\n\t"
        "v_min_u32 v205, v181, v205\n\t"
        "v_min_u32 v206, v181, v206\n\t"
        "v_mad_u32_u24 v204, v192, v180, v204\n\t"
        "v_mad_u32_u24 v205, v193, v180, v205\n\t"
        "v_mad_u32_u24 v206, v194, v180, v206\n\t"
        "ds_write2st64_b32 v204, v216, v228 offset1:10\n\t"
        "s_mov_b64 exec, s[74:75]\n\t"
        "ds_write2st64_b32 v205, v217, v229 offset1:10\n\t"
        "s_mov_b64 exec, s[76:77]\n\t"
        "ds_write2st64_b32 v206, v218, v230 offset1:10\n\t"
        "30:\n\t"
        "s_mov_b64 exec, s[78:79]\n\t"
        "s_cbranch_execz 31f\n\t"
        "v_min_u32 v207, v181, v207\n\t"
        "v_min_u32 v208, v181, v208\n\t"
        "v_min_u32 v209, v181, v209\n\t"
        "v_mad_u32_u24 v207, v195, v180, v207\n\t"
        "v_mad_u32_u24 v208, v196, v180, v208\n\t"
        "v_mad_u32_u24 v209, v197, v180, v209\n\t"
        "ds_write2st64_b32 v207, v219, v231 offset1:10\n\t"
        "s_mov_b64 exec, s[80:81]\n\t"
        "ds_write2st64_b32 v208, v220, v232 offset1:10\n\t"
        "s_mov_b64 exec, s[82:83]\n\t"
        "ds_write2st64_b32 v209, v221, v233 offset1:10\n\t"
        "31:\n\t"
        "s_mov_b64 exec, s[84:85]\n\t"
        "s_cbranch_execz 32f\n\t"
        "v_min_u32 v210, v181, v210\n\t"
        "v_min_u32 v211, v181, v211\n\t"
        "v_min_u32 v212, v181, v212\n\t"
        "v_mad_u32_u24 v210, v198, v180, v210\n\t"
        "v_mad_u32_u24 v211, v199, v180, v211\n\t"
        "v_mad_u32_u24 v212, v200, v180, v212\n\t"
        "ds_write2st64_b32 v210, v222, v234 offset1:10\n\t"
        "s_mov_b64 exec, s[86:87]\n\t"
        "ds_write2st64_b32 v211, v223, v235 offset1:10\n\t"
        "s_mov_b64 exec, s[88:89]\n\t"
        "ds_write2st64_b32 v212, v224, v236 offset1:10\n\t"
        "32:\n\t"
        "s_mov_b64 exec, s[90:91]\n\t"
        "s_cbranch_execz 33f\n\t"
        "v_min_u32 v213, v181, v213\n\t"
        "v_min_u32 v214, v181, v214\n\t"
        "v_min_u32 v215, v181, v215\n\t"
        "v_mad_u32_u24 v213, v201, v180, v213\n\t"
        "v_mad_u32_u24 v214, v202, v180, v214\n\t"
        "v_mad_u32_u24 v215, v203, v180, v215\n\t"
        "ds_write2st64_b32 v213, v225, v237 offset1:10\n\t"
        "s_mov_b64 exec, s[92:93]\n\t"
        "ds_write2st64_b32 v214, v226, v238 offset1:10\n\t"
        "s_mov_b64 exec, s[94:95]\n\t"
        "ds_write2st64_b32 v215, v227, v239 offset1:10\n\t"
        "33:\n\t"
        "s_mov_b64 exec, -1\n\t"
        : "+&s"(pend[0]), "+&s"(pend[1]), "+&s"(pend[2]), "+&s"(pend[3]), "+v"(vo[0]), "+v"(vo[1]), "+v"(vo[2]),
          "+v"(vo[3]), "+{v[240:243]}"(cv[0]), "+{v[244:247]}"(cv[1]), "+{v[248:251]}"(cv[2]), "+{v[252:255]}"(cv[3])
        : "v"(eb[0]), "v"(eb[1]), "v"(eb[2]), "v"(eb[3]), "s"(ib), "n"(OFF_L + LARR - 4), "v"(tag[0]), "v"(tag[1]),
          "s"(sq), "s"(just)
        :
          "memory", "vcc", "scc", "v176", "v177", "v178", "v179", "v180", "v181", "v188", "v189", "v190", "v191",
          "v192", "v193", "v194", "v195", "v196", "v197", "v198", "v199", "v200", "v201", "v202", "v203", "v204",
          "v205", "v206", "v207", "v208", "v209", "v210", "v211", "v212", "v213", "v214", "v215", "v216", "v217",
          "v218", "v219", "v220", "v221", "v222", "v223", "v224", "v225", "v226", "v227", "v228", "v229", "v230",
          "v231", "v232", "v233", "v234", "v235", "v236", "v237", "v238", "v239", "s52", "s53", "s54", "s55", "s56",
          "s57", "s58", "s59", "s60", "s61", "s62", "s63", "s64", "s65", "s66", "s67", "s68", "s69", "s70", "s71",
          "s72", "s73", "s74", "s75", "s76", "s77", "s78", "s79", "s80", "s81", "s82", "s83", "s84", "s85", "s86",
          "s87", "s88", "s89", "s90", "s91", "s92", "s93", "s94", "s95", "s96");
    uni();
    just = 0u;
    asm volatile("v_readlane_b32 s92, %0, 14\n\ts_nop 3\n\ts_add_u32 s92, s92, 1\n\ts_nop 3\n\tv_writelane_b32 %0, s92, 14" : "+v"(stv) :: "s92", "scc");
  }
  PT(10);
  *(LAS unsigned*)((LAS char*)smem + OFF_CNT + 16 + wave * 32) = early;
  asm volatile("s_waitcnt lgkmcnt(0)\n\ts_barrier" ::: "memory");
  if (wave >= 4) __builtin_amdgcn_s_setprio(1);
  PT(11);
  const long long tmA = __builtin_amdgcn_s_memrealtime();
  asm volatile("" : "+v"(tm0v));
  const unsigned tm0s = __builtin_amdgcn_readfirstlane(tm0v);
  const u32x4 cw = *(const LAS u32x4*)((LAS char*)smem + OFF_CNT + rid * 32);
  const unsigned ef = *(const LAS unsigned*)((LAS char*)smem + OFF_CNT + 16 + rid * 32);
  unsigned slowf = *(const LAS unsigned*)((LAS char*)smem + OFF_CNT + 20);
  asm volatile("" : "+v"(slowf));
  const int key = (int)((cw[0] + cw[1] + cw[2] + cw[3]) * 2u + rid);
  int gb;
  {
    unsigned acc = 0u;
#define RK(k) acc = __builtin_amdgcn_alignbit(acc, (unsigned)(__builtin_amdgcn_update_dpp(0, key, 0x120 + k, 0xf, 0xf, false) - key), 31)
    RK(1); RK(2); RK(3); RK(4); RK(5); RK(6); RK(7);
#undef RK
    const int want = RANKOF(wave);
    gb = __builtin_ctzll(__ballot(__builtin_popcount(acc) == want)) & 7;
  }
  const unsigned ring0 = lds0 + OFF_R0 + gb * RING_SLOT;
  const unsigned la = l0 + gb * LIST_BYTES;
  u32x16 XAf;
#pragma unroll
  for (int q = 0; q < 4; ++q) {
    const u32x4 t = ld128(ring0 + ringd + q * 1024 + lane * 16);
#pragma unroll
    for (int j = 0; j < 4; ++j) XAf[4 * q + j] = t[j];
  }
  unsigned sl[4];
#pragma unroll
  for (int q = 0; q < 4; ++q) sl[q] = __builtin_amdgcn_readlane(cw[q], gb) - (l0 + q * SUBL);
  const unsigned o2 = sl[0] + sl[1], o3 = o2 + sl[2];
  const bool slow = o3 + sl[3] > LCAP * 4u || max(max(sl[0], sl[1]), max(sl[2], sl[3])) > SUBL - 4u ||
                    __builtin_amdgcn_readfirstlane(slowf) != 0u;
  const int cnt = slow ? 0 : (int)(o3 + sl[3]) >> 2;
  const unsigned cg = (bu * 8 + gb) * 32;
  u32x16 AD;
  AD[2] = aLX + gb * LIST_BYTES;
  AD[3] = aLE + gb * LIST_BYTES;
  AD[4] = cx0; AD[5] = cx1; AD[6] = ce;
  auto issue_gather = [&](int tile, int rs) {
    const u32x4 sx = ld128(AD[2] + tile * 128);
    glds16e(ES, *(const LAS unsigned*)(size_t)(AD[3] + tile * 128), AD[6], ring0 + rs * ringd + 4096);
#pragma unroll
    for (int q = 0; q < 4; ++q) glds16x(x, sx[q], AD[4 + (q & 1)], ring0 + rs * ringd + q * 1024);
  };
  auto pads = [&](int cnt, int lane) __attribute__((always_inline)) {
    const int u = cnt + lane;
    const unsigned c = cg + ((u >> 2) & 7) + 8 * (u & 3), n = nodeof(c);
    const unsigned padA = n | (__builtin_amdgcn_readlane(c, __builtin_ctzll(__ballot((n * Mw + Cw) >> 16 != c))) & 0xffu) << 16;
    const unsigned pa = la + u * 4;
    *(LAS unsigned*)(size_t)pa = padA;
    *(LAS unsigned*)(size_t)(pa + 256) = padA;
    *(LAS unsigned*)(size_t)(pa + LARR) = 0u;
    *(LAS unsigned*)(size_t)(pa + LARR + 256) = 0u;
  };
  constexpr int GEND = 64 + NCH;
  int gs = 0, gp = 0;
  auto gen = [&]() __attribute__((always_inline)) {
    int l2, n = 0;
    asm volatile("v_mbcnt_lo_u32_b32 %0, -1, 0\n\tv_mbcnt_hi_u32_b32 %0, -1, %0" : "=v"(l2));
    const LAS unsigned char* xf = (const LAS unsigned char*)smem + OFF_XF;
    auto put = [&](bool ok, unsigned t, unsigned c, int cc) {
      ok = ok && (int)(c >> 5) == gb;
      const unsigned long long bm = __ballot(ok);
      const unsigned pa = la + 4u * (n + __builtin_amdgcn_mbcnt_hi((unsigned)(bm >> 32), __builtin_amdgcn_mbcnt_lo((unsigned)bm, 0u)));
      if (ok) {
        *(LAS unsigned*)(size_t)pa = (t & 0xffffu) | (c << 16);
        *(LAS unsigned*)(size_t)(pa + LARR) = ((unsigned)(cc * chunk) + (t >> 16)) * 32u;
      }
      n += __builtin_popcountll(bm);
    };
#pragma unroll 1
    while (gs < GEND && n <= LCAP - 192) {
      if (gs < 64) {
        const int cc = (gs & 31) * 8 + (l2 >> 3);
        const unsigned* pp = reinterpret_cast<const unsigned*>(ib + (gs >> 5) * IEXT + (gs & 31) * 1024 + l2 * 16);
        const bool live = gs < 32 || xf[cc] != 0;
        const u32x2 tg = *(const LAS u32x2*)((LAS char*)smem + OFF_CNT + 56);
#pragma unroll 1
        for (int i = 0; i < (1 << 20); ++i) {
          const unsigned m = ldd(pp + 3);
          unsigned sm_ = ldd(pp) + ldd(pp + 1) + ldd(pp + 2) + m + tg[0];
          sm_ += __builtin_amdgcn_update_dpp(0u, sm_, 0xB1, 0xf, 0xf, false);
          sm_ += __builtin_amdgcn_update_dpp(0u, sm_, 0x4E, 0xf, 0xf, false);
          sm_ += __builtin_amdgcn_update_dpp(0u, sm_, 0x141, 0xf, 0xf, false);
          if (__ballot(live && (sm_ > 1u || m >> 24 != tg[1])) == 0ull) break;
        }
        const unsigned m = ldd(pp + 3);
#pragma unroll 1
        for (int j = 0; j < 3; ++j) {
          const unsigned t = ldd(pp + j);
          put(live && t != ~0u && (j < 2 || (l2 & 7) != 7), t, (m >> (8 * j)) & 0xffu, cc);
        }
        ++gs;
      } else {
        const int c2 = gs - 64 + l2;
        const unsigned long long fm = __ballot(c2 < NCH && xf[min(c2, NCH - 1)] == 2);
        if (fm == 0ull) { gs = min(gs + 64, GEND); continue; }
        gs += __builtin_ctzll(fm);
        const int cc = gs - 64;
        const unsigned long long ds = ldq(FL + 512 + bu * NCH + cc);
        const int len = __builtin_amdgcn_readfirstlane((int)(ds >> 32));
        const unsigned long long* tp = TS + (unsigned)ds;
#pragma unroll 1
        for (; gp < len && n <= LCAP - 64; gp += 64) {
          const unsigned long long t = ldq(tp + min(gp + l2, len - 1));
          put(gp + l2 < len, (unsigned)t, (unsigned)(t >> 32), cc);
        }
        if (gp >= len) { gp = 0; ++gs; }
      }
    }
    __builtin_amdgcn_s_waitcnt(0x0F70);
    pads(n, l2);
    return n;
  };
  const bool quick = !slow && sl[0] >= 256u && __builtin_amdgcn_readlane(ef, gb) != 0u;
  auto gather1 = [&](unsigned skip) {
    u32x4 sx = ld128(AD[2] + 128);
    unsigned re = *(const LAS unsigned*)(size_t)(AD[3] + 128);
    asm volatile("s_cmp_lg_u32 %13, 0\n\t"
                 "s_cbranch_scc1 1f\n\t"
                 "s_add_i32 m0, %11, 4096\n\t"
                 "v_add_u32 %4, %4, %8\n\t"
                 "v_mad_u32_u16 %0, %0, %12, %6\n\t"
                 "global_load_lds_dwordx4 %4, %10\n\t"
                 "s_mov_b32 m0, %11\n\t"
                 "v_mad_u32_u16 %1, %1, %12, %7\n\t"
                 "global_load_lds_dwordx4 %0, %9\n\t"
                 "s_addk_i32 m0, 1024\n\t"
                 "v_mad_u32_u16 %2, %2, %12, %6\n\t"
                 "global_load_lds_dwordx4 %1, %9\n\t"
                 "s_addk_i32 m0, 1024\n\t"
                 "v_mad_u32_u16 %3, %3, %12, %7\n\t"
                 "global_load_lds_dwordx4 %2, %9\n\t"
                 "s_addk_i32 m0, 1024\n\t"
                 "s_nop 0\n\t"
                 "global_load_lds_dwordx4 %3, %9\n"
                 "1:"
                 : "+v"(sx[0]), "+v"(sx[1]), "+v"(sx[2]), "+v"(sx[3]), "+v"(re), "+{v[128:143]}"(XAf)
                 : "v"(cx0), "v"(cx1), "v"(ce), "s"(x), "s"(ES), "s"(ring0 + ringd), "s"(128u), "s"(skip)
                 : "memory", "m0", "scc");
  };
  unsigned mode;
  asm volatile("s_cmp_lg_u64 %1, 0\n\ts_cselect_b32 %0, %2, 0" : "=s"(mode) : "s"(__ballot(quick)), "s"(1 + wave / G1W) : "scc");
  gather1(mode ^ 2u);
  if (__builtin_expect(!slow, 1)) {
    const unsigned p0 = la + lane * 4;
    asm volatile(
        "ds_read2st64_b32 v[222:223], %1 offset0:2 offset1:12\n\t"
        "ds_read2st64_b32 v[224:225], %1 offset0:3 offset1:13\n\t"
        "ds_read2st64_b32 v[226:227], %1 offset0:4 offset1:14\n\t"
        "ds_read2st64_b32 v[228:229], %0 offset0:5 offset1:15\n\t"
        "ds_read2st64_b32 v[230:231], %0 offset0:6 offset1:16\n\t"
        "ds_read2st64_b32 v[232:233], %0 offset0:7 offset1:17\n\t"
        "ds_read2st64_b32 v[234:235], %1 offset0:7 offset1:17\n\t"
        "ds_read2st64_b32 v[236:237], %1 offset0:8 offset1:18\n\t"
        "ds_read2st64_b32 v[238:239], %1 offset0:9 offset1:19\n\t"
        "s_waitcnt lgkmcnt(8)\n\t"
        "ds_write2st64_b32 %2, v222, v223 offset0:0 offset1:10\n\t"
        "s_waitcnt lgkmcnt(8)\n\t"
        "ds_write2st64_b32 %2, v224, v225 offset0:1 offset1:11\n\t"
        "s_waitcnt lgkmcnt(8)\n\t"
        "ds_write2st64_b32 %2, v226, v227 offset0:2 offset1:12\n\t"
        "s_waitcnt lgkmcnt(8)\n\t"
        "ds_write2st64_b32 %3, v228, v229 offset0:0 offset1:10\n\t"
        "s_waitcnt lgkmcnt(8)\n\t"
        "ds_write2st64_b32 %3, v230, v231 offset0:1 offset1:11\n\t"
        "s_waitcnt lgkmcnt(8)\n\t"
        "ds_write2st64_b32 %3, v232, v233 offset0:2 offset1:12\n\t"
        "s_waitcnt lgkmcnt(8)\n\t"
        "ds_write2st64_b32 %4, v234, v235 offset0:0 offset1:10\n\t"
        "s_waitcnt lgkmcnt(8)\n\t"
        "ds_write2st64_b32 %4, v236, v237 offset0:1 offset1:11\n\t"
        "s_mov_b64 exec, 0xffffffff\n\t"
        "s_waitcnt lgkmcnt(8)\n\t"
        "ds_write2st64_b32 %4, v238, v239 offset0:2 offset1:12\n\t"
        "s_mov_b64 exec, -1"
        :: "v"(p0), "v"(p0 + 128u), "v"(p0 + sl[0]), "v"(p0 + o2), "v"(p0 + o3)
        : "memory", "v222", "v223", "v224", "v225", "v226", "v227", "v228", "v229", "v230", "v231", "v232", "v233", "v234",
          "v235", "v236", "v237", "v238", "v239");
  }
  pads(cnt, lane);
  AD[0] = aLS4 + gb * LIST_BYTES;
  AD[1] = aLS1 + gb * LIST_BYTES;
  AD[7] = cS - gb * 32 * TPITCH;
  AD[8] = cSt - gb * 32 * TPITCH;
#pragma unroll
  for (int t = 0; t < 4; ++t) AD[9 + t] = fo[t] + gb * RING_SLOT;
  AD[13] = foe + gb * RING_SLOT;
  AD[14] = lds0 + lane * 16;
  AD[15] = to + 18 * 1024 + h * 4096;
  if (__builtin_expect(mode == 0u, 0)) asm volatile("s_waitcnt vmcnt(0)\n\ts_barrier" ::: "memory");
  const int gbin = bk * 8 + wave;
  long long tm1 = 0, c1 = 0;
  const long long tmB = __builtin_amdgcn_s_memrealtime(); __builtin_amdgcn_s_waitcnt(0xC07F);
#define STAMP1 "s_memrealtime %[t1]\n\ts_memtime %[c1]\n\ts_waitcnt lgkmcnt(0)\n\t"
  PT(12);
  PV(15, mode);
  if (lane < 16) *(LAS unsigned*)((LAS char*)smem + MAIN_LDS + wave * 64 + lane * 4) = stv;
  f32x16 X[2], U[2];
  if (__builtin_expect(mode == 0u, 0)) {
    issue_gather(0, 0);
    issue_gather(1, 1);
  }
  {
    int nt = __builtin_amdgcn_readfirstlane(max((cnt - 1) >> 5, 0));
    asm volatile(
        "s_movk_i32 s94, 128\n\t"
        "s_movk_i32 s95, 80\n\t"
        "s_mov_b32 s96, %[cu]\n\t"
        "ds_read_u16 v102, v161\n\t"
        "ds_read_b128 v[104:107], v162 offset:128\n\t"
        "ds_read_b32 v101, v163 offset:128\n\t"
        "ds_read_b128 v[96:99], v162 offset:256\n\t"
        "ds_read_b32 v100, v163 offset:256\n\t"
        "ds_read_b128 v[120:123], v160\n\t"
        "ds_read_u16 v124, v161 offset:128\n\t"
        "s_waitcnt lgkmcnt(6)\n\t"
        "v_mad_u32_u24 v102, v102, s95, v168\n\t"
        "ds_read_b128 v[184:187], v102\n\t"
        "ds_read_b128 v[188:191], v102 offset:16\n\t"
        "ds_read_b128 v[112:115], v174 offset:512\n\t"
        "ds_read_b128 v[116:119], v174 offset:2560\n\t"
        "ds_read_b128 v[108:111], v174 offset:8704\n\t"
        "ds_read_b128 v[196:199], v174 offset:16896\n\t"
        "ds_read_b128 v[200:203], v174 offset:1536\n\t"
        "ds_read_b128 v[204:207], v174 offset:3584\n\t"
        "s_waitcnt lgkmcnt(12)\n\t"
        "v_add_u32 v101, v101, v166\n\t"
        "v_mad_u32_u16 v104, v104, s94, v164\n\t"
        "v_mad_u32_u16 v105, v105, s94, v165\n\t"
        "v_mad_u32_u16 v106, v106, s94, v164\n\t"
        "v_mad_u32_u16 v107, v107, s94, v165\n\t"
        "s_waitcnt lgkmcnt(7)\n\t"
        "v_mfma_f32_32x32x16_f16 v[64:79], v[184:187], v[128:131], 0\n\t"
        "ds_read_b128 v[208:211], v174 offset:9728\n\t"
        "ds_read_b128 v[212:215], v174 offset:10752\n\t"
        "ds_read_b128 v[216:219], v174 offset:11776\n\t"
        "ds_read_b128 v[220:223], v174 offset:17920\n\t"
        "ds_read_b128 v[224:227], v174 offset:4608\n\t"
        "ds_read_b128 v[228:231], v174 offset:5632\n\t"
        "s_waitcnt lgkmcnt(12)\n\t"
        "v_mfma_f32_32x32x16_f16 v[64:79], v[188:191], v[136:139], v[64:79]\n\t"
        "ds_read_b128 v[232:235], v174 offset:6656\n\t"
        "ds_read_b128 v[236:239], v174 offset:7680\n\t"
        "ds_read_b128 v[240:243], v174 offset:12800\n\t"
        "ds_read_b128 v[244:247], v174 offset:13824\n\t"
        "ds_read_b128 v[248:251], v174 offset:14848\n\t"
        "ds_read_b128 v[252:255], v174 offset:15872\n\t"
        "s_add_i32 m0, %[ring], 45056\n\t"
        "s_waitcnt vmcnt(5)\n\t"
        "s_cmp_eq_u32 %[flag], 0\n\t"
        "s_cbranch_scc1 2f\n\t"
        "s_cmp_eq_u32 %[flag], 2\n\t"
        "s_cbranch_scc1 3f\n\t"
        "s_waitcnt vmcnt(0)\n\t"
        "3:\n\t"
        "s_barrier\n\t"
        "2:\n\t"
        "ds_read_b128 v[144:147], v169\n\t"
        "ds_read_b128 v[148:151], v170\n\t"
        "ds_read_b128 v[152:155], v171\n\t"
        "ds_read_b128 v[156:159], v172\n\t"
        "ds_read_b128 v[192:195], v173\n\t"
        "s_cbranch_scc1 4f\n\t"
        "global_load_lds_dwordx4 v101, %[es]\n\t"
        "s_add_i32 m0, %[ring], 40960\n\t"
        "s_nop 0\n\t"
        "global_load_lds_dwordx4 v104, %[x]\n\t"
        "s_add_i32 m0, %[ring], 41984\n\t"
        "s_nop 0\n\t"
        "global_load_lds_dwordx4 v105, %[x]\n\t"
        "s_add_i32 m0, %[ring], 43008\n\t"
        "s_nop 0\n\t"
        "global_load_lds_dwordx4 v106, %[x]\n\t"
        "s_add_i32 m0, %[ring], 44032\n\t"
        "s_nop 0\n\t"
        "global_load_lds_dwordx4 v107, %[x]\n\t"
        "4:\n\t"
        "s_waitcnt lgkmcnt(4)\n\t"
        "v_mfma_f32_32x32x16_bf16 v[64:79], v[144:147], v[112:115], v[64:79]\n\t"
        "s_waitcnt lgkmcnt(3)\n\t"
        "v_mfma_f32_32x32x16_bf16 v[64:79], v[148:151], v[200:203], v[64:79]\n\t"
        "s_waitcnt lgkmcnt(2)\n\t"
        "v_mfma_f32_32x32x16_bf16 v[64:79], v[152:155], v[116:119], v[64:79]\n\t"
        "s_waitcnt lgkmcnt(1)\n\t"
        "v_mfma_f32_32x32x16_bf16 v[64:79], v[156:159], v[204:207], v[64:79]\n\t"
        "s_waitcnt lgkmcnt(0)\n\t"
        "v_mfma_f32_32x32x16_bf16 v[64:79], v[192:195], v[196:199], v[64:79]\n\t"
        "s_add_i32 m0, %[ring], 4096\n\t"
        "v_add_u32 v100, v100, v166\n\t"
        "global_load_lds_dwordx4 v100, %[es]\n\t"
        "v_mfma_f32_32x32x16_bf16 v[80:95], v[144:147], v[108:111], 0\n\t"
        "s_mov_b32 m0, %[ring]\n\t"
        "v_mad_u32_u16 v96, v96, s94, v164\n\t"
        "global_load_lds_dwordx4 v96, %[x]\n\t"
        "s_add_i32 m0, %[ring], 1024\n\t"
        "v_mad_u32_u16 v97, v97, s94, v165\n\t"
        "global_load_lds_dwordx4 v97, %[x]\n\t"
        "v_mfma_f32_32x32x16_bf16 v[80:95], v[148:151], v[208:211], v[80:95]\n\t"
        "s_add_i32 m0, %[ring], 2048\n\t"
        "v_mad_u32_u16 v98, v98, s94, v164\n\t"
        "global_load_lds_dwordx4 v98, %[x]\n\t"
        "v_exp_f32 v64, v64\n\t"
        "v_exp_f32 v65, v65\n\t"
        "v_mfma_f32_32x32x16_bf16 v[80:95], v[152:155], v[212:215], v[80:95]\n\t"
        "s_add_i32 m0, %[ring], 3072\n\t"
        "v_mad_u32_u16 v99, v99, s94, v165\n\t"
        "global_load_lds_dwordx4 v99, %[x]\n\t"
        "v_exp_f32 v66, v66\n\t"
        "v_exp_f32 v67, v67\n\t"
        "v_mfma_f32_32x32x16_bf16 v[80:95], v[156:159], v[216:219], v[80:95]\n\t"
        "v_cvt_pk_f16_f32 v64, v64, v65\n\t"
        "v_exp_f32 v68, v68\n\t"
        "v_cvt_pk_f16_f32 v65, v66, v67\n\t"
        "v_pk_add_f16 v64, v64, s96\n\t"
        "v_exp_f32 v69, v69\n\t"
        "v_pk_add_f16 v65, v65, s96\n\t"
        "v_rcp_f16_sdwa v64, v64 dst_sel:WORD_0 dst_unused:UNUSED_PRESERVE src0_sel:WORD_0\n\t"
        "v_exp_f32 v70, v70\n\t"
        "v_cvt_pk_f16_f32 v66, v68, v69\n\t"
        "v_rcp_f16_sdwa v65, v65 dst_sel:WORD_0 dst_unused:UNUSED_PRESERVE src0_sel:WORD_0\n\t"
        "v_pk_add_f16 v66, v66, s96\n\t"
        "v_rcp_f16_sdwa v64, v64 dst_sel:WORD_1 dst_unused:UNUSED_PRESERVE src0_sel:WORD_1\n\t"
        "v_cvt_pk_f16_f32 v80, v80, v81\n\t"
        "v_rcp_f16_sdwa v65, v65 dst_sel:WORD_1 dst_unused:UNUSED_PRESERVE src0_sel:WORD_1\n\t"
        "v_pk_mul_f16 v80, v80, v64\n\t"
        STAMP1
        "s_branch .Lr4_%=\n\t"
        ".Lrt_%=:\n\t"
        "v_mfma_f32_32x32x16_f16 v[16:31], v[96:99], v[176:179], v[16:31]\n\t"
        "s_cmp_lt_u32 %[wave], 4\n\t"
        "s_cbranch_scc1 1f\n\t"
        "s_setprio 1\n\t"
        "1:\n\t"
        "ds_read_b128 v[120:123], v160\n\t"
        "ds_read_u16 v124, v161 offset:128\n\t"
        "v_rcp_f16_sdwa v103, v103 dst_sel:WORD_1 dst_unused:UNUSED_PRESERVE src0_sel:WORD_1\n\t"
        "v_cvt_pk_f16_f32 v119, v126, v127\n\t"
        "v_pk_add_f16 v64, v64, s96\n\t"
        "v_exp_f32 v67, v67\n\t"
        "v_rcp_f16_sdwa v64, v64 dst_sel:WORD_0 dst_unused:UNUSED_PRESERVE src0_sel:WORD_0\n\t"
        "v_mfma_f32_32x32x16_f16 v[48:63], v[112:115], v[176:179], v[48:63]\n\t"
        "v_pk_mul_f16 v119, v119, v103\n\t"
        "v_cvt_pk_f16_f32 v65, v66, v67\n\t"
        "v_rcp_f16_sdwa v64, v64 dst_sel:WORD_1 dst_unused:UNUSED_PRESERVE src0_sel:WORD_1\n\t"
        "v_pk_add_f16 v65, v65, s96\n\t"
        "v_exp_f32 v68, v68\n\t"
        "v_mfma_f32_32x32x16_f16 v[16:31], v[100:103], v[180:183], v[16:31]\n\t"
        "v_cvt_pk_f16_f32 v80, v80, v81\n\t"
        "v_rcp_f16_sdwa v65, v65 dst_sel:WORD_0 dst_unused:UNUSED_PRESERVE src0_sel:WORD_0\n\t"
        "v_pk_mul_f16 v80, v80, v64\n\t"
        "v_exp_f32 v69, v69\n\t"
        "v_mfma_f32_32x32x16_f16 v[48:63], v[116:119], v[180:183], v[48:63]\n\t"
        "v_cvt_pk_f16_f32 v66, v68, v69\n\t"
        "v_rcp_f16_sdwa v65, v65 dst_sel:WORD_1 dst_unused:UNUSED_PRESERVE src0_sel:WORD_1\n\t"
        "v_pk_add_f16 v66, v66, s96\n\t"
        "v_exp_f32 v70, v70\n\t"
        ".Lr4_%=:\n\t"
        "v_mfma_f32_32x32x16_f16 v[96:111], v[184:187], v[132:135], 0\n\t"
        "s_waitcnt lgkmcnt(1)\n\t"
        "v_mad_u32_u16 v120, v120, s95, v167 op_sel:[1,0,0,0]\n\t"
        "ds_read_b64_tr_b16 v[176:177], v120\n\t"
        "v_mad_u32_u16 v121, v121, s95, v167 op_sel:[1,0,0,0]\n\t"
        "ds_read_b64_tr_b16 v[178:179], v121\n\t"
        "v_mad_u32_u16 v122, v122, s95, v167 op_sel:[1,0,0,0]\n\t"
        "ds_read_b64_tr_b16 v[180:181], v122\n\t"
        "v_mad_u32_u16 v123, v123, s95, v167 op_sel:[1,0,0,0]\n\t"
        "ds_read_b64_tr_b16 v[182:183], v123\n\t"
        "v_cvt_pk_f16_f32 v81, v82, v83\n\t"
        "v_rcp_f16_sdwa v66, v66 dst_sel:WORD_0 dst_unused:UNUSED_PRESERVE src0_sel:WORD_0\n\t"
        "v_pk_mul_f16 v81, v81, v65\n\t"
        "v_exp_f32 v71, v71\n\t"
        "v_mfma_f32_32x32x16_f16 v[96:111], v[188:191], v[140:143], v[96:111]\n\t"
        "s_waitcnt lgkmcnt(4)\n\t"
        "v_mad_u32_u24 v124, v124, s95, v168\n\t"
        "ds_read_b128 v[184:187], v124\n\t"
        "ds_read_b128 v[188:191], v124 offset:16\n\t"
        "v_cvt_pk_f16_f32 v67, v70, v71\n\t"
        "v_rcp_f16_sdwa v66, v66 dst_sel:WORD_1 dst_unused:UNUSED_PRESERVE src0_sel:WORD_1\n\t"
        "v_pk_add_f16 v67, v67, s96\n\t"
        "v_exp_f32 v72, v72\n\t"
        "v_mfma_f32_32x32x16_bf16 v[96:111], v[192:195], v[220:223], v[96:111]\n\t"
        "s_waitcnt vmcnt(9)\n\t"
        "ds_read_b128 v[192:195], v173 offset:40960\n\t"
        "ds_read_b32 v125, v163 offset:384\n\t"
        "v_cvt_pk_f16_f32 v82, v84, v85\n\t"
        "v_rcp_f16_sdwa v67, v67 dst_sel:WORD_0 dst_unused:UNUSED_PRESERVE src0_sel:WORD_0\n\t"
        "v_pk_mul_f16 v82, v82, v66\n\t"
        "v_exp_f32 v73, v73\n\t"
        "v_mfma_f32_32x32x16_bf16 v[96:111], v[144:147], v[224:227], v[96:111]\n\t"
        "v_cvt_pk_f16_f32 v68, v72, v73\n\t"
        "v_rcp_f16_sdwa v67, v67 dst_sel:WORD_1 dst_unused:UNUSED_PRESERVE src0_sel:WORD_1\n\t"
        "v_pk_add_f16 v68, v68, s96\n\t"
        "v_exp_f32 v74, v74\n\t"
        "v_mfma_f32_32x32x16_bf16 v[96:111], v[148:151], v[228:231], v[96:111]\n\t"
        "v_cvt_pk_f16_f32 v83, v86, v87\n\t"
        "v_rcp_f16_sdwa v68, v68 dst_sel:WORD_0 dst_unused:UNUSED_PRESERVE src0_sel:WORD_0\n\t"
        "v_pk_mul_f16 v83, v83, v67\n\t"
        "v_exp_f32 v75, v75\n\t"
        "v_mfma_f32_32x32x16_bf16 v[96:111], v[152:155], v[232:235], v[96:111]\n\t"
        "s_add_i32 m0, %[ring], 45056\n\t"
        "s_waitcnt lgkmcnt(0)\n\t"
        "v_add_u32 v125, v125, v166\n\t"
        "global_load_lds_dwordx4 v125, %[es]\n\t"
        "v_cvt_pk_f16_f32 v69, v74, v75\n\t"
        "v_rcp_f16_sdwa v68, v68 dst_sel:WORD_1 dst_unused:UNUSED_PRESERVE src0_sel:WORD_1\n\t"
        "v_pk_add_f16 v69, v69, s96\n\t"
        "v_exp_f32 v76, v76\n\t"
        "v_mfma_f32_32x32x16_bf16 v[96:111], v[156:159], v[236:239], v[96:111]\n\t"
        "v_cvt_pk_f16_f32 v84, v88, v89\n\t"
        "v_rcp_f16_sdwa v69, v69 dst_sel:WORD_0 dst_unused:UNUSED_PRESERVE src0_sel:WORD_0\n\t"
        "v_pk_mul_f16 v84, v84, v68\n\t"
        "v_exp_f32 v77, v77\n\t"
        "v_mfma_f32_32x32x16_bf16 v[112:127], v[144:147], v[240:243], 0\n\t"
        "s_waitcnt vmcnt(6)\n\t"
        "ds_read_b128 v[144:147], v169 offset:40960\n\t"
        "v_cvt_pk_f16_f32 v70, v76, v77\n\t"
        "v_rcp_f16_sdwa v69, v69 dst_sel:WORD_1 dst_unused:UNUSED_PRESERVE src0_sel:WORD_1\n\t"
        "v_pk_add_f16 v70, v70, s96\n\t"
        "v_exp_f32 v78, v78\n\t"
        "v_mfma_f32_32x32x16_bf16 v[112:127], v[148:151], v[244:247], v[112:127]\n\t"
        "ds_read_b128 v[148:151], v170 offset:40960\n\t"
        "s_sub_u32 %[nt], %[nt], 1\n\t"
        "v_cvt_pk_f16_f32 v85, v90, v91\n\t"
        "v_rcp_f16_sdwa v70, v70 dst_sel:WORD_0 dst_unused:UNUSED_PRESERVE src0_sel:WORD_0\n\t"
        "v_pk_mul_f16 v85, v85, v69\n\t"
        "v_exp_f32 v79, v79\n\t"
        "v_mfma_f32_32x32x16_bf16 v[112:127], v[152:155], v[248:251], v[112:127]\n\t"
        "ds_read_b128 v[152:155], v171 offset:40960\n\t"
        "ds_read_b128 v[88:91], v162 offset:384\n\t"
        "v_cvt_pk_f16_f32 v71, v78, v79\n\t"
        "v_rcp_f16_sdwa v70, v70 dst_sel:WORD_1 dst_unused:UNUSED_PRESERVE src0_sel:WORD_1\n\t"
        "v_pk_add_f16 v71, v71, s96\n\t"
        "v_exp_f32 v96, v96\n\t"
        "v_exp_f32 v97, v97\n\t"
        "v_mfma_f32_32x32x16_bf16 v[112:127], v[156:159], v[252:255], v[112:127]\n\t"
        "ds_read_b128 v[156:159], v172 offset:40960\n\t"
        "v_cvt_pk_f16_f32 v86, v92, v93\n\t"
        "v_rcp_f16_sdwa v71, v71 dst_sel:WORD_0 dst_unused:UNUSED_PRESERVE src0_sel:WORD_0\n\t"
        "v_pk_mul_f16 v86, v86, v70\n\t"
        "v_exp_f32 v98, v98\n\t"
        "v_cvt_pk_f16_f32 v96, v96, v97\n\t"
        "s_cbranch_scc1 .Lrl_%=\n\t"
        "v_mfma_f32_32x32x16_f16 v[0:15], v[64:67], v[176:179], v[0:15]\n\t"
        "v_rcp_f16_sdwa v71, v71 dst_sel:WORD_1 dst_unused:UNUSED_PRESERVE src0_sel:WORD_1\n\t"
        "v_cvt_pk_f16_f32 v87, v94, v95\n\t"
        "v_pk_add_f16 v96, v96, s96\n\t"
        "v_exp_f32 v99, v99\n\t"
        "v_rcp_f16_sdwa v96, v96 dst_sel:WORD_0 dst_unused:UNUSED_PRESERVE src0_sel:WORD_0\n\t"
        "v_mfma_f32_32x32x16_f16 v[32:47], v[80:83], v[176:179], v[32:47]\n\t"
        "s_add_i32 m0, %[ring], 40960\n\t"
        "s_waitcnt lgkmcnt(1)\n\t"
        "v_mad_u32_u16 v88, v88, s94, v164\n\t"
        "s_waitcnt lgkmcnt(0)\n\t"
        "global_load_lds_dwordx4 v88, %[x]\n\t"
        "v_pk_mul_f16 v87, v87, v71\n\t"
        "v_cvt_pk_f16_f32 v97, v98, v99\n\t"
        "v_rcp_f16_sdwa v96, v96 dst_sel:WORD_1 dst_unused:UNUSED_PRESERVE src0_sel:WORD_1\n\t"
        "v_pk_add_f16 v97, v97, s96\n\t"
        "v_exp_f32 v100, v100\n\t"
        "v_mfma_f32_32x32x16_f16 v[0:15], v[68:71], v[180:183], v[0:15]\n\t"
        "ds_read_b128 v[92:95], v174 offset:512\n\t"
        "s_add_i32 m0, %[ring], 41984\n\t"
        "v_mad_u32_u16 v89, v89, s94, v165\n\t"
        "global_load_lds_dwordx4 v89, %[x]\n\t"
        "v_cvt_pk_f16_f32 v112, v112, v113\n\t"
        "v_rcp_f16_sdwa v97, v97 dst_sel:WORD_0 dst_unused:UNUSED_PRESERVE src0_sel:WORD_0\n\t"
        "v_pk_mul_f16 v112, v112, v96\n\t"
        "v_exp_f32 v101, v101\n\t"
        "v_mfma_f32_32x32x16_f16 v[32:47], v[84:87], v[180:183], v[32:47]\n\t"
        "s_add_i32 m0, %[ring], 43008\n\t"
        "v_mad_u32_u16 v90, v90, s94, v164\n\t"
        "global_load_lds_dwordx4 v90, %[x]\n\t"
        "v_cvt_pk_f16_f32 v98, v100, v101\n\t"
        "v_rcp_f16_sdwa v97, v97 dst_sel:WORD_1 dst_unused:UNUSED_PRESERVE src0_sel:WORD_1\n\t"
        "v_pk_add_f16 v98, v98, s96\n\t"
        "v_exp_f32 v102, v102\n\t"
        "v_mfma_f32_32x32x16_f16 v[64:79], v[184:187], v[128:131], 0\n\t"
        "s_add_i32 m0, %[ring], 44032\n\t"
        "v_mad_u32_u16 v91, v91, s94, v165\n\t"
        "global_load_lds_dwordx4 v91, %[x]\n\t"
        "v_cvt_pk_f16_f32 v113, v114, v115\n\t"
        "v_rcp_f16_sdwa v98, v98 dst_sel:WORD_0 dst_unused:UNUSED_PRESERVE src0_sel:WORD_0\n\t"
        "v_pk_mul_f16 v113, v113, v97\n\t"
        "v_exp_f32 v103, v103\n\t"
        "v_mfma_f32_32x32x16_f16 v[64:79], v[188:191], v[136:139], v[64:79]\n\t"
        "ds_read_b128 v[80:83], v174 offset:2560\n\t"
        "v_cvt_pk_f16_f32 v99, v102, v103\n\t"
        "v_rcp_f16_sdwa v98, v98 dst_sel:WORD_1 dst_unused:UNUSED_PRESERVE src0_sel:WORD_1\n\t"
        "v_pk_add_f16 v99, v99, s96\n\t"
        "v_exp_f32 v104, v104\n\t"
        "v_mfma_f32_32x32x16_bf16 v[64:79], v[192:195], v[196:199], v[64:79]\n\t"
        "v_cvt_pk_f16_f32 v114, v116, v117\n\t"
        "v_rcp_f16_sdwa v99, v99 dst_sel:WORD_0 dst_unused:UNUSED_PRESERVE src0_sel:WORD_0\n\t"
        "v_pk_mul_f16 v114, v114, v98\n\t"
        "v_exp_f32 v105, v105\n\t"
        "s_waitcnt lgkmcnt(1)\n\t"
        "v_mfma_f32_32x32x16_bf16 v[64:79], v[144:147], v[92:95], v[64:79]\n\t"
        "ds_read_b128 v[84:87], v174 offset:8704\n\t"
        "v_cvt_pk_f16_f32 v100, v104, v105\n\t"
        "v_rcp_f16_sdwa v99, v99 dst_sel:WORD_1 dst_unused:UNUSED_PRESERVE src0_sel:WORD_1\n\t"
        "v_pk_add_f16 v100, v100, s96\n\t"
        "v_exp_f32 v106, v106\n\t"
        "v_mfma_f32_32x32x16_bf16 v[64:79], v[148:151], v[200:203], v[64:79]\n\t"
        "v_cvt_pk_f16_f32 v115, v118, v119\n\t"
        "v_rcp_f16_sdwa v100, v100 dst_sel:WORD_0 dst_unused:UNUSED_PRESERVE src0_sel:WORD_0\n\t"
        "v_pk_mul_f16 v115, v115, v99\n\t"
        "v_exp_f32 v107, v107\n\t"
        "s_waitcnt lgkmcnt(1)\n\t"
        "v_mfma_f32_32x32x16_bf16 v[64:79], v[152:155], v[80:83], v[64:79]\n\t"
        "v_cvt_pk_f16_f32 v101, v106, v107\n\t"
        "v_rcp_f16_sdwa v100, v100 dst_sel:WORD_1 dst_unused:UNUSED_PRESERVE src0_sel:WORD_1\n\t"
        "v_pk_add_f16 v101, v101, s96\n\t"
        "v_exp_f32 v108, v108\n\t"
        "v_mfma_f32_32x32x16_bf16 v[64:79], v[156:159], v[204:207], v[64:79]\n\t"
        "v_cvt_pk_f16_f32 v116, v120, v121\n\t"
        "v_rcp_f16_sdwa v101, v101 dst_sel:WORD_0 dst_unused:UNUSED_PRESERVE src0_sel:WORD_0\n\t"
        "v_pk_mul_f16 v116, v116, v100\n\t"
        "v_exp_f32 v109, v109\n\t"
        "s_waitcnt lgkmcnt(0)\n\t"
        "v_mfma_f32_32x32x16_bf16 v[80:95], v[144:147], v[84:87], 0\n\t"
        "v_cvt_pk_f16_f32 v102, v108, v109\n\t"
        "v_rcp_f16_sdwa v101, v101 dst_sel:WORD_1 dst_unused:UNUSED_PRESERVE src0_sel:WORD_1\n\t"
        "v_pk_add_f16 v102, v102, s96\n\t"
        "v_exp_f32 v110, v110\n\t"
        "v_mfma_f32_32x32x16_bf16 v[80:95], v[148:151], v[208:211], v[80:95]\n\t"
        "v_cvt_pk_f16_f32 v117, v122, v123\n\t"
        "v_rcp_f16_sdwa v102, v102 dst_sel:WORD_0 dst_unused:UNUSED_PRESERVE src0_sel:WORD_0\n\t"
        "v_pk_mul_f16 v117, v117, v101\n\t"
        "v_exp_f32 v111, v111\n\t"
        "v_mfma_f32_32x32x16_bf16 v[80:95], v[152:155], v[212:215], v[80:95]\n\t"
        "v_cvt_pk_f16_f32 v103, v110, v111\n\t"
        "v_rcp_f16_sdwa v102, v102 dst_sel:WORD_1 dst_unused:UNUSED_PRESERVE src0_sel:WORD_1\n\t"
        "v_pk_add_f16 v103, v103, s96\n\t"
        "v_exp_f32 v64, v64\n\t"
        "v_exp_f32 v65, v65\n\t"
        "v_mfma_f32_32x32x16_bf16 v[80:95], v[156:159], v[216:219], v[80:95]\n\t"
        "v_cvt_pk_f16_f32 v118, v124, v125\n\t"
        "v_rcp_f16_sdwa v103, v103 dst_sel:WORD_0 dst_unused:UNUSED_PRESERVE src0_sel:WORD_0\n\t"
        "v_pk_mul_f16 v118, v118, v102\n\t"
        "v_exp_f32 v66, v66\n\t"
        "v_cvt_pk_f16_f32 v64, v64, v65\n\t"
        "v_mfma_f32_32x32x16_f16 v[16:31], v[96:99], v[176:179], v[16:31]\n\t"
        "s_setprio 0\n\t"
        "ds_read_b128 v[120:123], v160 offset:128\n\t"
        "ds_read_u16 v124, v161 offset:256\n\t"
        "v_rcp_f16_sdwa v103, v103 dst_sel:WORD_1 dst_unused:UNUSED_PRESERVE src0_sel:WORD_1\n\t"
        "v_cvt_pk_f16_f32 v119, v126, v127\n\t"
        "v_pk_add_f16 v64, v64, s96\n\t"
        "v_exp_f32 v67, v67\n\t"
        "v_rcp_f16_sdwa v64, v64 dst_sel:WORD_0 dst_unused:UNUSED_PRESERVE src0_sel:WORD_0\n\t"
        "v_mfma_f32_32x32x16_f16 v[48:63], v[112:115], v[176:179], v[48:63]\n\t"
        "v_pk_mul_f16 v119, v119, v103\n\t"
        "v_cvt_pk_f16_f32 v65, v66, v67\n\t"
        "v_rcp_f16_sdwa v64, v64 dst_sel:WORD_1 dst_unused:UNUSED_PRESERVE src0_sel:WORD_1\n\t"
        "v_pk_add_f16 v65, v65, s96\n\t"
        "v_exp_f32 v68, v68\n\t"
        "v_mfma_f32_32x32x16_f16 v[16:31], v[100:103], v[180:183], v[16:31]\n\t"
        "v_cvt_pk_f16_f32 v80, v80, v81\n\t"
        "v_rcp_f16_sdwa v65, v65 dst_sel:WORD_0 dst_unused:UNUSED_PRESERVE src0_sel:WORD_0\n\t"
        "v_pk_mul_f16 v80, v80, v64\n\t"
        "v_exp_f32 v69, v69\n\t"
        "v_mfma_f32_32x32x16_f16 v[48:63], v[116:119], v[180:183], v[48:63]\n\t"
        "v_cvt_pk_f16_f32 v66, v68, v69\n\t"
        "v_rcp_f16_sdwa v65, v65 dst_sel:WORD_1 dst_unused:UNUSED_PRESERVE src0_sel:WORD_1\n\t"
        "v_pk_add_f16 v66, v66, s96\n\t"
        "v_exp_f32 v70, v70\n\t"
        "v_mfma_f32_32x32x16_f16 v[96:111], v[184:187], v[132:135], 0\n\t"
        "s_waitcnt lgkmcnt(1)\n\t"
        "v_mad_u32_u16 v120, v120, s95, v167 op_sel:[1,0,0,0]\n\t"
        "ds_read_b64_tr_b16 v[176:177], v120\n\t"
        "v_mad_u32_u16 v121, v121, s95, v167 op_sel:[1,0,0,0]\n\t"
        "ds_read_b64_tr_b16 v[178:179], v121\n\t"
        "v_mad_u32_u16 v122, v122, s95, v167 op_sel:[1,0,0,0]\n\t"
        "ds_read_b64_tr_b16 v[180:181], v122\n\t"
        "v_mad_u32_u16 v123, v123, s95, v167 op_sel:[1,0,0,0]\n\t"
        "ds_read_b64_tr_b16 v[182:183], v123\n\t"
        "v_cvt_pk_f16_f32 v81, v82, v83\n\t"
        "v_rcp_f16_sdwa v66, v66 dst_sel:WORD_0 dst_unused:UNUSED_PRESERVE src0_sel:WORD_0\n\t"
        "v_pk_mul_f16 v81, v81, v65\n\t"
        "v_exp_f32 v71, v71\n\t"
        "v_mfma_f32_32x32x16_f16 v[96:111], v[188:191], v[140:143], v[96:111]\n\t"
        "s_waitcnt lgkmcnt(4)\n\t"
        "v_mad_u32_u24 v124, v124, s95, v168\n\t"
        "ds_read_b128 v[184:187], v124\n\t"
        "ds_read_b128 v[188:191], v124 offset:16\n\t"
        "v_cvt_pk_f16_f32 v67, v70, v71\n\t"
        "v_rcp_f16_sdwa v66, v66 dst_sel:WORD_1 dst_unused:UNUSED_PRESERVE src0_sel:WORD_1\n\t"
        "v_pk_add_f16 v67, v67, s96\n\t"
        "v_exp_f32 v72, v72\n\t"
        "v_mfma_f32_32x32x16_bf16 v[96:111], v[192:195], v[220:223], v[96:111]\n\t"
        "s_waitcnt vmcnt(9)\n\t"
        "ds_read_b128 v[192:195], v173\n\t"
        "ds_read_b32 v125, v163 offset:512\n\t"
        "v_cvt_pk_f16_f32 v82, v84, v85\n\t"
        "v_rcp_f16_sdwa v67, v67 dst_sel:WORD_0 dst_unused:UNUSED_PRESERVE src0_sel:WORD_0\n\t"
        "v_pk_mul_f16 v82, v82, v66\n\t"
        "v_exp_f32 v73, v73\n\t"
        "v_mfma_f32_32x32x16_bf16 v[96:111], v[144:147], v[224:227], v[96:111]\n\t"
        "v_cvt_pk_f16_f32 v68, v72, v73\n\t"
        "v_rcp_f16_sdwa v67, v67 dst_sel:WORD_1 dst_unused:UNUSED_PRESERVE src0_sel:WORD_1\n\t"
        "v_pk_add_f16 v68, v68, s96\n\t"
        "v_exp_f32 v74, v74\n\t"
        "v_mfma_f32_32x32x16_bf16 v[96:111], v[148:151], v[228:231], v[96:111]\n\t"
        "v_cvt_pk_f16_f32 v83, v86, v87\n\t"
        "v_rcp_f16_sdwa v68, v68 dst_sel:WORD_0 dst_unused:UNUSED_PRESERVE src0_sel:WORD_0\n\t"
        "v_pk_mul_f16 v83, v83, v67\n\t"
        "v_exp_f32 v75, v75\n\t"
        "v_mfma_f32_32x32x16_bf16 v[96:111], v[152:155], v[232:235], v[96:111]\n\t"
        "s_add_i32 m0, %[ring], 4096\n\t"
        "s_waitcnt lgkmcnt(0)\n\t"
        "v_add_u32 v125, v125, v166\n\t"
        "global_load_lds_dwordx4 v125, %[es]\n\t"
        "v_cvt_pk_f16_f32 v69, v74, v75\n\t"
        "v_rcp_f16_sdwa v68, v68 dst_sel:WORD_1 dst_unused:UNUSED_PRESERVE src0_sel:WORD_1\n\t"
        "v_pk_add_f16 v69, v69, s96\n\t"
        "v_exp_f32 v76, v76\n\t"
        "v_mfma_f32_32x32x16_bf16 v[96:111], v[156:159], v[236:239], v[96:111]\n\t"
        "v_cvt_pk_f16_f32 v84, v88, v89\n\t"
        "v_rcp_f16_sdwa v69, v69 dst_sel:WORD_0 dst_unused:UNUSED_PRESERVE src0_sel:WORD_0\n\t"
        "v_pk_mul_f16 v84, v84, v68\n\t"
        "v_exp_f32 v77, v77\n\t"
        "v_mfma_f32_32x32x16_bf16 v[112:127], v[144:147], v[240:243], 0\n\t"
        "s_waitcnt vmcnt(6)\n\t"
        "ds_read_b128 v[144:147], v169\n\t"
        "v_cvt_pk_f16_f32 v70, v76, v77\n\t"
        "v_rcp_f16_sdwa v69, v69 dst_sel:WORD_1 dst_unused:UNUSED_PRESERVE src0_sel:WORD_1\n\t"
        "v_pk_add_f16 v70, v70, s96\n\t"
        "v_exp_f32 v78, v78\n\t"
        "v_mfma_f32_32x32x16_bf16 v[112:127], v[148:151], v[244:247], v[112:127]\n\t"
        "ds_read_b128 v[148:151], v170\n\t"
        "s_sub_u32 %[nt], %[nt], 1\n\t"
        "v_cvt_pk_f16_f32 v85, v90, v91\n\t"
        "v_rcp_f16_sdwa v70, v70 dst_sel:WORD_0 dst_unused:UNUSED_PRESERVE src0_sel:WORD_0\n\t"
        "v_pk_mul_f16 v85, v85, v69\n\t"
        "v_exp_f32 v79, v79\n\t"
        "v_mfma_f32_32x32x16_bf16 v[112:127], v[152:155], v[248:251], v[112:127]\n\t"
        "ds_read_b128 v[152:155], v171\n\t"
        "ds_read_b128 v[88:91], v162 offset:512\n\t"
        "v_cvt_pk_f16_f32 v71, v78, v79\n\t"
        "v_rcp_f16_sdwa v70, v70 dst_sel:WORD_1 dst_unused:UNUSED_PRESERVE src0_sel:WORD_1\n\t"
        "v_pk_add_f16 v71, v71, s96\n\t"
        "v_exp_f32 v96, v96\n\t"
        "v_exp_f32 v97, v97\n\t"
        "v_mfma_f32_32x32x16_bf16 v[112:127], v[156:159], v[252:255], v[112:127]\n\t"
        "ds_read_b128 v[156:159], v172\n\t"
        "v_cvt_pk_f16_f32 v86, v92, v93\n\t"
        "v_rcp_f16_sdwa v71, v71 dst_sel:WORD_0 dst_unused:UNUSED_PRESERVE src0_sel:WORD_0\n\t"
        "v_pk_mul_f16 v86, v86, v70\n\t"
        "v_exp_f32 v98, v98\n\t"
        "v_cvt_pk_f16_f32 v96, v96, v97\n\t"
        "s_cbranch_scc1 .Lrl_%=\n\t"
        "v_mfma_f32_32x32x16_f16 v[0:15], v[64:67], v[176:179], v[0:15]\n\t"
        "v_rcp_f16_sdwa v71, v71 dst_sel:WORD_1 dst_unused:UNUSED_PRESERVE src0_sel:WORD_1\n\t"
        "v_cvt_pk_f16_f32 v87, v94, v95\n\t"
        "v_pk_add_f16 v96, v96, s96\n\t"
        "v_exp_f32 v99, v99\n\t"
        "v_rcp_f16_sdwa v96, v96 dst_sel:WORD_0 dst_unused:UNUSED_PRESERVE src0_sel:WORD_0\n\t"
        "v_mfma_f32_32x32x16_f16 v[32:47], v[80:83], v[176:179], v[32:47]\n\t"
        "s_mov_b32 m0, %[ring]\n\t"
        "s_waitcnt lgkmcnt(1)\n\t"
        "v_mad_u32_u16 v88, v88, s94, v164\n\t"
        "s_waitcnt lgkmcnt(0)\n\t"
        "global_load_lds_dwordx4 v88, %[x]\n\t"
        "v_pk_mul_f16 v87, v87, v71\n\t"
        "v_cvt_pk_f16_f32 v97, v98, v99\n\t"
        "v_rcp_f16_sdwa v96, v96 dst_sel:WORD_1 dst_unused:UNUSED_PRESERVE src0_sel:WORD_1\n\t"
        "v_pk_add_f16 v97, v97, s96\n\t"
        "v_exp_f32 v100, v100\n\t"
        "v_mfma_f32_32x32x16_f16 v[0:15], v[68:71], v[180:183], v[0:15]\n\t"
        "ds_read_b128 v[92:95], v174 offset:512\n\t"
        "s_add_i32 m0, %[ring], 1024\n\t"
        "v_mad_u32_u16 v89, v89, s94, v165\n\t"
        "global_load_lds_dwordx4 v89, %[x]\n\t"
        "v_cvt_pk_f16_f32 v112, v112, v113\n\t"
        "v_rcp_f16_sdwa v97, v97 dst_sel:WORD_0 dst_unused:UNUSED_PRESERVE src0_sel:WORD_0\n\t"
        "v_pk_mul_f16 v112, v112, v96\n\t"
        "v_exp_f32 v101, v101\n\t"
        "v_mfma_f32_32x32x16_f16 v[32:47], v[84:87], v[180:183], v[32:47]\n\t"
        "s_add_i32 m0, %[ring], 2048\n\t"
        "v_mad_u32_u16 v90, v90, s94, v164\n\t"
        "global_load_lds_dwordx4 v90, %[x]\n\t"
        "v_cvt_pk_f16_f32 v98, v100, v101\n\t"
        "v_rcp_f16_sdwa v97, v97 dst_sel:WORD_1 dst_unused:UNUSED_PRESERVE src0_sel:WORD_1\n\t"
        "v_pk_add_f16 v98, v98, s96\n\t"
        "v_exp_f32 v102, v102\n\t"
        "v_mfma_f32_32x32x16_f16 v[64:79], v[184:187], v[128:131], 0\n\t"
        "s_add_i32 m0, %[ring], 3072\n\t"
        "v_mad_u32_u16 v91, v91, s94, v165\n\t"
        "global_load_lds_dwordx4 v91, %[x]\n\t"
        "v_cvt_pk_f16_f32 v113, v114, v115\n\t"
        "v_rcp_f16_sdwa v98, v98 dst_sel:WORD_0 dst_unused:UNUSED_PRESERVE src0_sel:WORD_0\n\t"
        "v_pk_mul_f16 v113, v113, v97\n\t"
        "v_exp_f32 v103, v103\n\t"
        "v_mfma_f32_32x32x16_f16 v[64:79], v[188:191], v[136:139], v[64:79]\n\t"
        "ds_read_b128 v[80:83], v174 offset:2560\n\t"
        "v_cvt_pk_f16_f32 v99, v102, v103\n\t"
        "v_rcp_f16_sdwa v98, v98 dst_sel:WORD_1 dst_unused:UNUSED_PRESERVE src0_sel:WORD_1\n\t"
        "v_pk_add_f16 v99, v99, s96\n\t"
        "v_exp_f32 v104, v104\n\t"
        "v_mfma_f32_32x32x16_bf16 v[64:79], v[192:195], v[196:199], v[64:79]\n\t"
        "v_cvt_pk_f16_f32 v114, v116, v117\n\t"
        "v_rcp_f16_sdwa v99, v99 dst_sel:WORD_0 dst_unused:UNUSED_PRESERVE src0_sel:WORD_0\n\t"
        "v_pk_mul_f16 v114, v114, v98\n\t"
        "v_exp_f32 v105, v105\n\t"
        "s_waitcnt lgkmcnt(1)\n\t"
        "v_mfma_f32_32x32x16_bf16 v[64:79], v[144:147], v[92:95], v[64:79]\n\t"
        "ds_read_b128 v[84:87], v174 offset:8704\n\t"
        "v_cvt_pk_f16_f32 v100, v104, v105\n\t"
        "v_rcp_f16_sdwa v99, v99 dst_sel:WORD_1 dst_unused:UNUSED_PRESERVE src0_sel:WORD_1\n\t"
        "v_pk_add_f16 v100, v100, s96\n\t"
        "v_exp_f32 v106, v106\n\t"
        "v_mfma_f32_32x32x16_bf16 v[64:79], v[148:151], v[200:203], v[64:79]\n\t"
        "v_cvt_pk_f16_f32 v115, v118, v119\n\t"
        "v_rcp_f16_sdwa v100, v100 dst_sel:WORD_0 dst_unused:UNUSED_PRESERVE src0_sel:WORD_0\n\t"
        "v_pk_mul_f16 v115, v115, v99\n\t"
        "v_exp_f32 v107, v107\n\t"
        "s_waitcnt lgkmcnt(1)\n\t"
        "v_mfma_f32_32x32x16_bf16 v[64:79], v[152:155], v[80:83], v[64:79]\n\t"
        "v_cvt_pk_f16_f32 v101, v106, v107\n\t"
        "v_rcp_f16_sdwa v100, v100 dst_sel:WORD_1 dst_unused:UNUSED_PRESERVE src0_sel:WORD_1\n\t"
        "v_pk_add_f16 v101, v101, s96\n\t"
        "v_exp_f32 v108, v108\n\t"
        "v_mfma_f32_32x32x16_bf16 v[64:79], v[156:159], v[204:207], v[64:79]\n\t"
        "v_cvt_pk_f16_f32 v116, v120, v121\n\t"
        "v_rcp_f16_sdwa v101, v101 dst_sel:WORD_0 dst_unused:UNUSED_PRESERVE src0_sel:WORD_0\n\t"
        "v_pk_mul_f16 v116, v116, v100\n\t"
        "v_exp_f32 v109, v109\n\t"
        "s_waitcnt lgkmcnt(0)\n\t"
        "v_mfma_f32_32x32x16_bf16 v[80:95], v[144:147], v[84:87], 0\n\t"
        "v_add_u32 v160, 0x100, v160\n\t"
        "v_cvt_pk_f16_f32 v102, v108, v109\n\t"
        "v_rcp_f16_sdwa v101, v101 dst_sel:WORD_1 dst_unused:UNUSED_PRESERVE src0_sel:WORD_1\n\t"
        "v_pk_add_f16 v102, v102, s96\n\t"
        "v_exp_f32 v110, v110\n\t"
        "v_mfma_f32_32x32x16_bf16 v[80:95], v[148:151], v[208:211], v[80:95]\n\t"
        "v_add_u32 v161, 0x100, v161\n\t"
        "v_cvt_pk_f16_f32 v117, v122, v123\n\t"
        "v_rcp_f16_sdwa v102, v102 dst_sel:WORD_0 dst_unused:UNUSED_PRESERVE src0_sel:WORD_0\n\t"
        "v_pk_mul_f16 v117, v117, v101\n\t"
        "v_exp_f32 v111, v111\n\t"
        "v_mfma_f32_32x32x16_bf16 v[80:95], v[152:155], v[212:215], v[80:95]\n\t"
        "v_add_u32 v162, 0x100, v162\n\t"
        "v_cvt_pk_f16_f32 v103, v110, v111\n\t"
        "v_rcp_f16_sdwa v102, v102 dst_sel:WORD_1 dst_unused:UNUSED_PRESERVE src0_sel:WORD_1\n\t"
        "v_pk_add_f16 v103, v103, s96\n\t"
        "v_exp_f32 v64, v64\n\t"
        "v_exp_f32 v65, v65\n\t"
        "v_mfma_f32_32x32x16_bf16 v[80:95], v[156:159], v[216:219], v[80:95]\n\t"
        "v_add_u32 v163, 0x100, v163\n\t"
        "v_cvt_pk_f16_f32 v118, v124, v125\n\t"
        "v_rcp_f16_sdwa v103, v103 dst_sel:WORD_0 dst_unused:UNUSED_PRESERVE src0_sel:WORD_0\n\t"
        "v_pk_mul_f16 v118, v118, v102\n\t"
        "v_exp_f32 v66, v66\n\t"
        "v_cvt_pk_f16_f32 v64, v64, v65\n\t"
        "s_branch .Lrt_%=\n\t"
        ".Lrl_%=:\n\t"
        "v_mfma_f32_32x32x16_f16 v[0:15], v[64:67], v[176:179], v[0:15]\n\t"
        "ds_read_b64_tr_b16 v[184:185], v175\n\t"
        "ds_read_b64_tr_b16 v[186:187], v175 offset:512\n\t"
        "ds_read_b64_tr_b16 v[188:189], v175 offset:1024\n\t"
        "ds_read_b64_tr_b16 v[190:191], v175 offset:1536\n\t"
        "v_rcp_f16_sdwa v71, v71 dst_sel:WORD_1 dst_unused:UNUSED_PRESERVE src0_sel:WORD_1\n\t"
        "v_cvt_pk_f16_f32 v87, v94, v95\n\t"
        "v_pk_add_f16 v96, v96, s96\n\t"
        "v_exp_f32 v99, v99\n\t"
        "v_rcp_f16_sdwa v96, v96 dst_sel:WORD_0 dst_unused:UNUSED_PRESERVE src0_sel:WORD_0\n\t"
        "v_mfma_f32_32x32x16_f16 v[32:47], v[80:83], v[176:179], v[32:47]\n\t"
        "ds_read_b64_tr_b16 v[192:193], v175 offset:2048\n\t"
        "ds_read_b64_tr_b16 v[194:195], v175 offset:2560\n\t"
        "ds_read_b64_tr_b16 v[196:197], v175 offset:3072\n\t"
        "ds_read_b64_tr_b16 v[198:199], v175 offset:3584\n\t"
        "v_pk_mul_f16 v87, v87, v71\n\t"
        "v_cvt_pk_f16_f32 v97, v98, v99\n\t"
        "v_rcp_f16_sdwa v96, v96 dst_sel:WORD_1 dst_unused:UNUSED_PRESERVE src0_sel:WORD_1\n\t"
        "v_pk_add_f16 v97, v97, s96\n\t"
        "v_exp_f32 v100, v100\n\t"
        "v_mfma_f32_32x32x16_f16 v[0:15], v[68:71], v[180:183], v[0:15]\n\t"
        "ds_read_b64_tr_b16 v[200:201], v175 offset:32\n\t"
        "ds_read_b64_tr_b16 v[202:203], v175 offset:544\n\t"
        "ds_read_b64_tr_b16 v[204:205], v175 offset:1056\n\t"
        "ds_read_b64_tr_b16 v[206:207], v175 offset:1568\n\t"
        "s_waitcnt lgkmcnt(14)\n\t"
        "v_lshlrev_b32 v216, 16, v144\n\t"
        "v_and_b32 v217, 0xffff0000, v144\n\t"
        "v_cvt_pk_f16_f32 v112, v112, v113\n\t"
        "v_rcp_f16_sdwa v97, v97 dst_sel:WORD_0 dst_unused:UNUSED_PRESERVE src0_sel:WORD_0\n\t"
        "v_pk_mul_f16 v112, v112, v96\n\t"
        "v_exp_f32 v101, v101\n\t"
        "v_mfma_f32_32x32x16_f16 v[32:47], v[84:87], v[180:183], v[32:47]\n\t"
        "ds_read_b64_tr_b16 v[208:209], v175 offset:2080\n\t"
        "ds_read_b64_tr_b16 v[210:211], v175 offset:2592\n\t"
        "ds_read_b64_tr_b16 v[212:213], v175 offset:3104\n\t"
        "ds_read_b64_tr_b16 v[214:215], v175 offset:3616\n\t"
        "v_lshlrev_b32 v218, 16, v145\n\t"
        "v_and_b32 v219, 0xffff0000, v145\n\t"
        "v_cvt_pk_f16_f32 v98, v100, v101\n\t"
        "v_rcp_f16_sdwa v97, v97 dst_sel:WORD_1 dst_unused:UNUSED_PRESERVE src0_sel:WORD_1\n\t"
        "v_pk_add_f16 v98, v98, s96\n\t"
        "v_exp_f32 v102, v102\n\t"
        "v_lshlrev_b32 v220, 16, v146\n\t"
        "v_and_b32 v221, 0xffff0000, v146\n\t"
        "v_lshlrev_b32 v222, 16, v147\n\t"
        "v_and_b32 v223, 0xffff0000, v147\n\t"
        "s_waitcnt lgkmcnt(14)\n\t"
        "v_lshlrev_b32 v224, 16, v148\n\t"
        "v_and_b32 v225, 0xffff0000, v148\n\t"
        "v_cvt_pk_f16_f32 v113, v114, v115\n\t"
        "v_rcp_f16_sdwa v98, v98 dst_sel:WORD_0 dst_unused:UNUSED_PRESERVE src0_sel:WORD_0\n\t"
        "v_pk_mul_f16 v113, v113, v97\n\t"
        "v_exp_f32 v103, v103\n\t"
        "v_lshlrev_b32 v226, 16, v149\n\t"
        "v_and_b32 v227, 0xffff0000, v149\n\t"
        "v_lshlrev_b32 v228, 16, v150\n\t"
        "v_and_b32 v229, 0xffff0000, v150\n\t"
        "v_lshlrev_b32 v230, 16, v151\n\t"
        "v_and_b32 v231, 0xffff0000, v151\n\t"
        "v_cvt_pk_f16_f32 v99, v102, v103\n\t"
        "v_rcp_f16_sdwa v98, v98 dst_sel:WORD_1 dst_unused:UNUSED_PRESERVE src0_sel:WORD_1\n\t"
        "v_pk_add_f16 v99, v99, s96\n\t"
        "v_exp_f32 v104, v104\n\t"
        "s_waitcnt lgkmcnt(14)\n\t"
        "v_mfma_f32_32x32x16_bf16 v[64:79], v[184:187], v[144:147], v[216:231]\n\t"
        "v_lshlrev_b32 v232, 16, v152\n\t"
        "v_and_b32 v233, 0xffff0000, v152\n\t"
        "v_cvt_pk_f16_f32 v114, v116, v117\n\t"
        "v_rcp_f16_sdwa v99, v99 dst_sel:WORD_0 dst_unused:UNUSED_PRESERVE src0_sel:WORD_0\n\t"
        "v_pk_mul_f16 v114, v114, v98\n\t"
        "v_exp_f32 v105, v105\n\t"
        "s_waitcnt lgkmcnt(12)\n\t"
        "v_mfma_f32_32x32x16_bf16 v[64:79], v[188:191], v[148:151], v[64:79]\n\t"
        "v_lshlrev_b32 v234, 16, v153\n\t"
        "v_and_b32 v235, 0xffff0000, v153\n\t"
        "v_cvt_pk_f16_f32 v100, v104, v105\n\t"
        "v_rcp_f16_sdwa v99, v99 dst_sel:WORD_1 dst_unused:UNUSED_PRESERVE src0_sel:WORD_1\n\t"
        "v_pk_add_f16 v100, v100, s96\n\t"
        "v_exp_f32 v106, v106\n\t"
        "s_waitcnt lgkmcnt(10)\n\t"
        "v_mfma_f32_32x32x16_bf16 v[64:79], v[192:195], v[152:155], v[64:79]\n\t"
        "v_lshlrev_b32 v236, 16, v154\n\t"
        "v_and_b32 v237, 0xffff0000, v154\n\t"
        "v_cvt_pk_f16_f32 v115, v118, v119\n\t"
        "v_rcp_f16_sdwa v100, v100 dst_sel:WORD_0 dst_unused:UNUSED_PRESERVE src0_sel:WORD_0\n\t"
        "v_pk_mul_f16 v115, v115, v99\n\t"
        "v_exp_f32 v107, v107\n\t"
        "s_waitcnt lgkmcnt(8)\n\t"
        "v_mfma_f32_32x32x16_bf16 v[64:79], v[196:199], v[156:159], v[64:79]\n\t"
        "v_lshlrev_b32 v238, 16, v155\n\t"
        "v_and_b32 v239, 0xffff0000, v155\n\t"
        "v_cvt_pk_f16_f32 v101, v106, v107\n\t"
        "v_rcp_f16_sdwa v100, v100 dst_sel:WORD_1 dst_unused:UNUSED_PRESERVE src0_sel:WORD_1\n\t"
        "v_pk_add_f16 v101, v101, s96\n\t"
        "v_exp_f32 v108, v108\n\t"
        "v_lshlrev_b32 v240, 16, v156\n\t"
        "v_and_b32 v241, 0xffff0000, v156\n\t"
        "v_lshlrev_b32 v242, 16, v157\n\t"
        "v_and_b32 v243, 0xffff0000, v157\n\t"
        "v_lshlrev_b32 v244, 16, v158\n\t"
        "v_and_b32 v245, 0xffff0000, v158\n\t"
        "v_lshlrev_b32 v246, 16, v159\n\t"
        "v_and_b32 v247, 0xffff0000, v159\n\t"
        "v_cvt_pk_f16_f32 v116, v120, v121\n\t"
        "v_rcp_f16_sdwa v101, v101 dst_sel:WORD_0 dst_unused:UNUSED_PRESERVE src0_sel:WORD_0\n\t"
        "v_pk_mul_f16 v116, v116, v100\n\t"
        "v_exp_f32 v109, v109\n\t"
        "s_waitcnt lgkmcnt(6)\n\t"
        "v_mfma_f32_32x32x16_bf16 v[80:95], v[200:203], v[144:147], v[232:247]\n\t"
        "v_cvt_pk_f16_f32 v102, v108, v109\n\t"
        "v_rcp_f16_sdwa v101, v101 dst_sel:WORD_1 dst_unused:UNUSED_PRESERVE src0_sel:WORD_1\n\t"
        "v_pk_add_f16 v102, v102, s96\n\t"
        "v_exp_f32 v110, v110\n\t"
        "s_waitcnt lgkmcnt(4)\n\t"
        "v_mfma_f32_32x32x16_bf16 v[80:95], v[204:207], v[148:151], v[80:95]\n\t"
        "v_cvt_pk_f16_f32 v117, v122, v123\n\t"
        "v_rcp_f16_sdwa v102, v102 dst_sel:WORD_0 dst_unused:UNUSED_PRESERVE src0_sel:WORD_0\n\t"
        "v_pk_mul_f16 v117, v117, v101\n\t"
        "v_exp_f32 v111, v111\n\t"
        "s_waitcnt lgkmcnt(2)\n\t"
        "v_mfma_f32_32x32x16_bf16 v[80:95], v[208:211], v[152:155], v[80:95]\n\t"
        "v_cvt_pk_f16_f32 v103, v110, v111\n\t"
        "v_rcp_f16_sdwa v102, v102 dst_sel:WORD_1 dst_unused:UNUSED_PRESERVE src0_sel:WORD_1\n\t"
        "v_pk_add_f16 v103, v103, s96\n\t"
        "s_waitcnt lgkmcnt(0)\n\t"
        "v_mfma_f32_32x32x16_bf16 v[80:95], v[212:215], v[156:159], v[80:95]\n\t"
        "v_cvt_pk_f16_f32 v118, v124, v125\n\t"
        "v_rcp_f16_sdwa v103, v103 dst_sel:WORD_0 dst_unused:UNUSED_PRESERVE src0_sel:WORD_0\n\t"
        "v_pk_mul_f16 v118, v118, v102\n\t"
        "v_mfma_f32_32x32x16_f16 v[16:31], v[96:99], v[176:179], v[16:31]\n\t"
        "v_rcp_f16_sdwa v103, v103 dst_sel:WORD_1 dst_unused:UNUSED_PRESERVE src0_sel:WORD_1\n\t"
        "v_cvt_pk_f16_f32 v119, v126, v127\n\t"
        "v_mfma_f32_32x32x16_f16 v[48:63], v[112:115], v[176:179], v[48:63]\n\t"
        "v_pk_mul_f16 v119, v119, v103\n\t"
        "v_mfma_f32_32x32x16_f16 v[16:31], v[100:103], v[180:183], v[16:31]\n\t"
        "s_nop 0\n\t"
        "v_mfma_f32_32x32x16_f16 v[48:63], v[116:119], v[180:183], v[48:63]\n\t"
        : "+{v[0:15]}"(Y[0]), "+{v[16:31]}"(Y[1]), "+{v[32:47]}"(Y[2]), "+{v[48:63]}"(Y[3]), "={v[64:79]}"(U[0]),
          "={v[80:95]}"(U[1]), "={v[216:231]}"(X[0]), "={v[232:247]}"(X[1]), "+{v[160:175]}"(AD), [nt] "+s"(nt)
          , [t1] "=&s"(tm1), [c1] "=&s"(c1)
        : "{v[128:143]}"(XAf), [x] "s"(x), [es] "s"(ES), [ring] "s"(ring0), [wave] "s"(wave), [flag] "s"(mode),
          [cu] "n"(cu)
        :
          "memory", "scc", "m0", "s94", "s95", "s96", "v96", "v97", "v98", "v99", "v100", "v101", "v102", "v103",
          "v104", "v105", "v106", "v107", "v108", "v109", "v110", "v111", "v112", "v113", "v114", "v115", "v116",
          "v117", "v118", "v119", "v120", "v121", "v122", "v123", "v124", "v125", "v126", "v127", "v144", "v145",
          "v146", "v147", "v148", "v149", "v150", "v151", "v152", "v153", "v154", "v155", "v156", "v157", "v158",
          "v159", "v176", "v177", "v178", "v179", "v180", "v181", "v182", "v183", "v184", "v185", "v186", "v187",
          "v188", "v189", "v190", "v191", "v192", "v193", "v194", "v195", "v196", "v197", "v198", "v199", "v200",
          "v201", "v202", "v203", "v204", "v205", "v206", "v207", "v208", "v209", "v210", "v211", "v212", "v213",
          "v214", "v215", "v248", "v249", "v250", "v251", "v252", "v253", "v254", "v255");
  }
  SB();
  const long long tm2 = __builtin_amdgcn_s_memrealtime(); const long long c2 = __builtin_amdgcn_s_memtime();
  auto wide = [&]() __attribute__((always_inline)) {
    int l2;
    asm volatile("v_mbcnt_lo_u32_b32 %0, -1, 0\n\tv_mbcnt_hi_u32_b32 %0, -1, %0\n\ts_waitcnt vmcnt(0)" : "=v"(l2) :: "memory");
    AD[0] = la + (4 * (l2 >> 5) + ((l2 >> 2) & 3)) * 16;
    AD[1] = la + (((l2 & 7) << 2) | ((l2 >> 3) & 3)) * 4 + 2;
    AD[2] = la + (l2 >> 3) * 16;
    AD[3] = la + LARR + ((((l2 >> 1) & 7) << 2) | (l2 >> 4)) * 4;
    auto rd = [&](unsigned a) { return *(const LAS bf16x8*)(size_t)a; };
    auto one = [&](bf16x8 v) {
      const u32x4 t = __builtin_bit_cast(u32x4, v);
      return __builtin_bit_cast(bf16x8, t | (t >> 3));
    };
    bf16x8 xq[4];
    {
      f32x16 T[2] = {zero16, zero16};
#pragma unroll
      for (int k = 0; k < 4; ++k) {
        u32x4 t;
#pragma unroll
        for (int j = 0; j < 4; ++j) t[j] = pack_bf16(X[k >> 1][8 * (k & 1) + 2 * j], X[k >> 1][8 * (k & 1) + 2 * j + 1]);
#pragma unroll
        for (int ct = 0; ct < 2; ++ct)
          T[ct] = MFMA32(__builtin_bit_cast(bf16x8, t), rawfrag(AD[15] + (NBLOB - 18 + k) * 1024 + ct * 32), T[ct]);
      }
#pragma unroll
      for (int q = 0; q < 4; ++q) {
        u32x4 t;
#pragma unroll
        for (int j = 0; j < 4; ++j)
          t[j] = pack_bf16(T[q & 1][8 * (q >> 1) + 2 * j] * NLOG2E, T[q & 1][8 * (q >> 1) + 2 * j + 1] * NLOG2E);
        xq[q] = __builtin_bit_cast(bf16x8, t);
      }
    }
#pragma unroll
    for (int i = 0; i < 16; ++i) { Y[0][i] = 1e-6f * (1 << RA); Y[1][i] = 1e-6f * (1 << RA); Y[2][i] = 0.f; Y[3][i] = 0.f; }
#pragma unroll 1
    do {
      const int n = slow ? gen() : cnt;
      issue_gather(0, 0);
#pragma unroll 1
      for (int t = 0; t * 32 < n; ++t) {
        const unsigned ro = (t & 1) * ringd;
        asm volatile("s_waitcnt lgkmcnt(0)" ::: "memory");
        issue_gather(t + 1, ~t & 1);
        const u32x4 ss = ld128(AD[0] + t * 128);
        const unsigned st = *(const LAS unsigned short*)(size_t)(AD[1] + t * 128) * TPITCH + AD[8];
        s16x4 a[4];
#pragma unroll
        for (int q = 0; q < 4; ++q)
          a[q] = __builtin_amdgcn_ds_read_tr16_b64_v4i16((LAS s16x4*)(size_t)((ss[q] >> 16) * TPITCH + AD[7]));
        const bf16x8 S0 = one(__builtin_shufflevector(a[0], a[1], 0, 1, 2, 3, 4, 5, 6, 7));
        const bf16x8 S1 = one(__builtin_shufflevector(a[2], a[3], 0, 1, 2, 3, 4, 5, 6, 7));
        const bf16x8 T0 = one(rd(st)), T1 = one(rd(st + 16));
        asm volatile("s_waitcnt vmcnt(5)" ::: "memory");
#pragma unroll
        for (int ct = 0; ct < 2; ++ct) {
          const unsigned w = AD[14] + OFF_W + ct * 4096;
          f32x16 G = MFMA32(T0, xq[ct], zero16), V = zero16;
          G = MFMA32(T1, xq[2 + ct], G);
          G = MFMA32(rd(AD[13] + ro), rd(AD[14] + OFF_W + (16 + ct) * 1024), G);
#pragma unroll
          for (int k = 0; k < 4; ++k) {
            const bf16x8 as = rd(AD[9 + k] + ro);
            G = MFMA32(as, rd(w + k * 1024), G);
            V = MFMA32(as, rd(w + (8 + k) * 1024), V);
          }
          u32x4 pd[2], pn[2];
#pragma unroll
          for (int i = 0; i < 16; i += 2) {
            constexpr float u = 1.0f / (1 << RA);
            const float r0 = __builtin_amdgcn_rcpf(__builtin_fmaf(__builtin_amdgcn_exp2f(G[i]), u, u));
            const float r1 = __builtin_amdgcn_rcpf(__builtin_fmaf(__builtin_amdgcn_exp2f(G[i + 1]), u, u));
            pd[i >> 3][(i >> 1) & 3] = pack_bf16(r0, r1);
            pn[i >> 3][(i >> 1) & 3] = pack_bf16(r0 * V[i], r1 * V[i + 1]);
          }
          Y[ct] = MFMA32(__builtin_bit_cast(bf16x8, pd[0]), S0, Y[ct]);
          Y[ct] = MFMA32(__builtin_bit_cast(bf16x8, pd[1]), S1, Y[ct]);
          Y[2 + ct] = MFMA32(__builtin_bit_cast(bf16x8, pn[0]), S0, Y[2 + ct]);
          Y[2 + ct] = MFMA32(__builtin_bit_cast(bf16x8, pn[1]), S1, Y[2 + ct]);
          SB();
        }
      }
      asm volatile("s_waitcnt vmcnt(0) lgkmcnt(0)" ::: "memory");
    } while (slow && gs < GEND);
  };

  const unsigned ot = ring0 + (max(cnt - 1, 0) & 32 ? 0u : ringd);
  auto finish = [&](const bool check) __attribute__((always_inline)) {
    auto inv = [&](f32x16& y) {
      y *= VSC;
#pragma unroll
      for (int i = 0; i < 16; ++i) y[i] = __builtin_amdgcn_rcpf(y[i]);
    };
    inv(Y[0]);
    SB();
    int lane;
    asm volatile("v_mbcnt_lo_u32_b32 %0, -1, 0\n\tv_mbcnt_hi_u32_b32 %0, -1, %0" : "=v"(lane));
    const int h = lane >> 5;
    const unsigned nd = nodeof(cg + (lane & 31));
    const bool live = (nd * Mw + Cw) >> 16 == cg + (lane & 31);
    const unsigned lm = (unsigned)__ballot(live);
    const int nodes = __builtin_popcount(lm);
    const unsigned n0 = __builtin_amdgcn_readlane(nd, __builtin_ctz(lm | 0x80000000u));
    const int r = live ? nd - n0 : 31;
    SB();
    inv(Y[1]);
    unsigned ov[16];
#pragma unroll
    for (int p = 0; p < 4; ++p) {
      const int ct = p >> 1;
#pragma unroll
      for (int j = 0; j < 4; ++j) {
        const int i = 8 * (p & 1) + 2 * j;
        const f32x2 z = __builtin_elementwise_fma(f32x2{Y[2 + ct][i], Y[2 + ct][i + 1]}, f32x2{Y[ct][i], Y[ct][i + 1]},
                                                  f32x2{U[ct][i], U[ct][i + 1]});
        ov[4 * p + j] = pack_bf16(MAXN(z[0], X[ct][i]), MAXN(z[1], X[ct][i + 1]));
      }
      *(LAS u32x4*)(size_t)(ot + r * 128 + (((4 * h + p) ^ (r & 7)) * 16)) =
          u32x4{ov[4 * p], ov[4 * p + 1], ov[4 * p + 2], ov[4 * p + 3]};
    }
    u32x4 ow[4];
#pragma unroll
    for (int q = 0; q < 4; ++q)
      ow[q] = *(const LAS u32x4*)(size_t)(ot + (8 * q + (lane >> 3)) * 128 + (((lane & 7) ^ (lane >> 3)) * 16));
    asm volatile("v_cmpx_lt_i32 vcc, 0, %6\n\t"
                 "global_store_dwordx4 %0, %1, %5 sc1\n\t"
                 "v_cmpx_lt_i32 vcc, 8, %6\n\t"
                 "global_store_dwordx4 %0, %2, %5 offset:1024 sc1\n\t"
                 "v_cmpx_lt_i32 vcc, 16, %6\n\t"
                 "global_store_dwordx4 %0, %3, %5 offset:2048 sc1\n\t"
                 "v_cmpx_lt_i32 vcc, 24, %6\n\t"
                 "global_store_dwordx4 %0, %4, %5 offset:3072 sc1\n\t"
                 "s_mov_b64 exec, -1"
                 :: "v"((unsigned)n0 * 128u + lane * 16u), "v"(ow[0]), "v"(ow[1]), "v"(ow[2]), "v"(ow[3]), "s"(out),
                    "v"(nodes - (lane >> 3))
                 : "memory", "vcc");
    SB();
    if (!check) return false;
    constexpr float WLIM = 1 << (22 - RA + VK);
    float wm = MAXN(Y[0][0], Y[1][0]);
#pragma unroll
    for (int i = 1; i < 16; ++i) wm = MAXN(MAXN(wm, Y[0][i]), Y[1][i]);
    f16x2 om = __builtin_bit_cast(f16x2, ov[0]);
#pragma unroll
    for (int i = 1; i < 16; ++i) om = MAXN(om, __builtin_bit_cast(f16x2, ov[i]));
    const _Float16 o1 = MAXN(om[0], om[1]);
    const unsigned long long lv = lm * 0x100000001ull;
    unsigned long long bad = __ballot(o1 != o1) | (__ballot(o1 < __builtin_bit_cast(_Float16, (unsigned short)((127 - 9 + VK) << 7))) & lv);
    if (__builtin_expect((__ballot(wm > WLIM / LCAP) & lv) != 0ull, 0)) {
      LAS unsigned* dg = (LAS unsigned*)(size_t)ot;
      dg[lane & 31] = 0u;
#pragma unroll 1
      for (int i = lane; i < cnt; i += 64)
        __hip_atomic_fetch_add(dg + (*(const LAS unsigned short*)(size_t)(la + i * 4 + 2) & 31), 1u, __ATOMIC_RELAXED,
                               __HIP_MEMORY_SCOPE_WORKGROUP);
      bad |= __ballot(wm * (float)dg[lane & 31] > WLIM) & lv;
    }
    return slow || bad != 0ull;
  };
  if (__builtin_expect(finish(true), 0)) {
    wide();
    finish(false);
  }
  if (bk == 0 && wave == 7) asm volatile("global_store_dwordx2 %0, %1, %2 sc1" :: "v"(0u), "v"(ep), "s"(FL) : "memory");
  asm volatile("s_waitcnt vmcnt(0)" ::: "memory");
  long long* dbg = (long long*)FL + (127u << 17);
  { unsigned* xd_ = (unsigned*)(dbg + (4u << 20)); const int l_ = __builtin_amdgcn_mbcnt_hi(-1, __builtin_amdgcn_mbcnt_lo(-1, 0)); if (l_ < 16) xd_[gbin * 16 + l_] = *(LAS unsigned*)((LAS char*)smem + MAIN_LDS + wave * 64 + l_ * 4); }
  const long long tm3 = __builtin_amdgcn_s_memrealtime();
  const long long tm0 = tm3 - (long long)(unsigned)((unsigned)tm3 - tm0s);
  if (__builtin_amdgcn_mbcnt_hi(-1, __builtin_amdgcn_mbcnt_lo(-1, 0)) == 0) { const int g = gbin; if (wave == 0) (dbg - (16 << 20))[5000 + bk] = tm0; dbg[g*8+0]=tm0; dbg[g*8+1]=tm1; dbg[g*8+2]=tm2; dbg[g*8+3]=tm3; dbg[g*8+4]=(cnt+31)>>5; dbg[g*8+5]=c2-c1; dbg[g*8+6]=tmA-tm0; dbg[g*8+7]=tmB-tm0;
  }
}

__global__ void k_dbg(const long long* dbg, int ng) {
  __shared__ long long sh[4][256];
  const int t = threadIdx.x;
  const unsigned* q = (const unsigned*)(dbg + (4u << 20));
  long long pmin = 1ll << 62, sum = 0, mx = 0, cn = 0;
  const int md = QSEL / 100;
  for (int g = t; g < ng; g += 256) {
    const long long* d = dbg + g * 8;
    pmin = min(pmin, d[0]);
    long long v = 0;
    const int k = QSEL % 100;
    if (k == 0) v = d[0];
    else if (k <= 12) v = d[0] + (int)(q[g * 16 + k] - (unsigned)d[0]);
    else if (k == 14) v = q[g * 16 + 14] * 1000;
    else if (k == 16) v = d[1];
    else if (k == 17) v = d[2];
    else if (k == 18) v = d[3];
    else if (k == 22) v = d[5] * 100 / (d[2] - d[1]);
    sum += v; mx = max(mx, v); ++cn;
  }
  sh[0][t] = pmin; sh[1][t] = sum; sh[2][t] = mx; sh[3][t] = cn;
  __syncthreads();
  if (t == 0) {
    for (int i = 1; i < 256; ++i) { pmin = min(pmin, sh[0][i]); sum += sh[1][i]; mx = max(mx, sh[2][i]); cn += sh[3][i]; }
    long long r = md == 1 ? mx : sum / cn;
    if (QSEL % 100 != 14 && QSEL % 100 != 22) r -= pmin;
    if (QSEL == 99) r = 0;
    r = max(0ll, min(r, 5000ll));
    const long long tend = pmin + 10000 + 4 * r;
    for (int i = 0; i < 1000000; ++i) { if (__builtin_amdgcn_s_memrealtime() >= tend) break; __builtin_amdgcn_s_sleep(1); }
  }
}

extern "C" void kernel_launch(void* const* d_in, const int* in_sizes, int n_in,
                              void* d_out, int out_size, void* d_ws, size_t ws_size,
                              hipStream_t stream) {
  const unsigned short* x = (const unsigned short*)d_in[0];
  const u32x4* eattr = (const u32x4*)d_in[1];
  const int* src = (const int*)d_in[2];
  const int* dst = (const int*)d_in[3];
  const unsigned short* wa = (const unsigned short*)d_in[4];
  const unsigned short* wb = (const unsigned short*)d_in[5];
  const unsigned short* wc = (const unsigned short*)d_in[6];
  const unsigned short* wu = (const unsigned short*)d_in[7];
  const unsigned short* wv = (const unsigned short*)d_in[8];
  unsigned short* out = (unsigned short*)d_out;
  const int N = in_sizes[0] / 64;
  const int E = in_sizes[2];
  const int chunk = (((E + NCH - 1) / NCH) + 3) & ~3;

  const bool skew = N > 4096 && N < 60000;
  const unsigned M = N > 256 ? (unsigned)(4294967295.0 / ((double)N * (skew ? 1.0 + SKEWF : 1.0))) : 0xffffffu;
  const unsigned N1 = (0x7fffffffu / M) + 1u;
  const unsigned M2 = skew ? (0xffffffffu - N1 * M) / ((unsigned)N - N1) : M;

  auto fb = [](double v) { return __builtin_bit_cast(unsigned, (float)v); };
  char* ws = (char*)d_ws;
  size_t off = 0;
  off = 1 << 20;
  unsigned long long* FL = (unsigned long long*)(ws + off);   off += 1 << 20;
  u32x4* ENT = (u32x4*)(ws + off);                       off += (size_t)2 * IEXT;
  unsigned long long* TS = (unsigned long long*)(ws + off);

  k_main<<<NBK, 512, 0, stream>>>(x, eattr, src, dst, wa, wb, wc, wu, wv, ENT, TS, FL, out, N, E, chunk, M, N1,
                                  M2 - M, fb(65536.0 / M), fb(65536.0 / M2), fb(32768.0 / M + 0.5),
                                  fb((double)N1 + (32768.0 - (double)N1 * M) / M2 + 0.5)
     );
  k_dbg<<<1, 256, 0, stream>>>((const long long*)(ws + (128u<<20)), 2048);
}
```

```cpp
#include <hip/hip_runtime.h>
#include <hip/hip_bf16.h>
#include <stdint.h>

using bf16x8 = __attribute__((ext_vector_type(8))) short;
using s16x4 = __attribute__((ext_vector_type(4))) short;
using f32x16 = __attribute__((ext_vector_type(16))) float;
using f32x2 = __attribute__((ext_vector_type(2))) float;
using u32x4 = __attribute__((ext_vector_type(4))) unsigned;
using u32x16 = __attribute__((ext_vector_type(16))) unsigned;
using u32x2 = __attribute__((ext_vector_type(2))) unsigned;
using f16x2 = __attribute__((ext_vector_type(2))) _Float16;

#define MFMA32(a, b, c) __builtin_amdgcn_mfma_f32_32x32x16_bf16((a), (b), (c), 0, 0, 0)
#define RA 14
#define VK 4
#define LAS __attribute__((address_space(3)))
#define RANKOF(w) ((w) < 4 ? 7 - (w) : (w) - 4)
#define G1W 4
#define CPL 5
#define CPM 31
#define CPFB 19712
#define PSL 12
#define SKEWF 0.015
#define XLIKE(c) __builtin_expect(c, 0)
#define SB() __builtin_amdgcn_sched_barrier(0)

constexpr int NCH = 256;
constexpr int NBK = 256;
constexpr int NFRAG = 39;
constexpr float NLOG2E = -1.44269504088896340736f;
constexpr float VSC = 1.0f / (1 << VK);
constexpr unsigned TPITCH = 80;

__device__ __forceinline__ unsigned pack_bf16(float lo, float hi) {
  typedef __attribute__((ext_vector_type(2))) float f2;
  typedef __attribute__((ext_vector_type(2))) __bf16 b2;
  f2 v = {lo, hi};
  b2 r = __builtin_convertvector(v, b2);
  return __builtin_bit_cast(unsigned, r);
}

__device__ __forceinline__ unsigned pack_f16(float lo, float hi) {
  unsigned r;
  asm("v_cvt_pk_f16_f32 %0, %1, %2" : "=v"(r) : "v"(lo), "v"(hi));
  return r;
}

__device__ __forceinline__ void glds16(const void* sbase, unsigned voff, unsigned lds_dst) {
  asm volatile(
      "s_mov_b32 m0, %2\n\t"
      "s_nop 0\n\t"
      "global_load_lds_dwordx4 %0, %1"
      :: "v"(voff), "s"(sbase), "s"(lds_dst) : "memory", "m0");
}
__device__ __forceinline__ void glds16x(const void* sbase, unsigned a, unsigned c, unsigned lds_dst) {
  unsigned tmp;
  asm volatile(
      "s_mov_b32 m0, %4\n\t"
      "v_mad_u32_u16 %0, %1, %5, %2\n\t"
      "global_load_lds_dwordx4 %0, %3"
      : "=&v"(tmp) : "v"(a), "v"(c), "s"(sbase), "s"(lds_dst), "s"(128u) : "memory", "m0");
}
__device__ __forceinline__ void glds16e(const void* sbase, unsigned a, unsigned c, unsigned lds_dst) {
  unsigned tmp;
  asm volatile(
      "s_mov_b32 m0, %4\n\t"
      "v_add_u32 %0, %1, %2\n\t"
      "global_load_lds_dwordx4 %0, %3"
      : "=&v"(tmp) : "v"(a), "v"(c), "s"(sbase), "s"(lds_dst) : "memory", "m0");
}
__device__ __forceinline__ void gstore128wt(void* sbase, unsigned voff, u32x4 v) {
  asm volatile("global_store_dwordx4 %0, %1, %2 sc1\n\ts_nop 1" :: "v"(voff), "v"(v), "s"(sbase) : "memory");
}

constexpr int IBOX = 40960;
constexpr int IEXT = 1 << 24;
constexpr int CPIT = 208;
constexpr unsigned RINC = 87382;
constexpr int KP_HDR = 0;
constexpr int KP_ENT = KP_HDR + NBK * 4;
constexpr int KP_POS = KP_ENT + NBK * CPIT;
constexpr int KP_LDS = KP_POS + NBK * 4;
constexpr int NWL = 26;
constexpr int NBLOB = 31;

__device__ __forceinline__ unsigned mul24s(unsigned a, unsigned b) {
  unsigned r;
  asm("v_mul_u32_u24 %0, %1, %2" : "=v"(r) : "s"(b), "v"(a));
  return r;
}
__device__ __forceinline__ unsigned byte3x(unsigned a, unsigned b) {
  unsigned r;
  asm("v_mul_u32_u24_sdwa %0, %1, %2 dst_sel:DWORD dst_unused:UNUSED_PAD src0_sel:DWORD src1_sel:BYTE_3" : "=v"(r) : "s"(b), "v"(a));
  return r;
}
constexpr int RING_SLOT = 5 * 1024;
constexpr int LCAP = 512;
constexpr int LARR = 2560;
constexpr int LIST_BYTES = 2 * LARR;
constexpr int OFF_CNT = 0;
constexpr int SUBL = LARR / 4;
constexpr int OFF_XF = 256;
constexpr int OFF_W = 512;
constexpr int OFF_T1 = OFF_W + NWL * 1024;
constexpr int OFF_T2 = OFF_T1 + 32 * TPITCH;
constexpr int OFF_R0 = OFF_W + NFRAG * 1024;
constexpr int OFF_R1 = OFF_R0 + 8 * RING_SLOT;
constexpr int OFF_L = OFF_R1 + 8 * RING_SLOT;
constexpr int MAIN_LDS = OFF_L + 8 * LIST_BYTES;
static_assert(MAIN_LDS <= 160 * 1024, "LDS");
static_assert(KP_LDS <= 16 * RING_SLOT, "phase 1 lives in the ring");

__device__ __forceinline__ unsigned ldd(const unsigned* p) {
  return __hip_atomic_load(p, __ATOMIC_RELAXED, __HIP_MEMORY_SCOPE_AGENT);
}
__device__ __forceinline__ unsigned long long ldq(const unsigned long long* p) {
  return __hip_atomic_load(p, __ATOMIC_RELAXED, __HIP_MEMORY_SCOPE_AGENT);
}
__device__ __forceinline__ bf16x8 rawfrag(unsigned a) {
  const s16x4 a0 = __builtin_amdgcn_ds_read_tr16_b64_v4i16((LAS s16x4*)(size_t)a);
  const s16x4 a1 = __builtin_amdgcn_ds_read_tr16_b64_v4i16((LAS s16x4*)(size_t)(a + 512));
  return __builtin_shufflevector(a0, a1, 0, 1, 2, 3, 4, 5, 6, 7);
}
#define MAXN(a, b) __builtin_elementwise_maximum(a, b)

__global__ __launch_bounds__(512, 2) void k_main(const unsigned short* __restrict__ x,
                                                 const u32x4* __restrict__ ES,
                                                 const int* __restrict__ src, const int* __restrict__ dst,
                                                 const unsigned short* __restrict__ wa_g,
                                                 const unsigned short* __restrict__ wb_g,
                                                 const unsigned short* __restrict__ wc_g,
                                                 const unsigned short* __restrict__ wu_g,
                                                 const unsigned short* __restrict__ wv_g,
                                                 u32x4* ENT, unsigned long long* TS, unsigned long long* FL,
                                                 unsigned short* __restrict__ out, int N, int E, int chunk,
                                                 unsigned M, unsigned N1, unsigned DM, unsigned MF0, unsigned MF1,
                                                 unsigned MB0, unsigned MB1
                                                 ) {
  __shared__ __attribute__((aligned(16))) char smem[MAIN_LDS];
  const int tid0 = threadIdx.x;
  const int wave = __builtin_amdgcn_readfirstlane(tid0 >> 6);
  const int bk = blockIdx.x;
  const unsigned lds0 = (unsigned)(size_t)(LAS char*)smem;

  LAS char* sm = (LAS char*)smem + OFF_R0;
  const unsigned sm0 = lds0 + OFF_R0;
  const int tid = tid0, lane = tid & 63, r = lane & 31, h = lane >> 5;
  const unsigned pc = tid & 7, bq = tid >> 3;
  unsigned ia = sm0 + KP_ENT + bq * CPIT + pc * 16;
#pragma unroll
  for (int s = 0; s < 4; ++s) *(LAS u32x4*)(size_t)(ia + s * 64 * CPIT) = u32x4{~0u, ~0u, ~0u, ~0u};
  *(LAS unsigned*)(sm + KP_HDR + (tid & 255) * 4) = 0u;
  int q0 = 4 * tid + 3, q1 = 4 * tid + 1795;
  unsigned a0 = tid * 16, a1 = tid * 16 + 7168, z0 = 0u;
  unsigned long long m7 = __ballot(tid == 448);
  int n7 = wave == 7 ? 0 : 0x7fffffff;
  unsigned f0 = min((((bk >> 3) & CPM) * CPL + lane) * 128u, CPFB - 512u);
  unsigned long long p7 = __ballot(tid >= 448 && tid < 448 + CPL);
  asm volatile("" : "+v"(f0), "+s"(p7));
  asm volatile("" : "+v"(ia), "+v"(q0), "+v"(q1), "+v"(a0), "+v"(a1), "+v"(z0), "+s"(m7), "+s"(n7) :: "memory");
  SB();
  asm volatile("" :: "s"(x), "s"(ES), "s"(src), "s"(dst), "s"(wa_g), "s"(wb_g), "s"(wc_g), "s"(wu_g), "s"(wv_g), "s"(ENT),
               "s"(TS), "s"(FL), "s"(out), "s"(N), "s"(E), "s"(chunk), "s"(M), "s"(N1), "s"(DM), "s"(MF0), "s"(MF1), "s"(MB0), "s"(MB1));
  const int base = bk * chunk;
  const int ecnt = max(min(chunk, E - base), 0);
  unsigned long long on[2];
  on[0] = __ballot(q0 < min(min(chunk, E - base), n7));
  on[1] = __ballot(q1 < min(min(chunk, E - base), n7));
  a0 += base * 4u;
  a1 += base * 4u;
  u32x4 d[2], sv[2];
  u32x2 e0;
  asm volatile("s_mov_b64 exec, %11\n\t"
               "global_load_dwordx4 %0, %5, %8 nt\n\t"
               "s_mov_b64 exec, %12\n\t"
               "global_load_dwordx4 %1, %6, %8 nt\n\t"
               "s_mov_b64 exec, %13\n\t"
               "global_load_dwordx2 %2, %7, %9 sc1\n\t"
               "s_getpc_b64 s[96:97]\n\t"
               "s_mov_b32 m0, %15\n\t"
               "s_mov_b64 exec, %16\n\t"
               "global_load_lds_dword %14, s[96:97]\n\t"
               "s_mov_b64 exec, %11\n\t"
               "s_waitcnt lgkmcnt(0)\n\t"
               "s_barrier\n\t"
               "global_load_dwordx4 %3, %5, %10 nt\n\t"
               "s_mov_b64 exec, %12\n\t"
               "global_load_dwordx4 %4, %6, %10 nt\n\t"
               "s_mov_b64 exec, -1"
               : "=&{v[240:243]}"(d[0]), "=&{v[244:247]}"(d[1]), "=&{v[238:239]}"(e0), "=&{v[248:251]}"(sv[0]),
                 "=&{v[252:255]}"(sv[1])
               : "v"(a0), "v"(a1), "v"(z0), "s"(dst), "s"(FL), "s"(src), "s"(on[0]), "s"(on[1]), "s"(m7)
                 , "v"(f0), "n"(OFF_L + 7 * LIST_BYTES + 4096), "s"(p7)
               : "memory", "m0", "s96", "s97");
  SB();
  const int bu = (bk >> 1) | ((bk & 1) << 7);
  const unsigned Mw = bk & 1 ? M + DM : M, Cw = bk & 1 ? 0u - N1 * DM : 0u;
  unsigned MF = bk & 1 ? MF1 : MF0, MB = bk & 1 ? MB1 : MB0;
  asm volatile("" : "+s"(MF), "+s"(MB));
  char* ib = reinterpret_cast<char*>(ENT) + (size_t)bu * IBOX;
  auto nodeof = [&](unsigned c) { return min((unsigned)__builtin_fmaf((float)c, __uint_as_float(MF), __uint_as_float(MB)), (unsigned)N - 1u); };
  unsigned tup[8], val[8], pm[8];
  u32x4 xg[4];
  const unsigned xn0 = __builtin_amdgcn_readfirstlane(nodeof((bu * 8 + wave) * 32));
  unsigned xd[4];
  auto xprep = [&]() {
#pragma unroll
    for (int i = 0; i < 4; ++i)
      xd[i] = min(xn0 + 8 * i + (lane >> 3), N - 1u) * 128u + ((((lane & 7) ^ (lane >> 4)) * 16) ^ (i & 1) * 64) - i * 1024u;
    asm volatile("" : "+v"(xd[0]), "+v"(xd[1]), "+v"(xd[2]), "+v"(xd[3]));
  };
  auto xdma = [&]() {
    asm volatile("s_mov_b32 m0, %5\n\ts_nop 0\n\t"
                 "global_load_lds_dwordx4 %0, %4\n\t"
                 "global_load_lds_dwordx4 %1, %4 offset:1024\n\t"
                 "global_load_lds_dwordx4 %2, %4 offset:2048\n\t"
                 "global_load_lds_dwordx4 %3, %4 offset:3072"
                 :: "v"(xd[0]), "v"(xd[1]), "v"(xd[2]), "v"(xd[3]), "s"(x), "s"(lds0 + OFF_L + wave * LIST_BYTES)
                 : "memory", "m0");
  };
  auto wload = [&](const unsigned short* w, int f) {
    asm volatile("s_mov_b32 m0, %3\n\ts_nop 0\n\t"
                 "global_load_lds_dwordx4 %0, %2\n\t"
                 "global_load_lds_dwordx4 %0, %2 offset:1024\n\t"
                 "global_load_lds_dwordx4 %0, %2 offset:2048\n\t"
                 "global_load_lds_dwordx4 %0, %2 offset:3072\n\t"
                 "s_addk_i32 m0, 4096\n\ts_nop 0\n\t"
                 "global_load_lds_dwordx4 %1, %2\n\t"
                 "global_load_lds_dwordx4 %1, %2 offset:1024\n\t"
                 "global_load_lds_dwordx4 %1, %2 offset:2048\n\t"
                 "global_load_lds_dwordx4 %1, %2 offset:3072"
                 :: "v"(lane * 16u), "v"(lane * 16u + 4096u), "s"(w), "s"(lds0 + OFF_W + f * 1024) : "memory", "m0", "scc");
  };
  auto wall = [&]() {
    wload(wa_g, NBLOB);
    wload(wb_g, 0);
    wload(wv_g, 8);
    glds16(wc_g, lane * 16, lds0 + OFF_W + 16 * 1024);
    glds16(wc_g + 512, lane * 16, lds0 + OFF_W + 17 * 1024);
  };
  int spin = 1 << 20;
  u32x2 ep = {0u, 0u};
  if (wave == 7) {
    wall();
    *(LAS u32x4*)((LAS char*)smem + OFF_CNT + r * 16) =
        (r & 17) == 0 ? u32x4{lds0 + OFF_L, lds0 + OFF_L + SUBL, lds0 + OFF_L + 2 * SUBL, lds0 + OFF_L + 3 * SUBL}
                      : u32x4{0u, 0u, 0u, 0u};
    {
      const unsigned col = h ? ((r >> 1) & 12u) | ((r << 2) & 16u) | (r & 3u) : r;
      LAS char* tr = (LAS char*)smem + OFF_T1 + lane * TPITCH;
#pragma unroll
      for (int j = 0; j < 4; ++j) *(LAS u32x4*)(tr + j * 16) = u32x4{0u, 0u, 0u, 0u};
      *(LAS unsigned short*)(tr + col * 2) = 0x3C00;
    }
#pragma unroll
    for (int i = 0; i < 8; ++i) { tup[i] = 0u; val[i] = 0u; pm[i] = 0u; }
    asm volatile("s_waitcnt vmcnt(%1)" : "+{v[238:239]}"(e0) : "n"(29) : "memory");
    ep[0] = __builtin_amdgcn_readfirstlane(e0[0]) + 0x9E3779B1u;
    ep[1] = __builtin_amdgcn_readfirstlane(e0[1]) + 0x7F4A7C15u;
    *(LAS u32x2*)((LAS char*)smem + OFF_CNT + 56) = u32x2{ep[0], ep[1] >> 24};
    if (__builtin_expect(ecnt & 3, 0)) {
      const int i = (ecnt & ~3) + lane;
      on[0] = __ballot(i < ecnt);
      if (i < ecnt) {
        pm[0] = (unsigned)dst[base + i] * M + (unsigned)max(dst[base + i] - (int)N1, 0) * DM;
        tup[0] = (unsigned)src[base + i] | (unsigned)i << 16;
        val[0] = __hip_atomic_fetch_add((LAS unsigned*)(sm + KP_HDR) + (pm[0] >> 24), RINC, __ATOMIC_RELAXED, __HIP_MEMORY_SCOPE_WORKGROUP);
        const unsigned u = min(val[0] >> 16, 48u);
        *(LAS unsigned*)(sm + KP_ENT + (pm[0] >> 24) * CPIT + u * 4) = tup[0];
        *(LAS unsigned char*)(sm + KP_ENT + (pm[0] >> 24) * CPIT + ((u * 4) | (u & 3) | 12)) = pm[0] >> 16;
      }
      __builtin_amdgcn_s_waitcnt(0x0F70);
    }
    xprep();
    xdma();
  } else {
    xprep();
    unsigned inc = RINC, t0 = (unsigned)tid << 18;
    asm volatile("s_waitcnt vmcnt(2)" : "+{v[240:243]}"(d[0]), "+{v[244:247]}"(d[1]), "+v"(inc), "+v"(t0) :: "memory");
    unsigned cb[8];
#pragma unroll
    for (int s = 0; s < 2; ++s) {
      unsigned* rr = val + 4 * s;
#pragma unroll
      for (int k = 0; k < 4; ++k) {
        unsigned t;
        asm("v_sub_u32 %0, %1, %2 clamp" : "=v"(t) : "v"(d[s][k]), "s"(N1));
        asm("v_mad_u32_u24 %0, %1, %2, %3" : "=v"(pm[4 * s + k]) : "v"(t), "s"(DM), "v"(mul24s(d[s][k], M)));
      }
#pragma unroll
      for (int k = 0; k < 4; ++k) rr[k] = byte3x(pm[4 * s + k], 4u);
      asm volatile(
          "s_mov_b64 exec, %5\n\t"
          "ds_add_rtn_u32 %0, %0, %4 offset:%6\n\t"
          "ds_add_rtn_u32 %1, %1, %4 offset:%6\n\t"
          "ds_add_rtn_u32 %2, %2, %4 offset:%6\n\t"
          "ds_add_rtn_u32 %3, %3, %4 offset:%6\n\t"
          "s_mov_b64 exec, -1"
          : "+v"(rr[0]), "+v"(rr[1]), "+v"(rr[2]), "+v"(rr[3])
          : "v"(inc), "s"(on[s]), "n"(OFF_R0 + KP_HDR)
          : "memory");
    }
#pragma unroll
    for (int i = 0; i < 8; ++i) cb[i] = byte3x(pm[i], (unsigned)CPIT);
    asm volatile("s_waitcnt vmcnt(0)"
                 : "+{v[248:251]}"(sv[0]), "+{v[252:255]}"(sv[1]), "+v"(cb[0]), "+v"(cb[1]), "+v"(cb[2]), "+v"(cb[3]), "+v"(cb[4]), "+v"(cb[5]),
                   "+v"(cb[6]), "+v"(cb[7])
                 :: "memory");
    xdma();
#pragma unroll
    for (int i = 0; i < 8; ++i) tup[i] = sv[i >> 2][i & 3] + t0 + (((i >> 2) * 1792u + (i & 3)) << 16);
#pragma unroll
    for (int s = 0; s < 2; ++s) {
      asm volatile("s_waitcnt lgkmcnt(%8)"
                   : "+v"(val[4 * s]), "+v"(val[4 * s + 1]), "+v"(val[4 * s + 2]), "+v"(val[4 * s + 3]), "+v"(tup[4 * s]),
                     "+v"(tup[4 * s + 1]), "+v"(tup[4 * s + 2]), "+v"(tup[4 * s + 3])
                   : "n"(s ? 8 : 4) : "memory");
      unsigned pa[4], pb[4];
#pragma unroll
      for (int k = 0; k < 4; ++k) {
        const unsigned u = min(val[4 * s + k], 48u << 16);
        asm("v_mad_u32_u16 %0, %1, 4, %2 op_sel:[1,0,0,0]" : "=v"(pa[k]) : "v"(u), "v"(cb[4 * s + k]));
        pb[k] = pa[k] | ((u >> 16) & 3u) | 12u;
      }
      asm volatile(
          "s_mov_b64 exec, %16\n\t"
          "ds_write_b32 %0, %8 offset:%17\n\t"
          "ds_write_b8_d16_hi %4, %12 offset:%17\n\t"
          "ds_write_b32 %1, %9 offset:%17\n\t"
          "ds_write_b8_d16_hi %5, %13 offset:%17\n\t"
          "ds_write_b32 %2, %10 offset:%17\n\t"
          "ds_write_b8_d16_hi %6, %14 offset:%17\n\t"
          "ds_write_b32 %3, %11 offset:%17\n\t"
          "ds_write_b8_d16_hi %7, %15 offset:%17\n\t"
          "s_mov_b64 exec, -1"
          :: "v"(pa[0]), "v"(pa[1]), "v"(pa[2]), "v"(pa[3]), "v"(pb[0]), "v"(pb[1]), "v"(pb[2]), "v"(pb[3]),
             "v"(tup[4 * s]), "v"(tup[4 * s + 1]), "v"(tup[4 * s + 2]), "v"(tup[4 * s + 3]), "v"(pm[4 * s]),
             "v"(pm[4 * s + 1]), "v"(pm[4 * s + 2]), "v"(pm[4 * s + 3]), "s"(on[s]), "n"(OFF_R0 + KP_ENT)
          : "memory");
    }
  }
  u32x2 tag;
  int xw = 0;
  {
    unsigned ha = sm0 + KP_HDR + bq * 4, hb = sm0 + KP_HDR + lane * 16;
    unsigned go = bq * IBOX + bk * 128 + pc * 16;
    asm volatile("s_waitcnt lgkmcnt(0)\n\ts_barrier" : "+v"(ha), "+v"(hb), "+v"(go) :: "memory");
    tag = *(const LAS u32x2*)((LAS char*)smem + OFF_CNT + 56);
    const u32x4 call = *(const LAS u32x4*)(size_t)hb;
    unsigned cn[4];
    u32x4 v[4];
#pragma unroll
    for (int s = 0; s < 4; ++s) {
      cn[s] = *(const LAS unsigned*)(size_t)(ha + s * 256);
      v[s] = *(const LAS u32x4*)(size_t)(ia + s * 64 * CPIT);
    }
    asm volatile("" : "+v"(v[0]), "+v"(v[1]), "+v"(v[2]), "+v"(v[3]), "+v"(tag));
    if (__builtin_expect(__ballot(max(max(call[0], call[1]), max(call[2], call[3])) > 36u * RINC) != 0ull, 0)) {
      LAS unsigned* ovc = (LAS unsigned*)((LAS char*)smem + OFF_CNT + 24);
      LAS unsigned* pos = (LAS unsigned*)(sm + KP_POS);
      if (tid < NBK) {
        const unsigned u = *(const LAS unsigned*)(sm + KP_HDR + tid * 4) >> 16, n = u - (u >> 2);
        if (n > 36u) {
          const unsigned p = (unsigned)base + __hip_atomic_fetch_add(ovc, n - 36u, __ATOMIC_RELAXED, __HIP_MEMORY_SCOPE_WORKGROUP);
          pos[tid] = p;
          __hip_atomic_store(FL + 512 + tid * NCH + bk, ((unsigned long long)(n - 36u) << 32) | p, __ATOMIC_RELAXED,
                             __HIP_MEMORY_SCOPE_AGENT);
        }
      }
      asm volatile("s_waitcnt lgkmcnt(0)\n\ts_barrier" ::: "memory");
#pragma unroll
      for (int i = 0; i < 8; ++i) {
        const unsigned u = val[i] >> 16, n = u - (u >> 2);
        if (((on[i >> 2] >> lane) & 1ull) && n >= 36u)
          __hip_atomic_store(TS + pos[pm[i] >> 24] + (n - 36u), ((unsigned long long)((pm[i] >> 16) & 0xffu) << 32) | tup[i],
                             __ATOMIC_RELAXED, __HIP_MEMORY_SCOPE_AGENT);
      }
      __builtin_amdgcn_s_waitcnt(0x0F70);
      asm volatile("s_waitcnt vmcnt(0)\n\ts_barrier" ::: "memory");
    }
    auto seal = [&](u32x4& w, bool more) {
      w[3] = __builtin_amdgcn_perm(tag[1], w[3], 0x04020100u);
      unsigned sm_ = w[0] + w[1] + w[2] + w[3] + tag[0];
      sm_ += __builtin_amdgcn_update_dpp(0u, sm_, 0xB1, 0xf, 0xf, false);
      sm_ += __builtin_amdgcn_update_dpp(0u, sm_, 0x4E, 0xf, 0xf, false);
      sm_ += __builtin_amdgcn_update_dpp(0u, sm_, 0x141, 0xf, 0xf, false);
      w[2] = pc == 7u ? w[2] - sm_ + (more ? 1u : 0u) : w[2];
    };
#pragma unroll
    for (int s = 0; s < 4; ++s) seal(v[s], cn[s] > 23u * RINC);
    asm volatile("global_store_dwordx4 %0, %1, %5 sc1\n\t"
                 "global_store_dwordx4 %0, %2, %6 sc1\n\t"
                 "global_store_dwordx4 %0, %3, %7 sc1\n\t"
                 "global_store_dwordx4 %0, %4, %8 sc1\n\t"
                 "s_nop 1"
                 :: "v"(go), "v"(v[0]), "v"(v[1]), "v"(v[2]), "v"(v[3]), "s"(ENT), "s"(ENT + 4 * IBOX), "s"(ENT + 8 * IBOX),
                    "s"(ENT + 12 * IBOX)
                 : "memory");
    if (XLIKE(__ballot(max(max(cn[0], cn[1]), max(cn[2], cn[3])) > 23u * RINC) != 0ull)) {
      unsigned xm = 0u;
#pragma unroll
      for (int s = 0; s < 4; ++s) xm |= cn[s] > 23u * RINC ? 1u << s : 0u;
#pragma unroll 1
      do {
        const unsigned s = __builtin_ctz(xm | 8u);
        const unsigned c = *(const LAS unsigned*)(size_t)(ha + s * 256);
        u32x4 w = *(const LAS u32x4*)(size_t)(ia + s * (64 * CPIT) + (pc < 4u ? 128 : 0));
        const unsigned lim = pc < 4u ? (c >> 16) - 4u * pc : 0u;
        w[3] = pc == 7u ? w[3] >> 16 : w[3];
        w[0] = pc == 7u ? w[2] : 32u < lim ? w[0] : ~0u;
        w[1] = 33u < lim ? w[1] : ~0u;
        w[2] = 34u < lim ? w[2] : ~0u;
        seal(w, c > 36u * RINC);
        const unsigned ga = go + s * (64 * IBOX);
        if (xm) {
          w[3] += (((bk >> 3) & 3) + 1) << 24;
          gstore128wt(ENT + 2048 + ((bk >> 5) * 8 + (bk & 7) - bk) * 8, ga, w);
          w[3] -= (((bk >> 3) & 3) + 1) << 24;
          gstore128wt(ENT + IEXT / 16, ga, w);
        }
        xm &= xm - 1u;
        ++xw;
      } while (__ballot(xm != 0u) != 0ull);
    }
  }
  u32x4 cv[4];
  unsigned vo[4], eb[4];
  unsigned long long pend[4];
#pragma unroll
  for (int k = 0; k < 4; ++k) {
    vo[k] = wave * 4096u + k * 1024u + lane * 16u;
    eb[k] = (unsigned)((wave * 32 + k * 8 + (lane >> 3)) * chunk) * 32u;
  }
#define POLL0()                                                                                                       \
  asm volatile("global_load_dwordx4 %0, %4, %8 sc1\n\t"                                                               \
               "global_load_dwordx4 %1, %5, %8 sc1\n\t"                                                               \
               "global_load_dwordx4 %2, %6, %8 sc1\n\t"                                                               \
               "global_load_dwordx4 %3, %7, %8 sc1"                                                                   \
               : "=&{v[240:243]}"(cv[0]), "=&{v[244:247]}"(cv[1]), "=&{v[248:251]}"(cv[2]), "=&{v[252:255]}"(cv[3])   \
               : "v"(vo[0]), "v"(vo[1]), "v"(vo[2]), "v"(vo[3]), "s"(ib)                                              \
               : "memory")
  const f32x16 zero16 = {0.f, 0.f, 0.f, 0.f, 0.f, 0.f, 0.f, 0.f, 0.f, 0.f, 0.f, 0.f, 0.f, 0.f, 0.f, 0.f};
  const unsigned to = lds0 + OFF_W + ((lane >> 2) & 3) * 128 + (lane & 1) * 64 + ((lane >> 4) & 1) * 16 + ((lane >> 1) & 1) * 8;
  auto xa = [&]() {
    bf16x8 wa[8];
#pragma unroll
    for (int k = 0; k < 8; ++k) wa[k] = rawfrag(to + (NBLOB + (k >> 1)) * 1024 + h * 4096 + (k & 1) * 32);
    asm volatile("" : "+v"(wa[0]), "+v"(wa[1]), "+v"(wa[2]), "+v"(wa[3]), "+v"(wa[4]), "+v"(wa[5]), "+v"(wa[6]), "+v"(wa[7]),
                      "+{v[160:163]}"(xg[0]), "+{v[164:167]}"(xg[1]), "+{v[168:171]}"(xg[2]), "+{v[172:175]}"(xg[3]));
    f32x16 XA[2] = {zero16, zero16};
#pragma unroll
    for (int k = 0; k < 4; ++k)
#pragma unroll
      for (int ct = 0; ct < 2; ++ct) XA[ct] = MFMA32(__builtin_bit_cast(bf16x8, xg[k]), wa[k * 2 + ct], XA[ct]);
#pragma unroll
    for (int q = 0; q < 4; ++q) {
      u32x4 xp;
#pragma unroll
      for (int j = 0; j < 4; ++j)
        xp[j] = pack_f16(XA[q & 1][8 * (q >> 1) + 2 * j] * NLOG2E - RA, XA[q & 1][8 * (q >> 1) + 2 * j + 1] * NLOG2E - RA);
      *(LAS u32x4*)((LAS char*)smem + OFF_R1 + wave * RING_SLOT + q * 1024 + lane * 16) = xp;
    }
  };
  {
    auto prep = [&](unsigned f, unsigned hs, float scale) {
      u32x4 w[2];
#pragma unroll
      for (int ct = 0; ct < 2; ++ct) w[ct] = __builtin_bit_cast(u32x4, rawfrag(to + f + h * hs + ct * 32));
      asm volatile("s_waitcnt lgkmcnt(0)" : "+v"(w[0]), "+v"(w[1]) :: "memory");
#pragma unroll
      for (int ct = 0; ct < 2; ++ct) {
#pragma unroll
        for (int j = 0; j < 4; ++j)
          w[ct][j] = pack_bf16(__uint_as_float(w[ct][j] << 16) * scale, __uint_as_float(w[ct][j] & 0xffff0000u) * scale);
        *(LAS u32x4*)((LAS char*)smem + OFF_W + f + ct * hs + lane * 16) = w[ct];
      }
    };
    {
      const unsigned xr = min(nodeof((bu * 8 + wave) * 32 + r) - xn0, 31u);
      unsigned xa_ = lds0 + OFF_L + wave * LIST_BYTES + xr * 128 + (((4 * h) ^ ((xr >> 1) & 7)) * 16);
      asm volatile("s_cmp_eq_u32 %1, 1\n\ts_cbranch_scc1 1f\n\ts_waitcnt vmcnt(4)\n1:\n\ts_waitcnt vmcnt(6)"
                   : "+v"(xa_) : "s"(xw) : "memory", "scc");
#pragma unroll
      for (int t = 0; t < 4; ++t) xg[t] = *(const LAS u32x4*)(size_t)(xa_ ^ (t * 16));
      asm volatile("" : "+v"(xg[0]), "+v"(xg[1]), "+v"(xg[2]), "+v"(xg[3]) :: "memory");
      *(LAS unsigned*)((LAS char*)smem + OFF_L + wave * LIST_BYTES + h * LARR + r * 4) = ~0u;
    }
    asm volatile("s_waitcnt lgkmcnt(0)\n\ts_barrier"
                 : "+v"(vo[0]), "+v"(vo[1]), "+v"(vo[2]), "+v"(vo[3]), "+v"(eb[0]), "+v"(eb[1]), "+v"(eb[2]), "+v"(eb[3])
                 :: "memory");
    xa();
    {
      unsigned f = (wave + (wave & 4)) * 1024, hs = 4096;
      const float scale = wave < 4 ? NLOG2E : VSC;
#pragma unroll 1
      while (true) {
        asm volatile("" : "+s"(f), "+s"(hs));
        prep(f, hs, scale);
        if (f != 0u) break;
        f = 16 * 1024;
        hs = 1024;
      }
    }
  }
  const unsigned l0 = lds0 + OFF_L;
  unsigned aLX = l0 + (lane >> 3) * 16;
  unsigned aLE = l0 + LARR + ((((lane >> 1) & 7) << 2) | (lane >> 4)) * 4;
  unsigned aLS4 = l0 + (4 * h + ((lane >> 2) & 3)) * 16;
  unsigned aLS1 = l0 + (((r & 7) << 2) | (r >> 3)) * 4 + 2;
  unsigned cx0 = (unsigned)(((lane & 7) ^ (lane >> 4)) * 16);
  unsigned cx1 = cx0 ^ 64u;
  unsigned ce = (unsigned)((((lane & 1) ^ ((lane >> 4) & 1))) * 16);
  const unsigned ringd = 8 * RING_SLOT;
  unsigned cS = lds0 + OFF_T1 + (((lane >> 4) & 1) * 16 + (lane & 3) * 4) * 2;
  unsigned cSt = lds0 + OFF_T2 + h * 32;
  unsigned fo[4];
#pragma unroll
  for (int t = 0; t < 4; ++t) fo[t] = lds0 + OFF_R0 + (unsigned)(r * 128 + (((4 * h + t) ^ ((r >> 1) & 7)) * 16));
  unsigned foe = lds0 + OFF_R0 + (unsigned)(4096 + r * 32 + ((h ^ ((r >> 3) & 1)) * 16));
  f32x16 Y[4];
#pragma unroll
  for (int q = 0; q < 4; ++q)
#pragma unroll
    for (int i = 0; i < 16; i += 2) {
      f32x2 y;
      if (q < 2) asm volatile("v_mov_b64 %0, %1" : "=v"(y) : "s"(0x358637bd358637bdull + (RA * 0x0080000000800000ull)));
      else asm volatile("v_mov_b64 %0, 0" : "=v"(y));
      Y[q][i] = y[0];
      Y[q][i + 1] = y[1];
    }
  constexpr unsigned cu = RA == 14 ? 0x04000400u : 0x02000200u;
  unsigned rid = lane & 7;
  asm volatile("" : "+v"(aLX), "+v"(aLE), "+v"(aLS4), "+v"(aLS1), "+v"(cx0), "+v"(cx1), "+v"(ce), "+v"(cS), "+v"(cSt),
                    "+v"(fo[0]), "+v"(fo[1]), "+v"(fo[2]), "+v"(fo[3]), "+v"(foe), "+v"(rid));
  asm volatile("" : "+{v[0:15]}"(Y[0]), "+{v[16:31]}"(Y[1]), "+{v[32:47]}"(Y[2]), "+{v[48:63]}"(Y[3]));
  SB();
  __builtin_amdgcn_s_sleep(PSL);
  POLL0();
  glds16(wu_g + wave * 512, lane * 16, lds0 + OFF_W + (18 + wave) * 1024);
  auto ld128 = [&](unsigned a) { return *(const LAS u32x4*)(size_t)a; };
  auto gather5 = [&](const u32x4 sx, unsigned re, unsigned dstb) {
    glds16e(ES, re, ce, dstb + 4096);
    glds16x(x, sx[0], cx0, dstb);
    glds16x(x, sx[1], cx1, dstb + 1024);
    glds16x(x, sx[2], cx0, dstb + 2048);
    glds16x(x, sx[3], cx1, dstb + 3072);
  };
  const unsigned sq = min(4u * M, 0xffffffu);
  unsigned early, just;
  asm volatile(
        "v_mov_b32 v181, %18\n\t"
        "v_mov_b32 v180, 160\n\t"
        "v_mov_b32 v176, 0xe0\n\t"
        "v_mov_b32 v177, 0x0c040100\n\t"
        "v_mov_b32 v178, 0x0c050100\n\t"
        "v_mov_b32 v179, 0x0c060100\n\t"
        "s_mov_b32 s52, 0x7f7f7f7f\n\t"
        "s_mov_b32 s53, 0x7f7f7f7f\n\t"
        "s_mov_b64 s[54:55], 0\n\t"
        "s_mov_b32 %12, 0\n\t"
        "v_mov_b32 v191, 4\n\t"
        "s_waitcnt vmcnt(3)\n\t"
        "v_add3_u32 v228, v240, v241, v242\n\t"
        "v_add3_u32 v231, v244, v245, v246\n\t"
        "v_add3_u32 v228, v228, v243, %19\n\t"
        "v_add3_u32 v231, v231, v247, %19\n\t"
        "v_cmp_ne_u32_sdwa s[72:73], v243, %20 src0_sel:BYTE_3 src1_sel:DWORD\n\t"
        "v_add_u32_dpp v228, v228, v228 quad_perm:[1,0,3,2] row_mask:0xf bank_mask:0xf\n\t"
        "v_add_u32_dpp v231, v231, v231 quad_perm:[1,0,3,2] row_mask:0xf bank_mask:0xf\n\t"
        "v_cmp_ne_u32_sdwa s[78:79], v247, %20 src0_sel:BYTE_3 src1_sel:DWORD\n\t"
        "v_add_u32_dpp v228, v228, v228 quad_perm:[2,3,0,1] row_mask:0xf bank_mask:0xf\n\t"
        "v_add_u32_dpp v231, v231, v231 quad_perm:[2,3,0,1] row_mask:0xf bank_mask:0xf\n\t"
        "s_nop 0\n\t"
        "v_add_u32_dpp v228, v228, v228 row_half_mirror row_mask:0xf bank_mask:0xf\n\t"
        "v_add_u32_dpp v231, v231, v231 row_half_mirror row_mask:0xf bank_mask:0xf\n\t"
        "v_cmp_gt_u32 s[64:65], 2, v228\n\t"
        "v_cmp_eq_u32 s[56:57], 1, v228\n\t"
        "v_cmp_gt_u32 s[66:67], 2, v231\n\t"
        "v_cmp_eq_u32 s[58:59], 1, v231\n\t"
        "s_and_b64 s[72:73], s[72:73], s[64:65]\n\t"
        "s_cselect_b64 s[64:65], 0, s[64:65]\n\t"
        "s_and_b64 s[56:57], s[56:57], s[64:65]\n\t"
        "s_not_b64 %0, s[64:65]\n\t"
        "s_or_b64 s[54:55], s[54:55], %0\n\t"
        "s_and_b64 s[78:79], s[78:79], s[66:67]\n\t"
        "s_cselect_b64 s[66:67], 0, s[66:67]\n\t"
        "s_and_b64 s[58:59], s[58:59], s[66:67]\n\t"
        "s_not_b64 %1, s[66:67]\n\t"
        "s_or_b64 s[54:55], s[54:55], %1\n\t"
        "s_mov_b64 exec, s[64:65]\n\t"
        "v_cmpx_ne_u32 s[72:73], -1, v240\n\t"
        "v_mul_hi_u32_u24_sdwa v204, v240, %21 dst_sel:DWORD dst_unused:UNUSED_PAD src0_sel:WORD_0 src1_sel:DWORD\n\t"
        "v_and_b32_sdwa v192, v243, v176 dst_sel:DWORD dst_unused:UNUSED_PAD src0_sel:BYTE_0 src1_sel:DWORD\n\t"
        "v_lshl_add_u32 v204, v204, 2, v192\n\t"
        "ds_add_rtn_u32 v204, v204, v191\n\t"
        "v_perm_b32 v216, v243, v240, v177\n\t"
        "v_mad_u32_u16 v228, v240, 32, %13 op_sel:[1,0,0,0]\n\t"
        "v_cmpx_ne_u32 s[74:75], -1, v241\n\t"
        "v_mul_hi_u32_u24_sdwa v205, v241, %21 dst_sel:DWORD dst_unused:UNUSED_PAD src0_sel:WORD_0 src1_sel:DWORD\n\t"
        "v_and_b32_sdwa v193, v243, v176 dst_sel:DWORD dst_unused:UNUSED_PAD src0_sel:BYTE_1 src1_sel:DWORD\n\t"
        "v_lshl_add_u32 v205, v205, 2, v193\n\t"
        "ds_add_rtn_u32 v205, v205, v191\n\t"
        "v_perm_b32 v217, v243, v241, v178\n\t"
        "v_mad_u32_u16 v229, v241, 32, %13 op_sel:[1,0,0,0]\n\t"
        "s_and_b64 exec, exec, s[52:53]\n\t"
        "v_cmpx_ne_u32 s[76:77], -1, v242\n\t"
        "v_mul_hi_u32_u24_sdwa v206, v242, %21 dst_sel:DWORD dst_unused:UNUSED_PAD src0_sel:WORD_0 src1_sel:DWORD\n\t"
        "v_and_b32_sdwa v194, v243, v176 dst_sel:DWORD dst_unused:UNUSED_PAD src0_sel:BYTE_2 src1_sel:DWORD\n\t"
        "v_lshl_add_u32 v206, v206, 2, v194\n\t"
        "ds_add_rtn_u32 v206, v206, v191\n\t"
        "v_perm_b32 v218, v243, v242, v179\n\t"
        "v_mad_u32_u16 v230, v242, 32, %13 op_sel:[1,0,0,0]\n\t"
        "s_mov_b64 exec, s[66:67]\n\t"
        "v_cmpx_ne_u32 s[78:79], -1, v244\n\t"
        "v_mul_hi_u32_u24_sdwa v207, v244, %21 dst_sel:DWORD dst_unused:UNUSED_PAD src0_sel:WORD_0 src1_sel:DWORD\n\t"
        "v_and_b32_sdwa v195, v247, v176 dst_sel:DWORD dst_unused:UNUSED_PAD src0_sel:BYTE_0 src1_sel:DWORD\n\t"
        "v_lshl_add_u32 v207, v207, 2, v195\n\t"
        "ds_add_rtn_u32 v207, v207, v191\n\t"
        "v_perm_b32 v219, v247, v244, v177\n\t"
        "v_mad_u32_u16 v231, v244, 32, %14 op_sel:[1,0,0,0]\n\t"
        "v_cmpx_ne_u32 s[80:81], -1, v245\n\t"
        "v_mul_hi_u32_u24_sdwa v208, v245, %21 dst_sel:DWORD dst_unused:UNUSED_PAD src0_sel:WORD_0 src1_sel:DWORD\n\t"
        "v_and_b32_sdwa v196, v247, v176 dst_sel:DWORD dst_unused:UNUSED_PAD src0_sel:BYTE_1 src1_sel:DWORD\n\t"
        "v_lshl_add_u32 v208, v208, 2, v196\n\t"
        "ds_add_rtn_u32 v208, v208, v191\n\t"
        "v_perm_b32 v220, v247, v245, v178\n\t"
        "v_mad_u32_u16 v232, v245, 32, %14 op_sel:[1,0,0,0]\n\t"
        "s_and_b64 exec, exec, s[52:53]\n\t"
        "v_cmpx_ne_u32 s[82:83], -1, v246\n\t"
        "v_mul_hi_u32_u24_sdwa v209, v246, %21 dst_sel:DWORD dst_unused:UNUSED_PAD src0_sel:WORD_0 src1_sel:DWORD\n\t"
        "v_and_b32_sdwa v197, v247, v176 dst_sel:DWORD dst_unused:UNUSED_PAD src0_sel:BYTE_2 src1_sel:DWORD\n\t"
        "v_lshl_add_u32 v209, v209, 2, v197\n\t"
        "ds_add_rtn_u32 v209, v209, v191\n\t"
        "v_perm_b32 v221, v247, v246, v179\n\t"
        "v_mad_u32_u16 v233, v246, 32, %14 op_sel:[1,0,0,0]\n\t"
        "s_mov_b64 exec, -1\n\t"
        "s_waitcnt vmcnt(1)\n\t"
        "v_add3_u32 v234, v248, v249, v250\n\t"
        "v_add3_u32 v237, v252, v253, v254\n\t"
        "v_add3_u32 v234, v234, v251, %19\n\t"
        "v_add3_u32 v237, v237, v255, %19\n\t"
        "v_cmp_ne_u32_sdwa s[84:85], v251, %20 src0_sel:BYTE_3 src1_sel:DWORD\n\t"
        "v_add_u32_dpp v234, v234, v234 quad_perm:[1,0,3,2] row_mask:0xf bank_mask:0xf\n\t"
        "v_add_u32_dpp v237, v237, v237 quad_perm:[1,0,3,2] row_mask:0xf bank_mask:0xf\n\t"
        "v_cmp_ne_u32_sdwa s[90:91], v255, %20 src0_sel:BYTE_3 src1_sel:DWORD\n\t"
        "v_add_u32_dpp v234, v234, v234 quad_perm:[2,3,0,1] row_mask:0xf bank_mask:0xf\n\t"
        "v_add_u32_dpp v237, v237, v237 quad_perm:[2,3,0,1] row_mask:0xf bank_mask:0xf\n\t"
        "s_nop 0\n\t"
        "v_add_u32_dpp v234, v234, v234 row_half_mirror row_mask:0xf bank_mask:0xf\n\t"
        "v_add_u32_dpp v237, v237, v237 row_half_mirror row_mask:0xf bank_mask:0xf\n\t"
        "v_cmp_gt_u32 s[68:69], 2, v234\n\t"
        "v_cmp_eq_u32 s[60:61], 1, v234\n\t"
        "v_cmp_gt_u32 s[70:71], 2, v237\n\t"
        "v_cmp_eq_u32 s[62:63], 1, v237\n\t"
        "s_and_b64 s[84:85], s[84:85], s[68:69]\n\t"
        "s_cselect_b64 s[68:69], 0, s[68:69]\n\t"
        "s_and_b64 s[60:61], s[60:61], s[68:69]\n\t"
        "s_not_b64 %2, s[68:69]\n\t"
        "s_or_b64 s[54:55], s[54:55], %2\n\t"
        "s_and_b64 s[90:91], s[90:91], s[70:71]\n\t"
        "s_cselect_b64 s[70:71], 0, s[70:71]\n\t"
        "s_and_b64 s[62:63], s[62:63], s[70:71]\n\t"
        "s_not_b64 %3, s[70:71]\n\t"
        "s_or_b64 s[54:55], s[54:55], %3\n\t"
        "s_or_b64 s[50:51], s[56:57], s[58:59]\n\t"
        "s_or_b64 exec, s[60:61], s[62:63]\n\t"
        "s_or_b64 s[50:51], s[50:51], exec\n\t"
        "s_cbranch_scc0 57f\n\t"
        "s_mov_b64 exec, s[50:51]\n\t"
        "global_load_dwordx4 v[172:175], %30, %17 sc1\n\t"
        "57:\n\t"
        "s_mov_b64 exec, s[72:73]\n\t"
        "s_waitcnt lgkmcnt(3)\n\t"
        "v_min_u32 v204, v181, v204\n\t"
        "v_min_u32 v205, v181, v205\n\t"
        "v_min_u32 v206, v181, v206\n\t"
        "v_mad_u32_u24 v204, v192, v180, v204\n\t"
        "v_mad_u32_u24 v205, v193, v180, v205\n\t"
        "v_mad_u32_u24 v206, v194, v180, v206\n\t"
        "ds_write2st64_b32 v204, v216, v228 offset1:10\n\t"
        "s_mov_b64 exec, s[74:75]\n\t"
        "ds_write2st64_b32 v205, v217, v229 offset1:10\n\t"
        "s_mov_b64 exec, s[76:77]\n\t"
        "ds_write2st64_b32 v206, v218, v230 offset1:10\n\t"
        "s_mov_b64 exec, s[68:69]\n\t"
        "v_cmpx_ne_u32 s[84:85], -1, v248\n\t"
        "v_mul_hi_u32_u24_sdwa v210, v248, %21 dst_sel:DWORD dst_unused:UNUSED_PAD src0_sel:WORD_0 src1_sel:DWORD\n\t"
        "v_and_b32_sdwa v198, v251, v176 dst_sel:DWORD dst_unused:UNUSED_PAD src0_sel:BYTE_0 src1_sel:DWORD\n\t"
        "v_lshl_add_u32 v210, v210, 2, v198\n\t"
        "ds_add_rtn_u32 v210, v210, v191\n\t"
        "v_perm_b32 v222, v251, v248, v177\n\t"
        "v_mad_u32_u16 v234, v248, 32, %15 op_sel:[1,0,0,0]\n\t"
        "v_cmpx_ne_u32 s[86:87], -1, v249\n\t"
        "v_mul_hi_u32_u24_sdwa v211, v249, %21 dst_sel:DWORD dst_unused:UNUSED_PAD src0_sel:WORD_0 src1_sel:DWORD\n\t"
        "v_and_b32_sdwa v199, v251, v176 dst_sel:DWORD dst_unused:UNUSED_PAD src0_sel:BYTE_1 src1_sel:DWORD\n\t"
        "v_lshl_add_u32 v211, v211, 2, v199\n\t"
        "ds_add_rtn_u32 v211, v211, v191\n\t"
        "v_perm_b32 v223, v251, v249, v178\n\t"
        "v_mad_u32_u16 v235, v249, 32, %15 op_sel:[1,0,0,0]\n\t"
        "s_and_b64 exec, exec, s[52:53]\n\t"
        "v_cmpx_ne_u32 s[88:89], -1, v250\n\t"
        "v_mul_hi_u32_u24_sdwa v212, v250, %21 dst_sel:DWORD dst_unused:UNUSED_PAD src0_sel:WORD_0 src1_sel:DWORD\n\t"
        "v_and_b32_sdwa v200, v251, v176 dst_sel:DWORD dst_unused:UNUSED_PAD src0_sel:BYTE_2 src1_sel:DWORD\n\t"
        "v_lshl_add_u32 v212, v212, 2, v200\n\t"
        "ds_add_rtn_u32 v212, v212, v191\n\t"
        "v_perm_b32 v224, v251, v250, v179\n\t"
        "v_mad_u32_u16 v236, v250, 32, %15 op_sel:[1,0,0,0]\n\t"
        "s_mov_b64 exec, s[78:79]\n\t"
        "s_waitcnt lgkmcnt(6)\n\t"
        "v_min_u32 v207, v181, v207\n\t"
        "v_min_u32 v208, v181, v208\n\t"
        "v_min_u32 v209, v181, v209\n\t"
        "v_mad_u32_u24 v207, v195, v180, v207\n\t"
        "v_mad_u32_u24 v208, v196, v180, v208\n\t"
        "v_mad_u32_u24 v209, v197, v180, v209\n\t"
        "ds_write2st64_b32 v207, v219, v231 offset1:10\n\t"
        "s_mov_b64 exec, s[80:81]\n\t"
        "ds_write2st64_b32 v208, v220, v232 offset1:10\n\t"
        "s_mov_b64 exec, s[82:83]\n\t"
        "ds_write2st64_b32 v209, v221, v233 offset1:10\n\t"
        "s_mov_b64 exec, -1\n\t"
        "ds_read_b128 v[184:187], %22\n\t"
        "ds_read_b32 v183, %23\n\t"
        "s_mov_b64 exec, s[70:71]\n\t"
        "v_cmpx_ne_u32 s[90:91], -1, v252\n\t"
        "v_mul_hi_u32_u24_sdwa v213, v252, %21 dst_sel:DWORD dst_unused:UNUSED_PAD src0_sel:WORD_0 src1_sel:DWORD\n\t"
        "v_and_b32_sdwa v201, v255, v176 dst_sel:DWORD dst_unused:UNUSED_PAD src0_sel:BYTE_0 src1_sel:DWORD\n\t"
        "v_lshl_add_u32 v213, v213, 2, v201\n\t"
        "ds_add_rtn_u32 v213, v213, v191\n\t"
        "v_perm_b32 v225, v255, v252, v177\n\t"
        "v_mad_u32_u16 v237, v252, 32, %16 op_sel:[1,0,0,0]\n\t"
        "v_cmpx_ne_u32 s[92:93], -1, v253\n\t"
        "v_mul_hi_u32_u24_sdwa v214, v253, %21 dst_sel:DWORD dst_unused:UNUSED_PAD src0_sel:WORD_0 src1_sel:DWORD\n\t"
        "v_and_b32_sdwa v202, v255, v176 dst_sel:DWORD dst_unused:UNUSED_PAD src0_sel:BYTE_1 src1_sel:DWORD\n\t"
        "v_lshl_add_u32 v214, v214, 2, v202\n\t"
        "ds_add_rtn_u32 v214, v214, v191\n\t"
        "v_perm_b32 v226, v255, v253, v178\n\t"
        "v_mad_u32_u16 v238, v253, 32, %16 op_sel:[1,0,0,0]\n\t"
        "s_and_b64 exec, exec, s[52:53]\n\t"
        "v_cmpx_ne_u32 s[94:95], -1, v254\n\t"
        "v_mul_hi_u32_u24_sdwa v215, v254, %21 dst_sel:DWORD dst_unused:UNUSED_PAD src0_sel:WORD_0 src1_sel:DWORD\n\t"
        "v_and_b32_sdwa v203, v255, v176 dst_sel:DWORD dst_unused:UNUSED_PAD src0_sel:BYTE_2 src1_sel:DWORD\n\t"
        "v_lshl_add_u32 v215, v215, 2, v203\n\t"
        "ds_add_rtn_u32 v215, v215, v191\n\t"
        "v_perm_b32 v227, v255, v254, v179\n\t"
        "v_mad_u32_u16 v239, v254, 32, %16 op_sel:[1,0,0,0]\n\t"
        "s_cmp_eq_u64 s[54:55], 0\n\t"
        "s_cbranch_scc1 59f\n\t"
        "s_mov_b64 exec, %0\n\t"
        "s_cbranch_execz 50f\n\t"
        "global_load_dwordx4 v[240:243], %4, %17 sc1\n\t"
        "50:\n\t"
        "s_mov_b64 exec, %1\n\t"
        "s_cbranch_execz 51f\n\t"
        "global_load_dwordx4 v[244:247], %5, %17 sc1\n\t"
        "51:\n\t"
        "s_mov_b64 exec, %2\n\t"
        "s_cbranch_execz 52f\n\t"
        "global_load_dwordx4 v[248:251], %6, %17 sc1\n\t"
        "52:\n\t"
        "s_mov_b64 exec, %3\n\t"
        "s_cbranch_execz 53f\n\t"
        "global_load_dwordx4 v[252:255], %7, %17 sc1\n\t"
        "53:\n\t"
        "59:\n\t"
        "s_mov_b64 exec, s[84:85]\n\t"
        "s_waitcnt lgkmcnt(8)\n\t"
        "v_min_u32 v210, v181, v210\n\t"
        "v_min_u32 v211, v181, v211\n\t"
        "v_min_u32 v212, v181, v212\n\t"
        "v_mad_u32_u24 v210, v198, v180, v210\n\t"
        "v_mad_u32_u24 v211, v199, v180, v211\n\t"
        "v_mad_u32_u24 v212, v200, v180, v212\n\t"
        "ds_write2st64_b32 v210, v222, v234 offset1:10\n\t"
        "s_mov_b64 exec, s[86:87]\n\t"
        "ds_write2st64_b32 v211, v223, v235 offset1:10\n\t"
        "s_mov_b64 exec, s[88:89]\n\t"
        "ds_write2st64_b32 v212, v224, v236 offset1:10\n\t"
        "s_mov_b64 exec, -1\n\t"
        "s_movk_i32 s96, 128\n\t"
        "s_mov_b32 m0, %29\n\t"
        "s_waitcnt lgkmcnt(6)\n\t"
        "v_max3_u32 v182, v184, v185, v186\n\t"
        "v_max3_u32 v182, v182, v187, v183\n\t"
        "v_cmp_eq_u32 vcc, -1, v182\n\t"
        "v_mad_u32_u16 v184, v184, s96, %24\n\t"
        "v_mad_u32_u16 v185, v185, s96, %25\n\t"
        "v_mad_u32_u16 v186, v186, s96, %24\n\t"
        "v_mad_u32_u16 v187, v187, s96, %25\n\t"
        "v_add_u32 v183, v183, %26\n\t"
        "s_cbranch_vccnz 60f\n\t"
        "global_load_lds_dwordx4 v184, %27\n\t"
        "s_addk_i32 m0, 1024\n\t"
        "s_nop 0\n\t"
        "global_load_lds_dwordx4 v185, %27\n\t"
        "s_addk_i32 m0, 1024\n\t"
        "s_nop 0\n\t"
        "global_load_lds_dwordx4 v186, %27\n\t"
        "s_addk_i32 m0, 1024\n\t"
        "s_nop 0\n\t"
        "global_load_lds_dwordx4 v187, %27\n\t"
        "s_addk_i32 m0, 1024\n\t"
        "s_nop 0\n\t"
        "global_load_lds_dwordx4 v183, %28\n\t"
        "s_mov_b32 %12, 1\n\t"
        "60:\n\t"
        "s_mov_b64 exec, s[90:91]\n\t"
        "s_waitcnt lgkmcnt(3)\n\t"
        "v_min_u32 v213, v181, v213\n\t"
        "v_min_u32 v214, v181, v214\n\t"
        "v_min_u32 v215, v181, v215\n\t"
        "v_mad_u32_u24 v213, v201, v180, v213\n\t"
        "v_mad_u32_u24 v214, v202, v180, v214\n\t"
        "v_mad_u32_u24 v215, v203, v180, v215\n\t"
        "ds_write2st64_b32 v213, v225, v237 offset1:10\n\t"
        "s_mov_b64 exec, s[92:93]\n\t"
        "ds_write2st64_b32 v214, v226, v238 offset1:10\n\t"
        "s_mov_b64 exec, s[94:95]\n\t"
        "ds_write2st64_b32 v215, v227, v239 offset1:10\n\t"
        "s_cmp_eq_u64 s[50:51], 0\n\t"
        "s_cbranch_scc1 49f\n\t"
        "s_mov_b64 exec, s[56:57]\n\t"
        "v_mov_b32 v188, 0xff000000\n\t"
        "v_mov_b32 v189, %13\n\t"
        "v_bfe_u32 v190, %4, 7, 8\n\t"
        "ds_write_b8 v190, v191 offset:256\n\t"
        "s_mov_b64 exec, s[58:59]\n\t"
        "v_mov_b32 v188, 0xfe000000\n\t"
        "v_mov_b32 v189, %14\n\t"
        "v_bfe_u32 v190, %5, 7, 8\n\t"
        "ds_write_b8 v190, v191 offset:256\n\t"
        "s_mov_b64 exec, s[60:61]\n\t"
        "v_mov_b32 v188, 0xfd000000\n\t"
        "v_mov_b32 v189, %15\n\t"
        "v_bfe_u32 v190, %6, 7, 8\n\t"
        "ds_write_b8 v190, v191 offset:256\n\t"
        "s_mov_b64 exec, s[62:63]\n\t"
        "v_mov_b32 v188, 0xfc000000\n\t"
        "v_mov_b32 v189, %16\n\t"
        "v_bfe_u32 v190, %7, 7, 8\n\t"
        "ds_write_b8 v190, v191 offset:256\n\t"
        "s_mov_b64 exec, s[50:51]\n\t"
        "s_waitcnt vmcnt(5)\n\t"
        "s_cmp_lg_u32 %12, 0\n\t"
        "s_cbranch_scc1 47f\n\t"
        "s_waitcnt vmcnt(0)\n\t"
        "47:\n\t"
        "v_add_u32 v175, v175, v188\n\t"
        "v_add3_u32 v228, v172, v173, v174\n\t"
        "v_add3_u32 v228, v228, v175, %19\n\t"
        "v_cmp_ne_u32_sdwa s[72:73], v175, %20 src0_sel:BYTE_3 src1_sel:DWORD\n\t"
        "s_nop 0\n\t"
        "v_add_u32_dpp v228, v228, v228 quad_perm:[1,0,3,2] row_mask:0xf bank_mask:0xf\n\t"
        "s_nop 1\n\t"
        "v_add_u32_dpp v228, v228, v228 quad_perm:[2,3,0,1] row_mask:0xf bank_mask:0xf\n\t"
        "s_nop 1\n\t"
        "v_add_u32_dpp v228, v228, v228 row_half_mirror row_mask:0xf bank_mask:0xf\n\t"
        "v_cmp_eq_u32 s[68:69], 0, v228\n\t"
        "s_and_b64 s[72:73], s[72:73], s[68:69]\n\t"
        "s_cselect_b64 exec, 0, s[68:69]\n\t"
        "s_cbranch_execz 48f\n\t"
        "v_cmpx_ne_u32 s[72:73], -1, v172\n\t"
        "v_mul_hi_u32_u24_sdwa v204, v172, %21 dst_sel:DWORD dst_unused:UNUSED_PAD src0_sel:WORD_0 src1_sel:DWORD\n\t"
        "v_and_b32_sdwa v192, v175, v176 dst_sel:DWORD dst_unused:UNUSED_PAD src0_sel:BYTE_0 src1_sel:DWORD\n\t"
        "v_lshl_add_u32 v204, v204, 2, v192\n\t"
        "ds_add_rtn_u32 v204, v204, v191\n\t"
        "v_perm_b32 v216, v175, v172, v177\n\t"
        "v_mad_u32_u16 v228, v172, 32, v189 op_sel:[1,0,0,0]\n\t"
        "v_cmpx_ne_u32 s[74:75], -1, v173\n\t"
        "v_mul_hi_u32_u24_sdwa v205, v173, %21 dst_sel:DWORD dst_unused:UNUSED_PAD src0_sel:WORD_0 src1_sel:DWORD\n\t"
        "v_and_b32_sdwa v193, v175, v176 dst_sel:DWORD dst_unused:UNUSED_PAD src0_sel:BYTE_1 src1_sel:DWORD\n\t"
        "v_lshl_add_u32 v205, v205, 2, v193\n\t"
        "ds_add_rtn_u32 v205, v205, v191\n\t"
        "v_perm_b32 v217, v175, v173, v178\n\t"
        "v_mad_u32_u16 v229, v173, 32, v189 op_sel:[1,0,0,0]\n\t"
        "s_and_b64 exec, exec, s[52:53]\n\t"
        "v_cmpx_ne_u32 s[76:77], -1, v174\n\t"
        "v_mul_hi_u32_u24_sdwa v206, v174, %21 dst_sel:DWORD dst_unused:UNUSED_PAD src0_sel:WORD_0 src1_sel:DWORD\n\t"
        "v_and_b32_sdwa v194, v175, v176 dst_sel:DWORD dst_unused:UNUSED_PAD src0_sel:BYTE_2 src1_sel:DWORD\n\t"
        "v_lshl_add_u32 v206, v206, 2, v194\n\t"
        "ds_add_rtn_u32 v206, v206, v191\n\t"
        "v_perm_b32 v218, v175, v174, v179\n\t"
        "v_mad_u32_u16 v230, v174, 32, v189 op_sel:[1,0,0,0]\n\t"
        "s_mov_b64 s[50:51], 0\n\t"
        "s_and_b64 s[70:71], s[68:69], s[62:63]\n\t"
        "s_xor_b64 s[62:63], s[62:63], s[70:71]\n\t"
        "s_xor_b64 s[68:69], s[68:69], s[70:71]\n\t"
        "s_or_b64 s[50:51], s[50:51], s[62:63]\n\t"
        "s_and_b64 s[70:71], s[68:69], s[60:61]\n\t"
        "s_xor_b64 s[60:61], s[60:61], s[70:71]\n\t"
        "s_xor_b64 s[68:69], s[68:69], s[70:71]\n\t"
        "s_or_b64 s[50:51], s[50:51], s[60:61]\n\t"
        "s_and_b64 s[70:71], s[68:69], s[58:59]\n\t"
        "s_xor_b64 s[58:59], s[58:59], s[70:71]\n\t"
        "s_xor_b64 s[68:69], s[68:69], s[70:71]\n\t"
        "s_or_b64 s[50:51], s[50:51], s[58:59]\n\t"
        "s_and_b64 s[70:71], s[68:69], s[56:57]\n\t"
        "s_xor_b64 s[56:57], s[56:57], s[70:71]\n\t"
        "s_or_b64 s[50:51], s[50:51], s[56:57]\n\t"
        "s_mov_b64 exec, s[72:73]\n\t"
        "s_waitcnt lgkmcnt(0)\n\t"
        "v_min_u32 v204, v181, v204\n\t"
        "v_min_u32 v205, v181, v205\n\t"
        "v_min_u32 v206, v181, v206\n\t"
        "v_mad_u32_u24 v204, v192, v180, v204\n\t"
        "v_mad_u32_u24 v205, v193, v180, v205\n\t"
        "v_mad_u32_u24 v206, v194, v180, v206\n\t"
        "ds_write2st64_b32 v204, v216, v228 offset1:10\n\t"
        "s_mov_b64 exec, s[74:75]\n\t"
        "ds_write2st64_b32 v205, v217, v229 offset1:10\n\t"
        "s_mov_b64 exec, s[76:77]\n\t"
        "ds_write2st64_b32 v206, v218, v230 offset1:10\n\t"
        "s_cbranch_scc0 49f\n\t"
        "48:\n\t"
        "s_mov_b32 s96, 0x1000000\n\t"
        "s_or_b32 %12, %12, 2\n\t"
        "s_mov_b64 exec, s[56:57]\n\t"
        "s_cbranch_execz 40f\n\t"
        "v_add_u32 %4, s96, %4\n\t"
        "s_or_b64 %0, %0, exec\n\t"
        "global_load_dwordx4 v[240:243], %4, %17 sc1\n\t"
        "40:\n\t"
        "s_mov_b64 exec, s[58:59]\n\t"
        "s_cbranch_execz 41f\n\t"
        "v_add_u32 %5, s96, %5\n\t"
        "s_or_b64 %1, %1, exec\n\t"
        "global_load_dwordx4 v[244:247], %5, %17 sc1\n\t"
        "41:\n\t"
        "s_mov_b64 exec, s[60:61]\n\t"
        "s_cbranch_execz 42f\n\t"
        "v_add_u32 %6, s96, %6\n\t"
        "s_or_b64 %2, %2, exec\n\t"
        "global_load_dwordx4 v[248:251], %6, %17 sc1\n\t"
        "42:\n\t"
        "s_mov_b64 exec, s[62:63]\n\t"
        "s_cbranch_execz 43f\n\t"
        "v_add_u32 %7, s96, %7\n\t"
        "s_or_b64 %3, %3, exec\n\t"
        "global_load_dwordx4 v[252:255], %7, %17 sc1\n\t"
        "43:\n\t"
        "49:\n\t"
        "s_or_b32 s54, s54, s55\n\t"
        "s_or_b32 s54, s54, %12\n\t"
        "s_cbranch_scc1 60f\n\t"
        "s_movk_i32 s54, 12\n\t"
        "61:\n\t"
        "s_sub_u32 s54, s54, 1\n\t"
        "s_cbranch_scc1 60f\n\t"
        "s_mov_b64 exec, -1\n\t"
        "ds_read_b128 v[184:187], %22\n\t"
        "ds_read_b32 v183, %23\n\t"
        "s_mov_b64 exec, -1\n\t"
        "s_movk_i32 s96, 128\n\t"
        "s_mov_b32 m0, %29\n\t"
        "s_waitcnt lgkmcnt(0)\n\t"
        "v_max3_u32 v182, v184, v185, v186\n\t"
        "v_max3_u32 v182, v182, v187, v183\n\t"
        "v_cmp_eq_u32 vcc, -1, v182\n\t"
        "v_mad_u32_u16 v184, v184, s96, %24\n\t"
        "v_mad_u32_u16 v185, v185, s96, %25\n\t"
        "v_mad_u32_u16 v186, v186, s96, %24\n\t"
        "v_mad_u32_u16 v187, v187, s96, %25\n\t"
        "v_add_u32 v183, v183, %26\n\t"
        "s_cbranch_vccnz 61b\n\t"
        "global_load_lds_dwordx4 v184, %27\n\t"
        "s_addk_i32 m0, 1024\n\t"
        "s_nop 0\n\t"
        "global_load_lds_dwordx4 v185, %27\n\t"
        "s_addk_i32 m0, 1024\n\t"
        "s_nop 0\n\t"
        "global_load_lds_dwordx4 v186, %27\n\t"
        "s_addk_i32 m0, 1024\n\t"
        "s_nop 0\n\t"
        "global_load_lds_dwordx4 v187, %27\n\t"
        "s_addk_i32 m0, 1024\n\t"
        "s_nop 0\n\t"
        "global_load_lds_dwordx4 v183, %28\n\t"
        "s_mov_b32 %12, 1\n\t"
        "60:\n\t"
        "s_mov_b64 exec, -1\n\t"
        : "=&s"(pend[0]), "=&s"(pend[1]), "=&s"(pend[2]), "=&s"(pend[3]), "+v"(vo[0]), "+v"(vo[1]), "+v"(vo[2]),
          "+v"(vo[3]), "+{v[240:243]}"(cv[0]), "+{v[244:247]}"(cv[1]), "+{v[248:251]}"(cv[2]), "+{v[252:255]}"(cv[3]),
          "=&s"(early)
        : "v"(eb[0]), "v"(eb[1]), "v"(eb[2]), "v"(eb[3]), "s"(ib), "n"(OFF_L + LARR - 4), "v"(tag[0]), "v"(tag[1]),
          "s"(sq), "v"(aLX + wave * LIST_BYTES), "v"(aLE + wave * LIST_BYTES), "v"(cx0), "v"(cx1), "v"(ce), "s"(x),
          "s"(ES), "s"(lds0 + OFF_R0 + wave * RING_SLOT), "v"(32768u + wave * 1024u + lane * 16u)
        :
          "memory", "vcc", "scc", "m0", "v172", "v173", "v174", "v175", "v176", "v177", "v178", "v179", "v180",
          "v181", "v182", "v183", "v184", "v185", "v186", "v187", "v188", "v189", "v190", "v191", "v192", "v193",
          "v194", "v195", "v196", "v197", "v198", "v199", "v200", "v201", "v202", "v203", "v204", "v205", "v206",
          "v207", "v208", "v209", "v210", "v211", "v212", "v213", "v214", "v215", "v216", "v217", "v218", "v219",
          "v220", "v221", "v222", "v223", "v224", "v225", "v226", "v227", "v228", "v229", "v230", "v231", "v232",
          "v233", "v234", "v235", "v236", "v237", "v238", "v239", "s50", "s51", "s52", "s53", "s54", "s55", "s56",
          "s57", "s58", "s59", "s60", "s61", "s62", "s63", "s64", "s65", "s66", "s67", "s68", "s69", "s70", "s71",
          "s72", "s73", "s74", "s75", "s76", "s77", "s78", "s79", "s80", "s81", "s82", "s83", "s84", "s85", "s86",
          "s87", "s88", "s89", "s90", "s91", "s92", "s93", "s94", "s95", "s96");
  auto uni = [&]() {
#pragma unroll
    for (int k = 0; k < 4; ++k)
      pend[k] = ((unsigned long long)(unsigned)__builtin_amdgcn_readfirstlane((unsigned)(pend[k] >> 32)) << 32) |
                (unsigned)__builtin_amdgcn_readfirstlane((unsigned)pend[k]);
  };
  uni();
  early = __builtin_amdgcn_readfirstlane(early);
  just = early;
  if (__builtin_expect((pend[0] | pend[1] | pend[2] | pend[3] | (unsigned long long)(early ^ 1u)) != 0ull, 0))
#pragma clang loop unroll(disable)
  while (true) {
    early &= 1u;
    if (!early) {
      const u32x4 sx = ld128(aLX + wave * LIST_BYTES);
      const unsigned re = *(const LAS unsigned*)(size_t)(aLE + wave * LIST_BYTES);
      if (__ballot(max(max(max(sx[0], sx[1]), max(sx[2], sx[3])), re) == ~0u) == 0ull) {
        gather5(sx, re, lds0 + OFF_R0 + wave * RING_SLOT);
        early = 1u;
        just = 1u;
      }
    }
    if ((pend[0] | pend[1] | pend[2] | pend[3]) == 0ull || --spin < 0) break;
    asm volatile(
        "s_mov_b32 s96, 0x1000000\n\t"
        "v_mov_b32 v181, %17\n\t"
        "v_mov_b32 v180, 160\n\t"
        "v_mov_b32 v176, 0xe0\n\t"
        "v_mov_b32 v177, 0x0c040100\n\t"
        "v_mov_b32 v178, 0x0c050100\n\t"
        "v_mov_b32 v179, 0x0c060100\n\t"
        "s_mov_b32 s52, 0x7f7f7f7f\n\t"
        "s_mov_b32 s53, 0x7f7f7f7f\n\t"
        "v_mov_b32 v191, 4\n\t"
        "s_waitcnt vmcnt(5)\n\t"
        "s_cmp_eq_u32 %21, 1\n\t"
        "s_cbranch_scc1 9f\n\t"
        "s_waitcnt vmcnt(0)\n\t"
        "9:\n\t"
        "v_add3_u32 v228, v240, v241, v242\n\t"
        "v_add3_u32 v229, v244, v245, v246\n\t"
        "v_add3_u32 v230, v248, v249, v250\n\t"
        "v_add3_u32 v231, v252, v253, v254\n\t"
        "v_add3_u32 v228, v228, v243, %18\n\t"
        "v_add3_u32 v229, v229, v247, %18\n\t"
        "v_add3_u32 v230, v230, v251, %18\n\t"
        "v_add3_u32 v231, v231, v255, %18\n\t"
        "v_add_u32_dpp v228, v228, v228 quad_perm:[1,0,3,2] row_mask:0xf bank_mask:0xf\n\t"
        "v_add_u32_dpp v229, v229, v229 quad_perm:[1,0,3,2] row_mask:0xf bank_mask:0xf\n\t"
        "v_add_u32_dpp v230, v230, v230 quad_perm:[1,0,3,2] row_mask:0xf bank_mask:0xf\n\t"
        "v_add_u32_dpp v231, v231, v231 quad_perm:[1,0,3,2] row_mask:0xf bank_mask:0xf\n\t"
        "v_add_u32_dpp v228, v228, v228 quad_perm:[2,3,0,1] row_mask:0xf bank_mask:0xf\n\t"
        "v_add_u32_dpp v229, v229, v229 quad_perm:[2,3,0,1] row_mask:0xf bank_mask:0xf\n\t"
        "v_add_u32_dpp v230, v230, v230 quad_perm:[2,3,0,1] row_mask:0xf bank_mask:0xf\n\t"
        "v_add_u32_dpp v231, v231, v231 quad_perm:[2,3,0,1] row_mask:0xf bank_mask:0xf\n\t"
        "v_add_u32_dpp v228, v228, v228 row_half_mirror row_mask:0xf bank_mask:0xf\n\t"
        "v_add_u32_dpp v229, v229, v229 row_half_mirror row_mask:0xf bank_mask:0xf\n\t"
        "v_add_u32_dpp v230, v230, v230 row_half_mirror row_mask:0xf bank_mask:0xf\n\t"
        "v_add_u32_dpp v231, v231, v231 row_half_mirror row_mask:0xf bank_mask:0xf\n\t"
        "v_cmp_ne_u32_sdwa s[72:73], v243, %19 src0_sel:BYTE_3 src1_sel:DWORD\n\t"
        "v_cmp_gt_u32 s[64:65], 2, v228\n\t"
        "v_cmp_eq_u32 s[56:57], 1, v228\n\t"
        "v_cmp_ne_u32_sdwa s[74:75], v247, %19 src0_sel:BYTE_3 src1_sel:DWORD\n\t"
        "v_cmp_gt_u32 s[66:67], 2, v229\n\t"
        "v_cmp_eq_u32 s[58:59], 1, v229\n\t"
        "v_cmp_ne_u32_sdwa s[76:77], v251, %19 src0_sel:BYTE_3 src1_sel:DWORD\n\t"
        "v_cmp_gt_u32 s[68:69], 2, v230\n\t"
        "v_cmp_eq_u32 s[60:61], 1, v230\n\t"
        "v_cmp_ne_u32_sdwa s[78:79], v255, %19 src0_sel:BYTE_3 src1_sel:DWORD\n\t"
        "v_cmp_gt_u32 s[70:71], 2, v231\n\t"
        "v_cmp_eq_u32 s[62:63], 1, v231\n\t"
        "s_and_b64 s[64:65], s[64:65], %0\n\t"
        "s_and_b64 s[72:73], s[72:73], s[64:65]\n\t"
        "s_cselect_b64 s[64:65], 0, s[64:65]\n\t"
        "s_and_b64 s[56:57], s[56:57], s[64:65]\n\t"
        "s_andn2_b64 %0, %0, s[64:65]\n\t"
        "s_cbranch_scc0 10f\n\t"
        "s_mov_b64 exec, %0\n\t"
        "global_load_dwordx4 v[240:243], %4, %16 sc1\n\t"
        "10:\n\t"
        "s_and_b64 s[66:67], s[66:67], %1\n\t"
        "s_and_b64 s[74:75], s[74:75], s[66:67]\n\t"
        "s_cselect_b64 s[66:67], 0, s[66:67]\n\t"
        "s_and_b64 s[58:59], s[58:59], s[66:67]\n\t"
        "s_andn2_b64 %1, %1, s[66:67]\n\t"
        "s_cbranch_scc0 11f\n\t"
        "s_mov_b64 exec, %1\n\t"
        "global_load_dwordx4 v[244:247], %5, %16 sc1\n\t"
        "11:\n\t"
        "s_and_b64 s[68:69], s[68:69], %2\n\t"
        "s_and_b64 s[76:77], s[76:77], s[68:69]\n\t"
        "s_cselect_b64 s[68:69], 0, s[68:69]\n\t"
        "s_and_b64 s[60:61], s[60:61], s[68:69]\n\t"
        "s_andn2_b64 %2, %2, s[68:69]\n\t"
        "s_cbranch_scc0 12f\n\t"
        "s_mov_b64 exec, %2\n\t"
        "global_load_dwordx4 v[248:251], %6, %16 sc1\n\t"
        "12:\n\t"
        "s_and_b64 s[70:71], s[70:71], %3\n\t"
        "s_and_b64 s[78:79], s[78:79], s[70:71]\n\t"
        "s_cselect_b64 s[70:71], 0, s[70:71]\n\t"
        "s_and_b64 s[62:63], s[62:63], s[70:71]\n\t"
        "s_andn2_b64 %3, %3, s[70:71]\n\t"
        "s_cbranch_scc0 13f\n\t"
        "s_mov_b64 exec, %3\n\t"
        "global_load_dwordx4 v[252:255], %7, %16 sc1\n\t"
        "13:\n\t"
        "s_mov_b64 s[54:55], 0\n\t"
        "s_mov_b64 s[72:73], 0\n\t"
        "s_mov_b64 exec, s[64:65]\n\t"
        "s_cbranch_execz 20f\n\t"
        "v_cmpx_ne_u32 s[72:73], -1, v240\n\t"
        "v_mul_hi_u32_u24_sdwa v204, v240, %20 dst_sel:DWORD dst_unused:UNUSED_PAD src0_sel:WORD_0 src1_sel:DWORD\n\t"
        "v_and_b32_sdwa v192, v243, v176 dst_sel:DWORD dst_unused:UNUSED_PAD src0_sel:BYTE_0 src1_sel:DWORD\n\t"
        "v_lshl_add_u32 v204, v204, 2, v192\n\t"
        "ds_add_rtn_u32 v204, v204, v191\n\t"
        "v_perm_b32 v216, v243, v240, v177\n\t"
        "v_mad_u32_u16 v228, v240, 32, %12 op_sel:[1,0,0,0]\n\t"
        "v_cmpx_ne_u32 s[74:75], -1, v241\n\t"
        "v_mul_hi_u32_u24_sdwa v205, v241, %20 dst_sel:DWORD dst_unused:UNUSED_PAD src0_sel:WORD_0 src1_sel:DWORD\n\t"
        "v_and_b32_sdwa v193, v243, v176 dst_sel:DWORD dst_unused:UNUSED_PAD src0_sel:BYTE_1 src1_sel:DWORD\n\t"
        "v_lshl_add_u32 v205, v205, 2, v193\n\t"
        "ds_add_rtn_u32 v205, v205, v191\n\t"
        "v_perm_b32 v217, v243, v241, v178\n\t"
        "v_mad_u32_u16 v229, v241, 32, %12 op_sel:[1,0,0,0]\n\t"
        "s_and_b64 exec, exec, s[52:53]\n\t"
        "v_cmpx_ne_u32 s[76:77], -1, v242\n\t"
        "v_mul_hi_u32_u24_sdwa v206, v242, %20 dst_sel:DWORD dst_unused:UNUSED_PAD src0_sel:WORD_0 src1_sel:DWORD\n\t"
        "v_and_b32_sdwa v194, v243, v176 dst_sel:DWORD dst_unused:UNUSED_PAD src0_sel:BYTE_2 src1_sel:DWORD\n\t"
        "v_lshl_add_u32 v206, v206, 2, v194\n\t"
        "ds_add_rtn_u32 v206, v206, v191\n\t"
        "v_perm_b32 v218, v243, v242, v179\n\t"
        "v_mad_u32_u16 v230, v242, 32, %12 op_sel:[1,0,0,0]\n\t"
        "s_or_b64 s[54:55], s[54:55], s[56:57]\n\t"
        "20:\n\t"
        "s_mov_b64 s[78:79], 0\n\t"
        "s_mov_b64 exec, s[66:67]\n\t"
        "s_cbranch_execz 21f\n\t"
        "v_cmpx_ne_u32 s[78:79], -1, v244\n\t"
        "v_mul_hi_u32_u24_sdwa v207, v244, %20 dst_sel:DWORD dst_unused:UNUSED_PAD src0_sel:WORD_0 src1_sel:DWORD\n\t"
        "v_and_b32_sdwa v195, v247, v176 dst_sel:DWORD dst_unused:UNUSED_PAD src0_sel:BYTE_0 src1_sel:DWORD\n\t"
        "v_lshl_add_u32 v207, v207, 2, v195\n\t"
        "ds_add_rtn_u32 v207, v207, v191\n\t"
        "v_perm_b32 v219, v247, v244, v177\n\t"
        "v_mad_u32_u16 v231, v244, 32, %13 op_sel:[1,0,0,0]\n\t"
        "v_cmpx_ne_u32 s[80:81], -1, v245\n\t"
        "v_mul_hi_u32_u24_sdwa v208, v245, %20 dst_sel:DWORD dst_unused:UNUSED_PAD src0_sel:WORD_0 src1_sel:DWORD\n\t"
        "v_and_b32_sdwa v196, v247, v176 dst_sel:DWORD dst_unused:UNUSED_PAD src0_sel:BYTE_1 src1_sel:DWORD\n\t"
        "v_lshl_add_u32 v208, v208, 2, v196\n\t"
        "ds_add_rtn_u32 v208, v208, v191\n\t"
        "v_perm_b32 v220, v247, v245, v178\n\t"
        "v_mad_u32_u16 v232, v245, 32, %13 op_sel:[1,0,0,0]\n\t"
        "s_and_b64 exec, exec, s[52:53]\n\t"
        "v_cmpx_ne_u32 s[82:83], -1, v246\n\t"
        "v_mul_hi_u32_u24_sdwa v209, v246, %20 dst_sel:DWORD dst_unused:UNUSED_PAD src0_sel:WORD_0 src1_sel:DWORD\n\t"
        "v_and_b32_sdwa v197, v247, v176 dst_sel:DWORD dst_unused:UNUSED_PAD src0_sel:BYTE_2 src1_sel:DWORD\n\t"
        "v_lshl_add_u32 v209, v209, 2, v197\n\t"
        "ds_add_rtn_u32 v209, v209, v191\n\t"
        "v_perm_b32 v221, v247, v246, v179\n\t"
        "v_mad_u32_u16 v233, v246, 32, %13 op_sel:[1,0,0,0]\n\t"
        "s_or_b64 s[54:55], s[54:55], s[58:59]\n\t"
        "21:\n\t"
        "s_mov_b64 s[84:85], 0\n\t"
        "s_mov_b64 exec, s[68:69]\n\t"
        "s_cbranch_execz 22f\n\t"
        "v_cmpx_ne_u32 s[84:85], -1, v248\n\t"
        "v_mul_hi_u32_u24_sdwa v210, v248, %20 dst_sel:DWORD dst_unused:UNUSED_PAD src0_sel:WORD_0 src1_sel:DWORD\n\t"
        "v_and_b32_sdwa v198, v251, v176 dst_sel:DWORD dst_unused:UNUSED_PAD src0_sel:BYTE_0 src1_sel:DWORD\n\t"
        "v_lshl_add_u32 v210, v210, 2, v198\n\t"
        "ds_add_rtn_u32 v210, v210, v191\n\t"
        "v_perm_b32 v222, v251, v248, v177\n\t"
        "v_mad_u32_u16 v234, v248, 32, %14 op_sel:[1,0,0,0]\n\t"
        "v_cmpx_ne_u32 s[86:87], -1, v249\n\t"
        "v_mul_hi_u32_u24_sdwa v211, v249, %20 dst_sel:DWORD dst_unused:UNUSED_PAD src0_sel:WORD_0 src1_sel:DWORD\n\t"
        "v_and_b32_sdwa v199, v251, v176 dst_sel:DWORD dst_unused:UNUSED_PAD src0_sel:BYTE_1 src1_sel:DWORD\n\t"
        "v_lshl_add_u32 v211, v211, 2, v199\n\t"
        "ds_add_rtn_u32 v211, v211, v191\n\t"
        "v_perm_b32 v223, v251, v249, v178\n\t"
        "v_mad_u32_u16 v235, v249, 32, %14 op_sel:[1,0,0,0]\n\t"
        "s_and_b64 exec, exec, s[52:53]\n\t"
        "v_cmpx_ne_u32 s[88:89], -1, v250\n\t"
        "v_mul_hi_u32_u24_sdwa v212, v250, %20 dst_sel:DWORD dst_unused:UNUSED_PAD src0_sel:WORD_0 src1_sel:DWORD\n\t"
        "v_and_b32_sdwa v200, v251, v176 dst_sel:DWORD dst_unused:UNUSED_PAD src0_sel:BYTE_2 src1_sel:DWORD\n\t"
        "v_lshl_add_u32 v212, v212, 2, v200\n\t"
        "ds_add_rtn_u32 v212, v212, v191\n\t"
        "v_perm_b32 v224, v251, v250, v179\n\t"
        "v_mad_u32_u16 v236, v250, 32, %14 op_sel:[1,0,0,0]\n\t"
        "s_or_b64 s[54:55], s[54:55], s[60:61]\n\t"
        "22:\n\t"
        "s_mov_b64 s[90:91], 0\n\t"
        "s_mov_b64 exec, s[70:71]\n\t"
        "s_cbranch_execz 23f\n\t"
        "v_cmpx_ne_u32 s[90:91], -1, v252\n\t"
        "v_mul_hi_u32_u24_sdwa v213, v252, %20 dst_sel:DWORD dst_unused:UNUSED_PAD src0_sel:WORD_0 src1_sel:DWORD\n\t"
        "v_and_b32_sdwa v201, v255, v176 dst_sel:DWORD dst_unused:UNUSED_PAD src0_sel:BYTE_0 src1_sel:DWORD\n\t"
        "v_lshl_add_u32 v213, v213, 2, v201\n\t"
        "ds_add_rtn_u32 v213, v213, v191\n\t"
        "v_perm_b32 v225, v255, v252, v177\n\t"
        "v_mad_u32_u16 v237, v252, 32, %15 op_sel:[1,0,0,0]\n\t"
        "v_cmpx_ne_u32 s[92:93], -1, v253\n\t"
        "v_mul_hi_u32_u24_sdwa v214, v253, %20 dst_sel:DWORD dst_unused:UNUSED_PAD src0_sel:WORD_0 src1_sel:DWORD\n\t"
        "v_and_b32_sdwa v202, v255, v176 dst_sel:DWORD dst_unused:UNUSED_PAD src0_sel:BYTE_1 src1_sel:DWORD\n\t"
        "v_lshl_add_u32 v214, v214, 2, v202\n\t"
        "ds_add_rtn_u32 v214, v214, v191\n\t"
        "v_perm_b32 v226, v255, v253, v178\n\t"
        "v_mad_u32_u16 v238, v253, 32, %15 op_sel:[1,0,0,0]\n\t"
        "s_and_b64 exec, exec, s[52:53]\n\t"
        "v_cmpx_ne_u32 s[94:95], -1, v254\n\t"
        "v_mul_hi_u32_u24_sdwa v215, v254, %20 dst_sel:DWORD dst_unused:UNUSED_PAD src0_sel:WORD_0 src1_sel:DWORD\n\t"
        "v_and_b32_sdwa v203, v255, v176 dst_sel:DWORD dst_unused:UNUSED_PAD src0_sel:BYTE_2 src1_sel:DWORD\n\t"
        "v_lshl_add_u32 v215, v215, 2, v203\n\t"
        "ds_add_rtn_u32 v215, v215, v191\n\t"
        "v_perm_b32 v227, v255, v254, v179\n\t"
        "v_mad_u32_u16 v239, v254, 32, %15 op_sel:[1,0,0,0]\n\t"
        "s_or_b64 s[54:55], s[54:55], s[62:63]\n\t"
        "23:\n\t"
        "s_cmp_eq_u64 s[54:55], 0\n\t"
        "s_cbranch_scc1 59f\n\t"
        "s_mov_b64 exec, s[56:57]\n\t"
        "s_cbranch_execz 40f\n\t"
        "v_bfe_u32 v188, %4, 7, 8\n\t"
        "v_lshrrev_b32 v189, 24, %4\n\t"
        "v_add_u32 v189, 1, v189\n\t"
        "ds_write_b8 v188, v189 offset:256\n\t"
        "v_add_u32 %4, s96, %4\n\t"
        "v_mov_b32 v190, 0\n\t"
        "v_cmpx_eq_u32 vcc, 2, v189\n\t"
        "ds_write_b32 v190, v191 offset:20\n\t"
        "s_andn2_b64 exec, s[56:57], vcc\n\t"
        "s_cbranch_execz 40f\n\t"
        "s_or_b64 %0, %0, exec\n\t"
        "global_load_dwordx4 v[240:243], %4, %16 sc1\n\t"
        "40:\n\t"
        "s_mov_b64 exec, s[58:59]\n\t"
        "s_cbranch_execz 41f\n\t"
        "v_bfe_u32 v188, %5, 7, 8\n\t"
        "v_lshrrev_b32 v189, 24, %5\n\t"
        "v_add_u32 v189, 1, v189\n\t"
        "ds_write_b8 v188, v189 offset:256\n\t"
        "v_add_u32 %5, s96, %5\n\t"
        "v_mov_b32 v190, 0\n\t"
        "v_cmpx_eq_u32 vcc, 2, v189\n\t"
        "ds_write_b32 v190, v191 offset:20\n\t"
        "s_andn2_b64 exec, s[58:59], vcc\n\t"
        "s_cbranch_execz 41f\n\t"
        "s_or_b64 %1, %1, exec\n\t"
        "global_load_dwordx4 v[244:247], %5, %16 sc1\n\t"
        "41:\n\t"
        "s_mov_b64 exec, s[60:61]\n\t"
        "s_cbranch_execz 42f\n\t"
        "v_bfe_u32 v188, %6, 7, 8\n\t"
        "v_lshrrev_b32 v189, 24, %6\n\t"
        "v_add_u32 v189, 1, v189\n\t"
        "ds_write_b8 v188, v189 offset:256\n\t"
        "v_add_u32 %6, s96, %6\n\t"
        "v_mov_b32 v190, 0\n\t"
        "v_cmpx_eq_u32 vcc, 2, v189\n\t"
        "ds_write_b32 v190, v191 offset:20\n\t"
        "s_andn2_b64 exec, s[60:61], vcc\n\t"
        "s_cbranch_execz 42f\n\t"
        "s_or_b64 %2, %2, exec\n\t"
        "global_load_dwordx4 v[248:251], %6, %16 sc1\n\t"
        "42:\n\t"
        "s_mov_b64 exec, s[62:63]\n\t"
        "s_cbranch_execz 43f\n\t"
        "v_bfe_u32 v188, %7, 7, 8\n\t"
        "v_lshrrev_b32 v189, 24, %7\n\t"
        "v_add_u32 v189, 1, v189\n\t"
        "ds_write_b8 v188, v189 offset:256\n\t"
        "v_add_u32 %7, s96, %7\n\t"
        "v_mov_b32 v190, 0\n\t"
        "v_cmpx_eq_u32 vcc, 2, v189\n\t"
        "ds_write_b32 v190, v191 offset:20\n\t"
        "s_andn2_b64 exec, s[62:63], vcc\n\t"
        "s_cbranch_execz 43f\n\t"
        "s_or_b64 %3, %3, exec\n\t"
        "global_load_dwordx4 v[252:255], %7, %16 sc1\n\t"
        "43:\n\t"
        "59:\n\t"
        "s_waitcnt lgkmcnt(0)\n\t"
        "s_mov_b64 exec, s[72:73]\n\t"
        "s_cbranch_execz 30f\n\t"
        "v_min_u32 v204, v181, v204\n\t"
        "v_min_u32 v205, v181, v205\n\t"
        "v_min_u32 v206, v181, v206\n\t"
        "v_mad_u32_u24 v204, v192, v180, v204\n\t"
        "v_mad_u32_u24 v205, v193, v180, v205\n\t"
        "v_mad_u32_u24 v206, v194, v180, v206\n\t"
        "ds_write2st64_b32 v204, v216, v228 offset1:10\n\t"
        "s_mov_b64 exec, s[74:75]\n\t"
        "ds_write2st64_b32 v205, v217, v229 offset1:10\n\t"
        "s_mov_b64 exec, s[76:77]\n\t"
        "ds_write2st64_b32 v206, v218, v230 offset1:10\n\t"
        "30:\n\t"
        "s_mov_b64 exec, s[78:79]\n\t"
        "s_cbranch_execz 31f\n\t"
        "v_min_u32 v207, v181, v207\n\t"
        "v_min_u32 v208, v181, v208\n\t"
        "v_min_u32 v209, v181, v209\n\t"
        "v_mad_u32_u24 v207, v195, v180, v207\n\t"
        "v_mad_u32_u24 v208, v196, v180, v208\n\t"
        "v_mad_u32_u24 v209, v197, v180, v209\n\t"
        "ds_write2st64_b32 v207, v219, v231 offset1:10\n\t"
        "s_mov_b64 exec, s[80:81]\n\t"
        "ds_write2st64_b32 v208, v220, v232 offset1:10\n\t"
        "s_mov_b64 exec, s[82:83]\n\t"
        "ds_write2st64_b32 v209, v221, v233 offset1:10\n\t"
        "31:\n\t"
        "s_mov_b64 exec, s[84:85]\n\t"
        "s_cbranch_execz 32f\n\t"
        "v_min_u32 v210, v181, v210\n\t"
        "v_min_u32 v211, v181, v211\n\t"
        "v_min_u32 v212, v181, v212\n\t"
        "v_mad_u32_u24 v210, v198, v180, v210\n\t"
        "v_mad_u32_u24 v211, v199, v180, v211\n\t"
        "v_mad_u32_u24 v212, v200, v180, v212\n\t"
        "ds_write2st64_b32 v210, v222, v234 offset1:10\n\t"
        "s_mov_b64 exec, s[86:87]\n\t"
        "ds_write2st64_b32 v211, v223, v235 offset1:10\n\t"
        "s_mov_b64 exec, s[88:89]\n\t"
        "ds_write2st64_b32 v212, v224, v236 offset1:10\n\t"
        "32:\n\t"
        "s_mov_b64 exec, s[90:91]\n\t"
        "s_cbranch_execz 33f\n\t"
        "v_min_u32 v213, v181, v213\n\t"
        "v_min_u32 v214, v181, v214\n\t"
        "v_min_u32 v215, v181, v215\n\t"
        "v_mad_u32_u24 v213, v201, v180, v213\n\t"
        "v_mad_u32_u24 v214, v202, v180, v214\n\t"
        "v_mad_u32_u24 v215, v203, v180, v215\n\t"
        "ds_write2st64_b32 v213, v225, v237 offset1:10\n\t"
        "s_mov_b64 exec, s[92:93]\n\t"
        "ds_write2st64_b32 v214, v226, v238 offset1:10\n\t"
        "s_mov_b64 exec, s[94:95]\n\t"
        "ds_write2st64_b32 v215, v227, v239 offset1:10\n\t"
        "33:\n\t"
        "s_mov_b64 exec, -1\n\t"
        : "+&s"(pend[0]), "+&s"(pend[1]), "+&s"(pend[2]), "+&s"(pend[3]), "+v"(vo[0]), "+v"(vo[1]), "+v"(vo[2]),
          "+v"(vo[3]), "+{v[240:243]}"(cv[0]), "+{v[244:247]}"(cv[1]), "+{v[248:251]}"(cv[2]), "+{v[252:255]}"(cv[3])
        : "v"(eb[0]), "v"(eb[1]), "v"(eb[2]), "v"(eb[3]), "s"(ib), "n"(OFF_L + LARR - 4), "v"(tag[0]), "v"(tag[1]),
          "s"(sq), "s"(just)
        :
          "memory", "vcc", "scc", "v176", "v177", "v178", "v179", "v180", "v181", "v188", "v189", "v190", "v191",
          "v192", "v193", "v194", "v195", "v196", "v197", "v198", "v199", "v200", "v201", "v202", "v203", "v204",
          "v205", "v206", "v207", "v208", "v209", "v210", "v211", "v212", "v213", "v214", "v215", "v216", "v217",
          "v218", "v219", "v220", "v221", "v222", "v223", "v224", "v225", "v226", "v227", "v228", "v229", "v230",
          "v231", "v232", "v233", "v234", "v235", "v236", "v237", "v238", "v239", "s52", "s53", "s54", "s55", "s56",
          "s57", "s58", "s59", "s60", "s61", "s62", "s63", "s64", "s65", "s66", "s67", "s68", "s69", "s70", "s71",
          "s72", "s73", "s74", "s75", "s76", "s77", "s78", "s79", "s80", "s81", "s82", "s83", "s84", "s85", "s86",
          "s87", "s88", "s89", "s90", "s91", "s92", "s93", "s94", "s95", "s96");
    uni();
    just = 0u;
  }
  *(LAS unsigned*)((LAS char*)smem + OFF_CNT + 16 + wave * 32) = early;
  asm volatile("s_waitcnt lgkmcnt(0)\n\ts_barrier" ::: "memory");
  if (wave >= 4) __builtin_amdgcn_s_setprio(1);
  const u32x4 cw = *(const LAS u32x4*)((LAS char*)smem + OFF_CNT + rid * 32);
  const unsigned ef = *(const LAS unsigned*)((LAS char*)smem + OFF_CNT + 16 + rid * 32);
  unsigned slowf = *(const LAS unsigned*)((LAS char*)smem + OFF_CNT + 20);
  asm volatile("" : "+v"(slowf));
  const int key = (int)((cw[0] + cw[1] + cw[2] + cw[3]) * 2u + rid);
  int gb;
  {
    unsigned acc = 0u;
#define RK(k) acc = __builtin_amdgcn_alignbit(acc, (unsigned)(__builtin_amdgcn_update_dpp(0, key, 0x120 + k, 0xf, 0xf, false) - key), 31)
    RK(1); RK(2); RK(3); RK(4); RK(5); RK(6); RK(7);
#undef RK
    const int want = RANKOF(wave);
    gb = __builtin_ctzll(__ballot(__builtin_popcount(acc) == want)) & 7;
  }
  const unsigned ring0 = lds0 + OFF_R0 + gb * RING_SLOT;
  const unsigned la = l0 + gb * LIST_BYTES;
  u32x16 XAf;
#pragma unroll
  for (int q = 0; q < 4; ++q) {
    const u32x4 t = ld128(ring0 + ringd + q * 1024 + lane * 16);
#pragma unroll
    for (int j = 0; j < 4; ++j) XAf[4 * q + j] = t[j];
  }
  unsigned sl[4];
#pragma unroll
  for (int q = 0; q < 4; ++q) sl[q] = __builtin_amdgcn_readlane(cw[q], gb) - (l0 + q * SUBL);
  const unsigned o2 = sl[0] + sl[1], o3 = o2 + sl[2];
  const bool slow = o3 + sl[3] > LCAP * 4u || max(max(sl[0], sl[1]), max(sl[2], sl[3])) > SUBL - 4u ||
                    __builtin_amdgcn_readfirstlane(slowf) != 0u;
  const int cnt = slow ? 0 : (int)(o3 + sl[3]) >> 2;
  const unsigned cg = (bu * 8 + gb) * 32;
  u32x16 AD;
  AD[2] = aLX + gb * LIST_BYTES;
  AD[3] = aLE + gb * LIST_BYTES;
  AD[4] = cx0; AD[5] = cx1; AD[6] = ce;
  auto issue_gather = [&](int tile, int rs) {
    const u32x4 sx = ld128(AD[2] + tile * 128);
    glds16e(ES, *(const LAS unsigned*)(size_t)(AD[3] + tile * 128), AD[6], ring0 + rs * ringd + 4096);
#pragma unroll
    for (int q = 0; q < 4; ++q) glds16x(x, sx[q], AD[4 + (q & 1)], ring0 + rs * ringd + q * 1024);
  };
  auto pads = [&](int cnt, int lane) __attribute__((always_inline)) {
    const int u = cnt + lane;
    const unsigned c = cg + ((u >> 2) & 7) + 8 * (u & 3), n = nodeof(c);
    const unsigned padA = n | (__builtin_amdgcn_readlane(c, __builtin_ctzll(__ballot((n * Mw + Cw) >> 16 != c))) & 0xffu) << 16;
    const unsigned pa = la + u * 4;
    *(LAS unsigned*)(size_t)pa = padA;
    *(LAS unsigned*)(size_t)(pa + 256) = padA;
    *(LAS unsigned*)(size_t)(pa + LARR) = 0u;
    *(LAS unsigned*)(size_t)(pa + LARR + 256) = 0u;
  };
  constexpr int GEND = 64 + NCH;
  int gs = 0, gp = 0;
  auto gen = [&]() __attribute__((always_inline)) {
    int l2, n = 0;
    asm volatile("v_mbcnt_lo_u32_b32 %0, -1, 0\n\tv_mbcnt_hi_u32_b32 %0, -1, %0" : "=v"(l2));
    const LAS unsigned char* xf = (const LAS unsigned char*)smem + OFF_XF;
    auto put = [&](bool ok, unsigned t, unsigned c, int cc) {
      ok = ok && (int)(c >> 5) == gb;
      const unsigned long long bm = __ballot(ok);
      const unsigned pa = la + 4u * (n + __builtin_amdgcn_mbcnt_hi((unsigned)(bm >> 32), __builtin_amdgcn_mbcnt_lo((unsigned)bm, 0u)));
      if (ok) {
        *(LAS unsigned*)(size_t)pa = (t & 0xffffu) | (c << 16);
        *(LAS unsigned*)(size_t)(pa + LARR) = ((unsigned)(cc * chunk) + (t >> 16)) * 32u;
      }
      n += __builtin_popcountll(bm);
    };
#pragma unroll 1
    while (gs < GEND && n <= LCAP - 192) {
      if (gs < 64) {
        const int cc = (gs & 31) * 8 + (l2 >> 3);
        const unsigned* pp = reinterpret_cast<const unsigned*>(ib + (gs >> 5) * IEXT + (gs & 31) * 1024 + l2 * 16);
        const bool live = gs < 32 || xf[cc] != 0;
        const u32x2 tg = *(const LAS u32x2*)((LAS char*)smem + OFF_CNT + 56);
#pragma unroll 1
        for (int i = 0; i < (1 << 20); ++i) {
          const unsigned m = ldd(pp + 3);
          unsigned sm_ = ldd(pp) + ldd(pp + 1) + ldd(pp + 2) + m + tg[0];
          sm_ += __builtin_amdgcn_update_dpp(0u, sm_, 0xB1, 0xf, 0xf, false);
          sm_ += __builtin_amdgcn_update_dpp(0u, sm_, 0x4E, 0xf, 0xf, false);
          sm_ += __builtin_amdgcn_update_dpp(0u, sm_, 0x141, 0xf, 0xf, false);
          if (__ballot(live && (sm_ > 1u || m >> 24 != tg[1])) == 0ull) break;
        }
        const unsigned m = ldd(pp + 3);
#pragma unroll 1
        for (int j = 0; j < 3; ++j) {
          const unsigned t = ldd(pp + j);
          put(live && t != ~0u && (j < 2 || (l2 & 7) != 7), t, (m >> (8 * j)) & 0xffu, cc);
        }
        ++gs;
      } else {
        const int c2 = gs - 64 + l2;
        const unsigned long long fm = __ballot(c2 < NCH && xf[min(c2, NCH - 1)] == 2);
        if (fm == 0ull) { gs = min(gs + 64, GEND); continue; }
        gs += __builtin_ctzll(fm);
        const int cc = gs - 64;
        const unsigned long long ds = ldq(FL + 512 + bu * NCH + cc);
        const int len = __builtin_amdgcn_readfirstlane((int)(ds >> 32));
        const unsigned long long* tp = TS + (unsigned)ds;
#pragma unroll 1
        for (; gp < len && n <= LCAP - 64; gp += 64) {
          const unsigned long long t = ldq(tp + min(gp + l2, len - 1));
          put(gp + l2 < len, (unsigned)t, (unsigned)(t >> 32), cc);
        }
        if (gp >= len) { gp = 0; ++gs; }
      }
    }
    __builtin_amdgcn_s_waitcnt(0x0F70);
    pads(n, l2);
    return n;
  };
  const bool quick = !slow && sl[0] >= 256u && __builtin_amdgcn_readlane(ef, gb) != 0u;
  auto gather1 = [&](unsigned skip) {
    u32x4 sx = ld128(AD[2] + 128);
    unsigned re = *(const LAS unsigned*)(size_t)(AD[3] + 128);
    asm volatile("s_cmp_lg_u32 %13, 0\n\t"
                 "s_cbranch_scc1 1f\n\t"
                 "s_add_i32 m0, %11, 4096\n\t"
                 "v_add_u32 %4, %4, %8\n\t"
                 "v_mad_u32_u16 %0, %0, %12, %6\n\t"
                 "global_load_lds_dwordx4 %4, %10\n\t"
                 "s_mov_b32 m0, %11\n\t"
                 "v_mad_u32_u16 %1, %1, %12, %7\n\t"
                 "global_load_lds_dwordx4 %0, %9\n\t"
                 "s_addk_i32 m0, 1024\n\t"
                 "v_mad_u32_u16 %2, %2, %12, %6\n\t"
                 "global_load_lds_dwordx4 %1, %9\n\t"
                 "s_addk_i32 m0, 1024\n\t"
                 "v_mad_u32_u16 %3, %3, %12, %7\n\t"
                 "global_load_lds_dwordx4 %2, %9\n\t"
                 "s_addk_i32 m0, 1024\n\t"
                 "s_nop 0\n\t"
                 "global_load_lds_dwordx4 %3, %9\n"
                 "1:"
                 : "+v"(sx[0]), "+v"(sx[1]), "+v"(sx[2]), "+v"(sx[3]), "+v"(re), "+{v[128:143]}"(XAf)
                 : "v"(cx0), "v"(cx1), "v"(ce), "s"(x), "s"(ES), "s"(ring0 + ringd), "s"(128u), "s"(skip)
                 : "memory", "m0", "scc");
  };
  unsigned mode;
  asm volatile("s_cmp_lg_u64 %1, 0\n\ts_cselect_b32 %0, %2, 0" : "=s"(mode) : "s"(__ballot(quick)), "s"(1 + wave / G1W) : "scc");
  gather1(mode ^ 2u);
  if (__builtin_expect(!slow, 1)) {
    const unsigned p0 = la + lane * 4;
    asm volatile(
        "ds_read2st64_b32 v[222:223], %1 offset0:2 offset1:12\n\t"
        "ds_read2st64_b32 v[224:225], %1 offset0:3 offset1:13\n\t"
        "ds_read2st64_b32 v[226:227], %1 offset0:4 offset1:14\n\t"
        "ds_read2st64_b32 v[228:229], %0 offset0:5 offset1:15\n\t"
        "ds_read2st64_b32 v[230:231], %0 offset0:6 offset1:16\n\t"
        "ds_read2st64_b32 v[232:233], %0 offset0:7 offset1:17\n\t"
        "ds_read2st64_b32 v[234:235], %1 offset0:7 offset1:17\n\t"
        "ds_read2st64_b32 v[236:237], %1 offset0:8 offset1:18\n\t"
        "ds_read2st64_b32 v[238:239], %1 offset0:9 offset1:19\n\t"
        "s_waitcnt lgkmcnt(8)\n\t"
        "ds_write2st64_b32 %2, v222, v223 offset0:0 offset1:10\n\t"
        "s_waitcnt lgkmcnt(8)\n\t"
        "ds_write2st64_b32 %2, v224, v225 offset0:1 offset1:11\n\t"
        "s_waitcnt lgkmcnt(8)\n\t"
        "ds_write2st64_b32 %2, v226, v227 offset0:2 offset1:12\n\t"
        "s_waitcnt lgkmcnt(8)\n\t"
        "ds_write2st64_b32 %3, v228, v229 offset0:0 offset1:10\n\t"
        "s_waitcnt lgkmcnt(8)\n\t"
        "ds_write2st64_b32 %3, v230, v231 offset0:1 offset1:11\n\t"
        "s_waitcnt lgkmcnt(8)\n\t"
        "ds_write2st64_b32 %3, v232, v233 offset0:2 offset1:12\n\t"
        "s_waitcnt lgkmcnt(8)\n\t"
        "ds_write2st64_b32 %4, v234, v235 offset0:0 offset1:10\n\t"
        "s_waitcnt lgkmcnt(8)\n\t"
        "ds_write2st64_b32 %4, v236, v237 offset0:1 offset1:11\n\t"
        "s_mov_b64 exec, 0xffffffff\n\t"
        "s_waitcnt lgkmcnt(8)\n\t"
        "ds_write2st64_b32 %4, v238, v239 offset0:2 offset1:12\n\t"
        "s_mov_b64 exec, -1"
        :: "v"(p0), "v"(p0 + 128u), "v"(p0 + sl[0]), "v"(p0 + o2), "v"(p0 + o3)
        : "memory", "v222", "v223", "v224", "v225", "v226", "v227", "v228", "v229", "v230", "v231", "v232", "v233", "v234",
          "v235", "v236", "v237", "v238", "v239");
  }
  pads(cnt, lane);
  AD[0] = aLS4 + gb * LIST_BYTES;
  AD[1] = aLS1 + gb * LIST_BYTES;
  AD[7] = cS - gb * 32 * TPITCH;
  AD[8] = cSt - gb * 32 * TPITCH;
#pragma unroll
  for (int t = 0; t < 4; ++t) AD[9 + t] = fo[t] + gb * RING_SLOT;
  AD[13] = foe + gb * RING_SLOT;
  AD[14] = lds0 + lane * 16;
  AD[15] = to + 18 * 1024 + h * 4096;
  if (__builtin_expect(mode == 0u, 0)) asm volatile("s_waitcnt vmcnt(0)\n\ts_barrier" ::: "memory");
  f32x16 X[2], U[2];
  if (__builtin_expect(mode == 0u, 0)) {
    issue_gather(0, 0);
    issue_gather(1, 1);
  }
  {
    int nt = __builtin_amdgcn_readfirstlane(max((cnt - 1) >> 5, 0));
    asm volatile(
        "s_movk_i32 s94, 128\n\t"
        "s_movk_i32 s95, 80\n\t"
        "s_mov_b32 s96, %[cu]\n\t"
        "ds_read_u16 v102, v161\n\t"
        "ds_read_b128 v[104:107], v162 offset:128\n\t"
        "ds_read_b32 v101, v163 offset:128\n\t"
        "ds_read_b128 v[96:99], v162 offset:256\n\t"
        "ds_read_b32 v100, v163 offset:256\n\t"
        "ds_read_b128 v[120:123], v160\n\t"
        "ds_read_u16 v124, v161 offset:128\n\t"
        "s_waitcnt lgkmcnt(6)\n\t"
        "v_mad_u32_u24 v102, v102, s95, v168\n\t"
        "ds_read_b128 v[184:187], v102\n\t"
        "ds_read_b128 v[188:191], v102 offset:16\n\t"
        "ds_read_b128 v[112:115], v174 offset:512\n\t"
        "ds_read_b128 v[116:119], v174 offset:2560\n\t"
        "ds_read_b128 v[108:111], v174 offset:8704\n\t"
        "ds_read_b128 v[196:199], v174 offset:16896\n\t"
        "ds_read_b128 v[200:203], v174 offset:1536\n\t"
        "ds_read_b128 v[204:207], v174 offset:3584\n\t"
        "s_waitcnt lgkmcnt(12)\n\t"
        "v_add_u32 v101, v101, v166\n\t"
        "v_mad_u32_u16 v104, v104, s94, v164\n\t"
        "v_mad_u32_u16 v105, v105, s94, v165\n\t"
        "v_mad_u32_u16 v106, v106, s94, v164\n\t"
        "v_mad_u32_u16 v107, v107, s94, v165\n\t"
        "s_waitcnt lgkmcnt(7)\n\t"
        "v_mfma_f32_32x32x16_f16 v[64:79], v[184:187], v[128:131], 0\n\t"
        "ds_read_b128 v[208:211], v174 offset:9728\n\t"
        "ds_read_b128 v[212:215], v174 offset:10752\n\t"
        "ds_read_b128 v[216:219], v174 offset:11776\n\t"
        "ds_read_b128 v[220:223], v174 offset:17920\n\t"
        "ds_read_b128 v[224:227], v174 offset:4608\n\t"
        "ds_read_b128 v[228:231], v174 offset:5632\n\t"
        "s_waitcnt lgkmcnt(12)\n\t"
        "v_mfma_f32_32x32x16_f16 v[64:79], v[188:191], v[136:139], v[64:79]\n\t"
        "ds_read_b128 v[232:235], v174 offset:6656\n\t"
        "ds_read_b128 v[236:239], v174 offset:7680\n\t"
        "ds_read_b128 v[240:243], v174 offset:12800\n\t"
        "ds_read_b128 v[244:247], v174 offset:13824\n\t"
        "ds_read_b128 v[248:251], v174 offset:14848\n\t"
        "ds_read_b128 v[252:255], v174 offset:15872\n\t"
        "s_add_i32 m0, %[ring], 45056\n\t"
        "s_waitcnt vmcnt(5)\n\t"
        "s_cmp_eq_u32 %[flag], 0\n\t"
        "s_cbranch_scc1 2f\n\t"
        "s_cmp_eq_u32 %[flag], 2\n\t"
        "s_cbranch_scc1 3f\n\t"
        "s_waitcnt vmcnt(0)\n\t"
        "3:\n\t"
        "s_barrier\n\t"
        "2:\n\t"
        "ds_read_b128 v[144:147], v169\n\t"
        "ds_read_b128 v[148:151], v170\n\t"
        "ds_read_b128 v[152:155], v171\n\t"
        "ds_read_b128 v[156:159], v172\n\t"
        "ds_read_b128 v[192:195], v173\n\t"
        "s_cbranch_scc1 4f\n\t"
        "global_load_lds_dwordx4 v101, %[es]\n\t"
        "s_add_i32 m0, %[ring], 40960\n\t"
        "s_nop 0\n\t"
        "global_load_lds_dwordx4 v104, %[x]\n\t"
        "s_add_i32 m0, %[ring], 41984\n\t"
        "s_nop 0\n\t"
        "global_load_lds_dwordx4 v105, %[x]\n\t"
        "s_add_i32 m0, %[ring], 43008\n\t"
        "s_nop 0\n\t"
        "global_load_lds_dwordx4 v106, %[x]\n\t"
        "s_add_i32 m0, %[ring], 44032\n\t"
        "s_nop 0\n\t"
        "global_load_lds_dwordx4 v107, %[x]\n\t"
        "4:\n\t"
        "s_waitcnt lgkmcnt(4)\n\t"
        "v_mfma_f32_32x32x16_bf16 v[64:79], v[144:147], v[112:115], v[64:79]\n\t"
        "s_waitcnt lgkmcnt(3)\n\t"
        "v_mfma_f32_32x32x16_bf16 v[64:79], v[148:151], v[200:203], v[64:79]\n\t"
        "s_waitcnt lgkmcnt(2)\n\t"
        "v_mfma_f32_32x32x16_bf16 v[64:79], v[152:155], v[116:119], v[64:79]\n\t"
        "s_waitcnt lgkmcnt(1)\n\t"
        "v_mfma_f32_32x32x16_bf16 v[64:79], v[156:159], v[204:207], v[64:79]\n\t"
        "s_waitcnt lgkmcnt(0)\n\t"
        "v_mfma_f32_32x32x16_bf16 v[64:79], v[192:195], v[196:199], v[64:79]\n\t"
        "s_add_i32 m0, %[ring], 4096\n\t"
        "v_add_u32 v100, v100, v166\n\t"
        "global_load_lds_dwordx4 v100, %[es]\n\t"
        "v_mfma_f32_32x32x16_bf16 v[80:95], v[144:147], v[108:111], 0\n\t"
        "s_mov_b32 m0, %[ring]\n\t"
        "v_mad_u32_u16 v96, v96, s94, v164\n\t"
        "global_load_lds_dwordx4 v96, %[x]\n\t"
        "s_add_i32 m0, %[ring], 1024\n\t"
        "v_mad_u32_u16 v97, v97, s94, v165\n\t"
        "global_load_lds_dwordx4 v97, %[x]\n\t"
        "v_mfma_f32_32x32x16_bf16 v[80:95], v[148:151], v[208:211], v[80:95]\n\t"
        "s_add_i32 m0, %[ring], 2048\n\t"
        "v_mad_u32_u16 v98, v98, s94, v164\n\t"
        "global_load_lds_dwordx4 v98, %[x]\n\t"
        "v_exp_f32 v64, v64\n\t"
        "v_exp_f32 v65, v65\n\t"
        "v_mfma_f32_32x32x16_bf16 v[80:95], v[152:155], v[212:215], v[80:95]\n\t"
        "s_add_i32 m0, %[ring], 3072\n\t"
        "v_mad_u32_u16 v99, v99, s94, v165\n\t"
        "global_load_lds_dwordx4 v99, %[x]\n\t"
        "v_exp_f32 v66, v66\n\t"
        "v_exp_f32 v67, v67\n\t"
        "v_mfma_f32_32x32x16_bf16 v[80:95], v[156:159], v[216:219], v[80:95]\n\t"
        "v_cvt_pk_f16_f32 v64, v64, v65\n\t"
        "v_exp_f32 v68, v68\n\t"
        "v_cvt_pk_f16_f32 v65, v66, v67\n\t"
        "v_pk_add_f16 v64, v64, s96\n\t"
        "v_exp_f32 v69, v69\n\t"
        "v_pk_add_f16 v65, v65, s96\n\t"
        "v_rcp_f16_sdwa v64, v64 dst_sel:WORD_0 dst_unused:UNUSED_PRESERVE src0_sel:WORD_0\n\t"
        "v_exp_f32 v70, v70\n\t"
        "v_cvt_pk_f16_f32 v66, v68, v69\n\t"
        "v_rcp_f16_sdwa v65, v65 dst_sel:WORD_0 dst_unused:UNUSED_PRESERVE src0_sel:WORD_0\n\t"
        "v_pk_add_f16 v66, v66, s96\n\t"
        "v_rcp_f16_sdwa v64, v64 dst_sel:WORD_1 dst_unused:UNUSED_PRESERVE src0_sel:WORD_1\n\t"
        "v_cvt_pk_f16_f32 v80, v80, v81\n\t"
        "v_rcp_f16_sdwa v65, v65 dst_sel:WORD_1 dst_unused:UNUSED_PRESERVE src0_sel:WORD_1\n\t"
        "v_pk_mul_f16 v80, v80, v64\n\t"
        "s_branch .Lr4_%=\n\t"
        ".Lrt_%=:\n\t"
        "v_mfma_f32_32x32x16_f16 v[16:31], v[96:99], v[176:179], v[16:31]\n\t"
        "s_cmp_lt_u32 %[wave], 4\n\t"
        "s_cbranch_scc1 1f\n\t"
        "s_setprio 1\n\t"
        "1:\n\t"
        "ds_read_b128 v[120:123], v160\n\t"
        "ds_read_u16 v124, v161 offset:128\n\t"
        "v_rcp_f16_sdwa v103, v103 dst_sel:WORD_1 dst_unused:UNUSED_PRESERVE src0_sel:WORD_1\n\t"
        "v_cvt_pk_f16_f32 v119, v126, v127\n\t"
        "v_pk_add_f16 v64, v64, s96\n\t"
        "v_exp_f32 v67, v67\n\t"
        "v_rcp_f16_sdwa v64, v64 dst_sel:WORD_0 dst_unused:UNUSED_PRESERVE src0_sel:WORD_0\n\t"
        "v_mfma_f32_32x32x16_f16 v[48:63], v[112:115], v[176:179], v[48:63]\n\t"
        "v_pk_mul_f16 v119, v119, v103\n\t"
        "v_cvt_pk_f16_f32 v65, v66, v67\n\t"
        "v_rcp_f16_sdwa v64, v64 dst_sel:WORD_1 dst_unused:UNUSED_PRESERVE src0_sel:WORD_1\n\t"
        "v_pk_add_f16 v65, v65, s96\n\t"
        "v_exp_f32 v68, v68\n\t"
        "v_mfma_f32_32x32x16_f16 v[16:31], v[100:103], v[180:183], v[16:31]\n\t"
        "v_cvt_pk_f16_f32 v80, v80, v81\n\t"
        "v_rcp_f16_sdwa v65, v65 dst_sel:WORD_0 dst_unused:UNUSED_PRESERVE src0_sel:WORD_0\n\t"
        "v_pk_mul_f16 v80, v80, v64\n\t"
        "v_exp_f32 v69, v69\n\t"
        "v_mfma_f32_32x32x16_f16 v[48:63], v[116:119], v[180:183], v[48:63]\n\t"
        "v_cvt_pk_f16_f32 v66, v68, v69\n\t"
        "v_rcp_f16_sdwa v65, v65 dst_sel:WORD_1 dst_unused:UNUSED_PRESERVE src0_sel:WORD_1\n\t"
        "v_pk_add_f16 v66, v66, s96\n\t"
        "v_exp_f32 v70, v70\n\t"
        ".Lr4_%=:\n\t"
        "v_mfma_f32_32x32x16_f16 v[96:111], v[184:187], v[132:135], 0\n\t"
        "s_waitcnt lgkmcnt(1)\n\t"
        "v_mad_u32_u16 v120, v120, s95, v167 op_sel:[1,0,0,0]\n\t"
        "ds_read_b64_tr_b16 v[176:177], v120\n\t"
        "v_mad_u32_u16 v121, v121, s95, v167 op_sel:[1,0,0,0]\n\t"
        "ds_read_b64_tr_b16 v[178:179], v121\n\t"
        "v_mad_u32_u16 v122, v122, s95, v167 op_sel:[1,0,0,0]\n\t"
        "ds_read_b64_tr_b16 v[180:181], v122\n\t"
        "v_mad_u32_u16 v123, v123, s95, v167 op_sel:[1,0,0,0]\n\t"
        "ds_read_b64_tr_b16 v[182:183], v123\n\t"
        "v_cvt_pk_f16_f32 v81, v82, v83\n\t"
        "v_rcp_f16_sdwa v66, v66 dst_sel:WORD_0 dst_unused:UNUSED_PRESERVE src0_sel:WORD_0\n\t"
        "v_pk_mul_f16 v81, v81, v65\n\t"
        "v_exp_f32 v71, v71\n\t"
        "v_mfma_f32_32x32x16_f16 v[96:111], v[188:191], v[140:143], v[96:111]\n\t"
        "s_waitcnt lgkmcnt(4)\n\t"
        "v_mad_u32_u24 v124, v124, s95, v168\n\t"
        "ds_read_b128 v[184:187], v124\n\t"
        "ds_read_b128 v[188:191], v124 offset:16\n\t"
        "v_cvt_pk_f16_f32 v67, v70, v71\n\t"
        "v_rcp_f16_sdwa v66, v66 dst_sel:WORD_1 dst_unused:UNUSED_PRESERVE src0_sel:WORD_1\n\t"
        "v_pk_add_f16 v67, v67, s96\n\t"
        "v_exp_f32 v72, v72\n\t"
        "v_mfma_f32_32x32x16_bf16 v[96:111], v[192:195], v[220:223], v[96:111]\n\t"
        "s_waitcnt vmcnt(9)\n\t"
        "ds_read_b128 v[192:195], v173 offset:40960\n\t"
        "ds_read_b32 v125, v163 offset:384\n\t"
        "v_cvt_pk_f16_f32 v82, v84, v85\n\t"
        "v_rcp_f16_sdwa v67, v67 dst_sel:WORD_0 dst_unused:UNUSED_PRESERVE src0_sel:WORD_0\n\t"
        "v_pk_mul_f16 v82, v82, v66\n\t"
        "v_exp_f32 v73, v73\n\t"
        "v_mfma_f32_32x32x16_bf16 v[96:111], v[144:147], v[224:227], v[96:111]\n\t"
        "v_cvt_pk_f16_f32 v68, v72, v73\n\t"
        "v_rcp_f16_sdwa v67, v67 dst_sel:WORD_1 dst_unused:UNUSED_PRESERVE src0_sel:WORD_1\n\t"
        "v_pk_add_f16 v68, v68, s96\n\t"
        "v_exp_f32 v74, v74\n\t"
        "v_mfma_f32_32x32x16_bf16 v[96:111], v[148:151], v[228:231], v[96:111]\n\t"
        "v_cvt_pk_f16_f32 v83, v86, v87\n\t"
        "v_rcp_f16_sdwa v68, v68 dst_sel:WORD_0 dst_unused:UNUSED_PRESERVE src0_sel:WORD_0\n\t"
        "v_pk_mul_f16 v83, v83, v67\n\t"
        "v_exp_f32 v75, v75\n\t"
        "v_mfma_f32_32x32x16_bf16 v[96:111], v[152:155], v[232:235], v[96:111]\n\t"
        "s_add_i32 m0, %[ring], 45056\n\t"
        "s_waitcnt lgkmcnt(0)\n\t"
        "v_add_u32 v125, v125, v166\n\t"
        "global_load_lds_dwordx4 v125, %[es]\n\t"
        "v_cvt_pk_f16_f32 v69, v74, v75\n\t"
        "v_rcp_f16_sdwa v68, v68 dst_sel:WORD_1 dst_unused:UNUSED_PRESERVE src0_sel:WORD_1\n\t"
        "v_pk_add_f16 v69, v69, s96\n\t"
        "v_exp_f32 v76, v76\n\t"
        "v_mfma_f32_32x32x16_bf16 v[96:111], v[156:159], v[236:239], v[96:111]\n\t"
        "v_cvt_pk_f16_f32 v84, v88, v89\n\t"
        "v_rcp_f16_sdwa v69, v69 dst_sel:WORD_0 dst_unused:UNUSED_PRESERVE src0_sel:WORD_0\n\t"
        "v_pk_mul_f16 v84, v84, v68\n\t"
        "v_exp_f32 v77, v77\n\t"
        "v_mfma_f32_32x32x16_bf16 v[112:127], v[144:147], v[240:243], 0\n\t"
        "s_waitcnt vmcnt(6)\n\t"
        "ds_read_b128 v[144:147], v169 offset:40960\n\t"
        "v_cvt_pk_f16_f32 v70, v76, v77\n\t"
        "v_rcp_f16_sdwa v69, v69 dst_sel:WORD_1 dst_unused:UNUSED_PRESERVE src0_sel:WORD_1\n\t"
        "v_pk_add_f16 v70, v70, s96\n\t"
        "v_exp_f32 v78, v78\n\t"
        "v_mfma_f32_32x32x16_bf16 v[112:127], v[148:151], v[244:247], v[112:127]\n\t"
        "ds_read_b128 v[148:151], v170 offset:40960\n\t"
        "s_sub_u32 %[nt], %[nt], 1\n\t"
        "v_cvt_pk_f16_f32 v85, v90, v91\n\t"
        "v_rcp_f16_sdwa v70, v70 dst_sel:WORD_0 dst_unused:UNUSED_PRESERVE src0_sel:WORD_0\n\t"
        "v_pk_mul_f16 v85, v85, v69\n\t"
        "v_exp_f32 v79, v79\n\t"
        "v_mfma_f32_32x32x16_bf16 v[112:127], v[152:155], v[248:251], v[112:127]\n\t"
        "ds_read_b128 v[152:155], v171 offset:40960\n\t"
        "ds_read_b128 v[88:91], v162 offset:384\n\t"
        "v_cvt_pk_f16_f32 v71, v78, v79\n\t"
        "v_rcp_f16_sdwa v70, v70 dst_sel:WORD_1 dst_unused:UNUSED_PRESERVE src0_sel:WORD_1\n\t"
        "v_pk_add_f16 v71, v71, s96\n\t"
        "v_exp_f32 v96, v96\n\t"
        "v_exp_f32 v97, v97\n\t"
        "v_mfma_f32_32x32x16_bf16 v[112:127], v[156:159], v[252:255], v[112:127]\n\t"
        "ds_read_b128 v[156:159], v172 offset:40960\n\t"
        "v_cvt_pk_f16_f32 v86, v92, v93\n\t"
        "v_rcp_f16_sdwa v71, v71 dst_sel:WORD_0 dst_unused:UNUSED_PRESERVE src0_sel:WORD_0\n\t"
        "v_pk_mul_f16 v86, v86, v70\n\t"
        "v_exp_f32 v98, v98\n\t"
        "v_cvt_pk_f16_f32 v96, v96, v97\n\t"
        "s_cbranch_scc1 .Lrl_%=\n\t"
        "v_mfma_f32_32x32x16_f16 v[0:15], v[64:67], v[176:179], v[0:15]\n\t"
        "v_rcp_f16_sdwa v71, v71 dst_sel:WORD_1 dst_unused:UNUSED_PRESERVE src0_sel:WORD_1\n\t"
        "v_cvt_pk_f16_f32 v87, v94, v95\n\t"
        "v_pk_add_f16 v96, v96, s96\n\t"
        "v_exp_f32 v99, v99\n\t"
        "v_rcp_f16_sdwa v96, v96 dst_sel:WORD_0 dst_unused:UNUSED_PRESERVE src0_sel:WORD_0\n\t"
        "v_mfma_f32_32x32x16_f16 v[32:47], v[80:83], v[176:179], v[32:47]\n\t"
        "s_add_i32 m0, %[ring], 40960\n\t"
        "s_waitcnt lgkmcnt(1)\n\t"
        "v_mad_u32_u16 v88, v88, s94, v164\n\t"
        "s_waitcnt lgkmcnt(0)\n\t"
        "global_load_lds_dwordx4 v88, %[x]\n\t"
        "v_pk_mul_f16 v87, v87, v71\n\t"
        "v_cvt_pk_f16_f32 v97, v98, v99\n\t"
        "v_rcp_f16_sdwa v96, v96 dst_sel:WORD_1 dst_unused:UNUSED_PRESERVE src0_sel:WORD_1\n\t"
        "v_pk_add_f16 v97, v97, s96\n\t"
        "v_exp_f32 v100, v100\n\t"
        "v_mfma_f32_32x32x16_f16 v[0:15], v[68:71], v[180:183], v[0:15]\n\t"
        "ds_read_b128 v[92:95], v174 offset:512\n\t"
        "s_add_i32 m0, %[ring], 41984\n\t"
        "v_mad_u32_u16 v89, v89, s94, v165\n\t"
        "global_load_lds_dwordx4 v89, %[x]\n\t"
        "v_cvt_pk_f16_f32 v112, v112, v113\n\t"
        "v_rcp_f16_sdwa v97, v97 dst_sel:WORD_0 dst_unused:UNUSED_PRESERVE src0_sel:WORD_0\n\t"
        "v_pk_mul_f16 v112, v112, v96\n\t"
        "v_exp_f32 v101, v101\n\t"
        "v_mfma_f32_32x32x16_f16 v[32:47], v[84:87], v[180:183], v[32:47]\n\t"
        "s_add_i32 m0, %[ring], 43008\n\t"
        "v_mad_u32_u16 v90, v90, s94, v164\n\t"
        "global_load_lds_dwordx4 v90, %[x]\n\t"
        "v_cvt_pk_f16_f32 v98, v100, v101\n\t"
        "v_rcp_f16_sdwa v97, v97 dst_sel:WORD_1 dst_unused:UNUSED_PRESERVE src0_sel:WORD_1\n\t"
        "v_pk_add_f16 v98, v98, s96\n\t"
        "v_exp_f32 v102, v102\n\t"
        "v_mfma_f32_32x32x16_f16 v[64:79], v[184:187], v[128:131], 0\n\t"
        "s_add_i32 m0, %[ring], 44032\n\t"
        "v_mad_u32_u16 v91, v91, s94, v165\n\t"
        "global_load_lds_dwordx4 v91, %[x]\n\t"
        "v_cvt_pk_f16_f32 v113, v114, v115\n\t"
        "v_rcp_f16_sdwa v98, v98 dst_sel:WORD_0 dst_unused:UNUSED_PRESERVE src0_sel:WORD_0\n\t"
        "v_pk_mul_f16 v113, v113, v97\n\t"
        "v_exp_f32 v103, v103\n\t"
        "v_mfma_f32_32x32x16_f16 v[64:79], v[188:191], v[136:139], v[64:79]\n\t"
        "ds_read_b128 v[80:83], v174 offset:2560\n\t"
        "v_cvt_pk_f16_f32 v99, v102, v103\n\t"
        "v_rcp_f16_sdwa v98, v98 dst_sel:WORD_1 dst_unused:UNUSED_PRESERVE src0_sel:WORD_1\n\t"
        "v_pk_add_f16 v99, v99, s96\n\t"
        "v_exp_f32 v104, v104\n\t"
        "v_mfma_f32_32x32x16_bf16 v[64:79], v[192:195], v[196:199], v[64:79]\n\t"
        "v_cvt_pk_f16_f32 v114, v116, v117\n\t"
        "v_rcp_f16_sdwa v99, v99 dst_sel:WORD_0 dst_unused:UNUSED_PRESERVE src0_sel:WORD_0\n\t"
        "v_pk_mul_f16 v114, v114, v98\n\t"
        "v_exp_f32 v105, v105\n\t"
        "s_waitcnt lgkmcnt(1)\n\t"
        "v_mfma_f32_32x32x16_bf16 v[64:79], v[144:147], v[92:95], v[64:79]\n\t"
        "ds_read_b128 v[84:87], v174 offset:8704\n\t"
        "v_cvt_pk_f16_f32 v100, v104, v105\n\t"
        "v_rcp_f16_sdwa v99, v99 dst_sel:WORD_1 dst_unused:UNUSED_PRESERVE src0_sel:WORD_1\n\t"
        "v_pk_add_f16 v100, v100, s96\n\t"
        "v_exp_f32 v106, v106\n\t"
        "v_mfma_f32_32x32x16_bf16 v[64:79], v[148:151], v[200:203], v[64:79]\n\t"
        "v_cvt_pk_f16_f32 v115, v118, v119\n\t"
        "v_rcp_f16_sdwa v100, v100 dst_sel:WORD_0 dst_unused:UNUSED_PRESERVE src0_sel:WORD_0\n\t"
        "v_pk_mul_f16 v115, v115, v99\n\t"
        "v_exp_f32 v107, v107\n\t"
        "s_waitcnt lgkmcnt(1)\n\t"
        "v_mfma_f32_32x32x16_bf16 v[64:79], v[152:155], v[80:83], v[64:79]\n\t"
        "v_cvt_pk_f16_f32 v101, v106, v107\n\t"
        "v_rcp_f16_sdwa v100, v100 dst_sel:WORD_1 dst_unused:UNUSED_PRESERVE src0_sel:WORD_1\n\t"
        "v_pk_add_f16 v101, v101, s96\n\t"
        "v_exp_f32 v108, v108\n\t"
        "v_mfma_f32_32x32x16_bf16 v[64:79], v[156:159], v[204:207], v[64:79]\n\t"
        "v_cvt_pk_f16_f32 v116, v120, v121\n\t"
        "v_rcp_f16_sdwa v101, v101 dst_sel:WORD_0 dst_unused:UNUSED_PRESERVE src0_sel:WORD_0\n\t"
        "v_pk_mul_f16 v116, v116, v100\n\t"
        "v_exp_f32 v109, v109\n\t"
        "s_waitcnt lgkmcnt(0)\n\t"
        "v_mfma_f32_32x32x16_bf16 v[80:95], v[144:147], v[84:87], 0\n\t"
        "v_cvt_pk_f16_f32 v102, v108, v109\n\t"
        "v_rcp_f16_sdwa v101, v101 dst_sel:WORD_1 dst_unused:UNUSED_PRESERVE src0_sel:WORD_1\n\t"
        "v_pk_add_f16 v102, v102, s96\n\t"
        "v_exp_f32 v110, v110\n\t"
        "v_mfma_f32_32x32x16_bf16 v[80:95], v[148:151], v[208:211], v[80:95]\n\t"
        "v_cvt_pk_f16_f32 v117, v122, v123\n\t"
        "v_rcp_f16_sdwa v102, v102 dst_sel:WORD_0 dst_unused:UNUSED_PRESERVE src0_sel:WORD_0\n\t"
        "v_pk_mul_f16 v117, v117, v101\n\t"
        "v_exp_f32 v111, v111\n\t"
        "v_mfma_f32_32x32x16_bf16 v[80:95], v[152:155], v[212:215], v[80:95]\n\t"
        "v_cvt_pk_f16_f32 v103, v110, v111\n\t"
        "v_rcp_f16_sdwa v102, v102 dst_sel:WORD_1 dst_unused:UNUSED_PRESERVE src0_sel:WORD_1\n\t"
        "v_pk_add_f16 v103, v103, s96\n\t"
        "v_exp_f32 v64, v64\n\t"
        "v_exp_f32 v65, v65\n\t"
        "v_mfma_f32_32x32x16_bf16 v[80:95], v[156:159], v[216:219], v[80:95]\n\t"
        "v_cvt_pk_f16_f32 v118, v124, v125\n\t"
        "v_rcp_f16_sdwa v103, v103 dst_sel:WORD_0 dst_unused:UNUSED_PRESERVE src0_sel:WORD_0\n\t"
        "v_pk_mul_f16 v118, v118, v102\n\t"
        "v_exp_f32 v66, v66\n\t"
        "v_cvt_pk_f16_f32 v64, v64, v65\n\t"
        "v_mfma_f32_32x32x16_f16 v[16:31], v[96:99], v[176:179], v[16:31]\n\t"
        "s_setprio 0\n\t"
        "ds_read_b128 v[120:123], v160 offset:128\n\t"
        "ds_read_u16 v124, v161 offset:256\n\t"
        "v_rcp_f16_sdwa v103, v103 dst_sel:WORD_1 dst_unused:UNUSED_PRESERVE src0_sel:WORD_1\n\t"
        "v_cvt_pk_f16_f32 v119, v126, v127\n\t"
        "v_pk_add_f16 v64, v64, s96\n\t"
        "v_exp_f32 v67, v67\n\t"
        "v_rcp_f16_sdwa v64, v64 dst_sel:WORD_0 dst_unused:UNUSED_PRESERVE src0_sel:WORD_0\n\t"
        "v_mfma_f32_32x32x16_f16 v[48:63], v[112:115], v[176:179], v[48:63]\n\t"
        "v_pk_mul_f16 v119, v119, v103\n\t"
        "v_cvt_pk_f16_f32 v65, v66, v67\n\t"
        "v_rcp_f16_sdwa v64, v64 dst_sel:WORD_1 dst_unused:UNUSED_PRESERVE src0_sel:WORD_1\n\t"
        "v_pk_add_f16 v65, v65, s96\n\t"
        "v_exp_f32 v68, v68\n\t"
        "v_mfma_f32_32x32x16_f16 v[16:31], v[100:103], v[180:183], v[16:31]\n\t"
        "v_cvt_pk_f16_f32 v80, v80, v81\n\t"
        "v_rcp_f16_sdwa v65, v65 dst_sel:WORD_0 dst_unused:UNUSED_PRESERVE src0_sel:WORD_0\n\t"
        "v_pk_mul_f16 v80, v80, v64\n\t"
        "v_exp_f32 v69, v69\n\t"
        "v_mfma_f32_32x32x16_f16 v[48:63], v[116:119], v[180:183], v[48:63]\n\t"
        "v_cvt_pk_f16_f32 v66, v68, v69\n\t"
        "v_rcp_f16_sdwa v65, v65 dst_sel:WORD_1 dst_unused:UNUSED_PRESERVE src0_sel:WORD_1\n\t"
        "v_pk_add_f16 v66, v66, s96\n\t"
        "v_exp_f32 v70, v70\n\t"
        "v_mfma_f32_32x32x16_f16 v[96:111], v[184:187], v[132:135], 0\n\t"
        "s_waitcnt lgkmcnt(1)\n\t"
        "v_mad_u32_u16 v120, v120, s95, v167 op_sel:[1,0,0,0]\n\t"
        "ds_read_b64_tr_b16 v[176:177], v120\n\t"
        "v_mad_u32_u16 v121, v121, s95, v167 op_sel:[1,0,0,0]\n\t"
        "ds_read_b64_tr_b16 v[178:179], v121\n\t"
        "v_mad_u32_u16 v122, v122, s95, v167 op_sel:[1,0,0,0]\n\t"
        "ds_read_b64_tr_b16 v[180:181], v122\n\t"
        "v_mad_u32_u16 v123, v123, s95, v167 op_sel:[1,0,0,0]\n\t"
        "ds_read_b64_tr_b16 v[182:183], v123\n\t"
        "v_cvt_pk_f16_f32 v81, v82, v83\n\t"
        "v_rcp_f16_sdwa v66, v66 dst_sel:WORD_0 dst_unused:UNUSED_PRESERVE src0_sel:WORD_0\n\t"
        "v_pk_mul_f16 v81, v81, v65\n\t"
        "v_exp_f32 v71, v71\n\t"
        "v_mfma_f32_32x32x16_f16 v[96:111], v[188:191], v[140:143], v[96:111]\n\t"
        "s_waitcnt lgkmcnt(4)\n\t"
        "v_mad_u32_u24 v124, v124, s95, v168\n\t"
        "ds_read_b128 v[184:187], v124\n\t"
        "ds_read_b128 v[188:191], v124 offset:16\n\t"
        "v_cvt_pk_f16_f32 v67, v70, v71\n\t"
        "v_rcp_f16_sdwa v66, v66 dst_sel:WORD_1 dst_unused:UNUSED_PRESERVE src0_sel:WORD_1\n\t"
        "v_pk_add_f16 v67, v67, s96\n\t"
        "v_exp_f32 v72, v72\n\t"
        "v_mfma_f32_32x32x16_bf16 v[96:111], v[192:195], v[220:223], v[96:111]\n\t"
        "s_waitcnt vmcnt(9)\n\t"
        "ds_read_b128 v[192:195], v173\n\t"
        "ds_read_b32 v125, v163 offset:512\n\t"
        "v_cvt_pk_f16_f32 v82, v84, v85\n\t"
        "v_rcp_f16_sdwa v67, v67 dst_sel:WORD_0 dst_unused:UNUSED_PRESERVE src0_sel:WORD_0\n\t"
        "v_pk_mul_f16 v82, v82, v66\n\t"
        "v_exp_f32 v73, v73\n\t"
        "v_mfma_f32_32x32x16_bf16 v[96:111], v[144:147], v[224:227], v[96:111]\n\t"
        "v_cvt_pk_f16_f32 v68, v72, v73\n\t"
        "v_rcp_f16_sdwa v67, v67 dst_sel:WORD_1 dst_unused:UNUSED_PRESERVE src0_sel:WORD_1\n\t"
        "v_pk_add_f16 v68, v68, s96\n\t"
        "v_exp_f32 v74, v74\n\t"
        "v_mfma_f32_32x32x16_bf16 v[96:111], v[148:151], v[228:231], v[96:111]\n\t"
        "v_cvt_pk_f16_f32 v83, v86, v87\n\t"
        "v_rcp_f16_sdwa v68, v68 dst_sel:WORD_0 dst_unused:UNUSED_PRESERVE src0_sel:WORD_0\n\t"
        "v_pk_mul_f16 v83, v83, v67\n\t"
        "v_exp_f32 v75, v75\n\t"
        "v_mfma_f32_32x32x16_bf16 v[96:111], v[152:155], v[232:235], v[96:111]\n\t"
        "s_add_i32 m0, %[ring], 4096\n\t"
        "s_waitcnt lgkmcnt(0)\n\t"
        "v_add_u32 v125, v125, v166\n\t"
        "global_load_lds_dwordx4 v125, %[es]\n\t"
        "v_cvt_pk_f16_f32 v69, v74, v75\n\t"
        "v_rcp_f16_sdwa v68, v68 dst_sel:WORD_1 dst_unused:UNUSED_PRESERVE src0_sel:WORD_1\n\t"
        "v_pk_add_f16 v69, v69, s96\n\t"
        "v_exp_f32 v76, v76\n\t"
        "v_mfma_f32_32x32x16_bf16 v[96:111], v[156:159], v[236:239], v[96:111]\n\t"
        "v_cvt_pk_f16_f32 v84, v88, v89\n\t"
        "v_rcp_f16_sdwa v69, v69 dst_sel:WORD_0 dst_unused:UNUSED_PRESERVE src0_sel:WORD_0\n\t"
        "v_pk_mul_f16 v84, v84, v68\n\t"
        "v_exp_f32 v77, v77\n\t"
        "v_mfma_f32_32x32x16_bf16 v[112:127], v[144:147], v[240:243], 0\n\t"
        "s_waitcnt vmcnt(6)\n\t"
        "ds_read_b128 v[144:147], v169\n\t"
        "v_cvt_pk_f16_f32 v70, v76, v77\n\t"
        "v_rcp_f16_sdwa v69, v69 dst_sel:WORD_1 dst_unused:UNUSED_PRESERVE src0_sel:WORD_1\n\t"
        "v_pk_add_f16 v70, v70, s96\n\t"
        "v_exp_f32 v78, v78\n\t"
        "v_mfma_f32_32x32x16_bf16 v[112:127], v[148:151], v[244:247], v[112:127]\n\t"
        "ds_read_b128 v[148:151], v170\n\t"
        "s_sub_u32 %[nt], %[nt], 1\n\t"
        "v_cvt_pk_f16_f32 v85, v90, v91\n\t"
        "v_rcp_f16_sdwa v70, v70 dst_sel:WORD_0 dst_unused:UNUSED_PRESERVE src0_sel:WORD_0\n\t"
        "v_pk_mul_f16 v85, v85, v69\n\t"
        "v_exp_f32 v79, v79\n\t"
        "v_mfma_f32_32x32x16_bf16 v[112:127], v[152:155], v[248:251], v[112:127]\n\t"
        "ds_read_b128 v[152:155], v171\n\t"
        "ds_read_b128 v[88:91], v162 offset:512\n\t"
        "v_cvt_pk_f16_f32 v71, v78, v79\n\t"
        "v_rcp_f16_sdwa v70, v70 dst_sel:WORD_1 dst_unused:UNUSED_PRESERVE src0_sel:WORD_1\n\t"
        "v_pk_add_f16 v71, v71, s96\n\t"
        "v_exp_f32 v96, v96\n\t"
        "v_exp_f32 v97, v97\n\t"
        "v_mfma_f32_32x32x16_bf16 v[112:127], v[156:159], v[252:255], v[112:127]\n\t"
        "ds_read_b128 v[156:159], v172\n\t"
        "v_cvt_pk_f16_f32 v86, v92, v93\n\t"
        "v_rcp_f16_sdwa v71, v71 dst_sel:WORD_0 dst_unused:UNUSED_PRESERVE src0_sel:WORD_0\n\t"
        "v_pk_mul_f16 v86, v86, v70\n\t"
        "v_exp_f32 v98, v98\n\t"
        "v_cvt_pk_f16_f32 v96, v96, v97\n\t"
        "s_cbranch_scc1 .Lrl_%=\n\t"
        "v_mfma_f32_32x32x16_f16 v[0:15], v[64:67], v[176:179], v[0:15]\n\t"
        "v_rcp_f16_sdwa v71, v71 dst_sel:WORD_1 dst_unused:UNUSED_PRESERVE src0_sel:WORD_1\n\t"
        "v_cvt_pk_f16_f32 v87, v94, v95\n\t"
        "v_pk_add_f16 v96, v96, s96\n\t"
        "v_exp_f32 v99, v99\n\t"
        "v_rcp_f16_sdwa v96, v96 dst_sel:WORD_0 dst_unused:UNUSED_PRESERVE src0_sel:WORD_0\n\t"
        "v_mfma_f32_32x32x16_f16 v[32:47], v[80:83], v[176:179], v[32:47]\n\t"
        "s_mov_b32 m0, %[ring]\n\t"
        "s_waitcnt lgkmcnt(1)\n\t"
        "v_mad_u32_u16 v88, v88, s94, v164\n\t"
        "s_waitcnt lgkmcnt(0)\n\t"
        "global_load_lds_dwordx4 v88, %[x]\n\t"
        "v_pk_mul_f16 v87, v87, v71\n\t"
        "v_cvt_pk_f16_f32 v97, v98, v99\n\t"
        "v_rcp_f16_sdwa v96, v96 dst_sel:WORD_1 dst_unused:UNUSED_PRESERVE src0_sel:WORD_1\n\t"
        "v_pk_add_f16 v97, v97, s96\n\t"
        "v_exp_f32 v100, v100\n\t"
        "v_mfma_f32_32x32x16_f16 v[0:15], v[68:71], v[180:183], v[0:15]\n\t"
        "ds_read_b128 v[92:95], v174 offset:512\n\t"
        "s_add_i32 m0, %[ring], 1024\n\t"
        "v_mad_u32_u16 v89, v89, s94, v165\n\t"
        "global_load_lds_dwordx4 v89, %[x]\n\t"
        "v_cvt_pk_f16_f32 v112, v112, v113\n\t"
        "v_rcp_f16_sdwa v97, v97 dst_sel:WORD_0 dst_unused:UNUSED_PRESERVE src0_sel:WORD_0\n\t"
        "v_pk_mul_f16 v112, v112, v96\n\t"
        "v_exp_f32 v101, v101\n\t"
        "v_mfma_f32_32x32x16_f16 v[32:47], v[84:87], v[180:183], v[32:47]\n\t"
        "s_add_i32 m0, %[ring], 2048\n\t"
        "v_mad_u32_u16 v90, v90, s94, v164\n\t"
        "global_load_lds_dwordx4 v90, %[x]\n\t"
        "v_cvt_pk_f16_f32 v98, v100, v101\n\t"
        "v_rcp_f16_sdwa v97, v97 dst_sel:WORD_1 dst_unused:UNUSED_PRESERVE src0_sel:WORD_1\n\t"
        "v_pk_add_f16 v98, v98, s96\n\t"
        "v_exp_f32 v102, v102\n\t"
        "v_mfma_f32_32x32x16_f16 v[64:79], v[184:187], v[128:131], 0\n\t"
        "s_add_i32 m0, %[ring], 3072\n\t"
        "v_mad_u32_u16 v91, v91, s94, v165\n\t"
        "global_load_lds_dwordx4 v91, %[x]\n\t"
        "v_cvt_pk_f16_f32 v113, v114, v115\n\t"
        "v_rcp_f16_sdwa v98, v98 dst_sel:WORD_0 dst_unused:UNUSED_PRESERVE src0_sel:WORD_0\n\t"
        "v_pk_mul_f16 v113, v113, v97\n\t"
        "v_exp_f32 v103, v103\n\t"
        "v_mfma_f32_32x32x16_f16 v[64:79], v[188:191], v[136:139], v[64:79]\n\t"
        "ds_read_b128 v[80:83], v174 offset:2560\n\t"
        "v_cvt_pk_f16_f32 v99, v102, v103\n\t"
        "v_rcp_f16_sdwa v98, v98 dst_sel:WORD_1 dst_unused:UNUSED_PRESERVE src0_sel:WORD_1\n\t"
        "v_pk_add_f16 v99, v99, s96\n\t"
        "v_exp_f32 v104, v104\n\t"
        "v_mfma_f32_32x32x16_bf16 v[64:79], v[192:195], v[196:199], v[64:79]\n\t"
        "v_cvt_pk_f16_f32 v114, v116, v117\n\t"
        "v_rcp_f16_sdwa v99, v99 dst_sel:WORD_0 dst_unused:UNUSED_PRESERVE src0_sel:WORD_0\n\t"
        "v_pk_mul_f16 v114, v114, v98\n\t"
        "v_exp_f32 v105, v105\n\t"
        "s_waitcnt lgkmcnt(1)\n\t"
        "v_mfma_f32_32x32x16_bf16 v[64:79], v[144:147], v[92:95], v[64:79]\n\t"
        "ds_read_b128 v[84:87], v174 offset:8704\n\t"
        "v_cvt_pk_f16_f32 v100, v104, v105\n\t"
        "v_rcp_f16_sdwa v99, v99 dst_sel:WORD_1 dst_unused:UNUSED_PRESERVE src0_sel:WORD_1\n\t"
        "v_pk_add_f16 v100, v100, s96\n\t"
        "v_exp_f32 v106, v106\n\t"
        "v_mfma_f32_32x32x16_bf16 v[64:79], v[148:151], v[200:203], v[64:79]\n\t"
        "v_cvt_pk_f16_f32 v115, v118, v119\n\t"
        "v_rcp_f16_sdwa v100, v100 dst_sel:WORD_0 dst_unused:UNUSED_PRESERVE src0_sel:WORD_0\n\t"
        "v_pk_mul_f16 v115, v115, v99\n\t"
        "v_exp_f32 v107, v107\n\t"
        "s_waitcnt lgkmcnt(1)\n\t"
        "v_mfma_f32_32x32x16_bf16 v[64:79], v[152:155], v[80:83], v[64:79]\n\t"
        "v_cvt_pk_f16_f32 v101, v106, v107\n\t"
        "v_rcp_f16_sdwa v100, v100 dst_sel:WORD_1 dst_unused:UNUSED_PRESERVE src0_sel:WORD_1\n\t"
        "v_pk_add_f16 v101, v101, s96\n\t"
        "v_exp_f32 v108, v108\n\t"
        "v_mfma_f32_32x32x16_bf16 v[64:79], v[156:159], v[204:207], v[64:79]\n\t"
        "v_cvt_pk_f16_f32 v116, v120, v121\n\t"
        "v_rcp_f16_sdwa v101, v101 dst_sel:WORD_0 dst_unused:UNUSED_PRESERVE src0_sel:WORD_0\n\t"
        "v_pk_mul_f16 v116, v116, v100\n\t"
        "v_exp_f32 v109, v109\n\t"
        "s_waitcnt lgkmcnt(0)\n\t"
        "v_mfma_f32_32x32x16_bf16 v[80:95], v[144:147], v[84:87], 0\n\t"
        "v_add_u32 v160, 0x100, v160\n\t"
        "v_cvt_pk_f16_f32 v102, v108, v109\n\t"
        "v_rcp_f16_sdwa v101, v101 dst_sel:WORD_1 dst_unused:UNUSED_PRESERVE src0_sel:WORD_1\n\t"
        "v_pk_add_f16 v102, v102, s96\n\t"
        "v_exp_f32 v110, v110\n\t"
        "v_mfma_f32_32x32x16_bf16 v[80:95], v[148:151], v[208:211], v[80:95]\n\t"
        "v_add_u32 v161, 0x100, v161\n\t"
        "v_cvt_pk_f16_f32 v117, v122, v123\n\t"
        "v_rcp_f16_sdwa v102, v102 dst_sel:WORD_0 dst_unused:UNUSED_PRESERVE src0_sel:WORD_0\n\t"
        "v_pk_mul_f16 v117, v117, v101\n\t"
        "v_exp_f32 v111, v111\n\t"
        "v_mfma_f32_32x32x16_bf16 v[80:95], v[152:155], v[212:215], v[80:95]\n\t"
        "v_add_u32 v162, 0x100, v162\n\t"
        "v_cvt_pk_f16_f32 v103, v110, v111\n\t"
        "v_rcp_f16_sdwa v102, v102 dst_sel:WORD_1 dst_unused:UNUSED_PRESERVE src0_sel:WORD_1\n\t"
        "v_pk_add_f16 v103, v103, s96\n\t"
        "v_exp_f32 v64, v64\n\t"
        "v_exp_f32 v65, v65\n\t"
        "v_mfma_f32_32x32x16_bf16 v[80:95], v[156:159], v[216:219], v[80:95]\n\t"
        "v_add_u32 v163, 0x100, v163\n\t"
        "v_cvt_pk_f16_f32 v118, v124, v125\n\t"
        "v_rcp_f16_sdwa v103, v103 dst_sel:WORD_0 dst_unused:UNUSED_PRESERVE src0_sel:WORD_0\n\t"
        "v_pk_mul_f16 v118, v118, v102\n\t"
        "v_exp_f32 v66, v66\n\t"
        "v_cvt_pk_f16_f32 v64, v64, v65\n\t"
        "s_branch .Lrt_%=\n\t"
        ".Lrl_%=:\n\t"
        "v_mfma_f32_32x32x16_f16 v[0:15], v[64:67], v[176:179], v[0:15]\n\t"
        "ds_read_b64_tr_b16 v[184:185], v175\n\t"
        "ds_read_b64_tr_b16 v[186:187], v175 offset:512\n\t"
        "ds_read_b64_tr_b16 v[188:189], v175 offset:1024\n\t"
        "ds_read_b64_tr_b16 v[190:191], v175 offset:1536\n\t"
        "v_rcp_f16_sdwa v71, v71 dst_sel:WORD_1 dst_unused:UNUSED_PRESERVE src0_sel:WORD_1\n\t"
        "v_cvt_pk_f16_f32 v87, v94, v95\n\t"
        "v_pk_add_f16 v96, v96, s96\n\t"
        "v_exp_f32 v99, v99\n\t"
        "v_rcp_f16_sdwa v96, v96 dst_sel:WORD_0 dst_unused:UNUSED_PRESERVE src0_sel:WORD_0\n\t"
        "v_mfma_f32_32x32x16_f16 v[32:47], v[80:83], v[176:179], v[32:47]\n\t"
        "ds_read_b64_tr_b16 v[192:193], v175 offset:2048\n\t"
        "ds_read_b64_tr_b16 v[194:195], v175 offset:2560\n\t"
        "ds_read_b64_tr_b16 v[196:197], v175 offset:3072\n\t"
        "ds_read_b64_tr_b16 v[198:199], v175 offset:3584\n\t"
        "v_pk_mul_f16 v87, v87, v71\n\t"
        "v_cvt_pk_f16_f32 v97, v98, v99\n\t"
        "v_rcp_f16_sdwa v96, v96 dst_sel:WORD_1 dst_unused:UNUSED_PRESERVE src0_sel:WORD_1\n\t"
        "v_pk_add_f16 v97, v97, s96\n\t"
        "v_exp_f32 v100, v100\n\t"
        "v_mfma_f32_32x32x16_f16 v[0:15], v[68:71], v[180:183], v[0:15]\n\t"
        "ds_read_b64_tr_b16 v[200:201], v175 offset:32\n\t"
        "ds_read_b64_tr_b16 v[202:203], v175 offset:544\n\t"
        "ds_read_b64_tr_b16 v[204:205], v175 offset:1056\n\t"
        "ds_read_b64_tr_b16 v[206:207], v175 offset:1568\n\t"
        "s_waitcnt lgkmcnt(14)\n\t"
        "v_lshlrev_b32 v216, 16, v144\n\t"
        "v_and_b32 v217, 0xffff0000, v144\n\t"
        "v_cvt_pk_f16_f32 v112, v112, v113\n\t"
        "v_rcp_f16_sdwa v97, v97 dst_sel:WORD_0 dst_unused:UNUSED_PRESERVE src0_sel:WORD_0\n\t"
        "v_pk_mul_f16 v112, v112, v96\n\t"
        "v_exp_f32 v101, v101\n\t"
        "v_mfma_f32_32x32x16_f16 v[32:47], v[84:87], v[180:183], v[32:47]\n\t"
        "ds_read_b64_tr_b16 v[208:209], v175 offset:2080\n\t"
        "ds_read_b64_tr_b16 v[210:211], v175 offset:2592\n\t"
        "ds_read_b64_tr_b16 v[212:213], v175 offset:3104\n\t"
        "ds_read_b64_tr_b16 v[214:215], v175 offset:3616\n\t"
        "v_lshlrev_b32 v218, 16, v145\n\t"
        "v_and_b32 v219, 0xffff0000, v145\n\t"
        "v_cvt_pk_f16_f32 v98, v100, v101\n\t"
        "v_rcp_f16_sdwa v97, v97 dst_sel:WORD_1 dst_unused:UNUSED_PRESERVE src0_sel:WORD_1\n\t"
        "v_pk_add_f16 v98, v98, s96\n\t"
        "v_exp_f32 v102, v102\n\t"
        "v_lshlrev_b32 v220, 16, v146\n\t"
        "v_and_b32 v221, 0xffff0000, v146\n\t"
        "v_lshlrev_b32 v222, 16, v147\n\t"
        "v_and_b32 v223, 0xffff0000, v147\n\t"
        "s_waitcnt lgkmcnt(14)\n\t"
        "v_lshlrev_b32 v224, 16, v148\n\t"
        "v_and_b32 v225, 0xffff0000, v148\n\t"
        "v_cvt_pk_f16_f32 v113, v114, v115\n\t"
        "v_rcp_f16_sdwa v98, v98 dst_sel:WORD_0 dst_unused:UNUSED_PRESERVE src0_sel:WORD_0\n\t"
        "v_pk_mul_f16 v113, v113, v97\n\t"
        "v_exp_f32 v103, v103\n\t"
        "v_lshlrev_b32 v226, 16, v149\n\t"
        "v_and_b32 v227, 0xffff0000, v149\n\t"
        "v_lshlrev_b32 v228, 16, v150\n\t"
        "v_and_b32 v229, 0xffff0000, v150\n\t"
        "v_lshlrev_b32 v230, 16, v151\n\t"
        "v_and_b32 v231, 0xffff0000, v151\n\t"
        "v_cvt_pk_f16_f32 v99, v102, v103\n\t"
        "v_rcp_f16_sdwa v98, v98 dst_sel:WORD_1 dst_unused:UNUSED_PRESERVE src0_sel:WORD_1\n\t"
        "v_pk_add_f16 v99, v99, s96\n\t"
        "v_exp_f32 v104, v104\n\t"
        "s_waitcnt lgkmcnt(14)\n\t"
        "v_mfma_f32_32x32x16_bf16 v[64:79], v[184:187], v[144:147], v[216:231]\n\t"
        "v_lshlrev_b32 v232, 16, v152\n\t"
        "v_and_b32 v233, 0xffff0000, v152\n\t"
        "v_cvt_pk_f16_f32 v114, v116, v117\n\t"
        "v_rcp_f16_sdwa v99, v99 dst_sel:WORD_0 dst_unused:UNUSED_PRESERVE src0_sel:WORD_0\n\t"
        "v_pk_mul_f16 v114, v114, v98\n\t"
        "v_exp_f32 v105, v105\n\t"
        "s_waitcnt lgkmcnt(12)\n\t"
        "v_mfma_f32_32x32x16_bf16 v[64:79], v[188:191], v[148:151], v[64:79]\n\t"
        "v_lshlrev_b32 v234, 16, v153\n\t"
        "v_and_b32 v235, 0xffff0000, v153\n\t"
        "v_cvt_pk_f16_f32 v100, v104, v105\n\t"
        "v_rcp_f16_sdwa v99, v99 dst_sel:WORD_1 dst_unused:UNUSED_PRESERVE src0_sel:WORD_1\n\t"
        "v_pk_add_f16 v100, v100, s96\n\t"
        "v_exp_f32 v106, v106\n\t"
        "s_waitcnt lgkmcnt(10)\n\t"
        "v_mfma_f32_32x32x16_bf16 v[64:79], v[192:195], v[152:155], v[64:79]\n\t"
        "v_lshlrev_b32 v236, 16, v154\n\t"
        "v_and_b32 v237, 0xffff0000, v154\n\t"
        "v_cvt_pk_f16_f32 v115, v118, v119\n\t"
        "v_rcp_f16_sdwa v100, v100 dst_sel:WORD_0 dst_unused:UNUSED_PRESERVE src0_sel:WORD_0\n\t"
        "v_pk_mul_f16 v115, v115, v99\n\t"
        "v_exp_f32 v107, v107\n\t"
        "s_waitcnt lgkmcnt(8)\n\t"
        "v_mfma_f32_32x32x16_bf16 v[64:79], v[196:199], v[156:159], v[64:79]\n\t"
        "v_lshlrev_b32 v238, 16, v155\n\t"
        "v_and_b32 v239, 0xffff0000, v155\n\t"
        "v_cvt_pk_f16_f32 v101, v106, v107\n\t"
        "v_rcp_f16_sdwa v100, v100 dst_sel:WORD_1 dst_unused:UNUSED_PRESERVE src0_sel:WORD_1\n\t"
        "v_pk_add_f16 v101, v101, s96\n\t"
        "v_exp_f32 v108, v108\n\t"
        "v_lshlrev_b32 v240, 16, v156\n\t"
        "v_and_b32 v241, 0xffff0000, v156\n\t"
        "v_lshlrev_b32 v242, 16, v157\n\t"
        "v_and_b32 v243, 0xffff0000, v157\n\t"
        "v_lshlrev_b32 v244, 16, v158\n\t"
        "v_and_b32 v245, 0xffff0000, v158\n\t"
        "v_lshlrev_b32 v246, 16, v159\n\t"
        "v_and_b32 v247, 0xffff0000, v159\n\t"
        "v_cvt_pk_f16_f32 v116, v120, v121\n\t"
        "v_rcp_f16_sdwa v101, v101 dst_sel:WORD_0 dst_unused:UNUSED_PRESERVE src0_sel:WORD_0\n\t"
        "v_pk_mul_f16 v116, v116, v100\n\t"
        "v_exp_f32 v109, v109\n\t"
        "s_waitcnt lgkmcnt(6)\n\t"
        "v_mfma_f32_32x32x16_bf16 v[80:95], v[200:203], v[144:147], v[232:247]\n\t"
        "v_cvt_pk_f16_f32 v102, v108, v109\n\t"
        "v_rcp_f16_sdwa v101, v101 dst_sel:WORD_1 dst_unused:UNUSED_PRESERVE src0_sel:WORD_1\n\t"
        "v_pk_add_f16 v102, v102, s96\n\t"
        "v_exp_f32 v110, v110\n\t"
        "s_waitcnt lgkmcnt(4)\n\t"
        "v_mfma_f32_32x32x16_bf16 v[80:95], v[204:207], v[148:151], v[80:95]\n\t"
        "v_cvt_pk_f16_f32 v117, v122, v123\n\t"
        "v_rcp_f16_sdwa v102, v102 dst_sel:WORD_0 dst_unused:UNUSED_PRESERVE src0_sel:WORD_0\n\t"
        "v_pk_mul_f16 v117, v117, v101\n\t"
        "v_exp_f32 v111, v111\n\t"
        "s_waitcnt lgkmcnt(2)\n\t"
        "v_mfma_f32_32x32x16_bf16 v[80:95], v[208:211], v[152:155], v[80:95]\n\t"
        "v_cvt_pk_f16_f32 v103, v110, v111\n\t"
        "v_rcp_f16_sdwa v102, v102 dst_sel:WORD_1 dst_unused:UNUSED_PRESERVE src0_sel:WORD_1\n\t"
        "v_pk_add_f16 v103, v103, s96\n\t"
        "s_waitcnt lgkmcnt(0)\n\t"
        "v_mfma_f32_32x32x16_bf16 v[80:95], v[212:215], v[156:159], v[80:95]\n\t"
        "v_cvt_pk_f16_f32 v118, v124, v125\n\t"
        "v_rcp_f16_sdwa v103, v103 dst_sel:WORD_0 dst_unused:UNUSED_PRESERVE src0_sel:WORD_0\n\t"
        "v_pk_mul_f16 v118, v118, v102\n\t"
        "v_mfma_f32_32x32x16_f16 v[16:31], v[96:99], v[176:179], v[16:31]\n\t"
        "v_rcp_f16_sdwa v103, v103 dst_sel:WORD_1 dst_unused:UNUSED_PRESERVE src0_sel:WORD_1\n\t"
        "v_cvt_pk_f16_f32 v119, v126, v127\n\t"
        "v_mfma_f32_32x32x16_f16 v[48:63], v[112:115], v[176:179], v[48:63]\n\t"
        "v_pk_mul_f16 v119, v119, v103\n\t"
        "v_mfma_f32_32x32x16_f16 v[16:31], v[100:103], v[180:183], v[16:31]\n\t"
        "s_nop 0\n\t"
        "v_mfma_f32_32x32x16_f16 v[48:63], v[116:119], v[180:183], v[48:63]\n\t"
        : "+{v[0:15]}"(Y[0]), "+{v[16:31]}"(Y[1]), "+{v[32:47]}"(Y[2]), "+{v[48:63]}"(Y[3]), "={v[64:79]}"(U[0]),
          "={v[80:95]}"(U[1]), "={v[216:231]}"(X[0]), "={v[232:247]}"(X[1]), "+{v[160:175]}"(AD), [nt] "+s"(nt)
        : "{v[128:143]}"(XAf), [x] "s"(x), [es] "s"(ES), [ring] "s"(ring0), [wave] "s"(wave), [flag] "s"(mode),
          [cu] "n"(cu)
        :
          "memory", "scc", "m0", "s94", "s95", "s96", "v96", "v97", "v98", "v99", "v100", "v101", "v102", "v103",
          "v104", "v105", "v106", "v107", "v108", "v109", "v110", "v111", "v112", "v113", "v114", "v115", "v116",
          "v117", "v118", "v119", "v120", "v121", "v122", "v123", "v124", "v125", "v126", "v127", "v144", "v145",
          "v146", "v147", "v148", "v149", "v150", "v151", "v152", "v153", "v154", "v155", "v156", "v157", "v158",
          "v159", "v176", "v177", "v178", "v179", "v180", "v181", "v182", "v183", "v184", "v185", "v186", "v187",
          "v188", "v189", "v190", "v191", "v192", "v193", "v194", "v195", "v196", "v197", "v198", "v199", "v200",
          "v201", "v202", "v203", "v204", "v205", "v206", "v207", "v208", "v209", "v210", "v211", "v212", "v213",
          "v214", "v215", "v248", "v249", "v250", "v251", "v252", "v253", "v254", "v255");
  }
  SB();
  auto wide = [&]() __attribute__((always_inline)) {
    int l2;
    asm volatile("v_mbcnt_lo_u32_b32 %0, -1, 0\n\tv_mbcnt_hi_u32_b32 %0, -1, %0\n\ts_waitcnt vmcnt(0)" : "=v"(l2) :: "memory");
    AD[0] = la + (4 * (l2 >> 5) + ((l2 >> 2) & 3)) * 16;
    AD[1] = la + (((l2 & 7) << 2) | ((l2 >> 3) & 3)) * 4 + 2;
    AD[2] = la + (l2 >> 3) * 16;
    AD[3] = la + LARR + ((((l2 >> 1) & 7) << 2) | (l2 >> 4)) * 4;
    auto rd = [&](unsigned a) { return *(const LAS bf16x8*)(size_t)a; };
    auto one = [&](bf16x8 v) {
      const u32x4 t = __builtin_bit_cast(u32x4, v);
      return __builtin_bit_cast(bf16x8, t | (t >> 3));
    };
    bf16x8 xq[4];
    {
      f32x16 T[2] = {zero16, zero16};
#pragma unroll
      for (int k = 0; k < 4; ++k) {
        u32x4 t;
#pragma unroll
        for (int j = 0; j < 4; ++j) t[j] = pack_bf16(X[k >> 1][8 * (k & 1) + 2 * j], X[k >> 1][8 * (k & 1) + 2 * j + 1]);
#pragma unroll
        for (int ct = 0; ct < 2; ++ct)
          T[ct] = MFMA32(__builtin_bit_cast(bf16x8, t), rawfrag(AD[15] + (NBLOB - 18 + k) * 1024 + ct * 32), T[ct]);
      }
#pragma unroll
      for (int q = 0; q < 4; ++q) {
        u32x4 t;
#pragma unroll
        for (int j = 0; j < 4; ++j)
          t[j] = pack_bf16(T[q & 1][8 * (q >> 1) + 2 * j] * NLOG2E, T[q & 1][8 * (q >> 1) + 2 * j + 1] * NLOG2E);
        xq[q] = __builtin_bit_cast(bf16x8, t);
      }
    }
#pragma unroll
    for (int i = 0; i < 16; ++i) { Y[0][i] = 1e-6f * (1 << RA); Y[1][i] = 1e-6f * (1 << RA); Y[2][i] = 0.f; Y[3][i] = 0.f; }
#pragma unroll 1
    do {
      const int n = slow ? gen() : cnt;
      issue_gather(0, 0);
#pragma unroll 1
      for (int t = 0; t * 32 < n; ++t) {
        const unsigned ro = (t & 1) * ringd;
        asm volatile("s_waitcnt lgkmcnt(0)" ::: "memory");
        issue_gather(t + 1, ~t & 1);
        const u32x4 ss = ld128(AD[0] + t * 128);
        const unsigned st = *(const LAS unsigned short*)(size_t)(AD[1] + t * 128) * TPITCH + AD[8];
        s16x4 a[4];
#pragma unroll
        for (int q = 0; q < 4; ++q)
          a[q] = __builtin_amdgcn_ds_read_tr16_b64_v4i16((LAS s16x4*)(size_t)((ss[q] >> 16) * TPITCH + AD[7]));
        const bf16x8 S0 = one(__builtin_shufflevector(a[0], a[1], 0, 1, 2, 3, 4, 5, 6, 7));
        const bf16x8 S1 = one(__builtin_shufflevector(a[2], a[3], 0, 1, 2, 3, 4, 5, 6, 7));
        const bf16x8 T0 = one(rd(st)), T1 = one(rd(st + 16));
        asm volatile("s_waitcnt vmcnt(5)" ::: "memory");
#pragma unroll
        for (int ct = 0; ct < 2; ++ct) {
          const unsigned w = AD[14] + OFF_W + ct * 4096;
          f32x16 G = MFMA32(T0, xq[ct], zero16), V = zero16;
          G = MFMA32(T1, xq[2 + ct], G);
          G = MFMA32(rd(AD[13] + ro), rd(AD[14] + OFF_W + (16 + ct) * 1024), G);
#pragma unroll
          for (int k = 0; k < 4; ++k) {
            const bf16x8 as = rd(AD[9 + k] + ro);
            G = MFMA32(as, rd(w + k * 1024), G);
            V = MFMA32(as, rd(w + (8 + k) * 1024), V);
          }
          u32x4 pd[2], pn[2];
#pragma unroll
          for (int i = 0; i < 16; i += 2) {
            constexpr float u = 1.0f / (1 << RA);
            const float r0 = __builtin_amdgcn_rcpf(__builtin_fmaf(__builtin_amdgcn_exp2f(G[i]), u, u));
            const float r1 = __builtin_amdgcn_rcpf(__builtin_fmaf(__builtin_amdgcn_exp2f(G[i + 1]), u, u));
            pd[i >> 3][(i >> 1) & 3] = pack_bf16(r0, r1);
            pn[i >> 3][(i >> 1) & 3] = pack_bf16(r0 * V[i], r1 * V[i + 1]);
          }
          Y[ct] = MFMA32(__builtin_bit_cast(bf16x8, pd[0]), S0, Y[ct]);
          Y[ct] = MFMA32(__builtin_bit_cast(bf16x8, pd[1]), S1, Y[ct]);
          Y[2 + ct] = MFMA32(__builtin_bit_cast(bf16x8, pn[0]), S0, Y[2 + ct]);
          Y[2 + ct] = MFMA32(__builtin_bit_cast(bf16x8, pn[1]), S1, Y[2 + ct]);
          SB();
        }
      }
      asm volatile("s_waitcnt vmcnt(0) lgkmcnt(0)" ::: "memory");
    } while (slow && gs < GEND);
  };

  const unsigned ot = ring0 + (max(cnt - 1, 0) & 32 ? 0u : ringd);
  auto finish = [&](const bool check) __attribute__((always_inline)) {
    auto inv = [&](f32x16& y) {
      y *= VSC;
#pragma unroll
      for (int i = 0; i < 16; ++i) y[i] = __builtin_amdgcn_rcpf(y[i]);
    };
    inv(Y[0]);
    SB();
    int lane;
    asm volatile("v_mbcnt_lo_u32_b32 %0, -1, 0\n\tv_mbcnt_hi_u32_b32 %0, -1, %0" : "=v"(lane));
    const int h = lane >> 5;
    const unsigned nd = nodeof(cg + (lane & 31));
    const bool live = (nd * Mw + Cw) >> 16 == cg + (lane & 31);
    const unsigned lm = (unsigned)__ballot(live);
    const int nodes = __builtin_popcount(lm);
    const unsigned n0 = __builtin_amdgcn_readlane(nd, __builtin_ctz(lm | 0x80000000u));
    const int r = live ? nd - n0 : 31;
    SB();
    inv(Y[1]);
    unsigned ov[16];
#pragma unroll
    for (int p = 0; p < 4; ++p) {
      const int ct = p >> 1;
#pragma unroll
      for (int j = 0; j < 4; ++j) {
        const int i = 8 * (p & 1) + 2 * j;
        const f32x2 z = __builtin_elementwise_fma(f32x2{Y[2 + ct][i], Y[2 + ct][i + 1]}, f32x2{Y[ct][i], Y[ct][i + 1]},
                                                  f32x2{U[ct][i], U[ct][i + 1]});
        ov[4 * p + j] = pack_bf16(MAXN(z[0], X[ct][i]), MAXN(z[1], X[ct][i + 1]));
      }
      *(LAS u32x4*)(size_t)(ot + r * 128 + (((4 * h + p) ^ (r & 7)) * 16)) =
          u32x4{ov[4 * p], ov[4 * p + 1], ov[4 * p + 2], ov[4 * p + 3]};
    }
    u32x4 ow[4];
#pragma unroll
    for (int q = 0; q < 4; ++q)
      ow[q] = *(const LAS u32x4*)(size_t)(ot + (8 * q + (lane >> 3)) * 128 + (((lane & 7) ^ (lane >> 3)) * 16));
    asm volatile("v_cmpx_lt_i32 vcc, 0, %6\n\t"
                 "global_store_dwordx4 %0, %1, %5 sc1\n\t"
                 "v_cmpx_lt_i32 vcc, 8, %6\n\t"
                 "global_store_dwordx4 %0, %2, %5 offset:1024 sc1\n\t"
                 "v_cmpx_lt_i32 vcc, 16, %6\n\t"
                 "global_store_dwordx4 %0, %3, %5 offset:2048 sc1\n\t"
                 "v_cmpx_lt_i32 vcc, 24, %6\n\t"
                 "global_store_dwordx4 %0, %4, %5 offset:3072 sc1\n\t"
                 "s_mov_b64 exec, -1"
                 :: "v"((unsigned)n0 * 128u + lane * 16u), "v"(ow[0]), "v"(ow[1]), "v"(ow[2]), "v"(ow[3]), "s"(out),
                    "v"(nodes - (lane >> 3))
                 : "memory", "vcc");
    SB();
    if (!check) return false;
    constexpr float WLIM = 1 << (22 - RA + VK);
    float wm = MAXN(Y[0][0], Y[1][0]);
#pragma unroll
    for (int i = 1; i < 16; ++i) wm = MAXN(MAXN(wm, Y[0][i]), Y[1][i]);
    f16x2 om = __builtin_bit_cast(f16x2, ov[0]);
#pragma unroll
    for (int i = 1; i < 16; ++i) om = MAXN(om, __builtin_bit_cast(f16x2, ov[i]));
    const _Float16 o1 = MAXN(om[0], om[1]);
    const unsigned long long lv = lm * 0x100000001ull;
    unsigned long long bad = __ballot(o1 != o1) | (__ballot(o1 < __builtin_bit_cast(_Float16, (unsigned short)((127 - 9 + VK) << 7))) & lv);
    if (__builtin_expect((__ballot(wm > WLIM / LCAP) & lv) != 0ull, 0)) {
      LAS unsigned* dg = (LAS unsigned*)(size_t)ot;
      dg[lane & 31] = 0u;
#pragma unroll 1
      for (int i = lane; i < cnt; i += 64)
        __hip_atomic_fetch_add(dg + (*(const LAS unsigned short*)(size_t)(la + i * 4 + 2) & 31), 1u, __ATOMIC_RELAXED,
                               __HIP_MEMORY_SCOPE_WORKGROUP);
      bad |= __ballot(wm * (float)dg[lane & 31] > WLIM) & lv;
    }
    return slow || bad != 0ull;
  };
  if (__builtin_expect(finish(true), 0)) {
    wide();
    finish(false);
  }
  if (bk == 0 && wave == 7) asm volatile("global_store_dwordx2 %0, %1, %2 sc1" :: "v"(0u), "v"(ep), "s"(FL) : "memory");
  asm volatile("s_waitcnt vmcnt(0)" ::: "memory");
}

extern "C" void kernel_launch(void* const* d_in, const int* in_sizes, int n_in,
                              void* d_out, int out_size, void* d_ws, size_t ws_size,
                              hipStream_t stream) {
  const unsigned short* x = (const unsigned short*)d_in[0];
  const u32x4* eattr = (const u32x4*)d_in[1];
  const int* src = (const int*)d_in[2];
  const int* dst = (const int*)d_in[3];
  const unsigned short* wa = (const unsigned short*)d_in[4];
  const unsigned short* wb = (const unsigned short*)d_in[5];
  const unsigned short* wc = (const unsigned short*)d_in[6];
  const unsigned short* wu = (const unsigned short*)d_in[7];
  const unsigned short* wv = (const unsigned short*)d_in[8];
  unsigned short* out = (unsigned short*)d_out;
  const int N = in_sizes[0] / 64;
  const int E = in_sizes[2];
  const int chunk = (((E + NCH - 1) / NCH) + 3) & ~3;

  const bool skew = N > 4096 && N < 60000;
  const unsigned M = N > 256 ? (unsigned)(4294967295.0 / ((double)N * (skew ? 1.0 + SKEWF : 1.0))) : 0xffffffu;
  const unsigned N1 = (0x7fffffffu / M) + 1u;
  const unsigned M2 = skew ? (0xffffffffu - N1 * M) / ((unsigned)N - N1) : M;

  auto fb = [](double v) { return __builtin_bit_cast(unsigned, (float)v); };
  char* ws = (char*)d_ws;
  size_t off = 0;
  off = 1 << 20;
  unsigned long long* FL = (unsigned long long*)(ws + off);   off += 1 << 20;
  u32x4* ENT = (u32x4*)(ws + off);                       off += (size_t)2 * IEXT;
  unsigned long long* TS = (unsigned long long*)(ws + off);

  k_main<<<NBK, 512, 0, stream>>>(x, eattr, src, dst, wa, wb, wc, wu, wv, ENT, TS, FL, out, N, E, chunk, M, N1,
                                  M2 - M, fb(65536.0 / M), fb(65536.0 / M2), fb(32768.0 / M + 0.5),
                                  fb((double)N1 + (32768.0 - (double)N1 * M) / M2 + 0.5)
     );
}
```

```cpp
#define QSEL 14
#define DBGDUMP 1
#include <hip/hip_runtime.h>
#include <hip/hip_bf16.h>
#include <stdint.h>

using bf16x8 = __attribute__((ext_vector_type(8))) short;
using s16x4 = __attribute__((ext_vector_type(4))) short;
using f32x16 = __attribute__((ext_vector_type(16))) float;
using f32x2 = __attribute__((ext_vector_type(2))) float;
using u32x4 = __attribute__((ext_vector_type(4))) unsigned;
using u32x16 = __attribute__((ext_vector_type(16))) unsigned;
using u32x2 = __attribute__((ext_vector_type(2))) unsigned;
using f16x2 = __attribute__((ext_vector_type(2))) _Float16;

#define MFMA32(a, b, c) __builtin_amdgcn_mfma_f32_32x32x16_bf16((a), (b), (c), 0, 0, 0)
#define RA 14
#define VK 4
#define LAS __attribute__((address_space(3)))
#define RANKOF(w) ((w) < 4 ? 7 - (w) : (w) - 4)
#define G1W 4
#define DBGWAIT 1
#define CPL 5
#define CPM 31
#define CPFB 19712
#define PSL 12
#define SKEWF 0.015
#define XLIKE(c) __builtin_expect(c, 0)
#define SB() __builtin_amdgcn_sched_barrier(0)

constexpr int NCH = 256;
constexpr int NBK = 256;
constexpr int NFRAG = 39;
constexpr float NLOG2E = -1.44269504088896340736f;
constexpr float VSC = 1.0f / (1 << VK);
constexpr unsigned TPITCH = 80;

__device__ __forceinline__ unsigned pack_bf16(float lo, float hi) {
  typedef __attribute__((ext_vector_type(2))) float f2;
  typedef __attribute__((ext_vector_type(2))) __bf16 b2;
  f2 v = {lo, hi};
  b2 r = __builtin_convertvector(v, b2);
  return __builtin_bit_cast(unsigned, r);
}

__device__ __forceinline__ unsigned pack_f16(float lo, float hi) {
  unsigned r;
  asm("v_cvt_pk_f16_f32 %0, %1, %2" : "=v"(r) : "v"(lo), "v"(hi));
  return r;
}

__device__ __forceinline__ void glds16(const void* sbase, unsigned voff, unsigned lds_dst) {
  asm volatile(
      "s_mov_b32 m0, %2\n\t"
      "s_nop 0\n\t"
      "global_load_lds_dwordx4 %0, %1"
      :: "v"(voff), "s"(sbase), "s"(lds_dst) : "memory", "m0");
}
__device__ __forceinline__ void glds16x(const void* sbase, unsigned a, unsigned c, unsigned lds_dst) {
  unsigned tmp;
  asm volatile(
      "s_mov_b32 m0, %4\n\t"
      "v_mad_u32_u16 %0, %1, %5, %2\n\t"
      "global_load_lds_dwordx4 %0, %3"
      : "=&v"(tmp) : "v"(a), "v"(c), "s"(sbase), "s"(lds_dst), "s"(128u) : "memory", "m0");
}
__device__ __forceinline__ void glds16e(const void* sbase, unsigned a, unsigned c, unsigned lds_dst) {
  unsigned tmp;
  asm volatile(
      "s_mov_b32 m0, %4\n\t"
      "v_add_u32 %0, %1, %2\n\t"
      "global_load_lds_dwordx4 %0, %3"
      : "=&v"(tmp) : "v"(a), "v"(c), "s"(sbase), "s"(lds_dst) : "memory", "m0");
}
__device__ __forceinline__ void gstore128wt(void* sbase, unsigned voff, u32x4 v) {
  asm volatile("global_store_dwordx4 %0, %1, %2 sc1\n\ts_nop 1" :: "v"(voff), "v"(v), "s"(sbase) : "memory");
}

constexpr int IBOX = 40960;
constexpr int IEXT = 1 << 24;
constexpr int CPIT = 208;
constexpr unsigned RINC = 87382;
constexpr int KP_HDR = 0;
constexpr int KP_ENT = KP_HDR + NBK * 4;
constexpr int KP_POS = KP_ENT + NBK * CPIT;
constexpr int KP_LDS = KP_POS + NBK * 4;
constexpr int NWL = 26;
constexpr int NBLOB = 31;

__device__ __forceinline__ unsigned mul24s(unsigned a, unsigned b) {
  unsigned r;
  asm("v_mul_u32_u24 %0, %1, %2" : "=v"(r) : "s"(b), "v"(a));
  return r;
}
__device__ __forceinline__ unsigned byte3x(unsigned a, unsigned b) {
  unsigned r;
  asm("v_mul_u32_u24_sdwa %0, %1, %2 dst_sel:DWORD dst_unused:UNUSED_PAD src0_sel:DWORD src1_sel:BYTE_3" : "=v"(r) : "s"(b), "v"(a));
  return r;
}
constexpr int RING_SLOT = 5 * 1024;
constexpr int LCAP = 512;
constexpr int LARR = 2560;
constexpr int LIST_BYTES = 2 * LARR;
constexpr int OFF_CNT = 0;
constexpr int SUBL = LARR / 4;
constexpr int OFF_XF = 256;
constexpr int OFF_W = 512;
constexpr int OFF_T1 = OFF_W + NWL * 1024;
constexpr int OFF_T2 = OFF_T1 + 32 * TPITCH;
constexpr int OFF_R0 = OFF_W + NFRAG * 1024;
constexpr int OFF_R1 = OFF_R0 + 8 * RING_SLOT;
constexpr int OFF_L = OFF_R1 + 8 * RING_SLOT;
constexpr int MAIN_LDS = OFF_L + 8 * LIST_BYTES;
static_assert(MAIN_LDS <= 160 * 1024, "LDS");
static_assert(KP_LDS <= 16 * RING_SLOT, "phase 1 lives in the ring");

__device__ __forceinline__ unsigned ldd(const unsigned* p) {
  return __hip_atomic_load(p, __ATOMIC_RELAXED, __HIP_MEMORY_SCOPE_AGENT);
}
__device__ __forceinline__ unsigned long long ldq(const unsigned long long* p) {
  return __hip_atomic_load(p, __ATOMIC_RELAXED, __HIP_MEMORY_SCOPE_AGENT);
}
__device__ __forceinline__ bf16x8 rawfrag(unsigned a) {
  const s16x4 a0 = __builtin_amdgcn_ds_read_tr16_b64_v4i16((LAS s16x4*)(size_t)a);
  const s16x4 a1 = __builtin_amdgcn_ds_read_tr16_b64_v4i16((LAS s16x4*)(size_t)(a + 512));
  return __builtin_shufflevector(a0, a1, 0, 1, 2, 3, 4, 5, 6, 7);
}
#define MAXN(a, b) __builtin_elementwise_maximum(a, b)

__global__ __launch_bounds__(512, 2) void k_main(const unsigned short* __restrict__ x,
                                                 const u32x4* __restrict__ ES,
                                                 const int* __restrict__ src, const int* __restrict__ dst,
                                                 const unsigned short* __restrict__ wa_g,
                                                 const unsigned short* __restrict__ wb_g,
                                                 const unsigned short* __restrict__ wc_g,
                                                 const unsigned short* __restrict__ wu_g,
                                                 const unsigned short* __restrict__ wv_g,
                                                 u32x4* ENT, unsigned long long* TS, unsigned long long* FL,
                                                 unsigned short* __restrict__ out, int N, int E, int chunk,
                                                 unsigned M, unsigned N1, unsigned DM, unsigned MF0, unsigned MF1,
                                                 unsigned MB0, unsigned MB1
                                                 ) {
  unsigned tm0v = (unsigned)__builtin_amdgcn_s_memrealtime(); __builtin_amdgcn_sched_barrier(0);
  unsigned stv = 0;
#define PT(i) asm volatile("s_memrealtime s[92:93]\n\ts_waitcnt lgkmcnt(0)\n\tv_writelane_b32 %0, s92, " #i : "+v"(stv) :: "memory", "s92", "s93")
#define PV(i, v) asm volatile("v_writelane_b32 %0, %1, " #i : "+v"(stv) : "s"(v))

  __shared__ __attribute__((aligned(16))) char smem[MAIN_LDS + 512];
  const int tid0 = threadIdx.x;
  const int wave = __builtin_amdgcn_readfirstlane(tid0 >> 6);
  const int bk = blockIdx.x;
  const unsigned lds0 = (unsigned)(size_t)(LAS char*)smem;

  LAS char* sm = (LAS char*)smem + OFF_R0;
  const unsigned sm0 = lds0 + OFF_R0;
  const int tid = tid0, lane = tid & 63, r = lane & 31, h = lane >> 5;
  const unsigned pc = tid & 7, bq = tid >> 3;
  unsigned ia = sm0 + KP_ENT + bq * CPIT + pc * 16;
#pragma unroll
  for (int s = 0; s < 4; ++s) *(LAS u32x4*)(size_t)(ia + s * 64 * CPIT) = u32x4{~0u, ~0u, ~0u, ~0u};
  *(LAS unsigned*)(sm + KP_HDR + (tid & 255) * 4) = 0u;
  int q0 = 4 * tid + 3, q1 = 4 * tid + 1795;
  unsigned a0 = tid * 16, a1 = tid * 16 + 7168, z0 = 0u;
  unsigned long long m7 = __ballot(tid == 448);
  int n7 = wave == 7 ? 0 : 0x7fffffff;
  unsigned f0 = min((((bk >> 3) & CPM) * CPL + lane) * 128u, CPFB - 512u);
  unsigned long long p7 = __ballot(tid >= 448 && tid < 448 + CPL);
  asm volatile("" : "+v"(f0), "+s"(p7));
  asm volatile("" : "+v"(ia), "+v"(q0), "+v"(q1), "+v"(a0), "+v"(a1), "+v"(z0), "+s"(m7), "+s"(n7) :: "memory");
  SB();
  asm volatile("" :: "s"(x), "s"(ES), "s"(src), "s"(dst), "s"(wa_g), "s"(wb_g), "s"(wc_g), "s"(wu_g), "s"(wv_g), "s"(ENT),
               "s"(TS), "s"(FL), "s"(out), "s"(N), "s"(E), "s"(chunk), "s"(M), "s"(N1), "s"(DM), "s"(MF0), "s"(MF1), "s"(MB0), "s"(MB1));
  const int base = bk * chunk;
  asm volatile("" : "+v"(tm0v));
  const int ecnt = max(min(chunk, E - base), 0);
  unsigned long long on[2];
  on[0] = __ballot(q0 < min(min(chunk, E - base), n7));
  on[1] = __ballot(q1 < min(min(chunk, E - base), n7));
  a0 += base * 4u;
  a1 += base * 4u;
  u32x4 d[2], sv[2];
  u32x2 e0;
  asm volatile("s_mov_b64 exec, %11\n\t"
               "global_load_dwordx4 %0, %5, %8 nt\n\t"
               "s_mov_b64 exec, %12\n\t"
               "global_load_dwordx4 %1, %6, %8 nt\n\t"
               "s_mov_b64 exec, %13\n\t"
               "global_load_dwordx2 %2, %7, %9 sc1\n\t"
               "s_getpc_b64 s[96:97]\n\t"
               "s_mov_b32 m0, %15\n\t"
               "s_mov_b64 exec, %16\n\t"
               "global_load_lds_dword %14, s[96:97]\n\t"
               "s_mov_b64 exec, %11\n\t"
               "s_waitcnt lgkmcnt(0)\n\t"
               "s_barrier\n\t"
               "global_load_dwordx4 %3, %5, %10 nt\n\t"
               "s_mov_b64 exec, %12\n\t"
               "global_load_dwordx4 %4, %6, %10 nt\n\t"
               "s_mov_b64 exec, -1"
               : "=&{v[240:243]}"(d[0]), "=&{v[244:247]}"(d[1]), "=&{v[238:239]}"(e0), "=&{v[248:251]}"(sv[0]),
                 "=&{v[252:255]}"(sv[1])
               : "v"(a0), "v"(a1), "v"(z0), "s"(dst), "s"(FL), "s"(src), "s"(on[0]), "s"(on[1]), "s"(m7)
                 , "v"(f0), "n"(OFF_L + 7 * LIST_BYTES + 4096), "s"(p7)
               : "memory", "m0", "s96", "s97");
  SB();
  const int bu = (bk >> 1) | ((bk & 1) << 7);
  const unsigned Mw = bk & 1 ? M + DM : M, Cw = bk & 1 ? 0u - N1 * DM : 0u;
  unsigned MF = bk & 1 ? MF1 : MF0, MB = bk & 1 ? MB1 : MB0;
  asm volatile("" : "+s"(MF), "+s"(MB));
  char* ib = reinterpret_cast<char*>(ENT) + (size_t)bu * IBOX;
  auto nodeof = [&](unsigned c) { return min((unsigned)__builtin_fmaf((float)c, __uint_as_float(MF), __uint_as_float(MB)), (unsigned)N - 1u); };
  unsigned tup[8], val[8], pm[8];
  u32x4 xg[4];
  const unsigned xn0 = __builtin_amdgcn_readfirstlane(nodeof((bu * 8 + wave) * 32));
  unsigned xd[4];
  auto xprep = [&]() {
#pragma unroll
    for (int i = 0; i < 4; ++i)
      xd[i] = min(xn0 + 8 * i + (lane >> 3), N - 1u) * 128u + ((((lane & 7) ^ (lane >> 4)) * 16) ^ (i & 1) * 64) - i * 1024u;
    asm volatile("" : "+v"(xd[0]), "+v"(xd[1]), "+v"(xd[2]), "+v"(xd[3]));
  };
  auto xdma = [&]() {
    asm volatile("s_mov_b32 m0, %5\n\ts_nop 0\n\t"
                 "global_load_lds_dwordx4 %0, %4\n\t"
                 "global_load_lds_dwordx4 %1, %4 offset:1024\n\t"
                 "global_load_lds_dwordx4 %2, %4 offset:2048\n\t"
                 "global_load_lds_dwordx4 %3, %4 offset:3072"
                 :: "v"(xd[0]), "v"(xd[1]), "v"(xd[2]), "v"(xd[3]), "s"(x), "s"(lds0 + OFF_L + wave * LIST_BYTES)
                 : "memory", "m0");
  };
  auto wload = [&](const unsigned short* w, int f) {
    asm volatile("s_mov_b32 m0, %3\n\ts_nop 0\n\t"
                 "global_load_lds_dwordx4 %0, %2\n\t"
                 "global_load_lds_dwordx4 %0, %2 offset:1024\n\t"
                 "global_load_lds_dwordx4 %0, %2 offset:2048\n\t"
                 "global_load_lds_dwordx4 %0, %2 offset:3072\n\t"
                 "s_addk_i32 m0, 4096\n\ts_nop 0\n\t"
                 "global_load_lds_dwordx4 %1, %2\n\t"
                 "global_load_lds_dwordx4 %1, %2 offset:1024\n\t"
                 "global_load_lds_dwordx4 %1, %2 offset:2048\n\t"
                 "global_load_lds_dwordx4 %1, %2 offset:3072"
                 :: "v"(lane * 16u), "v"(lane * 16u + 4096u), "s"(w), "s"(lds0 + OFF_W + f * 1024) : "memory", "m0", "scc");
  };
  auto wall = [&]() {
    wload(wa_g, NBLOB);
    wload(wb_g, 0);
    wload(wv_g, 8);
    glds16(wc_g, lane * 16, lds0 + OFF_W + 16 * 1024);
    glds16(wc_g + 512, lane * 16, lds0 + OFF_W + 17 * 1024);
  };
  int spin = 1 << 20;
  u32x2 ep = {0u, 0u};
  if (wave == 7) {
    wall();
    *(LAS u32x4*)((LAS char*)smem + OFF_CNT + r * 16) =
        (r & 17) == 0 ? u32x4{lds0 + OFF_L, lds0 + OFF_L + SUBL, lds0 + OFF_L + 2 * SUBL, lds0 + OFF_L + 3 * SUBL}
                      : u32x4{0u, 0u, 0u, 0u};
    {
      const unsigned col = h ? ((r >> 1) & 12u) | ((r << 2) & 16u) | (r & 3u) : r;
      LAS char* tr = (LAS char*)smem + OFF_T1 + lane * TPITCH;
#pragma unroll
      for (int j = 0; j < 4; ++j) *(LAS u32x4*)(tr + j * 16) = u32x4{0u, 0u, 0u, 0u};
      *(LAS unsigned short*)(tr + col * 2) = 0x3C00;
    }
#pragma unroll
    for (int i = 0; i < 8; ++i) { tup[i] = 0u; val[i] = 0u; pm[i] = 0u; }
    asm volatile("s_waitcnt vmcnt(%1)" : "+{v[238:239]}"(e0) : "n"(29) : "memory");
    ep[0] = __builtin_amdgcn_readfirstlane(e0[0]) + 0x9E3779B1u;
    ep[1] = __builtin_amdgcn_readfirstlane(e0[1]) + 0x7F4A7C15u;
    *(LAS u32x2*)((LAS char*)smem + OFF_CNT + 56) = u32x2{ep[0], ep[1] >> 24};
    if (__builtin_expect(ecnt & 3, 0)) {
      const int i = (ecnt & ~3) + lane;
      on[0] = __ballot(i < ecnt);
      if (i < ecnt) {
        pm[0] = (unsigned)dst[base + i] * M + (unsigned)max(dst[base + i] - (int)N1, 0) * DM;
        tup[0] = (unsigned)src[base + i] | (unsigned)i << 16;
        val[0] = __hip_atomic_fetch_add((LAS unsigned*)(sm + KP_HDR) + (pm[0] >> 24), RINC, __ATOMIC_RELAXED, __HIP_MEMORY_SCOPE_WORKGROUP);
        const unsigned u = min(val[0] >> 16, 48u);
        *(LAS unsigned*)(sm + KP_ENT + (pm[0] >> 24) * CPIT + u * 4) = tup[0];
        *(LAS unsigned char*)(sm + KP_ENT + (pm[0] >> 24) * CPIT + ((u * 4) | (u & 3) | 12)) = pm[0] >> 16;
      }
      __builtin_amdgcn_s_waitcnt(0x0F70);
    }
    PT(1);
    xprep();
    xdma();
  } else {
    xprep();
    unsigned inc = RINC, t0 = (unsigned)tid << 18;
    asm volatile("s_waitcnt vmcnt(2)" : "+{v[240:243]}"(d[0]), "+{v[244:247]}"(d[1]), "+v"(inc), "+v"(t0) :: "memory");
    PT(1);
    unsigned cb[8];
#pragma unroll
    for (int s = 0; s < 2; ++s) {
      unsigned* rr = val + 4 * s;
#pragma unroll
      for (int k = 0; k < 4; ++k) {
        unsigned t;
        asm("v_sub_u32 %0, %1, %2 clamp" : "=v"(t) : "v"(d[s][k]), "s"(N1));
        asm("v_mad_u32_u24 %0, %1, %2, %3" : "=v"(pm[4 * s + k]) : "v"(t), "s"(DM), "v"(mul24s(d[s][k], M)));
      }
#pragma unroll
      for (int k = 0; k < 4; ++k) rr[k] = byte3x(pm[4 * s + k], 4u);
      asm volatile(
          "s_mov_b64 exec, %5\n\t"
          "ds_add_rtn_u32 %0, %0, %4 offset:%6\n\t"
          "ds_add_rtn_u32 %1, %1, %4 offset:%6\n\t"
          "ds_add_rtn_u32 %2, %2, %4 offset:%6\n\t"
          "ds_add_rtn_u32 %3, %3, %4 offset:%6\n\t"
          "s_mov_b64 exec, -1"
          : "+v"(rr[0]), "+v"(rr[1]), "+v"(rr[2]), "+v"(rr[3])
          : "v"(inc), "s"(on[s]), "n"(OFF_R0 + KP_HDR)
          : "memory");
    }
#pragma unroll
    for (int i = 0; i < 8; ++i) cb[i] = byte3x(pm[i], (unsigned)CPIT);
    asm volatile("s_waitcnt vmcnt(0)"
                 : "+{v[248:251]}"(sv[0]), "+{v[252:255]}"(sv[1]), "+v"(cb[0]), "+v"(cb[1]), "+v"(cb[2]), "+v"(cb[3]), "+v"(cb[4]), "+v"(cb[5]),
                   "+v"(cb[6]), "+v"(cb[7])
                 :: "memory");
    xdma();
#pragma unroll
    for (int i = 0; i < 8; ++i) tup[i] = sv[i >> 2][i & 3] + t0 + (((i >> 2) * 1792u + (i & 3)) << 16);
#pragma unroll
    for (int s = 0; s < 2; ++s) {
      asm volatile("s_waitcnt lgkmcnt(%8)"
                   : "+v"(val[4 * s]), "+v"(val[4 * s + 1]), "+v"(val[4 * s + 2]), "+v"(val[4 * s + 3]), "+v"(tup[4 * s]),
                     "+v"(tup[4 * s + 1]), "+v"(tup[4 * s + 2]), "+v"(tup[4 * s + 3])
                   : "n"(DBGWAIT ? 0 : s ? 8 : 4) : "memory");
      unsigned pa[4], pb[4];
#pragma unroll
      for (int k = 0; k < 4; ++k) {
        const unsigned u = min(val[4 * s + k], 48u << 16);
        asm("v_mad_u32_u16 %0, %1, 4, %2 op_sel:[1,0,0,0]" : "=v"(pa[k]) : "v"(u), "v"(cb[4 * s + k]));
        pb[k] = pa[k] | ((u >> 16) & 3u) | 12u;
      }
      asm volatile(
          "s_mov_b64 exec, %16\n\t"
          "ds_write_b32 %0, %8 offset:%17\n\t"
          "ds_write_b8_d16_hi %4, %12 offset:%17\n\t"
          "ds_write_b32 %1, %9 offset:%17\n\t"
          "ds_write_b8_d16_hi %5, %13 offset:%17\n\t"
          "ds_write_b32 %2, %10 offset:%17\n\t"
          "ds_write_b8_d16_hi %6, %14 offset:%17\n\t"
          "ds_write_b32 %3, %11 offset:%17\n\t"
          "ds_write_b8_d16_hi %7, %15 offset:%17\n\t"
          "s_mov_b64 exec, -1"
          :: "v"(pa[0]), "v"(pa[1]), "v"(pa[2]), "v"(pa[3]), "v"(pb[0]), "v"(pb[1]), "v"(pb[2]), "v"(pb[3]),
             "v"(tup[4 * s]), "v"(tup[4 * s + 1]), "v"(tup[4 * s + 2]), "v"(tup[4 * s + 3]), "v"(pm[4 * s]),
             "v"(pm[4 * s + 1]), "v"(pm[4 * s + 2]), "v"(pm[4 * s + 3]), "s"(on[s]), "n"(OFF_R0 + KP_ENT)
          : "memory");
    }
  }
  PT(2);
  u32x2 tag;
  int xw = 0;
  {
    unsigned ha = sm0 + KP_HDR + bq * 4, hb = sm0 + KP_HDR + lane * 16;
    unsigned go = bq * IBOX + bk * 128 + pc * 16;
    asm volatile("s_waitcnt lgkmcnt(0)\n\ts_barrier" : "+v"(ha), "+v"(hb), "+v"(go) :: "memory");
    PT(3);
    tag = *(const LAS u32x2*)((LAS char*)smem + OFF_CNT + 56);
    const u32x4 call = *(const LAS u32x4*)(size_t)hb;
    unsigned cn[4];
    u32x4 v[4];
#pragma unroll
    for (int s = 0; s < 4; ++s) {
      cn[s] = *(const LAS unsigned*)(size_t)(ha + s * 256);
      v[s] = *(const LAS u32x4*)(size_t)(ia + s * 64 * CPIT);
    }
    asm volatile("" : "+v"(v[0]), "+v"(v[1]), "+v"(v[2]), "+v"(v[3]), "+v"(tag));
    if (__builtin_expect(__ballot(max(max(call[0], call[1]), max(call[2], call[3])) > 36u * RINC) != 0ull, 0)) {
      LAS unsigned* ovc = (LAS unsigned*)((LAS char*)smem + OFF_CNT + 24);
      LAS unsigned* pos = (LAS unsigned*)(sm + KP_POS);
      if (tid < NBK) {
        const unsigned u = *(const LAS unsigned*)(sm + KP_HDR + tid * 4) >> 16, n = u - (u >> 2);
        if (n > 36u) {
          const unsigned p = (unsigned)base + __hip_atomic_fetch_add(ovc, n - 36u, __ATOMIC_RELAXED, __HIP_MEMORY_SCOPE_WORKGROUP);
          pos[tid] = p;
          __hip_atomic_store(FL + 512 + tid * NCH + bk, ((unsigned long long)(n - 36u) << 32) | p, __ATOMIC_RELAXED,
                             __HIP_MEMORY_SCOPE_AGENT);
        }
      }
      asm volatile("s_waitcnt lgkmcnt(0)\n\ts_barrier" ::: "memory");
#pragma unroll
      for (int i = 0; i < 8; ++i) {
        const unsigned u = val[i] >> 16, n = u - (u >> 2);
        if (((on[i >> 2] >> lane) & 1ull) && n >= 36u)
          __hip_atomic_store(TS + pos[pm[i] >> 24] + (n - 36u), ((unsigned long long)((pm[i] >> 16) & 0xffu) << 32) | tup[i],
                             __ATOMIC_RELAXED, __HIP_MEMORY_SCOPE_AGENT);
      }
      __builtin_amdgcn_s_waitcnt(0x0F70);
      asm volatile("s_waitcnt vmcnt(0)\n\ts_barrier" ::: "memory");
    }
    auto seal = [&](u32x4& w, bool more) {
      w[3] = __builtin_amdgcn_perm(tag[1], w[3], 0x04020100u);
      unsigned sm_ = w[0] + w[1] + w[2] + w[3] + tag[0];
      sm_ += __builtin_amdgcn_update_dpp(0u, sm_, 0xB1, 0xf, 0xf, false);
      sm_ += __builtin_amdgcn_update_dpp(0u, sm_, 0x4E, 0xf, 0xf, false);
      sm_ += __builtin_amdgcn_update_dpp(0u, sm_, 0x141, 0xf, 0xf, false);
      w[2] = pc == 7u ? w[2] - sm_ + (more ? 1u : 0u) : w[2];
    };
#pragma unroll
    for (int s = 0; s < 4; ++s) seal(v[s], cn[s] > 23u * RINC);
    asm volatile("global_store_dwordx4 %0, %1, %5 sc1\n\t"
                 "global_store_dwordx4 %0, %2, %6 sc1\n\t"
                 "global_store_dwordx4 %0, %3, %7 sc1\n\t"
                 "global_store_dwordx4 %0, %4, %8 sc1\n\t"
                 "s_nop 1"
                 :: "v"(go), "v"(v[0]), "v"(v[1]), "v"(v[2]), "v"(v[3]), "s"(ENT), "s"(ENT + 4 * IBOX), "s"(ENT + 8 * IBOX),
                    "s"(ENT + 12 * IBOX)
                 : "memory");
    PT(4);
    if (XLIKE(__ballot(max(max(cn[0], cn[1]), max(cn[2], cn[3])) > 23u * RINC) != 0ull)) {
      unsigned xm = 0u;
#pragma unroll
      for (int s = 0; s < 4; ++s) xm |= cn[s] > 23u * RINC ? 1u << s : 0u;
#pragma unroll 1
      do {
        const unsigned s = __builtin_ctz(xm | 8u);
        const unsigned c = *(const LAS unsigned*)(size_t)(ha + s * 256);
        u32x4 w = *(const LAS u32x4*)(size_t)(ia + s * (64 * CPIT) + (pc < 4u ? 128 : 0));
        const unsigned lim = pc < 4u ? (c >> 16) - 4u * pc : 0u;
        w[3] = pc == 7u ? w[3] >> 16 : w[3];
        w[0] = pc == 7u ? w[2] : 32u < lim ? w[0] : ~0u;
        w[1] = 33u < lim ? w[1] : ~0u;
        w[2] = 34u < lim ? w[2] : ~0u;
        seal(w, c > 36u * RINC);
        const unsigned ga = go + s * (64 * IBOX);
        if (xm) {
          w[3] += (((bk >> 3) & 3) + 1) << 24;
          gstore128wt(ENT + 2048 + ((bk >> 5) * 8 + (bk & 7) - bk) * 8, ga, w);
          w[3] -= (((bk >> 3) & 3) + 1) << 24;
          gstore128wt(ENT + IEXT / 16, ga, w);
        }
        xm &= xm - 1u;
        ++xw;
      } while (__ballot(xm != 0u) != 0ull);
    }
    PT(5);
    PV(0, xw);
  }
  u32x4 cv[4];
  unsigned vo[4], eb[4];
  unsigned long long pend[4];
#pragma unroll
  for (int k = 0; k < 4; ++k) {
    vo[k] = wave * 4096u + k * 1024u + lane * 16u;
    eb[k] = (unsigned)((wave * 32 + k * 8 + (lane >> 3)) * chunk) * 32u;
  }
#define POLL0()                                                                                                       \
  asm volatile("global_load_dwordx4 %0, %4, %8 sc1\n\t"                                                               \
               "global_load_dwordx4 %1, %5, %8 sc1\n\t"                                                               \
               "global_load_dwordx4 %2, %6, %8 sc1\n\t"                                                               \
               "global_load_dwordx4 %3, %7, %8 sc1"                                                                   \
               : "=&{v[240:243]}"(cv[0]), "=&{v[244:247]}"(cv[1]), "=&{v[248:251]}"(cv[2]), "=&{v[252:255]}"(cv[3])   \
               : "v"(vo[0]), "v"(vo[1]), "v"(vo[2]), "v"(vo[3]), "s"(ib)                                              \
               : "memory")
  const f32x16 zero16 = {0.f, 0.f, 0.f, 0.f, 0.f, 0.f, 0.f, 0.f, 0.f, 0.f, 0.f, 0.f, 0.f, 0.f, 0.f, 0.f};
  const unsigned to = lds0 + OFF_W + ((lane >> 2) & 3) * 128 + (lane & 1) * 64 + ((lane >> 4) & 1) * 16 + ((lane >> 1) & 1) * 8;
  auto xa = [&]() {
    bf16x8 wa[8];
#pragma unroll
    for (int k = 0; k < 8; ++k) wa[k] = rawfrag(to + (NBLOB + (k >> 1)) * 1024 + h * 4096 + (k & 1) * 32);
    asm volatile("" : "+v"(wa[0]), "+v"(wa[1]), "+v"(wa[2]), "+v"(wa[3]), "+v"(wa[4]), "+v"(wa[5]), "+v"(wa[6]), "+v"(wa[7]),
                      "+{v[160:163]}"(xg[0]), "+{v[164:167]}"(xg[1]), "+{v[168:171]}"(xg[2]), "+{v[172:175]}"(xg[3]));
    f32x16 XA[2] = {zero16, zero16};
#pragma unroll
    for (int k = 0; k < 4; ++k)
#pragma unroll
      for (int ct = 0; ct < 2; ++ct) XA[ct] = MFMA32(__builtin_bit_cast(bf16x8, xg[k]), wa[k * 2 + ct], XA[ct]);
#pragma unroll
    for (int q = 0; q < 4; ++q) {
      u32x4 xp;
#pragma unroll
      for (int j = 0; j < 4; ++j)
        xp[j] = pack_f16(XA[q & 1][8 * (q >> 1) + 2 * j] * NLOG2E - RA, XA[q & 1][8 * (q >> 1) + 2 * j + 1] * NLOG2E - RA);
      *(LAS u32x4*)((LAS char*)smem + OFF_R1 + wave * RING_SLOT + q * 1024 + lane * 16) = xp;
    }
  };
  {
    auto prep = [&](unsigned f, unsigned hs, float scale) {
      u32x4 w[2];
#pragma unroll
      for (int ct = 0; ct < 2; ++ct) w[ct] = __builtin_bit_cast(u32x4, rawfrag(to + f + h * hs + ct * 32));
      asm volatile("s_waitcnt lgkmcnt(0)" : "+v"(w[0]), "+v"(w[1]) :: "memory");
#pragma unroll
      for (int ct = 0; ct < 2; ++ct) {
#pragma unroll
        for (int j = 0; j < 4; ++j)
          w[ct][j] = pack_bf16(__uint_as_float(w[ct][j] << 16) * scale, __uint_as_float(w[ct][j] & 0xffff0000u) * scale);
        *(LAS u32x4*)((LAS char*)smem + OFF_W + f + ct * hs + lane * 16) = w[ct];
      }
    };
    {
      const unsigned xr = min(nodeof((bu * 8 + wave) * 32 + r) - xn0, 31u);
      unsigned xa_ = lds0 + OFF_L + wave * LIST_BYTES + xr * 128 + (((4 * h) ^ ((xr >> 1) & 7)) * 16);
      asm volatile("s_cmp_eq_u32 %1, 1\n\ts_cbranch_scc1 1f\n\ts_waitcnt vmcnt(4)\n1:\n\ts_waitcnt vmcnt(6)"
                   : "+v"(xa_) : "s"(xw) : "memory", "scc");
#pragma unroll
      for (int t = 0; t < 4; ++t) xg[t] = *(const LAS u32x4*)(size_t)(xa_ ^ (t * 16));
      asm volatile("" : "+v"(xg[0]), "+v"(xg[1]), "+v"(xg[2]), "+v"(xg[3]) :: "memory");
      *(LAS unsigned*)((LAS char*)smem + OFF_L + wave * LIST_BYTES + h * LARR + r * 4) = ~0u;
    }
    asm volatile("s_waitcnt lgkmcnt(0)\n\ts_barrier"
                 : "+v"(vo[0]), "+v"(vo[1]), "+v"(vo[2]), "+v"(vo[3]), "+v"(eb[0]), "+v"(eb[1]), "+v"(eb[2]), "+v"(eb[3])
                 :: "memory");
    xa();
    {
      unsigned f = (wave + (wave & 4)) * 1024, hs = 4096;
      const float scale = wave < 4 ? NLOG2E : VSC;
#pragma unroll 1
      while (true) {
        asm volatile("" : "+s"(f), "+s"(hs));
        prep(f, hs, scale);
        if (f != 0u) break;
        f = 16 * 1024;
        hs = 1024;
      }
    }
    PT(6);
  }
  const unsigned l0 = lds0 + OFF_L;
  unsigned aLX = l0 + (lane >> 3) * 16;
  unsigned aLE = l0 + LARR + ((((lane >> 1) & 7) << 2) | (lane >> 4)) * 4;
  unsigned aLS4 = l0 + (4 * h + ((lane >> 2) & 3)) * 16;
  unsigned aLS1 = l0 + (((r & 7) << 2) | (r >> 3)) * 4 + 2;
  unsigned cx0 = (unsigned)(((lane & 7) ^ (lane >> 4)) * 16);
  unsigned cx1 = cx0 ^ 64u;
  unsigned ce = (unsigned)((((lane & 1) ^ ((lane >> 4) & 1))) * 16);
  const unsigned ringd = 8 * RING_SLOT;
  unsigned cS = lds0 + OFF_T1 + (((lane >> 4) & 1) * 16 + (lane & 3) * 4) * 2;
  unsigned cSt = lds0 + OFF_T2 + h * 32;
  unsigned fo[4];
#pragma unroll
  for (int t = 0; t < 4; ++t) fo[t] = lds0 + OFF_R0 + (unsigned)(r * 128 + (((4 * h + t) ^ ((r >> 1) & 7)) * 16));
  unsigned foe = lds0 + OFF_R0 + (unsigned)(4096 + r * 32 + ((h ^ ((r >> 3) & 1)) * 16));
  f32x16 Y[4];
#pragma unroll
  for (int q = 0; q < 4; ++q)
#pragma unroll
    for (int i = 0; i < 16; i += 2) {
      f32x2 y;
      if (q < 2) asm volatile("v_mov_b64 %0, %1" : "=v"(y) : "s"(0x358637bd358637bdull + (RA * 0x0080000000800000ull)));
      else asm volatile("v_mov_b64 %0, 0" : "=v"(y));
      Y[q][i] = y[0];
      Y[q][i + 1] = y[1];
    }
  constexpr unsigned cu = RA == 14 ? 0x04000400u : 0x02000200u;
  unsigned rid = lane & 7;
  asm volatile("" : "+v"(aLX), "+v"(aLE), "+v"(aLS4), "+v"(aLS1), "+v"(cx0), "+v"(cx1), "+v"(ce), "+v"(cS), "+v"(cSt),
                    "+v"(fo[0]), "+v"(fo[1]), "+v"(fo[2]), "+v"(fo[3]), "+v"(foe), "+v"(rid));
  asm volatile("" : "+{v[0:15]}"(Y[0]), "+{v[16:31]}"(Y[1]), "+{v[32:47]}"(Y[2]), "+{v[48:63]}"(Y[3]));
  SB();
  __builtin_amdgcn_s_sleep(PSL);
  POLL0();
  glds16(wu_g + wave * 512, lane * 16, lds0 + OFF_W + (18 + wave) * 1024);
  auto ld128 = [&](unsigned a) { return *(const LAS u32x4*)(size_t)a; };
  auto gather5 = [&](const u32x4 sx, unsigned re, unsigned dstb) {
    glds16e(ES, re, ce, dstb + 4096);
    glds16x(x, sx[0], cx0, dstb);
    glds16x(x, sx[1], cx1, dstb + 1024);
    glds16x(x, sx[2], cx0, dstb + 2048);
    glds16x(x, sx[3], cx1, dstb + 3072);
  };
  const unsigned sq = min(4u * M, 0xffffffu);
  unsigned early, just;
  PT(7);
  asm volatile("s_waitcnt vmcnt(3)" ::: "memory");
  PT(8);
  asm volatile(
        "v_mov_b32 v181, %18\n\t"
        "v_mov_b32 v180, 160\n\t"
        "v_mov_b32 v176, 0xe0\n\t"
        "v_mov_b32 v177, 0x0c040100\n\t"
        "v_mov_b32 v178, 0x0c050100\n\t"
        "v_mov_b32 v179, 0x0c060100\n\t"
        "s_mov_b32 s52, 0x7f7f7f7f\n\t"
        "s_mov_b32 s53, 0x7f7f7f7f\n\t"
        "s_mov_b64 s[54:55], 0\n\t"
        "s_mov_b32 %12, 0\n\t"
        "v_mov_b32 v191, 4\n\t"
        "s_waitcnt vmcnt(3)\n\t"
        "v_add3_u32 v228, v240, v241, v242\n\t"
        "v_add3_u32 v231, v244, v245, v246\n\t"
        "v_add3_u32 v228, v228, v243, %19\n\t"
        "v_add3_u32 v231, v231, v247, %19\n\t"
        "v_cmp_ne_u32_sdwa s[72:73], v243, %20 src0_sel:BYTE_3 src1_sel:DWORD\n\t"
        "v_add_u32_dpp v228, v228, v228 quad_perm:[1,0,3,2] row_mask:0xf bank_mask:0xf\n\t"
        "v_add_u32_dpp v231, v231, v231 quad_perm:[1,0,3,2] row_mask:0xf bank_mask:0xf\n\t"
        "v_cmp_ne_u32_sdwa s[78:79], v247, %20 src0_sel:BYTE_3 src1_sel:DWORD\n\t"
        "v_add_u32_dpp v228, v228, v228 quad_perm:[2,3,0,1] row_mask:0xf bank_mask:0xf\n\t"
        "v_add_u32_dpp v231, v231, v231 quad_perm:[2,3,0,1] row_mask:0xf bank_mask:0xf\n\t"
        "s_nop 0\n\t"
        "v_add_u32_dpp v228, v228, v228 row_half_mirror row_mask:0xf bank_mask:0xf\n\t"
        "v_add_u32_dpp v231, v231, v231 row_half_mirror row_mask:0xf bank_mask:0xf\n\t"
        "v_cmp_gt_u32 s[64:65], 2, v228\n\t"
        "v_cmp_eq_u32 s[56:57], 1, v228\n\t"
        "v_cmp_gt_u32 s[66:67], 2, v231\n\t"
        "v_cmp_eq_u32 s[58:59], 1, v231\n\t"
        "s_and_b64 s[72:73], s[72:73], s[64:65]\n\t"
        "s_cselect_b64 s[64:65], 0, s[64:65]\n\t"
        "s_and_b64 s[56:57], s[56:57], s[64:65]\n\t"
        "s_not_b64 %0, s[64:65]\n\t"
        "s_or_b64 s[54:55], s[54:55], %0\n\t"
        "s_and_b64 s[78:79], s[78:79], s[66:67]\n\t"
        "s_cselect_b64 s[66:67], 0, s[66:67]\n\t"
        "s_and_b64 s[58:59], s[58:59], s[66:67]\n\t"
        "s_not_b64 %1, s[66:67]\n\t"
        "s_or_b64 s[54:55], s[54:55], %1\n\t"
        "s_mov_b64 exec, s[64:65]\n\t"
        "v_cmpx_ne_u32 s[72:73], -1, v240\n\t"
        "v_mul_hi_u32_u24_sdwa v204, v240, %21 dst_sel:DWORD dst_unused:UNUSED_PAD src0_sel:WORD_0 src1_sel:DWORD\n\t"
        "v_and_b32_sdwa v192, v243, v176 dst_sel:DWORD dst_unused:UNUSED_PAD src0_sel:BYTE_0 src1_sel:DWORD\n\t"
        "v_lshl_add_u32 v204, v204, 2, v192\n\t"
        "ds_add_rtn_u32 v204, v204, v191\n\t"
        "v_perm_b32 v216, v243, v240, v177\n\t"
        "v_mad_u32_u16 v228, v240, 32, %13 op_sel:[1,0,0,0]\n\t"
        "v_cmpx_ne_u32 s[74:75], -1, v241\n\t"
        "v_mul_hi_u32_u24_sdwa v205, v241, %21 dst_sel:DWORD dst_unused:UNUSED_PAD src0_sel:WORD_0 src1_sel:DWORD\n\t"
        "v_and_b32_sdwa v193, v243, v176 dst_sel:DWORD dst_unused:UNUSED_PAD src0_sel:BYTE_1 src1_sel:DWORD\n\t"
        "v_lshl_add_u32 v205, v205, 2, v193\n\t"
        "ds_add_rtn_u32 v205, v205, v191\n\t"
        "v_perm_b32 v217, v243, v241, v178\n\t"
        "v_mad_u32_u16 v229, v241, 32, %13 op_sel:[1,0,0,0]\n\t"
        "s_and_b64 exec, exec, s[52:53]\n\t"
        "v_cmpx_ne_u32 s[76:77], -1, v242\n\t"
        "v_mul_hi_u32_u24_sdwa v206, v242, %21 dst_sel:DWORD dst_unused:UNUSED_PAD src0_sel:WORD_0 src1_sel:DWORD\n\t"
        "v_and_b32_sdwa v194, v243, v176 dst_sel:DWORD dst_unused:UNUSED_PAD src0_sel:BYTE_2 src1_sel:DWORD\n\t"
        "v_lshl_add_u32 v206, v206, 2, v194\n\t"
        "ds_add_rtn_u32 v206, v206, v191\n\t"
        "v_perm_b32 v218, v243, v242, v179\n\t"
        "v_mad_u32_u16 v230, v242, 32, %13 op_sel:[1,0,0,0]\n\t"
        "s_mov_b64 exec, s[66:67]\n\t"
        "v_cmpx_ne_u32 s[78:79], -1, v244\n\t"
        "v_mul_hi_u32_u24_sdwa v207, v244, %21 dst_sel:DWORD dst_unused:UNUSED_PAD src0_sel:WORD_0 src1_sel:DWORD\n\t"
        "v_and_b32_sdwa v195, v247, v176 dst_sel:DWORD dst_unused:UNUSED_PAD src0_sel:BYTE_0 src1_sel:DWORD\n\t"
        "v_lshl_add_u32 v207, v207, 2, v195\n\t"
        "ds_add_rtn_u32 v207, v207, v191\n\t"
        "v_perm_b32 v219, v247, v244, v177\n\t"
        "v_mad_u32_u16 v231, v244, 32, %14 op_sel:[1,0,0,0]\n\t"
        "v_cmpx_ne_u32 s[80:81], -1, v245\n\t"
        "v_mul_hi_u32_u24_sdwa v208, v245, %21 dst_sel:DWORD dst_unused:UNUSED_PAD src0_sel:WORD_0 src1_sel:DWORD\n\t"
        "v_and_b32_sdwa v196, v247, v176 dst_sel:DWORD dst_unused:UNUSED_PAD src0_sel:BYTE_1 src1_sel:DWORD\n\t"
        "v_lshl_add_u32 v208, v208, 2, v196\n\t"
        "ds_add_rtn_u32 v208, v208, v191\n\t"
        "v_perm_b32 v220, v247, v245, v178\n\t"
        "v_mad_u32_u16 v232, v245, 32, %14 op_sel:[1,0,0,0]\n\t"
        "s_and_b64 exec, exec, s[52:53]\n\t"
        "v_cmpx_ne_u32 s[82:83], -1, v246\n\t"
        "v_mul_hi_u32_u24_sdwa v209, v246, %21 dst_sel:DWORD dst_unused:UNUSED_PAD src0_sel:WORD_0 src1_sel:DWORD\n\t"
        "v_and_b32_sdwa v197, v247, v176 dst_sel:DWORD dst_unused:UNUSED_PAD src0_sel:BYTE_2 src1_sel:DWORD\n\t"
        "v_lshl_add_u32 v209, v209, 2, v197\n\t"
        "ds_add_rtn_u32 v209, v209, v191\n\t"
        "v_perm_b32 v221, v247, v246, v179\n\t"
        "v_mad_u32_u16 v233, v246, 32, %14 op_sel:[1,0,0,0]\n\t"
        "s_mov_b64 exec, -1\n\t"
        "s_waitcnt vmcnt(1)\n\t"
        "v_add3_u32 v234, v248, v249, v250\n\t"
        "v_add3_u32 v237, v252, v253, v254\n\t"
        "v_add3_u32 v234, v234, v251, %19\n\t"
        "v_add3_u32 v237, v237, v255, %19\n\t"
        "v_cmp_ne_u32_sdwa s[84:85], v251, %20 src0_sel:BYTE_3 src1_sel:DWORD\n\t"
        "v_add_u32_dpp v234, v234, v234 quad_perm:[1,0,3,2] row_mask:0xf bank_mask:0xf\n\t"
        "v_add_u32_dpp v237, v237, v237 quad_perm:[1,0,3,2] row_mask:0xf bank_mask:0xf\n\t"
        "v_cmp_ne_u32_sdwa s[90:91], v255, %20 src0_sel:BYTE_3 src1_sel:DWORD\n\t"
        "v_add_u32_dpp v234, v234, v234 quad_perm:[2,3,0,1] row_mask:0xf bank_mask:0xf\n\t"
        "v_add_u32_dpp v237, v237, v237 quad_perm:[2,3,0,1] row_mask:0xf bank_mask:0xf\n\t"
        "s_nop 0\n\t"
        "v_add_u32_dpp v234, v234, v234 row_half_mirror row_mask:0xf bank_mask:0xf\n\t"
        "v_add_u32_dpp v237, v237, v237 row_half_mirror row_mask:0xf bank_mask:0xf\n\t"
        "v_cmp_gt_u32 s[68:69], 2, v234\n\t"
        "v_cmp_eq_u32 s[60:61], 1, v234\n\t"
        "v_cmp_gt_u32 s[70:71], 2, v237\n\t"
        "v_cmp_eq_u32 s[62:63], 1, v237\n\t"
        "s_and_b64 s[84:85], s[84:85], s[68:69]\n\t"
        "s_cselect_b64 s[68:69], 0, s[68:69]\n\t"
        "s_and_b64 s[60:61], s[60:61], s[68:69]\n\t"
        "s_not_b64 %2, s[68:69]\n\t"
        "s_or_b64 s[54:55], s[54:55], %2\n\t"
        "s_and_b64 s[90:91], s[90:91], s[70:71]\n\t"
        "s_cselect_b64 s[70:71], 0, s[70:71]\n\t"
        "s_and_b64 s[62:63], s[62:63], s[70:71]\n\t"
        "s_not_b64 %3, s[70:71]\n\t"
        "s_or_b64 s[54:55], s[54:55], %3\n\t"
        "s_or_b64 s[50:51], s[56:57], s[58:59]\n\t"
        "s_or_b64 exec, s[60:61], s[62:63]\n\t"
        "s_or_b64 s[50:51], s[50:51], exec\n\t"
        "s_cbranch_scc0 57f\n\t"
        "s_mov_b64 exec, s[50:51]\n\t"
        "global_load_dwordx4 v[172:175], %30, %17 sc1\n\t"
        "57:\n\t"
        "s_mov_b64 exec, s[72:73]\n\t"
        "s_waitcnt lgkmcnt(3)\n\t"
        "v_min_u32 v204, v181, v204\n\t"
        "v_min_u32 v205, v181, v205\n\t"
        "v_min_u32 v206, v181, v206\n\t"
        "v_mad_u32_u24 v204, v192, v180, v204\n\t"
        "v_mad_u32_u24 v205, v193, v180, v205\n\t"
        "v_mad_u32_u24 v206, v194, v180, v206\n\t"
        "ds_write2st64_b32 v204, v216, v228 offset1:10\n\t"
        "s_mov_b64 exec, s[74:75]\n\t"
        "ds_write2st64_b32 v205, v217, v229 offset1:10\n\t"
        "s_mov_b64 exec, s[76:77]\n\t"
        "ds_write2st64_b32 v206, v218, v230 offset1:10\n\t"
        "s_mov_b64 exec, s[68:69]\n\t"
        "v_cmpx_ne_u32 s[84:85], -1, v248\n\t"
        "v_mul_hi_u32_u24_sdwa v210, v248, %21 dst_sel:DWORD dst_unused:UNUSED_PAD src0_sel:WORD_0 src1_sel:DWORD\n\t"
        "v_and_b32_sdwa v198, v251, v176 dst_sel:DWORD dst_unused:UNUSED_PAD src0_sel:BYTE_0 src1_sel:DWORD\n\t"
        "v_lshl_add_u32 v210, v210, 2, v198\n\t"
        "ds_add_rtn_u32 v210, v210, v191\n\t"
        "v_perm_b32 v222, v251, v248, v177\n\t"
        "v_mad_u32_u16 v234, v248, 32, %15 op_sel:[1,0,0,0]\n\t"
        "v_cmpx_ne_u32 s[86:87], -1, v249\n\t"
        "v_mul_hi_u32_u24_sdwa v211, v249, %21 dst_sel:DWORD dst_unused:UNUSED_PAD src0_sel:WORD_0 src1_sel:DWORD\n\t"
        "v_and_b32_sdwa v199, v251, v176 dst_sel:DWORD dst_unused:UNUSED_PAD src0_sel:BYTE_1 src1_sel:DWORD\n\t"
        "v_lshl_add_u32 v211, v211, 2, v199\n\t"
        "ds_add_rtn_u32 v211, v211, v191\n\t"
        "v_perm_b32 v223, v251, v249, v178\n\t"
        "v_mad_u32_u16 v235, v249, 32, %15 op_sel:[1,0,0,0]\n\t"
        "s_and_b64 exec, exec, s[52:53]\n\t"
        "v_cmpx_ne_u32 s[88:89], -1, v250\n\t"
        "v_mul_hi_u32_u24_sdwa v212, v250, %21 dst_sel:DWORD dst_unused:UNUSED_PAD src0_sel:WORD_0 src1_sel:DWORD\n\t"
        "v_and_b32_sdwa v200, v251, v176 dst_sel:DWORD dst_unused:UNUSED_PAD src0_sel:BYTE_2 src1_sel:DWORD\n\t"
        "v_lshl_add_u32 v212, v212, 2, v200\n\t"
        "ds_add_rtn_u32 v212, v212, v191\n\t"
        "v_perm_b32 v224, v251, v250, v179\n\t"
        "v_mad_u32_u16 v236, v250, 32, %15 op_sel:[1,0,0,0]\n\t"
        "s_mov_b64 exec, s[78:79]\n\t"
        "s_waitcnt lgkmcnt(6)\n\t"
        "v_min_u32 v207, v181, v207\n\t"
        "v_min_u32 v208, v181, v208\n\t"
        "v_min_u32 v209, v181, v209\n\t"
        "v_mad_u32_u24 v207, v195, v180, v207\n\t"
        "v_mad_u32_u24 v208, v196, v180, v208\n\t"
        "v_mad_u32_u24 v209, v197, v180, v209\n\t"
        "ds_write2st64_b32 v207, v219, v231 offset1:10\n\t"
        "s_mov_b64 exec, s[80:81]\n\t"
        "ds_write2st64_b32 v208, v220, v232 offset1:10\n\t"
        "s_mov_b64 exec, s[82:83]\n\t"
        "ds_write2st64_b32 v209, v221, v233 offset1:10\n\t"
        "s_mov_b64 exec, -1\n\t"
        "ds_read_b128 v[184:187], %22\n\t"
        "ds_read_b32 v183, %23\n\t"
        "s_mov_b64 exec, s[70:71]\n\t"
        "v_cmpx_ne_u32 s[90:91], -1, v252\n\t"
        "v_mul_hi_u32_u24_sdwa v213, v252, %21 dst_sel:DWORD dst_unused:UNUSED_PAD src0_sel:WORD_0 src1_sel:DWORD\n\t"
        "v_and_b32_sdwa v201, v255, v176 dst_sel:DWORD dst_unused:UNUSED_PAD src0_sel:BYTE_0 src1_sel:DWORD\n\t"
        "v_lshl_add_u32 v213, v213, 2, v201\n\t"
        "ds_add_rtn_u32 v213, v213, v191\n\t"
        "v_perm_b32 v225, v255, v252, v177\n\t"
        "v_mad_u32_u16 v237, v252, 32, %16 op_sel:[1,0,0,0]\n\t"
        "v_cmpx_ne_u32 s[92:93], -1, v253\n\t"
        "v_mul_hi_u32_u24_sdwa v214, v253, %21 dst_sel:DWORD dst_unused:UNUSED_PAD src0_sel:WORD_0 src1_sel:DWORD\n\t"
        "v_and_b32_sdwa v202, v255, v176 dst_sel:DWORD dst_unused:UNUSED_PAD src0_sel:BYTE_1 src1_sel:DWORD\n\t"
        "v_lshl_add_u32 v214, v214, 2, v202\n\t"
        "ds_add_rtn_u32 v214, v214, v191\n\t"
        "v_perm_b32 v226, v255, v253, v178\n\t"
        "v_mad_u32_u16 v238, v253, 32, %16 op_sel:[1,0,0,0]\n\t"
        "s_and_b64 exec, exec, s[52:53]\n\t"
        "v_cmpx_ne_u32 s[94:95], -1, v254\n\t"
        "v_mul_hi_u32_u24_sdwa v215, v254, %21 dst_sel:DWORD dst_unused:UNUSED_PAD src0_sel:WORD_0 src1_sel:DWORD\n\t"
        "v_and_b32_sdwa v203, v255, v176 dst_sel:DWORD dst_unused:UNUSED_PAD src0_sel:BYTE_2 src1_sel:DWORD\n\t"
        "v_lshl_add_u32 v215, v215, 2, v203\n\t"
        "ds_add_rtn_u32 v215, v215, v191\n\t"
        "v_perm_b32 v227, v255, v254, v179\n\t"
        "v_mad_u32_u16 v239, v254, 32, %16 op_sel:[1,0,0,0]\n\t"
        "s_cmp_eq_u64 s[54:55], 0\n\t"
        "s_cbranch_scc1 59f\n\t"
        "s_mov_b64 exec, %0\n\t"
        "s_cbranch_execz 50f\n\t"
        "global_load_dwordx4 v[240:243], %4, %17 sc1\n\t"
        "50:\n\t"
        "s_mov_b64 exec, %1\n\t"
        "s_cbranch_execz 51f\n\t"
        "global_load_dwordx4 v[244:247], %5, %17 sc1\n\t"
        "51:\n\t"
        "s_mov_b64 exec, %2\n\t"
        "s_cbranch_execz 52f\n\t"
        "global_load_dwordx4 v[248:251], %6, %17 sc1\n\t"
        "52:\n\t"
        "s_mov_b64 exec, %3\n\t"
        "s_cbranch_execz 53f\n\t"
        "global_load_dwordx4 v[252:255], %7, %17 sc1\n\t"
        "53:\n\t"
        "59:\n\t"
        "s_mov_b64 exec, s[84:85]\n\t"
        "s_waitcnt lgkmcnt(8)\n\t"
        "v_min_u32 v210, v181, v210\n\t"
        "v_min_u32 v211, v181, v211\n\t"
        "v_min_u32 v212, v181, v212\n\t"
        "v_mad_u32_u24 v210, v198, v180, v210\n\t"
        "v_mad_u32_u24 v211, v199, v180, v211\n\t"
        "v_mad_u32_u24 v212, v200, v180, v212\n\t"
        "ds_write2st64_b32 v210, v222, v234 offset1:10\n\t"
        "s_mov_b64 exec, s[86:87]\n\t"
        "ds_write2st64_b32 v211, v223, v235 offset1:10\n\t"
        "s_mov_b64 exec, s[88:89]\n\t"
        "ds_write2st64_b32 v212, v224, v236 offset1:10\n\t"
        "s_mov_b64 exec, -1\n\t"
        "s_movk_i32 s96, 128\n\t"
        "s_mov_b32 m0, %29\n\t"
        "s_waitcnt lgkmcnt(6)\n\t"
        "v_max3_u32 v182, v184, v185, v186\n\t"
        "v_max3_u32 v182, v182, v187, v183\n\t"
        "v_cmp_eq_u32 vcc, -1, v182\n\t"
        "v_mad_u32_u16 v184, v184, s96, %24\n\t"
        "v_mad_u32_u16 v185, v185, s96, %25\n\t"
        "v_mad_u32_u16 v186, v186, s96, %24\n\t"
        "v_mad_u32_u16 v187, v187, s96, %25\n\t"
        "v_add_u32 v183, v183, %26\n\t"
        "s_cbranch_vccnz 60f\n\t"
        "global_load_lds_dwordx4 v184, %27\n\t"
        "s_addk_i32 m0, 1024\n\t"
        "s_nop 0\n\t"
        "global_load_lds_dwordx4 v185, %27\n\t"
        "s_addk_i32 m0, 1024\n\t"
        "s_nop 0\n\t"
        "global_load_lds_dwordx4 v186, %27\n\t"
        "s_addk_i32 m0, 1024\n\t"
        "s_nop 0\n\t"
        "global_load_lds_dwordx4 v187, %27\n\t"
        "s_addk_i32 m0, 1024\n\t"
        "s_nop 0\n\t"
        "global_load_lds_dwordx4 v183, %28\n\t"
        "s_mov_b32 %12, 1\n\t"
        "60:\n\t"
        "s_mov_b64 exec, s[90:91]\n\t"
        "s_waitcnt lgkmcnt(3)\n\t"
        "v_min_u32 v213, v181, v213\n\t"
        "v_min_u32 v214, v181, v214\n\t"
        "v_min_u32 v215, v181, v215\n\t"
        "v_mad_u32_u24 v213, v201, v180, v213\n\t"
        "v_mad_u32_u24 v214, v202, v180, v214\n\t"
        "v_mad_u32_u24 v215, v203, v180, v215\n\t"
        "ds_write2st64_b32 v213, v225, v237 offset1:10\n\t"
        "s_mov_b64 exec, s[92:93]\n\t"
        "ds_write2st64_b32 v214, v226, v238 offset1:10\n\t"
        "s_mov_b64 exec, s[94:95]\n\t"
        "ds_write2st64_b32 v215, v227, v239 offset1:10\n\t"
        "s_cmp_eq_u64 s[50:51], 0\n\t"
        "s_cbranch_scc1 49f\n\t"
        "s_mov_b64 exec, s[56:57]\n\t"
        "v_mov_b32 v188, 0xff000000\n\t"
        "v_mov_b32 v189, %13\n\t"
        "v_bfe_u32 v190, %4, 7, 8\n\t"
        "ds_write_b8 v190, v191 offset:256\n\t"
        "s_mov_b64 exec, s[58:59]\n\t"
        "v_mov_b32 v188, 0xfe000000\n\t"
        "v_mov_b32 v189, %14\n\t"
        "v_bfe_u32 v190, %5, 7, 8\n\t"
        "ds_write_b8 v190, v191 offset:256\n\t"
        "s_mov_b64 exec, s[60:61]\n\t"
        "v_mov_b32 v188, 0xfd000000\n\t"
        "v_mov_b32 v189, %15\n\t"
        "v_bfe_u32 v190, %6, 7, 8\n\t"
        "ds_write_b8 v190, v191 offset:256\n\t"
        "s_mov_b64 exec, s[62:63]\n\t"
        "v_mov_b32 v188, 0xfc000000\n\t"
        "v_mov_b32 v189, %16\n\t"
        "v_bfe_u32 v190, %7, 7, 8\n\t"
        "ds_write_b8 v190, v191 offset:256\n\t"
        "s_mov_b64 exec, s[50:51]\n\t"
        "s_waitcnt vmcnt(5)\n\t"
        "s_cmp_lg_u32 %12, 0\n\t"
        "s_cbranch_scc1 47f\n\t"
        "s_waitcnt vmcnt(0)\n\t"
        "47:\n\t"
        "v_add_u32 v175, v175, v188\n\t"
        "v_add3_u32 v228, v172, v173, v174\n\t"
        "v_add3_u32 v228, v228, v175, %19\n\t"
        "v_cmp_ne_u32_sdwa s[72:73], v175, %20 src0_sel:BYTE_3 src1_sel:DWORD\n\t"
        "s_nop 0\n\t"
        "v_add_u32_dpp v228, v228, v228 quad_perm:[1,0,3,2] row_mask:0xf bank_mask:0xf\n\t"
        "s_nop 1\n\t"
        "v_add_u32_dpp v228, v228, v228 quad_perm:[2,3,0,1] row_mask:0xf bank_mask:0xf\n\t"
        "s_nop 1\n\t"
        "v_add_u32_dpp v228, v228, v228 row_half_mirror row_mask:0xf bank_mask:0xf\n\t"
        "v_cmp_eq_u32 s[68:69], 0, v228\n\t"
        "s_and_b64 s[72:73], s[72:73], s[68:69]\n\t"
        "s_cselect_b64 exec, 0, s[68:69]\n\t"
        "s_cbranch_execz 48f\n\t"
        "v_cmpx_ne_u32 s[72:73], -1, v172\n\t"
        "v_mul_hi_u32_u24_sdwa v204, v172, %21 dst_sel:DWORD dst_unused:UNUSED_PAD src0_sel:WORD_0 src1_sel:DWORD\n\t"
        "v_and_b32_sdwa v192, v175, v176 dst_sel:DWORD dst_unused:UNUSED_PAD src0_sel:BYTE_0 src1_sel:DWORD\n\t"
        "v_lshl_add_u32 v204, v204, 2, v192\n\t"
        "ds_add_rtn_u32 v204, v204, v191\n\t"
        "v_perm_b32 v216, v175, v172, v177\n\t"
        "v_mad_u32_u16 v228, v172, 32, v189 op_sel:[1,0,0,0]\n\t"
        "v_cmpx_ne_u32 s[74:75], -1, v173\n\t"
        "v_mul_hi_u32_u24_sdwa v205, v173, %21 dst_sel:DWORD dst_unused:UNUSED_PAD src0_sel:WORD_0 src1_sel:DWORD\n\t"
        "v_and_b32_sdwa v193, v175, v176 dst_sel:DWORD dst_unused:UNUSED_PAD src0_sel:BYTE_1 src1_sel:DWORD\n\t"
        "v_lshl_add_u32 v205, v205, 2, v193\n\t"
        "ds_add_rtn_u32 v205, v205, v191\n\t"
        "v_perm_b32 v217, v175, v173, v178\n\t"
        "v_mad_u32_u16 v229, v173, 32, v189 op_sel:[1,0,0,0]\n\t"
        "s_and_b64 exec, exec, s[52:53]\n\t"
        "v_cmpx_ne_u32 s[76:77], -1, v174\n\t"
        "v_mul_hi_u32_u24_sdwa v206, v174, %21 dst_sel:DWORD dst_unused:UNUSED_PAD src0_sel:WORD_0 src1_sel:DWORD\n\t"
        "v_and_b32_sdwa v194, v175, v176 dst_sel:DWORD dst_unused:UNUSED_PAD src0_sel:BYTE_2 src1_sel:DWORD\n\t"
        "v_lshl_add_u32 v206, v206, 2, v194\n\t"
        "ds_add_rtn_u32 v206, v206, v191\n\t"
        "v_perm_b32 v218, v175, v174, v179\n\t"
        "v_mad_u32_u16 v230, v174, 32, v189 op_sel:[1,0,0,0]\n\t"
        "s_mov_b64 s[50:51], 0\n\t"
        "s_and_b64 s[70:71], s[68:69], s[62:63]\n\t"
        "s_xor_b64 s[62:63], s[62:63], s[70:71]\n\t"
        "s_xor_b64 s[68:69], s[68:69], s[70:71]\n\t"
        "s_or_b64 s[50:51], s[50:51], s[62:63]\n\t"
        "s_and_b64 s[70:71], s[68:69], s[60:61]\n\t"
        "s_xor_b64 s[60:61], s[60:61], s[70:71]\n\t"
        "s_xor_b64 s[68:69], s[68:69], s[70:71]\n\t"
        "s_or_b64 s[50:51], s[50:51], s[60:61]\n\t"
        "s_and_b64 s[70:71], s[68:69], s[58:59]\n\t"
        "s_xor_b64 s[58:59], s[58:59], s[70:71]\n\t"
        "s_xor_b64 s[68:69], s[68:69], s[70:71]\n\t"
        "s_or_b64 s[50:51], s[50:51], s[58:59]\n\t"
        "s_and_b64 s[70:71], s[68:69], s[56:57]\n\t"
        "s_xor_b64 s[56:57], s[56:57], s[70:71]\n\t"
        "s_or_b64 s[50:51], s[50:51], s[56:57]\n\t"
        "s_mov_b64 exec, s[72:73]\n\t"
        "s_waitcnt lgkmcnt(0)\n\t"
        "v_min_u32 v204, v181, v204\n\t"
        "v_min_u32 v205, v181, v205\n\t"
        "v_min_u32 v206, v181, v206\n\t"
        "v_mad_u32_u24 v204, v192, v180, v204\n\t"
        "v_mad_u32_u24 v205, v193, v180, v205\n\t"
        "v_mad_u32_u24 v206, v194, v180, v206\n\t"
        "ds_write2st64_b32 v204, v216, v228 offset1:10\n\t"
        "s_mov_b64 exec, s[74:75]\n\t"
        "ds_write2st64_b32 v205, v217, v229 offset1:10\n\t"
        "s_mov_b64 exec, s[76:77]\n\t"
        "ds_write2st64_b32 v206, v218, v230 offset1:10\n\t"
        "s_cbranch_scc0 49f\n\t"
        "48:\n\t"
        "s_mov_b32 s96, 0x1000000\n\t"
        "s_or_b32 %12, %12, 2\n\t"
        "s_mov_b64 exec, s[56:57]\n\t"
        "s_cbranch_execz 40f\n\t"
        "v_add_u32 %4, s96, %4\n\t"
        "s_or_b64 %0, %0, exec\n\t"
        "global_load_dwordx4 v[240:243], %4, %17 sc1\n\t"
        "40:\n\t"
        "s_mov_b64 exec, s[58:59]\n\t"
        "s_cbranch_execz 41f\n\t"
        "v_add_u32 %5, s96, %5\n\t"
        "s_or_b64 %1, %1, exec\n\t"
        "global_load_dwordx4 v[244:247], %5, %17 sc1\n\t"
        "41:\n\t"
        "s_mov_b64 exec, s[60:61]\n\t"
        "s_cbranch_execz 42f\n\t"
        "v_add_u32 %6, s96, %6\n\t"
        "s_or_b64 %2, %2, exec\n\t"
        "global_load_dwordx4 v[248:251], %6, %17 sc1\n\t"
        "42:\n\t"
        "s_mov_b64 exec, s[62:63]\n\t"
        "s_cbranch_execz 43f\n\t"
        "v_add_u32 %7, s96, %7\n\t"
        "s_or_b64 %3, %3, exec\n\t"
        "global_load_dwordx4 v[252:255], %7, %17 sc1\n\t"
        "43:\n\t"
        "49:\n\t"
        "s_or_b32 s54, s54, s55\n\t"
        "s_or_b32 s54, s54, %12\n\t"
        "s_cbranch_scc1 60f\n\t"
        "s_movk_i32 s54, 12\n\t"
        "61:\n\t"
        "s_sub_u32 s54, s54, 1\n\t"
        "s_cbranch_scc1 60f\n\t"
        "s_mov_b64 exec, -1\n\t"
        "ds_read_b128 v[184:187], %22\n\t"
        "ds_read_b32 v183, %23\n\t"
        "s_mov_b64 exec, -1\n\t"
        "s_movk_i32 s96, 128\n\t"
        "s_mov_b32 m0, %29\n\t"
        "s_waitcnt lgkmcnt(0)\n\t"
        "v_max3_u32 v182, v184, v185, v186\n\t"
        "v_max3_u32 v182, v182, v187, v183\n\t"
        "v_cmp_eq_u32 vcc, -1, v182\n\t"
        "v_mad_u32_u16 v184, v184, s96, %24\n\t"
        "v_mad_u32_u16 v185, v185, s96, %25\n\t"
        "v_mad_u32_u16 v186, v186, s96, %24\n\t"
        "v_mad_u32_u16 v187, v187, s96, %25\n\t"
        "v_add_u32 v183, v183, %26\n\t"
        "s_cbranch_vccnz 61b\n\t"
        "global_load_lds_dwordx4 v184, %27\n\t"
        "s_addk_i32 m0, 1024\n\t"
        "s_nop 0\n\t"
        "global_load_lds_dwordx4 v185, %27\n\t"
        "s_addk_i32 m0, 1024\n\t"
        "s_nop 0\n\t"
        "global_load_lds_dwordx4 v186, %27\n\t"
        "s_addk_i32 m0, 1024\n\t"
        "s_nop 0\n\t"
        "global_load_lds_dwordx4 v187, %27\n\t"
        "s_addk_i32 m0, 1024\n\t"
        "s_nop 0\n\t"
        "global_load_lds_dwordx4 v183, %28\n\t"
        "s_mov_b32 %12, 1\n\t"
        "60:\n\t"
        "s_mov_b64 exec, -1\n\t"
        : "=&s"(pend[0]), "=&s"(pend[1]), "=&s"(pend[2]), "=&s"(pend[3]), "+v"(vo[0]), "+v"(vo[1]), "+v"(vo[2]),
          "+v"(vo[3]), "+{v[240:243]}"(cv[0]), "+{v[244:247]}"(cv[1]), "+{v[248:251]}"(cv[2]), "+{v[252:255]}"(cv[3]),
          "=&s"(early)
        : "v"(eb[0]), "v"(eb[1]), "v"(eb[2]), "v"(eb[3]), "s"(ib), "n"(OFF_L + LARR - 4), "v"(tag[0]), "v"(tag[1]),
          "s"(sq), "v"(aLX + wave * LIST_BYTES), "v"(aLE + wave * LIST_BYTES), "v"(cx0), "v"(cx1), "v"(ce), "s"(x),
          "s"(ES), "s"(lds0 + OFF_R0 + wave * RING_SLOT), "v"(32768u + wave * 1024u + lane * 16u)
        :
          "memory", "vcc", "scc", "m0", "v172", "v173", "v174", "v175", "v176", "v177", "v178", "v179", "v180",
          "v181", "v182", "v183", "v184", "v185", "v186", "v187", "v188", "v189", "v190", "v191", "v192", "v193",
          "v194", "v195", "v196", "v197", "v198", "v199", "v200", "v201", "v202", "v203", "v204", "v205", "v206",
          "v207", "v208", "v209", "v210", "v211", "v212", "v213", "v214", "v215", "v216", "v217", "v218", "v219",
          "v220", "v221", "v222", "v223", "v224", "v225", "v226", "v227", "v228", "v229", "v230", "v231", "v232",
          "v233", "v234", "v235", "v236", "v237", "v238", "v239", "s50", "s51", "s52", "s53", "s54", "s55", "s56",
          "s57", "s58", "s59", "s60", "s61", "s62", "s63", "s64", "s65", "s66", "s67", "s68", "s69", "s70", "s71",
          "s72", "s73", "s74", "s75", "s76", "s77", "s78", "s79", "s80", "s81", "s82", "s83", "s84", "s85", "s86",
          "s87", "s88", "s89", "s90", "s91", "s92", "s93", "s94", "s95", "s96");
  auto uni = [&]() {
#pragma unroll
    for (int k = 0; k < 4; ++k)
      pend[k] = ((unsigned long long)(unsigned)__builtin_amdgcn_readfirstlane((unsigned)(pend[k] >> 32)) << 32) |
                (unsigned)__builtin_amdgcn_readfirstlane((unsigned)pend[k]);
  };
  uni();
  early = __builtin_amdgcn_readfirstlane(early);
  just = early;
  PT(9);
  PV(13, early);
  if (__builtin_expect((pend[0] | pend[1] | pend[2] | pend[3] | (unsigned long long)(early ^ 1u)) != 0ull, 0))
#pragma clang loop unroll(disable)
  while (true) {
    early &= 1u;
    if (!early) {
      const u32x4 sx = ld128(aLX + wave * LIST_BYTES);
      const unsigned re = *(const LAS unsigned*)(size_t)(aLE + wave * LIST_BYTES);
      if (__ballot(max(max(max(sx[0], sx[1]), max(sx[2], sx[3])), re) == ~0u) == 0ull) {
        gather5(sx, re, lds0 + OFF_R0 + wave * RING_SLOT);
        early = 1u;
        just = 1u;
      }
    }
    if ((pend[0] | pend[1] | pend[2] | pend[3]) == 0ull || --spin < 0) break;
    asm volatile(
        "s_mov_b32 s96, 0x1000000\n\t"
        "v_mov_b32 v181, %17\n\t"
        "v_mov_b32 v180, 160\n\t"
        "v_mov_b32 v176, 0xe0\n\t"
        "v_mov_b32 v177, 0x0c040100\n\t"
        "v_mov_b32 v178, 0x0c050100\n\t"
        "v_mov_b32 v179, 0x0c060100\n\t"
        "s_mov_b32 s52, 0x7f7f7f7f\n\t"
        "s_mov_b32 s53, 0x7f7f7f7f\n\t"
        "v_mov_b32 v191, 4\n\t"
        "s_waitcnt vmcnt(5)\n\t"
        "s_cmp_eq_u32 %21, 1\n\t"
        "s_cbranch_scc1 9f\n\t"
        "s_waitcnt vmcnt(0)\n\t"
        "9:\n\t"
        "v_add3_u32 v228, v240, v241, v242\n\t"
        "v_add3_u32 v229, v244, v245, v246\n\t"
        "v_add3_u32 v230, v248, v249, v250\n\t"
        "v_add3_u32 v231, v252, v253, v254\n\t"
        "v_add3_u32 v228, v228, v243, %18\n\t"
        "v_add3_u32 v229, v229, v247, %18\n\t"
        "v_add3_u32 v230, v230, v251, %18\n\t"
        "v_add3_u32 v231, v231, v255, %18\n\t"
        "v_add_u32_dpp v228, v228, v228 quad_perm:[1,0,3,2] row_mask:0xf bank_mask:0xf\n\t"
        "v_add_u32_dpp v229, v229, v229 quad_perm:[1,0,3,2] row_mask:0xf bank_mask:0xf\n\t"
        "v_add_u32_dpp v230, v230, v230 quad_perm:[1,0,3,2] row_mask:0xf bank_mask:0xf\n\t"
        "v_add_u32_dpp v231, v231, v231 quad_perm:[1,0,3,2] row_mask:0xf bank_mask:0xf\n\t"
        "v_add_u32_dpp v228, v228, v228 quad_perm:[2,3,0,1] row_mask:0xf bank_mask:0xf\n\t"
        "v_add_u32_dpp v229, v229, v229 quad_perm:[2,3,0,1] row_mask:0xf bank_mask:0xf\n\t"
        "v_add_u32_dpp v230, v230, v230 quad_perm:[2,3,0,1] row_mask:0xf bank_mask:0xf\n\t"
        "v_add_u32_dpp v231, v231, v231 quad_perm:[2,3,0,1] row_mask:0xf bank_mask:0xf\n\t"
        "v_add_u32_dpp v228, v228, v228 row_half_mirror row_mask:0xf bank_mask:0xf\n\t"
        "v_add_u32_dpp v229, v229, v229 row_half_mirror row_mask:0xf bank_mask:0xf\n\t"
        "v_add_u32_dpp v230, v230, v230 row_half_mirror row_mask:0xf bank_mask:0xf\n\t"
        "v_add_u32_dpp v231, v231, v231 row_half_mirror row_mask:0xf bank_mask:0xf\n\t"
        "v_cmp_ne_u32_sdwa s[72:73], v243, %19 src0_sel:BYTE_3 src1_sel:DWORD\n\t"
        "v_cmp_gt_u32 s[64:65], 2, v228\n\t"
        "v_cmp_eq_u32 s[56:57], 1, v228\n\t"
        "v_cmp_ne_u32_sdwa s[74:75], v247, %19 src0_sel:BYTE_3 src1_sel:DWORD\n\t"
        "v_cmp_gt_u32 s[66:67], 2, v229\n\t"
        "v_cmp_eq_u32 s[58:59], 1, v229\n\t"
        "v_cmp_ne_u32_sdwa s[76:77], v251, %19 src0_sel:BYTE_3 src1_sel:DWORD\n\t"
        "v_cmp_gt_u32 s[68:69], 2, v230\n\t"
        "v_cmp_eq_u32 s[60:61], 1, v230\n\t"
        "v_cmp_ne_u32_sdwa s[78:79], v255, %19 src0_sel:BYTE_3 src1_sel:DWORD\n\t"
        "v_cmp_gt_u32 s[70:71], 2, v231\n\t"
        "v_cmp_eq_u32 s[62:63], 1, v231\n\t"
        "s_and_b64 s[64:65], s[64:65], %0\n\t"
        "s_and_b64 s[72:73], s[72:73], s[64:65]\n\t"
        "s_cselect_b64 s[64:65], 0, s[64:65]\n\t"
        "s_and_b64 s[56:57], s[56:57], s[64:65]\n\t"
        "s_andn2_b64 %0, %0, s[64:65]\n\t"
        "s_cbranch_scc0 10f\n\t"
        "s_mov_b64 exec, %0\n\t"
        "global_load_dwordx4 v[240:243], %4, %16 sc1\n\t"
        "10:\n\t"
        "s_and_b64 s[66:67], s[66:67], %1\n\t"
        "s_and_b64 s[74:75], s[74:75], s[66:67]\n\t"
        "s_cselect_b64 s[66:67], 0, s[66:67]\n\t"
        "s_and_b64 s[58:59], s[58:59], s[66:67]\n\t"
        "s_andn2_b64 %1, %1, s[66:67]\n\t"
        "s_cbranch_scc0 11f\n\t"
        "s_mov_b64 exec, %1\n\t"
        "global_load_dwordx4 v[244:247], %5, %16 sc1\n\t"
        "11:\n\t"
        "s_and_b64 s[68:69], s[68:69], %2\n\t"
        "s_and_b64 s[76:77], s[76:77], s[68:69]\n\t"
        "s_cselect_b64 s[68:69], 0, s[68:69]\n\t"
        "s_and_b64 s[60:61], s[60:61], s[68:69]\n\t"
        "s_andn2_b64 %2, %2, s[68:69]\n\t"
        "s_cbranch_scc0 12f\n\t"
        "s_mov_b64 exec, %2\n\t"
        "global_load_dwordx4 v[248:251], %6, %16 sc1\n\t"
        "12:\n\t"
        "s_and_b64 s[70:71], s[70:71], %3\n\t"
        "s_and_b64 s[78:79], s[78:79], s[70:71]\n\t"
        "s_cselect_b64 s[70:71], 0, s[70:71]\n\t"
        "s_and_b64 s[62:63], s[62:63], s[70:71]\n\t"
        "s_andn2_b64 %3, %3, s[70:71]\n\t"
        "s_cbranch_scc0 13f\n\t"
        "s_mov_b64 exec, %3\n\t"
        "global_load_dwordx4 v[252:255], %7, %16 sc1\n\t"
        "13:\n\t"
        "s_mov_b64 s[54:55], 0\n\t"
        "s_mov_b64 s[72:73], 0\n\t"
        "s_mov_b64 exec, s[64:65]\n\t"
        "s_cbranch_execz 20f\n\t"
        "v_cmpx_ne_u32 s[72:73], -1, v240\n\t"
        "v_mul_hi_u32_u24_sdwa v204, v240, %20 dst_sel:DWORD dst_unused:UNUSED_PAD src0_sel:WORD_0 src1_sel:DWORD\n\t"
        "v_and_b32_sdwa v192, v243, v176 dst_sel:DWORD dst_unused:UNUSED_PAD src0_sel:BYTE_0 src1_sel:DWORD\n\t"
        "v_lshl_add_u32 v204, v204, 2, v192\n\t"
        "ds_add_rtn_u32 v204, v204, v191\n\t"
        "v_perm_b32 v216, v243, v240, v177\n\t"
        "v_mad_u32_u16 v228, v240, 32, %12 op_sel:[1,0,0,0]\n\t"
        "v_cmpx_ne_u32 s[74:75], -1, v241\n\t"
        "v_mul_hi_u32_u24_sdwa v205, v241, %20 dst_sel:DWORD dst_unused:UNUSED_PAD src0_sel:WORD_0 src1_sel:DWORD\n\t"
        "v_and_b32_sdwa v193, v243, v176 dst_sel:DWORD dst_unused:UNUSED_PAD src0_sel:BYTE_1 src1_sel:DWORD\n\t"
        "v_lshl_add_u32 v205, v205, 2, v193\n\t"
        "ds_add_rtn_u32 v205, v205, v191\n\t"
        "v_perm_b32 v217, v243, v241, v178\n\t"
        "v_mad_u32_u16 v229, v241, 32, %12 op_sel:[1,0,0,0]\n\t"
        "s_and_b64 exec, exec, s[52:53]\n\t"
        "v_cmpx_ne_u32 s[76:77], -1, v242\n\t"
        "v_mul_hi_u32_u24_sdwa v206, v242, %20 dst_sel:DWORD dst_unused:UNUSED_PAD src0_sel:WORD_0 src1_sel:DWORD\n\t"
        "v_and_b32_sdwa v194, v243, v176 dst_sel:DWORD dst_unused:UNUSED_PAD src0_sel:BYTE_2 src1_sel:DWORD\n\t"
        "v_lshl_add_u32 v206, v206, 2, v194\n\t"
        "ds_add_rtn_u32 v206, v206, v191\n\t"
        "v_perm_b32 v218, v243, v242, v179\n\t"
        "v_mad_u32_u16 v230, v242, 32, %12 op_sel:[1,0,0,0]\n\t"
        "s_or_b64 s[54:55], s[54:55], s[56:57]\n\t"
        "20:\n\t"
        "s_mov_b64 s[78:79], 0\n\t"
        "s_mov_b64 exec, s[66:67]\n\t"
        "s_cbranch_execz 21f\n\t"
        "v_cmpx_ne_u32 s[78:79], -1, v244\n\t"
        "v_mul_hi_u32_u24_sdwa v207, v244, %20 dst_sel:DWORD dst_unused:UNUSED_PAD src0_sel:WORD_0 src1_sel:DWORD\n\t"
        "v_and_b32_sdwa v195, v247, v176 dst_sel:DWORD dst_unused:UNUSED_PAD src0_sel:BYTE_0 src1_sel:DWORD\n\t"
        "v_lshl_add_u32 v207, v207, 2, v195\n\t"
        "ds_add_rtn_u32 v207, v207, v191\n\t"
        "v_perm_b32 v219, v247, v244, v177\n\t"
        "v_mad_u32_u16 v231, v244, 32, %13 op_sel:[1,0,0,0]\n\t"
        "v_cmpx_ne_u32 s[80:81], -1, v245\n\t"
        "v_mul_hi_u32_u24_sdwa v208, v245, %20 dst_sel:DWORD dst_unused:UNUSED_PAD src0_sel:WORD_0 src1_sel:DWORD\n\t"
        "v_and_b32_sdwa v196, v247, v176 dst_sel:DWORD dst_unused:UNUSED_PAD src0_sel:BYTE_1 src1_sel:DWORD\n\t"
        "v_lshl_add_u32 v208, v208, 2, v196\n\t"
        "ds_add_rtn_u32 v208, v208, v191\n\t"
        "v_perm_b32 v220, v247, v245, v178\n\t"
        "v_mad_u32_u16 v232, v245, 32, %13 op_sel:[1,0,0,0]\n\t"
        "s_and_b64 exec, exec, s[52:53]\n\t"
        "v_cmpx_ne_u32 s[82:83], -1, v246\n\t"
        "v_mul_hi_u32_u24_sdwa v209, v246, %20 dst_sel:DWORD dst_unused:UNUSED_PAD src0_sel:WORD_0 src1_sel:DWORD\n\t"
        "v_and_b32_sdwa v197, v247, v176 dst_sel:DWORD dst_unused:UNUSED_PAD src0_sel:BYTE_2 src1_sel:DWORD\n\t"
        "v_lshl_add_u32 v209, v209, 2, v197\n\t"
        "ds_add_rtn_u32 v209, v209, v191\n\t"
        "v_perm_b32 v221, v247, v246, v179\n\t"
        "v_mad_u32_u16 v233, v246, 32, %13 op_sel:[1,0,0,0]\n\t"
        "s_or_b64 s[54:55], s[54:55], s[58:59]\n\t"
        "21:\n\t"
        "s_mov_b64 s[84:85], 0\n\t"
        "s_mov_b64 exec, s[68:69]\n\t"
        "s_cbranch_execz 22f\n\t"
        "v_cmpx_ne_u32 s[84:85], -1, v248\n\t"
        "v_mul_hi_u32_u24_sdwa v210, v248, %20 dst_sel:DWORD dst_unused:UNUSED_PAD src0_sel:WORD_0 src1_sel:DWORD\n\t"
        "v_and_b32_sdwa v198, v251, v176 dst_sel:DWORD dst_unused:UNUSED_PAD src0_sel:BYTE_0 src1_sel:DWORD\n\t"
        "v_lshl_add_u32 v210, v210, 2, v198\n\t"
        "ds_add_rtn_u32 v210, v210, v191\n\t"
        "v_perm_b32 v222, v251, v248, v177\n\t"
        "v_mad_u32_u16 v234, v248, 32, %14 op_sel:[1,0,0,0]\n\t"
        "v_cmpx_ne_u32 s[86:87], -1, v249\n\t"
        "v_mul_hi_u32_u24_sdwa v211, v249, %20 dst_sel:DWORD dst_unused:UNUSED_PAD src0_sel:WORD_0 src1_sel:DWORD\n\t"
        "v_and_b32_sdwa v199, v251, v176 dst_sel:DWORD dst_unused:UNUSED_PAD src0_sel:BYTE_1 src1_sel:DWORD\n\t"
        "v_lshl_add_u32 v211, v211, 2, v199\n\t"
        "ds_add_rtn_u32 v211, v211, v191\n\t"
        "v_perm_b32 v223, v251, v249, v178\n\t"
        "v_mad_u32_u16 v235, v249, 32, %14 op_sel:[1,0,0,0]\n\t"
        "s_and_b64 exec, exec, s[52:53]\n\t"
        "v_cmpx_ne_u32 s[88:89], -1, v250\n\t"
        "v_mul_hi_u32_u24_sdwa v212, v250, %20 dst_sel:DWORD dst_unused:UNUSED_PAD src0_sel:WORD_0 src1_sel:DWORD\n\t"
        "v_and_b32_sdwa v200, v251, v176 dst_sel:DWORD dst_unused:UNUSED_PAD src0_sel:BYTE_2 src1_sel:DWORD\n\t"
        "v_lshl_add_u32 v212, v212, 2, v200\n\t"
        "ds_add_rtn_u32 v212, v212, v191\n\t"
        "v_perm_b32 v224, v251, v250, v179\n\t"
        "v_mad_u32_u16 v236, v250, 32, %14 op_sel:[1,0,0,0]\n\t"
        "s_or_b64 s[54:55], s[54:55], s[60:61]\n\t"
        "22:\n\t"
        "s_mov_b64 s[90:91], 0\n\t"
        "s_mov_b64 exec, s[70:71]\n\t"
        "s_cbranch_execz 23f\n\t"
        "v_cmpx_ne_u32 s[90:91], -1, v252\n\t"
        "v_mul_hi_u32_u24_sdwa v213, v252, %20 dst_sel:DWORD dst_unused:UNUSED_PAD src0_sel:WORD_0 src1_sel:DWORD\n\t"
        "v_and_b32_sdwa v201, v255, v176 dst_sel:DWORD dst_unused:UNUSED_PAD src0_sel:BYTE_0 src1_sel:DWORD\n\t"
        "v_lshl_add_u32 v213, v213, 2, v201\n\t"
        "ds_add_rtn_u32 v213, v213, v191\n\t"
        "v_perm_b32 v225, v255, v252, v177\n\t"
        "v_mad_u32_u16 v237, v252, 32, %15 op_sel:[1,0,0,0]\n\t"
        "v_cmpx_ne_u32 s[92:93], -1, v253\n\t"
        "v_mul_hi_u32_u24_sdwa v214, v253, %20 dst_sel:DWORD dst_unused:UNUSED_PAD src0_sel:WORD_0 src1_sel:DWORD\n\t"
        "v_and_b32_sdwa v202, v255, v176 dst_sel:DWORD dst_unused:UNUSED_PAD src0_sel:BYTE_1 src1_sel:DWORD\n\t"
        "v_lshl_add_u32 v214, v214, 2, v202\n\t"
        "ds_add_rtn_u32 v214, v214, v191\n\t"
        "v_perm_b32 v226, v255, v253, v178\n\t"
        "v_mad_u32_u16 v238, v253, 32, %15 op_sel:[1,0,0,0]\n\t"
        "s_and_b64 exec, exec, s[52:53]\n\t"
        "v_cmpx_ne_u32 s[94:95], -1, v254\n\t"
        "v_mul_hi_u32_u24_sdwa v215, v254, %20 dst_sel:DWORD dst_unused:UNUSED_PAD src0_sel:WORD_0 src1_sel:DWORD\n\t"
        "v_and_b32_sdwa v203, v255, v176 dst_sel:DWORD dst_unused:UNUSED_PAD src0_sel:BYTE_2 src1_sel:DWORD\n\t"
        "v_lshl_add_u32 v215, v215, 2, v203\n\t"
        "ds_add_rtn_u32 v215, v215, v191\n\t"
        "v_perm_b32 v227, v255, v254, v179\n\t"
        "v_mad_u32_u16 v239, v254, 32, %15 op_sel:[1,0,0,0]\n\t"
        "s_or_b64 s[54:55], s[54:55], s[62:63]\n\t"
        "23:\n\t"
        "s_cmp_eq_u64 s[54:55], 0\n\t"
        "s_cbranch_scc1 59f\n\t"
        "s_mov_b64 exec, s[56:57]\n\t"
        "s_cbranch_execz 40f\n\t"
        "v_bfe_u32 v188, %4, 7, 8\n\t"
        "v_lshrrev_b32 v189, 24, %4\n\t"
        "v_add_u32 v189, 1, v189\n\t"
        "ds_write_b8 v188, v189 offset:256\n\t"
        "v_add_u32 %4, s96, %4\n\t"
        "v_mov_b32 v190, 0\n\t"
        "v_cmpx_eq_u32 vcc, 2, v189\n\t"
        "ds_write_b32 v190, v191 offset:20\n\t"
        "s_andn2_b64 exec, s[56:57], vcc\n\t"
        "s_cbranch_execz 40f\n\t"
        "s_or_b64 %0, %0, exec\n\t"
        "global_load_dwordx4 v[240:243], %4, %16 sc1\n\t"
        "40:\n\t"
        "s_mov_b64 exec, s[58:59]\n\t"
        "s_cbranch_execz 41f\n\t"
        "v_bfe_u32 v188, %5, 7, 8\n\t"
        "v_lshrrev_b32 v189, 24, %5\n\t"
        "v_add_u32 v189, 1, v189\n\t"
        "ds_write_b8 v188, v189 offset:256\n\t"
        "v_add_u32 %5, s96, %5\n\t"
        "v_mov_b32 v190, 0\n\t"
        "v_cmpx_eq_u32 vcc, 2, v189\n\t"
        "ds_write_b32 v190, v191 offset:20\n\t"
        "s_andn2_b64 exec, s[58:59], vcc\n\t"
        "s_cbranch_execz 41f\n\t"
        "s_or_b64 %1, %1, exec\n\t"
        "global_load_dwordx4 v[244:247], %5, %16 sc1\n\t"
        "41:\n\t"
        "s_mov_b64 exec, s[60:61]\n\t"
        "s_cbranch_execz 42f\n\t"
        "v_bfe_u32 v188, %6, 7, 8\n\t"
        "v_lshrrev_b32 v189, 24, %6\n\t"
        "v_add_u32 v189, 1, v189\n\t"
        "ds_write_b8 v188, v189 offset:256\n\t"
        "v_add_u32 %6, s96, %6\n\t"
        "v_mov_b32 v190, 0\n\t"
        "v_cmpx_eq_u32 vcc, 2, v189\n\t"
        "ds_write_b32 v190, v191 offset:20\n\t"
        "s_andn2_b64 exec, s[60:61], vcc\n\t"
        "s_cbranch_execz 42f\n\t"
        "s_or_b64 %2, %2, exec\n\t"
        "global_load_dwordx4 v[248:251], %6, %16 sc1\n\t"
        "42:\n\t"
        "s_mov_b64 exec, s[62:63]\n\t"
        "s_cbranch_execz 43f\n\t"
        "v_bfe_u32 v188, %7, 7, 8\n\t"
        "v_lshrrev_b32 v189, 24, %7\n\t"
        "v_add_u32 v189, 1, v189\n\t"
        "ds_write_b8 v188, v189 offset:256\n\t"
        "v_add_u32 %7, s96, %7\n\t"
        "v_mov_b32 v190, 0\n\t"
        "v_cmpx_eq_u32 vcc, 2, v189\n\t"
        "ds_write_b32 v190, v191 offset:20\n\t"
        "s_andn2_b64 exec, s[62:63], vcc\n\t"
        "s_cbranch_execz 43f\n\t"
        "s_or_b64 %3, %3, exec\n\t"
        "global_load_dwordx4 v[252:255], %7, %16 sc1\n\t"
        "43:\n\t"
        "59:\n\t"
        "s_waitcnt lgkmcnt(0)\n\t"
        "s_mov_b64 exec, s[72:73]\n\t"
        "s_cbranch_execz 30f\n\t"
        "v_min_u32 v204, v181, v204\n\t"
        "v_min_u32 v205, v181, v205\n\t"
        "v_min_u32 v206, v181, v206\n\t"
        "v_mad_u32_u24 v204, v192, v180, v204\n\t"
        "v_mad_u32_u24 v205, v193, v180, v205\n\t"
        "v_mad_u32_u24 v206, v194, v180, v206\n\t"
        "ds_write2st64_b32 v204, v216, v228 offset1:10\n\t"
        "s_mov_b64 exec, s[74:75]\n\t"
        "ds_write2st64_b32 v205, v217, v229 offset1:10\n\t"
        "s_mov_b64 exec, s[76:77]\n\t"
        "ds_write2st64_b32 v206, v218, v230 offset1:10\n\t"
        "30:\n\t"
        "s_mov_b64 exec, s[78:79]\n\t"
        "s_cbranch_execz 31f\n\t"
        "v_min_u32 v207, v181, v207\n\t"
        "v_min_u32 v208, v181, v208\n\t"
        "v_min_u32 v209, v181, v209\n\t"
        "v_mad_u32_u24 v207, v195, v180, v207\n\t"
        "v_mad_u32_u24 v208, v196, v180, v208\n\t"
        "v_mad_u32_u24 v209, v197, v180, v209\n\t"
        "ds_write2st64_b32 v207, v219, v231 offset1:10\n\t"
        "s_mov_b64 exec, s[80:81]\n\t"
        "ds_write2st64_b32 v208, v220, v232 offset1:10\n\t"
        "s_mov_b64 exec, s[82:83]\n\t"
        "ds_write2st64_b32 v209, v221, v233 offset1:10\n\t"
        "31:\n\t"
        "s_mov_b64 exec, s[84:85]\n\t"
        "s_cbranch_execz 32f\n\t"
        "v_min_u32 v210, v181, v210\n\t"
        "v_min_u32 v211, v181, v211\n\t"
        "v_min_u32 v212, v181, v212\n\t"
        "v_mad_u32_u24 v210, v198, v180, v210\n\t"
        "v_mad_u32_u24 v211, v199, v180, v211\n\t"
        "v_mad_u32_u24 v212, v200, v180, v212\n\t"
        "ds_write2st64_b32 v210, v222, v234 offset1:10\n\t"
        "s_mov_b64 exec, s[86:87]\n\t"
        "ds_write2st64_b32 v211, v223, v235 offset1:10\n\t"
        "s_mov_b64 exec, s[88:89]\n\t"
        "ds_write2st64_b32 v212, v224, v236 offset1:10\n\t"
        "32:\n\t"
        "s_mov_b64 exec, s[90:91]\n\t"
        "s_cbranch_execz 33f\n\t"
        "v_min_u32 v213, v181, v213\n\t"
        "v_min_u32 v214, v181, v214\n\t"
        "v_min_u32 v215, v181, v215\n\t"
        "v_mad_u32_u24 v213, v201, v180, v213\n\t"
        "v_mad_u32_u24 v214, v202, v180, v214\n\t"
        "v_mad_u32_u24 v215, v203, v180, v215\n\t"
        "ds_write2st64_b32 v213, v225, v237 offset1:10\n\t"
        "s_mov_b64 exec, s[92:93]\n\t"
        "ds_write2st64_b32 v214, v226, v238 offset1:10\n\t"
        "s_mov_b64 exec, s[94:95]\n\t"
        "ds_write2st64_b32 v215, v227, v239 offset1:10\n\t"
        "33:\n\t"
        "s_mov_b64 exec, -1\n\t"
        : "+&s"(pend[0]), "+&s"(pend[1]), "+&s"(pend[2]), "+&s"(pend[3]), "+v"(vo[0]), "+v"(vo[1]), "+v"(vo[2]),
          "+v"(vo[3]), "+{v[240:243]}"(cv[0]), "+{v[244:247]}"(cv[1]), "+{v[248:251]}"(cv[2]), "+{v[252:255]}"(cv[3])
        : "v"(eb[0]), "v"(eb[1]), "v"(eb[2]), "v"(eb[3]), "s"(ib), "n"(OFF_L + LARR - 4), "v"(tag[0]), "v"(tag[1]),
          "s"(sq), "s"(just)
        :
          "memory", "vcc", "scc", "v176", "v177", "v178", "v179", "v180", "v181", "v188", "v189", "v190", "v191",
          "v192", "v193", "v194", "v195", "v196", "v197", "v198", "v199", "v200", "v201", "v202", "v203", "v204",
          "v205", "v206", "v207", "v208", "v209", "v210", "v211", "v212", "v213", "v214", "v215", "v216", "v217",
          "v218", "v219", "v220", "v221", "v222", "v223", "v224", "v225", "v226", "v227", "v228", "v229", "v230",
          "v231", "v232", "v233", "v234", "v235", "v236", "v237", "v238", "v239", "s52", "s53", "s54", "s55", "s56",
          "s57", "s58", "s59", "s60", "s61", "s62", "s63", "s64", "s65", "s66", "s67", "s68", "s69", "s70", "s71",
          "s72", "s73", "s74", "s75", "s76", "s77", "s78", "s79", "s80", "s81", "s82", "s83", "s84", "s85", "s86",
          "s87", "s88", "s89", "s90", "s91", "s92", "s93", "s94", "s95", "s96");
    uni();
    just = 0u;
    asm volatile("v_readlane_b32 s92, %0, 14\n\ts_nop 3\n\ts_add_u32 s92, s92, 1\n\ts_nop 3\n\tv_writelane_b32 %0, s92, 14" : "+v"(stv) :: "s92", "scc");
  }
  PT(10);
  *(LAS unsigned*)((LAS char*)smem + OFF_CNT + 16 + wave * 32) = early;
  asm volatile("s_waitcnt lgkmcnt(0)\n\ts_barrier" ::: "memory");
  if (wave >= 4) __builtin_amdgcn_s_setprio(1);
  PT(11);
  const long long tmA = __builtin_amdgcn_s_memrealtime();
  asm volatile("" : "+v"(tm0v));
  const unsigned tm0s = __builtin_amdgcn_readfirstlane(tm0v);
  const u32x4 cw = *(const LAS u32x4*)((LAS char*)smem + OFF_CNT + rid * 32);
  const unsigned ef = *(const LAS unsigned*)((LAS char*)smem + OFF_CNT + 16 + rid * 32);
  unsigned slowf = *(const LAS unsigned*)((LAS char*)smem + OFF_CNT + 20);
  asm volatile("" : "+v"(slowf));
  const int key = (int)((cw[0] + cw[1] + cw[2] + cw[3]) * 2u + rid);
  int gb;
  {
    unsigned acc = 0u;
#define RK(k) acc = __builtin_amdgcn_alignbit(acc, (unsigned)(__builtin_amdgcn_update_dpp(0, key, 0x120 + k, 0xf, 0xf, false) - key), 31)
    RK(1); RK(2); RK(3); RK(4); RK(5); RK(6); RK(7);
#undef RK
    const int want = RANKOF(wave);
    gb = __builtin_ctzll(__ballot(__builtin_popcount(acc) == want)) & 7;
  }
  const unsigned ring0 = lds0 + OFF_R0 + gb * RING_SLOT;
  const unsigned la = l0 + gb * LIST_BYTES;
  u32x16 XAf;
#pragma unroll
  for (int q = 0; q < 4; ++q) {
    const u32x4 t = ld128(ring0 + ringd + q * 1024 + lane * 16);
#pragma unroll
    for (int j = 0; j < 4; ++j) XAf[4 * q + j] = t[j];
  }
  unsigned sl[4];
#pragma unroll
  for (int q = 0; q < 4; ++q) sl[q] = __builtin_amdgcn_readlane(cw[q], gb) - (l0 + q * SUBL);
  const unsigned o2 = sl[0] + sl[1], o3 = o2 + sl[2];
  const bool slow = o3 + sl[3] > LCAP * 4u || max(max(sl[0], sl[1]), max(sl[2], sl[3])) > SUBL - 4u ||
                    __builtin_amdgcn_readfirstlane(slowf) != 0u;
  const int cnt = slow ? 0 : (int)(o3 + sl[3]) >> 2;
  const unsigned cg = (bu * 8 + gb) * 32;
  u32x16 AD;
  AD[2] = aLX + gb * LIST_BYTES;
  AD[3] = aLE + gb * LIST_BYTES;
  AD[4] = cx0; AD[5] = cx1; AD[6] = ce;
  auto issue_gather = [&](int tile, int rs) {
    const u32x4 sx = ld128(AD[2] + tile * 128);
    glds16e(ES, *(const LAS unsigned*)(size_t)(AD[3] + tile * 128), AD[6], ring0 + rs * ringd + 4096);
#pragma unroll
    for (int q = 0; q < 4; ++q) glds16x(x, sx[q], AD[4 + (q & 1)], ring0 + rs * ringd + q * 1024);
  };
  auto pads = [&](int cnt, int lane) __attribute__((always_inline)) {
    const int u = cnt + lane;
    const unsigned c = cg + ((u >> 2) & 7) + 8 * (u & 3), n = nodeof(c);
    const unsigned padA = n | (__builtin_amdgcn_readlane(c, __builtin_ctzll(__ballot((n * Mw + Cw) >> 16 != c))) & 0xffu) << 16;
    const unsigned pa = la + u * 4;
    *(LAS unsigned*)(size_t)pa = padA;
    *(LAS unsigned*)(size_t)(pa + 256) = padA;
    *(LAS unsigned*)(size_t)(pa + LARR) = 0u;
    *(LAS unsigned*)(size_t)(pa + LARR + 256) = 0u;
  };
  constexpr int GEND = 64 + NCH;
  int gs = 0, gp = 0;
  auto gen = [&]() __attribute__((always_inline)) {
    int l2, n = 0;
    asm volatile("v_mbcnt_lo_u32_b32 %0, -1, 0\n\tv_mbcnt_hi_u32_b32 %0, -1, %0" : "=v"(l2));
    const LAS unsigned char* xf = (const LAS unsigned char*)smem + OFF_XF;
    auto put = [&](bool ok, unsigned t, unsigned c, int cc) {
      ok = ok && (int)(c >> 5) == gb;
      const unsigned long long bm = __ballot(ok);
      const unsigned pa = la + 4u * (n + __builtin_amdgcn_mbcnt_hi((unsigned)(bm >> 32), __builtin_amdgcn_mbcnt_lo((unsigned)bm, 0u)));
      if (ok) {
        *(LAS unsigned*)(size_t)pa = (t & 0xffffu) | (c << 16);
        *(LAS unsigned*)(size_t)(pa + LARR) = ((unsigned)(cc * chunk) + (t >> 16)) * 32u;
      }
      n += __builtin_popcountll(bm);
    };
#pragma unroll 1
    while (gs < GEND && n <= LCAP - 192) {
      if (gs < 64) {
        const int cc = (gs & 31) * 8 + (l2 >> 3);
        const unsigned* pp = reinterpret_cast<const unsigned*>(ib + (gs >> 5) * IEXT + (gs & 31) * 1024 + l2 * 16);
        const bool live = gs < 32 || xf[cc] != 0;
        const u32x2 tg = *(const LAS u32x2*)((LAS char*)smem + OFF_CNT + 56);
#pragma unroll 1
        for (int i = 0; i < (1 << 20); ++i) {
          const unsigned m = ldd(pp + 3);
          unsigned sm_ = ldd(pp) + ldd(pp + 1) + ldd(pp + 2) + m + tg[0];
          sm_ += __builtin_amdgcn_update_dpp(0u, sm_, 0xB1, 0xf, 0xf, false);
          sm_ += __builtin_amdgcn_update_dpp(0u, sm_, 0x4E, 0xf, 0xf, false);
          sm_ += __builtin_amdgcn_update_dpp(0u, sm_, 0x141, 0xf, 0xf, false);
          if (__ballot(live && (sm_ > 1u || m >> 24 != tg[1])) == 0ull) break;
        }
        const unsigned m = ldd(pp + 3);
#pragma unroll 1
        for (int j = 0; j < 3; ++j) {
          const unsigned t = ldd(pp + j);
          put(live && t != ~0u && (j < 2 || (l2 & 7) != 7), t, (m >> (8 * j)) & 0xffu, cc);
        }
        ++gs;
      } else {
        const int c2 = gs - 64 + l2;
        const unsigned long long fm = __ballot(c2 < NCH && xf[min(c2, NCH - 1)] == 2);
        if (fm == 0ull) { gs = min(gs + 64, GEND); continue; }
        gs += __builtin_ctzll(fm);
        const int cc = gs - 64;
        const unsigned long long ds = ldq(FL + 512 + bu * NCH + cc);
        const int len = __builtin_amdgcn_readfirstlane((int)(ds >> 32));
        const unsigned long long* tp = TS + (unsigned)ds;
#pragma unroll 1
        for (; gp < len && n <= LCAP - 64; gp += 64) {
          const unsigned long long t = ldq(tp + min(gp + l2, len - 1));
          put(gp + l2 < len, (unsigned)t, (unsigned)(t >> 32), cc);
        }
        if (gp >= len) { gp = 0; ++gs; }
      }
    }
    __builtin_amdgcn_s_waitcnt(0x0F70);
    pads(n, l2);
    return n;
  };
  const bool quick = !slow && sl[0] >= 256u && __builtin_amdgcn_readlane(ef, gb) != 0u;
  auto gather1 = [&](unsigned skip) {
    u32x4 sx = ld128(AD[2] + 128);
    unsigned re = *(const LAS unsigned*)(size_t)(AD[3] + 128);
    asm volatile("s_cmp_lg_u32 %13, 0\n\t"
                 "s_cbranch_scc1 1f\n\t"
                 "s_add_i32 m0, %11, 4096\n\t"
                 "v_add_u32 %4, %4, %8\n\t"
                 "v_mad_u32_u16 %0, %0, %12, %6\n\t"
                 "global_load_lds_dwordx4 %4, %10\n\t"
                 "s_mov_b32 m0, %11\n\t"
                 "v_mad_u32_u16 %1, %1, %12, %7\n\t"
                 "global_load_lds_dwordx4 %0, %9\n\t"
                 "s_addk_i32 m0, 1024\n\t"
                 "v_mad_u32_u16 %2, %2, %12, %6\n\t"
                 "global_load_lds_dwordx4 %1, %9\n\t"
                 "s_addk_i32 m0, 1024\n\t"
                 "v_mad_u32_u16 %3, %3, %12, %7\n\t"
                 "global_load_lds_dwordx4 %2, %9\n\t"
                 "s_addk_i32 m0, 1024\n\t"
                 "s_nop 0\n\t"
                 "global_load_lds_dwordx4 %3, %9\n"
                 "1:"
                 : "+v"(sx[0]), "+v"(sx[1]), "+v"(sx[2]), "+v"(sx[3]), "+v"(re), "+{v[128:143]}"(XAf)
                 : "v"(cx0), "v"(cx1), "v"(ce), "s"(x), "s"(ES), "s"(ring0 + ringd), "s"(128u), "s"(skip)
                 : "memory", "m0", "scc");
  };
  unsigned mode;
  asm volatile("s_cmp_lg_u64 %1, 0\n\ts_cselect_b32 %0, %2, 0" : "=s"(mode) : "s"(__ballot(quick)), "s"(1 + wave / G1W) : "scc");
  gather1(mode ^ 2u);
  if (__builtin_expect(!slow, 1)) {
    const unsigned p0 = la + lane * 4;
    asm volatile(
        "ds_read2st64_b32 v[222:223], %1 offset0:2 offset1:12\n\t"
        "ds_read2st64_b32 v[224:225], %1 offset0:3 offset1:13\n\t"
        "ds_read2st64_b32 v[226:227], %1 offset0:4 offset1:14\n\t"
        "ds_read2st64_b32 v[228:229], %0 offset0:5 offset1:15\n\t"
        "ds_read2st64_b32 v[230:231], %0 offset0:6 offset1:16\n\t"
        "ds_read2st64_b32 v[232:233], %0 offset0:7 offset1:17\n\t"
        "ds_read2st64_b32 v[234:235], %1 offset0:7 offset1:17\n\t"
        "ds_read2st64_b32 v[236:237], %1 offset0:8 offset1:18\n\t"
        "ds_read2st64_b32 v[238:239], %1 offset0:9 offset1:19\n\t"
        "s_waitcnt lgkmcnt(8)\n\t"
        "ds_write2st64_b32 %2, v222, v223 offset0:0 offset1:10\n\t"
        "s_waitcnt lgkmcnt(8)\n\t"
        "ds_write2st64_b32 %2, v224, v225 offset0:1 offset1:11\n\t"
        "s_waitcnt lgkmcnt(8)\n\t"
        "ds_write2st64_b32 %2, v226, v227 offset0:2 offset1:12\n\t"
        "s_waitcnt lgkmcnt(8)\n\t"
        "ds_write2st64_b32 %3, v228, v229 offset0:0 offset1:10\n\t"
        "s_waitcnt lgkmcnt(8)\n\t"
        "ds_write2st64_b32 %3, v230, v231 offset0:1 offset1:11\n\t"
        "s_waitcnt lgkmcnt(8)\n\t"
        "ds_write2st64_b32 %3, v232, v233 offset0:2 offset1:12\n\t"
        "s_waitcnt lgkmcnt(8)\n\t"
        "ds_write2st64_b32 %4, v234, v235 offset0:0 offset1:10\n\t"
        "s_waitcnt lgkmcnt(8)\n\t"
        "ds_write2st64_b32 %4, v236, v237 offset0:1 offset1:11\n\t"
        "s_mov_b64 exec, 0xffffffff\n\t"
        "s_waitcnt lgkmcnt(8)\n\t"
        "ds_write2st64_b32 %4, v238, v239 offset0:2 offset1:12\n\t"
        "s_mov_b64 exec, -1"
        :: "v"(p0), "v"(p0 + 128u), "v"(p0 + sl[0]), "v"(p0 + o2), "v"(p0 + o3)
        : "memory", "v222", "v223", "v224", "v225", "v226", "v227", "v228", "v229", "v230", "v231", "v232", "v233", "v234",
          "v235", "v236", "v237", "v238", "v239");
  }
  pads(cnt, lane);
  AD[0] = aLS4 + gb * LIST_BYTES;
  AD[1] = aLS1 + gb * LIST_BYTES;
  AD[7] = cS - gb * 32 * TPITCH;
  AD[8] = cSt - gb * 32 * TPITCH;
#pragma unroll
  for (int t = 0; t < 4; ++t) AD[9 + t] = fo[t] + gb * RING_SLOT;
  AD[13] = foe + gb * RING_SLOT;
  AD[14] = lds0 + lane * 16;
  AD[15] = to + 18 * 1024 + h * 4096;
  if (__builtin_expect(mode == 0u, 0)) asm volatile("s_waitcnt vmcnt(0)\n\ts_barrier" ::: "memory");
  const int gbin = bk * 8 + wave;
  long long tm1 = 0, c1 = 0;
  const long long tmB = __builtin_amdgcn_s_memrealtime(); __builtin_amdgcn_s_waitcnt(0xC07F);
#define STAMP1 "s_memrealtime %[t1]\n\ts_memtime %[c1]\n\ts_waitcnt lgkmcnt(0)\n\t"
  PT(12);
  PV(15, mode);
  if (lane < 16) *(LAS unsigned*)((LAS char*)smem + MAIN_LDS + wave * 64 + lane * 4) = stv;
  f32x16 X[2], U[2];
  if (__builtin_expect(mode == 0u, 0)) {
    issue_gather(0, 0);
    issue_gather(1, 1);
  }
  {
    int nt = __builtin_amdgcn_readfirstlane(max((cnt - 1) >> 5, 0));
    asm volatile(
        "s_movk_i32 s94, 128\n\t"
        "s_movk_i32 s95, 80\n\t"
        "s_mov_b32 s96, %[cu]\n\t"
        "ds_read_u16 v102, v161\n\t"
        "ds_read_b128 v[104:107], v162 offset:128\n\t"
        "ds_read_b32 v101, v163 offset:128\n\t"
        "ds_read_b128 v[96:99], v162 offset:256\n\t"
        "ds_read_b32 v100, v163 offset:256\n\t"
        "ds_read_b128 v[120:123], v160\n\t"
        "ds_read_u16 v124, v161 offset:128\n\t"
        "s_waitcnt lgkmcnt(6)\n\t"
        "v_mad_u32_u24 v102, v102, s95, v168\n\t"
        "ds_read_b128 v[184:187], v102\n\t"
        "ds_read_b128 v[188:191], v102 offset:16\n\t"
        "ds_read_b128 v[112:115], v174 offset:512\n\t"
        "ds_read_b128 v[116:119], v174 offset:2560\n\t"
        "ds_read_b128 v[108:111], v174 offset:8704\n\t"
        "ds_read_b128 v[196:199], v174 offset:16896\n\t"
        "ds_read_b128 v[200:203], v174 offset:1536\n\t"
        "ds_read_b128 v[204:207], v174 offset:3584\n\t"
        "s_waitcnt lgkmcnt(12)\n\t"
        "v_add_u32 v101, v101, v166\n\t"
        "v_mad_u32_u16 v104, v104, s94, v164\n\t"
        "v_mad_u32_u16 v105, v105, s94, v165\n\t"
        "v_mad_u32_u16 v106, v106, s94, v164\n\t"
        "v_mad_u32_u16 v107, v107, s94, v165\n\t"
        "s_waitcnt lgkmcnt(7)\n\t"
        "v_mfma_f32_32x32x16_f16 v[64:79], v[184:187], v[128:131], 0\n\t"
        "ds_read_b128 v[208:211], v174 offset:9728\n\t"
        "ds_read_b128 v[212:215], v174 offset:10752\n\t"
        "ds_read_b128 v[216:219], v174 offset:11776\n\t"
        "ds_read_b128 v[220:223], v174 offset:17920\n\t"
        "ds_read_b128 v[224:227], v174 offset:4608\n\t"
        "ds_read_b128 v[228:231], v174 offset:5632\n\t"
        "s_waitcnt lgkmcnt(12)\n\t"
        "v_mfma_f32_32x32x16_f16 v[64:79], v[188:191], v[136:139], v[64:79]\n\t"
        "ds_read_b128 v[232:235], v174 offset:6656\n\t"
        "ds_read_b128 v[236:239], v174 offset:7680\n\t"
        "ds_read_b128 v[240:243], v174 offset:12800\n\t"
        "ds_read_b128 v[244:247], v174 offset:13824\n\t"
        "ds_read_b128 v[248:251], v174 offset:14848\n\t"
        "ds_read_b128 v[252:255], v174 offset:15872\n\t"
        "s_add_i32 m0, %[ring], 45056\n\t"
        "s_waitcnt vmcnt(5)\n\t"
        "s_cmp_eq_u32 %[flag], 0\n\t"
        "s_cbranch_scc1 2f\n\t"
        "s_cmp_eq_u32 %[flag], 2\n\t"
        "s_cbranch_scc1 3f\n\t"
        "s_waitcnt vmcnt(0)\n\t"
        "3:\n\t"
        "s_barrier\n\t"
        "2:\n\t"
        "ds_read_b128 v[144:147], v169\n\t"
        "ds_read_b128 v[148:151], v170\n\t"
        "ds_read_b128 v[152:155], v171\n\t"
        "ds_read_b128 v[156:159], v172\n\t"
        "ds_read_b128 v[192:195], v173\n\t"
        "s_cbranch_scc1 4f\n\t"
        "global_load_lds_dwordx4 v101, %[es]\n\t"
        "s_add_i32 m0, %[ring], 40960\n\t"
        "s_nop 0\n\t"
        "global_load_lds_dwordx4 v104, %[x]\n\t"
        "s_add_i32 m0, %[ring], 41984\n\t"
        "s_nop 0\n\t"
        "global_load_lds_dwordx4 v105, %[x]\n\t"
        "s_add_i32 m0, %[ring], 43008\n\t"
        "s_nop 0\n\t"
        "global_load_lds_dwordx4 v106, %[x]\n\t"
        "s_add_i32 m0, %[ring], 44032\n\t"
        "s_nop 0\n\t"
        "global_load_lds_dwordx4 v107, %[x]\n\t"
        "4:\n\t"
        "s_waitcnt lgkmcnt(4)\n\t"
        "v_mfma_f32_32x32x16_bf16 v[64:79], v[144:147], v[112:115], v[64:79]\n\t"
        "s_waitcnt lgkmcnt(3)\n\t"
        "v_mfma_f32_32x32x16_bf16 v[64:79], v[148:151], v[200:203], v[64:79]\n\t"
        "s_waitcnt lgkmcnt(2)\n\t"
        "v_mfma_f32_32x32x16_bf16 v[64:79], v[152:155], v[116:119], v[64:79]\n\t"
        "s_waitcnt lgkmcnt(1)\n\t"
        "v_mfma_f32_32x32x16_bf16 v[64:79], v[156:159], v[204:207], v[64:79]\n\t"
        "s_waitcnt lgkmcnt(0)\n\t"
        "v_mfma_f32_32x32x16_bf16 v[64:79], v[192:195], v[196:199], v[64:79]\n\t"
        "s_add_i32 m0, %[ring], 4096\n\t"
        "v_add_u32 v100, v100, v166\n\t"
        "global_load_lds_dwordx4 v100, %[es]\n\t"
        "v_mfma_f32_32x32x16_bf16 v[80:95], v[144:147], v[108:111], 0\n\t"
        "s_mov_b32 m0, %[ring]\n\t"
        "v_mad_u32_u16 v96, v96, s94, v164\n\t"
        "global_load_lds_dwordx4 v96, %[x]\n\t"
        "s_add_i32 m0, %[ring], 1024\n\t"
        "v_mad_u32_u16 v97, v97, s94, v165\n\t"
        "global_load_lds_dwordx4 v97, %[x]\n\t"
        "v_mfma_f32_32x32x16_bf16 v[80:95], v[148:151], v[208:211], v[80:95]\n\t"
        "s_add_i32 m0, %[ring], 2048\n\t"
        "v_mad_u32_u16 v98, v98, s94, v164\n\t"
        "global_load_lds_dwordx4 v98, %[x]\n\t"
        "v_exp_f32 v64, v64\n\t"
        "v_exp_f32 v65, v65\n\t"
        "v_mfma_f32_32x32x16_bf16 v[80:95], v[152:155], v[212:215], v[80:95]\n\t"
        "s_add_i32 m0, %[ring], 3072\n\t"
        "v_mad_u32_u16 v99, v99, s94, v165\n\t"
        "global_load_lds_dwordx4 v99, %[x]\n\t"
        "v_exp_f32 v66, v66\n\t"
        "v_exp_f32 v67, v67\n\t"
        "v_mfma_f32_32x32x16_bf16 v[80:95], v[156:159], v[216:219], v[80:95]\n\t"
        "v_cvt_pk_f16_f32 v64, v64, v65\n\t"
        "v_exp_f32 v68, v68\n\t"
        "v_cvt_pk_f16_f32 v65, v66, v67\n\t"
        "v_pk_add_f16 v64, v64, s96\n\t"
        "v_exp_f32 v69, v69\n\t"
        "v_pk_add_f16 v65, v65, s96\n\t"
        "v_rcp_f16_sdwa v64, v64 dst_sel:WORD_0 dst_unused:UNUSED_PRESERVE src0_sel:WORD_0\n\t"
        "v_exp_f32 v70, v70\n\t"
        "v_cvt_pk_f16_f32 v66, v68, v69\n\t"
        "v_rcp_f16_sdwa v65, v65 dst_sel:WORD_0 dst_unused:UNUSED_PRESERVE src0_sel:WORD_0\n\t"
        "v_pk_add_f16 v66, v66, s96\n\t"
        "v_rcp_f16_sdwa v64, v64 dst_sel:WORD_1 dst_unused:UNUSED_PRESERVE src0_sel:WORD_1\n\t"
        "v_cvt_pk_f16_f32 v80, v80, v81\n\t"
        "v_rcp_f16_sdwa v65, v65 dst_sel:WORD_1 dst_unused:UNUSED_PRESERVE src0_sel:WORD_1\n\t"
        "v_pk_mul_f16 v80, v80, v64\n\t"
        STAMP1
        "s_branch .Lr4_%=\n\t"
        ".Lrt_%=:\n\t"
        "v_mfma_f32_32x32x16_f16 v[16:31], v[96:99], v[176:179], v[16:31]\n\t"
        "s_cmp_lt_u32 %[wave], 4\n\t"
        "s_cbranch_scc1 1f\n\t"
        "s_setprio 1\n\t"
        "1:\n\t"
        "ds_read_b128 v[120:123], v160\n\t"
        "ds_read_u16 v124, v161 offset:128\n\t"
        "v_rcp_f16_sdwa v103, v103 dst_sel:WORD_1 dst_unused:UNUSED_PRESERVE src0_sel:WORD_1\n\t"
        "v_cvt_pk_f16_f32 v119, v126, v127\n\t"
        "v_pk_add_f16 v64, v64, s96\n\t"
        "v_exp_f32 v67, v67\n\t"
        "v_rcp_f16_sdwa v64, v64 dst_sel:WORD_0 dst_unused:UNUSED_PRESERVE src0_sel:WORD_0\n\t"
        "v_mfma_f32_32x32x16_f16 v[48:63], v[112:115], v[176:179], v[48:63]\n\t"
        "v_pk_mul_f16 v119, v119, v103\n\t"
        "v_cvt_pk_f16_f32 v65, v66, v67\n\t"
        "v_rcp_f16_sdwa v64, v64 dst_sel:WORD_1 dst_unused:UNUSED_PRESERVE src0_sel:WORD_1\n\t"
        "v_pk_add_f16 v65, v65, s96\n\t"
        "v_exp_f32 v68, v68\n\t"
        "v_mfma_f32_32x32x16_f16 v[16:31], v[100:103], v[180:183], v[16:31]\n\t"
        "v_cvt_pk_f16_f32 v80, v80, v81\n\t"
        "v_rcp_f16_sdwa v65, v65 dst_sel:WORD_0 dst_unused:UNUSED_PRESERVE src0_sel:WORD_0\n\t"
        "v_pk_mul_f16 v80, v80, v64\n\t"
        "v_exp_f32 v69, v69\n\t"
        "v_mfma_f32_32x32x16_f16 v[48:63], v[116:119], v[180:183], v[48:63]\n\t"
        "v_cvt_pk_f16_f32 v66, v68, v69\n\t"
        "v_rcp_f16_sdwa v65, v65 dst_sel:WORD_1 dst_unused:UNUSED_PRESERVE src0_sel:WORD_1\n\t"
        "v_pk_add_f16 v66, v66, s96\n\t"
        "v_exp_f32 v70, v70\n\t"
        ".Lr4_%=:\n\t"
        "v_mfma_f32_32x32x16_f16 v[96:111], v[184:187], v[132:135], 0\n\t"
        "s_waitcnt lgkmcnt(1)\n\t"
        "v_mad_u32_u16 v120, v120, s95, v167 op_sel:[1,0,0,0]\n\t"
        "ds_read_b64_tr_b16 v[176:177], v120\n\t"
        "v_mad_u32_u16 v121, v121, s95, v167 op_sel:[1,0,0,0]\n\t"
        "ds_read_b64_tr_b16 v[178:179], v121\n\t"
        "v_mad_u32_u16 v122, v122, s95, v167 op_sel:[1,0,0,0]\n\t"
        "ds_read_b64_tr_b16 v[180:181], v122\n\t"
        "v_mad_u32_u16 v123, v123, s95, v167 op_sel:[1,0,0,0]\n\t"
        "ds_read_b64_tr_b16 v[182:183], v123\n\t"
        "v_cvt_pk_f16_f32 v81, v82, v83\n\t"
        "v_rcp_f16_sdwa v66, v66 dst_sel:WORD_0 dst_unused:UNUSED_PRESERVE src0_sel:WORD_0\n\t"
        "v_pk_mul_f16 v81, v81, v65\n\t"
        "v_exp_f32 v71, v71\n\t"
        "v_mfma_f32_32x32x16_f16 v[96:111], v[188:191], v[140:143], v[96:111]\n\t"
        "s_waitcnt lgkmcnt(4)\n\t"
        "v_mad_u32_u24 v124, v124, s95, v168\n\t"
        "ds_read_b128 v[184:187], v124\n\t"
        "ds_read_b128 v[188:191], v124 offset:16\n\t"
        "v_cvt_pk_f16_f32 v67, v70, v71\n\t"
        "v_rcp_f16_sdwa v66, v66 dst_sel:WORD_1 dst_unused:UNUSED_PRESERVE src0_sel:WORD_1\n\t"
        "v_pk_add_f16 v67, v67, s96\n\t"
        "v_exp_f32 v72, v72\n\t"
        "v_mfma_f32_32x32x16_bf16 v[96:111], v[192:195], v[220:223], v[96:111]\n\t"
        "s_waitcnt vmcnt(9)\n\t"
        "ds_read_b128 v[192:195], v173 offset:40960\n\t"
        "ds_read_b32 v125, v163 offset:384\n\t"
        "v_cvt_pk_f16_f32 v82, v84, v85\n\t"
        "v_rcp_f16_sdwa v67, v67 dst_sel:WORD_0 dst_unused:UNUSED_PRESERVE src0_sel:WORD_0\n\t"
        "v_pk_mul_f16 v82, v82, v66\n\t"
        "v_exp_f32 v73, v73\n\t"
        "v_mfma_f32_32x32x16_bf16 v[96:111], v[144:147], v[224:227], v[96:111]\n\t"
        "v_cvt_pk_f16_f32 v68, v72, v73\n\t"
        "v_rcp_f16_sdwa v67, v67 dst_sel:WORD_1 dst_unused:UNUSED_PRESERVE src0_sel:WORD_1\n\t"
        "v_pk_add_f16 v68, v68, s96\n\t"
        "v_exp_f32 v74, v74\n\t"
        "v_mfma_f32_32x32x16_bf16 v[96:111], v[148:151], v[228:231], v[96:111]\n\t"
        "v_cvt_pk_f16_f32 v83, v86, v87\n\t"
        "v_rcp_f16_sdwa v68, v68 dst_sel:WORD_0 dst_unused:UNUSED_PRESERVE src0_sel:WORD_0\n\t"
        "v_pk_mul_f16 v83, v83, v67\n\t"
        "v_exp_f32 v75, v75\n\t"
        "v_mfma_f32_32x32x16_bf16 v[96:111], v[152:155], v[232:235], v[96:111]\n\t"
        "s_add_i32 m0, %[ring], 45056\n\t"
        "s_waitcnt lgkmcnt(0)\n\t"
        "v_add_u32 v125, v125, v166\n\t"
        "global_load_lds_dwordx4 v125, %[es]\n\t"
        "v_cvt_pk_f16_f32 v69, v74, v75\n\t"
        "v_rcp_f16_sdwa v68, v68 dst_sel:WORD_1 dst_unused:UNUSED_PRESERVE src0_sel:WORD_1\n\t"
        "v_pk_add_f16 v69, v69, s96\n\t"
        "v_exp_f32 v76, v76\n\t"
        "v_mfma_f32_32x32x16_bf16 v[96:111], v[156:159], v[236:239], v[96:111]\n\t"
        "v_cvt_pk_f16_f32 v84, v88, v89\n\t"
        "v_rcp_f16_sdwa v69, v69 dst_sel:WORD_0 dst_unused:UNUSED_PRESERVE src0_sel:WORD_0\n\t"
        "v_pk_mul_f16 v84, v84, v68\n\t"
        "v_exp_f32 v77, v77\n\t"
        "v_mfma_f32_32x32x16_bf16 v[112:127], v[144:147], v[240:243], 0\n\t"
        "s_waitcnt vmcnt(6)\n\t"
        "ds_read_b128 v[144:147], v169 offset:40960\n\t"
        "v_cvt_pk_f16_f32 v70, v76, v77\n\t"
        "v_rcp_f16_sdwa v69, v69 dst_sel:WORD_1 dst_unused:UNUSED_PRESERVE src0_sel:WORD_1\n\t"
        "v_pk_add_f16 v70, v70, s96\n\t"
        "v_exp_f32 v78, v78\n\t"
        "v_mfma_f32_32x32x16_bf16 v[112:127], v[148:151], v[244:247], v[112:127]\n\t"
        "ds_read_b128 v[148:151], v170 offset:40960\n\t"
        "s_sub_u32 %[nt], %[nt], 1\n\t"
        "v_cvt_pk_f16_f32 v85, v90, v91\n\t"
        "v_rcp_f16_sdwa v70, v70 dst_sel:WORD_0 dst_unused:UNUSED_PRESERVE src0_sel:WORD_0\n\t"
        "v_pk_mul_f16 v85, v85, v69\n\t"
        "v_exp_f32 v79, v79\n\t"
        "v_mfma_f32_32x32x16_bf16 v[112:127], v[152:155], v[248:251], v[112:127]\n\t"
        "ds_read_b128 v[152:155], v171 offset:40960\n\t"
        "ds_read_b128 v[88:91], v162 offset:384\n\t"
        "v_cvt_pk_f16_f32 v71, v78, v79\n\t"
        "v_rcp_f16_sdwa v70, v70 dst_sel:WORD_1 dst_unused:UNUSED_PRESERVE src0_sel:WORD_1\n\t"
        "v_pk_add_f16 v71, v71, s96\n\t"
        "v_exp_f32 v96, v96\n\t"
        "v_exp_f32 v97, v97\n\t"
        "v_mfma_f32_32x32x16_bf16 v[112:127], v[156:159], v[252:255], v[112:127]\n\t"
        "ds_read_b128 v[156:159], v172 offset:40960\n\t"
        "v_cvt_pk_f16_f32 v86, v92, v93\n\t"
        "v_rcp_f16_sdwa v71, v71 dst_sel:WORD_0 dst_unused:UNUSED_PRESERVE src0_sel:WORD_0\n\t"
        "v_pk_mul_f16 v86, v86, v70\n\t"
        "v_exp_f32 v98, v98\n\t"
        "v_cvt_pk_f16_f32 v96, v96, v97\n\t"
        "s_cbranch_scc1 .Lrl_%=\n\t"
        "v_mfma_f32_32x32x16_f16 v[0:15], v[64:67], v[176:179], v[0:15]\n\t"
        "v_rcp_f16_sdwa v71, v71 dst_sel:WORD_1 dst_unused:UNUSED_PRESERVE src0_sel:WORD_1\n\t"
        "v_cvt_pk_f16_f32 v87, v94, v95\n\t"
        "v_pk_add_f16 v96, v96, s96\n\t"
        "v_exp_f32 v99, v99\n\t"
        "v_rcp_f16_sdwa v96, v96 dst_sel:WORD_0 dst_unused:UNUSED_PRESERVE src0_sel:WORD_0\n\t"
        "v_mfma_f32_32x32x16_f16 v[32:47], v[80:83], v[176:179], v[32:47]\n\t"
        "s_add_i32 m0, %[ring], 40960\n\t"
        "s_waitcnt lgkmcnt(1)\n\t"
        "v_mad_u32_u16 v88, v88, s94, v164\n\t"
        "s_waitcnt lgkmcnt(0)\n\t"
        "global_load_lds_dwordx4 v88, %[x]\n\t"
        "v_pk_mul_f16 v87, v87, v71\n\t"
        "v_cvt_pk_f16_f32 v97, v98, v99\n\t"
        "v_rcp_f16_sdwa v96, v96 dst_sel:WORD_1 dst_unused:UNUSED_PRESERVE src0_sel:WORD_1\n\t"
        "v_pk_add_f16 v97, v97, s96\n\t"
        "v_exp_f32 v100, v100\n\t"
        "v_mfma_f32_32x32x16_f16 v[0:15], v[68:71], v[180:183], v[0:15]\n\t"
        "ds_read_b128 v[92:95], v174 offset:512\n\t"
        "s_add_i32 m0, %[ring], 41984\n\t"
        "v_mad_u32_u16 v89, v89, s94, v165\n\t"
        "global_load_lds_dwordx4 v89, %[x]\n\t"
        "v_cvt_pk_f16_f32 v112, v112, v113\n\t"
        "v_rcp_f16_sdwa v97, v97 dst_sel:WORD_0 dst_unused:UNUSED_PRESERVE src0_sel:WORD_0\n\t"
        "v_pk_mul_f16 v112, v112, v96\n\t"
        "v_exp_f32 v101, v101\n\t"
        "v_mfma_f32_32x32x16_f16 v[32:47], v[84:87], v[180:183], v[32:47]\n\t"
        "s_add_i32 m0, %[ring], 43008\n\t"
        "v_mad_u32_u16 v90, v90, s94, v164\n\t"
        "global_load_lds_dwordx4 v90, %[x]\n\t"
        "v_cvt_pk_f16_f32 v98, v100, v101\n\t"
        "v_rcp_f16_sdwa v97, v97 dst_sel:WORD_1 dst_unused:UNUSED_PRESERVE src0_sel:WORD_1\n\t"
        "v_pk_add_f16 v98, v98, s96\n\t"
        "v_exp_f32 v102, v102\n\t"
        "v_mfma_f32_32x32x16_f16 v[64:79], v[184:187], v[128:131], 0\n\t"
        "s_add_i32 m0, %[ring], 44032\n\t"
        "v_mad_u32_u16 v91, v91, s94, v165\n\t"
        "global_load_lds_dwordx4 v91, %[x]\n\t"
        "v_cvt_pk_f16_f32 v113, v114, v115\n\t"
        "v_rcp_f16_sdwa v98, v98 dst_sel:WORD_0 dst_unused:UNUSED_PRESERVE src0_sel:WORD_0\n\t"
        "v_pk_mul_f16 v113, v113, v97\n\t"
        "v_exp_f32 v103, v103\n\t"
        "v_mfma_f32_32x32x16_f16 v[64:79], v[188:191], v[136:139], v[64:79]\n\t"
        "ds_read_b128 v[80:83], v174 offset:2560\n\t"
        "v_cvt_pk_f16_f32 v99, v102, v103\n\t"
        "v_rcp_f16_sdwa v98, v98 dst_sel:WORD_1 dst_unused:UNUSED_PRESERVE src0_sel:WORD_1\n\t"
        "v_pk_add_f16 v99, v99, s96\n\t"
        "v_exp_f32 v104, v104\n\t"
        "v_mfma_f32_32x32x16_bf16 v[64:79], v[192:195], v[196:199], v[64:79]\n\t"
        "v_cvt_pk_f16_f32 v114, v116, v117\n\t"
        "v_rcp_f16_sdwa v99, v99 dst_sel:WORD_0 dst_unused:UNUSED_PRESERVE src0_sel:WORD_0\n\t"
        "v_pk_mul_f16 v114, v114, v98\n\t"
        "v_exp_f32 v105, v105\n\t"
        "s_waitcnt lgkmcnt(1)\n\t"
        "v_mfma_f32_32x32x16_bf16 v[64:79], v[144:147], v[92:95], v[64:79]\n\t"
        "ds_read_b128 v[84:87], v174 offset:8704\n\t"
        "v_cvt_pk_f16_f32 v100, v104, v105\n\t"
        "v_rcp_f16_sdwa v99, v99 dst_sel:WORD_1 dst_unused:UNUSED_PRESERVE src0_sel:WORD_1\n\t"
        "v_pk_add_f16 v100, v100, s96\n\t"
        "v_exp_f32 v106, v106\n\t"
        "v_mfma_f32_32x32x16_bf16 v[64:79], v[148:151], v[200:203], v[64:79]\n\t"
        "v_cvt_pk_f16_f32 v115, v118, v119\n\t"
        "v_rcp_f16_sdwa v100, v100 dst_sel:WORD_0 dst_unused:UNUSED_PRESERVE src0_sel:WORD_0\n\t"
        "v_pk_mul_f16 v115, v115, v99\n\t"
        "v_exp_f32 v107, v107\n\t"
        "s_waitcnt lgkmcnt(1)\n\t"
        "v_mfma_f32_32x32x16_bf16 v[64:79], v[152:155], v[80:83], v[64:79]\n\t"
        "v_cvt_pk_f16_f32 v101, v106, v107\n\t"
        "v_rcp_f16_sdwa v100, v100 dst_sel:WORD_1 dst_unused:UNUSED_PRESERVE src0_sel:WORD_1\n\t"
        "v_pk_add_f16 v101, v101, s96\n\t"
        "v_exp_f32 v108, v108\n\t"
        "v_mfma_f32_32x32x16_bf16 v[64:79], v[156:159], v[204:207], v[64:79]\n\t"
        "v_cvt_pk_f16_f32 v116, v120, v121\n\t"
        "v_rcp_f16_sdwa v101, v101 dst_sel:WORD_0 dst_unused:UNUSED_PRESERVE src0_sel:WORD_0\n\t"
        "v_pk_mul_f16 v116, v116, v100\n\t"
        "v_exp_f32 v109, v109\n\t"
        "s_waitcnt lgkmcnt(0)\n\t"
        "v_mfma_f32_32x32x16_bf16 v[80:95], v[144:147], v[84:87], 0\n\t"
        "v_cvt_pk_f16_f32 v102, v108, v109\n\t"
        "v_rcp_f16_sdwa v101, v101 dst_sel:WORD_1 dst_unused:UNUSED_PRESERVE src0_sel:WORD_1\n\t"
        "v_pk_add_f16 v102, v102, s96\n\t"
        "v_exp_f32 v110, v110\n\t"
        "v_mfma_f32_32x32x16_bf16 v[80:95], v[148:151], v[208:211], v[80:95]\n\t"
        "v_cvt_pk_f16_f32 v117, v122, v123\n\t"
        "v_rcp_f16_sdwa v102, v102 dst_sel:WORD_0 dst_unused:UNUSED_PRESERVE src0_sel:WORD_0\n\t"
        "v_pk_mul_f16 v117, v117, v101\n\t"
        "v_exp_f32 v111, v111\n\t"
        "v_mfma_f32_32x32x16_bf16 v[80:95], v[152:155], v[212:215], v[80:95]\n\t"
        "v_cvt_pk_f16_f32 v103, v110, v111\n\t"
        "v_rcp_f16_sdwa v102, v102 dst_sel:WORD_1 dst_unused:UNUSED_PRESERVE src0_sel:WORD_1\n\t"
        "v_pk_add_f16 v103, v103, s96\n\t"
        "v_exp_f32 v64, v64\n\t"
        "v_exp_f32 v65, v65\n\t"
        "v_mfma_f32_32x32x16_bf16 v[80:95], v[156:159], v[216:219], v[80:95]\n\t"
        "v_cvt_pk_f16_f32 v118, v124, v125\n\t"
        "v_rcp_f16_sdwa v103, v103 dst_sel:WORD_0 dst_unused:UNUSED_PRESERVE src0_sel:WORD_0\n\t"
        "v_pk_mul_f16 v118, v118, v102\n\t"
        "v_exp_f32 v66, v66\n\t"
        "v_cvt_pk_f16_f32 v64, v64, v65\n\t"
        "v_mfma_f32_32x32x16_f16 v[16:31], v[96:99], v[176:179], v[16:31]\n\t"
        "s_setprio 0\n\t"
        "ds_read_b128 v[120:123], v160 offset:128\n\t"
        "ds_read_u16 v124, v161 offset:256\n\t"
        "v_rcp_f16_sdwa v103, v103 dst_sel:WORD_1 dst_unused:UNUSED_PRESERVE src0_sel:WORD_1\n\t"
        "v_cvt_pk_f16_f32 v119, v126, v127\n\t"
        "v_pk_add_f16 v64, v64, s96\n\t"
        "v_exp_f32 v67, v67\n\t"
        "v_rcp_f16_sdwa v64, v64 dst_sel:WORD_0 dst_unused:UNUSED_PRESERVE src0_sel:WORD_0\n\t"
        "v_mfma_f32_32x32x16_f16 v[48:63], v[112:115], v[176:179], v[48:63]\n\t"
        "v_pk_mul_f16 v119, v119, v103\n\t"
        "v_cvt_pk_f16_f32 v65, v66, v67\n\t"
        "v_rcp_f16_sdwa v64, v64 dst_sel:WORD_1 dst_unused:UNUSED_PRESERVE src0_sel:WORD_1\n\t"
        "v_pk_add_f16 v65, v65, s96\n\t"
        "v_exp_f32 v68, v68\n\t"
        "v_mfma_f32_32x32x16_f16 v[16:31], v[100:103], v[180:183], v[16:31]\n\t"
        "v_cvt_pk_f16_f32 v80, v80, v81\n\t"
        "v_rcp_f16_sdwa v65, v65 dst_sel:WORD_0 dst_unused:UNUSED_PRESERVE src0_sel:WORD_0\n\t"
        "v_pk_mul_f16 v80, v80, v64\n\t"
        "v_exp_f32 v69, v69\n\t"
        "v_mfma_f32_32x32x16_f16 v[48:63], v[116:119], v[180:183], v[48:63]\n\t"
        "v_cvt_pk_f16_f32 v66, v68, v69\n\t"
        "v_rcp_f16_sdwa v65, v65 dst_sel:WORD_1 dst_unused:UNUSED_PRESERVE src0_sel:WORD_1\n\t"
        "v_pk_add_f16 v66, v66, s96\n\t"
        "v_exp_f32 v70, v70\n\t"
        "v_mfma_f32_32x32x16_f16 v[96:111], v[184:187], v[132:135], 0\n\t"
        "s_waitcnt lgkmcnt(1)\n\t"
        "v_mad_u32_u16 v120, v120, s95, v167 op_sel:[1,0,0,0]\n\t"
        "ds_read_b64_tr_b16 v[176:177], v120\n\t"
        "v_mad_u32_u16 v121, v121, s95, v167 op_sel:[1,0,0,0]\n\t"
        "ds_read_b64_tr_b16 v[178:179], v121\n\t"
        "v_mad_u32_u16 v122, v122, s95, v167 op_sel:[1,0,0,0]\n\t"
        "ds_read_b64_tr_b16 v[180:181], v122\n\t"
        "v_mad_u32_u16 v123, v123, s95, v167 op_sel:[1,0,0,0]\n\t"
        "ds_read_b64_tr_b16 v[182:183], v123\n\t"
        "v_cvt_pk_f16_f32 v81, v82, v83\n\t"
        "v_rcp_f16_sdwa v66, v66 dst_sel:WORD_0 dst_unused:UNUSED_PRESERVE src0_sel:WORD_0\n\t"
        "v_pk_mul_f16 v81, v81, v65\n\t"
        "v_exp_f32 v71, v71\n\t"
        "v_mfma_f32_32x32x16_f16 v[96:111], v[188:191], v[140:143], v[96:111]\n\t"
        "s_waitcnt lgkmcnt(4)\n\t"
        "v_mad_u32_u24 v124, v124, s95, v168\n\t"
        "ds_read_b128 v[184:187], v124\n\t"
        "ds_read_b128 v[188:191], v124 offset:16\n\t"
        "v_cvt_pk_f16_f32 v67, v70, v71\n\t"
        "v_rcp_f16_sdwa v66, v66 dst_sel:WORD_1 dst_unused:UNUSED_PRESERVE src0_sel:WORD_1\n\t"
        "v_pk_add_f16 v67, v67, s96\n\t"
        "v_exp_f32 v72, v72\n\t"
        "v_mfma_f32_32x32x16_bf16 v[96:111], v[192:195], v[220:223], v[96:111]\n\t"
        "s_waitcnt vmcnt(9)\n\t"
        "ds_read_b128 v[192:195], v173\n\t"
        "ds_read_b32 v125, v163 offset:512\n\t"
        "v_cvt_pk_f16_f32 v82, v84, v85\n\t"
        "v_rcp_f16_sdwa v67, v67 dst_sel:WORD_0 dst_unused:UNUSED_PRESERVE src0_sel:WORD_0\n\t"
        "v_pk_mul_f16 v82, v82, v66\n\t"
        "v_exp_f32 v73, v73\n\t"
        "v_mfma_f32_32x32x16_bf16 v[96:111], v[144:147], v[224:227], v[96:111]\n\t"
        "v_cvt_pk_f16_f32 v68, v72, v73\n\t"
        "v_rcp_f16_sdwa v67, v67 dst_sel:WORD_1 dst_unused:UNUSED_PRESERVE src0_sel:WORD_1\n\t"
        "v_pk_add_f16 v68, v68, s96\n\t"
        "v_exp_f32 v74, v74\n\t"
        "v_mfma_f32_32x32x16_bf16 v[96:111], v[148:151], v[228:231], v[96:111]\n\t"
        "v_cvt_pk_f16_f32 v83, v86, v87\n\t"
        "v_rcp_f16_sdwa v68, v68 dst_sel:WORD_0 dst_unused:UNUSED_PRESERVE src0_sel:WORD_0\n\t"
        "v_pk_mul_f16 v83, v83, v67\n\t"
        "v_exp_f32 v75, v75\n\t"
        "v_mfma_f32_32x32x16_bf16 v[96:111], v[152:155], v[232:235], v[96:111]\n\t"
        "s_add_i32 m0, %[ring], 4096\n\t"
        "s_waitcnt lgkmcnt(0)\n\t"
        "v_add_u32 v125, v125, v166\n\t"
        "global_load_lds_dwordx4 v125, %[es]\n\t"
        "v_cvt_pk_f16_f32 v69, v74, v75\n\t"
        "v_rcp_f16_sdwa v68, v68 dst_sel:WORD_1 dst_unused:UNUSED_PRESERVE src0_sel:WORD_1\n\t"
        "v_pk_add_f16 v69, v69, s96\n\t"
        "v_exp_f32 v76, v76\n\t"
        "v_mfma_f32_32x32x16_bf16 v[96:111], v[156:159], v[236:239], v[96:111]\n\t"
        "v_cvt_pk_f16_f32 v84, v88, v89\n\t"
        "v_rcp_f16_sdwa v69, v69 dst_sel:WORD_0 dst_unused:UNUSED_PRESERVE src0_sel:WORD_0\n\t"
        "v_pk_mul_f16 v84, v84, v68\n\t"
        "v_exp_f32 v77, v77\n\t"
        "v_mfma_f32_32x32x16_bf16 v[112:127], v[144:147], v[240:243], 0\n\t"
        "s_waitcnt vmcnt(6)\n\t"
        "ds_read_b128 v[144:147], v169\n\t"
        "v_cvt_pk_f16_f32 v70, v76, v77\n\t"
        "v_rcp_f16_sdwa v69, v69 dst_sel:WORD_1 dst_unused:UNUSED_PRESERVE src0_sel:WORD_1\n\t"
        "v_pk_add_f16 v70, v70, s96\n\t"
        "v_exp_f32 v78, v78\n\t"
        "v_mfma_f32_32x32x16_bf16 v[112:127], v[148:151], v[244:247], v[112:127]\n\t"
        "ds_read_b128 v[148:151], v170\n\t"
        "s_sub_u32 %[nt], %[nt], 1\n\t"
        "v_cvt_pk_f16_f32 v85, v90, v91\n\t"
        "v_rcp_f16_sdwa v70, v70 dst_sel:WORD_0 dst_unused:UNUSED_PRESERVE src0_sel:WORD_0\n\t"
        "v_pk_mul_f16 v85, v85, v69\n\t"
        "v_exp_f32 v79, v79\n\t"
        "v_mfma_f32_32x32x16_bf16 v[112:127], v[152:155], v[248:251], v[112:127]\n\t"
        "ds_read_b128 v[152:155], v171\n\t"
        "ds_read_b128 v[88:91], v162 offset:512\n\t"
        "v_cvt_pk_f16_f32 v71, v78, v79\n\t"
        "v_rcp_f16_sdwa v70, v70 dst_sel:WORD_1 dst_unused:UNUSED_PRESERVE src0_sel:WORD_1\n\t"
        "v_pk_add_f16 v71, v71, s96\n\t"
        "v_exp_f32 v96, v96\n\t"
        "v_exp_f32 v97, v97\n\t"
        "v_mfma_f32_32x32x16_bf16 v[112:127], v[156:159], v[252:255], v[112:127]\n\t"
        "ds_read_b128 v[156:159], v172\n\t"
        "v_cvt_pk_f16_f32 v86, v92, v93\n\t"
        "v_rcp_f16_sdwa v71, v71 dst_sel:WORD_0 dst_unused:UNUSED_PRESERVE src0_sel:WORD_0\n\t"
        "v_pk_mul_f16 v86, v86, v70\n\t"
        "v_exp_f32 v98, v98\n\t"
        "v_cvt_pk_f16_f32 v96, v96, v97\n\t"
        "s_cbranch_scc1 .Lrl_%=\n\t"
        "v_mfma_f32_32x32x16_f16 v[0:15], v[64:67], v[176:179], v[0:15]\n\t"
        "v_rcp_f16_sdwa v71, v71 dst_sel:WORD_1 dst_unused:UNUSED_PRESERVE src0_sel:WORD_1\n\t"
        "v_cvt_pk_f16_f32 v87, v94, v95\n\t"
        "v_pk_add_f16 v96, v96, s96\n\t"
        "v_exp_f32 v99, v99\n\t"
        "v_rcp_f16_sdwa v96, v96 dst_sel:WORD_0 dst_unused:UNUSED_PRESERVE src0_sel:WORD_0\n\t"
        "v_mfma_f32_32x32x16_f16 v[32:47], v[80:83], v[176:179], v[32:47]\n\t"
        "s_mov_b32 m0, %[ring]\n\t"
        "s_waitcnt lgkmcnt(1)\n\t"
        "v_mad_u32_u16 v88, v88, s94, v164\n\t"
        "s_waitcnt lgkmcnt(0)\n\t"
        "global_load_lds_dwordx4 v88, %[x]\n\t"
        "v_pk_mul_f16 v87, v87, v71\n\t"
        "v_cvt_pk_f16_f32 v97, v98, v99\n\t"
        "v_rcp_f16_sdwa v96, v96 dst_sel:WORD_1 dst_unused:UNUSED_PRESERVE src0_sel:WORD_1\n\t"
        "v_pk_add_f16 v97, v97, s96\n\t"
        "v_exp_f32 v100, v100\n\t"
        "v_mfma_f32_32x32x16_f16 v[0:15], v[68:71], v[180:183], v[0:15]\n\t"
        "ds_read_b128 v[92:95], v174 offset:512\n\t"
        "s_add_i32 m0, %[ring], 1024\n\t"
        "v_mad_u32_u16 v89, v89, s94, v165\n\t"
        "global_load_lds_dwordx4 v89, %[x]\n\t"
        "v_cvt_pk_f16_f32 v112, v112, v113\n\t"
        "v_rcp_f16_sdwa v97, v97 dst_sel:WORD_0 dst_unused:UNUSED_PRESERVE src0_sel:WORD_0\n\t"
        "v_pk_mul_f16 v112, v112, v96\n\t"
        "v_exp_f32 v101, v101\n\t"
        "v_mfma_f32_32x32x16_f16 v[32:47], v[84:87], v[180:183], v[32:47]\n\t"
        "s_add_i32 m0, %[ring], 2048\n\t"
        "v_mad_u32_u16 v90, v90, s94, v164\n\t"
        "global_load_lds_dwordx4 v90, %[x]\n\t"
        "v_cvt_pk_f16_f32 v98, v100, v101\n\t"
        "v_rcp_f16_sdwa v97, v97 dst_sel:WORD_1 dst_unused:UNUSED_PRESERVE src0_sel:WORD_1\n\t"
        "v_pk_add_f16 v98, v98, s96\n\t"
        "v_exp_f32 v102, v102\n\t"
        "v_mfma_f32_32x32x16_f16 v[64:79], v[184:187], v[128:131], 0\n\t"
        "s_add_i32 m0, %[ring], 3072\n\t"
        "v_mad_u32_u16 v91, v91, s94, v165\n\t"
        "global_load_lds_dwordx4 v91, %[x]\n\t"
        "v_cvt_pk_f16_f32 v113, v114, v115\n\t"
        "v_rcp_f16_sdwa v98, v98 dst_sel:WORD_0 dst_unused:UNUSED_PRESERVE src0_sel:WORD_0\n\t"
        "v_pk_mul_f16 v113, v113, v97\n\t"
        "v_exp_f32 v103, v103\n\t"
        "v_mfma_f32_32x32x16_f16 v[64:79], v[188:191], v[136:139], v[64:79]\n\t"
        "ds_read_b128 v[80:83], v174 offset:2560\n\t"
        "v_cvt_pk_f16_f32 v99, v102, v103\n\t"
        "v_rcp_f16_sdwa v98, v98 dst_sel:WORD_1 dst_unused:UNUSED_PRESERVE src0_sel:WORD_1\n\t"
        "v_pk_add_f16 v99, v99, s96\n\t"
        "v_exp_f32 v104, v104\n\t"
        "v_mfma_f32_32x32x16_bf16 v[64:79], v[192:195], v[196:199], v[64:79]\n\t"
        "v_cvt_pk_f16_f32 v114, v116, v117\n\t"
        "v_rcp_f16_sdwa v99, v99 dst_sel:WORD_0 dst_unused:UNUSED_PRESERVE src0_sel:WORD_0\n\t"
        "v_pk_mul_f16 v114, v114, v98\n\t"
        "v_exp_f32 v105, v105\n\t"
        "s_waitcnt lgkmcnt(1)\n\t"
        "v_mfma_f32_32x32x16_bf16 v[64:79], v[144:147], v[92:95], v[64:79]\n\t"
        "ds_read_b128 v[84:87], v174 offset:8704\n\t"
        "v_cvt_pk_f16_f32 v100, v104, v105\n\t"
        "v_rcp_f16_sdwa v99, v99 dst_sel:WORD_1 dst_unused:UNUSED_PRESERVE src0_sel:WORD_1\n\t"
        "v_pk_add_f16 v100, v100, s96\n\t"
        "v_exp_f32 v106, v106\n\t"
        "v_mfma_f32_32x32x16_bf16 v[64:79], v[148:151], v[200:203], v[64:79]\n\t"
        "v_cvt_pk_f16_f32 v115, v118, v119\n\t"
        "v_rcp_f16_sdwa v100, v100 dst_sel:WORD_0 dst_unused:UNUSED_PRESERVE src0_sel:WORD_0\n\t"
        "v_pk_mul_f16 v115, v115, v99\n\t"
        "v_exp_f32 v107, v107\n\t"
        "s_waitcnt lgkmcnt(1)\n\t"
        "v_mfma_f32_32x32x16_bf16 v[64:79], v[152:155], v[80:83], v[64:79]\n\t"
        "v_cvt_pk_f16_f32 v101, v106, v107\n\t"
        "v_rcp_f16_sdwa v100, v100 dst_sel:WORD_1 dst_unused:UNUSED_PRESERVE src0_sel:WORD_1\n\t"
        "v_pk_add_f16 v101, v101, s96\n\t"
        "v_exp_f32 v108, v108\n\t"
        "v_mfma_f32_32x32x16_bf16 v[64:79], v[156:159], v[204:207], v[64:79]\n\t"
        "v_cvt_pk_f16_f32 v116, v120, v121\n\t"
        "v_rcp_f16_sdwa v101, v101 dst_sel:WORD_0 dst_unused:UNUSED_PRESERVE src0_sel:WORD_0\n\t"
        "v_pk_mul_f16 v116, v116, v100\n\t"
        "v_exp_f32 v109, v109\n\t"
        "s_waitcnt lgkmcnt(0)\n\t"
        "v_mfma_f32_32x32x16_bf16 v[80:95], v[144:147], v[84:87], 0\n\t"
        "v_add_u32 v160, 0x100, v160\n\t"
        "v_cvt_pk_f16_f32 v102, v108, v109\n\t"
        "v_rcp_f16_sdwa v101, v101 dst_sel:WORD_1 dst_unused:UNUSED_PRESERVE src0_sel:WORD_1\n\t"
        "v_pk_add_f16 v102, v102, s96\n\t"
        "v_exp_f32 v110, v110\n\t"
        "v_mfma_f32_32x32x16_bf16 v[80:95], v[148:151], v[208:211], v[80:95]\n\t"
        "v_add_u32 v161, 0x100, v161\n\t"
        "v_cvt_pk_f16_f32 v117, v122, v123\n\t"
        "v_rcp_f16_sdwa v102, v102 dst_sel:WORD_0 dst_unused:UNUSED_PRESERVE src0_sel:WORD_0\n\t"
        "v_pk_mul_f16 v117, v117, v101\n\t"
        "v_exp_f32 v111, v111\n\t"
        "v_mfma_f32_32x32x16_bf16 v[80:95], v[152:155], v[212:215], v[80:95]\n\t"
        "v_add_u32 v162, 0x100, v162\n\t"
        "v_cvt_pk_f16_f32 v103, v110, v111\n\t"
        "v_rcp_f16_sdwa v102, v102 dst_sel:WORD_1 dst_unused:UNUSED_PRESERVE src0_sel:WORD_1\n\t"
        "v_pk_add_f16 v103, v103, s96\n\t"
        "v_exp_f32 v64, v64\n\t"
        "v_exp_f32 v65, v65\n\t"
        "v_mfma_f32_32x32x16_bf16 v[80:95], v[156:159], v[216:219], v[80:95]\n\t"
        "v_add_u32 v163, 0x100, v163\n\t"
        "v_cvt_pk_f16_f32 v118, v124, v125\n\t"
        "v_rcp_f16_sdwa v103, v103 dst_sel:WORD_0 dst_unused:UNUSED_PRESERVE src0_sel:WORD_0\n\t"
        "v_pk_mul_f16 v118, v118, v102\n\t"
        "v_exp_f32 v66, v66\n\t"
        "v_cvt_pk_f16_f32 v64, v64, v65\n\t"
        "s_branch .Lrt_%=\n\t"
        ".Lrl_%=:\n\t"
        "v_mfma_f32_32x32x16_f16 v[0:15], v[64:67], v[176:179], v[0:15]\n\t"
        "ds_read_b64_tr_b16 v[184:185], v175\n\t"
        "ds_read_b64_tr_b16 v[186:187], v175 offset:512\n\t"
        "ds_read_b64_tr_b16 v[188:189], v175 offset:1024\n\t"
        "ds_read_b64_tr_b16 v[190:191], v175 offset:1536\n\t"
        "v_rcp_f16_sdwa v71, v71 dst_sel:WORD_1 dst_unused:UNUSED_PRESERVE src0_sel:WORD_1\n\t"
        "v_cvt_pk_f16_f32 v87, v94, v95\n\t"
        "v_pk_add_f16 v96, v96, s96\n\t"
        "v_exp_f32 v99, v99\n\t"
        "v_rcp_f16_sdwa v96, v96 dst_sel:WORD_0 dst_unused:UNUSED_PRESERVE src0_sel:WORD_0\n\t"
        "v_mfma_f32_32x32x16_f16 v[32:47], v[80:83], v[176:179], v[32:47]\n\t"
        "ds_read_b64_tr_b16 v[192:193], v175 offset:2048\n\t"
        "ds_read_b64_tr_b16 v[194:195], v175 offset:2560\n\t"
        "ds_read_b64_tr_b16 v[196:197], v175 offset:3072\n\t"
        "ds_read_b64_tr_b16 v[198:199], v175 offset:3584\n\t"
        "v_pk_mul_f16 v87, v87, v71\n\t"
        "v_cvt_pk_f16_f32 v97, v98, v99\n\t"
        "v_rcp_f16_sdwa v96, v96 dst_sel:WORD_1 dst_unused:UNUSED_PRESERVE src0_sel:WORD_1\n\t"
        "v_pk_add_f16 v97, v97, s96\n\t"
        "v_exp_f32 v100, v100\n\t"
        "v_mfma_f32_32x32x16_f16 v[0:15], v[68:71], v[180:183], v[0:15]\n\t"
        "ds_read_b64_tr_b16 v[200:201], v175 offset:32\n\t"
        "ds_read_b64_tr_b16 v[202:203], v175 offset:544\n\t"
        "ds_read_b64_tr_b16 v[204:205], v175 offset:1056\n\t"
        "ds_read_b64_tr_b16 v[206:207], v175 offset:1568\n\t"
        "s_waitcnt lgkmcnt(14)\n\t"
        "v_lshlrev_b32 v216, 16, v144\n\t"
        "v_and_b32 v217, 0xffff0000, v144\n\t"
        "v_cvt_pk_f16_f32 v112, v112, v113\n\t"
        "v_rcp_f16_sdwa v97, v97 dst_sel:WORD_0 dst_unused:UNUSED_PRESERVE src0_sel:WORD_0\n\t"
        "v_pk_mul_f16 v112, v112, v96\n\t"
        "v_exp_f32 v101, v101\n\t"
        "v_mfma_f32_32x32x16_f16 v[32:47], v[84:87], v[180:183], v[32:47]\n\t"
        "ds_read_b64_tr_b16 v[208:209], v175 offset:2080\n\t"
        "ds_read_b64_tr_b16 v[210:211], v175 offset:2592\n\t"
        "ds_read_b64_tr_b16 v[212:213], v175 offset:3104\n\t"
        "ds_read_b64_tr_b16 v[214:215], v175 offset:3616\n\t"
        "v_lshlrev_b32 v218, 16, v145\n\t"
        "v_and_b32 v219, 0xffff0000, v145\n\t"
        "v_cvt_pk_f16_f32 v98, v100, v101\n\t"
        "v_rcp_f16_sdwa v97, v97 dst_sel:WORD_1 dst_unused:UNUSED_PRESERVE src0_sel:WORD_1\n\t"
        "v_pk_add_f16 v98, v98, s96\n\t"
        "v_exp_f32 v102, v102\n\t"
        "v_lshlrev_b32 v220, 16, v146\n\t"
        "v_and_b32 v221, 0xffff0000, v146\n\t"
        "v_lshlrev_b32 v222, 16, v147\n\t"
        "v_and_b32 v223, 0xffff0000, v147\n\t"
        "s_waitcnt lgkmcnt(14)\n\t"
        "v_lshlrev_b32 v224, 16, v148\n\t"
        "v_and_b32 v225, 0xffff0000, v148\n\t"
        "v_cvt_pk_f16_f32 v113, v114, v115\n\t"
        "v_rcp_f16_sdwa v98, v98 dst_sel:WORD_0 dst_unused:UNUSED_PRESERVE src0_sel:WORD_0\n\t"
        "v_pk_mul_f16 v113, v113, v97\n\t"
        "v_exp_f32 v103, v103\n\t"
        "v_lshlrev_b32 v226, 16, v149\n\t"
        "v_and_b32 v227, 0xffff0000, v149\n\t"
        "v_lshlrev_b32 v228, 16, v150\n\t"
        "v_and_b32 v229, 0xffff0000, v150\n\t"
        "v_lshlrev_b32 v230, 16, v151\n\t"
        "v_and_b32 v231, 0xffff0000, v151\n\t"
        "v_cvt_pk_f16_f32 v99, v102, v103\n\t"
        "v_rcp_f16_sdwa v98, v98 dst_sel:WORD_1 dst_unused:UNUSED_PRESERVE src0_sel:WORD_1\n\t"
        "v_pk_add_f16 v99, v99, s96\n\t"
        "v_exp_f32 v104, v104\n\t"
        "s_waitcnt lgkmcnt(14)\n\t"
        "v_mfma_f32_32x32x16_bf16 v[64:79], v[184:187], v[144:147], v[216:231]\n\t"
        "v_lshlrev_b32 v232, 16, v152\n\t"
        "v_and_b32 v233, 0xffff0000, v152\n\t"
        "v_cvt_pk_f16_f32 v114, v116, v117\n\t"
        "v_rcp_f16_sdwa v99, v99 dst_sel:WORD_0 dst_unused:UNUSED_PRESERVE src0_sel:WORD_0\n\t"
        "v_pk_mul_f16 v114, v114, v98\n\t"
        "v_exp_f32 v105, v105\n\t"
        "s_waitcnt lgkmcnt(12)\n\t"
        "v_mfma_f32_32x32x16_bf16 v[64:79], v[188:191], v[148:151], v[64:79]\n\t"
        "v_lshlrev_b32 v234, 16, v153\n\t"
        "v_and_b32 v235, 0xffff0000, v153\n\t"
        "v_cvt_pk_f16_f32 v100, v104, v105\n\t"
        "v_rcp_f16_sdwa v99, v99 dst_sel:WORD_1 dst_unused:UNUSED_PRESERVE src0_sel:WORD_1\n\t"
        "v_pk_add_f16 v100, v100, s96\n\t"
        "v_exp_f32 v106, v106\n\t"
        "s_waitcnt lgkmcnt(10)\n\t"
        "v_mfma_f32_32x32x16_bf16 v[64:79], v[192:195], v[152:155], v[64:79]\n\t"
        "v_lshlrev_b32 v236, 16, v154\n\t"
        "v_and_b32 v237, 0xffff0000, v154\n\t"
        "v_cvt_pk_f16_f32 v115, v118, v119\n\t"
        "v_rcp_f16_sdwa v100, v100 dst_sel:WORD_0 dst_unused:UNUSED_PRESERVE src0_sel:WORD_0\n\t"
        "v_pk_mul_f16 v115, v115, v99\n\t"
        "v_exp_f32 v107, v107\n\t"
        "s_waitcnt lgkmcnt(8)\n\t"
        "v_mfma_f32_32x32x16_bf16 v[64:79], v[196:199], v[156:159], v[64:79]\n\t"
        "v_lshlrev_b32 v238, 16, v155\n\t"
        "v_and_b32 v239, 0xffff0000, v155\n\t"
        "v_cvt_pk_f16_f32 v101, v106, v107\n\t"
        "v_rcp_f16_sdwa v100, v100 dst_sel:WORD_1 dst_unused:UNUSED_PRESERVE src0_sel:WORD_1\n\t"
        "v_pk_add_f16 v101, v101, s96\n\t"
        "v_exp_f32 v108, v108\n\t"
        "v_lshlrev_b32 v240, 16, v156\n\t"
        "v_and_b32 v241, 0xffff0000, v156\n\t"
        "v_lshlrev_b32 v242, 16, v157\n\t"
        "v_and_b32 v243, 0xffff0000, v157\n\t"
        "v_lshlrev_b32 v244, 16, v158\n\t"
        "v_and_b32 v245, 0xffff0000, v158\n\t"
        "v_lshlrev_b32 v246, 16, v159\n\t"
        "v_and_b32 v247, 0xffff0000, v159\n\t"
        "v_cvt_pk_f16_f32 v116, v120, v121\n\t"
        "v_rcp_f16_sdwa v101, v101 dst_sel:WORD_0 dst_unused:UNUSED_PRESERVE src0_sel:WORD_0\n\t"
        "v_pk_mul_f16 v116, v116, v100\n\t"
        "v_exp_f32 v109, v109\n\t"
        "s_waitcnt lgkmcnt(6)\n\t"
        "v_mfma_f32_32x32x16_bf16 v[80:95], v[200:203], v[144:147], v[232:247]\n\t"
        "v_cvt_pk_f16_f32 v102, v108, v109\n\t"
        "v_rcp_f16_sdwa v101, v101 dst_sel:WORD_1 dst_unused:UNUSED_PRESERVE src0_sel:WORD_1\n\t"
        "v_pk_add_f16 v102, v102, s96\n\t"
        "v_exp_f32 v110, v110\n\t"
        "s_waitcnt lgkmcnt(4)\n\t"
        "v_mfma_f32_32x32x16_bf16 v[80:95], v[204:207], v[148:151], v[80:95]\n\t"
        "v_cvt_pk_f16_f32 v117, v122, v123\n\t"
        "v_rcp_f16_sdwa v102, v102 dst_sel:WORD_0 dst_unused:UNUSED_PRESERVE src0_sel:WORD_0\n\t"
        "v_pk_mul_f16 v117, v117, v101\n\t"
        "v_exp_f32 v111, v111\n\t"
        "s_waitcnt lgkmcnt(2)\n\t"
        "v_mfma_f32_32x32x16_bf16 v[80:95], v[208:211], v[152:155], v[80:95]\n\t"
        "v_cvt_pk_f16_f32 v103, v110, v111\n\t"
        "v_rcp_f16_sdwa v102, v102 dst_sel:WORD_1 dst_unused:UNUSED_PRESERVE src0_sel:WORD_1\n\t"
        "v_pk_add_f16 v103, v103, s96\n\t"
        "s_waitcnt lgkmcnt(0)\n\t"
        "v_mfma_f32_32x32x16_bf16 v[80:95], v[212:215], v[156:159], v[80:95]\n\t"
        "v_cvt_pk_f16_f32 v118, v124, v125\n\t"
        "v_rcp_f16_sdwa v103, v103 dst_sel:WORD_0 dst_unused:UNUSED_PRESERVE src0_sel:WORD_0\n\t"
        "v_pk_mul_f16 v118, v118, v102\n\t"
        "v_mfma_f32_32x32x16_f16 v[16:31], v[96:99], v[176:179], v[16:31]\n\t"
        "v_rcp_f16_sdwa v103, v103 dst_sel:WORD_1 dst_unused:UNUSED_PRESERVE src0_sel:WORD_1\n\t"
        "v_cvt_pk_f16_f32 v119, v126, v127\n\t"
        "v_mfma_f32_32x32x16_f16 v[48:63], v[112:115], v[176:179], v[48:63]\n\t"
        "v_pk_mul_f16 v119, v119, v103\n\t"
        "v_mfma_f32_32x32x16_f16 v[16:31], v[100:103], v[180:183], v[16:31]\n\t"
        "s_nop 0\n\t"
        "v_mfma_f32_32x32x16_f16 v[48:63], v[116:119], v[180:183], v[48:63]\n\t"
        : "+{v[0:15]}"(Y[0]), "+{v[16:31]}"(Y[1]), "+{v[32:47]}"(Y[2]), "+{v[48:63]}"(Y[3]), "={v[64:79]}"(U[0]),
          "={v[80:95]}"(U[1]), "={v[216:231]}"(X[0]), "={v[232:247]}"(X[1]), "+{v[160:175]}"(AD), [nt] "+s"(nt)
          , [t1] "=&s"(tm1), [c1] "=&s"(c1)
        : "{v[128:143]}"(XAf), [x] "s"(x), [es] "s"(ES), [ring] "s"(ring0), [wave] "s"(wave), [flag] "s"(mode),
          [cu] "n"(cu)
        :
          "memory", "scc", "m0", "s94", "s95", "s96", "v96", "v97", "v98", "v99", "v100", "v101", "v102", "v103",
          "v104", "v105", "v106", "v107", "v108", "v109", "v110", "v111", "v112", "v113", "v114", "v115", "v116",
          "v117", "v118", "v119", "v120", "v121", "v122", "v123", "v124", "v125", "v126", "v127", "v144", "v145",
          "v146", "v147", "v148", "v149", "v150", "v151", "v152", "v153", "v154", "v155", "v156", "v157", "v158",
          "v159", "v176", "v177", "v178", "v179", "v180", "v181", "v182", "v183", "v184", "v185", "v186", "v187",
          "v188", "v189", "v190", "v191", "v192", "v193", "v194", "v195", "v196", "v197", "v198", "v199", "v200",
          "v201", "v202", "v203", "v204", "v205", "v206", "v207", "v208", "v209", "v210", "v211", "v212", "v213",
          "v214", "v215", "v248", "v249", "v250", "v251", "v252", "v253", "v254", "v255");
  }
  SB();
  const long long tm2 = __builtin_amdgcn_s_memrealtime(); const long long c2 = __builtin_amdgcn_s_memtime();
  auto wide = [&]() __attribute__((always_inline)) {
    int l2;
    asm volatile("v_mbcnt_lo_u32_b32 %0, -1, 0\n\tv_mbcnt_hi_u32_b32 %0, -1, %0\n\ts_waitcnt vmcnt(0)" : "=v"(l2) :: "memory");
    AD[0] = la + (4 * (l2 >> 5) + ((l2 >> 2) & 3)) * 16;
    AD[1] = la + (((l2 & 7) << 2) | ((l2 >> 3) & 3)) * 4 + 2;
    AD[2] = la + (l2 >> 3) * 16;
    AD[3] = la + LARR + ((((l2 >> 1) & 7) << 2) | (l2 >> 4)) * 4;
    auto rd = [&](unsigned a) { return *(const LAS bf16x8*)(size_t)a; };
    auto one = [&](bf16x8 v) {
      const u32x4 t = __builtin_bit_cast(u32x4, v);
      return __builtin_bit_cast(bf16x8, t | (t >> 3));
    };
    bf16x8 xq[4];
    {
      f32x16 T[2] = {zero16, zero16};
#pragma unroll
      for (int k = 0; k < 4; ++k) {
        u32x4 t;
#pragma unroll
        for (int j = 0; j < 4; ++j) t[j] = pack_bf16(X[k >> 1][8 * (k & 1) + 2 * j], X[k >> 1][8 * (k & 1) + 2 * j + 1]);
#pragma unroll
        for (int ct = 0; ct < 2; ++ct)
          T[ct] = MFMA32(__builtin_bit_cast(bf16x8, t), rawfrag(AD[15] + (NBLOB - 18 + k) * 1024 + ct * 32), T[ct]);
      }
#pragma unroll
      for (int q = 0; q < 4; ++q) {
        u32x4 t;
#pragma unroll
        for (int j = 0; j < 4; ++j)
          t[j] = pack_bf16(T[q & 1][8 * (q >> 1) + 2 * j] * NLOG2E, T[q & 1][8 * (q >> 1) + 2 * j + 1] * NLOG2E);
        xq[q] = __builtin_bit_cast(bf16x8, t);
      }
    }
#pragma unroll
    for (int i = 0; i < 16; ++i) { Y[0][i] = 1e-6f * (1 << RA); Y[1][i] = 1e-6f * (1 << RA); Y[2][i] = 0.f; Y[3][i] = 0.f; }
#pragma unroll 1
    do {
      const int n = slow ? gen() : cnt;
      issue_gather(0, 0);
#pragma unroll 1
      for (int t = 0; t * 32 < n; ++t) {
        const unsigned ro = (t & 1) * ringd;
        asm volatile("s_waitcnt lgkmcnt(0)" ::: "memory");
        issue_gather(t + 1, ~t & 1);
        const u32x4 ss = ld128(AD[0] + t * 128);
        const unsigned st = *(const LAS unsigned short*)(size_t)(AD[1] + t * 128) * TPITCH + AD[8];
        s16x4 a[4];
#pragma unroll
        for (int q = 0; q < 4; ++q)
          a[q] = __builtin_amdgcn_ds_read_tr16_b64_v4i16((LAS s16x4*)(size_t)((ss[q] >> 16) * TPITCH + AD[7]));
        const bf16x8 S0 = one(__builtin_shufflevector(a[0], a[1], 0, 1, 2, 3, 4, 5, 6, 7));
        const bf16x8 S1 = one(__builtin_shufflevector(a[2], a[3], 0, 1, 2, 3, 4, 5, 6, 7));
        const bf16x8 T0 = one(rd(st)), T1 = one(rd(st + 16));
        asm volatile("s_waitcnt vmcnt(5)" ::: "memory");
#pragma unroll
        for (int ct = 0; ct < 2; ++ct) {
          const unsigned w = AD[14] + OFF_W + ct * 4096;
          f32x16 G = MFMA32(T0, xq[ct], zero16), V = zero16;
          G = MFMA32(T1, xq[2 + ct], G);
          G = MFMA32(rd(AD[13] + ro), rd(AD[14] + OFF_W + (16 + ct) * 1024), G);
#pragma unroll
          for (int k = 0; k < 4; ++k) {
            const bf16x8 as = rd(AD[9 + k] + ro);
            G = MFMA32(as, rd(w + k * 1024), G);
            V = MFMA32(as, rd(w + (8 + k) * 1024), V);
          }
          u32x4 pd[2], pn[2];
#pragma unroll
          for (int i = 0; i < 16; i += 2) {
            constexpr float u = 1.0f / (1 << RA);
            const float r0 = __builtin_amdgcn_rcpf(__builtin_fmaf(__builtin_amdgcn_exp2f(G[i]), u, u));
            const float r1 = __builtin_amdgcn_rcpf(__builtin_fmaf(__builtin_amdgcn_exp2f(G[i + 1]), u, u));
            pd[i >> 3][(i >> 1) & 3] = pack_bf16(r0, r1);
            pn[i >> 3][(i >> 1) & 3] = pack_bf16(r0 * V[i], r1 * V[i + 1]);
          }
          Y[ct] = MFMA32(__builtin_bit_cast(bf16x8, pd[0]), S0, Y[ct]);
          Y[ct] = MFMA32(__builtin_bit_cast(bf16x8, pd[1]), S1, Y[ct]);
          Y[2 + ct] = MFMA32(__builtin_bit_cast(bf16x8, pn[0]), S0, Y[2 + ct]);
          Y[2 + ct] = MFMA32(__builtin_bit_cast(bf16x8, pn[1]), S1, Y[2 + ct]);
          SB();
        }
      }
      asm volatile("s_waitcnt vmcnt(0) lgkmcnt(0)" ::: "memory");
    } while (slow && gs < GEND);
  };

  const unsigned ot = ring0 + (max(cnt - 1, 0) & 32 ? 0u : ringd);
  auto finish = [&](const bool check) __attribute__((always_inline)) {
    auto inv = [&](f32x16& y) {
      y *= VSC;
#pragma unroll
      for (int i = 0; i < 16; ++i) y[i] = __builtin_amdgcn_rcpf(y[i]);
    };
    inv(Y[0]);
    SB();
    int lane;
    asm volatile("v_mbcnt_lo_u32_b32 %0, -1, 0\n\tv_mbcnt_hi_u32_b32 %0, -1, %0" : "=v"(lane));
    const int h = lane >> 5;
    const unsigned nd = nodeof(cg + (lane & 31));
    const bool live = (nd * Mw + Cw) >> 16 == cg + (lane & 31);
    const unsigned lm = (unsigned)__ballot(live);
    const int nodes = __builtin_popcount(lm);
    const unsigned n0 = __builtin_amdgcn_readlane(nd, __builtin_ctz(lm | 0x80000000u));
    const int r = live ? nd - n0 : 31;
    SB();
    inv(Y[1]);
    unsigned ov[16];
#pragma unroll
    for (int p = 0; p < 4; ++p) {
      const int ct = p >> 1;
#pragma unroll
      for (int j = 0; j < 4; ++j) {
        const int i = 8 * (p & 1) + 2 * j;
        const f32x2 z = __builtin_elementwise_fma(f32x2{Y[2 + ct][i], Y[2 + ct][i + 1]}, f32x2{Y[ct][i], Y[ct][i + 1]},
                                                  f32x2{U[ct][i], U[ct][i + 1]});
        ov[4 * p + j] = pack_bf16(MAXN(z[0], X[ct][i]), MAXN(z[1], X[ct][i + 1]));
      }
      *(LAS u32x4*)(size_t)(ot + r * 128 + (((4 * h + p) ^ (r & 7)) * 16)) =
          u32x4{ov[4 * p], ov[4 * p + 1], ov[4 * p + 2], ov[4 * p + 3]};
    }
    u32x4 ow[4];
#pragma unroll
    for (int q = 0; q < 4; ++q)
      ow[q] = *(const LAS u32x4*)(size_t)(ot + (8 * q + (lane >> 3)) * 128 + (((lane & 7) ^ (lane >> 3)) * 16));
    asm volatile("v_cmpx_lt_i32 vcc, 0, %6\n\t"
                 "global_store_dwordx4 %0, %1, %5 sc1\n\t"
                 "v_cmpx_lt_i32 vcc, 8, %6\n\t"
                 "global_store_dwordx4 %0, %2, %5 offset:1024 sc1\n\t"
                 "v_cmpx_lt_i32 vcc, 16, %6\n\t"
                 "global_store_dwordx4 %0, %3, %5 offset:2048 sc1\n\t"
                 "v_cmpx_lt_i32 vcc, 24, %6\n\t"
                 "global_store_dwordx4 %0, %4, %5 offset:3072 sc1\n\t"
                 "s_mov_b64 exec, -1"
                 :: "v"((unsigned)n0 * 128u + lane * 16u), "v"(ow[0]), "v"(ow[1]), "v"(ow[2]), "v"(ow[3]), "s"(out),
                    "v"(nodes - (lane >> 3))
                 : "memory", "vcc");
    SB();
    if (!check) return false;
    constexpr float WLIM = 1 << (22 - RA + VK);
    float wm = MAXN(Y[0][0], Y[1][0]);
#pragma unroll
    for (int i = 1; i < 16; ++i) wm = MAXN(MAXN(wm, Y[0][i]), Y[1][i]);
    f16x2 om = __builtin_bit_cast(f16x2, ov[0]);
#pragma unroll
    for (int i = 1; i < 16; ++i) om = MAXN(om, __builtin_bit_cast(f16x2, ov[i]));
    const _Float16 o1 = MAXN(om[0], om[1]);
    const unsigned long long lv = lm * 0x100000001ull;
    unsigned long long bad = __ballot(o1 != o1) | (__ballot(o1 < __builtin_bit_cast(_Float16, (unsigned short)((127 - 9 + VK) << 7))) & lv);
    if (__builtin_expect((__ballot(wm > WLIM / LCAP) & lv) != 0ull, 0)) {
      LAS unsigned* dg = (LAS unsigned*)(size_t)ot;
      dg[lane & 31] = 0u;
#pragma unroll 1
      for (int i = lane; i < cnt; i += 64)
        __hip_atomic_fetch_add(dg + (*(const LAS unsigned short*)(size_t)(la + i * 4 + 2) & 31), 1u, __ATOMIC_RELAXED,
                               __HIP_MEMORY_SCOPE_WORKGROUP);
      bad |= __ballot(wm * (float)dg[lane & 31] > WLIM) & lv;
    }
    return slow || bad != 0ull;
  };
  if (__builtin_expect(finish(true), 0)) {
    wide();
    finish(false);
  }
  if (bk == 0 && wave == 7) asm volatile("global_store_dwordx2 %0, %1, %2 sc1" :: "v"(0u), "v"(ep), "s"(FL) : "memory");
  asm volatile("s_waitcnt vmcnt(0)" ::: "memory");
  long long* dbg = (long long*)FL + (127u << 17);
  { unsigned* xd_ = (unsigned*)(dbg + (4u << 20)); const int l_ = __builtin_amdgcn_mbcnt_hi(-1, __builtin_amdgcn_mbcnt_lo(-1, 0)); if (l_ < 16) xd_[gbin * 16 + l_] = *(LAS unsigned*)((LAS char*)smem + MAIN_LDS + wave * 64 + l_ * 4); }
  const long long tm3 = __builtin_amdgcn_s_memrealtime();
  const long long tm0 = tm3 - (long long)(unsigned)((unsigned)tm3 - tm0s);
  if (__builtin_amdgcn_mbcnt_hi(-1, __builtin_amdgcn_mbcnt_lo(-1, 0)) == 0) { const int g = gbin; if (wave == 0) (dbg - (16 << 20))[5000 + bk] = tm0; dbg[g*8+0]=tm0; dbg[g*8+1]=tm1; dbg[g*8+2]=tm2; dbg[g*8+3]=tm3; dbg[g*8+4]=(cnt+31)>>5; dbg[g*8+5]=c2-c1; dbg[g*8+6]=tmA-tm0; dbg[g*8+7]=tmB-tm0;
  }
}

__global__ void k_dbg(const long long* dbg, int ng) {
  __shared__ long long sh[4][256];
  const int t = threadIdx.x;
  const unsigned* q = (const unsigned*)(dbg + (4u << 20));
  long long pmin = 1ll << 62, sum = 0, mx = 0, cn = 0;
  const int md = QSEL / 100;
  for (int g = t; g < ng; g += 256) {
    const long long* d = dbg + g * 8;
    pmin = min(pmin, d[0]);
    long long v = 0;
    const int k = QSEL % 100;
    if (k == 0) v = d[0];
    else if (k <= 12) v = d[0] + (int)(q[g * 16 + k] - (unsigned)d[0]);
    else if (k == 14) v = q[g * 16 + 14] * 1000;
    else if (k == 16) v = d[1];
    else if (k == 17) v = d[2];
    else if (k == 18) v = d[3];
    else if (k == 22) v = d[5] * 100 / (d[2] - d[1]);
    sum += v; mx = max(mx, v); ++cn;
  }
  sh[0][t] = pmin; sh[1][t] = sum; sh[2][t] = mx; sh[3][t] = cn;
  __syncthreads();
  if (t == 0) {
    for (int i = 1; i < 256; ++i) { pmin = min(pmin, sh[0][i]); sum += sh[1][i]; mx = max(mx, sh[2][i]); cn += sh[3][i]; }
    long long r = md == 1 ? mx : sum / cn;
    if (QSEL % 100 != 14 && QSEL % 100 != 22) r -= pmin;
    if (QSEL == 99) r = 0;
    r = max(0ll, min(r, 5000ll));
    const long long tend = pmin + 10000 + 4 * r;
    for (int i = 0; i < 1000000; ++i) { if (__builtin_amdgcn_s_memrealtime() >= tend) break; __builtin_amdgcn_s_sleep(1); }
  }
}

extern "C" void kernel_launch(void* const* d_in, const int* in_sizes, int n_in,
                              void* d_out, int out_size, void* d_ws, size_t ws_size,
                              hipStream_t stream) {
  const unsigned short* x = (const unsigned short*)d_in[0];
  const u32x4* eattr = (const u32x4*)d_in[1];
  const int* src = (const int*)d_in[2];
  const int* dst = (const int*)d_in[3];
  const unsigned short* wa = (const unsigned short*)d_in[4];
  const unsigned short* wb = (const unsigned short*)d_in[5];
  const unsigned short* wc = (const unsigned short*)d_in[6];
  const unsigned short* wu = (const unsigned short*)d_in[7];
  const unsigned short* wv = (const unsigned short*)d_in[8];
  unsigned short* out = (unsigned short*)d_out;
  const int N = in_sizes[0] / 64;
  const int E = in_sizes[2];
  const int chunk = (((E + NCH - 1) / NCH) + 3) & ~3;

  const bool skew = N > 4096 && N < 60000;
  const unsigned M = N > 256 ? (unsigned)(4294967295.0 / ((double)N * (skew ? 1.0 + SKEWF : 1.0))) : 0xffffffu;
  const unsigned N1 = (0x7fffffffu / M) + 1u;
  const unsigned M2 = skew ? (0xffffffffu - N1 * M) / ((unsigned)N - N1) : M;

  auto fb = [](double v) { return __builtin_bit_cast(unsigned, (float)v); };
  char* ws = (char*)d_ws;
  size_t off = 0;
  off = 1 << 20;
  unsigned long long* FL = (unsigned long long*)(ws + off);   off += 1 << 20;
  u32x4* ENT = (u32x4*)(ws + off);                       off += (size_t)2 * IEXT;
  unsigned long long* TS = (unsigned long long*)(ws + off);

  k_main<<<NBK, 512, 0, stream>>>(x, eattr, src, dst, wa, wb, wc, wu, wv, ENT, TS, FL, out, N, E, chunk, M, N1,
                                  M2 - M, fb(65536.0 / M), fb(65536.0 / M2), fb(32768.0 / M + 0.5),
                                  fb((double)N1 + (32768.0 - (double)N1 * M) / M2 + 0.5)
     );
  k_dbg<<<1, 256, 0, stream>>>((const long long*)(ws + (128u<<20)), 2048);
}
```

```cpp
#include <hip/hip_runtime.h>
#include <hip/hip_bf16.h>
#include <stdint.h>

using bf16x8 = __attribute__((ext_vector_type(8))) short;
using s16x4 = __attribute__((ext_vector_type(4))) short;
using f32x16 = __attribute__((ext_vector_type(16))) float;
using f32x2 = __attribute__((ext_vector_type(2))) float;
using u32x4 = __attribute__((ext_vector_type(4))) unsigned;
using u32x16 = __attribute__((ext_vector_type(16))) unsigned;
using u32x2 = __attribute__((ext_vector_type(2))) unsigned;
using f16x2 = __attribute__((ext_vector_type(2))) _Float16;

#define MFMA32(a, b, c) __builtin_amdgcn_mfma_f32_32x32x16_bf16((a), (b), (c), 0, 0, 0)
#define RA 14
#define VK 4
#define LAS __attribute__((address_space(3)))
#define RANKOF(w) ((w) < 4 ? 7 - (w) : (w) - 4)
#define G1W 4
#define CPL 5
#define PSL 12
#define SKEWF 0.015
#define XLIKE(c) __builtin_expect(c, 0)
#define SB() __builtin_amdgcn_sched_barrier(0)

constexpr int NCH = 256;
constexpr int NBK = 256;
constexpr int NFRAG = 39;
constexpr float NLOG2E = -1.44269504088896340736f;
constexpr float VSC = 1.0f / (1 << VK);
constexpr unsigned TPITCH = 80;

__device__ __forceinline__ unsigned pack_bf16(float lo, float hi) {
  typedef __attribute__((ext_vector_type(2))) float f2;
  typedef __attribute__((ext_vector_type(2))) __bf16 b2;
  f2 v = {lo, hi};
  b2 r = __builtin_convertvector(v, b2);
  return __builtin_bit_cast(unsigned, r);
}

__device__ __forceinline__ unsigned pack_f16(float lo, float hi) {
  unsigned r;
  asm("v_cvt_pk_f16_f32 %0, %1, %2" : "=v"(r) : "v"(lo), "v"(hi));
  return r;
}

__device__ __forceinline__ void glds16(const void* sbase, unsigned voff, unsigned lds_dst) {
  asm volatile(
      "s_mov_b32 m0, %2\n\t"
      "s_nop 0\n\t"
      "global_load_lds_dwordx4 %0, %1"
      :: "v"(voff), "s"(sbase), "s"(lds_dst) : "memory", "m0");
}
__device__ __forceinline__ void glds16x(const void* sbase, unsigned a, unsigned c, unsigned lds_dst) {
  unsigned tmp;
  asm volatile(
      "s_mov_b32 m0, %4\n\t"
      "v_mad_u32_u16 %0, %1, %5, %2\n\t"
      "global_load_lds_dwordx4 %0, %3"
      : "=&v"(tmp) : "v"(a), "v"(c), "s"(sbase), "s"(lds_dst), "s"(128u) : "memory", "m0");
}
__device__ __forceinline__ void glds16e(const void* sbase, unsigned a, unsigned c, unsigned lds_dst) {
  unsigned tmp;
  asm volatile(
      "s_mov_b32 m0, %4\n\t"
      "v_add_u32 %0, %1, %2\n\t"
      "global_load_lds_dwordx4 %0, %3"
      : "=&v"(tmp) : "v"(a), "v"(c), "s"(sbase), "s"(lds_dst) : "memory", "m0");
}
__device__ __forceinline__ void gstore128wt(void* sbase, unsigned voff, u32x4 v) {
  asm volatile("global_store_dwordx4 %0, %1, %2 sc1\n\ts_nop 1" :: "v"(voff), "v"(v), "s"(sbase) : "memory");
}

#define IBX 40960
constexpr int IBOX = IBX;
constexpr int IEXT = 1 << 24;
constexpr unsigned TS = 5u << 20;
constexpr int CPIT = 208;
constexpr unsigned RINC = 87382;
constexpr int KP_HDR = 0;
constexpr int KP_ENT = KP_HDR + NBK * 4;
constexpr int KP_POS = KP_ENT + NBK * CPIT;
constexpr int KP_LDS = KP_POS + NBK * 4;
constexpr int NWL = 26;
constexpr int NBLOB = 31;

__device__ __forceinline__ unsigned mul24s(unsigned a, unsigned b) {
  unsigned r;
  asm("v_mul_u32_u24 %0, %1, %2" : "=v"(r) : "s"(b), "v"(a));
  return r;
}
__device__ __forceinline__ unsigned byte3x(unsigned a, unsigned b) {
  unsigned r;
  asm("v_mul_u32_u24_sdwa %0, %1, %2 dst_sel:DWORD dst_unused:UNUSED_PAD src0_sel:DWORD src1_sel:BYTE_3" : "=v"(r) : "s"(b), "v"(a));
  return r;
}
constexpr int RING_SLOT = 5 * 1024;
constexpr int LCAP = 512;
constexpr int LARR = 2560;
constexpr int LIST_BYTES = 2 * LARR;
constexpr int OFF_CNT = 0;
constexpr int SUBL = LARR / 4;
constexpr int OFF_XF = 256;
constexpr int OFF_W = 512;
constexpr int OFF_T1 = OFF_W + NWL * 1024;
constexpr int OFF_T2 = OFF_T1 + 32 * TPITCH;
constexpr int OFF_R0 = OFF_W + NFRAG * 1024;
constexpr int OFF_R1 = OFF_R0 + 8 * RING_SLOT;
constexpr int OFF_L = OFF_R1 + 8 * RING_SLOT;
constexpr int MAIN_LDS = OFF_L + 8 * LIST_BYTES;
static_assert(MAIN_LDS <= 160 * 1024, "LDS");
static_assert(KP_LDS <= 16 * RING_SLOT, "phase 1 lives in the ring");

__device__ __forceinline__ unsigned ldd(const unsigned* p) {
  return __hip_atomic_load(p, __ATOMIC_RELAXED, __HIP_MEMORY_SCOPE_AGENT);
}
__device__ __forceinline__ unsigned long long ldq(const unsigned long long* p) {
  return __hip_atomic_load(p, __ATOMIC_RELAXED, __HIP_MEMORY_SCOPE_AGENT);
}
__device__ __forceinline__ bf16x8 rawfrag(unsigned a) {
  const s16x4 a0 = __builtin_amdgcn_ds_read_tr16_b64_v4i16((LAS s16x4*)(size_t)a);
  const s16x4 a1 = __builtin_amdgcn_ds_read_tr16_b64_v4i16((LAS s16x4*)(size_t)(a + 512));
  return __builtin_shufflevector(a0, a1, 0, 1, 2, 3, 4, 5, 6, 7);
}
#define MAXN(a, b) __builtin_elementwise_maximum(a, b)

__global__ __launch_bounds__(512, 2) void k_main(const int* __restrict__ dst, const int* __restrict__ src,
                                                 unsigned long long* FL, const unsigned short* __restrict__ x, int E,
                                                 unsigned M, unsigned N1, unsigned DM, int N, unsigned MF0, unsigned MF1,
                                                 unsigned MB1, const void* wa_, const void* wb_, const void* wv_,
                                                 const void* wc_, const void* wu_, const void* e_, void* out_) {
  SB();
  {
    unsigned f;
    asm volatile("s_lshr_b32 s96, %2, 3\n\t"
                 "v_add_u32 %0, -448, %1\n\t"
                 "s_mul_i32 s98, s96, %3\n\t"
                 "v_cmpx_gt_u32 vcc, %3, %0\n\t"
                 "s_getpc_b64 s[96:97]\n\t"
                 "s_mov_b32 m0, %4\n\t"
                 "s_cbranch_execz 1f\n\t"
                 "v_add_lshl_u32 %0, %0, s98, 7\n\t"
                 "global_load_lds_dword %0, s[96:97]\n"
                 "1:\n\t"
                 "s_mov_b64 exec, -1"
                 : "=&v"(f) : "v"(threadIdx.x), "s"(blockIdx.x), "n"(CPL), "n"(OFF_L + 7 * LIST_BYTES + 4096)
                 : "memory", "m0", "s96", "s97", "s98", "vcc", "scc");
  }
  SB();
  __shared__ __attribute__((aligned(16))) char smem[MAIN_LDS];
  const int tid0 = threadIdx.x;
  const int wave = __builtin_amdgcn_readfirstlane(tid0 >> 6);
  const int bk = blockIdx.x;
  const unsigned lds0 = (unsigned)(size_t)(LAS char*)smem;

  LAS char* sm = (LAS char*)smem + OFF_R0;
  const unsigned sm0 = lds0 + OFF_R0;
  const int tid = tid0, lane = tid & 63, r = lane & 31, h = lane >> 5;
  const unsigned pc = tid & 7, bq = tid >> 3;
  unsigned ia = sm0 + KP_ENT + bq * CPIT + pc * 16;
#pragma unroll
  for (int s = 0; s < 4; ++s) *(LAS u32x4*)(size_t)(ia + s * 64 * CPIT) = u32x4{~0u, ~0u, ~0u, ~0u};
  *(LAS unsigned*)(sm + KP_HDR + (tid & 255) * 4) = 0u;
  int q0 = 4 * tid + 3, q1 = 4 * tid + 1795;
  unsigned a0 = tid * 16, a1 = tid * 16 + 7168, z0 = 0u;
  unsigned long long m7 = __ballot(tid == 448);
  int n7 = wave == 7 ? 0 : 0x7fffffff;
  asm volatile("" : "+v"(ia), "+v"(q0), "+v"(q1), "+v"(a0), "+v"(a1), "+v"(z0), "+s"(m7), "+s"(n7) :: "memory");
  SB();
  asm volatile("" :: "s"(x), "s"(src), "s"(dst), "s"(FL), "s"(N), "s"(E), "s"(M), "s"(N1), "s"(DM), "s"(MF0), "s"(MF1), "s"(MB1));
  const int chunk = (((E + NCH - 1) >> 8) + 3) & ~3;
  const int base = bk * chunk;
  const int ecnt = max(min(chunk, E - base), 0);
  unsigned long long on[2];
  on[0] = __ballot(q0 < min(min(chunk, E - base), n7));
  on[1] = __ballot(q1 < min(min(chunk, E - base), n7));
  a0 += base * 4u;
  a1 += base * 4u;
  u32x4 d[2], sv[2];
  u32x2 e0;
  asm volatile("s_mov_b64 exec, %11\n\t"
               "global_load_dwordx4 %0, %5, %8 nt\n\t"
               "s_mov_b64 exec, %12\n\t"
               "global_load_dwordx4 %1, %6, %8 nt\n\t"
               "s_mov_b64 exec, %13\n\t"
               "global_load_dwordx2 %2, %7, %9 sc1\n\t"
               "s_mov_b64 exec, %11\n\t"
               "s_waitcnt lgkmcnt(0)\n\t"
               "s_barrier\n\t"
               "global_load_dwordx4 %3, %5, %10 nt\n\t"
               "s_mov_b64 exec, %12\n\t"
               "global_load_dwordx4 %4, %6, %10 nt\n\t"
               "s_mov_b64 exec, -1"
               : "=&{v[240:243]}"(d[0]), "=&{v[244:247]}"(d[1]), "=&{v[238:239]}"(e0), "=&{v[248:251]}"(sv[0]),
                 "=&{v[252:255]}"(sv[1])
               : "v"(a0), "v"(a1), "v"(z0), "s"(dst), "s"(FL), "s"(src), "s"(on[0]), "s"(on[1]), "s"(m7)
               : "memory");
  auto ka = (const __attribute__((address_space(4))) unsigned long long*)__builtin_amdgcn_kernarg_segment_ptr();
  asm volatile("" : "+s"(ka));
  const unsigned short* wu_g = (const unsigned short*)ka[12];
  const u32x4* ES = (const u32x4*)ka[13];
  unsigned short* out = (unsigned short*)ka[14];
  SB();
  u32x4* ENT = (u32x4*)(FL + (1 << 17));
  const int bu = (bk >> 1) | ((bk & 1) << 7);
  const unsigned Mw = bk & 1 ? M + DM : M, Cw = bk & 1 ? 0u - N1 * DM : 0u;
  unsigned MB0;
  asm("v_fma_f32 %0, %1, 0.5, 0.5" : "=v"(MB0) : "s"(MF0));
  MB0 = __builtin_amdgcn_readfirstlane(MB0);
  asm volatile("" : "+s"(MB0));
  unsigned MF = bk & 1 ? MF1 : MF0, MB = bk & 1 ? MB1 : MB0;
  asm volatile("" : "+s"(MF), "+s"(MB));
  char* ib = reinterpret_cast<char*>(ENT) + (size_t)bu * IBOX;
  auto nodeof = [&](unsigned c) { return min((unsigned)__builtin_fmaf((float)c, __uint_as_float(MF), __uint_as_float(MB)), (unsigned)N - 1u); };
  unsigned tup[8], val[8], pm[8];
  u32x4 xg[4];
  const unsigned xn0 = __builtin_amdgcn_readfirstlane(nodeof((bu * 8 + wave) * 32));
  unsigned xd[4];
  auto xprep = [&]() {
#pragma unroll
    for (int i = 0; i < 4; ++i)
      xd[i] = min(xn0 + 8 * i + (lane >> 3), N - 1u) * 128u + ((((lane & 7) ^ (lane >> 4)) * 16) ^ (i & 1) * 64) - i * 1024u;
    asm volatile("" : "+v"(xd[0]), "+v"(xd[1]), "+v"(xd[2]), "+v"(xd[3]));
  };
  auto xdma = [&]() {
    asm volatile("s_mov_b32 m0, %5\n\ts_nop 0\n\t"
                 "global_load_lds_dwordx4 %0, %4\n\t"
                 "global_load_lds_dwordx4 %1, %4 offset:1024\n\t"
                 "global_load_lds_dwordx4 %2, %4 offset:2048\n\t"
                 "global_load_lds_dwordx4 %3, %4 offset:3072"
                 :: "v"(xd[0]), "v"(xd[1]), "v"(xd[2]), "v"(xd[3]), "s"(x), "s"(lds0 + OFF_L + wave * LIST_BYTES)
                 : "memory", "m0");
  };
  auto wload = [&](const unsigned short* w, int f) {
    asm volatile("s_mov_b32 m0, %3\n\ts_nop 0\n\t"
                 "global_load_lds_dwordx4 %0, %2\n\t"
                 "global_load_lds_dwordx4 %0, %2 offset:1024\n\t"
                 "global_load_lds_dwordx4 %0, %2 offset:2048\n\t"
                 "global_load_lds_dwordx4 %0, %2 offset:3072\n\t"
                 "s_addk_i32 m0, 4096\n\ts_nop 0\n\t"
                 "global_load_lds_dwordx4 %1, %2\n\t"
                 "global_load_lds_dwordx4 %1, %2 offset:1024\n\t"
                 "global_load_lds_dwordx4 %1, %2 offset:2048\n\t"
                 "global_load_lds_dwordx4 %1, %2 offset:3072"
                 :: "v"(lane * 16u), "v"(lane * 16u + 4096u), "s"(w), "s"(lds0 + OFF_W + f * 1024) : "memory", "m0", "scc");
  };
  auto wall = [&]() {
    const unsigned short *wa_g = (const unsigned short*)ka[8], *wb_g = (const unsigned short*)ka[9],
                         *wv_g = (const unsigned short*)ka[10], *wc_g = (const unsigned short*)ka[11];
    wload(wa_g, NBLOB);
    wload(wb_g, 0);
    wload(wv_g, 8);
    glds16(wc_g, lane * 16, lds0 + OFF_W + 16 * 1024);
    glds16(wc_g + 512, lane * 16, lds0 + OFF_W + 17 * 1024);
  };
  int spin = 1 << 20;
  u32x2 ep = {0u, 0u};
  if (wave == 7) {
    wall();
    *(LAS u32x4*)((LAS char*)smem + OFF_CNT + r * 16) =
        (r & 17) == 0 ? u32x4{lds0 + OFF_L, lds0 + OFF_L + SUBL, lds0 + OFF_L + 2 * SUBL, lds0 + OFF_L + 3 * SUBL}
                      : u32x4{0u, 0u, 0u, 0u};
    {
      const unsigned col = h ? ((r >> 1) & 12u) | ((r << 2) & 16u) | (r & 3u) : r;
      LAS char* tr = (LAS char*)smem + OFF_T1 + lane * TPITCH;
#pragma unroll
      for (int j = 0; j < 4; ++j) *(LAS u32x4*)(tr + j * 16) = u32x4{0u, 0u, 0u, 0u};
      *(LAS unsigned short*)(tr + col * 2) = 0x3C00;
    }
#pragma unroll
    for (int i = 0; i < 8; ++i) { tup[i] = 0u; val[i] = 0u; pm[i] = 0u; }
    asm volatile("s_waitcnt vmcnt(%1)" : "+{v[238:239]}"(e0) : "n"(28) : "memory");
    ep[0] = __builtin_amdgcn_readfirstlane(e0[0]) + 0x9E3779B1u;
    ep[1] = __builtin_amdgcn_readfirstlane(e0[1]) + 0x7F4A7C15u;
    *(LAS u32x2*)((LAS char*)smem + OFF_CNT + 56) = u32x2{ep[0], ep[1] >> 24};
    if (__builtin_expect(ecnt & 3, 0)) {
      const int i = (ecnt & ~3) + lane;
      on[0] = __ballot(i < ecnt);
      if (i < ecnt) {
        pm[0] = (unsigned)dst[base + i] * M + (unsigned)max(dst[base + i] - (int)N1, 0) * DM;
        tup[0] = (unsigned)src[base + i] | (unsigned)i << 16;
        val[0] = __hip_atomic_fetch_add((LAS unsigned*)(sm + KP_HDR) + (pm[0] >> 24), RINC, __ATOMIC_RELAXED, __HIP_MEMORY_SCOPE_WORKGROUP);
        const unsigned u = min(val[0] >> 16, 48u);
        *(LAS unsigned*)(sm + KP_ENT + (pm[0] >> 24) * CPIT + u * 4) = tup[0];
        *(LAS unsigned char*)(sm + KP_ENT + (pm[0] >> 24) * CPIT + ((u * 4) | (u & 3) | 12)) = pm[0] >> 16;
      }
      __builtin_amdgcn_s_waitcnt(0x0F70);
    }
    xprep();
    xdma();
  } else {
    xprep();
    unsigned inc = RINC, t0 = (unsigned)tid << 18;
    asm volatile("s_waitcnt vmcnt(2)" : "+{v[240:243]}"(d[0]), "+{v[244:247]}"(d[1]), "+v"(inc), "+v"(t0) :: "memory");
    unsigned cb[8];
#pragma unroll
    for (int s = 0; s < 2; ++s) {
      unsigned* rr = val + 4 * s;
#pragma unroll
      for (int k = 0; k < 4; ++k) {
        unsigned t;
        asm("v_sub_u32 %0, %1, %2 clamp" : "=v"(t) : "v"(d[s][k]), "s"(N1));
        asm("v_mad_u32_u24 %0, %1, %2, %3" : "=v"(pm[4 * s + k]) : "v"(t), "s"(DM), "v"(mul24s(d[s][k], M)));
      }
#pragma unroll
      for (int k = 0; k < 4; ++k) rr[k] = byte3x(pm[4 * s + k], 4u);
      asm volatile(
          "s_mov_b64 exec, %5\n\t"
          "ds_add_rtn_u32 %0, %0, %4 offset:%6\n\t"
          "ds_add_rtn_u32 %1, %1, %4 offset:%6\n\t"
          "ds_add_rtn_u32 %2, %2, %4 offset:%6\n\t"
          "ds_add_rtn_u32 %3, %3, %4 offset:%6\n\t"
          "s_mov_b64 exec, -1"
          : "+v"(rr[0]), "+v"(rr[1]), "+v"(rr[2]), "+v"(rr[3])
          : "v"(inc), "s"(on[s]), "n"(OFF_R0 + KP_HDR)
          : "memory");
    }
#pragma unroll
    for (int i = 0; i < 8; ++i) cb[i] = byte3x(pm[i], (unsigned)CPIT);
    asm volatile("s_waitcnt vmcnt(0)"
                 : "+{v[248:251]}"(sv[0]), "+{v[252:255]}"(sv[1]), "+v"(cb[0]), "+v"(cb[1]), "+v"(cb[2]), "+v"(cb[3]), "+v"(cb[4]), "+v"(cb[5]),
                   "+v"(cb[6]), "+v"(cb[7])
                 :: "memory");
    xdma();
#pragma unroll
    for (int i = 0; i < 8; ++i) tup[i] = sv[i >> 2][i & 3] + t0 + (((i >> 2) * 1792u + (i & 3)) << 16);
#pragma unroll
    for (int s = 0; s < 2; ++s) {
      asm volatile("s_waitcnt lgkmcnt(%8)"
                   : "+v"(val[4 * s]), "+v"(val[4 * s + 1]), "+v"(val[4 * s + 2]), "+v"(val[4 * s + 3]), "+v"(tup[4 * s]),
                     "+v"(tup[4 * s + 1]), "+v"(tup[4 * s + 2]), "+v"(tup[4 * s + 3])
                   : "n"(s ? 8 : 4) : "memory");
      unsigned pa[4], pb[4];
#pragma unroll
      for (int k = 0; k < 4; ++k) {
        const unsigned u = min(val[4 * s + k], 48u << 16);
        asm("v_mad_u32_u16 %0, %1, 4, %2 op_sel:[1,0,0,0]" : "=v"(pa[k]) : "v"(u), "v"(cb[4 * s + k]));
        pb[k] = pa[k] | ((u >> 16) & 3u) | 12u;
      }
      asm volatile(
          "s_mov_b64 exec, %16\n\t"
          "ds_write_b32 %0, %8 offset:%17\n\t"
          "ds_write_b8_d16_hi %4, %12 offset:%17\n\t"
          "ds_write_b32 %1, %9 offset:%17\n\t"
          "ds_write_b8_d16_hi %5, %13 offset:%17\n\t"
          "ds_write_b32 %2, %10 offset:%17\n\t"
          "ds_write_b8_d16_hi %6, %14 offset:%17\n\t"
          "ds_write_b32 %3, %11 offset:%17\n\t"
          "ds_write_b8_d16_hi %7, %15 offset:%17\n\t"
          "s_mov_b64 exec, -1"
          :: "v"(pa[0]), "v"(pa[1]), "v"(pa[2]), "v"(pa[3]), "v"(pb[0]), "v"(pb[1]), "v"(pb[2]), "v"(pb[3]),
             "v"(tup[4 * s]), "v"(tup[4 * s + 1]), "v"(tup[4 * s + 2]), "v"(tup[4 * s + 3]), "v"(pm[4 * s]),
             "v"(pm[4 * s + 1]), "v"(pm[4 * s + 2]), "v"(pm[4 * s + 3]), "s"(on[s]), "n"(OFF_R0 + KP_ENT)
          : "memory");
    }
  }
  u32x2 tag;
  int xw = 0;
  {
    unsigned ha = sm0 + KP_HDR + bq * 4, hb = sm0 + KP_HDR + lane * 16;
    unsigned go = bq * IBOX + bk * 128 + pc * 16;
    asm volatile("s_waitcnt lgkmcnt(0)\n\ts_barrier" : "+v"(ha), "+v"(hb), "+v"(go) : "s"(wu_g), "s"(ES), "s"(out) : "memory");
    tag = *(const LAS u32x2*)((LAS char*)smem + OFF_CNT + 56);
    const u32x4 call = *(const LAS u32x4*)(size_t)hb;
    unsigned cn[4];
    u32x4 v[4];
#pragma unroll
    for (int s = 0; s < 4; ++s) {
      cn[s] = *(const LAS unsigned*)(size_t)(ha + s * 256);
      v[s] = *(const LAS u32x4*)(size_t)(ia + s * 64 * CPIT);
    }
    asm volatile("" : "+v"(v[0]), "+v"(v[1]), "+v"(v[2]), "+v"(v[3]), "+v"(tag));
    if (__builtin_expect(__ballot(max(max(call[0], call[1]), max(call[2], call[3])) > 36u * RINC) != 0ull, 0)) {
      LAS unsigned* ovc = (LAS unsigned*)((LAS char*)smem + OFF_CNT + 24);
      LAS unsigned* pos = (LAS unsigned*)(sm + KP_POS);
      if (tid < NBK) {
        const unsigned u = *(const LAS unsigned*)(sm + KP_HDR + tid * 4) >> 16, n = u - (u >> 2);
        if (n > 36u) {
          const unsigned p = (unsigned)base + __hip_atomic_fetch_add(ovc, n - 36u, __ATOMIC_RELAXED, __HIP_MEMORY_SCOPE_WORKGROUP);
          pos[tid] = p;
          __hip_atomic_store(FL + 512 + tid * NCH + bk, ((unsigned long long)(n - 36u) << 32) | p, __ATOMIC_RELAXED,
                             __HIP_MEMORY_SCOPE_AGENT);
        }
      }
      asm volatile("s_waitcnt lgkmcnt(0)\n\ts_barrier" ::: "memory");
#pragma unroll
      for (int i = 0; i < 8; ++i) {
        const unsigned u = val[i] >> 16, n = u - (u >> 2);
        if (((on[i >> 2] >> lane) & 1ull) && n >= 36u)
          __hip_atomic_store(FL + (TS + pos[pm[i] >> 24] + (n - 36u)), ((unsigned long long)((pm[i] >> 16) & 0xffu) << 32) | tup[i],
                             __ATOMIC_RELAXED, __HIP_MEMORY_SCOPE_AGENT);
      }
      __builtin_amdgcn_s_waitcnt(0x0F70);
      asm volatile("s_waitcnt vmcnt(0)\n\ts_barrier" ::: "memory");
    }
    auto seal = [&](u32x4& w, bool more) {
      w[3] = __builtin_amdgcn_perm(tag[1], w[3], 0x04020100u);
      unsigned sm_ = w[0] + w[1] + w[2] + w[3] + tag[0];
      sm_ += __builtin_amdgcn_update_dpp(0u, sm_, 0xB1, 0xf, 0xf, false);
      sm_ += __builtin_amdgcn_update_dpp(0u, sm_, 0x4E, 0xf, 0xf, false);
      sm_ += __builtin_amdgcn_update_dpp(0u, sm_, 0x141, 0xf, 0xf, false);
      w[2] = pc == 7u ? w[2] - sm_ + (more ? 1u : 0u) : w[2];
    };
#pragma unroll
    for (int s = 0; s < 4; ++s) seal(v[s], cn[s] > 23u * RINC);
    asm volatile("global_store_dwordx4 %0, %1, %5 sc1\n\t"
                 "global_store_dwordx4 %0, %2, %6 sc1\n\t"
                 "global_store_dwordx4 %0, %3, %7 sc1\n\t"
                 "global_store_dwordx4 %0, %4, %8 sc1\n\t"
                 "s_nop 1"
                 :: "v"(go), "v"(v[0]), "v"(v[1]), "v"(v[2]), "v"(v[3]), "s"(ENT), "s"(ENT + 4 * IBOX), "s"(ENT + 8 * IBOX),
                    "s"(ENT + 12 * IBOX)
                 : "memory");
    if (XLIKE(__ballot(max(max(cn[0], cn[1]), max(cn[2], cn[3])) > 23u * RINC) != 0ull)) {
      unsigned xm = 0u;
#pragma unroll
      for (int s = 0; s < 4; ++s) xm |= cn[s] > 23u * RINC ? 1u << s : 0u;
#pragma unroll 1
      do {
        const unsigned s = __builtin_ctz(xm | 8u);
        const unsigned c = *(const LAS unsigned*)(size_t)(ha + s * 256);
        u32x4 w = *(const LAS u32x4*)(size_t)(ia + s * (64 * CPIT) + (pc < 4u ? 128 : 0));
        const unsigned lim = pc < 4u ? (c >> 16) - 4u * pc : 0u;
        w[3] = pc == 7u ? w[3] >> 16 : w[3];
        w[0] = pc == 7u ? w[2] : 32u < lim ? w[0] : ~0u;
        w[1] = 33u < lim ? w[1] : ~0u;
        w[2] = 34u < lim ? w[2] : ~0u;
        seal(w, c > 36u * RINC);
        const unsigned ga = go + s * (64 * IBOX);
        if (xm) {
          w[3] += (((bk >> 3) & 3) + 1) << 24;
          gstore128wt(ENT + 2048 + ((bk >> 5) * 8 + (bk & 7) - bk) * 8, ga, w);
          w[3] -= (((bk >> 3) & 3) + 1) << 24;
          gstore128wt(ENT + IEXT / 16, ga, w);
        }
        xm &= xm - 1u;
        ++xw;
      } while (__ballot(xm != 0u) != 0ull);
    }
  }
  u32x4 cv[4];
  unsigned vo[4], eb[4];
  unsigned long long pend[4];
#pragma unroll
  for (int k = 0; k < 4; ++k) {
    vo[k] = wave * 4096u + k * 1024u + lane * 16u;
    eb[k] = (unsigned)((wave * 32 + k * 8 + (lane >> 3)) * chunk) * 32u;
  }
#define POLL0()                                                                                                       \
  asm volatile("global_load_dwordx4 %0, %4, %8 sc1\n\t"                                                               \
               "global_load_dwordx4 %1, %5, %8 sc1\n\t"                                                               \
               "global_load_dwordx4 %2, %6, %8 sc1\n\t"                                                               \
               "global_load_dwordx4 %3, %7, %8 sc1"                                                                   \
               : "=&{v[240:243]}"(cv[0]), "=&{v[244:247]}"(cv[1]), "=&{v[248:251]}"(cv[2]), "=&{v[252:255]}"(cv[3])   \
               : "v"(vo[0]), "v"(vo[1]), "v"(vo[2]), "v"(vo[3]), "s"(ib)                                              \
               : "memory")
  const f32x16 zero16 = {0.f, 0.f, 0.f, 0.f, 0.f, 0.f, 0.f, 0.f, 0.f, 0.f, 0.f, 0.f, 0.f, 0.f, 0.f, 0.f};
  const unsigned to = lds0 + OFF_W + ((lane >> 2) & 3) * 128 + (lane & 1) * 64 + ((lane >> 4) & 1) * 16 + ((lane >> 1) & 1) * 8;
  auto xa = [&]() {
    bf16x8 wa[8];
#pragma unroll
    for (int k = 0; k < 8; ++k) wa[k] = rawfrag(to + (NBLOB + (k >> 1)) * 1024 + h * 4096 + (k & 1) * 32);
    asm volatile("" : "+v"(wa[0]), "+v"(wa[1]), "+v"(wa[2]), "+v"(wa[3]), "+v"(wa[4]), "+v"(wa[5]), "+v"(wa[6]), "+v"(wa[7]),
                      "+{v[160:163]}"(xg[0]), "+{v[164:167]}"(xg[1]), "+{v[168:171]}"(xg[2]), "+{v[172:175]}"(xg[3]));
    f32x16 XA[2] = {zero16, zero16};
#pragma unroll
    for (int k = 0; k < 4; ++k)
#pragma unroll
      for (int ct = 0; ct < 2; ++ct) XA[ct] = MFMA32(__builtin_bit_cast(bf16x8, xg[k]), wa[k * 2 + ct], XA[ct]);
#pragma unroll
    for (int q = 0; q < 4; ++q) {
      u32x4 xp;
#pragma unroll
      for (int j = 0; j < 4; ++j)
        xp[j] = pack_f16(XA[q & 1][8 * (q >> 1) + 2 * j] * NLOG2E - RA, XA[q & 1][8 * (q >> 1) + 2 * j + 1] * NLOG2E - RA);
      *(LAS u32x4*)((LAS char*)smem + OFF_R1 + wave * RING_SLOT + q * 1024 + lane * 16) = xp;
    }
  };
  {
    auto prep = [&](unsigned f, unsigned hs, float scale) {
      u32x4 w[2];
#pragma unroll
      for (int ct = 0; ct < 2; ++ct) w[ct] = __builtin_bit_cast(u32x4, rawfrag(to + f + h * hs + ct * 32));
      asm volatile("s_waitcnt lgkmcnt(0)" : "+v"(w[0]), "+v"(w[1]) :: "memory");
#pragma unroll
      for (int ct = 0; ct < 2; ++ct) {
#pragma unroll
        for (int j = 0; j < 4; ++j)
          w[ct][j] = pack_bf16(__uint_as_float(w[ct][j] << 16) * scale, __uint_as_float(w[ct][j] & 0xffff0000u) * scale);
        *(LAS u32x4*)((LAS char*)smem + OFF_W + f + ct * hs + lane * 16) = w[ct];
      }
    };
    {
      const unsigned xr = min(nodeof((bu * 8 + wave) * 32 + r) - xn0, 31u);
      unsigned xa_ = lds0 + OFF_L + wave * LIST_BYTES + xr * 128 + (((4 * h) ^ ((xr >> 1) & 7)) * 16);
      asm volatile("s_cmp_eq_u32 %1, 1\n\ts_cbranch_scc1 1f\n\ts_waitcnt vmcnt(4)\n1:\n\ts_waitcnt vmcnt(6)"
                   : "+v"(xa_) : "s"(xw) : "memory", "scc");
#pragma unroll
      for (int t = 0; t < 4; ++t) xg[t] = *(const LAS u32x4*)(size_t)(xa_ ^ (t * 16));
      asm volatile("" : "+v"(xg[0]), "+v"(xg[1]), "+v"(xg[2]), "+v"(xg[3]) :: "memory");
      *(LAS unsigned*)((LAS char*)smem + OFF_L + wave * LIST_BYTES + h * LARR + r * 4) = ~0u;
    }
    asm volatile("s_waitcnt lgkmcnt(0)\n\ts_barrier"
                 : "+v"(vo[0]), "+v"(vo[1]), "+v"(vo[2]), "+v"(vo[3]), "+v"(eb[0]), "+v"(eb[1]), "+v"(eb[2]), "+v"(eb[3])
                 :: "memory");
    xa();
    {
      unsigned f = (wave + (wave & 4)) * 1024, hs = 4096;
      const float scale = wave < 4 ? NLOG2E : VSC;
#pragma unroll 1
      while (true) {
        asm volatile("" : "+s"(f), "+s"(hs));
        prep(f, hs, scale);
        if (f != 0u) break;
        f = 16 * 1024;
        hs = 1024;
      }
    }
  }
  const unsigned l0 = lds0 + OFF_L;
  unsigned aLX = l0 + (lane >> 3) * 16;
  unsigned aLE = l0 + LARR + ((((lane >> 1) & 7) << 2) | (lane >> 4)) * 4;
  unsigned aLS4 = l0 + (4 * h + ((lane >> 2) & 3)) * 16;
  unsigned aLS1 = l0 + (((r & 7) << 2) | (r >> 3)) * 4 + 2;
  unsigned cx0 = (unsigned)(((lane & 7) ^ (lane >> 4)) * 16);
  unsigned cx1 = cx0 ^ 64u;
  unsigned ce = (unsigned)((((lane & 1) ^ ((lane >> 4) & 1))) * 16);
  const unsigned ringd = 8 * RING_SLOT;
  unsigned cS = lds0 + OFF_T1 + (((lane >> 4) & 1) * 16 + (lane & 3) * 4) * 2;
  unsigned cSt = lds0 + OFF_T2 + h * 32;
  unsigned fo[4];
#pragma unroll
  for (int t = 0; t < 4; ++t) fo[t] = lds0 + OFF_R0 + (unsigned)(r * 128 + (((4 * h + t) ^ ((r >> 1) & 7)) * 16));
  unsigned foe = lds0 + OFF_R0 + (unsigned)(4096 + r * 32 + ((h ^ ((r >> 3) & 1)) * 16));
  f32x16 Y[4];
#pragma unroll
  for (int q = 0; q < 4; ++q)
#pragma unroll
    for (int i = 0; i < 16; i += 2) {
      f32x2 y;
      if (q < 2) asm volatile("v_mov_b64 %0, %1" : "=v"(y) : "s"(0x358637bd358637bdull + (RA * 0x0080000000800000ull)));
      else asm volatile("v_mov_b64 %0, 0" : "=v"(y));
      Y[q][i] = y[0];
      Y[q][i + 1] = y[1];
    }
  constexpr unsigned cu = RA == 14 ? 0x04000400u : 0x02000200u;
  unsigned rid = lane & 7;
  asm volatile("" : "+v"(aLX), "+v"(aLE), "+v"(aLS4), "+v"(aLS1), "+v"(cx0), "+v"(cx1), "+v"(ce), "+v"(cS), "+v"(cSt),
                    "+v"(fo[0]), "+v"(fo[1]), "+v"(fo[2]), "+v"(fo[3]), "+v"(foe), "+v"(rid));
  asm volatile("" : "+{v[0:15]}"(Y[0]), "+{v[16:31]}"(Y[1]), "+{v[32:47]}"(Y[2]), "+{v[48:63]}"(Y[3]));
  SB();
  __builtin_amdgcn_s_sleep(PSL);
  POLL0();
  glds16(wu_g + wave * 512, lane * 16, lds0 + OFF_W + (18 + wave) * 1024);
  auto ld128 = [&](unsigned a) { return *(const LAS u32x4*)(size_t)a; };
  auto gather5 = [&](const u32x4 sx, unsigned re, unsigned dstb) {
    glds16e(ES, re, ce, dstb + 4096);
    glds16x(x, sx[0], cx0, dstb);
    glds16x(x, sx[1], cx1, dstb + 1024);
    glds16x(x, sx[2], cx0, dstb + 2048);
    glds16x(x, sx[3], cx1, dstb + 3072);
  };
  const unsigned sq = min(4u * M, 0xffffffu);
  unsigned early, just;
  asm volatile(
        "v_mov_b32 v181, %18\n\t"
        "v_mov_b32 v180, 160\n\t"
        "v_mov_b32 v176, 0xe0\n\t"
        "v_mov_b32 v177, 0x0c040100\n\t"
        "v_mov_b32 v178, 0x0c050100\n\t"
        "v_mov_b32 v179, 0x0c060100\n\t"
        "s_mov_b32 s52, 0x7f7f7f7f\n\t"
        "s_mov_b32 s53, 0x7f7f7f7f\n\t"
        "s_mov_b64 s[54:55], 0\n\t"
        "s_mov_b32 %12, 0\n\t"
        "v_mov_b32 v191, 4\n\t"
        "s_waitcnt vmcnt(3)\n\t"
        "v_add3_u32 v228, v240, v241, v242\n\t"
        "v_add3_u32 v231, v244, v245, v246\n\t"
        "v_add3_u32 v228, v228, v243, %19\n\t"
        "v_add3_u32 v231, v231, v247, %19\n\t"
        "v_cmp_ne_u32_sdwa s[72:73], v243, %20 src0_sel:BYTE_3 src1_sel:DWORD\n\t"
        "v_add_u32_dpp v228, v228, v228 quad_perm:[1,0,3,2] row_mask:0xf bank_mask:0xf\n\t"
        "v_add_u32_dpp v231, v231, v231 quad_perm:[1,0,3,2] row_mask:0xf bank_mask:0xf\n\t"
        "v_cmp_ne_u32_sdwa s[78:79], v247, %20 src0_sel:BYTE_3 src1_sel:DWORD\n\t"
        "v_add_u32_dpp v228, v228, v228 quad_perm:[2,3,0,1] row_mask:0xf bank_mask:0xf\n\t"
        "v_add_u32_dpp v231, v231, v231 quad_perm:[2,3,0,1] row_mask:0xf bank_mask:0xf\n\t"
        "s_nop 0\n\t"
        "v_add_u32_dpp v228, v228, v228 row_half_mirror row_mask:0xf bank_mask:0xf\n\t"
        "v_add_u32_dpp v231, v231, v231 row_half_mirror row_mask:0xf bank_mask:0xf\n\t"
        "v_cmp_gt_u32 s[64:65], 2, v228\n\t"
        "v_cmp_eq_u32 s[56:57], 1, v228\n\t"
        "v_cmp_gt_u32 s[66:67], 2, v231\n\t"
        "v_cmp_eq_u32 s[58:59], 1, v231\n\t"
        "s_and_b64 s[72:73], s[72:73], s[64:65]\n\t"
        "s_cselect_b64 s[64:65], 0, s[64:65]\n\t"
        "s_and_b64 s[56:57], s[56:57], s[64:65]\n\t"
        "s_not_b64 %0, s[64:65]\n\t"
        "s_or_b64 s[54:55], s[54:55], %0\n\t"
        "s_and_b64 s[78:79], s[78:79], s[66:67]\n\t"
        "s_cselect_b64 s[66:67], 0, s[66:67]\n\t"
        "s_and_b64 s[58:59], s[58:59], s[66:67]\n\t"
        "s_not_b64 %1, s[66:67]\n\t"
        "s_or_b64 s[54:55], s[54:55], %1\n\t"
        "s_mov_b64 exec, s[64:65]\n\t"
        "v_cmpx_ne_u32 s[72:73], -1, v240\n\t"
        "v_mul_hi_u32_u24_sdwa v204, v240, %21 dst_sel:DWORD dst_unused:UNUSED_PAD src0_sel:WORD_0 src1_sel:DWORD\n\t"
        "v_and_b32_sdwa v192, v243, v176 dst_sel:DWORD dst_unused:UNUSED_PAD src0_sel:BYTE_0 src1_sel:DWORD\n\t"
        "v_lshl_add_u32 v204, v204, 2, v192\n\t"
        "ds_add_rtn_u32 v204, v204, v191\n\t"
        "v_perm_b32 v216, v243, v240, v177\n\t"
        "v_mad_u32_u16 v228, v240, 32, %13 op_sel:[1,0,0,0]\n\t"
        "v_cmpx_ne_u32 s[74:75], -1, v241\n\t"
        "v_mul_hi_u32_u24_sdwa v205, v241, %21 dst_sel:DWORD dst_unused:UNUSED_PAD src0_sel:WORD_0 src1_sel:DWORD\n\t"
        "v_and_b32_sdwa v193, v243, v176 dst_sel:DWORD dst_unused:UNUSED_PAD src0_sel:BYTE_1 src1_sel:DWORD\n\t"
        "v_lshl_add_u32 v205, v205, 2, v193\n\t"
        "ds_add_rtn_u32 v205, v205, v191\n\t"
        "v_perm_b32 v217, v243, v241, v178\n\t"
        "v_mad_u32_u16 v229, v241, 32, %13 op_sel:[1,0,0,0]\n\t"
        "s_and_b64 exec, exec, s[52:53]\n\t"
        "v_cmpx_ne_u32 s[76:77], -1, v242\n\t"
        "v_mul_hi_u32_u24_sdwa v206, v242, %21 dst_sel:DWORD dst_unused:UNUSED_PAD src0_sel:WORD_0 src1_sel:DWORD\n\t"
        "v_and_b32_sdwa v194, v243, v176 dst_sel:DWORD dst_unused:UNUSED_PAD src0_sel:BYTE_2 src1_sel:DWORD\n\t"
        "v_lshl_add_u32 v206, v206, 2, v194\n\t"
        "ds_add_rtn_u32 v206, v206, v191\n\t"
        "v_perm_b32 v218, v243, v242, v179\n\t"
        "v_mad_u32_u16 v230, v242, 32, %13 op_sel:[1,0,0,0]\n\t"
        "s_mov_b64 exec, s[66:67]\n\t"
        "v_cmpx_ne_u32 s[78:79], -1, v244\n\t"
        "v_mul_hi_u32_u24_sdwa v207, v244, %21 dst_sel:DWORD dst_unused:UNUSED_PAD src0_sel:WORD_0 src1_sel:DWORD\n\t"
        "v_and_b32_sdwa v195, v247, v176 dst_sel:DWORD dst_unused:UNUSED_PAD src0_sel:BYTE_0 src1_sel:DWORD\n\t"
        "v_lshl_add_u32 v207, v207, 2, v195\n\t"
        "ds_add_rtn_u32 v207, v207, v191\n\t"
        "v_perm_b32 v219, v247, v244, v177\n\t"
        "v_mad_u32_u16 v231, v244, 32, %14 op_sel:[1,0,0,0]\n\t"
        "v_cmpx_ne_u32 s[80:81], -1, v245\n\t"
        "v_mul_hi_u32_u24_sdwa v208, v245, %21 dst_sel:DWORD dst_unused:UNUSED_PAD src0_sel:WORD_0 src1_sel:DWORD\n\t"
        "v_and_b32_sdwa v196, v247, v176 dst_sel:DWORD dst_unused:UNUSED_PAD src0_sel:BYTE_1 src1_sel:DWORD\n\t"
        "v_lshl_add_u32 v208, v208, 2, v196\n\t"
        "ds_add_rtn_u32 v208, v208, v191\n\t"
        "v_perm_b32 v220, v247, v245, v178\n\t"
        "v_mad_u32_u16 v232, v245, 32, %14 op_sel:[1,0,0,0]\n\t"
        "s_and_b64 exec, exec, s[52:53]\n\t"
        "v_cmpx_ne_u32 s[82:83], -1, v246\n\t"
        "v_mul_hi_u32_u24_sdwa v209, v246, %21 dst_sel:DWORD dst_unused:UNUSED_PAD src0_sel:WORD_0 src1_sel:DWORD\n\t"
        "v_and_b32_sdwa v197, v247, v176 dst_sel:DWORD dst_unused:UNUSED_PAD src0_sel:BYTE_2 src1_sel:DWORD\n\t"
        "v_lshl_add_u32 v209, v209, 2, v197\n\t"
        "ds_add_rtn_u32 v209, v209, v191\n\t"
        "v_perm_b32 v221, v247, v246, v179\n\t"
        "v_mad_u32_u16 v233, v246, 32, %14 op_sel:[1,0,0,0]\n\t"
        "s_mov_b64 exec, -1\n\t"
        "s_waitcnt vmcnt(1)\n\t"
        "v_add3_u32 v234, v248, v249, v250\n\t"
        "v_add3_u32 v237, v252, v253, v254\n\t"
        "v_add3_u32 v234, v234, v251, %19\n\t"
        "v_add3_u32 v237, v237, v255, %19\n\t"
        "v_cmp_ne_u32_sdwa s[84:85], v251, %20 src0_sel:BYTE_3 src1_sel:DWORD\n\t"
        "v_add_u32_dpp v234, v234, v234 quad_perm:[1,0,3,2] row_mask:0xf bank_mask:0xf\n\t"
        "v_add_u32_dpp v237, v237, v237 quad_perm:[1,0,3,2] row_mask:0xf bank_mask:0xf\n\t"
        "v_cmp_ne_u32_sdwa s[90:91], v255, %20 src0_sel:BYTE_3 src1_sel:DWORD\n\t"
        "v_add_u32_dpp v234, v234, v234 quad_perm:[2,3,0,1] row_mask:0xf bank_mask:0xf\n\t"
        "v_add_u32_dpp v237, v237, v237 quad_perm:[2,3,0,1] row_mask:0xf bank_mask:0xf\n\t"
        "s_nop 0\n\t"
        "v_add_u32_dpp v234, v234, v234 row_half_mirror row_mask:0xf bank_mask:0xf\n\t"
        "v_add_u32_dpp v237, v237, v237 row_half_mirror row_mask:0xf bank_mask:0xf\n\t"
        "v_cmp_gt_u32 s[68:69], 2, v234\n\t"
        "v_cmp_eq_u32 s[60:61], 1, v234\n\t"
        "v_cmp_gt_u32 s[70:71], 2, v237\n\t"
        "v_cmp_eq_u32 s[62:63], 1, v237\n\t"
        "s_and_b64 s[84:85], s[84:85], s[68:69]\n\t"
        "s_cselect_b64 s[68:69], 0, s[68:69]\n\t"
        "s_and_b64 s[60:61], s[60:61], s[68:69]\n\t"
        "s_not_b64 %2, s[68:69]\n\t"
        "s_or_b64 s[54:55], s[54:55], %2\n\t"
        "s_and_b64 s[90:91], s[90:91], s[70:71]\n\t"
        "s_cselect_b64 s[70:71], 0, s[70:71]\n\t"
        "s_and_b64 s[62:63], s[62:63], s[70:71]\n\t"
        "s_not_b64 %3, s[70:71]\n\t"
        "s_or_b64 s[54:55], s[54:55], %3\n\t"
        "s_or_b64 s[50:51], s[56:57], s[58:59]\n\t"
        "s_or_b64 exec, s[60:61], s[62:63]\n\t"
        "s_or_b64 s[50:51], s[50:51], exec\n\t"
        "s_cbranch_scc0 57f\n\t"
        "s_mov_b64 exec, s[50:51]\n\t"
        "global_load_dwordx4 v[172:175], %30, %17 sc1\n\t"
        "57:\n\t"
        "s_mov_b64 exec, s[72:73]\n\t"
        "s_waitcnt lgkmcnt(3)\n\t"
        "v_min_u32 v204, v181, v204\n\t"
        "v_min_u32 v205, v181, v205\n\t"
        "v_min_u32 v206, v181, v206\n\t"
        "v_mad_u32_u24 v204, v192, v180, v204\n\t"
        "v_mad_u32_u24 v205, v193, v180, v205\n\t"
        "v_mad_u32_u24 v206, v194, v180, v206\n\t"
        "ds_write2st64_b32 v204, v216, v228 offset1:10\n\t"
        "s_mov_b64 exec, s[74:75]\n\t"
        "ds_write2st64_b32 v205, v217, v229 offset1:10\n\t"
        "s_mov_b64 exec, s[76:77]\n\t"
        "ds_write2st64_b32 v206, v218, v230 offset1:10\n\t"
        "s_mov_b64 exec, s[68:69]\n\t"
        "v_cmpx_ne_u32 s[84:85], -1, v248\n\t"
        "v_mul_hi_u32_u24_sdwa v210, v248, %21 dst_sel:DWORD dst_unused:UNUSED_PAD src0_sel:WORD_0 src1_sel:DWORD\n\t"
        "v_and_b32_sdwa v198, v251, v176 dst_sel:DWORD dst_unused:UNUSED_PAD src0_sel:BYTE_0 src1_sel:DWORD\n\t"
        "v_lshl_add_u32 v210, v210, 2, v198\n\t"
        "ds_add_rtn_u32 v210, v210, v191\n\t"
        "v_perm_b32 v222, v251, v248, v177\n\t"
        "v_mad_u32_u16 v234, v248, 32, %15 op_sel:[1,0,0,0]\n\t"
        "v_cmpx_ne_u32 s[86:87], -1, v249\n\t"
        "v_mul_hi_u32_u24_sdwa v211, v249, %21 dst_sel:DWORD dst_unused:UNUSED_PAD src0_sel:WORD_0 src1_sel:DWORD\n\t"
        "v_and_b32_sdwa v199, v251, v176 dst_sel:DWORD dst_unused:UNUSED_PAD src0_sel:BYTE_1 src1_sel:DWORD\n\t"
        "v_lshl_add_u32 v211, v211, 2, v199\n\t"
        "ds_add_rtn_u32 v211, v211, v191\n\t"
        "v_perm_b32 v223, v251, v249, v178\n\t"
        "v_mad_u32_u16 v235, v249, 32, %15 op_sel:[1,0,0,0]\n\t"
        "s_and_b64 exec, exec, s[52:53]\n\t"
        "v_cmpx_ne_u32 s[88:89], -1, v250\n\t"
        "v_mul_hi_u32_u24_sdwa v212, v250, %21 dst_sel:DWORD dst_unused:UNUSED_PAD src0_sel:WORD_0 src1_sel:DWORD\n\t"
        "v_and_b32_sdwa v200, v251, v176 dst_sel:DWORD dst_unused:UNUSED_PAD src0_sel:BYTE_2 src1_sel:DWORD\n\t"
        "v_lshl_add_u32 v212, v212, 2, v200\n\t"
        "ds_add_rtn_u32 v212, v212, v191\n\t"
        "v_perm_b32 v224, v251, v250, v179\n\t"
        "v_mad_u32_u16 v236, v250, 32, %15 op_sel:[1,0,0,0]\n\t"
        "s_mov_b64 exec, s[78:79]\n\t"
        "s_waitcnt lgkmcnt(6)\n\t"
        "v_min_u32 v207, v181, v207\n\t"
        "v_min_u32 v208, v181, v208\n\t"
        "v_min_u32 v209, v181, v209\n\t"
        "v_mad_u32_u24 v207, v195, v180, v207\n\t"
        "v_mad_u32_u24 v208, v196, v180, v208\n\t"
        "v_mad_u32_u24 v209, v197, v180, v209\n\t"
        "ds_write2st64_b32 v207, v219, v231 offset1:10\n\t"
        "s_mov_b64 exec, s[80:81]\n\t"
        "ds_write2st64_b32 v208, v220, v232 offset1:10\n\t"
        "s_mov_b64 exec, s[82:83]\n\t"
        "ds_write2st64_b32 v209, v221, v233 offset1:10\n\t"
        "s_mov_b64 exec, -1\n\t"
        "ds_read_b128 v[184:187], %22\n\t"
        "ds_read_b32 v183, %23\n\t"
        "s_mov_b64 exec, s[70:71]\n\t"
        "v_cmpx_ne_u32 s[90:91], -1, v252\n\t"
        "v_mul_hi_u32_u24_sdwa v213, v252, %21 dst_sel:DWORD dst_unused:UNUSED_PAD src0_sel:WORD_0 src1_sel:DWORD\n\t"
        "v_and_b32_sdwa v201, v255, v176 dst_sel:DWORD dst_unused:UNUSED_PAD src0_sel:BYTE_0 src1_sel:DWORD\n\t"
        "v_lshl_add_u32 v213, v213, 2, v201\n\t"
        "ds_add_rtn_u32 v213, v213, v191\n\t"
        "v_perm_b32 v225, v255, v252, v177\n\t"
        "v_mad_u32_u16 v237, v252, 32, %16 op_sel:[1,0,0,0]\n\t"
        "v_cmpx_ne_u32 s[92:93], -1, v253\n\t"
        "v_mul_hi_u32_u24_sdwa v214, v253, %21 dst_sel:DWORD dst_unused:UNUSED_PAD src0_sel:WORD_0 src1_sel:DWORD\n\t"
        "v_and_b32_sdwa v202, v255, v176 dst_sel:DWORD dst_unused:UNUSED_PAD src0_sel:BYTE_1 src1_sel:DWORD\n\t"
        "v_lshl_add_u32 v214, v214, 2, v202\n\t"
        "ds_add_rtn_u32 v214, v214, v191\n\t"
        "v_perm_b32 v226, v255, v253, v178\n\t"
        "v_mad_u32_u16 v238, v253, 32, %16 op_sel:[1,0,0,0]\n\t"
        "s_and_b64 exec, exec, s[52:53]\n\t"
        "v_cmpx_ne_u32 s[94:95], -1, v254\n\t"
        "v_mul_hi_u32_u24_sdwa v215, v254, %21 dst_sel:DWORD dst_unused:UNUSED_PAD src0_sel:WORD_0 src1_sel:DWORD\n\t"
        "v_and_b32_sdwa v203, v255, v176 dst_sel:DWORD dst_unused:UNUSED_PAD src0_sel:BYTE_2 src1_sel:DWORD\n\t"
        "v_lshl_add_u32 v215, v215, 2, v203\n\t"
        "ds_add_rtn_u32 v215, v215, v191\n\t"
        "v_perm_b32 v227, v255, v254, v179\n\t"
        "v_mad_u32_u16 v239, v254, 32, %16 op_sel:[1,0,0,0]\n\t"
        "s_cmp_eq_u64 s[54:55], 0\n\t"
        "s_cbranch_scc1 59f\n\t"
        "s_mov_b64 exec, %0\n\t"
        "s_cbranch_execz 50f\n\t"
        "global_load_dwordx4 v[240:243], %4, %17 sc1\n\t"
        "50:\n\t"
        "s_mov_b64 exec, %1\n\t"
        "s_cbranch_execz 51f\n\t"
        "global_load_dwordx4 v[244:247], %5, %17 sc1\n\t"
        "51:\n\t"
        "s_mov_b64 exec, %2\n\t"
        "s_cbranch_execz 52f\n\t"
        "global_load_dwordx4 v[248:251], %6, %17 sc1\n\t"
        "52:\n\t"
        "s_mov_b64 exec, %3\n\t"
        "s_cbranch_execz 53f\n\t"
        "global_load_dwordx4 v[252:255], %7, %17 sc1\n\t"
        "53:\n\t"
        "59:\n\t"
        "s_mov_b64 exec, s[84:85]\n\t"
        "s_waitcnt lgkmcnt(8)\n\t"
        "v_min_u32 v210, v181, v210\n\t"
        "v_min_u32 v211, v181, v211\n\t"
        "v_min_u32 v212, v181, v212\n\t"
        "v_mad_u32_u24 v210, v198, v180, v210\n\t"
        "v_mad_u32_u24 v211, v199, v180, v211\n\t"
        "v_mad_u32_u24 v212, v200, v180, v212\n\t"
        "ds_write2st64_b32 v210, v222, v234 offset1:10\n\t"
        "s_mov_b64 exec, s[86:87]\n\t"
        "ds_write2st64_b32 v211, v223, v235 offset1:10\n\t"
        "s_mov_b64 exec, s[88:89]\n\t"
        "ds_write2st64_b32 v212, v224, v236 offset1:10\n\t"
        "s_mov_b64 exec, -1\n\t"
        "s_movk_i32 s96, 128\n\t"
        "s_mov_b32 m0, %29\n\t"
        "s_waitcnt lgkmcnt(6)\n\t"
        "v_max3_u32 v182, v184, v185, v186\n\t"
        "v_max3_u32 v182, v182, v187, v183\n\t"
        "v_cmp_eq_u32 vcc, -1, v182\n\t"
        "v_mad_u32_u16 v184, v184, s96, %24\n\t"
        "v_mad_u32_u16 v185, v185, s96, %25\n\t"
        "v_mad_u32_u16 v186, v186, s96, %24\n\t"
        "v_mad_u32_u16 v187, v187, s96, %25\n\t"
        "v_add_u32 v183, v183, %26\n\t"
        "s_cbranch_vccnz 60f\n\t"
        "global_load_lds_dwordx4 v184, %27\n\t"
        "s_addk_i32 m0, 1024\n\t"
        "s_nop 0\n\t"
        "global_load_lds_dwordx4 v185, %27\n\t"
        "s_addk_i32 m0, 1024\n\t"
        "s_nop 0\n\t"
        "global_load_lds_dwordx4 v186, %27\n\t"
        "s_addk_i32 m0, 1024\n\t"
        "s_nop 0\n\t"
        "global_load_lds_dwordx4 v187, %27\n\t"
        "s_addk_i32 m0, 1024\n\t"
        "s_nop 0\n\t"
        "global_load_lds_dwordx4 v183, %28\n\t"
        "s_mov_b32 %12, 1\n\t"
        "60:\n\t"
        "s_mov_b64 exec, s[90:91]\n\t"
        "s_waitcnt lgkmcnt(3)\n\t"
        "v_min_u32 v213, v181, v213\n\t"
        "v_min_u32 v214, v181, v214\n\t"
        "v_min_u32 v215, v181, v215\n\t"
        "v_mad_u32_u24 v213, v201, v180, v213\n\t"
        "v_mad_u32_u24 v214, v202, v180, v214\n\t"
        "v_mad_u32_u24 v215, v203, v180, v215\n\t"
        "ds_write2st64_b32 v213, v225, v237 offset1:10\n\t"
        "s_mov_b64 exec, s[92:93]\n\t"
        "ds_write2st64_b32 v214, v226, v238 offset1:10\n\t"
        "s_mov_b64 exec, s[94:95]\n\t"
        "ds_write2st64_b32 v215, v227, v239 offset1:10\n\t"
        "s_cmp_eq_u64 s[50:51], 0\n\t"
        "s_cbranch_scc1 49f\n\t"
        "s_mov_b64 exec, s[56:57]\n\t"
        "v_mov_b32 v188, 0xff000000\n\t"
        "v_mov_b32 v189, %13\n\t"
        "v_bfe_u32 v190, %4, 7, 8\n\t"
        "ds_write_b8 v190, v191 offset:256\n\t"
        "s_mov_b64 exec, s[58:59]\n\t"
        "v_mov_b32 v188, 0xfe000000\n\t"
        "v_mov_b32 v189, %14\n\t"
        "v_bfe_u32 v190, %5, 7, 8\n\t"
        "ds_write_b8 v190, v191 offset:256\n\t"
        "s_mov_b64 exec, s[60:61]\n\t"
        "v_mov_b32 v188, 0xfd000000\n\t"
        "v_mov_b32 v189, %15\n\t"
        "v_bfe_u32 v190, %6, 7, 8\n\t"
        "ds_write_b8 v190, v191 offset:256\n\t"
        "s_mov_b64 exec, s[62:63]\n\t"
        "v_mov_b32 v188, 0xfc000000\n\t"
        "v_mov_b32 v189, %16\n\t"
        "v_bfe_u32 v190, %7, 7, 8\n\t"
        "ds_write_b8 v190, v191 offset:256\n\t"
        "s_mov_b64 exec, s[50:51]\n\t"
        "s_waitcnt vmcnt(5)\n\t"
        "s_cmp_lg_u32 %12, 0\n\t"
        "s_cbranch_scc1 47f\n\t"
        "s_waitcnt vmcnt(0)\n\t"
        "47:\n\t"
        "v_add_u32 v175, v175, v188\n\t"
        "v_add3_u32 v228, v172, v173, v174\n\t"
        "v_add3_u32 v228, v228, v175, %19\n\t"
        "v_cmp_ne_u32_sdwa s[72:73], v175, %20 src0_sel:BYTE_3 src1_sel:DWORD\n\t"
        "s_nop 0\n\t"
        "v_add_u32_dpp v228, v228, v228 quad_perm:[1,0,3,2] row_mask:0xf bank_mask:0xf\n\t"
        "s_nop 1\n\t"
        "v_add_u32_dpp v228, v228, v228 quad_perm:[2,3,0,1] row_mask:0xf bank_mask:0xf\n\t"
        "s_nop 1\n\t"
        "v_add_u32_dpp v228, v228, v228 row_half_mirror row_mask:0xf bank_mask:0xf\n\t"
        "v_cmp_eq_u32 s[68:69], 0, v228\n\t"
        "s_and_b64 s[72:73], s[72:73], s[68:69]\n\t"
        "s_cselect_b64 exec, 0, s[68:69]\n\t"
        "s_cbranch_execz 48f\n\t"
        "v_cmpx_ne_u32 s[72:73], -1, v172\n\t"
        "v_mul_hi_u32_u24_sdwa v204, v172, %21 dst_sel:DWORD dst_unused:UNUSED_PAD src0_sel:WORD_0 src1_sel:DWORD\n\t"
        "v_and_b32_sdwa v192, v175, v176 dst_sel:DWORD dst_unused:UNUSED_PAD src0_sel:BYTE_0 src1_sel:DWORD\n\t"
        "v_lshl_add_u32 v204, v204, 2, v192\n\t"
        "ds_add_rtn_u32 v204, v204, v191\n\t"
        "v_perm_b32 v216, v175, v172, v177\n\t"
        "v_mad_u32_u16 v228, v172, 32, v189 op_sel:[1,0,0,0]\n\t"
        "v_cmpx_ne_u32 s[74:75], -1, v173\n\t"
        "v_mul_hi_u32_u24_sdwa v205, v173, %21 dst_sel:DWORD dst_unused:UNUSED_PAD src0_sel:WORD_0 src1_sel:DWORD\n\t"
        "v_and_b32_sdwa v193, v175, v176 dst_sel:DWORD dst_unused:UNUSED_PAD src0_sel:BYTE_1 src1_sel:DWORD\n\t"
        "v_lshl_add_u32 v205, v205, 2, v193\n\t"
        "ds_add_rtn_u32 v205, v205, v191\n\t"
        "v_perm_b32 v217, v175, v173, v178\n\t"
        "v_mad_u32_u16 v229, v173, 32, v189 op_sel:[1,0,0,0]\n\t"
        "s_and_b64 exec, exec, s[52:53]\n\t"
        "v_cmpx_ne_u32 s[76:77], -1, v174\n\t"
        "v_mul_hi_u32_u24_sdwa v206, v174, %21 dst_sel:DWORD dst_unused:UNUSED_PAD src0_sel:WORD_0 src1_sel:DWORD\n\t"
        "v_and_b32_sdwa v194, v175, v176 dst_sel:DWORD dst_unused:UNUSED_PAD src0_sel:BYTE_2 src1_sel:DWORD\n\t"
        "v_lshl_add_u32 v206, v206, 2, v194\n\t"
        "ds_add_rtn_u32 v206, v206, v191\n\t"
        "v_perm_b32 v218, v175, v174, v179\n\t"
        "v_mad_u32_u16 v230, v174, 32, v189 op_sel:[1,0,0,0]\n\t"
        "s_mov_b64 s[50:51], 0\n\t"
        "s_and_b64 s[70:71], s[68:69], s[62:63]\n\t"
        "s_xor_b64 s[62:63], s[62:63], s[70:71]\n\t"
        "s_xor_b64 s[68:69], s[68:69], s[70:71]\n\t"
        "s_or_b64 s[50:51], s[50:51], s[62:63]\n\t"
        "s_and_b64 s[70:71], s[68:69], s[60:61]\n\t"
        "s_xor_b64 s[60:61], s[60:61], s[70:71]\n\t"
        "s_xor_b64 s[68:69], s[68:69], s[70:71]\n\t"
        "s_or_b64 s[50:51], s[50:51], s[60:61]\n\t"
        "s_and_b64 s[70:71], s[68:69], s[58:59]\n\t"
        "s_xor_b64 s[58:59], s[58:59], s[70:71]\n\t"
        "s_xor_b64 s[68:69], s[68:69], s[70:71]\n\t"
        "s_or_b64 s[50:51], s[50:51], s[58:59]\n\t"
        "s_and_b64 s[70:71], s[68:69], s[56:57]\n\t"
        "s_xor_b64 s[56:57], s[56:57], s[70:71]\n\t"
        "s_or_b64 s[50:51], s[50:51], s[56:57]\n\t"
        "s_mov_b64 exec, s[72:73]\n\t"
        "s_waitcnt lgkmcnt(0)\n\t"
        "v_min_u32 v204, v181, v204\n\t"
        "v_min_u32 v205, v181, v205\n\t"
        "v_min_u32 v206, v181, v206\n\t"
        "v_mad_u32_u24 v204, v192, v180, v204\n\t"
        "v_mad_u32_u24 v205, v193, v180, v205\n\t"
        "v_mad_u32_u24 v206, v194, v180, v206\n\t"
        "ds_write2st64_b32 v204, v216, v228 offset1:10\n\t"
        "s_mov_b64 exec, s[74:75]\n\t"
        "ds_write2st64_b32 v205, v217, v229 offset1:10\n\t"
        "s_mov_b64 exec, s[76:77]\n\t"
        "ds_write2st64_b32 v206, v218, v230 offset1:10\n\t"
        "s_cbranch_scc0 49f\n\t"
        "48:\n\t"
        "s_mov_b32 s96, 0x1000000\n\t"
        "s_or_b32 %12, %12, 2\n\t"
        "s_mov_b64 exec, s[56:57]\n\t"
        "s_cbranch_execz 40f\n\t"
        "v_add_u32 %4, s96, %4\n\t"
        "s_or_b64 %0, %0, exec\n\t"
        "global_load_dwordx4 v[240:243], %4, %17 sc1\n\t"
        "40:\n\t"
        "s_mov_b64 exec, s[58:59]\n\t"
        "s_cbranch_execz 41f\n\t"
        "v_add_u32 %5, s96, %5\n\t"
        "s_or_b64 %1, %1, exec\n\t"
        "global_load_dwordx4 v[244:247], %5, %17 sc1\n\t"
        "41:\n\t"
        "s_mov_b64 exec, s[60:61]\n\t"
        "s_cbranch_execz 42f\n\t"
        "v_add_u32 %6, s96, %6\n\t"
        "s_or_b64 %2, %2, exec\n\t"
        "global_load_dwordx4 v[248:251], %6, %17 sc1\n\t"
        "42:\n\t"
        "s_mov_b64 exec, s[62:63]\n\t"
        "s_cbranch_execz 43f\n\t"
        "v_add_u32 %7, s96, %7\n\t"
        "s_or_b64 %3, %3, exec\n\t"
        "global_load_dwordx4 v[252:255], %7, %17 sc1\n\t"
        "43:\n\t"
        "49:\n\t"
        "s_or_b32 s54, s54, s55\n\t"
        "s_or_b32 s54, s54, %12\n\t"
        "s_cbranch_scc1 60f\n\t"
        "s_movk_i32 s54, 12\n\t"
        "61:\n\t"
        "s_sub_u32 s54, s54, 1\n\t"
        "s_cbranch_scc1 60f\n\t"
        "s_mov_b64 exec, -1\n\t"
        "ds_read_b128 v[184:187], %22\n\t"
        "ds_read_b32 v183, %23\n\t"
        "s_mov_b64 exec, -1\n\t"
        "s_movk_i32 s96, 128\n\t"
        "s_mov_b32 m0, %29\n\t"
        "s_waitcnt lgkmcnt(0)\n\t"
        "v_max3_u32 v182, v184, v185, v186\n\t"
        "v_max3_u32 v182, v182, v187, v183\n\t"
        "v_cmp_eq_u32 vcc, -1, v182\n\t"
        "v_mad_u32_u16 v184, v184, s96, %24\n\t"
        "v_mad_u32_u16 v185, v185, s96, %25\n\t"
        "v_mad_u32_u16 v186, v186, s96, %24\n\t"
        "v_mad_u32_u16 v187, v187, s96, %25\n\t"
        "v_add_u32 v183, v183, %26\n\t"
        "s_cbranch_vccnz 61b\n\t"
        "global_load_lds_dwordx4 v184, %27\n\t"
        "s_addk_i32 m0, 1024\n\t"
        "s_nop 0\n\t"
        "global_load_lds_dwordx4 v185, %27\n\t"
        "s_addk_i32 m0, 1024\n\t"
        "s_nop 0\n\t"
        "global_load_lds_dwordx4 v186, %27\n\t"
        "s_addk_i32 m0, 1024\n\t"
        "s_nop 0\n\t"
        "global_load_lds_dwordx4 v187, %27\n\t"
        "s_addk_i32 m0, 1024\n\t"
        "s_nop 0\n\t"
        "global_load_lds_dwordx4 v183, %28\n\t"
        "s_mov_b32 %12, 1\n\t"
        "60:\n\t"
        "s_mov_b64 exec, -1\n\t"
        : "=&s"(pend[0]), "=&s"(pend[1]), "=&s"(pend[2]), "=&s"(pend[3]), "+v"(vo[0]), "+v"(vo[1]), "+v"(vo[2]),
          "+v"(vo[3]), "+{v[240:243]}"(cv[0]), "+{v[244:247]}"(cv[1]), "+{v[248:251]}"(cv[2]), "+{v[252:255]}"(cv[3]),
          "=&s"(early)
        : "v"(eb[0]), "v"(eb[1]), "v"(eb[2]), "v"(eb[3]), "s"(ib), "n"(OFF_L + LARR - 4), "v"(tag[0]), "v"(tag[1]),
          "s"(sq), "v"(aLX + wave * LIST_BYTES), "v"(aLE + wave * LIST_BYTES), "v"(cx0), "v"(cx1), "v"(ce), "s"(x),
          "s"(ES), "s"(lds0 + OFF_R0 + wave * RING_SLOT), "v"(32768u + wave * 1024u + lane * 16u)
        :
          "memory", "vcc", "scc", "m0", "v172", "v173", "v174", "v175", "v176", "v177", "v178", "v179", "v180",
          "v181", "v182", "v183", "v184", "v185", "v186", "v187", "v188", "v189", "v190", "v191", "v192", "v193",
          "v194", "v195", "v196", "v197", "v198", "v199", "v200", "v201", "v202", "v203", "v204", "v205", "v206",
          "v207", "v208", "v209", "v210", "v211", "v212", "v213", "v214", "v215", "v216", "v217", "v218", "v219",
          "v220", "v221", "v222", "v223", "v224", "v225", "v226", "v227", "v228", "v229", "v230", "v231", "v232",
          "v233", "v234", "v235", "v236", "v237", "v238", "v239", "s50", "s51", "s52", "s53", "s54", "s55", "s56",
          "s57", "s58", "s59", "s60", "s61", "s62", "s63", "s64", "s65", "s66", "s67", "s68", "s69", "s70", "s71",
          "s72", "s73", "s74", "s75", "s76", "s77", "s78", "s79", "s80", "s81", "s82", "s83", "s84", "s85", "s86",
          "s87", "s88", "s89", "s90", "s91", "s92", "s93", "s94", "s95", "s96");
  auto uni = [&]() {
#pragma unroll
    for (int k = 0; k < 4; ++k)
      pend[k] = ((unsigned long long)(unsigned)__builtin_amdgcn_readfirstlane((unsigned)(pend[k] >> 32)) << 32) |
                (unsigned)__builtin_amdgcn_readfirstlane((unsigned)pend[k]);
  };
  uni();
  early = __builtin_amdgcn_readfirstlane(early);
  just = early;
  if (__builtin_expect((pend[0] | pend[1] | pend[2] | pend[3] | (unsigned long long)(early ^ 1u)) != 0ull, 0))
#pragma clang loop unroll(disable)
  while (true) {
    early &= 1u;
    if (!early) {
      const u32x4 sx = ld128(aLX + wave * LIST_BYTES);
      const unsigned re = *(const LAS unsigned*)(size_t)(aLE + wave * LIST_BYTES);
      if (__ballot(max(max(max(sx[0], sx[1]), max(sx[2], sx[3])), re) == ~0u) == 0ull) {
        gather5(sx, re, lds0 + OFF_R0 + wave * RING_SLOT);
        early = 1u;
        just = 1u;
      }
    }
    if ((pend[0] | pend[1] | pend[2] | pend[3]) == 0ull || --spin < 0) break;
    asm volatile(
        "s_mov_b32 s96, 0x1000000\n\t"
        "v_mov_b32 v181, %17\n\t"
        "v_mov_b32 v180, 160\n\t"
        "v_mov_b32 v176, 0xe0\n\t"
        "v_mov_b32 v177, 0x0c040100\n\t"
        "v_mov_b32 v178, 0x0c050100\n\t"
        "v_mov_b32 v179, 0x0c060100\n\t"
        "s_mov_b32 s52, 0x7f7f7f7f\n\t"
        "s_mov_b32 s53, 0x7f7f7f7f\n\t"
        "v_mov_b32 v191, 4\n\t"
        "s_waitcnt vmcnt(5)\n\t"
        "s_cmp_eq_u32 %21, 1\n\t"
        "s_cbranch_scc1 9f\n\t"
        "s_waitcnt vmcnt(0)\n\t"
        "9:\n\t"
        "v_add3_u32 v228, v240, v241, v242\n\t"
        "v_add3_u32 v229, v244, v245, v246\n\t"
        "v_add3_u32 v230, v248, v249, v250\n\t"
        "v_add3_u32 v231, v252, v253, v254\n\t"
        "v_add3_u32 v228, v228, v243, %18\n\t"
        "v_add3_u32 v229, v229, v247, %18\n\t"
        "v_add3_u32 v230, v230, v251, %18\n\t"
        "v_add3_u32 v231, v231, v255, %18\n\t"
        "v_add_u32_dpp v228, v228, v228 quad_perm:[1,0,3,2] row_mask:0xf bank_mask:0xf\n\t"
        "v_add_u32_dpp v229, v229, v229 quad_perm:[1,0,3,2] row_mask:0xf bank_mask:0xf\n\t"
        "v_add_u32_dpp v230, v230, v230 quad_perm:[1,0,3,2] row_mask:0xf bank_mask:0xf\n\t"
        "v_add_u32_dpp v231, v231, v231 quad_perm:[1,0,3,2] row_mask:0xf bank_mask:0xf\n\t"
        "v_add_u32_dpp v228, v228, v228 quad_perm:[2,3,0,1] row_mask:0xf bank_mask:0xf\n\t"
        "v_add_u32_dpp v229, v229, v229 quad_perm:[2,3,0,1] row_mask:0xf bank_mask:0xf\n\t"
        "v_add_u32_dpp v230, v230, v230 quad_perm:[2,3,0,1] row_mask:0xf bank_mask:0xf\n\t"
        "v_add_u32_dpp v231, v231, v231 quad_perm:[2,3,0,1] row_mask:0xf bank_mask:0xf\n\t"
        "v_add_u32_dpp v228, v228, v228 row_half_mirror row_mask:0xf bank_mask:0xf\n\t"
        "v_add_u32_dpp v229, v229, v229 row_half_mirror row_mask:0xf bank_mask:0xf\n\t"
        "v_add_u32_dpp v230, v230, v230 row_half_mirror row_mask:0xf bank_mask:0xf\n\t"
        "v_add_u32_dpp v231, v231, v231 row_half_mirror row_mask:0xf bank_mask:0xf\n\t"
        "v_cmp_ne_u32_sdwa s[72:73], v243, %19 src0_sel:BYTE_3 src1_sel:DWORD\n\t"
        "v_cmp_gt_u32 s[64:65], 2, v228\n\t"
        "v_cmp_eq_u32 s[56:57], 1, v228\n\t"
        "v_cmp_ne_u32_sdwa s[74:75], v247, %19 src0_sel:BYTE_3 src1_sel:DWORD\n\t"
        "v_cmp_gt_u32 s[66:67], 2, v229\n\t"
        "v_cmp_eq_u32 s[58:59], 1, v229\n\t"
        "v_cmp_ne_u32_sdwa s[76:77], v251, %19 src0_sel:BYTE_3 src1_sel:DWORD\n\t"
        "v_cmp_gt_u32 s[68:69], 2, v230\n\t"
        "v_cmp_eq_u32 s[60:61], 1, v230\n\t"
        "v_cmp_ne_u32_sdwa s[78:79], v255, %19 src0_sel:BYTE_3 src1_sel:DWORD\n\t"
        "v_cmp_gt_u32 s[70:71], 2, v231\n\t"
        "v_cmp_eq_u32 s[62:63], 1, v231\n\t"
        "s_and_b64 s[64:65], s[64:65], %0\n\t"
        "s_and_b64 s[72:73], s[72:73], s[64:65]\n\t"
        "s_cselect_b64 s[64:65], 0, s[64:65]\n\t"
        "s_and_b64 s[56:57], s[56:57], s[64:65]\n\t"
        "s_andn2_b64 %0, %0, s[64:65]\n\t"
        "s_cbranch_scc0 10f\n\t"
        "s_mov_b64 exec, %0\n\t"
        "global_load_dwordx4 v[240:243], %4, %16 sc1\n\t"
        "10:\n\t"
        "s_and_b64 s[66:67], s[66:67], %1\n\t"
        "s_and_b64 s[74:75], s[74:75], s[66:67]\n\t"
        "s_cselect_b64 s[66:67], 0, s[66:67]\n\t"
        "s_and_b64 s[58:59], s[58:59], s[66:67]\n\t"
        "s_andn2_b64 %1, %1, s[66:67]\n\t"
        "s_cbranch_scc0 11f\n\t"
        "s_mov_b64 exec, %1\n\t"
        "global_load_dwordx4 v[244:247], %5, %16 sc1\n\t"
        "11:\n\t"
        "s_and_b64 s[68:69], s[68:69], %2\n\t"
        "s_and_b64 s[76:77], s[76:77], s[68:69]\n\t"
        "s_cselect_b64 s[68:69], 0, s[68:69]\n\t"
        "s_and_b64 s[60:61], s[60:61], s[68:69]\n\t"
        "s_andn2_b64 %2, %2, s[68:69]\n\t"
        "s_cbranch_scc0 12f\n\t"
        "s_mov_b64 exec, %2\n\t"
        "global_load_dwordx4 v[248:251], %6, %16 sc1\n\t"
        "12:\n\t"
        "s_and_b64 s[70:71], s[70:71], %3\n\t"
        "s_and_b64 s[78:79], s[78:79], s[70:71]\n\t"
        "s_cselect_b64 s[70:71], 0, s[70:71]\n\t"
        "s_and_b64 s[62:63], s[62:63], s[70:71]\n\t"
        "s_andn2_b64 %3, %3, s[70:71]\n\t"
        "s_cbranch_scc0 13f\n\t"
        "s_mov_b64 exec, %3\n\t"
        "global_load_dwordx4 v[252:255], %7, %16 sc1\n\t"
        "13:\n\t"
        "s_mov_b64 s[54:55], 0\n\t"
        "s_mov_b64 s[72:73], 0\n\t"
        "s_mov_b64 exec, s[64:65]\n\t"
        "s_cbranch_execz 20f\n\t"
        "v_cmpx_ne_u32 s[72:73], -1, v240\n\t"
        "v_mul_hi_u32_u24_sdwa v204, v240, %20 dst_sel:DWORD dst_unused:UNUSED_PAD src0_sel:WORD_0 src1_sel:DWORD\n\t"
        "v_and_b32_sdwa v192, v243, v176 dst_sel:DWORD dst_unused:UNUSED_PAD src0_sel:BYTE_0 src1_sel:DWORD\n\t"
        "v_lshl_add_u32 v204, v204, 2, v192\n\t"
        "ds_add_rtn_u32 v204, v204, v191\n\t"
        "v_perm_b32 v216, v243, v240, v177\n\t"
        "v_mad_u32_u16 v228, v240, 32, %12 op_sel:[1,0,0,0]\n\t"
        "v_cmpx_ne_u32 s[74:75], -1, v241\n\t"
        "v_mul_hi_u32_u24_sdwa v205, v241, %20 dst_sel:DWORD dst_unused:UNUSED_PAD src0_sel:WORD_0 src1_sel:DWORD\n\t"
        "v_and_b32_sdwa v193, v243, v176 dst_sel:DWORD dst_unused:UNUSED_PAD src0_sel:BYTE_1 src1_sel:DWORD\n\t"
        "v_lshl_add_u32 v205, v205, 2, v193\n\t"
        "ds_add_rtn_u32 v205, v205, v191\n\t"
        "v_perm_b32 v217, v243, v241, v178\n\t"
        "v_mad_u32_u16 v229, v241, 32, %12 op_sel:[1,0,0,0]\n\t"
        "s_and_b64 exec, exec, s[52:53]\n\t"
        "v_cmpx_ne_u32 s[76:77], -1, v242\n\t"
        "v_mul_hi_u32_u24_sdwa v206, v242, %20 dst_sel:DWORD dst_unused:UNUSED_PAD src0_sel:WORD_0 src1_sel:DWORD\n\t"
        "v_and_b32_sdwa v194, v243, v176 dst_sel:DWORD dst_unused:UNUSED_PAD src0_sel:BYTE_2 src1_sel:DWORD\n\t"
        "v_lshl_add_u32 v206, v206, 2, v194\n\t"
        "ds_add_rtn_u32 v206, v206, v191\n\t"
        "v_perm_b32 v218, v243, v242, v179\n\t"
        "v_mad_u32_u16 v230, v242, 32, %12 op_sel:[1,0,0,0]\n\t"
        "s_or_b64 s[54:55], s[54:55], s[56:57]\n\t"
        "20:\n\t"
        "s_mov_b64 s[78:79], 0\n\t"
        "s_mov_b64 exec, s[66:67]\n\t"
        "s_cbranch_execz 21f\n\t"
        "v_cmpx_ne_u32 s[78:79], -1, v244\n\t"
        "v_mul_hi_u32_u24_sdwa v207, v244, %20 dst_sel:DWORD dst_unused:UNUSED_PAD src0_sel:WORD_0 src1_sel:DWORD\n\t"
        "v_and_b32_sdwa v195, v247, v176 dst_sel:DWORD dst_unused:UNUSED_PAD src0_sel:BYTE_0 src1_sel:DWORD\n\t"
        "v_lshl_add_u32 v207, v207, 2, v195\n\t"
        "ds_add_rtn_u32 v207, v207, v191\n\t"
        "v_perm_b32 v219, v247, v244, v177\n\t"
        "v_mad_u32_u16 v231, v244, 32, %13 op_sel:[1,0,0,0]\n\t"
        "v_cmpx_ne_u32 s[80:81], -1, v245\n\t"
        "v_mul_hi_u32_u24_sdwa v208, v245, %20 dst_sel:DWORD dst_unused:UNUSED_PAD src0_sel:WORD_0 src1_sel:DWORD\n\t"
        "v_and_b32_sdwa v196, v247, v176 dst_sel:DWORD dst_unused:UNUSED_PAD src0_sel:BYTE_1 src1_sel:DWORD\n\t"
        "v_lshl_add_u32 v208, v208, 2, v196\n\t"
        "ds_add_rtn_u32 v208, v208, v191\n\t"
        "v_perm_b32 v220, v247, v245, v178\n\t"
        "v_mad_u32_u16 v232, v245, 32, %13 op_sel:[1,0,0,0]\n\t"
        "s_and_b64 exec, exec, s[52:53]\n\t"
        "v_cmpx_ne_u32 s[82:83], -1, v246\n\t"
        "v_mul_hi_u32_u24_sdwa v209, v246, %20 dst_sel:DWORD dst_unused:UNUSED_PAD src0_sel:WORD_0 src1_sel:DWORD\n\t"
        "v_and_b32_sdwa v197, v247, v176 dst_sel:DWORD dst_unused:UNUSED_PAD src0_sel:BYTE_2 src1_sel:DWORD\n\t"
        "v_lshl_add_u32 v209, v209, 2, v197\n\t"
        "ds_add_rtn_u32 v209, v209, v191\n\t"
        "v_perm_b32 v221, v247, v246, v179\n\t"
        "v_mad_u32_u16 v233, v246, 32, %13 op_sel:[1,0,0,0]\n\t"
        "s_or_b64 s[54:55], s[54:55], s[58:59]\n\t"
        "21:\n\t"
        "s_mov_b64 s[84:85], 0\n\t"
        "s_mov_b64 exec, s[68:69]\n\t"
        "s_cbranch_execz 22f\n\t"
        "v_cmpx_ne_u32 s[84:85], -1, v248\n\t"
        "v_mul_hi_u32_u24_sdwa v210, v248, %20 dst_sel:DWORD dst_unused:UNUSED_PAD src0_sel:WORD_0 src1_sel:DWORD\n\t"
        "v_and_b32_sdwa v198, v251, v176 dst_sel:DWORD dst_unused:UNUSED_PAD src0_sel:BYTE_0 src1_sel:DWORD\n\t"
        "v_lshl_add_u32 v210, v210, 2, v198\n\t"
        "ds_add_rtn_u32 v210, v210, v191\n\t"
        "v_perm_b32 v222, v251, v248, v177\n\t"
        "v_mad_u32_u16 v234, v248, 32, %14 op_sel:[1,0,0,0]\n\t"
        "v_cmpx_ne_u32 s[86:87], -1, v249\n\t"
        "v_mul_hi_u32_u24_sdwa v211, v249, %20 dst_sel:DWORD dst_unused:UNUSED_PAD src0_sel:WORD_0 src1_sel:DWORD\n\t"
        "v_and_b32_sdwa v199, v251, v176 dst_sel:DWORD dst_unused:UNUSED_PAD src0_sel:BYTE_1 src1_sel:DWORD\n\t"
        "v_lshl_add_u32 v211, v211, 2, v199\n\t"
        "ds_add_rtn_u32 v211, v211, v191\n\t"
        "v_perm_b32 v223, v251, v249, v178\n\t"
        "v_mad_u32_u16 v235, v249, 32, %14 op_sel:[1,0,0,0]\n\t"
        "s_and_b64 exec, exec, s[52:53]\n\t"
        "v_cmpx_ne_u32 s[88:89], -1, v250\n\t"
        "v_mul_hi_u32_u24_sdwa v212, v250, %20 dst_sel:DWORD dst_unused:UNUSED_PAD src0_sel:WORD_0 src1_sel:DWORD\n\t"
        "v_and_b32_sdwa v200, v251, v176 dst_sel:DWORD dst_unused:UNUSED_PAD src0_sel:BYTE_2 src1_sel:DWORD\n\t"
        "v_lshl_add_u32 v212, v212, 2, v200\n\t"
        "ds_add_rtn_u32 v212, v212, v191\n\t"
        "v_perm_b32 v224, v251, v250, v179\n\t"
        "v_mad_u32_u16 v236, v250, 32, %14 op_sel:[1,0,0,0]\n\t"
        "s_or_b64 s[54:55], s[54:55], s[60:61]\n\t"
        "22:\n\t"
        "s_mov_b64 s[90:91], 0\n\t"
        "s_mov_b64 exec, s[70:71]\n\t"
        "s_cbranch_execz 23f\n\t"
        "v_cmpx_ne_u32 s[90:91], -1, v252\n\t"
        "v_mul_hi_u32_u24_sdwa v213, v252, %20 dst_sel:DWORD dst_unused:UNUSED_PAD src0_sel:WORD_0 src1_sel:DWORD\n\t"
        "v_and_b32_sdwa v201, v255, v176 dst_sel:DWORD dst_unused:UNUSED_PAD src0_sel:BYTE_0 src1_sel:DWORD\n\t"
        "v_lshl_add_u32 v213, v213, 2, v201\n\t"
        "ds_add_rtn_u32 v213, v213, v191\n\t"
        "v_perm_b32 v225, v255, v252, v177\n\t"
        "v_mad_u32_u16 v237, v252, 32, %15 op_sel:[1,0,0,0]\n\t"
        "v_cmpx_ne_u32 s[92:93], -1, v253\n\t"
        "v_mul_hi_u32_u24_sdwa v214, v253, %20 dst_sel:DWORD dst_unused:UNUSED_PAD src0_sel:WORD_0 src1_sel:DWORD\n\t"
        "v_and_b32_sdwa v202, v255, v176 dst_sel:DWORD dst_unused:UNUSED_PAD src0_sel:BYTE_1 src1_sel:DWORD\n\t"
        "v_lshl_add_u32 v214, v214, 2, v202\n\t"
        "ds_add_rtn_u32 v214, v214, v191\n\t"
        "v_perm_b32 v226, v255, v253, v178\n\t"
        "v_mad_u32_u16 v238, v253, 32, %15 op_sel:[1,0,0,0]\n\t"
        "s_and_b64 exec, exec, s[52:53]\n\t"
        "v_cmpx_ne_u32 s[94:95], -1, v254\n\t"
        "v_mul_hi_u32_u24_sdwa v215, v254, %20 dst_sel:DWORD dst_unused:UNUSED_PAD src0_sel:WORD_0 src1_sel:DWORD\n\t"
        "v_and_b32_sdwa v203, v255, v176 dst_sel:DWORD dst_unused:UNUSED_PAD src0_sel:BYTE_2 src1_sel:DWORD\n\t"
        "v_lshl_add_u32 v215, v215, 2, v203\n\t"
        "ds_add_rtn_u32 v215, v215, v191\n\t"
        "v_perm_b32 v227, v255, v254, v179\n\t"
        "v_mad_u32_u16 v239, v254, 32, %15 op_sel:[1,0,0,0]\n\t"
        "s_or_b64 s[54:55], s[54:55], s[62:63]\n\t"
        "23:\n\t"
        "s_cmp_eq_u64 s[54:55], 0\n\t"
        "s_cbranch_scc1 59f\n\t"
        "s_mov_b64 exec, s[56:57]\n\t"
        "s_cbranch_execz 40f\n\t"
        "v_bfe_u32 v188, %4, 7, 8\n\t"
        "v_lshrrev_b32 v189, 24, %4\n\t"
        "v_add_u32 v189, 1, v189\n\t"
        "ds_write_b8 v188, v189 offset:256\n\t"
        "v_add_u32 %4, s96, %4\n\t"
        "v_mov_b32 v190, 0\n\t"
        "v_cmpx_eq_u32 vcc, 2, v189\n\t"
        "ds_write_b32 v190, v191 offset:20\n\t"
        "s_andn2_b64 exec, s[56:57], vcc\n\t"
        "s_cbranch_execz 40f\n\t"
        "s_or_b64 %0, %0, exec\n\t"
        "global_load_dwordx4 v[240:243], %4, %16 sc1\n\t"
        "40:\n\t"
        "s_mov_b64 exec, s[58:59]\n\t"
        "s_cbranch_execz 41f\n\t"
        "v_bfe_u32 v188, %5, 7, 8\n\t"
        "v_lshrrev_b32 v189, 24, %5\n\t"
        "v_add_u32 v189, 1, v189\n\t"
        "ds_write_b8 v188, v189 offset:256\n\t"
        "v_add_u32 %5, s96, %5\n\t"
        "v_mov_b32 v190, 0\n\t"
        "v_cmpx_eq_u32 vcc, 2, v189\n\t"
        "ds_write_b32 v190, v191 offset:20\n\t"
        "s_andn2_b64 exec, s[58:59], vcc\n\t"
        "s_cbranch_execz 41f\n\t"
        "s_or_b64 %1, %1, exec\n\t"
        "global_load_dwordx4 v[244:247], %5, %16 sc1\n\t"
        "41:\n\t"
        "s_mov_b64 exec, s[60:61]\n\t"
        "s_cbranch_execz 42f\n\t"
        "v_bfe_u32 v188, %6, 7, 8\n\t"
        "v_lshrrev_b32 v189, 24, %6\n\t"
        "v_add_u32 v189, 1, v189\n\t"
        "ds_write_b8 v188, v189 offset:256\n\t"
        "v_add_u32 %6, s96, %6\n\t"
        "v_mov_b32 v190, 0\n\t"
        "v_cmpx_eq_u32 vcc, 2, v189\n\t"
        "ds_write_b32 v190, v191 offset:20\n\t"
        "s_andn2_b64 exec, s[60:61], vcc\n\t"
        "s_cbranch_execz 42f\n\t"
        "s_or_b64 %2, %2, exec\n\t"
        "global_load_dwordx4 v[248:251], %6, %16 sc1\n\t"
        "42:\n\t"
        "s_mov_b64 exec, s[62:63]\n\t"
        "s_cbranch_execz 43f\n\t"
        "v_bfe_u32 v188, %7, 7, 8\n\t"
        "v_lshrrev_b32 v189, 24, %7\n\t"
        "v_add_u32 v189, 1, v189\n\t"
        "ds_write_b8 v188, v189 offset:256\n\t"
        "v_add_u32 %7, s96, %7\n\t"
        "v_mov_b32 v190, 0\n\t"
        "v_cmpx_eq_u32 vcc, 2, v189\n\t"
        "ds_write_b32 v190, v191 offset:20\n\t"
        "s_andn2_b64 exec, s[62:63], vcc\n\t"
        "s_cbranch_execz 43f\n\t"
        "s_or_b64 %3, %3, exec\n\t"
        "global_load_dwordx4 v[252:255], %7, %16 sc1\n\t"
        "43:\n\t"
        "59:\n\t"
        "s_waitcnt lgkmcnt(0)\n\t"
        "s_mov_b64 exec, s[72:73]\n\t"
        "s_cbranch_execz 30f\n\t"
        "v_min_u32 v204, v181, v204\n\t"
        "v_min_u32 v205, v181, v205\n\t"
        "v_min_u32 v206, v181, v206\n\t"
        "v_mad_u32_u24 v204, v192, v180, v204\n\t"
        "v_mad_u32_u24 v205, v193, v180, v205\n\t"
        "v_mad_u32_u24 v206, v194, v180, v206\n\t"
        "ds_write2st64_b32 v204, v216, v228 offset1:10\n\t"
        "s_mov_b64 exec, s[74:75]\n\t"
        "ds_write2st64_b32 v205, v217, v229 offset1:10\n\t"
        "s_mov_b64 exec, s[76:77]\n\t"
        "ds_write2st64_b32 v206, v218, v230 offset1:10\n\t"
        "30:\n\t"
        "s_mov_b64 exec, s[78:79]\n\t"
        "s_cbranch_execz 31f\n\t"
        "v_min_u32 v207, v181, v207\n\t"
        "v_min_u32 v208, v181, v208\n\t"
        "v_min_u32 v209, v181, v209\n\t"
        "v_mad_u32_u24 v207, v195, v180, v207\n\t"
        "v_mad_u32_u24 v208, v196, v180, v208\n\t"
        "v_mad_u32_u24 v209, v197, v180, v209\n\t"
        "ds_write2st64_b32 v207, v219, v231 offset1:10\n\t"
        "s_mov_b64 exec, s[80:81]\n\t"
        "ds_write2st64_b32 v208, v220, v232 offset1:10\n\t"
        "s_mov_b64 exec, s[82:83]\n\t"
        "ds_write2st64_b32 v209, v221, v233 offset1:10\n\t"
        "31:\n\t"
        "s_mov_b64 exec, s[84:85]\n\t"
        "s_cbranch_execz 32f\n\t"
        "v_min_u32 v210, v181, v210\n\t"
        "v_min_u32 v211, v181, v211\n\t"
        "v_min_u32 v212, v181, v212\n\t"
        "v_mad_u32_u24 v210, v198, v180, v210\n\t"
        "v_mad_u32_u24 v211, v199, v180, v211\n\t"
        "v_mad_u32_u24 v212, v200, v180, v212\n\t"
        "ds_write2st64_b32 v210, v222, v234 offset1:10\n\t"
        "s_mov_b64 exec, s[86:87]\n\t"
        "ds_write2st64_b32 v211, v223, v235 offset1:10\n\t"
        "s_mov_b64 exec, s[88:89]\n\t"
        "ds_write2st64_b32 v212, v224, v236 offset1:10\n\t"
        "32:\n\t"
        "s_mov_b64 exec, s[90:91]\n\t"
        "s_cbranch_execz 33f\n\t"
        "v_min_u32 v213, v181, v213\n\t"
        "v_min_u32 v214, v181, v214\n\t"
        "v_min_u32 v215, v181, v215\n\t"
        "v_mad_u32_u24 v213, v201, v180, v213\n\t"
        "v_mad_u32_u24 v214, v202, v180, v214\n\t"
        "v_mad_u32_u24 v215, v203, v180, v215\n\t"
        "ds_write2st64_b32 v213, v225, v237 offset1:10\n\t"
        "s_mov_b64 exec, s[92:93]\n\t"
        "ds_write2st64_b32 v214, v226, v238 offset1:10\n\t"
        "s_mov_b64 exec, s[94:95]\n\t"
        "ds_write2st64_b32 v215, v227, v239 offset1:10\n\t"
        "33:\n\t"
        "s_mov_b64 exec, -1\n\t"
        : "+&s"(pend[0]), "+&s"(pend[1]), "+&s"(pend[2]), "+&s"(pend[3]), "+v"(vo[0]), "+v"(vo[1]), "+v"(vo[2]),
          "+v"(vo[3]), "+{v[240:243]}"(cv[0]), "+{v[244:247]}"(cv[1]), "+{v[248:251]}"(cv[2]), "+{v[252:255]}"(cv[3])
        : "v"(eb[0]), "v"(eb[1]), "v"(eb[2]), "v"(eb[3]), "s"(ib), "n"(OFF_L + LARR - 4), "v"(tag[0]), "v"(tag[1]),
          "s"(sq), "s"(just)
        :
          "memory", "vcc", "scc", "v176", "v177", "v178", "v179", "v180", "v181", "v188", "v189", "v190", "v191",
          "v192", "v193", "v194", "v195", "v196", "v197", "v198", "v199", "v200", "v201", "v202", "v203", "v204",
          "v205", "v206", "v207", "v208", "v209", "v210", "v211", "v212", "v213", "v214", "v215", "v216", "v217",
          "v218", "v219", "v220", "v221", "v222", "v223", "v224", "v225", "v226", "v227", "v228", "v229", "v230",
          "v231", "v232", "v233", "v234", "v235", "v236", "v237", "v238", "v239", "s52", "s53", "s54", "s55", "s56",
          "s57", "s58", "s59", "s60", "s61", "s62", "s63", "s64", "s65", "s66", "s67", "s68", "s69", "s70", "s71",
          "s72", "s73", "s74", "s75", "s76", "s77", "s78", "s79", "s80", "s81", "s82", "s83", "s84", "s85", "s86",
          "s87", "s88", "s89", "s90", "s91", "s92", "s93", "s94", "s95", "s96");
    uni();
    just = 0u;
  }
  *(LAS unsigned*)((LAS char*)smem + OFF_CNT + 16 + wave * 32) = early;
  asm volatile("s_waitcnt lgkmcnt(0)\n\ts_barrier" ::: "memory");
  if (wave >= 4) __builtin_amdgcn_s_setprio(1);
  const u32x4 cw = *(const LAS u32x4*)((LAS char*)smem + OFF_CNT + rid * 32);
  const unsigned ef = *(const LAS unsigned*)((LAS char*)smem + OFF_CNT + 16 + rid * 32);
  unsigned slowf = *(const LAS unsigned*)((LAS char*)smem + OFF_CNT + 20);
  asm volatile("" : "+v"(slowf));
  const int key = (int)((cw[0] + cw[1] + cw[2] + cw[3]) * 2u + rid);
  int gb;
  {
    unsigned acc = 0u;
#define RK(k) acc = __builtin_amdgcn_alignbit(acc, (unsigned)(__builtin_amdgcn_update_dpp(0, key, 0x120 + k, 0xf, 0xf, false) - key), 31)
    RK(1); RK(2); RK(3); RK(4); RK(5); RK(6); RK(7);
#undef RK
    const int want = RANKOF(wave);
    gb = __builtin_ctzll(__ballot(__builtin_popcount(acc) == want)) & 7;
  }
  const unsigned ring0 = lds0 + OFF_R0 + gb * RING_SLOT;
  const unsigned la = l0 + gb * LIST_BYTES;
  u32x16 XAf;
#pragma unroll
  for (int q = 0; q < 4; ++q) {
    const u32x4 t = ld128(ring0 + ringd + q * 1024 + lane * 16);
#pragma unroll
    for (int j = 0; j < 4; ++j) XAf[4 * q + j] = t[j];
  }
  unsigned sl[4];
#pragma unroll
  for (int q = 0; q < 4; ++q) sl[q] = __builtin_amdgcn_readlane(cw[q], gb) - (l0 + q * SUBL);
  const unsigned o2 = sl[0] + sl[1], o3 = o2 + sl[2];
  const bool slow = o3 + sl[3] > LCAP * 4u || max(max(sl[0], sl[1]), max(sl[2], sl[3])) > SUBL - 4u ||
                    __builtin_amdgcn_readfirstlane(slowf) != 0u;
  const int cnt = slow ? 0 : (int)(o3 + sl[3]) >> 2;
  const unsigned cg = (bu * 8 + gb) * 32;
  u32x16 AD;
  AD[2] = aLX + gb * LIST_BYTES;
  AD[3] = aLE + gb * LIST_BYTES;
  AD[4] = cx0; AD[5] = cx1; AD[6] = ce;
  auto issue_gather = [&](int tile, int rs) {
    const u32x4 sx = ld128(AD[2] + tile * 128);
    glds16e(ES, *(const LAS unsigned*)(size_t)(AD[3] + tile * 128), AD[6], ring0 + rs * ringd + 4096);
#pragma unroll
    for (int q = 0; q < 4; ++q) glds16x(x, sx[q], AD[4 + (q & 1)], ring0 + rs * ringd + q * 1024);
  };
  auto pads = [&](int cnt, int lane) __attribute__((always_inline)) {
    const int u = cnt + lane;
    const unsigned c = cg + ((u >> 2) & 7) + 8 * (u & 3), n = nodeof(c);
    const unsigned padA = n | (__builtin_amdgcn_readlane(c, __builtin_ctzll(__ballot((n * Mw + Cw) >> 16 != c))) & 0xffu) << 16;
    const unsigned pa = la + u * 4;
    *(LAS unsigned*)(size_t)pa = padA;
    *(LAS unsigned*)(size_t)(pa + 256) = padA;
    *(LAS unsigned*)(size_t)(pa + LARR) = 0u;
    *(LAS unsigned*)(size_t)(pa + LARR + 256) = 0u;
  };
  constexpr int GEND = 64 + NCH;
  int gs = 0, gp = 0;
  auto gen = [&]() __attribute__((always_inline)) {
    int l2, n = 0;
    asm volatile("v_mbcnt_lo_u32_b32 %0, -1, 0\n\tv_mbcnt_hi_u32_b32 %0, -1, %0" : "=v"(l2));
    const LAS unsigned char* xf = (const LAS unsigned char*)smem + OFF_XF;
    auto put = [&](bool ok, unsigned t, unsigned c, int cc) {
      ok = ok && (int)(c >> 5) == gb;
      const unsigned long long bm = __ballot(ok);
      const unsigned pa = la + 4u * (n + __builtin_amdgcn_mbcnt_hi((unsigned)(bm >> 32), __builtin_amdgcn_mbcnt_lo((unsigned)bm, 0u)));
      if (ok) {
        *(LAS unsigned*)(size_t)pa = (t & 0xffffu) | (c << 16);
        *(LAS unsigned*)(size_t)(pa + LARR) = ((unsigned)(cc * chunk) + (t >> 16)) * 32u;
      }
      n += __builtin_popcountll(bm);
    };
#pragma unroll 1
    while (gs < GEND && n <= LCAP - 192) {
      if (gs < 64) {
        const int cc = (gs & 31) * 8 + (l2 >> 3);
        const unsigned* pp = reinterpret_cast<const unsigned*>(ib + (gs >> 5) * IEXT + (gs & 31) * 1024 + l2 * 16);
        const bool live = gs < 32 || xf[cc] != 0;
        const u32x2 tg = *(const LAS u32x2*)((LAS char*)smem + OFF_CNT + 56);
#pragma unroll 1
        for (int i = 0; i < (1 << 20); ++i) {
          const unsigned m = ldd(pp + 3);
          unsigned sm_ = ldd(pp) + ldd(pp + 1) + ldd(pp + 2) + m + tg[0];
          sm_ += __builtin_amdgcn_update_dpp(0u, sm_, 0xB1, 0xf, 0xf, false);
          sm_ += __builtin_amdgcn_update_dpp(0u, sm_, 0x4E, 0xf, 0xf, false);
          sm_ += __builtin_amdgcn_update_dpp(0u, sm_, 0x141, 0xf, 0xf, false);
          if (__ballot(live && (sm_ > 1u || m >> 24 != tg[1])) == 0ull) break;
        }
        const unsigned m = ldd(pp + 3);
#pragma unroll 1
        for (int j = 0; j < 3; ++j) {
          const unsigned t = ldd(pp + j);
          put(live && t != ~0u && (j < 2 || (l2 & 7) != 7), t, (m >> (8 * j)) & 0xffu, cc);
        }
        ++gs;
      } else {
        const int c2 = gs - 64 + l2;
        const unsigned long long fm = __ballot(c2 < NCH && xf[min(c2, NCH - 1)] == 2);
        if (fm == 0ull) { gs = min(gs + 64, GEND); continue; }
        gs += __builtin_ctzll(fm);
        const int cc = gs - 64;
        const unsigned long long ds = ldq(FL + 512 + bu * NCH + cc);
        const int len = __builtin_amdgcn_readfirstlane((int)(ds >> 32));
        const unsigned long long* tp = FL + (TS + (unsigned)ds);
#pragma unroll 1
        for (; gp < len && n <= LCAP - 64; gp += 64) {
          const unsigned long long t = ldq(tp + min(gp + l2, len - 1));
          put(gp + l2 < len, (unsigned)t, (unsigned)(t >> 32), cc);
        }
        if (gp >= len) { gp = 0; ++gs; }
      }
    }
    __builtin_amdgcn_s_waitcnt(0x0F70);
    pads(n, l2);
    return n;
  };
  const bool quick = !slow && sl[0] >= 256u && __builtin_amdgcn_readlane(ef, gb) != 0u;
  auto gather1 = [&](unsigned skip) {
    u32x4 sx = ld128(AD[2] + 128);
    unsigned re = *(const LAS unsigned*)(size_t)(AD[3] + 128);
    asm volatile("s_cmp_lg_u32 %13, 0\n\t"
                 "s_cbranch_scc1 1f\n\t"
                 "s_add_i32 m0, %11, 4096\n\t"
                 "v_add_u32 %4, %4, %8\n\t"
                 "v_mad_u32_u16 %0, %0, %12, %6\n\t"
                 "global_load_lds_dwordx4 %4, %10\n\t"
                 "s_mov_b32 m0, %11\n\t"
                 "v_mad_u32_u16 %1, %1, %12, %7\n\t"
                 "global_load_lds_dwordx4 %0, %9\n\t"
                 "s_addk_i32 m0, 1024\n\t"
                 "v_mad_u32_u16 %2, %2, %12, %6\n\t"
                 "global_load_lds_dwordx4 %1, %9\n\t"
                 "s_addk_i32 m0, 1024\n\t"
                 "v_mad_u32_u16 %3, %3, %12, %7\n\t"
                 "global_load_lds_dwordx4 %2, %9\n\t"
                 "s_addk_i32 m0, 1024\n\t"
                 "s_nop 0\n\t"
                 "global_load_lds_dwordx4 %3, %9\n"
                 "1:"
                 : "+v"(sx[0]), "+v"(sx[1]), "+v"(sx[2]), "+v"(sx[3]), "+v"(re), "+{v[128:143]}"(XAf)
                 : "v"(cx0), "v"(cx1), "v"(ce), "s"(x), "s"(ES), "s"(ring0 + ringd), "s"(128u), "s"(skip)
                 : "memory", "m0", "scc");
  };
  unsigned mode;
  asm volatile("s_cmp_lg_u64 %1, 0\n\ts_cselect_b32 %0, %2, 0" : "=s"(mode) : "s"(__ballot(quick)), "s"(1 + wave / G1W) : "scc");
  gather1(mode ^ 2u);
  if (__builtin_expect(!slow, 1)) {
    const unsigned p0 = la + lane * 4;
    asm volatile(
        "ds_read2st64_b32 v[222:223], %1 offset0:2 offset1:12\n\t"
        "ds_read2st64_b32 v[224:225], %1 offset0:3 offset1:13\n\t"
        "ds_read2st64_b32 v[226:227], %1 offset0:4 offset1:14\n\t"
        "ds_read2st64_b32 v[228:229], %0 offset0:5 offset1:15\n\t"
        "ds_read2st64_b32 v[230:231], %0 offset0:6 offset1:16\n\t"
        "ds_read2st64_b32 v[232:233], %0 offset0:7 offset1:17\n\t"
        "ds_read2st64_b32 v[234:235], %1 offset0:7 offset1:17\n\t"
        "ds_read2st64_b32 v[236:237], %1 offset0:8 offset1:18\n\t"
        "ds_read2st64_b32 v[238:239], %1 offset0:9 offset1:19\n\t"
        "s_waitcnt lgkmcnt(8)\n\t"
        "ds_write2st64_b32 %2, v222, v223 offset0:0 offset1:10\n\t"
        "s_waitcnt lgkmcnt(8)\n\t"
        "ds_write2st64_b32 %2, v224, v225 offset0:1 offset1:11\n\t"
        "s_waitcnt lgkmcnt(8)\n\t"
        "ds_write2st64_b32 %2, v226, v227 offset0:2 offset1:12\n\t"
        "s_waitcnt lgkmcnt(8)\n\t"
        "ds_write2st64_b32 %3, v228, v229 offset0:0 offset1:10\n\t"
        "s_waitcnt lgkmcnt(8)\n\t"
        "ds_write2st64_b32 %3, v230, v231 offset0:1 offset1:11\n\t"
        "s_waitcnt lgkmcnt(8)\n\t"
        "ds_write2st64_b32 %3, v232, v233 offset0:2 offset1:12\n\t"
        "s_waitcnt lgkmcnt(8)\n\t"
        "ds_write2st64_b32 %4, v234, v235 offset0:0 offset1:10\n\t"
        "s_waitcnt lgkmcnt(8)\n\t"
        "ds_write2st64_b32 %4, v236, v237 offset0:1 offset1:11\n\t"
        "s_mov_b64 exec, 0xffffffff\n\t"
        "s_waitcnt lgkmcnt(8)\n\t"
        "ds_write2st64_b32 %4, v238, v239 offset0:2 offset1:12\n\t"
        "s_mov_b64 exec, -1"
        :: "v"(p0), "v"(p0 + 128u), "v"(p0 + sl[0]), "v"(p0 + o2), "v"(p0 + o3)
        : "memory", "v222", "v223", "v224", "v225", "v226", "v227", "v228", "v229", "v230", "v231", "v232", "v233", "v234",
          "v235", "v236", "v237", "v238", "v239");
  }
  pads(cnt, lane);
  AD[0] = aLS4 + gb * LIST_BYTES;
  AD[1] = aLS1 + gb * LIST_BYTES;
  AD[7] = cS - gb * 32 * TPITCH;
  AD[8] = cSt - gb * 32 * TPITCH;
#pragma unroll
  for (int t = 0; t < 4; ++t) AD[9 + t] = fo[t] + gb * RING_SLOT;
  AD[13] = foe + gb * RING_SLOT;
  AD[14] = lds0 + lane * 16;
  AD[15] = to + 18 * 1024 + h * 4096;
  if (__builtin_expect(mode == 0u, 0)) asm volatile("s_waitcnt vmcnt(0)\n\ts_barrier" ::: "memory");
  f32x16 X[2], U[2];
  if (__builtin_expect(mode == 0u, 0)) {
    issue_gather(0, 0);
    issue_gather(1, 1);
  }
  {
    int nt = __builtin_amdgcn_readfirstlane(max((cnt - 1) >> 5, 0));
    asm volatile(
        "s_movk_i32 s94, 128\n\t"
        "s_movk_i32 s95, 80\n\t"
        "s_mov_b32 s96, %[cu]\n\t"
        "ds_read_u16 v102, v161\n\t"
        "ds_read_b128 v[104:107], v162 offset:128\n\t"
        "ds_read_b32 v101, v163 offset:128\n\t"
        "ds_read_b128 v[96:99], v162 offset:256\n\t"
        "ds_read_b32 v100, v163 offset:256\n\t"
        "ds_read_b128 v[120:123], v160\n\t"
        "ds_read_u16 v124, v161 offset:128\n\t"
        "s_waitcnt lgkmcnt(6)\n\t"
        "v_mad_u32_u24 v102, v102, s95, v168\n\t"
        "ds_read_b128 v[184:187], v102\n\t"
        "ds_read_b128 v[188:191], v102 offset:16\n\t"
        "ds_read_b128 v[112:115], v174 offset:512\n\t"
        "ds_read_b128 v[116:119], v174 offset:2560\n\t"
        "ds_read_b128 v[108:111], v174 offset:8704\n\t"
        "ds_read_b128 v[196:199], v174 offset:16896\n\t"
        "ds_read_b128 v[200:203], v174 offset:1536\n\t"
        "ds_read_b128 v[204:207], v174 offset:3584\n\t"
        "s_waitcnt lgkmcnt(12)\n\t"
        "v_add_u32 v101, v101, v166\n\t"
        "v_mad_u32_u16 v104, v104, s94, v164\n\t"
        "v_mad_u32_u16 v105, v105, s94, v165\n\t"
        "v_mad_u32_u16 v106, v106, s94, v164\n\t"
        "v_mad_u32_u16 v107, v107, s94, v165\n\t"
        "s_waitcnt lgkmcnt(7)\n\t"
        "v_mfma_f32_32x32x16_f16 v[64:79], v[184:187], v[128:131], 0\n\t"
        "ds_read_b128 v[208:211], v174 offset:9728\n\t"
        "ds_read_b128 v[212:215], v174 offset:10752\n\t"
        "ds_read_b128 v[216:219], v174 offset:11776\n\t"
        "ds_read_b128 v[220:223], v174 offset:17920\n\t"
        "ds_read_b128 v[224:227], v174 offset:4608\n\t"
        "ds_read_b128 v[228:231], v174 offset:5632\n\t"
        "s_waitcnt lgkmcnt(12)\n\t"
        "v_mfma_f32_32x32x16_f16 v[64:79], v[188:191], v[136:139], v[64:79]\n\t"
        "ds_read_b128 v[232:235], v174 offset:6656\n\t"
        "ds_read_b128 v[236:239], v174 offset:7680\n\t"
        "ds_read_b128 v[240:243], v174 offset:12800\n\t"
        "ds_read_b128 v[244:247], v174 offset:13824\n\t"
        "ds_read_b128 v[248:251], v174 offset:14848\n\t"
        "ds_read_b128 v[252:255], v174 offset:15872\n\t"
        "s_add_i32 m0, %[ring], 45056\n\t"
        "s_waitcnt vmcnt(5)\n\t"
        "s_cmp_eq_u32 %[flag], 0\n\t"
        "s_cbranch_scc1 2f\n\t"
        "s_cmp_eq_u32 %[flag], 2\n\t"
        "s_cbranch_scc1 3f\n\t"
        "s_waitcnt vmcnt(0)\n\t"
        "3:\n\t"
        "s_barrier\n\t"
        "2:\n\t"
        "ds_read_b128 v[144:147], v169\n\t"
        "ds_read_b128 v[148:151], v170\n\t"
        "ds_read_b128 v[152:155], v171\n\t"
        "ds_read_b128 v[156:159], v172\n\t"
        "ds_read_b128 v[192:195], v173\n\t"
        "s_cbranch_scc1 4f\n\t"
        "global_load_lds_dwordx4 v101, %[es]\n\t"
        "s_add_i32 m0, %[ring], 40960\n\t"
        "s_nop 0\n\t"
        "global_load_lds_dwordx4 v104, %[x]\n\t"
        "s_add_i32 m0, %[ring], 41984\n\t"
        "s_nop 0\n\t"
        "global_load_lds_dwordx4 v105, %[x]\n\t"
        "s_add_i32 m0, %[ring], 43008\n\t"
        "s_nop 0\n\t"
        "global_load_lds_dwordx4 v106, %[x]\n\t"
        "s_add_i32 m0, %[ring], 44032\n\t"
        "s_nop 0\n\t"
        "global_load_lds_dwordx4 v107, %[x]\n\t"
        "4:\n\t"
        "s_waitcnt lgkmcnt(4)\n\t"
        "v_mfma_f32_32x32x16_bf16 v[64:79], v[144:147], v[112:115], v[64:79]\n\t"
        "s_waitcnt lgkmcnt(3)\n\t"
        "v_mfma_f32_32x32x16_bf16 v[64:79], v[148:151], v[200:203], v[64:79]\n\t"
        "s_waitcnt lgkmcnt(2)\n\t"
        "v_mfma_f32_32x32x16_bf16 v[64:79], v[152:155], v[116:119], v[64:79]\n\t"
        "s_waitcnt lgkmcnt(1)\n\t"
        "v_mfma_f32_32x32x16_bf16 v[64:79], v[156:159], v[204:207], v[64:79]\n\t"
        "s_waitcnt lgkmcnt(0)\n\t"
        "v_mfma_f32_32x32x16_bf16 v[64:79], v[192:195], v[196:199], v[64:79]\n\t"
        "s_add_i32 m0, %[ring], 4096\n\t"
        "v_add_u32 v100, v100, v166\n\t"
        "global_load_lds_dwordx4 v100, %[es]\n\t"
        "v_mfma_f32_32x32x16_bf16 v[80:95], v[144:147], v[108:111], 0\n\t"
        "s_mov_b32 m0, %[ring]\n\t"
        "v_mad_u32_u16 v96, v96, s94, v164\n\t"
        "global_load_lds_dwordx4 v96, %[x]\n\t"
        "s_add_i32 m0, %[ring], 1024\n\t"
        "v_mad_u32_u16 v97, v97, s94, v165\n\t"
        "global_load_lds_dwordx4 v97, %[x]\n\t"
        "v_mfma_f32_32x32x16_bf16 v[80:95], v[148:151], v[208:211], v[80:95]\n\t"
        "s_add_i32 m0, %[ring], 2048\n\t"
        "v_mad_u32_u16 v98, v98, s94, v164\n\t"
        "global_load_lds_dwordx4 v98, %[x]\n\t"
        "v_exp_f32 v64, v64\n\t"
        "v_exp_f32 v65, v65\n\t"
        "v_mfma_f32_32x32x16_bf16 v[80:95], v[152:155], v[212:215], v[80:95]\n\t"
        "s_add_i32 m0, %[ring], 3072\n\t"
        "v_mad_u32_u16 v99, v99, s94, v165\n\t"
        "global_load_lds_dwordx4 v99, %[x]\n\t"
        "v_exp_f32 v66, v66\n\t"
        "v_exp_f32 v67, v67\n\t"
        "v_mfma_f32_32x32x16_bf16 v[80:95], v[156:159], v[216:219], v[80:95]\n\t"
        "v_cvt_pk_f16_f32 v64, v64, v65\n\t"
        "v_exp_f32 v68, v68\n\t"
        "v_cvt_pk_f16_f32 v65, v66, v67\n\t"
        "v_pk_add_f16 v64, v64, s96\n\t"
        "v_exp_f32 v69, v69\n\t"
        "v_pk_add_f16 v65, v65, s96\n\t"
        "v_rcp_f16_sdwa v64, v64 dst_sel:WORD_0 dst_unused:UNUSED_PRESERVE src0_sel:WORD_0\n\t"
        "v_exp_f32 v70, v70\n\t"
        "v_cvt_pk_f16_f32 v66, v68, v69\n\t"
        "v_rcp_f16_sdwa v65, v65 dst_sel:WORD_0 dst_unused:UNUSED_PRESERVE src0_sel:WORD_0\n\t"
        "v_pk_add_f16 v66, v66, s96\n\t"
        "v_rcp_f16_sdwa v64, v64 dst_sel:WORD_1 dst_unused:UNUSED_PRESERVE src0_sel:WORD_1\n\t"
        "v_cvt_pk_f16_f32 v80, v80, v81\n\t"
        "v_rcp_f16_sdwa v65, v65 dst_sel:WORD_1 dst_unused:UNUSED_PRESERVE src0_sel:WORD_1\n\t"
        "v_pk_mul_f16 v80, v80, v64\n\t"
        "s_branch .Lr4_%=\n\t"
        ".Lrt_%=:\n\t"
        "v_mfma_f32_32x32x16_f16 v[16:31], v[96:99], v[176:179], v[16:31]\n\t"
        "s_cmp_lt_u32 %[wave], 4\n\t"
        "s_cbranch_scc1 1f\n\t"
        "s_setprio 1\n\t"
        "1:\n\t"
        "ds_read_b128 v[120:123], v160\n\t"
        "ds_read_u16 v124, v161 offset:128\n\t"
        "v_rcp_f16_sdwa v103, v103 dst_sel:WORD_1 dst_unused:UNUSED_PRESERVE src0_sel:WORD_1\n\t"
        "v_cvt_pk_f16_f32 v119, v126, v127\n\t"
        "v_pk_add_f16 v64, v64, s96\n\t"
        "v_exp_f32 v67, v67\n\t"
        "v_rcp_f16_sdwa v64, v64 dst_sel:WORD_0 dst_unused:UNUSED_PRESERVE src0_sel:WORD_0\n\t"
        "v_mfma_f32_32x32x16_f16 v[48:63], v[112:115], v[176:179], v[48:63]\n\t"
        "v_pk_mul_f16 v119, v119, v103\n\t"
        "v_cvt_pk_f16_f32 v65, v66, v67\n\t"
        "v_rcp_f16_sdwa v64, v64 dst_sel:WORD_1 dst_unused:UNUSED_PRESERVE src0_sel:WORD_1\n\t"
        "v_pk_add_f16 v65, v65, s96\n\t"
        "v_exp_f32 v68, v68\n\t"
        "v_mfma_f32_32x32x16_f16 v[16:31], v[100:103], v[180:183], v[16:31]\n\t"
        "v_cvt_pk_f16_f32 v80, v80, v81\n\t"
        "v_rcp_f16_sdwa v65, v65 dst_sel:WORD_0 dst_unused:UNUSED_PRESERVE src0_sel:WORD_0\n\t"
        "v_pk_mul_f16 v80, v80, v64\n\t"
        "v_exp_f32 v69, v69\n\t"
        "v_mfma_f32_32x32x16_f16 v[48:63], v[116:119], v[180:183], v[48:63]\n\t"
        "v_cvt_pk_f16_f32 v66, v68, v69\n\t"
        "v_rcp_f16_sdwa v65, v65 dst_sel:WORD_1 dst_unused:UNUSED_PRESERVE src0_sel:WORD_1\n\t"
        "v_pk_add_f16 v66, v66, s96\n\t"
        "v_exp_f32 v70, v70\n\t"
        ".Lr4_%=:\n\t"
        "v_mfma_f32_32x32x16_f16 v[96:111], v[184:187], v[132:135], 0\n\t"
        "s_waitcnt lgkmcnt(1)\n\t"
        "v_mad_u32_u16 v120, v120, s95, v167 op_sel:[1,0,0,0]\n\t"
        "ds_read_b64_tr_b16 v[176:177], v120\n\t"
        "v_mad_u32_u16 v121, v121, s95, v167 op_sel:[1,0,0,0]\n\t"
        "ds_read_b64_tr_b16 v[178:179], v121\n\t"
        "v_mad_u32_u16 v122, v122, s95, v167 op_sel:[1,0,0,0]\n\t"
        "ds_read_b64_tr_b16 v[180:181], v122\n\t"
        "v_mad_u32_u16 v123, v123, s95, v167 op_sel:[1,0,0,0]\n\t"
        "ds_read_b64_tr_b16 v[182:183], v123\n\t"
        "v_cvt_pk_f16_f32 v81, v82, v83\n\t"
        "v_rcp_f16_sdwa v66, v66 dst_sel:WORD_0 dst_unused:UNUSED_PRESERVE src0_sel:WORD_0\n\t"
        "v_pk_mul_f16 v81, v81, v65\n\t"
        "v_exp_f32 v71, v71\n\t"
        "v_mfma_f32_32x32x16_f16 v[96:111], v[188:191], v[140:143], v[96:111]\n\t"
        "s_waitcnt lgkmcnt(4)\n\t"
        "v_mad_u32_u24 v124, v124, s95, v168\n\t"
        "ds_read_b128 v[184:187], v124\n\t"
        "ds_read_b128 v[188:191], v124 offset:16\n\t"
        "v_cvt_pk_f16_f32 v67, v70, v71\n\t"
        "v_rcp_f16_sdwa v66, v66 dst_sel:WORD_1 dst_unused:UNUSED_PRESERVE src0_sel:WORD_1\n\t"
        "v_pk_add_f16 v67, v67, s96\n\t"
        "v_exp_f32 v72, v72\n\t"
        "v_mfma_f32_32x32x16_bf16 v[96:111], v[192:195], v[220:223], v[96:111]\n\t"
        "s_waitcnt vmcnt(9)\n\t"
        "ds_read_b128 v[192:195], v173 offset:40960\n\t"
        "ds_read_b32 v125, v163 offset:384\n\t"
        "v_cvt_pk_f16_f32 v82, v84, v85\n\t"
        "v_rcp_f16_sdwa v67, v67 dst_sel:WORD_0 dst_unused:UNUSED_PRESERVE src0_sel:WORD_0\n\t"
        "v_pk_mul_f16 v82, v82, v66\n\t"
        "v_exp_f32 v73, v73\n\t"
        "v_mfma_f32_32x32x16_bf16 v[96:111], v[144:147], v[224:227], v[96:111]\n\t"
        "v_cvt_pk_f16_f32 v68, v72, v73\n\t"
        "v_rcp_f16_sdwa v67, v67 dst_sel:WORD_1 dst_unused:UNUSED_PRESERVE src0_sel:WORD_1\n\t"
        "v_pk_add_f16 v68, v68, s96\n\t"
        "v_exp_f32 v74, v74\n\t"
        "v_mfma_f32_32x32x16_bf16 v[96:111], v[148:151], v[228:231], v[96:111]\n\t"
        "v_cvt_pk_f16_f32 v83, v86, v87\n\t"
        "v_rcp_f16_sdwa v68, v68 dst_sel:WORD_0 dst_unused:UNUSED_PRESERVE src0_sel:WORD_0\n\t"
        "v_pk_mul_f16 v83, v83, v67\n\t"
        "v_exp_f32 v75, v75\n\t"
        "v_mfma_f32_32x32x16_bf16 v[96:111], v[152:155], v[232:235], v[96:111]\n\t"
        "s_add_i32 m0, %[ring], 45056\n\t"
        "s_waitcnt lgkmcnt(0)\n\t"
        "v_add_u32 v125, v125, v166\n\t"
        "global_load_lds_dwordx4 v125, %[es]\n\t"
        "v_cvt_pk_f16_f32 v69, v74, v75\n\t"
        "v_rcp_f16_sdwa v68, v68 dst_sel:WORD_1 dst_unused:UNUSED_PRESERVE src0_sel:WORD_1\n\t"
        "v_pk_add_f16 v69, v69, s96\n\t"
        "v_exp_f32 v76, v76\n\t"
        "v_mfma_f32_32x32x16_bf16 v[96:111], v[156:159], v[236:239], v[96:111]\n\t"
        "v_cvt_pk_f16_f32 v84, v88, v89\n\t"
        "v_rcp_f16_sdwa v69, v69 dst_sel:WORD_0 dst_unused:UNUSED_PRESERVE src0_sel:WORD_0\n\t"
        "v_pk_mul_f16 v84, v84, v68\n\t"
        "v_exp_f32 v77, v77\n\t"
        "v_mfma_f32_32x32x16_bf16 v[112:127], v[144:147], v[240:243], 0\n\t"
        "s_waitcnt vmcnt(6)\n\t"
        "ds_read_b128 v[144:147], v169 offset:40960\n\t"
        "v_cvt_pk_f16_f32 v70, v76, v77\n\t"
        "v_rcp_f16_sdwa v69, v69 dst_sel:WORD_1 dst_unused:UNUSED_PRESERVE src0_sel:WORD_1\n\t"
        "v_pk_add_f16 v70, v70, s96\n\t"
        "v_exp_f32 v78, v78\n\t"
        "v_mfma_f32_32x32x16_bf16 v[112:127], v[148:151], v[244:247], v[112:127]\n\t"
        "ds_read_b128 v[148:151], v170 offset:40960\n\t"
        "s_sub_u32 %[nt], %[nt], 1\n\t"
        "v_cvt_pk_f16_f32 v85, v90, v91\n\t"
        "v_rcp_f16_sdwa v70, v70 dst_sel:WORD_0 dst_unused:UNUSED_PRESERVE src0_sel:WORD_0\n\t"
        "v_pk_mul_f16 v85, v85, v69\n\t"
        "v_exp_f32 v79, v79\n\t"
        "v_mfma_f32_32x32x16_bf16 v[112:127], v[152:155], v[248:251], v[112:127]\n\t"
        "ds_read_b128 v[152:155], v171 offset:40960\n\t"
        "ds_read_b128 v[88:91], v162 offset:384\n\t"
        "v_cvt_pk_f16_f32 v71, v78, v79\n\t"
        "v_rcp_f16_sdwa v70, v70 dst_sel:WORD_1 dst_unused:UNUSED_PRESERVE src0_sel:WORD_1\n\t"
        "v_pk_add_f16 v71, v71, s96\n\t"
        "v_exp_f32 v96, v96\n\t"
        "v_exp_f32 v97, v97\n\t"
        "v_mfma_f32_32x32x16_bf16 v[112:127], v[156:159], v[252:255], v[112:127]\n\t"
        "ds_read_b128 v[156:159], v172 offset:40960\n\t"
        "v_cvt_pk_f16_f32 v86, v92, v93\n\t"
        "v_rcp_f16_sdwa v71, v71 dst_sel:WORD_0 dst_unused:UNUSED_PRESERVE src0_sel:WORD_0\n\t"
        "v_pk_mul_f16 v86, v86, v70\n\t"
        "v_exp_f32 v98, v98\n\t"
        "v_cvt_pk_f16_f32 v96, v96, v97\n\t"
        "s_cbranch_scc1 .Lrl_%=\n\t"
        "v_mfma_f32_32x32x16_f16 v[0:15], v[64:67], v[176:179], v[0:15]\n\t"
        "v_rcp_f16_sdwa v71, v71 dst_sel:WORD_1 dst_unused:UNUSED_PRESERVE src0_sel:WORD_1\n\t"
        "v_cvt_pk_f16_f32 v87, v94, v95\n\t"
        "v_pk_add_f16 v96, v96, s96\n\t"
        "v_exp_f32 v99, v99\n\t"
        "v_rcp_f16_sdwa v96, v96 dst_sel:WORD_0 dst_unused:UNUSED_PRESERVE src0_sel:WORD_0\n\t"
        "v_mfma_f32_32x32x16_f16 v[32:47], v[80:83], v[176:179], v[32:47]\n\t"
        "s_add_i32 m0, %[ring], 40960\n\t"
        "s_waitcnt lgkmcnt(1)\n\t"
        "v_mad_u32_u16 v88, v88, s94, v164\n\t"
        "s_waitcnt lgkmcnt(0)\n\t"
        "global_load_lds_dwordx4 v88, %[x]\n\t"
        "v_pk_mul_f16 v87, v87, v71\n\t"
        "v_cvt_pk_f16_f32 v97, v98, v99\n\t"
        "v_rcp_f16_sdwa v96, v96 dst_sel:WORD_1 dst_unused:UNUSED_PRESERVE src0_sel:WORD_1\n\t"
        "v_pk_add_f16 v97, v97, s96\n\t"
        "v_exp_f32 v100, v100\n\t"
        "v_mfma_f32_32x32x16_f16 v[0:15], v[68:71], v[180:183], v[0:15]\n\t"
        "ds_read_b128 v[92:95], v174 offset:512\n\t"
        "s_add_i32 m0, %[ring], 41984\n\t"
        "v_mad_u32_u16 v89, v89, s94, v165\n\t"
        "global_load_lds_dwordx4 v89, %[x]\n\t"
        "v_cvt_pk_f16_f32 v112, v112, v113\n\t"
        "v_rcp_f16_sdwa v97, v97 dst_sel:WORD_0 dst_unused:UNUSED_PRESERVE src0_sel:WORD_0\n\t"
        "v_pk_mul_f16 v112, v112, v96\n\t"
        "v_exp_f32 v101, v101\n\t"
        "v_mfma_f32_32x32x16_f16 v[32:47], v[84:87], v[180:183], v[32:47]\n\t"
        "s_add_i32 m0, %[ring], 43008\n\t"
        "v_mad_u32_u16 v90, v90, s94, v164\n\t"
        "global_load_lds_dwordx4 v90, %[x]\n\t"
        "v_cvt_pk_f16_f32 v98, v100, v101\n\t"
        "v_rcp_f16_sdwa v97, v97 dst_sel:WORD_1 dst_unused:UNUSED_PRESERVE src0_sel:WORD_1\n\t"
        "v_pk_add_f16 v98, v98, s96\n\t"
        "v_exp_f32 v102, v102\n\t"
        "v_mfma_f32_32x32x16_f16 v[64:79], v[184:187], v[128:131], 0\n\t"
        "s_add_i32 m0, %[ring], 44032\n\t"
        "v_mad_u32_u16 v91, v91, s94, v165\n\t"
        "global_load_lds_dwordx4 v91, %[x]\n\t"
        "v_cvt_pk_f16_f32 v113, v114, v115\n\t"
        "v_rcp_f16_sdwa v98, v98 dst_sel:WORD_0 dst_unused:UNUSED_PRESERVE src0_sel:WORD_0\n\t"
        "v_pk_mul_f16 v113, v113, v97\n\t"
        "v_exp_f32 v103, v103\n\t"
        "v_mfma_f32_32x32x16_f16 v[64:79], v[188:191], v[136:139], v[64:79]\n\t"
        "ds_read_b128 v[80:83], v174 offset:2560\n\t"
        "v_cvt_pk_f16_f32 v99, v102, v103\n\t"
        "v_rcp_f16_sdwa v98, v98 dst_sel:WORD_1 dst_unused:UNUSED_PRESERVE src0_sel:WORD_1\n\t"
        "v_pk_add_f16 v99, v99, s96\n\t"
        "v_exp_f32 v104, v104\n\t"
        "v_mfma_f32_32x32x16_bf16 v[64:79], v[192:195], v[196:199], v[64:79]\n\t"
        "v_cvt_pk_f16_f32 v114, v116, v117\n\t"
        "v_rcp_f16_sdwa v99, v99 dst_sel:WORD_0 dst_unused:UNUSED_PRESERVE src0_sel:WORD_0\n\t"
        "v_pk_mul_f16 v114, v114, v98\n\t"
        "v_exp_f32 v105, v105\n\t"
        "s_waitcnt lgkmcnt(1)\n\t"
        "v_mfma_f32_32x32x16_bf16 v[64:79], v[144:147], v[92:95], v[64:79]\n\t"
        "ds_read_b128 v[84:87], v174 offset:8704\n\t"
        "v_cvt_pk_f16_f32 v100, v104, v105\n\t"
        "v_rcp_f16_sdwa v99, v99 dst_sel:WORD_1 dst_unused:UNUSED_PRESERVE src0_sel:WORD_1\n\t"
        "v_pk_add_f16 v100, v100, s96\n\t"
        "v_exp_f32 v106, v106\n\t"
        "v_mfma_f32_32x32x16_bf16 v[64:79], v[148:151], v[200:203], v[64:79]\n\t"
        "v_cvt_pk_f16_f32 v115, v118, v119\n\t"
        "v_rcp_f16_sdwa v100, v100 dst_sel:WORD_0 dst_unused:UNUSED_PRESERVE src0_sel:WORD_0\n\t"
        "v_pk_mul_f16 v115, v115, v99\n\t"
        "v_exp_f32 v107, v107\n\t"
        "s_waitcnt lgkmcnt(1)\n\t"
        "v_mfma_f32_32x32x16_bf16 v[64:79], v[152:155], v[80:83], v[64:79]\n\t"
        "v_cvt_pk_f16_f32 v101, v106, v107\n\t"
        "v_rcp_f16_sdwa v100, v100 dst_sel:WORD_1 dst_unused:UNUSED_PRESERVE src0_sel:WORD_1\n\t"
        "v_pk_add_f16 v101, v101, s96\n\t"
        "v_exp_f32 v108, v108\n\t"
        "v_mfma_f32_32x32x16_bf16 v[64:79], v[156:159], v[204:207], v[64:79]\n\t"
        "v_cvt_pk_f16_f32 v116, v120, v121\n\t"
        "v_rcp_f16_sdwa v101, v101 dst_sel:WORD_0 dst_unused:UNUSED_PRESERVE src0_sel:WORD_0\n\t"
        "v_pk_mul_f16 v116, v116, v100\n\t"
        "v_exp_f32 v109, v109\n\t"
        "s_waitcnt lgkmcnt(0)\n\t"
        "v_mfma_f32_32x32x16_bf16 v[80:95], v[144:147], v[84:87], 0\n\t"
        "v_cvt_pk_f16_f32 v102, v108, v109\n\t"
        "v_rcp_f16_sdwa v101, v101 dst_sel:WORD_1 dst_unused:UNUSED_PRESERVE src0_sel:WORD_1\n\t"
        "v_pk_add_f16 v102, v102, s96\n\t"
        "v_exp_f32 v110, v110\n\t"
        "v_mfma_f32_32x32x16_bf16 v[80:95], v[148:151], v[208:211], v[80:95]\n\t"
        "v_cvt_pk_f16_f32 v117, v122, v123\n\t"
        "v_rcp_f16_sdwa v102, v102 dst_sel:WORD_0 dst_unused:UNUSED_PRESERVE src0_sel:WORD_0\n\t"
        "v_pk_mul_f16 v117, v117, v101\n\t"
        "v_exp_f32 v111, v111\n\t"
        "v_mfma_f32_32x32x16_bf16 v[80:95], v[152:155], v[212:215], v[80:95]\n\t"
        "v_cvt_pk_f16_f32 v103, v110, v111\n\t"
        "v_rcp_f16_sdwa v102, v102 dst_sel:WORD_1 dst_unused:UNUSED_PRESERVE src0_sel:WORD_1\n\t"
        "v_pk_add_f16 v103, v103, s96\n\t"
        "v_exp_f32 v64, v64\n\t"
        "v_exp_f32 v65, v65\n\t"
        "v_mfma_f32_32x32x16_bf16 v[80:95], v[156:159], v[216:219], v[80:95]\n\t"
        "v_cvt_pk_f16_f32 v118, v124, v125\n\t"
        "v_rcp_f16_sdwa v103, v103 dst_sel:WORD_0 dst_unused:UNUSED_PRESERVE src0_sel:WORD_0\n\t"
        "v_pk_mul_f16 v118, v118, v102\n\t"
        "v_exp_f32 v66, v66\n\t"
        "v_cvt_pk_f16_f32 v64, v64, v65\n\t"
        "v_mfma_f32_32x32x16_f16 v[16:31], v[96:99], v[176:179], v[16:31]\n\t"
        "s_setprio 0\n\t"
        "ds_read_b128 v[120:123], v160 offset:128\n\t"
        "ds_read_u16 v124, v161 offset:256\n\t"
        "v_rcp_f16_sdwa v103, v103 dst_sel:WORD_1 dst_unused:UNUSED_PRESERVE src0_sel:WORD_1\n\t"
        "v_cvt_pk_f16_f32 v119, v126, v127\n\t"
        "v_pk_add_f16 v64, v64, s96\n\t"
        "v_exp_f32 v67, v67\n\t"
        "v_rcp_f16_sdwa v64, v64 dst_sel:WORD_0 dst_unused:UNUSED_PRESERVE src0_sel:WORD_0\n\t"
        "v_mfma_f32_32x32x16_f16 v[48:63], v[112:115], v[176:179], v[48:63]\n\t"
        "v_pk_mul_f16 v119, v119, v103\n\t"
        "v_cvt_pk_f16_f32 v65, v66, v67\n\t"
        "v_rcp_f16_sdwa v64, v64 dst_sel:WORD_1 dst_unused:UNUSED_PRESERVE src0_sel:WORD_1\n\t"
        "v_pk_add_f16 v65, v65, s96\n\t"
        "v_exp_f32 v68, v68\n\t"
        "v_mfma_f32_32x32x16_f16 v[16:31], v[100:103], v[180:183], v[16:31]\n\t"
        "v_cvt_pk_f16_f32 v80, v80, v81\n\t"
        "v_rcp_f16_sdwa v65, v65 dst_sel:WORD_0 dst_unused:UNUSED_PRESERVE src0_sel:WORD_0\n\t"
        "v_pk_mul_f16 v80, v80, v64\n\t"
        "v_exp_f32 v69, v69\n\t"
        "v_mfma_f32_32x32x16_f16 v[48:63], v[116:119], v[180:183], v[48:63]\n\t"
        "v_cvt_pk_f16_f32 v66, v68, v69\n\t"
        "v_rcp_f16_sdwa v65, v65 dst_sel:WORD_1 dst_unused:UNUSED_PRESERVE src0_sel:WORD_1\n\t"
        "v_pk_add_f16 v66, v66, s96\n\t"
        "v_exp_f32 v70, v70\n\t"
        "v_mfma_f32_32x32x16_f16 v[96:111], v[184:187], v[132:135], 0\n\t"
        "s_waitcnt lgkmcnt(1)\n\t"
        "v_mad_u32_u16 v120, v120, s95, v167 op_sel:[1,0,0,0]\n\t"
        "ds_read_b64_tr_b16 v[176:177], v120\n\t"
        "v_mad_u32_u16 v121, v121, s95, v167 op_sel:[1,0,0,0]\n\t"
        "ds_read_b64_tr_b16 v[178:179], v121\n\t"
        "v_mad_u32_u16 v122, v122, s95, v167 op_sel:[1,0,0,0]\n\t"
        "ds_read_b64_tr_b16 v[180:181], v122\n\t"
        "v_mad_u32_u16 v123, v123, s95, v167 op_sel:[1,0,0,0]\n\t"
        "ds_read_b64_tr_b16 v[182:183], v123\n\t"
        "v_cvt_pk_f16_f32 v81, v82, v83\n\t"
        "v_rcp_f16_sdwa v66, v66 dst_sel:WORD_0 dst_unused:UNUSED_PRESERVE src0_sel:WORD_0\n\t"
        "v_pk_mul_f16 v81, v81, v65\n\t"
        "v_exp_f32 v71, v71\n\t"
        "v_mfma_f32_32x32x16_f16 v[96:111], v[188:191], v[140:143], v[96:111]\n\t"
        "s_waitcnt lgkmcnt(4)\n\t"
        "v_mad_u32_u24 v124, v124, s95, v168\n\t"
        "ds_read_b128 v[184:187], v124\n\t"
        "ds_read_b128 v[188:191], v124 offset:16\n\t"
        "v_cvt_pk_f16_f32 v67, v70, v71\n\t"
        "v_rcp_f16_sdwa v66, v66 dst_sel:WORD_1 dst_unused:UNUSED_PRESERVE src0_sel:WORD_1\n\t"
        "v_pk_add_f16 v67, v67, s96\n\t"
        "v_exp_f32 v72, v72\n\t"
        "v_mfma_f32_32x32x16_bf16 v[96:111], v[192:195], v[220:223], v[96:111]\n\t"
        "s_waitcnt vmcnt(9)\n\t"
        "ds_read_b128 v[192:195], v173\n\t"
        "ds_read_b32 v125, v163 offset:512\n\t"
        "v_cvt_pk_f16_f32 v82, v84, v85\n\t"
        "v_rcp_f16_sdwa v67, v67 dst_sel:WORD_0 dst_unused:UNUSED_PRESERVE src0_sel:WORD_0\n\t"
        "v_pk_mul_f16 v82, v82, v66\n\t"
        "v_exp_f32 v73, v73\n\t"
        "v_mfma_f32_32x32x16_bf16 v[96:111], v[144:147], v[224:227], v[96:111]\n\t"
        "v_cvt_pk_f16_f32 v68, v72, v73\n\t"
        "v_rcp_f16_sdwa v67, v67 dst_sel:WORD_1 dst_unused:UNUSED_PRESERVE src0_sel:WORD_1\n\t"
        "v_pk_add_f16 v68, v68, s96\n\t"
        "v_exp_f32 v74, v74\n\t"
        "v_mfma_f32_32x32x16_bf16 v[96:111], v[148:151], v[228:231], v[96:111]\n\t"
        "v_cvt_pk_f16_f32 v83, v86, v87\n\t"
        "v_rcp_f16_sdwa v68, v68 dst_sel:WORD_0 dst_unused:UNUSED_PRESERVE src0_sel:WORD_0\n\t"
        "v_pk_mul_f16 v83, v83, v67\n\t"
        "v_exp_f32 v75, v75\n\t"
        "v_mfma_f32_32x32x16_bf16 v[96:111], v[152:155], v[232:235], v[96:111]\n\t"
        "s_add_i32 m0, %[ring], 4096\n\t"
        "s_waitcnt lgkmcnt(0)\n\t"
        "v_add_u32 v125, v125, v166\n\t"
        "global_load_lds_dwordx4 v125, %[es]\n\t"
        "v_cvt_pk_f16_f32 v69, v74, v75\n\t"
        "v_rcp_f16_sdwa v68, v68 dst_sel:WORD_1 dst_unused:UNUSED_PRESERVE src0_sel:WORD_1\n\t"
        "v_pk_add_f16 v69, v69, s96\n\t"
        "v_exp_f32 v76, v76\n\t"
        "v_mfma_f32_32x32x16_bf16 v[96:111], v[156:159], v[236:239], v[96:111]\n\t"
        "v_cvt_pk_f16_f32 v84, v88, v89\n\t"
        "v_rcp_f16_sdwa v69, v69 dst_sel:WORD_0 dst_unused:UNUSED_PRESERVE src0_sel:WORD_0\n\t"
        "v_pk_mul_f16 v84, v84, v68\n\t"
        "v_exp_f32 v77, v77\n\t"
        "v_mfma_f32_32x32x16_bf16 v[112:127], v[144:147], v[240:243], 0\n\t"
        "s_waitcnt vmcnt(6)\n\t"
        "ds_read_b128 v[144:147], v169\n\t"
        "v_cvt_pk_f16_f32 v70, v76, v77\n\t"
        "v_rcp_f16_sdwa v69, v69 dst_sel:WORD_1 dst_unused:UNUSED_PRESERVE src0_sel:WORD_1\n\t"
        "v_pk_add_f16 v70, v70, s96\n\t"
        "v_exp_f32 v78, v78\n\t"
        "v_mfma_f32_32x32x16_bf16 v[112:127], v[148:151], v[244:247], v[112:127]\n\t"
        "ds_read_b128 v[148:151], v170\n\t"
        "s_sub_u32 %[nt], %[nt], 1\n\t"
        "v_cvt_pk_f16_f32 v85, v90, v91\n\t"
        "v_rcp_f16_sdwa v70, v70 dst_sel:WORD_0 dst_unused:UNUSED_PRESERVE src0_sel:WORD_0\n\t"
        "v_pk_mul_f16 v85, v85, v69\n\t"
        "v_exp_f32 v79, v79\n\t"
        "v_mfma_f32_32x32x16_bf16 v[112:127], v[152:155], v[248:251], v[112:127]\n\t"
        "ds_read_b128 v[152:155], v171\n\t"
        "ds_read_b128 v[88:91], v162 offset:512\n\t"
        "v_cvt_pk_f16_f32 v71, v78, v79\n\t"
        "v_rcp_f16_sdwa v70, v70 dst_sel:WORD_1 dst_unused:UNUSED_PRESERVE src0_sel:WORD_1\n\t"
        "v_pk_add_f16 v71, v71, s96\n\t"
        "v_exp_f32 v96, v96\n\t"
        "v_exp_f32 v97, v97\n\t"
        "v_mfma_f32_32x32x16_bf16 v[112:127], v[156:159], v[252:255], v[112:127]\n\t"
        "ds_read_b128 v[156:159], v172\n\t"
        "v_cvt_pk_f16_f32 v86, v92, v93\n\t"
        "v_rcp_f16_sdwa v71, v71 dst_sel:WORD_0 dst_unused:UNUSED_PRESERVE src0_sel:WORD_0\n\t"
        "v_pk_mul_f16 v86, v86, v70\n\t"
        "v_exp_f32 v98, v98\n\t"
        "v_cvt_pk_f16_f32 v96, v96, v97\n\t"
        "s_cbranch_scc1 .Lrl_%=\n\t"
        "v_mfma_f32_32x32x16_f16 v[0:15], v[64:67], v[176:179], v[0:15]\n\t"
        "v_rcp_f16_sdwa v71, v71 dst_sel:WORD_1 dst_unused:UNUSED_PRESERVE src0_sel:WORD_1\n\t"
        "v_cvt_pk_f16_f32 v87, v94, v95\n\t"
        "v_pk_add_f16 v96, v96, s96\n\t"
        "v_exp_f32 v99, v99\n\t"
        "v_rcp_f16_sdwa v96, v96 dst_sel:WORD_0 dst_unused:UNUSED_PRESERVE src0_sel:WORD_0\n\t"
        "v_mfma_f32_32x32x16_f16 v[32:47], v[80:83], v[176:179], v[32:47]\n\t"
        "s_mov_b32 m0, %[ring]\n\t"
        "s_waitcnt lgkmcnt(1)\n\t"
        "v_mad_u32_u16 v88, v88, s94, v164\n\t"
        "s_waitcnt lgkmcnt(0)\n\t"
        "global_load_lds_dwordx4 v88, %[x]\n\t"
        "v_pk_mul_f16 v87, v87, v71\n\t"
        "v_cvt_pk_f16_f32 v97, v98, v99\n\t"
        "v_rcp_f16_sdwa v96, v96 dst_sel:WORD_1 dst_unused:UNUSED_PRESERVE src0_sel:WORD_1\n\t"
        "v_pk_add_f16 v97, v97, s96\n\t"
        "v_exp_f32 v100, v100\n\t"
        "v_mfma_f32_32x32x16_f16 v[0:15], v[68:71], v[180:183], v[0:15]\n\t"
        "ds_read_b128 v[92:95], v174 offset:512\n\t"
        "s_add_i32 m0, %[ring], 1024\n\t"
        "v_mad_u32_u16 v89, v89, s94, v165\n\t"
        "global_load_lds_dwordx4 v89, %[x]\n\t"
        "v_cvt_pk_f16_f32 v112, v112, v113\n\t"
        "v_rcp_f16_sdwa v97, v97 dst_sel:WORD_0 dst_unused:UNUSED_PRESERVE src0_sel:WORD_0\n\t"
        "v_pk_mul_f16 v112, v112, v96\n\t"
        "v_exp_f32 v101, v101\n\t"
        "v_mfma_f32_32x32x16_f16 v[32:47], v[84:87], v[180:183], v[32:47]\n\t"
        "s_add_i32 m0, %[ring], 2048\n\t"
        "v_mad_u32_u16 v90, v90, s94, v164\n\t"
        "global_load_lds_dwordx4 v90, %[x]\n\t"
        "v_cvt_pk_f16_f32 v98, v100, v101\n\t"
        "v_rcp_f16_sdwa v97, v97 dst_sel:WORD_1 dst_unused:UNUSED_PRESERVE src0_sel:WORD_1\n\t"
        "v_pk_add_f16 v98, v98, s96\n\t"
        "v_exp_f32 v102, v102\n\t"
        "v_mfma_f32_32x32x16_f16 v[64:79], v[184:187], v[128:131], 0\n\t"
        "s_add_i32 m0, %[ring], 3072\n\t"
        "v_mad_u32_u16 v91, v91, s94, v165\n\t"
        "global_load_lds_dwordx4 v91, %[x]\n\t"
        "v_cvt_pk_f16_f32 v113, v114, v115\n\t"
        "v_rcp_f16_sdwa v98, v98 dst_sel:WORD_0 dst_unused:UNUSED_PRESERVE src0_sel:WORD_0\n\t"
        "v_pk_mul_f16 v113, v113, v97\n\t"
        "v_exp_f32 v103, v103\n\t"
        "v_mfma_f32_32x32x16_f16 v[64:79], v[188:191], v[136:139], v[64:79]\n\t"
        "ds_read_b128 v[80:83], v174 offset:2560\n\t"
        "v_cvt_pk_f16_f32 v99, v102, v103\n\t"
        "v_rcp_f16_sdwa v98, v98 dst_sel:WORD_1 dst_unused:UNUSED_PRESERVE src0_sel:WORD_1\n\t"
        "v_pk_add_f16 v99, v99, s96\n\t"
        "v_exp_f32 v104, v104\n\t"
        "v_mfma_f32_32x32x16_bf16 v[64:79], v[192:195], v[196:199], v[64:79]\n\t"
        "v_cvt_pk_f16_f32 v114, v116, v117\n\t"
        "v_rcp_f16_sdwa v99, v99 dst_sel:WORD_0 dst_unused:UNUSED_PRESERVE src0_sel:WORD_0\n\t"
        "v_pk_mul_f16 v114, v114, v98\n\t"
        "v_exp_f32 v105, v105\n\t"
        "s_waitcnt lgkmcnt(1)\n\t"
        "v_mfma_f32_32x32x16_bf16 v[64:79], v[144:147], v[92:95], v[64:79]\n\t"
        "ds_read_b128 v[84:87], v174 offset:8704\n\t"
        "v_cvt_pk_f16_f32 v100, v104, v105\n\t"
        "v_rcp_f16_sdwa v99, v99 dst_sel:WORD_1 dst_unused:UNUSED_PRESERVE src0_sel:WORD_1\n\t"
        "v_pk_add_f16 v100, v100, s96\n\t"
        "v_exp_f32 v106, v106\n\t"
        "v_mfma_f32_32x32x16_bf16 v[64:79], v[148:151], v[200:203], v[64:79]\n\t"
        "v_cvt_pk_f16_f32 v115, v118, v119\n\t"
        "v_rcp_f16_sdwa v100, v100 dst_sel:WORD_0 dst_unused:UNUSED_PRESERVE src0_sel:WORD_0\n\t"
        "v_pk_mul_f16 v115, v115, v99\n\t"
        "v_exp_f32 v107, v107\n\t"
        "s_waitcnt lgkmcnt(1)\n\t"
        "v_mfma_f32_32x32x16_bf16 v[64:79], v[152:155], v[80:83], v[64:79]\n\t"
        "v_cvt_pk_f16_f32 v101, v106, v107\n\t"
        "v_rcp_f16_sdwa v100, v100 dst_sel:WORD_1 dst_unused:UNUSED_PRESERVE src0_sel:WORD_1\n\t"
        "v_pk_add_f16 v101, v101, s96\n\t"
        "v_exp_f32 v108, v108\n\t"
        "v_mfma_f32_32x32x16_bf16 v[64:79], v[156:159], v[204:207], v[64:79]\n\t"
        "v_cvt_pk_f16_f32 v116, v120, v121\n\t"
        "v_rcp_f16_sdwa v101, v101 dst_sel:WORD_0 dst_unused:UNUSED_PRESERVE src0_sel:WORD_0\n\t"
        "v_pk_mul_f16 v116, v116, v100\n\t"
        "v_exp_f32 v109, v109\n\t"
        "s_waitcnt lgkmcnt(0)\n\t"
        "v_mfma_f32_32x32x16_bf16 v[80:95], v[144:147], v[84:87], 0\n\t"
        "v_add_u32 v160, 0x100, v160\n\t"
        "v_cvt_pk_f16_f32 v102, v108, v109\n\t"
        "v_rcp_f16_sdwa v101, v101 dst_sel:WORD_1 dst_unused:UNUSED_PRESERVE src0_sel:WORD_1\n\t"
        "v_pk_add_f16 v102, v102, s96\n\t"
        "v_exp_f32 v110, v110\n\t"
        "v_mfma_f32_32x32x16_bf16 v[80:95], v[148:151], v[208:211], v[80:95]\n\t"
        "v_add_u32 v161, 0x100, v161\n\t"
        "v_cvt_pk_f16_f32 v117, v122, v123\n\t"
        "v_rcp_f16_sdwa v102, v102 dst_sel:WORD_0 dst_unused:UNUSED_PRESERVE src0_sel:WORD_0\n\t"
        "v_pk_mul_f16 v117, v117, v101\n\t"
        "v_exp_f32 v111, v111\n\t"
        "v_mfma_f32_32x32x16_bf16 v[80:95], v[152:155], v[212:215], v[80:95]\n\t"
        "v_add_u32 v162, 0x100, v162\n\t"
        "v_cvt_pk_f16_f32 v103, v110, v111\n\t"
        "v_rcp_f16_sdwa v102, v102 dst_sel:WORD_1 dst_unused:UNUSED_PRESERVE src0_sel:WORD_1\n\t"
        "v_pk_add_f16 v103, v103, s96\n\t"
        "v_exp_f32 v64, v64\n\t"
        "v_exp_f32 v65, v65\n\t"
        "v_mfma_f32_32x32x16_bf16 v[80:95], v[156:159], v[216:219], v[80:95]\n\t"
        "v_add_u32 v163, 0x100, v163\n\t"
        "v_cvt_pk_f16_f32 v118, v124, v125\n\t"
        "v_rcp_f16_sdwa v103, v103 dst_sel:WORD_0 dst_unused:UNUSED_PRESERVE src0_sel:WORD_0\n\t"
        "v_pk_mul_f16 v118, v118, v102\n\t"
        "v_exp_f32 v66, v66\n\t"
        "v_cvt_pk_f16_f32 v64, v64, v65\n\t"
        "s_branch .Lrt_%=\n\t"
        ".Lrl_%=:\n\t"
        "v_mfma_f32_32x32x16_f16 v[0:15], v[64:67], v[176:179], v[0:15]\n\t"
        "ds_read_b64_tr_b16 v[184:185], v175\n\t"
        "ds_read_b64_tr_b16 v[186:187], v175 offset:512\n\t"
        "ds_read_b64_tr_b16 v[188:189], v175 offset:1024\n\t"
        "ds_read_b64_tr_b16 v[190:191], v175 offset:1536\n\t"
        "v_rcp_f16_sdwa v71, v71 dst_sel:WORD_1 dst_unused:UNUSED_PRESERVE src0_sel:WORD_1\n\t"
        "v_cvt_pk_f16_f32 v87, v94, v95\n\t"
        "v_pk_add_f16 v96, v96, s96\n\t"
        "v_exp_f32 v99, v99\n\t"
        "v_rcp_f16_sdwa v96, v96 dst_sel:WORD_0 dst_unused:UNUSED_PRESERVE src0_sel:WORD_0\n\t"
        "v_mfma_f32_32x32x16_f16 v[32:47], v[80:83], v[176:179], v[32:47]\n\t"
        "ds_read_b64_tr_b16 v[192:193], v175 offset:2048\n\t"
        "ds_read_b64_tr_b16 v[194:195], v175 offset:2560\n\t"
        "ds_read_b64_tr_b16 v[196:197], v175 offset:3072\n\t"
        "ds_read_b64_tr_b16 v[198:199], v175 offset:3584\n\t"
        "v_pk_mul_f16 v87, v87, v71\n\t"
        "v_cvt_pk_f16_f32 v97, v98, v99\n\t"
        "v_rcp_f16_sdwa v96, v96 dst_sel:WORD_1 dst_unused:UNUSED_PRESERVE src0_sel:WORD_1\n\t"
        "v_pk_add_f16 v97, v97, s96\n\t"
        "v_exp_f32 v100, v100\n\t"
        "v_mfma_f32_32x32x16_f16 v[0:15], v[68:71], v[180:183], v[0:15]\n\t"
        "ds_read_b64_tr_b16 v[200:201], v175 offset:32\n\t"
        "ds_read_b64_tr_b16 v[202:203], v175 offset:544\n\t"
        "ds_read_b64_tr_b16 v[204:205], v175 offset:1056\n\t"
        "ds_read_b64_tr_b16 v[206:207], v175 offset:1568\n\t"
        "s_waitcnt lgkmcnt(14)\n\t"
        "v_lshlrev_b32 v216, 16, v144\n\t"
        "v_and_b32 v217, 0xffff0000, v144\n\t"
        "v_cvt_pk_f16_f32 v112, v112, v113\n\t"
        "v_rcp_f16_sdwa v97, v97 dst_sel:WORD_0 dst_unused:UNUSED_PRESERVE src0_sel:WORD_0\n\t"
        "v_pk_mul_f16 v112, v112, v96\n\t"
        "v_exp_f32 v101, v101\n\t"
        "v_mfma_f32_32x32x16_f16 v[32:47], v[84:87], v[180:183], v[32:47]\n\t"
        "ds_read_b64_tr_b16 v[208:209], v175 offset:2080\n\t"
        "ds_read_b64_tr_b16 v[210:211], v175 offset:2592\n\t"
        "ds_read_b64_tr_b16 v[212:213], v175 offset:3104\n\t"
        "ds_read_b64_tr_b16 v[214:215], v175 offset:3616\n\t"
        "v_lshlrev_b32 v218, 16, v145\n\t"
        "v_and_b32 v219, 0xffff0000, v145\n\t"
        "v_cvt_pk_f16_f32 v98, v100, v101\n\t"
        "v_rcp_f16_sdwa v97, v97 dst_sel:WORD_1 dst_unused:UNUSED_PRESERVE src0_sel:WORD_1\n\t"
        "v_pk_add_f16 v98, v98, s96\n\t"
        "v_exp_f32 v102, v102\n\t"
        "v_lshlrev_b32 v220, 16, v146\n\t"
        "v_and_b32 v221, 0xffff0000, v146\n\t"
        "v_lshlrev_b32 v222, 16, v147\n\t"
        "v_and_b32 v223, 0xffff0000, v147\n\t"
        "s_waitcnt lgkmcnt(14)\n\t"
        "v_lshlrev_b32 v224, 16, v148\n\t"
        "v_and_b32 v225, 0xffff0000, v148\n\t"
        "v_cvt_pk_f16_f32 v113, v114, v115\n\t"
        "v_rcp_f16_sdwa v98, v98 dst_sel:WORD_0 dst_unused:UNUSED_PRESERVE src0_sel:WORD_0\n\t"
        "v_pk_mul_f16 v113, v113, v97\n\t"
        "v_exp_f32 v103, v103\n\t"
        "v_lshlrev_b32 v226, 16, v149\n\t"
        "v_and_b32 v227, 0xffff0000, v149\n\t"
        "v_lshlrev_b32 v228, 16, v150\n\t"
        "v_and_b32 v229, 0xffff0000, v150\n\t"
        "v_lshlrev_b32 v230, 16, v151\n\t"
        "v_and_b32 v231, 0xffff0000, v151\n\t"
        "v_cvt_pk_f16_f32 v99, v102, v103\n\t"
        "v_rcp_f16_sdwa v98, v98 dst_sel:WORD_1 dst_unused:UNUSED_PRESERVE src0_sel:WORD_1\n\t"
        "v_pk_add_f16 v99, v99, s96\n\t"
        "v_exp_f32 v104, v104\n\t"
        "s_waitcnt lgkmcnt(14)\n\t"
        "v_mfma_f32_32x32x16_bf16 v[64:79], v[184:187], v[144:147], v[216:231]\n\t"
        "v_lshlrev_b32 v232, 16, v152\n\t"
        "v_and_b32 v233, 0xffff0000, v152\n\t"
        "v_cvt_pk_f16_f32 v114, v116, v117\n\t"
        "v_rcp_f16_sdwa v99, v99 dst_sel:WORD_0 dst_unused:UNUSED_PRESERVE src0_sel:WORD_0\n\t"
        "v_pk_mul_f16 v114, v114, v98\n\t"
        "v_exp_f32 v105, v105\n\t"
        "s_waitcnt lgkmcnt(12)\n\t"
        "v_mfma_f32_32x32x16_bf16 v[64:79], v[188:191], v[148:151], v[64:79]\n\t"
        "v_lshlrev_b32 v234, 16, v153\n\t"
        "v_and_b32 v235, 0xffff0000, v153\n\t"
        "v_cvt_pk_f16_f32 v100, v104, v105\n\t"
        "v_rcp_f16_sdwa v99, v99 dst_sel:WORD_1 dst_unused:UNUSED_PRESERVE src0_sel:WORD_1\n\t"
        "v_pk_add_f16 v100, v100, s96\n\t"
        "v_exp_f32 v106, v106\n\t"
        "s_waitcnt lgkmcnt(10)\n\t"
        "v_mfma_f32_32x32x16_bf16 v[64:79], v[192:195], v[152:155], v[64:79]\n\t"
        "v_lshlrev_b32 v236, 16, v154\n\t"
        "v_and_b32 v237, 0xffff0000, v154\n\t"
        "v_cvt_pk_f16_f32 v115, v118, v119\n\t"
        "v_rcp_f16_sdwa v100, v100 dst_sel:WORD_0 dst_unused:UNUSED_PRESERVE src0_sel:WORD_0\n\t"
        "v_pk_mul_f16 v115, v115, v99\n\t"
        "v_exp_f32 v107, v107\n\t"
        "s_waitcnt lgkmcnt(8)\n\t"
        "v_mfma_f32_32x32x16_bf16 v[64:79], v[196:199], v[156:159], v[64:79]\n\t"
        "v_lshlrev_b32 v238, 16, v155\n\t"
        "v_and_b32 v239, 0xffff0000, v155\n\t"
        "v_cvt_pk_f16_f32 v101, v106, v107\n\t"
        "v_rcp_f16_sdwa v100, v100 dst_sel:WORD_1 dst_unused:UNUSED_PRESERVE src0_sel:WORD_1\n\t"
        "v_pk_add_f16 v101, v101, s96\n\t"
        "v_exp_f32 v108, v108\n\t"
        "v_lshlrev_b32 v240, 16, v156\n\t"
        "v_and_b32 v241, 0xffff0000, v156\n\t"
        "v_lshlrev_b32 v242, 16, v157\n\t"
        "v_and_b32 v243, 0xffff0000, v157\n\t"
        "v_lshlrev_b32 v244, 16, v158\n\t"
        "v_and_b32 v245, 0xffff0000, v158\n\t"
        "v_lshlrev_b32 v246, 16, v159\n\t"
        "v_and_b32 v247, 0xffff0000, v159\n\t"
        "v_cvt_pk_f16_f32 v116, v120, v121\n\t"
        "v_rcp_f16_sdwa v101, v101 dst_sel:WORD_0 dst_unused:UNUSED_PRESERVE src0_sel:WORD_0\n\t"
        "v_pk_mul_f16 v116, v116, v100\n\t"
        "v_exp_f32 v109, v109\n\t"
        "s_waitcnt lgkmcnt(6)\n\t"
        "v_mfma_f32_32x32x16_bf16 v[80:95], v[200:203], v[144:147], v[232:247]\n\t"
        "v_cvt_pk_f16_f32 v102, v108, v109\n\t"
        "v_rcp_f16_sdwa v101, v101 dst_sel:WORD_1 dst_unused:UNUSED_PRESERVE src0_sel:WORD_1\n\t"
        "v_pk_add_f16 v102, v102, s96\n\t"
        "v_exp_f32 v110, v110\n\t"
        "s_waitcnt lgkmcnt(4)\n\t"
        "v_mfma_f32_32x32x16_bf16 v[80:95], v[204:207], v[148:151], v[80:95]\n\t"
        "v_cvt_pk_f16_f32 v117, v122, v123\n\t"
        "v_rcp_f16_sdwa v102, v102 dst_sel:WORD_0 dst_unused:UNUSED_PRESERVE src0_sel:WORD_0\n\t"
        "v_pk_mul_f16 v117, v117, v101\n\t"
        "v_exp_f32 v111, v111\n\t"
        "s_waitcnt lgkmcnt(2)\n\t"
        "v_mfma_f32_32x32x16_bf16 v[80:95], v[208:211], v[152:155], v[80:95]\n\t"
        "v_cvt_pk_f16_f32 v103, v110, v111\n\t"
        "v_rcp_f16_sdwa v102, v102 dst_sel:WORD_1 dst_unused:UNUSED_PRESERVE src0_sel:WORD_1\n\t"
        "v_pk_add_f16 v103, v103, s96\n\t"
        "s_waitcnt lgkmcnt(0)\n\t"
        "v_mfma_f32_32x32x16_bf16 v[80:95], v[212:215], v[156:159], v[80:95]\n\t"
        "v_cvt_pk_f16_f32 v118, v124, v125\n\t"
        "v_rcp_f16_sdwa v103, v103 dst_sel:WORD_0 dst_unused:UNUSED_PRESERVE src0_sel:WORD_0\n\t"
        "v_pk_mul_f16 v118, v118, v102\n\t"
        "v_mfma_f32_32x32x16_f16 v[16:31], v[96:99], v[176:179], v[16:31]\n\t"
        "v_rcp_f16_sdwa v103, v103 dst_sel:WORD_1 dst_unused:UNUSED_PRESERVE src0_sel:WORD_1\n\t"
        "v_cvt_pk_f16_f32 v119, v126, v127\n\t"
        "v_mfma_f32_32x32x16_f16 v[48:63], v[112:115], v[176:179], v[48:63]\n\t"
        "v_pk_mul_f16 v119, v119, v103\n\t"
        "v_mfma_f32_32x32x16_f16 v[16:31], v[100:103], v[180:183], v[16:31]\n\t"
        "s_nop 0\n\t"
        "v_mfma_f32_32x32x16_f16 v[48:63], v[116:119], v[180:183], v[48:63]\n\t"
        : "+{v[0:15]}"(Y[0]), "+{v[16:31]}"(Y[1]), "+{v[32:47]}"(Y[2]), "+{v[48:63]}"(Y[3]), "={v[64:79]}"(U[0]),
          "={v[80:95]}"(U[1]), "={v[216:231]}"(X[0]), "={v[232:247]}"(X[1]), "+{v[160:175]}"(AD), [nt] "+s"(nt)
        : "{v[128:143]}"(XAf), [x] "s"(x), [es] "s"(ES), [ring] "s"(ring0), [wave] "s"(wave), [flag] "s"(mode),
          [cu] "n"(cu)
        :
          "memory", "scc", "m0", "s94", "s95", "s96", "v96", "v97", "v98", "v99", "v100", "v101", "v102", "v103",
          "v104", "v105", "v106", "v107", "v108", "v109", "v110", "v111", "v112", "v113", "v114", "v115", "v116",
          "v117", "v118", "v119", "v120", "v121", "v122", "v123", "v124", "v125", "v126", "v127", "v144", "v145",
          "v146", "v147", "v148", "v149", "v150", "v151", "v152", "v153", "v154", "v155", "v156", "v157", "v158",
          "v159", "v176", "v177", "v178", "v179", "v180", "v181", "v182", "v183", "v184", "v185", "v186", "v187",
          "v188", "v189", "v190", "v191", "v192", "v193", "v194", "v195", "v196", "v197", "v198", "v199", "v200",
          "v201", "v202", "v203", "v204", "v205", "v206", "v207", "v208", "v209", "v210", "v211", "v212", "v213",
          "v214", "v215", "v248", "v249", "v250", "v251", "v252", "v253", "v254", "v255");
  }
  SB();
  auto wide = [&]() __attribute__((always_inline)) {
    int l2;
    asm volatile("v_mbcnt_lo_u32_b32 %0, -1, 0\n\tv_mbcnt_hi_u32_b32 %0, -1, %0\n\ts_waitcnt vmcnt(0)" : "=v"(l2) :: "memory");
    AD[0] = la + (4 * (l2 >> 5) + ((l2 >> 2) & 3)) * 16;
    AD[1] = la + (((l2 & 7) << 2) | ((l2 >> 3) & 3)) * 4 + 2;
    AD[2] = la + (l2 >> 3) * 16;
    AD[3] = la + LARR + ((((l2 >> 1) & 7) << 2) | (l2 >> 4)) * 4;
    auto rd = [&](unsigned a) { return *(const LAS bf16x8*)(size_t)a; };
    auto one = [&](bf16x8 v) {
      const u32x4 t = __builtin_bit_cast(u32x4, v);
      return __builtin_bit_cast(bf16x8, t | (t >> 3));
    };
    bf16x8 xq[4];
    {
      f32x16 T[2] = {zero16, zero16};
#pragma unroll
      for (int k = 0; k < 4; ++k) {
        u32x4 t;
#pragma unroll
        for (int j = 0; j < 4; ++j) t[j] = pack_bf16(X[k >> 1][8 * (k & 1) + 2 * j], X[k >> 1][8 * (k & 1) + 2 * j + 1]);
#pragma unroll
        for (int ct = 0; ct < 2; ++ct)
          T[ct] = MFMA32(__builtin_bit_cast(bf16x8, t), rawfrag(AD[15] + (NBLOB - 18 + k) * 1024 + ct * 32), T[ct]);
      }
#pragma unroll
      for (int q = 0; q < 4; ++q) {
        u32x4 t;
#pragma unroll
        for (int j = 0; j < 4; ++j)
          t[j] = pack_bf16(T[q & 1][8 * (q >> 1) + 2 * j] * NLOG2E, T[q & 1][8 * (q >> 1) + 2 * j + 1] * NLOG2E);
        xq[q] = __builtin_bit_cast(bf16x8, t);
      }
    }
#pragma unroll
    for (int i = 0; i < 16; ++i) { Y[0][i] = 1e-6f * (1 << RA); Y[1][i] = 1e-6f * (1 << RA); Y[2][i] = 0.f; Y[3][i] = 0.f; }
#pragma unroll 1
    do {
      const int n = slow ? gen() : cnt;
      issue_gather(0, 0);
#pragma unroll 1
      for (int t = 0; t * 32 < n; ++t) {
        const unsigned ro = (t & 1) * ringd;
        asm volatile("s_waitcnt lgkmcnt(0)" ::: "memory");
        issue_gather(t + 1, ~t & 1);
        const u32x4 ss = ld128(AD[0] + t * 128);
        const unsigned st = *(const LAS unsigned short*)(size_t)(AD[1] + t * 128) * TPITCH + AD[8];
        s16x4 a[4];
#pragma unroll
        for (int q = 0; q < 4; ++q)
          a[q] = __builtin_amdgcn_ds_read_tr16_b64_v4i16((LAS s16x4*)(size_t)((ss[q] >> 16) * TPITCH + AD[7]));
        const bf16x8 S0 = one(__builtin_shufflevector(a[0], a[1], 0, 1, 2, 3, 4, 5, 6, 7));
        const bf16x8 S1 = one(__builtin_shufflevector(a[2], a[3], 0, 1, 2, 3, 4, 5, 6, 7));
        const bf16x8 T0 = one(rd(st)), T1 = one(rd(st + 16));
        asm volatile("s_waitcnt vmcnt(5)" ::: "memory");
#pragma unroll
        for (int ct = 0; ct < 2; ++ct) {
          const unsigned w = AD[14] + OFF_W + ct * 4096;
          f32x16 G = MFMA32(T0, xq[ct], zero16), V = zero16;
          G = MFMA32(T1, xq[2 + ct], G);
          G = MFMA32(rd(AD[13] + ro), rd(AD[14] + OFF_W + (16 + ct) * 1024), G);
#pragma unroll
          for (int k = 0; k < 4; ++k) {
            const bf16x8 as = rd(AD[9 + k] + ro);
            G = MFMA32(as, rd(w + k * 1024), G);
            V = MFMA32(as, rd(w + (8 + k) * 1024), V);
          }
          u32x4 pd[2], pn[2];
#pragma unroll
          for (int i = 0; i < 16; i += 2) {
            constexpr float u = 1.0f / (1 << RA);
            const float r0 = __builtin_amdgcn_rcpf(__builtin_fmaf(__builtin_amdgcn_exp2f(G[i]), u, u));
            const float r1 = __builtin_amdgcn_rcpf(__builtin_fmaf(__builtin_amdgcn_exp2f(G[i + 1]), u, u));
            pd[i >> 3][(i >> 1) & 3] = pack_bf16(r0, r1);
            pn[i >> 3][(i >> 1) & 3] = pack_bf16(r0 * V[i], r1 * V[i + 1]);
          }
          Y[ct] = MFMA32(__builtin_bit_cast(bf16x8, pd[0]), S0, Y[ct]);
          Y[ct] = MFMA32(__builtin_bit_cast(bf16x8, pd[1]), S1, Y[ct]);
          Y[2 + ct] = MFMA32(__builtin_bit_cast(bf16x8, pn[0]), S0, Y[2 + ct]);
          Y[2 + ct] = MFMA32(__builtin_bit_cast(bf16x8, pn[1]), S1, Y[2 + ct]);
          SB();
        }
      }
      asm volatile("s_waitcnt vmcnt(0) lgkmcnt(0)" ::: "memory");
    } while (slow && gs < GEND);
  };

  const unsigned ot = ring0 + (max(cnt - 1, 0) & 32 ? 0u : ringd);
  auto finish = [&](const bool check) __attribute__((always_inline)) {
    auto inv = [&](f32x16& y) {
      y *= VSC;
#pragma unroll
      for (int i = 0; i < 16; ++i) y[i] = __builtin_amdgcn_rcpf(y[i]);
    };
    inv(Y[0]);
    SB();
    int lane;
    asm volatile("v_mbcnt_lo_u32_b32 %0, -1, 0\n\tv_mbcnt_hi_u32_b32 %0, -1, %0" : "=v"(lane));
    const int h = lane >> 5;
    const unsigned nd = nodeof(cg + (lane & 31));
    const bool live = (nd * Mw + Cw) >> 16 == cg + (lane & 31);
    const unsigned lm = (unsigned)__ballot(live);
    const int nodes = __builtin_popcount(lm);
    const unsigned n0 = __builtin_amdgcn_readlane(nd, __builtin_ctz(lm | 0x80000000u));
    const int r = live ? nd - n0 : 31;
    SB();
    inv(Y[1]);
    unsigned ov[16];
#pragma unroll
    for (int p = 0; p < 4; ++p) {
      const int ct = p >> 1;
#pragma unroll
      for (int j = 0; j < 4; ++j) {
        const int i = 8 * (p & 1) + 2 * j;
        const f32x2 z = __builtin_elementwise_fma(f32x2{Y[2 + ct][i], Y[2 + ct][i + 1]}, f32x2{Y[ct][i], Y[ct][i + 1]},
                                                  f32x2{U[ct][i], U[ct][i + 1]});
        ov[4 * p + j] = pack_bf16(MAXN(z[0], X[ct][i]), MAXN(z[1], X[ct][i + 1]));
      }
      *(LAS u32x4*)(size_t)(ot + r * 128 + (((4 * h + p) ^ (r & 7)) * 16)) =
          u32x4{ov[4 * p], ov[4 * p + 1], ov[4 * p + 2], ov[4 * p + 3]};
    }
    u32x4 ow[4];
#pragma unroll
    for (int q = 0; q < 4; ++q)
      ow[q] = *(const LAS u32x4*)(size_t)(ot + (8 * q + (lane >> 3)) * 128 + (((lane & 7) ^ (lane >> 3)) * 16));
    asm volatile("v_cmpx_lt_i32 vcc, 0, %6\n\t"
                 "global_store_dwordx4 %0, %1, %5 sc1\n\t"
                 "v_cmpx_lt_i32 vcc, 8, %6\n\t"
                 "global_store_dwordx4 %0, %2, %5 offset:1024 sc1\n\t"
                 "v_cmpx_lt_i32 vcc, 16, %6\n\t"
                 "global_store_dwordx4 %0, %3, %5 offset:2048 sc1\n\t"
                 "v_cmpx_lt_i32 vcc, 24, %6\n\t"
                 "global_store_dwordx4 %0, %4, %5 offset:3072 sc1\n\t"
                 "s_mov_b64 exec, -1"
                 :: "v"((unsigned)n0 * 128u + lane * 16u), "v"(ow[0]), "v"(ow[1]), "v"(ow[2]), "v"(ow[3]), "s"(out),
                    "v"(nodes - (lane >> 3))
                 : "memory", "vcc");
    SB();
    if (!check) return false;
    constexpr float WLIM = 1 << (22 - RA + VK);
    float wm = MAXN(Y[0][0], Y[1][0]);
#pragma unroll
    for (int i = 1; i < 16; ++i) wm = MAXN(MAXN(wm, Y[0][i]), Y[1][i]);
    f16x2 om = __builtin_bit_cast(f16x2, ov[0]);
#pragma unroll
    for (int i = 1; i < 16; ++i) om = MAXN(om, __builtin_bit_cast(f16x2, ov[i]));
    const _Float16 o1 = MAXN(om[0], om[1]);
    const unsigned long long lv = lm * 0x100000001ull;
    unsigned long long bad = __ballot(o1 != o1) | (__ballot(o1 < __builtin_bit_cast(_Float16, (unsigned short)((127 - 9 + VK) << 7))) & lv);
    if (__builtin_expect((__ballot(wm > WLIM / LCAP) & lv) != 0ull, 0)) {
      LAS unsigned* dg = (LAS unsigned*)(size_t)ot;
      dg[lane & 31] = 0u;
#pragma unroll 1
      for (int i = lane; i < cnt; i += 64)
        __hip_atomic_fetch_add(dg + (*(const LAS unsigned short*)(size_t)(la + i * 4 + 2) & 31), 1u, __ATOMIC_RELAXED,
                               __HIP_MEMORY_SCOPE_WORKGROUP);
      bad |= __ballot(wm * (float)dg[lane & 31] > WLIM) & lv;
    }
    return slow || bad != 0ull;
  };
  if (__builtin_expect(finish(true), 0)) {
    wide();
    finish(false);
  }
  if (bk == 0 && wave == 7) asm volatile("global_store_dwordx2 %0, %1, %2 sc1" :: "v"(0u), "v"(ep), "s"(FL) : "memory");
  asm volatile("s_waitcnt vmcnt(0)" ::: "memory");
}

extern "C" void kernel_launch(void* const* d_in, const int* in_sizes, int n_in,
                              void* d_out, int out_size, void* d_ws, size_t ws_size,
                              hipStream_t stream) {
  const unsigned short* x = (const unsigned short*)d_in[0];
  const u32x4* eattr = (const u32x4*)d_in[1];
  const int* src = (const int*)d_in[2];
  const int* dst = (const int*)d_in[3];
  const unsigned short* wa = (const unsigned short*)d_in[4];
  const unsigned short* wb = (const unsigned short*)d_in[5];
  const unsigned short* wc = (const unsigned short*)d_in[6];
  const unsigned short* wu = (const unsigned short*)d_in[7];
  const unsigned short* wv = (const unsigned short*)d_in[8];
  unsigned short* out = (unsigned short*)d_out;
  const int N = in_sizes[0] / 64;
  const int E = in_sizes[2];

  const bool skew = N > 4096 && N < 60000;
  const unsigned M = N > 256 ? (unsigned)(4294967295.0 / ((double)N * (skew ? 1.0 + SKEWF : 1.0))) : 0xffffffu;
  const unsigned N1 = (0x7fffffffu / M) + 1u;
  const unsigned M2 = skew ? (0xffffffffu - N1 * M) / ((unsigned)N - N1) : M;

  auto fb = [](double v) { return __builtin_bit_cast(unsigned, (float)v); };
  char* ws = (char*)d_ws;
  k_main<<<NBK, 512, 0, stream>>>(dst, src, (unsigned long long*)(ws + (1 << 20)), x, E, M, N1, M2 - M, N,
                                  fb(65536.0 / M), fb(65536.0 / M2),
                                  fb((double)N1 + (32768.0 - (double)N1 * M) / M2 + 0.5), wa, wb, wv, wc, wu, eattr, out
     );
}
```

```cpp
#include <hip/hip_runtime.h>
#include <hip/hip_bf16.h>
#include <stdint.h>

using bf16x8 = __attribute__((ext_vector_type(8))) short;
using s16x4 = __attribute__((ext_vector_type(4))) short;
using f32x16 = __attribute__((ext_vector_type(16))) float;
using f32x2 = __attribute__((ext_vector_type(2))) float;
using u32x4 = __attribute__((ext_vector_type(4))) unsigned;
using u32x16 = __attribute__((ext_vector_type(16))) unsigned;
using u32x2 = __attribute__((ext_vector_type(2))) unsigned;
using f16x2 = __attribute__((ext_vector_type(2))) _Float16;

#define MFMA32(a, b, c) __builtin_amdgcn_mfma_f32_32x32x16_bf16((a), (b), (c), 0, 0, 0)
#define RA 14
#define VK 4
#define LAS __attribute__((address_space(3)))
#define RANKOF(w) ((w) < 4 ? 7 - (w) : (w) - 4)
#define G1W 4
#define CPL 5
#define PSL 4
#define SKEWF 0.015
#define XLIKE(c) __builtin_expect(c, 0)
#define SB() __builtin_amdgcn_sched_barrier(0)

constexpr int NCH = 256;
constexpr int NBK = 256;
constexpr int NFRAG = 39;
constexpr float NLOG2E = -1.44269504088896340736f;
constexpr float VSC = 1.0f / (1 << VK);
constexpr unsigned TPITCH = 80;

__device__ __forceinline__ unsigned pack_bf16(float lo, float hi) {
  typedef __attribute__((ext_vector_type(2))) float f2;
  typedef __attribute__((ext_vector_type(2))) __bf16 b2;
  f2 v = {lo, hi};
  b2 r = __builtin_convertvector(v, b2);
  return __builtin_bit_cast(unsigned, r);
}

__device__ __forceinline__ unsigned pack_f16(float lo, float hi) {
  unsigned r;
  asm("v_cvt_pk_f16_f32 %0, %1, %2" : "=v"(r) : "v"(lo), "v"(hi));
  return r;
}

__device__ __forceinline__ void glds16(const void* sbase, unsigned voff, unsigned lds_dst) {
  asm volatile(
      "s_mov_b32 m0, %2\n\t"
      "s_nop 0\n\t"
      "global_load_lds_dwordx4 %0, %1"
      :: "v"(voff), "s"(sbase), "s"(lds_dst) : "memory", "m0");
}
__device__ __forceinline__ void glds16x(const void* sbase, unsigned a, unsigned c, unsigned lds_dst) {
  unsigned tmp;
  asm volatile(
      "s_mov_b32 m0, %4\n\t"
      "v_mad_u32_u16 %0, %1, %5, %2\n\t"
      "global_load_lds_dwordx4 %0, %3"
      : "=&v"(tmp) : "v"(a), "v"(c), "s"(sbase), "s"(lds_dst), "s"(128u) : "memory", "m0");
}
__device__ __forceinline__ void glds16e(const void* sbase, unsigned a, unsigned c, unsigned lds_dst) {
  unsigned tmp;
  asm volatile(
      "s_mov_b32 m0, %4\n\t"
      "v_add_u32 %0, %1, %2\n\t"
      "global_load_lds_dwordx4 %0, %3"
      : "=&v"(tmp) : "v"(a), "v"(c), "s"(sbase), "s"(lds_dst) : "memory", "m0");
}
__device__ __forceinline__ void gstore128wt(void* sbase, unsigned voff, u32x4 v) {
  asm volatile("global_store_dwordx4 %0, %1, %2 sc1\n\ts_nop 1" :: "v"(voff), "v"(v), "s"(sbase) : "memory");
}

#define IBX 40960
constexpr int IBOX = IBX;
constexpr int IEXT = 1 << 24;
constexpr unsigned TS = 5u << 20;
constexpr int CPIT = 208;
constexpr unsigned RINC = 87382;
constexpr int KP_HDR = 0;
constexpr int KP_ENT = KP_HDR + NBK * 4;
constexpr int KP_POS = KP_ENT + NBK * CPIT;
constexpr int KP_LDS = KP_POS + NBK * 4;
constexpr int NWL = 26;
constexpr int NBLOB = 31;

__device__ __forceinline__ unsigned mul24s(unsigned a, unsigned b) {
  unsigned r;
  asm("v_mul_u32_u24 %0, %1, %2" : "=v"(r) : "s"(b), "v"(a));
  return r;
}
__device__ __forceinline__ unsigned byte3x(unsigned a, unsigned b) {
  unsigned r;
  asm("v_mul_u32_u24_sdwa %0, %1, %2 dst_sel:DWORD dst_unused:UNUSED_PAD src0_sel:DWORD src1_sel:BYTE_3" : "=v"(r) : "s"(b), "v"(a));
  return r;
}
constexpr int RING_SLOT = 5 * 1024;
constexpr int LCAP = 512;
constexpr int LARR = 2560;
constexpr int LIST_BYTES = 2 * LARR;
constexpr int OFF_CNT = 0;
constexpr int SUBL = LARR / 4;
constexpr int OFF_XF = 256;
constexpr int OFF_W = 512;
constexpr int OFF_T1 = OFF_W + NWL * 1024;
constexpr int OFF_T2 = OFF_T1 + 32 * TPITCH;
constexpr int OFF_R0 = OFF_W + NFRAG * 1024;
constexpr int OFF_R1 = OFF_R0 + 8 * RING_SLOT;
constexpr int OFF_L = OFF_R1 + 8 * RING_SLOT;
constexpr int MAIN_LDS = OFF_L + 8 * LIST_BYTES;
static_assert(MAIN_LDS <= 160 * 1024, "LDS");
static_assert(KP_LDS <= 16 * RING_SLOT, "phase 1 lives in the ring");

__device__ __forceinline__ unsigned ldd(const unsigned* p) {
  return __hip_atomic_load(p, __ATOMIC_RELAXED, __HIP_MEMORY_SCOPE_AGENT);
}
__device__ __forceinline__ unsigned long long ldq(const unsigned long long* p) {
  return __hip_atomic_load(p, __ATOMIC_RELAXED, __HIP_MEMORY_SCOPE_AGENT);
}
__device__ __forceinline__ bf16x8 rawfrag(unsigned a) {
  const s16x4 a0 = __builtin_amdgcn_ds_read_tr16_b64_v4i16((LAS s16x4*)(size_t)a);
  const s16x4 a1 = __builtin_amdgcn_ds_read_tr16_b64_v4i16((LAS s16x4*)(size_t)(a + 512));
  return __builtin_shufflevector(a0, a1, 0, 1, 2, 3, 4, 5, 6, 7);
}
#define MAXN(a, b) __builtin_elementwise_maximum(a, b)

__global__ __launch_bounds__(512, 2) void k_main(const int* __restrict__ dst, const int* __restrict__ src,
                                                 unsigned long long* FL, const unsigned short* __restrict__ x, int E,
                                                 unsigned M, unsigned N1, unsigned DM, int N, unsigned MF0, unsigned MF1,
                                                 unsigned MB1, const void* wa_, const void* wb_, const void* wv_,
                                                 const void* wc_, const void* wu_, const void* e_, void* out_) {
  SB();
  {
    unsigned f;
    asm volatile("s_lshr_b32 s96, %2, 3\n\t"
                 "v_add_u32 %0, -448, %1\n\t"
                 "s_mul_i32 s98, s96, %3\n\t"
                 "v_cmpx_gt_u32 vcc, %3, %0\n\t"
                 "s_getpc_b64 s[96:97]\n\t"
                 "s_mov_b32 m0, %4\n\t"
                 "s_cbranch_execz 1f\n\t"
                 "v_add_lshl_u32 %0, %0, s98, 7\n\t"
                 "global_load_lds_dword %0, s[96:97]\n"
                 "1:\n\t"
                 "s_mov_b64 exec, -1"
                 : "=&v"(f) : "v"(threadIdx.x), "s"(blockIdx.x), "n"(CPL), "n"(OFF_L + 7 * LIST_BYTES + 4096)
                 : "memory", "m0", "s96", "s97", "s98", "vcc", "scc");
  }
  SB();
  __shared__ __attribute__((aligned(16))) char smem[MAIN_LDS];
  const int tid0 = threadIdx.x;
  const int wave = __builtin_amdgcn_readfirstlane(tid0 >> 6);
  const int bk = blockIdx.x;
  const unsigned lds0 = (unsigned)(size_t)(LAS char*)smem;

  LAS char* sm = (LAS char*)smem + OFF_R0;
  const unsigned sm0 = lds0 + OFF_R0;
  const int tid = tid0, lane = tid & 63, r = lane & 31, h = lane >> 5;
  const unsigned pc = tid & 7, bq = tid >> 3;
  unsigned ia = sm0 + KP_ENT + bq * CPIT + pc * 16;
#pragma unroll
  for (int s = 0; s < 4; ++s) *(LAS u32x4*)(size_t)(ia + s * 64 * CPIT) = u32x4{~0u, ~0u, ~0u, ~0u};
  *(LAS unsigned*)(sm + KP_HDR + (tid & 255) * 4) = 0u;
  int q0 = 4 * tid + 3, q1 = 4 * tid + 1795;
  unsigned a0 = tid * 16, a1 = tid * 16 + 7168, z0 = 0u;
  unsigned long long m7 = __ballot(tid == 448);
  int n7 = wave == 7 ? 0 : 0x7fffffff;
  asm volatile("" : "+v"(ia), "+v"(q0), "+v"(q1), "+v"(a0), "+v"(a1), "+v"(z0), "+s"(m7), "+s"(n7) :: "memory");
  SB();
  asm volatile("" :: "s"(x), "s"(src), "s"(dst), "s"(FL), "s"(N), "s"(E), "s"(M), "s"(N1), "s"(DM), "s"(MF0), "s"(MF1), "s"(MB1));
  const int chunk = (((E + NCH - 1) >> 8) + 3) & ~3;
  const int base = bk * chunk;
  const int ecnt = max(min(chunk, E - base), 0);
  unsigned long long on[2];
  on[0] = __ballot(q0 < min(min(chunk, E - base), n7));
  on[1] = __ballot(q1 < min(min(chunk, E - base), n7));
  a0 += base * 4u;
  a1 += base * 4u;
  u32x4 d[2], sv[2];
  u32x2 e0;
  asm volatile("s_mov_b64 exec, %11\n\t"
               "global_load_dwordx4 %0, %5, %8 nt\n\t"
               "s_mov_b64 exec, %12\n\t"
               "global_load_dwordx4 %1, %6, %8 nt\n\t"
               "s_mov_b64 exec, %13\n\t"
               "global_load_dwordx2 %2, %7, %9 sc1\n\t"
               "s_mov_b64 exec, %11\n\t"
               "s_waitcnt lgkmcnt(0)\n\t"
               "s_barrier\n\t"
               "global_load_dwordx4 %3, %5, %10 nt\n\t"
               "s_mov_b64 exec, %12\n\t"
               "global_load_dwordx4 %4, %6, %10 nt\n\t"
               "s_mov_b64 exec, -1"
               : "=&{v[240:243]}"(d[0]), "=&{v[244:247]}"(d[1]), "=&{v[238:239]}"(e0), "=&{v[248:251]}"(sv[0]),
                 "=&{v[252:255]}"(sv[1])
               : "v"(a0), "v"(a1), "v"(z0), "s"(dst), "s"(FL), "s"(src), "s"(on[0]), "s"(on[1]), "s"(m7)
               : "memory");
  auto ka = (const __attribute__((address_space(4))) unsigned long long*)__builtin_amdgcn_kernarg_segment_ptr();
  asm volatile("" : "+s"(ka));
  const unsigned short* wu_g = (const unsigned short*)ka[12];
  const u32x4* ES = (const u32x4*)ka[13];
  unsigned short* out = (unsigned short*)ka[14];
  SB();
  u32x4* ENT = (u32x4*)(FL + (1 << 17));
  const int bu = (bk >> 1) | ((bk & 1) << 7);
  const unsigned Mw = bk & 1 ? M + DM : M, Cw = bk & 1 ? 0u - N1 * DM : 0u;
  unsigned MB0;
  asm("v_fma_f32 %0, %1, 0.5, 0.5" : "=v"(MB0) : "s"(MF0));
  MB0 = __builtin_amdgcn_readfirstlane(MB0);
  asm volatile("" : "+s"(MB0));
  unsigned MF = bk & 1 ? MF1 : MF0, MB = bk & 1 ? MB1 : MB0;
  asm volatile("" : "+s"(MF), "+s"(MB));
  char* ib = reinterpret_cast<char*>(ENT) + (size_t)bu * IBOX;
  auto nodeof = [&](unsigned c) { return min((unsigned)__builtin_fmaf((float)c, __uint_as_float(MF), __uint_as_float(MB)), (unsigned)N - 1u); };
  unsigned tup[8], val[8], pm[8];
  u32x4 xg[4];
  const unsigned xn0 = __builtin_amdgcn_readfirstlane(nodeof((bu * 8 + wave) * 32));
  const unsigned xn1 = min(max(nodeof((bu * 8 + wave) * 32 + 31), 24u), N - 1u);
  unsigned xd[4];
  auto xprep = [&]() {
#pragma unroll
    for (int i = 0; i < 4; ++i)
      xd[i] = min(xn0 + 8 * i + (lane >> 3), xn1) * 128u + ((((lane & 7) ^ (lane >> 4)) * 16) ^ (i & 1) * 64) - i * 1024u;
    asm volatile("" : "+v"(xd[0]), "+v"(xd[1]), "+v"(xd[2]), "+v"(xd[3]));
  };
  auto xdma = [&]() {
    asm volatile("s_mov_b32 m0, %5\n\ts_nop 0\n\t"
                 "global_load_lds_dwordx4 %0, %4\n\t"
                 "global_load_lds_dwordx4 %1, %4 offset:1024\n\t"
                 "global_load_lds_dwordx4 %2, %4 offset:2048\n\t"
                 "global_load_lds_dwordx4 %3, %4 offset:3072"
                 :: "v"(xd[0]), "v"(xd[1]), "v"(xd[2]), "v"(xd[3]), "s"(x), "s"(lds0 + OFF_L + wave * LIST_BYTES)
                 : "memory", "m0");
  };
  auto wload = [&](const unsigned short* w, int f) {
    asm volatile("s_mov_b32 m0, %3\n\ts_nop 0\n\t"
                 "global_load_lds_dwordx4 %0, %2\n\t"
                 "global_load_lds_dwordx4 %0, %2 offset:1024\n\t"
                 "global_load_lds_dwordx4 %0, %2 offset:2048\n\t"
                 "global_load_lds_dwordx4 %0, %2 offset:3072\n\t"
                 "s_addk_i32 m0, 4096\n\ts_nop 0\n\t"
                 "global_load_lds_dwordx4 %1, %2\n\t"
                 "global_load_lds_dwordx4 %1, %2 offset:1024\n\t"
                 "global_load_lds_dwordx4 %1, %2 offset:2048\n\t"
                 "global_load_lds_dwordx4 %1, %2 offset:3072"
                 :: "v"(lane * 16u), "v"(lane * 16u + 4096u), "s"(w), "s"(lds0 + OFF_W + f * 1024) : "memory", "m0", "scc");
  };
  auto wall = [&]() {
    const unsigned short *wa_g = (const unsigned short*)ka[8], *wb_g = (const unsigned short*)ka[9],
                         *wv_g = (const unsigned short*)ka[10], *wc_g = (const unsigned short*)ka[11];
    wload(wa_g, NBLOB);
    wload(wb_g, 0);
    wload(wv_g, 8);
    glds16(wc_g, lane * 16, lds0 + OFF_W + 16 * 1024);
    glds16(wc_g + 512, lane * 16, lds0 + OFF_W + 17 * 1024);
  };
  int spin = 1 << 20;
  u32x2 ep = {0u, 0u};
  if (wave == 7) {
    wall();
    *(LAS u32x4*)((LAS char*)smem + OFF_CNT + r * 16) =
        (r & 17) == 0 ? u32x4{lds0 + OFF_L, lds0 + OFF_L + SUBL, lds0 + OFF_L + 2 * SUBL, lds0 + OFF_L + 3 * SUBL}
                      : u32x4{0u, 0u, 0u, 0u};
    {
      const unsigned col = h ? ((r >> 1) & 12u) | ((r << 2) & 16u) | (r & 3u) : r;
      LAS char* tr = (LAS char*)smem + OFF_T1 + lane * TPITCH;
#pragma unroll
      for (int j = 0; j < 4; ++j) *(LAS u32x4*)(tr + j * 16) = u32x4{0u, 0u, 0u, 0u};
      *(LAS unsigned short*)(tr + col * 2) = 0x3C00;
    }
#pragma unroll
    for (int i = 0; i < 8; ++i) { tup[i] = 0u; val[i] = 0u; pm[i] = 0u; }
    asm volatile("s_waitcnt vmcnt(%1)" : "+{v[238:239]}"(e0) : "n"(28) : "memory");
    ep[0] = __builtin_amdgcn_readfirstlane(e0[0]) + 0x9E3779B1u;
    ep[1] = __builtin_amdgcn_readfirstlane(e0[1]) + 0x7F4A7C15u;
    *(LAS u32x2*)((LAS char*)smem + OFF_CNT + 56) = u32x2{ep[0], ep[1] >> 24};
    if (__builtin_expect(ecnt & 3, 0)) {
      const int i = (ecnt & ~3) + lane;
      on[0] = __ballot(i < ecnt);
      if (i < ecnt) {
        pm[0] = (unsigned)dst[base + i] * M + (unsigned)max(dst[base + i] - (int)N1, 0) * DM;
        tup[0] = (unsigned)src[base + i] | (unsigned)i << 16;
        val[0] = __hip_atomic_fetch_add((LAS unsigned*)(sm + KP_HDR) + (pm[0] >> 24), RINC, __ATOMIC_RELAXED, __HIP_MEMORY_SCOPE_WORKGROUP);
        const unsigned u = min(val[0] >> 16, 48u);
        *(LAS unsigned*)(sm + KP_ENT + (pm[0] >> 24) * CPIT + u * 4) = tup[0];
        *(LAS unsigned char*)(sm + KP_ENT + (pm[0] >> 24) * CPIT + ((u * 4) | (u & 3) | 12)) = pm[0] >> 16;
      }
      __builtin_amdgcn_s_waitcnt(0x0F70);
    }
    xprep();
    xdma();
  } else {
    xprep();
    unsigned inc = RINC, t0 = (unsigned)tid << 18;
    asm volatile("s_waitcnt vmcnt(2)" : "+{v[240:243]}"(d[0]), "+{v[244:247]}"(d[1]), "+v"(inc), "+v"(t0) :: "memory");
    unsigned cb[8];
#pragma unroll
    for (int s = 0; s < 2; ++s) {
      unsigned* rr = val + 4 * s;
#pragma unroll
      for (int k = 0; k < 4; ++k) {
        unsigned t;
        asm("v_sub_u32 %0, %1, %2 clamp" : "=v"(t) : "v"(d[s][k]), "s"(N1));
        asm("v_mad_u32_u24 %0, %1, %2, %3" : "=v"(pm[4 * s + k]) : "v"(t), "s"(DM), "v"(mul24s(d[s][k], M)));
      }
#pragma unroll
      for (int k = 0; k < 4; ++k) rr[k] = byte3x(pm[4 * s + k], 4u);
      asm volatile(
          "s_mov_b64 exec, %5\n\t"
          "ds_add_rtn_u32 %0, %0, %4 offset:%6\n\t"
          "ds_add_rtn_u32 %1, %1, %4 offset:%6\n\t"
          "ds_add_rtn_u32 %2, %2, %4 offset:%6\n\t"
          "ds_add_rtn_u32 %3, %3, %4 offset:%6\n\t"
          "s_mov_b64 exec, -1"
          : "+v"(rr[0]), "+v"(rr[1]), "+v"(rr[2]), "+v"(rr[3])
          : "v"(inc), "s"(on[s]), "n"(OFF_R0 + KP_HDR)
          : "memory");
    }
#pragma unroll
    for (int i = 0; i < 8; ++i) cb[i] = byte3x(pm[i], (unsigned)CPIT);
    asm volatile("s_waitcnt vmcnt(0)"
                 : "+{v[248:251]}"(sv[0]), "+{v[252:255]}"(sv[1]), "+v"(cb[0]), "+v"(cb[1]), "+v"(cb[2]), "+v"(cb[3]), "+v"(cb[4]), "+v"(cb[5]),
                   "+v"(cb[6]), "+v"(cb[7])
                 :: "memory");
    xdma();
#pragma unroll
    for (int i = 0; i < 8; ++i) tup[i] = sv[i >> 2][i & 3] + t0 + (((i >> 2) * 1792u + (i & 3)) << 16);
#pragma unroll
    for (int s = 0; s < 2; ++s) {
      asm volatile("s_waitcnt lgkmcnt(%8)"
                   : "+v"(val[4 * s]), "+v"(val[4 * s + 1]), "+v"(val[4 * s + 2]), "+v"(val[4 * s + 3]), "+v"(tup[4 * s]),
                     "+v"(tup[4 * s + 1]), "+v"(tup[4 * s + 2]), "+v"(tup[4 * s + 3])
                   : "n"(s ? 8 : 4) : "memory");
      unsigned pa[4], pb[4];
#pragma unroll
      for (int k = 0; k < 4; ++k) {
        const unsigned u = min(val[4 * s + k], 48u << 16);
        asm("v_mad_u32_u16 %0, %1, 4, %2 op_sel:[1,0,0,0]" : "=v"(pa[k]) : "v"(u), "v"(cb[4 * s + k]));
        pb[k] = pa[k] | ((u >> 16) & 3u) | 12u;
      }
      asm volatile(
          "s_mov_b64 exec, %16\n\t"
          "ds_write_b32 %0, %8 offset:%17\n\t"
          "ds_write_b8_d16_hi %4, %12 offset:%17\n\t"
          "ds_write_b32 %1, %9 offset:%17\n\t"
          "ds_write_b8_d16_hi %5, %13 offset:%17\n\t"
          "ds_write_b32 %2, %10 offset:%17\n\t"
          "ds_write_b8_d16_hi %6, %14 offset:%17\n\t"
          "ds_write_b32 %3, %11 offset:%17\n\t"
          "ds_write_b8_d16_hi %7, %15 offset:%17\n\t"
          "s_mov_b64 exec, -1"
          :: "v"(pa[0]), "v"(pa[1]), "v"(pa[2]), "v"(pa[3]), "v"(pb[0]), "v"(pb[1]), "v"(pb[2]), "v"(pb[3]),
             "v"(tup[4 * s]), "v"(tup[4 * s + 1]), "v"(tup[4 * s + 2]), "v"(tup[4 * s + 3]), "v"(pm[4 * s]),
             "v"(pm[4 * s + 1]), "v"(pm[4 * s + 2]), "v"(pm[4 * s + 3]), "s"(on[s]), "n"(OFF_R0 + KP_ENT)
          : "memory");
    }
  }
  u32x2 tag;
  int xw = 0;
  {
    unsigned ha = sm0 + KP_HDR + bq * 4, hb = sm0 + KP_HDR + lane * 16;
    unsigned go = bq * IBOX + bk * 128 + pc * 16;
    asm volatile("s_waitcnt lgkmcnt(0)\n\ts_barrier" : "+v"(ha), "+v"(hb), "+v"(go) : "s"(wu_g), "s"(ES), "s"(out) : "memory");
    tag = *(const LAS u32x2*)((LAS char*)smem + OFF_CNT + 56);
    const u32x4 call = *(const LAS u32x4*)(size_t)hb;
    unsigned cn[4];
    u32x4 v[4];
#pragma unroll
    for (int s = 0; s < 4; ++s) {
      cn[s] = *(const LAS unsigned*)(size_t)(ha + s * 256);
      v[s] = *(const LAS u32x4*)(size_t)(ia + s * 64 * CPIT);
    }
    asm volatile("" : "+v"(v[0]), "+v"(v[1]), "+v"(v[2]), "+v"(v[3]), "+v"(tag));
    if (__builtin_expect(__ballot(max(max(call[0], call[1]), max(call[2], call[3])) > 36u * RINC) != 0ull, 0)) {
      LAS unsigned* ovc = (LAS unsigned*)((LAS char*)smem + OFF_CNT + 24);
      LAS unsigned* pos = (LAS unsigned*)(sm + KP_POS);
      if (tid < NBK) {
        const unsigned u = *(const LAS unsigned*)(sm + KP_HDR + tid * 4) >> 16, n = u - (u >> 2);
        if (n > 36u) {
          const unsigned p = (unsigned)base + __hip_atomic_fetch_add(ovc, n - 36u, __ATOMIC_RELAXED, __HIP_MEMORY_SCOPE_WORKGROUP);
          pos[tid] = p;
          __hip_atomic_store(FL + 512 + tid * NCH + bk, ((unsigned long long)(n - 36u) << 32) | p, __ATOMIC_RELAXED,
                             __HIP_MEMORY_SCOPE_AGENT);
        }
      }
      asm volatile("s_waitcnt lgkmcnt(0)\n\ts_barrier" ::: "memory");
#pragma unroll
      for (int i = 0; i < 8; ++i) {
        const unsigned u = val[i] >> 16, n = u - (u >> 2);
        if (((on[i >> 2] >> lane) & 1ull) && n >= 36u)
          __hip_atomic_store(FL + (TS + pos[pm[i] >> 24] + (n - 36u)), ((unsigned long long)((pm[i] >> 16) & 0xffu) << 32) | tup[i],
                             __ATOMIC_RELAXED, __HIP_MEMORY_SCOPE_AGENT);
      }
      __builtin_amdgcn_s_waitcnt(0x0F70);
      asm volatile("s_waitcnt vmcnt(0)\n\ts_barrier" ::: "memory");
    }
    auto seal = [&](u32x4& w, bool more) {
      w[3] = __builtin_amdgcn_perm(tag[1], w[3], 0x04020100u);
      unsigned sm_ = w[0] + w[1] + w[2] + w[3] + tag[0];
      sm_ += __builtin_amdgcn_update_dpp(0u, sm_, 0xB1, 0xf, 0xf, false);
      sm_ += __builtin_amdgcn_update_dpp(0u, sm_, 0x4E, 0xf, 0xf, false);
      sm_ += __builtin_amdgcn_update_dpp(0u, sm_, 0x141, 0xf, 0xf, false);
      w[2] = pc == 7u ? w[2] - sm_ + (more ? 1u : 0u) : w[2];
    };
#pragma unroll
    for (int s = 0; s < 4; ++s) seal(v[s], cn[s] > 23u * RINC);
    asm volatile("global_store_dwordx4 %0, %1, %5 sc1\n\t"
                 "global_store_dwordx4 %0, %2, %6 sc1\n\t"
                 "global_store_dwordx4 %0, %3, %7 sc1\n\t"
                 "global_store_dwordx4 %0, %4, %8 sc1\n\t"
                 "s_nop 1"
                 :: "v"(go), "v"(v[0]), "v"(v[1]), "v"(v[2]), "v"(v[3]), "s"(ENT), "s"(ENT + 4 * IBOX), "s"(ENT + 8 * IBOX),
                    "s"(ENT + 12 * IBOX)
                 : "memory");
    if (XLIKE(__ballot(max(max(cn[0], cn[1]), max(cn[2], cn[3])) > 23u * RINC) != 0ull)) {
      unsigned xm = 0u;
#pragma unroll
      for (int s = 0; s < 4; ++s) xm |= cn[s] > 23u * RINC ? 1u << s : 0u;
#pragma unroll 1
      do {
        const unsigned s = __builtin_ctz(xm | 8u);
        const unsigned c = *(const LAS unsigned*)(size_t)(ha + s * 256);
        u32x4 w = *(const LAS u32x4*)(size_t)(ia + s * (64 * CPIT) + (pc < 4u ? 128 : 0));
        const unsigned lim = pc < 4u ? (c >> 16) - 4u * pc : 0u;
        w[3] = pc == 7u ? w[3] >> 16 : w[3];
        w[0] = pc == 7u ? w[2] : 32u < lim ? w[0] : ~0u;
        w[1] = 33u < lim ? w[1] : ~0u;
        w[2] = 34u < lim ? w[2] : ~0u;
        seal(w, c > 36u * RINC);
        const unsigned ga = go + s * (64 * IBOX);
        if (xm) {
          w[3] += (((bk >> 3) & 3) + 1) << 24;
          gstore128wt(ENT + 2048 + ((bk >> 5) * 8 + (bk & 7) - bk) * 8, ga, w);
          w[3] -= (((bk >> 3) & 3) + 1) << 24;
          gstore128wt(ENT + IEXT / 16, ga, w);
        }
        xm &= xm - 1u;
        ++xw;
      } while (__ballot(xm != 0u) != 0ull);
    }
  }
  u32x4 cv[4];
  unsigned vo[4], eb[4];
  unsigned long long pend[4];
#pragma unroll
  for (int k = 0; k < 4; ++k) {
    vo[k] = wave * 4096u + k * 1024u + lane * 16u;
    eb[k] = (unsigned)((wave * 32 + k * 8 + (lane >> 3)) * chunk) * 32u;
  }
#define POLL0()                                                                                                       \
  asm volatile("global_load_dwordx4 %0, %4, %8 sc1\n\t"                                                               \
               "global_load_dwordx4 %1, %5, %8 sc1\n\t"                                                               \
               "global_load_dwordx4 %2, %6, %8 sc1\n\t"                                                               \
               "global_load_dwordx4 %3, %7, %8 sc1"                                                                   \
               : "=&{v[240:243]}"(cv[0]), "=&{v[244:247]}"(cv[1]), "=&{v[248:251]}"(cv[2]), "=&{v[252:255]}"(cv[3])   \
               : "v"(vo[0]), "v"(vo[1]), "v"(vo[2]), "v"(vo[3]), "s"(ib)                                              \
               : "memory")
  const f32x16 zero16 = {0.f, 0.f, 0.f, 0.f, 0.f, 0.f, 0.f, 0.f, 0.f, 0.f, 0.f, 0.f, 0.f, 0.f, 0.f, 0.f};
  const unsigned to = lds0 + OFF_W + ((lane >> 2) & 3) * 128 + (lane & 1) * 64 + ((lane >> 4) & 1) * 16 + ((lane >> 1) & 1) * 8;
  auto xa = [&]() {
    bf16x8 wa[8];
#pragma unroll
    for (int k = 0; k < 8; ++k) wa[k] = rawfrag(to + (NBLOB + (k >> 1)) * 1024 + h * 4096 + (k & 1) * 32);
    asm volatile("" : "+v"(wa[0]), "+v"(wa[1]), "+v"(wa[2]), "+v"(wa[3]), "+v"(wa[4]), "+v"(wa[5]), "+v"(wa[6]), "+v"(wa[7]),
                      "+{v[160:163]}"(xg[0]), "+{v[164:167]}"(xg[1]), "+{v[168:171]}"(xg[2]), "+{v[172:175]}"(xg[3]));
    f32x16 XA[2] = {zero16, zero16};
#pragma unroll
    for (int k = 0; k < 4; ++k)
#pragma unroll
      for (int ct = 0; ct < 2; ++ct) XA[ct] = MFMA32(__builtin_bit_cast(bf16x8, xg[k]), wa[k * 2 + ct], XA[ct]);
#pragma unroll
    for (int q = 0; q < 4; ++q) {
      u32x4 xp;
#pragma unroll
      for (int j = 0; j < 4; ++j)
        xp[j] = pack_f16(XA[q & 1][8 * (q >> 1) + 2 * j] * NLOG2E - RA, XA[q & 1][8 * (q >> 1) + 2 * j + 1] * NLOG2E - RA);
      *(LAS u32x4*)((LAS char*)smem + OFF_R1 + wave * RING_SLOT + q * 1024 + lane * 16) = xp;
    }
  };
  {
    auto prep = [&](unsigned f, unsigned hs, float scale) {
      u32x4 w[2];
#pragma unroll
      for (int ct = 0; ct < 2; ++ct) w[ct] = __builtin_bit_cast(u32x4, rawfrag(to + f + h * hs + ct * 32));
      asm volatile("s_waitcnt lgkmcnt(0)" : "+v"(w[0]), "+v"(w[1]) :: "memory");
#pragma unroll
      for (int ct = 0; ct < 2; ++ct) {
#pragma unroll
        for (int j = 0; j < 4; ++j)
          w[ct][j] = pack_bf16(__uint_as_float(w[ct][j] << 16) * scale, __uint_as_float(w[ct][j] & 0xffff0000u) * scale);
        *(LAS u32x4*)((LAS char*)smem + OFF_W + f + ct * hs + lane * 16) = w[ct];
      }
    };
    {
      const unsigned xr = min(nodeof((bu * 8 + wave) * 32 + r) - xn0, 31u);
      unsigned xa_ = lds0 + OFF_L + wave * LIST_BYTES + xr * 128 + (((4 * h) ^ ((xr >> 1) & 7)) * 16);
      asm volatile("s_cmp_eq_u32 %1, 1\n\ts_cbranch_scc1 1f\n\ts_waitcnt vmcnt(4)\n1:\n\ts_waitcnt vmcnt(6)"
                   : "+v"(xa_) : "s"(xw) : "memory", "scc");
#pragma unroll
      for (int t = 0; t < 4; ++t) xg[t] = *(const LAS u32x4*)(size_t)(xa_ ^ (t * 16));
      asm volatile("" : "+v"(xg[0]), "+v"(xg[1]), "+v"(xg[2]), "+v"(xg[3]) :: "memory");
      *(LAS unsigned*)((LAS char*)smem + OFF_L + wave * LIST_BYTES + h * LARR + r * 4) = ~0u;
    }
    asm volatile("s_waitcnt lgkmcnt(0)\n\ts_barrier"
                 : "+v"(vo[0]), "+v"(vo[1]), "+v"(vo[2]), "+v"(vo[3]), "+v"(eb[0]), "+v"(eb[1]), "+v"(eb[2]), "+v"(eb[3])
                 :: "memory");
    xa();
    {
      unsigned f = (wave + (wave & 4)) * 1024, hs = 4096;
      const float scale = wave < 4 ? NLOG2E : VSC;
#pragma unroll 1
      while (true) {
        asm volatile("" : "+s"(f), "+s"(hs));
        prep(f, hs, scale);
        if (f != 0u) break;
        f = 16 * 1024;
        hs = 1024;
      }
    }
  }
  const unsigned l0 = lds0 + OFF_L;
  unsigned aLX = l0 + (lane >> 3) * 16;
  unsigned aLE = l0 + LARR + ((((lane >> 1) & 7) << 2) | (lane >> 4)) * 4;
  unsigned aLS4 = l0 + (4 * h + ((lane >> 2) & 3)) * 16;
  unsigned aLS1 = l0 + (((r & 7) << 2) | (r >> 3)) * 4 + 2;
  unsigned cx0 = (unsigned)(((lane & 7) ^ (lane >> 4)) * 16);
  unsigned cx1 = cx0 ^ 64u;
  unsigned ce = (unsigned)((((lane & 1) ^ ((lane >> 4) & 1))) * 16);
  const unsigned ringd = 8 * RING_SLOT;
  unsigned cS = lds0 + OFF_T1 + (((lane >> 4) & 1) * 16 + (lane & 3) * 4) * 2;
  unsigned cSt = lds0 + OFF_T2 + h * 32;
  unsigned fo[4];
#pragma unroll
  for (int t = 0; t < 4; ++t) fo[t] = lds0 + OFF_R0 + (unsigned)(r * 128 + (((4 * h + t) ^ ((r >> 1) & 7)) * 16));
  unsigned foe = lds0 + OFF_R0 + (unsigned)(4096 + r * 32 + ((h ^ ((r >> 3) & 1)) * 16));
  f32x16 Y[4];
#pragma unroll
  for (int q = 0; q < 4; ++q)
#pragma unroll
    for (int i = 0; i < 16; i += 2) {
      f32x2 y;
      if (q < 2) asm volatile("v_mov_b64 %0, %1" : "=v"(y) : "s"(0x358637bd358637bdull + (RA * 0x0080000000800000ull)));
      else asm volatile("v_mov_b64 %0, 0" : "=v"(y));
      Y[q][i] = y[0];
      Y[q][i + 1] = y[1];
    }
  constexpr unsigned cu = RA == 14 ? 0x04000400u : 0x02000200u;
  unsigned rid = lane & 7;
  asm volatile("" : "+v"(aLX), "+v"(aLE), "+v"(aLS4), "+v"(aLS1), "+v"(cx0), "+v"(cx1), "+v"(ce), "+v"(cS), "+v"(cSt),
                    "+v"(fo[0]), "+v"(fo[1]), "+v"(fo[2]), "+v"(fo[3]), "+v"(foe), "+v"(rid));
  asm volatile("" : "+{v[0:15]}"(Y[0]), "+{v[16:31]}"(Y[1]), "+{v[32:47]}"(Y[2]), "+{v[48:63]}"(Y[3]));
  SB();
  __builtin_amdgcn_s_sleep(PSL);
  POLL0();
  glds16(wu_g + wave * 512, lane * 16, lds0 + OFF_W + (18 + wave) * 1024);
  auto ld128 = [&](unsigned a) { return *(const LAS u32x4*)(size_t)a; };
  auto gather5 = [&](const u32x4 sx, unsigned re, unsigned dstb) {
    glds16e(ES, re, ce, dstb + 4096);
    glds16x(x, sx[0], cx0, dstb);
    glds16x(x, sx[1], cx1, dstb + 1024);
    glds16x(x, sx[2], cx0, dstb + 2048);
    glds16x(x, sx[3], cx1, dstb + 3072);
  };
  const unsigned sq = min(4u * M, 0xffffffu);
  unsigned early, just;
  asm volatile(
        "v_mov_b32 v181, %18\n\t"
        "v_mov_b32 v180, 160\n\t"
        "v_mov_b32 v176, 0xe0\n\t"
        "v_mov_b32 v177, 0x0c040100\n\t"
        "v_mov_b32 v178, 0x0c050100\n\t"
        "v_mov_b32 v179, 0x0c060100\n\t"
        "s_mov_b32 s52, 0x7f7f7f7f\n\t"
        "s_mov_b32 s53, 0x7f7f7f7f\n\t"
        "s_mov_b64 s[54:55], 0\n\t"
        "s_mov_b32 %12, 0\n\t"
        "v_mov_b32 v191, 4\n\t"
        "s_waitcnt vmcnt(3)\n\t"
        "v_add3_u32 v228, v240, v241, v242\n\t"
        "v_add3_u32 v231, v244, v245, v246\n\t"
        "v_add3_u32 v228, v228, v243, %19\n\t"
        "v_add3_u32 v231, v231, v247, %19\n\t"
        "v_cmp_ne_u32_sdwa s[72:73], v243, %20 src0_sel:BYTE_3 src1_sel:DWORD\n\t"
        "v_add_u32_dpp v228, v228, v228 quad_perm:[1,0,3,2] row_mask:0xf bank_mask:0xf\n\t"
        "v_add_u32_dpp v231, v231, v231 quad_perm:[1,0,3,2] row_mask:0xf bank_mask:0xf\n\t"
        "v_cmp_ne_u32_sdwa s[78:79], v247, %20 src0_sel:BYTE_3 src1_sel:DWORD\n\t"
        "v_add_u32_dpp v228, v228, v228 quad_perm:[2,3,0,1] row_mask:0xf bank_mask:0xf\n\t"
        "v_add_u32_dpp v231, v231, v231 quad_perm:[2,3,0,1] row_mask:0xf bank_mask:0xf\n\t"
        "s_nop 0\n\t"
        "v_add_u32_dpp v228, v228, v228 row_half_mirror row_mask:0xf bank_mask:0xf\n\t"
        "v_add_u32_dpp v231, v231, v231 row_half_mirror row_mask:0xf bank_mask:0xf\n\t"
        "v_cmp_gt_u32 s[64:65], 2, v228\n\t"
        "v_cmp_eq_u32 s[56:57], 1, v228\n\t"
        "v_cmp_gt_u32 s[66:67], 2, v231\n\t"
        "v_cmp_eq_u32 s[58:59], 1, v231\n\t"
        "s_and_b64 s[72:73], s[72:73], s[64:65]\n\t"
        "s_cselect_b64 s[64:65], 0, s[64:65]\n\t"
        "s_and_b64 s[56:57], s[56:57], s[64:65]\n\t"
        "s_not_b64 %0, s[64:65]\n\t"
        "s_or_b64 s[54:55], s[54:55], %0\n\t"
        "s_and_b64 s[78:79], s[78:79], s[66:67]\n\t"
        "s_cselect_b64 s[66:67], 0, s[66:67]\n\t"
        "s_and_b64 s[58:59], s[58:59], s[66:67]\n\t"
        "s_not_b64 %1, s[66:67]\n\t"
        "s_or_b64 s[54:55], s[54:55], %1\n\t"
        "s_mov_b64 exec, s[64:65]\n\t"
        "v_cmpx_ne_u32 s[72:73], -1, v240\n\t"
        "v_mul_hi_u32_u24_sdwa v204, v240, %21 dst_sel:DWORD dst_unused:UNUSED_PAD src0_sel:WORD_0 src1_sel:DWORD\n\t"
        "v_and_b32_sdwa v192, v243, v176 dst_sel:DWORD dst_unused:UNUSED_PAD src0_sel:BYTE_0 src1_sel:DWORD\n\t"
        "v_lshl_add_u32 v204, v204, 2, v192\n\t"
        "ds_add_rtn_u32 v204, v204, v191\n\t"
        "v_perm_b32 v216, v243, v240, v177\n\t"
        "v_mad_u32_u16 v228, v240, 32, %13 op_sel:[1,0,0,0]\n\t"
        "v_cmpx_ne_u32 s[74:75], -1, v241\n\t"
        "v_mul_hi_u32_u24_sdwa v205, v241, %21 dst_sel:DWORD dst_unused:UNUSED_PAD src0_sel:WORD_0 src1_sel:DWORD\n\t"
        "v_and_b32_sdwa v193, v243, v176 dst_sel:DWORD dst_unused:UNUSED_PAD src0_sel:BYTE_1 src1_sel:DWORD\n\t"
        "v_lshl_add_u32 v205, v205, 2, v193\n\t"
        "ds_add_rtn_u32 v205, v205, v191\n\t"
        "v_perm_b32 v217, v243, v241, v178\n\t"
        "v_mad_u32_u16 v229, v241, 32, %13 op_sel:[1,0,0,0]\n\t"
        "s_and_b64 exec, exec, s[52:53]\n\t"
        "v_cmpx_ne_u32 s[76:77], -1, v242\n\t"
        "v_mul_hi_u32_u24_sdwa v206, v242, %21 dst_sel:DWORD dst_unused:UNUSED_PAD src0_sel:WORD_0 src1_sel:DWORD\n\t"
        "v_and_b32_sdwa v194, v243, v176 dst_sel:DWORD dst_unused:UNUSED_PAD src0_sel:BYTE_2 src1_sel:DWORD\n\t"
        "v_lshl_add_u32 v206, v206, 2, v194\n\t"
        "ds_add_rtn_u32 v206, v206, v191\n\t"
        "v_perm_b32 v218, v243, v242, v179\n\t"
        "v_mad_u32_u16 v230, v242, 32, %13 op_sel:[1,0,0,0]\n\t"
        "s_mov_b64 exec, s[66:67]\n\t"
        "v_cmpx_ne_u32 s[78:79], -1, v244\n\t"
        "v_mul_hi_u32_u24_sdwa v207, v244, %21 dst_sel:DWORD dst_unused:UNUSED_PAD src0_sel:WORD_0 src1_sel:DWORD\n\t"
        "v_and_b32_sdwa v195, v247, v176 dst_sel:DWORD dst_unused:UNUSED_PAD src0_sel:BYTE_0 src1_sel:DWORD\n\t"
        "v_lshl_add_u32 v207, v207, 2, v195\n\t"
        "ds_add_rtn_u32 v207, v207, v191\n\t"
        "v_perm_b32 v219, v247, v244, v177\n\t"
        "v_mad_u32_u16 v231, v244, 32, %14 op_sel:[1,0,0,0]\n\t"
        "v_cmpx_ne_u32 s[80:81], -1, v245\n\t"
        "v_mul_hi_u32_u24_sdwa v208, v245, %21 dst_sel:DWORD dst_unused:UNUSED_PAD src0_sel:WORD_0 src1_sel:DWORD\n\t"
        "v_and_b32_sdwa v196, v247, v176 dst_sel:DWORD dst_unused:UNUSED_PAD src0_sel:BYTE_1 src1_sel:DWORD\n\t"
        "v_lshl_add_u32 v208, v208, 2, v196\n\t"
        "ds_add_rtn_u32 v208, v208, v191\n\t"
        "v_perm_b32 v220, v247, v245, v178\n\t"
        "v_mad_u32_u16 v232, v245, 32, %14 op_sel:[1,0,0,0]\n\t"
        "s_and_b64 exec, exec, s[52:53]\n\t"
        "v_cmpx_ne_u32 s[82:83], -1, v246\n\t"
        "v_mul_hi_u32_u24_sdwa v209, v246, %21 dst_sel:DWORD dst_unused:UNUSED_PAD src0_sel:WORD_0 src1_sel:DWORD\n\t"
        "v_and_b32_sdwa v197, v247, v176 dst_sel:DWORD dst_unused:UNUSED_PAD src0_sel:BYTE_2 src1_sel:DWORD\n\t"
        "v_lshl_add_u32 v209, v209, 2, v197\n\t"
        "ds_add_rtn_u32 v209, v209, v191\n\t"
        "v_perm_b32 v221, v247, v246, v179\n\t"
        "v_mad_u32_u16 v233, v246, 32, %14 op_sel:[1,0,0,0]\n\t"
        "s_mov_b64 exec, -1\n\t"
        "s_waitcnt vmcnt(1)\n\t"
        "v_add3_u32 v234, v248, v249, v250\n\t"
        "v_add3_u32 v237, v252, v253, v254\n\t"
        "v_add3_u32 v234, v234, v251, %19\n\t"
        "v_add3_u32 v237, v237, v255, %19\n\t"
        "v_cmp_ne_u32_sdwa s[84:85], v251, %20 src0_sel:BYTE_3 src1_sel:DWORD\n\t"
        "v_add_u32_dpp v234, v234, v234 quad_perm:[1,0,3,2] row_mask:0xf bank_mask:0xf\n\t"
        "v_add_u32_dpp v237, v237, v237 quad_perm:[1,0,3,2] row_mask:0xf bank_mask:0xf\n\t"
        "v_cmp_ne_u32_sdwa s[90:91], v255, %20 src0_sel:BYTE_3 src1_sel:DWORD\n\t"
        "v_add_u32_dpp v234, v234, v234 quad_perm:[2,3,0,1] row_mask:0xf bank_mask:0xf\n\t"
        "v_add_u32_dpp v237, v237, v237 quad_perm:[2,3,0,1] row_mask:0xf bank_mask:0xf\n\t"
        "s_nop 0\n\t"
        "v_add_u32_dpp v234, v234, v234 row_half_mirror row_mask:0xf bank_mask:0xf\n\t"
        "v_add_u32_dpp v237, v237, v237 row_half_mirror row_mask:0xf bank_mask:0xf\n\t"
        "v_cmp_gt_u32 s[68:69], 2, v234\n\t"
        "v_cmp_eq_u32 s[60:61], 1, v234\n\t"
        "v_cmp_gt_u32 s[70:71], 2, v237\n\t"
        "v_cmp_eq_u32 s[62:63], 1, v237\n\t"
        "s_and_b64 s[84:85], s[84:85], s[68:69]\n\t"
        "s_cselect_b64 s[68:69], 0, s[68:69]\n\t"
        "s_and_b64 s[60:61], s[60:61], s[68:69]\n\t"
        "s_not_b64 %2, s[68:69]\n\t"
        "s_or_b64 s[54:55], s[54:55], %2\n\t"
        "s_and_b64 s[90:91], s[90:91], s[70:71]\n\t"
        "s_cselect_b64 s[70:71], 0, s[70:71]\n\t"
        "s_and_b64 s[62:63], s[62:63], s[70:71]\n\t"
        "s_not_b64 %3, s[70:71]\n\t"
        "s_or_b64 s[54:55], s[54:55], %3\n\t"
        "s_or_b64 s[50:51], s[56:57], s[58:59]\n\t"
        "s_or_b64 exec, s[60:61], s[62:63]\n\t"
        "s_or_b64 s[50:51], s[50:51], exec\n\t"
        "s_cbranch_scc0 57f\n\t"
        "s_mov_b64 exec, s[50:51]\n\t"
        "global_load_dwordx4 v[172:175], %30, %17 sc1\n\t"
        "57:\n\t"
        "s_mov_b64 exec, s[72:73]\n\t"
        "s_waitcnt lgkmcnt(3)\n\t"
        "v_min_u32 v204, v181, v204\n\t"
        "v_min_u32 v205, v181, v205\n\t"
        "v_min_u32 v206, v181, v206\n\t"
        "v_mad_u32_u24 v204, v192, v180, v204\n\t"
        "v_mad_u32_u24 v205, v193, v180, v205\n\t"
        "v_mad_u32_u24 v206, v194, v180, v206\n\t"
        "ds_write2st64_b32 v204, v216, v228 offset1:10\n\t"
        "s_mov_b64 exec, s[74:75]\n\t"
        "ds_write2st64_b32 v205, v217, v229 offset1:10\n\t"
        "s_mov_b64 exec, s[76:77]\n\t"
        "ds_write2st64_b32 v206, v218, v230 offset1:10\n\t"
        "s_mov_b64 exec, s[68:69]\n\t"
        "v_cmpx_ne_u32 s[84:85], -1, v248\n\t"
        "v_mul_hi_u32_u24_sdwa v210, v248, %21 dst_sel:DWORD dst_unused:UNUSED_PAD src0_sel:WORD_0 src1_sel:DWORD\n\t"
        "v_and_b32_sdwa v198, v251, v176 dst_sel:DWORD dst_unused:UNUSED_PAD src0_sel:BYTE_0 src1_sel:DWORD\n\t"
        "v_lshl_add_u32 v210, v210, 2, v198\n\t"
        "ds_add_rtn_u32 v210, v210, v191\n\t"
        "v_perm_b32 v222, v251, v248, v177\n\t"
        "v_mad_u32_u16 v234, v248, 32, %15 op_sel:[1,0,0,0]\n\t"
        "v_cmpx_ne_u32 s[86:87], -1, v249\n\t"
        "v_mul_hi_u32_u24_sdwa v211, v249, %21 dst_sel:DWORD dst_unused:UNUSED_PAD src0_sel:WORD_0 src1_sel:DWORD\n\t"
        "v_and_b32_sdwa v199, v251, v176 dst_sel:DWORD dst_unused:UNUSED_PAD src0_sel:BYTE_1 src1_sel:DWORD\n\t"
        "v_lshl_add_u32 v211, v211, 2, v199\n\t"
        "ds_add_rtn_u32 v211, v211, v191\n\t"
        "v_perm_b32 v223, v251, v249, v178\n\t"
        "v_mad_u32_u16 v235, v249, 32, %15 op_sel:[1,0,0,0]\n\t"
        "s_and_b64 exec, exec, s[52:53]\n\t"
        "v_cmpx_ne_u32 s[88:89], -1, v250\n\t"
        "v_mul_hi_u32_u24_sdwa v212, v250, %21 dst_sel:DWORD dst_unused:UNUSED_PAD src0_sel:WORD_0 src1_sel:DWORD\n\t"
        "v_and_b32_sdwa v200, v251, v176 dst_sel:DWORD dst_unused:UNUSED_PAD src0_sel:BYTE_2 src1_sel:DWORD\n\t"
        "v_lshl_add_u32 v212, v212, 2, v200\n\t"
        "ds_add_rtn_u32 v212, v212, v191\n\t"
        "v_perm_b32 v224, v251, v250, v179\n\t"
        "v_mad_u32_u16 v236, v250, 32, %15 op_sel:[1,0,0,0]\n\t"
        "s_mov_b64 exec, s[78:79]\n\t"
        "s_waitcnt lgkmcnt(6)\n\t"
        "v_min_u32 v207, v181, v207\n\t"
        "v_min_u32 v208, v181, v208\n\t"
        "v_min_u32 v209, v181, v209\n\t"
        "v_mad_u32_u24 v207, v195, v180, v207\n\t"
        "v_mad_u32_u24 v208, v196, v180, v208\n\t"
        "v_mad_u32_u24 v209, v197, v180, v209\n\t"
        "ds_write2st64_b32 v207, v219, v231 offset1:10\n\t"
        "s_mov_b64 exec, s[80:81]\n\t"
        "ds_write2st64_b32 v208, v220, v232 offset1:10\n\t"
        "s_mov_b64 exec, s[82:83]\n\t"
        "ds_write2st64_b32 v209, v221, v233 offset1:10\n\t"
        "s_mov_b64 exec, -1\n\t"
        "ds_read_b128 v[184:187], %22\n\t"
        "ds_read_b32 v183, %23\n\t"
        "s_mov_b64 exec, s[70:71]\n\t"
        "v_cmpx_ne_u32 s[90:91], -1, v252\n\t"
        "v_mul_hi_u32_u24_sdwa v213, v252, %21 dst_sel:DWORD dst_unused:UNUSED_PAD src0_sel:WORD_0 src1_sel:DWORD\n\t"
        "v_and_b32_sdwa v201, v255, v176 dst_sel:DWORD dst_unused:UNUSED_PAD src0_sel:BYTE_0 src1_sel:DWORD\n\t"
        "v_lshl_add_u32 v213, v213, 2, v201\n\t"
        "ds_add_rtn_u32 v213, v213, v191\n\t"
        "v_perm_b32 v225, v255, v252, v177\n\t"
        "v_mad_u32_u16 v237, v252, 32, %16 op_sel:[1,0,0,0]\n\t"
        "v_cmpx_ne_u32 s[92:93], -1, v253\n\t"
        "v_mul_hi_u32_u24_sdwa v214, v253, %21 dst_sel:DWORD dst_unused:UNUSED_PAD src0_sel:WORD_0 src1_sel:DWORD\n\t"
        "v_and_b32_sdwa v202, v255, v176 dst_sel:DWORD dst_unused:UNUSED_PAD src0_sel:BYTE_1 src1_sel:DWORD\n\t"
        "v_lshl_add_u32 v214, v214, 2, v202\n\t"
        "ds_add_rtn_u32 v214, v214, v191\n\t"
        "v_perm_b32 v226, v255, v253, v178\n\t"
        "v_mad_u32_u16 v238, v253, 32, %16 op_sel:[1,0,0,0]\n\t"
        "s_and_b64 exec, exec, s[52:53]\n\t"
        "v_cmpx_ne_u32 s[94:95], -1, v254\n\t"
        "v_mul_hi_u32_u24_sdwa v215, v254, %21 dst_sel:DWORD dst_unused:UNUSED_PAD src0_sel:WORD_0 src1_sel:DWORD\n\t"
        "v_and_b32_sdwa v203, v255, v176 dst_sel:DWORD dst_unused:UNUSED_PAD src0_sel:BYTE_2 src1_sel:DWORD\n\t"
        "v_lshl_add_u32 v215, v215, 2, v203\n\t"
        "ds_add_rtn_u32 v215, v215, v191\n\t"
        "v_perm_b32 v227, v255, v254, v179\n\t"
        "v_mad_u32_u16 v239, v254, 32, %16 op_sel:[1,0,0,0]\n\t"
        "s_cmp_eq_u64 s[54:55], 0\n\t"
        "s_cbranch_scc1 59f\n\t"
        "s_mov_b64 exec, %0\n\t"
        "s_cbranch_execz 50f\n\t"
        "global_load_dwordx4 v[240:243], %4, %17 sc1\n\t"
        "50:\n\t"
        "s_mov_b64 exec, %1\n\t"
        "s_cbranch_execz 51f\n\t"
        "global_load_dwordx4 v[244:247], %5, %17 sc1\n\t"
        "51:\n\t"
        "s_mov_b64 exec, %2\n\t"
        "s_cbranch_execz 52f\n\t"
        "global_load_dwordx4 v[248:251], %6, %17 sc1\n\t"
        "52:\n\t"
        "s_mov_b64 exec, %3\n\t"
        "s_cbranch_execz 53f\n\t"
        "global_load_dwordx4 v[252:255], %7, %17 sc1\n\t"
        "53:\n\t"
        "59:\n\t"
        "s_mov_b64 exec, s[84:85]\n\t"
        "s_waitcnt lgkmcnt(8)\n\t"
        "v_min_u32 v210, v181, v210\n\t"
        "v_min_u32 v211, v181, v211\n\t"
        "v_min_u32 v212, v181, v212\n\t"
        "v_mad_u32_u24 v210, v198, v180, v210\n\t"
        "v_mad_u32_u24 v211, v199, v180, v211\n\t"
        "v_mad_u32_u24 v212, v200, v180, v212\n\t"
        "ds_write2st64_b32 v210, v222, v234 offset1:10\n\t"
        "s_mov_b64 exec, s[86:87]\n\t"
        "ds_write2st64_b32 v211, v223, v235 offset1:10\n\t"
        "s_mov_b64 exec, s[88:89]\n\t"
        "ds_write2st64_b32 v212, v224, v236 offset1:10\n\t"
        "s_mov_b64 exec, -1\n\t"
        "s_movk_i32 s96, 128\n\t"
        "s_mov_b32 m0, %29\n\t"
        "s_waitcnt lgkmcnt(6)\n\t"
        "v_max3_u32 v182, v184, v185, v186\n\t"
        "v_max3_u32 v182, v182, v187, v183\n\t"
        "v_cmp_eq_u32 vcc, -1, v182\n\t"
        "v_mad_u32_u16 v184, v184, s96, %24\n\t"
        "v_mad_u32_u16 v185, v185, s96, %25\n\t"
        "v_mad_u32_u16 v186, v186, s96, %24\n\t"
        "v_mad_u32_u16 v187, v187, s96, %25\n\t"
        "v_add_u32 v183, v183, %26\n\t"
        "s_cbranch_vccnz 60f\n\t"
        "global_load_lds_dwordx4 v184, %27\n\t"
        "s_addk_i32 m0, 1024\n\t"
        "s_nop 0\n\t"
        "global_load_lds_dwordx4 v185, %27\n\t"
        "s_addk_i32 m0, 1024\n\t"
        "s_nop 0\n\t"
        "global_load_lds_dwordx4 v186, %27\n\t"
        "s_addk_i32 m0, 1024\n\t"
        "s_nop 0\n\t"
        "global_load_lds_dwordx4 v187, %27\n\t"
        "s_addk_i32 m0, 1024\n\t"
        "s_nop 0\n\t"
        "global_load_lds_dwordx4 v183, %28\n\t"
        "s_mov_b32 %12, 1\n\t"
        "60:\n\t"
        "s_mov_b64 exec, s[90:91]\n\t"
        "s_waitcnt lgkmcnt(3)\n\t"
        "v_min_u32 v213, v181, v213\n\t"
        "v_min_u32 v214, v181, v214\n\t"
        "v_min_u32 v215, v181, v215\n\t"
        "v_mad_u32_u24 v213, v201, v180, v213\n\t"
        "v_mad_u32_u24 v214, v202, v180, v214\n\t"
        "v_mad_u32_u24 v215, v203, v180, v215\n\t"
        "ds_write2st64_b32 v213, v225, v237 offset1:10\n\t"
        "s_mov_b64 exec, s[92:93]\n\t"
        "ds_write2st64_b32 v214, v226, v238 offset1:10\n\t"
        "s_mov_b64 exec, s[94:95]\n\t"
        "ds_write2st64_b32 v215, v227, v239 offset1:10\n\t"
        "s_cmp_eq_u64 s[50:51], 0\n\t"
        "s_cbranch_scc1 49f\n\t"
        "s_mov_b64 exec, s[56:57]\n\t"
        "v_mov_b32 v188, 0xff000000\n\t"
        "v_mov_b32 v189, %13\n\t"
        "v_bfe_u32 v190, %4, 7, 8\n\t"
        "ds_write_b8 v190, v191 offset:256\n\t"
        "s_mov_b64 exec, s[58:59]\n\t"
        "v_mov_b32 v188, 0xfe000000\n\t"
        "v_mov_b32 v189, %14\n\t"
        "v_bfe_u32 v190, %5, 7, 8\n\t"
        "ds_write_b8 v190, v191 offset:256\n\t"
        "s_mov_b64 exec, s[60:61]\n\t"
        "v_mov_b32 v188, 0xfd000000\n\t"
        "v_mov_b32 v189, %15\n\t"
        "v_bfe_u32 v190, %6, 7, 8\n\t"
        "ds_write_b8 v190, v191 offset:256\n\t"
        "s_mov_b64 exec, s[62:63]\n\t"
        "v_mov_b32 v188, 0xfc000000\n\t"
        "v_mov_b32 v189, %16\n\t"
        "v_bfe_u32 v190, %7, 7, 8\n\t"
        "ds_write_b8 v190, v191 offset:256\n\t"
        "s_mov_b64 exec, s[50:51]\n\t"
        "s_waitcnt vmcnt(5)\n\t"
        "s_cmp_lg_u32 %12, 0\n\t"
        "s_cbranch_scc1 47f\n\t"
        "s_waitcnt vmcnt(0)\n\t"
        "47:\n\t"
        "v_add_u32 v175, v175, v188\n\t"
        "v_add3_u32 v228, v172, v173, v174\n\t"
        "v_add3_u32 v228, v228, v175, %19\n\t"
        "v_cmp_ne_u32_sdwa s[72:73], v175, %20 src0_sel:BYTE_3 src1_sel:DWORD\n\t"
        "s_nop 0\n\t"
        "v_add_u32_dpp v228, v228, v228 quad_perm:[1,0,3,2] row_mask:0xf bank_mask:0xf\n\t"
        "s_nop 1\n\t"
        "v_add_u32_dpp v228, v228, v228 quad_perm:[2,3,0,1] row_mask:0xf bank_mask:0xf\n\t"
        "s_nop 1\n\t"
        "v_add_u32_dpp v228, v228, v228 row_half_mirror row_mask:0xf bank_mask:0xf\n\t"
        "v_cmp_eq_u32 s[68:69], 0, v228\n\t"
        "s_and_b64 s[72:73], s[72:73], s[68:69]\n\t"
        "s_cselect_b64 exec, 0, s[68:69]\n\t"
        "s_cbranch_execz 48f\n\t"
        "v_cmpx_ne_u32 s[72:73], -1, v172\n\t"
        "v_mul_hi_u32_u24_sdwa v204, v172, %21 dst_sel:DWORD dst_unused:UNUSED_PAD src0_sel:WORD_0 src1_sel:DWORD\n\t"
        "v_and_b32_sdwa v192, v175, v176 dst_sel:DWORD dst_unused:UNUSED_PAD src0_sel:BYTE_0 src1_sel:DWORD\n\t"
        "v_lshl_add_u32 v204, v204, 2, v192\n\t"
        "ds_add_rtn_u32 v204, v204, v191\n\t"
        "v_perm_b32 v216, v175, v172, v177\n\t"
        "v_mad_u32_u16 v228, v172, 32, v189 op_sel:[1,0,0,0]\n\t"
        "v_cmpx_ne_u32 s[74:75], -1, v173\n\t"
        "v_mul_hi_u32_u24_sdwa v205, v173, %21 dst_sel:DWORD dst_unused:UNUSED_PAD src0_sel:WORD_0 src1_sel:DWORD\n\t"
        "v_and_b32_sdwa v193, v175, v176 dst_sel:DWORD dst_unused:UNUSED_PAD src0_sel:BYTE_1 src1_sel:DWORD\n\t"
        "v_lshl_add_u32 v205, v205, 2, v193\n\t"
        "ds_add_rtn_u32 v205, v205, v191\n\t"
        "v_perm_b32 v217, v175, v173, v178\n\t"
        "v_mad_u32_u16 v229, v173, 32, v189 op_sel:[1,0,0,0]\n\t"
        "s_and_b64 exec, exec, s[52:53]\n\t"
        "v_cmpx_ne_u32 s[76:77], -1, v174\n\t"
        "v_mul_hi_u32_u24_sdwa v206, v174, %21 dst_sel:DWORD dst_unused:UNUSED_PAD src0_sel:WORD_0 src1_sel:DWORD\n\t"
        "v_and_b32_sdwa v194, v175, v176 dst_sel:DWORD dst_unused:UNUSED_PAD src0_sel:BYTE_2 src1_sel:DWORD\n\t"
        "v_lshl_add_u32 v206, v206, 2, v194\n\t"
        "ds_add_rtn_u32 v206, v206, v191\n\t"
        "v_perm_b32 v218, v175, v174, v179\n\t"
        "v_mad_u32_u16 v230, v174, 32, v189 op_sel:[1,0,0,0]\n\t"
        "s_mov_b64 s[50:51], 0\n\t"
        "s_and_b64 s[70:71], s[68:69], s[62:63]\n\t"
        "s_xor_b64 s[62:63], s[62:63], s[70:71]\n\t"
        "s_xor_b64 s[68:69], s[68:69], s[70:71]\n\t"
        "s_or_b64 s[50:51], s[50:51], s[62:63]\n\t"
        "s_and_b64 s[70:71], s[68:69], s[60:61]\n\t"
        "s_xor_b64 s[60:61], s[60:61], s[70:71]\n\t"
        "s_xor_b64 s[68:69], s[68:69], s[70:71]\n\t"
        "s_or_b64 s[50:51], s[50:51], s[60:61]\n\t"
        "s_and_b64 s[70:71], s[68:69], s[58:59]\n\t"
        "s_xor_b64 s[58:59], s[58:59], s[70:71]\n\t"
        "s_xor_b64 s[68:69], s[68:69], s[70:71]\n\t"
        "s_or_b64 s[50:51], s[50:51], s[58:59]\n\t"
        "s_and_b64 s[70:71], s[68:69], s[56:57]\n\t"
        "s_xor_b64 s[56:57], s[56:57], s[70:71]\n\t"
        "s_or_b64 s[50:51], s[50:51], s[56:57]\n\t"
        "s_mov_b64 exec, s[72:73]\n\t"
        "s_waitcnt lgkmcnt(0)\n\t"
        "v_min_u32 v204, v181, v204\n\t"
        "v_min_u32 v205, v181, v205\n\t"
        "v_min_u32 v206, v181, v206\n\t"
        "v_mad_u32_u24 v204, v192, v180, v204\n\t"
        "v_mad_u32_u24 v205, v193, v180, v205\n\t"
        "v_mad_u32_u24 v206, v194, v180, v206\n\t"
        "ds_write2st64_b32 v204, v216, v228 offset1:10\n\t"
        "s_mov_b64 exec, s[74:75]\n\t"
        "ds_write2st64_b32 v205, v217, v229 offset1:10\n\t"
        "s_mov_b64 exec, s[76:77]\n\t"
        "ds_write2st64_b32 v206, v218, v230 offset1:10\n\t"
        "s_cbranch_scc0 49f\n\t"
        "48:\n\t"
        "s_mov_b32 s96, 0x1000000\n\t"
        "s_or_b32 %12, %12, 2\n\t"
        "s_mov_b64 exec, s[56:57]\n\t"
        "s_cbranch_execz 40f\n\t"
        "v_add_u32 %4, s96, %4\n\t"
        "s_or_b64 %0, %0, exec\n\t"
        "global_load_dwordx4 v[240:243], %4, %17 sc1\n\t"
        "40:\n\t"
        "s_mov_b64 exec, s[58:59]\n\t"
        "s_cbranch_execz 41f\n\t"
        "v_add_u32 %5, s96, %5\n\t"
        "s_or_b64 %1, %1, exec\n\t"
        "global_load_dwordx4 v[244:247], %5, %17 sc1\n\t"
        "41:\n\t"
        "s_mov_b64 exec, s[60:61]\n\t"
        "s_cbranch_execz 42f\n\t"
        "v_add_u32 %6, s96, %6\n\t"
        "s_or_b64 %2, %2, exec\n\t"
        "global_load_dwordx4 v[248:251], %6, %17 sc1\n\t"
        "42:\n\t"
        "s_mov_b64 exec, s[62:63]\n\t"
        "s_cbranch_execz 43f\n\t"
        "v_add_u32 %7, s96, %7\n\t"
        "s_or_b64 %3, %3, exec\n\t"
        "global_load_dwordx4 v[252:255], %7, %17 sc1\n\t"
        "43:\n\t"
        "49:\n\t"
        "s_or_b32 s54, s54, s55\n\t"
        "s_or_b32 s54, s54, %12\n\t"
        "s_cbranch_scc1 60f\n\t"
        "s_movk_i32 s54, 12\n\t"
        "61:\n\t"
        "s_sub_u32 s54, s54, 1\n\t"
        "s_cbranch_scc1 60f\n\t"
        "s_mov_b64 exec, -1\n\t"
        "ds_read_b128 v[184:187], %22\n\t"
        "ds_read_b32 v183, %23\n\t"
        "s_mov_b64 exec, -1\n\t"
        "s_movk_i32 s96, 128\n\t"
        "s_mov_b32 m0, %29\n\t"
        "s_waitcnt lgkmcnt(0)\n\t"
        "v_max3_u32 v182, v184, v185, v186\n\t"
        "v_max3_u32 v182, v182, v187, v183\n\t"
        "v_cmp_eq_u32 vcc, -1, v182\n\t"
        "v_mad_u32_u16 v184, v184, s96, %24\n\t"
        "v_mad_u32_u16 v185, v185, s96, %25\n\t"
        "v_mad_u32_u16 v186, v186, s96, %24\n\t"
        "v_mad_u32_u16 v187, v187, s96, %25\n\t"
        "v_add_u32 v183, v183, %26\n\t"
        "s_cbranch_vccnz 61b\n\t"
        "global_load_lds_dwordx4 v184, %27\n\t"
        "s_addk_i32 m0, 1024\n\t"
        "s_nop 0\n\t"
        "global_load_lds_dwordx4 v185, %27\n\t"
        "s_addk_i32 m0, 1024\n\t"
        "s_nop 0\n\t"
        "global_load_lds_dwordx4 v186, %27\n\t"
        "s_addk_i32 m0, 1024\n\t"
        "s_nop 0\n\t"
        "global_load_lds_dwordx4 v187, %27\n\t"
        "s_addk_i32 m0, 1024\n\t"
        "s_nop 0\n\t"
        "global_load_lds_dwordx4 v183, %28\n\t"
        "s_mov_b32 %12, 1\n\t"
        "60:\n\t"
        "s_mov_b64 exec, -1\n\t"
        : "=&s"(pend[0]), "=&s"(pend[1]), "=&s"(pend[2]), "=&s"(pend[3]), "+v"(vo[0]), "+v"(vo[1]), "+v"(vo[2]),
          "+v"(vo[3]), "+{v[240:243]}"(cv[0]), "+{v[244:247]}"(cv[1]), "+{v[248:251]}"(cv[2]), "+{v[252:255]}"(cv[3]),
          "=&s"(early)
        : "v"(eb[0]), "v"(eb[1]), "v"(eb[2]), "v"(eb[3]), "s"(ib), "n"(OFF_L + LARR - 4), "v"(tag[0]), "v"(tag[1]),
          "s"(sq), "v"(aLX + wave * LIST_BYTES), "v"(aLE + wave * LIST_BYTES), "v"(cx0), "v"(cx1), "v"(ce), "s"(x),
          "s"(ES), "s"(lds0 + OFF_R0 + wave * RING_SLOT), "v"(32768u + wave * 1024u + lane * 16u)
        :
          "memory", "vcc", "scc", "m0", "v172", "v173", "v174", "v175", "v176", "v177", "v178", "v179", "v180",
          "v181", "v182", "v183", "v184", "v185", "v186", "v187", "v188", "v189", "v190", "v191", "v192", "v193",
          "v194", "v195", "v196", "v197", "v198", "v199", "v200", "v201", "v202", "v203", "v204", "v205", "v206",
          "v207", "v208", "v209", "v210", "v211", "v212", "v213", "v214", "v215", "v216", "v217", "v218", "v219",
          "v220", "v221", "v222", "v223", "v224", "v225", "v226", "v227", "v228", "v229", "v230", "v231", "v232",
          "v233", "v234", "v235", "v236", "v237", "v238", "v239", "s50", "s51", "s52", "s53", "s54", "s55", "s56",
          "s57", "s58", "s59", "s60", "s61", "s62", "s63", "s64", "s65", "s66", "s67", "s68", "s69", "s70", "s71",
          "s72", "s73", "s74", "s75", "s76", "s77", "s78", "s79", "s80", "s81", "s82", "s83", "s84", "s85", "s86",
          "s87", "s88", "s89", "s90", "s91", "s92", "s93", "s94", "s95", "s96");
  auto uni = [&]() {
#pragma unroll
    for (int k = 0; k < 4; ++k)
      pend[k] = ((unsigned long long)(unsigned)__builtin_amdgcn_readfirstlane((unsigned)(pend[k] >> 32)) << 32) |
                (unsigned)__builtin_amdgcn_readfirstlane((unsigned)pend[k]);
  };
  uni();
  early = __builtin_amdgcn_readfirstlane(early);
  just = early;
  if (__builtin_expect((pend[0] | pend[1] | pend[2] | pend[3] | (unsigned long long)(early ^ 1u)) != 0ull, 0))
#pragma clang loop unroll(disable)
  while (true) {
    early &= 1u;
    if (!early) {
      const u32x4 sx = ld128(aLX + wave * LIST_BYTES);
      const unsigned re = *(const LAS unsigned*)(size_t)(aLE + wave * LIST_BYTES);
      if (__ballot(max(max(max(sx[0], sx[1]), max(sx[2], sx[3])), re) == ~0u) == 0ull) {
        gather5(sx, re, lds0 + OFF_R0 + wave * RING_SLOT);
        early = 1u;
        just = 1u;
      }
    }
    if ((pend[0] | pend[1] | pend[2] | pend[3]) == 0ull || --spin < 0) break;
    asm volatile(
        "s_mov_b32 s96, 0x1000000\n\t"
        "v_mov_b32 v181, %17\n\t"
        "v_mov_b32 v180, 160\n\t"
        "v_mov_b32 v176, 0xe0\n\t"
        "v_mov_b32 v177, 0x0c040100\n\t"
        "v_mov_b32 v178, 0x0c050100\n\t"
        "v_mov_b32 v179, 0x0c060100\n\t"
        "s_mov_b32 s52, 0x7f7f7f7f\n\t"
        "s_mov_b32 s53, 0x7f7f7f7f\n\t"
        "v_mov_b32 v191, 4\n\t"
        "s_waitcnt vmcnt(5)\n\t"
        "s_cmp_eq_u32 %21, 1\n\t"
        "s_cbranch_scc1 9f\n\t"
        "s_waitcnt vmcnt(0)\n\t"
        "9:\n\t"
        "v_add3_u32 v228, v240, v241, v242\n\t"
        "v_add3_u32 v229, v244, v245, v246\n\t"
        "v_add3_u32 v230, v248, v249, v250\n\t"
        "v_add3_u32 v231, v252, v253, v254\n\t"
        "v_add3_u32 v228, v228, v243, %18\n\t"
        "v_add3_u32 v229, v229, v247, %18\n\t"
        "v_add3_u32 v230, v230, v251, %18\n\t"
        "v_add3_u32 v231, v231, v255, %18\n\t"
        "v_add_u32_dpp v228, v228, v228 quad_perm:[1,0,3,2] row_mask:0xf bank_mask:0xf\n\t"
        "v_add_u32_dpp v229, v229, v229 quad_perm:[1,0,3,2] row_mask:0xf bank_mask:0xf\n\t"
        "v_add_u32_dpp v230, v230, v230 quad_perm:[1,0,3,2] row_mask:0xf bank_mask:0xf\n\t"
        "v_add_u32_dpp v231, v231, v231 quad_perm:[1,0,3,2] row_mask:0xf bank_mask:0xf\n\t"
        "v_add_u32_dpp v228, v228, v228 quad_perm:[2,3,0,1] row_mask:0xf bank_mask:0xf\n\t"
        "v_add_u32_dpp v229, v229, v229 quad_perm:[2,3,0,1] row_mask:0xf bank_mask:0xf\n\t"
        "v_add_u32_dpp v230, v230, v230 quad_perm:[2,3,0,1] row_mask:0xf bank_mask:0xf\n\t"
        "v_add_u32_dpp v231, v231, v231 quad_perm:[2,3,0,1] row_mask:0xf bank_mask:0xf\n\t"
        "v_add_u32_dpp v228, v228, v228 row_half_mirror row_mask:0xf bank_mask:0xf\n\t"
        "v_add_u32_dpp v229, v229, v229 row_half_mirror row_mask:0xf bank_mask:0xf\n\t"
        "v_add_u32_dpp v230, v230, v230 row_half_mirror row_mask:0xf bank_mask:0xf\n\t"
        "v_add_u32_dpp v231, v231, v231 row_half_mirror row_mask:0xf bank_mask:0xf\n\t"
        "v_cmp_ne_u32_sdwa s[72:73], v243, %19 src0_sel:BYTE_3 src1_sel:DWORD\n\t"
        "v_cmp_gt_u32 s[64:65], 2, v228\n\t"
        "v_cmp_eq_u32 s[56:57], 1, v228\n\t"
        "v_cmp_ne_u32_sdwa s[74:75], v247, %19 src0_sel:BYTE_3 src1_sel:DWORD\n\t"
        "v_cmp_gt_u32 s[66:67], 2, v229\n\t"
        "v_cmp_eq_u32 s[58:59], 1, v229\n\t"
        "v_cmp_ne_u32_sdwa s[76:77], v251, %19 src0_sel:BYTE_3 src1_sel:DWORD\n\t"
        "v_cmp_gt_u32 s[68:69], 2, v230\n\t"
        "v_cmp_eq_u32 s[60:61], 1, v230\n\t"
        "v_cmp_ne_u32_sdwa s[78:79], v255, %19 src0_sel:BYTE_3 src1_sel:DWORD\n\t"
        "v_cmp_gt_u32 s[70:71], 2, v231\n\t"
        "v_cmp_eq_u32 s[62:63], 1, v231\n\t"
        "s_and_b64 s[64:65], s[64:65], %0\n\t"
        "s_and_b64 s[72:73], s[72:73], s[64:65]\n\t"
        "s_cselect_b64 s[64:65], 0, s[64:65]\n\t"
        "s_and_b64 s[56:57], s[56:57], s[64:65]\n\t"
        "s_andn2_b64 %0, %0, s[64:65]\n\t"
        "s_cbranch_scc0 10f\n\t"
        "s_mov_b64 exec, %0\n\t"
        "global_load_dwordx4 v[240:243], %4, %16 sc1\n\t"
        "10:\n\t"
        "s_and_b64 s[66:67], s[66:67], %1\n\t"
        "s_and_b64 s[74:75], s[74:75], s[66:67]\n\t"
        "s_cselect_b64 s[66:67], 0, s[66:67]\n\t"
        "s_and_b64 s[58:59], s[58:59], s[66:67]\n\t"
        "s_andn2_b64 %1, %1, s[66:67]\n\t"
        "s_cbranch_scc0 11f\n\t"
        "s_mov_b64 exec, %1\n\t"
        "global_load_dwordx4 v[244:247], %5, %16 sc1\n\t"
        "11:\n\t"
        "s_and_b64 s[68:69], s[68:69], %2\n\t"
        "s_and_b64 s[76:77], s[76:77], s[68:69]\n\t"
        "s_cselect_b64 s[68:69], 0, s[68:69]\n\t"
        "s_and_b64 s[60:61], s[60:61], s[68:69]\n\t"
        "s_andn2_b64 %2, %2, s[68:69]\n\t"
        "s_cbranch_scc0 12f\n\t"
        "s_mov_b64 exec, %2\n\t"
        "global_load_dwordx4 v[248:251], %6, %16 sc1\n\t"
        "12:\n\t"
        "s_and_b64 s[70:71], s[70:71], %3\n\t"
        "s_and_b64 s[78:79], s[78:79], s[70:71]\n\t"
        "s_cselect_b64 s[70:71], 0, s[70:71]\n\t"
        "s_and_b64 s[62:63], s[62:63], s[70:71]\n\t"
        "s_andn2_b64 %3, %3, s[70:71]\n\t"
        "s_cbranch_scc0 13f\n\t"
        "s_mov_b64 exec, %3\n\t"
        "global_load_dwordx4 v[252:255], %7, %16 sc1\n\t"
        "13:\n\t"
        "s_mov_b64 s[54:55], 0\n\t"
        "s_mov_b64 s[72:73], 0\n\t"
        "s_mov_b64 exec, s[64:65]\n\t"
        "s_cbranch_execz 20f\n\t"
        "v_cmpx_ne_u32 s[72:73], -1, v240\n\t"
        "v_mul_hi_u32_u24_sdwa v204, v240, %20 dst_sel:DWORD dst_unused:UNUSED_PAD src0_sel:WORD_0 src1_sel:DWORD\n\t"
        "v_and_b32_sdwa v192, v243, v176 dst_sel:DWORD dst_unused:UNUSED_PAD src0_sel:BYTE_0 src1_sel:DWORD\n\t"
        "v_lshl_add_u32 v204, v204, 2, v192\n\t"
        "ds_add_rtn_u32 v204, v204, v191\n\t"
        "v_perm_b32 v216, v243, v240, v177\n\t"
        "v_mad_u32_u16 v228, v240, 32, %12 op_sel:[1,0,0,0]\n\t"
        "v_cmpx_ne_u32 s[74:75], -1, v241\n\t"
        "v_mul_hi_u32_u24_sdwa v205, v241, %20 dst_sel:DWORD dst_unused:UNUSED_PAD src0_sel:WORD_0 src1_sel:DWORD\n\t"
        "v_and_b32_sdwa v193, v243, v176 dst_sel:DWORD dst_unused:UNUSED_PAD src0_sel:BYTE_1 src1_sel:DWORD\n\t"
        "v_lshl_add_u32 v205, v205, 2, v193\n\t"
        "ds_add_rtn_u32 v205, v205, v191\n\t"
        "v_perm_b32 v217, v243, v241, v178\n\t"
        "v_mad_u32_u16 v229, v241, 32, %12 op_sel:[1,0,0,0]\n\t"
        "s_and_b64 exec, exec, s[52:53]\n\t"
        "v_cmpx_ne_u32 s[76:77], -1, v242\n\t"
        "v_mul_hi_u32_u24_sdwa v206, v242, %20 dst_sel:DWORD dst_unused:UNUSED_PAD src0_sel:WORD_0 src1_sel:DWORD\n\t"
        "v_and_b32_sdwa v194, v243, v176 dst_sel:DWORD dst_unused:UNUSED_PAD src0_sel:BYTE_2 src1_sel:DWORD\n\t"
        "v_lshl_add_u32 v206, v206, 2, v194\n\t"
        "ds_add_rtn_u32 v206, v206, v191\n\t"
        "v_perm_b32 v218, v243, v242, v179\n\t"
        "v_mad_u32_u16 v230, v242, 32, %12 op_sel:[1,0,0,0]\n\t"
        "s_or_b64 s[54:55], s[54:55], s[56:57]\n\t"
        "20:\n\t"
        "s_mov_b64 s[78:79], 0\n\t"
        "s_mov_b64 exec, s[66:67]\n\t"
        "s_cbranch_execz 21f\n\t"
        "v_cmpx_ne_u32 s[78:79], -1, v244\n\t"
        "v_mul_hi_u32_u24_sdwa v207, v244, %20 dst_sel:DWORD dst_unused:UNUSED_PAD src0_sel:WORD_0 src1_sel:DWORD\n\t"
        "v_and_b32_sdwa v195, v247, v176 dst_sel:DWORD dst_unused:UNUSED_PAD src0_sel:BYTE_0 src1_sel:DWORD\n\t"
        "v_lshl_add_u32 v207, v207, 2, v195\n\t"
        "ds_add_rtn_u32 v207, v207, v191\n\t"
        "v_perm_b32 v219, v247, v244, v177\n\t"
        "v_mad_u32_u16 v231, v244, 32, %13 op_sel:[1,0,0,0]\n\t"
        "v_cmpx_ne_u32 s[80:81], -1, v245\n\t"
        "v_mul_hi_u32_u24_sdwa v208, v245, %20 dst_sel:DWORD dst_unused:UNUSED_PAD src0_sel:WORD_0 src1_sel:DWORD\n\t"
        "v_and_b32_sdwa v196, v247, v176 dst_sel:DWORD dst_unused:UNUSED_PAD src0_sel:BYTE_1 src1_sel:DWORD\n\t"
        "v_lshl_add_u32 v208, v208, 2, v196\n\t"
        "ds_add_rtn_u32 v208, v208, v191\n\t"
        "v_perm_b32 v220, v247, v245, v178\n\t"
        "v_mad_u32_u16 v232, v245, 32, %13 op_sel:[1,0,0,0]\n\t"
        "s_and_b64 exec, exec, s[52:53]\n\t"
        "v_cmpx_ne_u32 s[82:83], -1, v246\n\t"
        "v_mul_hi_u32_u24_sdwa v209, v246, %20 dst_sel:DWORD dst_unused:UNUSED_PAD src0_sel:WORD_0 src1_sel:DWORD\n\t"
        "v_and_b32_sdwa v197, v247, v176 dst_sel:DWORD dst_unused:UNUSED_PAD src0_sel:BYTE_2 src1_sel:DWORD\n\t"
        "v_lshl_add_u32 v209, v209, 2, v197\n\t"
        "ds_add_rtn_u32 v209, v209, v191\n\t"
        "v_perm_b32 v221, v247, v246, v179\n\t"
        "v_mad_u32_u16 v233, v246, 32, %13 op_sel:[1,0,0,0]\n\t"
        "s_or_b64 s[54:55], s[54:55], s[58:59]\n\t"
        "21:\n\t"
        "s_mov_b64 s[84:85], 0\n\t"
        "s_mov_b64 exec, s[68:69]\n\t"
        "s_cbranch_execz 22f\n\t"
        "v_cmpx_ne_u32 s[84:85], -1, v248\n\t"
        "v_mul_hi_u32_u24_sdwa v210, v248, %20 dst_sel:DWORD dst_unused:UNUSED_PAD src0_sel:WORD_0 src1_sel:DWORD\n\t"
        "v_and_b32_sdwa v198, v251, v176 dst_sel:DWORD dst_unused:UNUSED_PAD src0_sel:BYTE_0 src1_sel:DWORD\n\t"
        "v_lshl_add_u32 v210, v210, 2, v198\n\t"
        "ds_add_rtn_u32 v210, v210, v191\n\t"
        "v_perm_b32 v222, v251, v248, v177\n\t"
        "v_mad_u32_u16 v234, v248, 32, %14 op_sel:[1,0,0,0]\n\t"
        "v_cmpx_ne_u32 s[86:87], -1, v249\n\t"
        "v_mul_hi_u32_u24_sdwa v211, v249, %20 dst_sel:DWORD dst_unused:UNUSED_PAD src0_sel:WORD_0 src1_sel:DWORD\n\t"
        "v_and_b32_sdwa v199, v251, v176 dst_sel:DWORD dst_unused:UNUSED_PAD src0_sel:BYTE_1 src1_sel:DWORD\n\t"
        "v_lshl_add_u32 v211, v211, 2, v199\n\t"
        "ds_add_rtn_u32 v211, v211, v191\n\t"
        "v_perm_b32 v223, v251, v249, v178\n\t"
        "v_mad_u32_u16 v235, v249, 32, %14 op_sel:[1,0,0,0]\n\t"
        "s_and_b64 exec, exec, s[52:53]\n\t"
        "v_cmpx_ne_u32 s[88:89], -1, v250\n\t"
        "v_mul_hi_u32_u24_sdwa v212, v250, %20 dst_sel:DWORD dst_unused:UNUSED_PAD src0_sel:WORD_0 src1_sel:DWORD\n\t"
        "v_and_b32_sdwa v200, v251, v176 dst_sel:DWORD dst_unused:UNUSED_PAD src0_sel:BYTE_2 src1_sel:DWORD\n\t"
        "v_lshl_add_u32 v212, v212, 2, v200\n\t"
        "ds_add_rtn_u32 v212, v212, v191\n\t"
        "v_perm_b32 v224, v251, v250, v179\n\t"
        "v_mad_u32_u16 v236, v250, 32, %14 op_sel:[1,0,0,0]\n\t"
        "s_or_b64 s[54:55], s[54:55], s[60:61]\n\t"
        "22:\n\t"
        "s_mov_b64 s[90:91], 0\n\t"
        "s_mov_b64 exec, s[70:71]\n\t"
        "s_cbranch_execz 23f\n\t"
        "v_cmpx_ne_u32 s[90:91], -1, v252\n\t"
        "v_mul_hi_u32_u24_sdwa v213, v252, %20 dst_sel:DWORD dst_unused:UNUSED_PAD src0_sel:WORD_0 src1_sel:DWORD\n\t"
        "v_and_b32_sdwa v201, v255, v176 dst_sel:DWORD dst_unused:UNUSED_PAD src0_sel:BYTE_0 src1_sel:DWORD\n\t"
        "v_lshl_add_u32 v213, v213, 2, v201\n\t"
        "ds_add_rtn_u32 v213, v213, v191\n\t"
        "v_perm_b32 v225, v255, v252, v177\n\t"
        "v_mad_u32_u16 v237, v252, 32, %15 op_sel:[1,0,0,0]\n\t"
        "v_cmpx_ne_u32 s[92:93], -1, v253\n\t"
        "v_mul_hi_u32_u24_sdwa v214, v253, %20 dst_sel:DWORD dst_unused:UNUSED_PAD src0_sel:WORD_0 src1_sel:DWORD\n\t"
        "v_and_b32_sdwa v202, v255, v176 dst_sel:DWORD dst_unused:UNUSED_PAD src0_sel:BYTE_1 src1_sel:DWORD\n\t"
        "v_lshl_add_u32 v214, v214, 2, v202\n\t"
        "ds_add_rtn_u32 v214, v214, v191\n\t"
        "v_perm_b32 v226, v255, v253, v178\n\t"
        "v_mad_u32_u16 v238, v253, 32, %15 op_sel:[1,0,0,0]\n\t"
        "s_and_b64 exec, exec, s[52:53]\n\t"
        "v_cmpx_ne_u32 s[94:95], -1, v254\n\t"
        "v_mul_hi_u32_u24_sdwa v215, v254, %20 dst_sel:DWORD dst_unused:UNUSED_PAD src0_sel:WORD_0 src1_sel:DWORD\n\t"
        "v_and_b32_sdwa v203, v255, v176 dst_sel:DWORD dst_unused:UNUSED_PAD src0_sel:BYTE_2 src1_sel:DWORD\n\t"
        "v_lshl_add_u32 v215, v215, 2, v203\n\t"
        "ds_add_rtn_u32 v215, v215, v191\n\t"
        "v_perm_b32 v227, v255, v254, v179\n\t"
        "v_mad_u32_u16 v239, v254, 32, %15 op_sel:[1,0,0,0]\n\t"
        "s_or_b64 s[54:55], s[54:55], s[62:63]\n\t"
        "23:\n\t"
        "s_cmp_eq_u64 s[54:55], 0\n\t"
        "s_cbranch_scc1 59f\n\t"
        "s_mov_b64 exec, s[56:57]\n\t"
        "s_cbranch_execz 40f\n\t"
        "v_bfe_u32 v188, %4, 7, 8\n\t"
        "v_lshrrev_b32 v189, 24, %4\n\t"
        "v_add_u32 v189, 1, v189\n\t"
        "ds_write_b8 v188, v189 offset:256\n\t"
        "v_add_u32 %4, s96, %4\n\t"
        "v_mov_b32 v190, 0\n\t"
        "v_cmpx_eq_u32 vcc, 2, v189\n\t"
        "ds_write_b32 v190, v191 offset:20\n\t"
        "s_andn2_b64 exec, s[56:57], vcc\n\t"
        "s_cbranch_execz 40f\n\t"
        "s_or_b64 %0, %0, exec\n\t"
        "global_load_dwordx4 v[240:243], %4, %16 sc1\n\t"
        "40:\n\t"
        "s_mov_b64 exec, s[58:59]\n\t"
        "s_cbranch_execz 41f\n\t"
        "v_bfe_u32 v188, %5, 7, 8\n\t"
        "v_lshrrev_b32 v189, 24, %5\n\t"
        "v_add_u32 v189, 1, v189\n\t"
        "ds_write_b8 v188, v189 offset:256\n\t"
        "v_add_u32 %5, s96, %5\n\t"
        "v_mov_b32 v190, 0\n\t"
        "v_cmpx_eq_u32 vcc, 2, v189\n\t"
        "ds_write_b32 v190, v191 offset:20\n\t"
        "s_andn2_b64 exec, s[58:59], vcc\n\t"
        "s_cbranch_execz 41f\n\t"
        "s_or_b64 %1, %1, exec\n\t"
        "global_load_dwordx4 v[244:247], %5, %16 sc1\n\t"
        "41:\n\t"
        "s_mov_b64 exec, s[60:61]\n\t"
        "s_cbranch_execz 42f\n\t"
        "v_bfe_u32 v188, %6, 7, 8\n\t"
        "v_lshrrev_b32 v189, 24, %6\n\t"
        "v_add_u32 v189, 1, v189\n\t"
        "ds_write_b8 v188, v189 offset:256\n\t"
        "v_add_u32 %6, s96, %6\n\t"
        "v_mov_b32 v190, 0\n\t"
        "v_cmpx_eq_u32 vcc, 2, v189\n\t"
        "ds_write_b32 v190, v191 offset:20\n\t"
        "s_andn2_b64 exec, s[60:61], vcc\n\t"
        "s_cbranch_execz 42f\n\t"
        "s_or_b64 %2, %2, exec\n\t"
        "global_load_dwordx4 v[248:251], %6, %16 sc1\n\t"
        "42:\n\t"
        "s_mov_b64 exec, s[62:63]\n\t"
        "s_cbranch_execz 43f\n\t"
        "v_bfe_u32 v188, %7, 7, 8\n\t"
        "v_lshrrev_b32 v189, 24, %7\n\t"
        "v_add_u32 v189, 1, v189\n\t"
        "ds_write_b8 v188, v189 offset:256\n\t"
        "v_add_u32 %7, s96, %7\n\t"
        "v_mov_b32 v190, 0\n\t"
        "v_cmpx_eq_u32 vcc, 2, v189\n\t"
        "ds_write_b32 v190, v191 offset:20\n\t"
        "s_andn2_b64 exec, s[62:63], vcc\n\t"
        "s_cbranch_execz 43f\n\t"
        "s_or_b64 %3, %3, exec\n\t"
        "global_load_dwordx4 v[252:255], %7, %16 sc1\n\t"
        "43:\n\t"
        "59:\n\t"
        "s_waitcnt lgkmcnt(0)\n\t"
        "s_mov_b64 exec, s[72:73]\n\t"
        "s_cbranch_execz 30f\n\t"
        "v_min_u32 v204, v181, v204\n\t"
        "v_min_u32 v205, v181, v205\n\t"
        "v_min_u32 v206, v181, v206\n\t"
        "v_mad_u32_u24 v204, v192, v180, v204\n\t"
        "v_mad_u32_u24 v205, v193, v180, v205\n\t"
        "v_mad_u32_u24 v206, v194, v180, v206\n\t"
        "ds_write2st64_b32 v204, v216, v228 offset1:10\n\t"
        "s_mov_b64 exec, s[74:75]\n\t"
        "ds_write2st64_b32 v205, v217, v229 offset1:10\n\t"
        "s_mov_b64 exec, s[76:77]\n\t"
        "ds_write2st64_b32 v206, v218, v230 offset1:10\n\t"
        "30:\n\t"
        "s_mov_b64 exec, s[78:79]\n\t"
        "s_cbranch_execz 31f\n\t"
        "v_min_u32 v207, v181, v207\n\t"
        "v_min_u32 v208, v181, v208\n\t"
        "v_min_u32 v209, v181, v209\n\t"
        "v_mad_u32_u24 v207, v195, v180, v207\n\t"
        "v_mad_u32_u24 v208, v196, v180, v208\n\t"
        "v_mad_u32_u24 v209, v197, v180, v209\n\t"
        "ds_write2st64_b32 v207, v219, v231 offset1:10\n\t"
        "s_mov_b64 exec, s[80:81]\n\t"
        "ds_write2st64_b32 v208, v220, v232 offset1:10\n\t"
        "s_mov_b64 exec, s[82:83]\n\t"
        "ds_write2st64_b32 v209, v221, v233 offset1:10\n\t"
        "31:\n\t"
        "s_mov_b64 exec, s[84:85]\n\t"
        "s_cbranch_execz 32f\n\t"
        "v_min_u32 v210, v181, v210\n\t"
        "v_min_u32 v211, v181, v211\n\t"
        "v_min_u32 v212, v181, v212\n\t"
        "v_mad_u32_u24 v210, v198, v180, v210\n\t"
        "v_mad_u32_u24 v211, v199, v180, v211\n\t"
        "v_mad_u32_u24 v212, v200, v180, v212\n\t"
        "ds_write2st64_b32 v210, v222, v234 offset1:10\n\t"
        "s_mov_b64 exec, s[86:87]\n\t"
        "ds_write2st64_b32 v211, v223, v235 offset1:10\n\t"
        "s_mov_b64 exec, s[88:89]\n\t"
        "ds_write2st64_b32 v212, v224, v236 offset1:10\n\t"
        "32:\n\t"
        "s_mov_b64 exec, s[90:91]\n\t"
        "s_cbranch_execz 33f\n\t"
        "v_min_u32 v213, v181, v213\n\t"
        "v_min_u32 v214, v181, v214\n\t"
        "v_min_u32 v215, v181, v215\n\t"
        "v_mad_u32_u24 v213, v201, v180, v213\n\t"
        "v_mad_u32_u24 v214, v202, v180, v214\n\t"
        "v_mad_u32_u24 v215, v203, v180, v215\n\t"
        "ds_write2st64_b32 v213, v225, v237 offset1:10\n\t"
        "s_mov_b64 exec, s[92:93]\n\t"
        "ds_write2st64_b32 v214, v226, v238 offset1:10\n\t"
        "s_mov_b64 exec, s[94:95]\n\t"
        "ds_write2st64_b32 v215, v227, v239 offset1:10\n\t"
        "33:\n\t"
        "s_mov_b64 exec, -1\n\t"
        : "+&s"(pend[0]), "+&s"(pend[1]), "+&s"(pend[2]), "+&s"(pend[3]), "+v"(vo[0]), "+v"(vo[1]), "+v"(vo[2]),
          "+v"(vo[3]), "+{v[240:243]}"(cv[0]), "+{v[244:247]}"(cv[1]), "+{v[248:251]}"(cv[2]), "+{v[252:255]}"(cv[3])
        : "v"(eb[0]), "v"(eb[1]), "v"(eb[2]), "v"(eb[3]), "s"(ib), "n"(OFF_L + LARR - 4), "v"(tag[0]), "v"(tag[1]),
          "s"(sq), "s"(just)
        :
          "memory", "vcc", "scc", "v176", "v177", "v178", "v179", "v180", "v181", "v188", "v189", "v190", "v191",
          "v192", "v193", "v194", "v195", "v196", "v197", "v198", "v199", "v200", "v201", "v202", "v203", "v204",
          "v205", "v206", "v207", "v208", "v209", "v210", "v211", "v212", "v213", "v214", "v215", "v216", "v217",
          "v218", "v219", "v220", "v221", "v222", "v223", "v224", "v225", "v226", "v227", "v228", "v229", "v230",
          "v231", "v232", "v233", "v234", "v235", "v236", "v237", "v238", "v239", "s52", "s53", "s54", "s55", "s56",
          "s57", "s58", "s59", "s60", "s61", "s62", "s63", "s64", "s65", "s66", "s67", "s68", "s69", "s70", "s71",
          "s72", "s73", "s74", "s75", "s76", "s77", "s78", "s79", "s80", "s81", "s82", "s83", "s84", "s85", "s86",
          "s87", "s88", "s89", "s90", "s91", "s92", "s93", "s94", "s95", "s96");
    uni();
    just = 0u;
  }
  *(LAS unsigned*)((LAS char*)smem + OFF_CNT + 16 + wave * 32) = early;
  asm volatile("s_waitcnt lgkmcnt(0)\n\ts_barrier" ::: "memory");
  if (wave >= 4) __builtin_amdgcn_s_setprio(1);
  const u32x4 cw = *(const LAS u32x4*)((LAS char*)smem + OFF_CNT + rid * 32);
  const unsigned ef = *(const LAS unsigned*)((LAS char*)smem + OFF_CNT + 16 + rid * 32);
  unsigned slowf = *(const LAS unsigned*)((LAS char*)smem + OFF_CNT + 20);
  asm volatile("" : "+v"(slowf));
  const int key = (int)((cw[0] + cw[1] + cw[2] + cw[3]) * 2u + rid);
  int gb;
  {
    unsigned acc = 0u;
#define RK(k) acc = __builtin_amdgcn_alignbit(acc, (unsigned)(__builtin_amdgcn_update_dpp(0, key, 0x120 + k, 0xf, 0xf, false) - key), 31)
    RK(1); RK(2); RK(3); RK(4); RK(5); RK(6); RK(7);
#undef RK
    const int want = RANKOF(wave);
    gb = __builtin_ctzll(__ballot(__builtin_popcount(acc) == want)) & 7;
  }
  const unsigned ring0 = lds0 + OFF_R0 + gb * RING_SLOT;
  const unsigned la = l0 + gb * LIST_BYTES;
  u32x16 XAf;
#pragma unroll
  for (int q = 0; q < 4; ++q) {
    const u32x4 t = ld128(ring0 + ringd + q * 1024 + lane * 16);
#pragma unroll
    for (int j = 0; j < 4; ++j) XAf[4 * q + j] = t[j];
  }
  unsigned sl[4];
#pragma unroll
  for (int q = 0; q < 4; ++q) sl[q] = __builtin_amdgcn_readlane(cw[q], gb) - (l0 + q * SUBL);
  const unsigned o2 = sl[0] + sl[1], o3 = o2 + sl[2];
  const bool slow = o3 + sl[3] > LCAP * 4u || max(max(sl[0], sl[1]), max(sl[2], sl[3])) > SUBL - 4u ||
                    __builtin_amdgcn_readfirstlane(slowf) != 0u;
  const int cnt = slow ? 0 : (int)(o3 + sl[3]) >> 2;
  const unsigned cg = (bu * 8 + gb) * 32;
  u32x16 AD;
  AD[2] = aLX + gb * LIST_BYTES;
  AD[3] = aLE + gb * LIST_BYTES;
  AD[4] = cx0; AD[5] = cx1; AD[6] = ce;
  auto issue_gather = [&](int tile, int rs) {
    const u32x4 sx = ld128(AD[2] + tile * 128);
    glds16e(ES, *(const LAS unsigned*)(size_t)(AD[3] + tile * 128), AD[6], ring0 + rs * ringd + 4096);
#pragma unroll
    for (int q = 0; q < 4; ++q) glds16x(x, sx[q], AD[4 + (q & 1)], ring0 + rs * ringd + q * 1024);
  };
  auto pads = [&](int cnt, int lane) __attribute__((always_inline)) {
    const int u = cnt + lane;
    const unsigned c = cg + ((u >> 2) & 7) + 8 * (u & 3), n = nodeof(c);
    const unsigned padA = n | (__builtin_amdgcn_readlane(c, __builtin_ctzll(__ballot((n * Mw + Cw) >> 16 != c))) & 0xffu) << 16;
    const unsigned pa = la + u * 4;
    *(LAS unsigned*)(size_t)pa = padA;
    *(LAS unsigned*)(size_t)(pa + 256) = padA;
    *(LAS unsigned*)(size_t)(pa + LARR) = 0u;
    *(LAS unsigned*)(size_t)(pa + LARR + 256) = 0u;
  };
  constexpr int GEND = 64 + NCH;
  int gs = 0, gp = 0;
  auto gen = [&]() __attribute__((always_inline)) {
    int l2, n = 0;
    asm volatile("v_mbcnt_lo_u32_b32 %0, -1, 0\n\tv_mbcnt_hi_u32_b32 %0, -1, %0" : "=v"(l2));
    const LAS unsigned char* xf = (const LAS unsigned char*)smem + OFF_XF;
    auto put = [&](bool ok, unsigned t, unsigned c, int cc) {
      ok = ok && (int)(c >> 5) == gb;
      const unsigned long long bm = __ballot(ok);
      const unsigned pa = la + 4u * (n + __builtin_amdgcn_mbcnt_hi((unsigned)(bm >> 32), __builtin_amdgcn_mbcnt_lo((unsigned)bm, 0u)));
      if (ok) {
        *(LAS unsigned*)(size_t)pa = (t & 0xffffu) | (c << 16);
        *(LAS unsigned*)(size_t)(pa + LARR) = ((unsigned)(cc * chunk) + (t >> 16)) * 32u;
      }
      n += __builtin_popcountll(bm);
    };
#pragma unroll 1
    while (gs < GEND && n <= LCAP - 192) {
      if (gs < 64) {
        const int cc = (gs & 31) * 8 + (l2 >> 3);
        const unsigned* pp = reinterpret_cast<const unsigned*>(ib + (gs >> 5) * IEXT + (gs & 31) * 1024 + l2 * 16);
        const bool live = gs < 32 || xf[cc] != 0;
        const u32x2 tg = *(const LAS u32x2*)((LAS char*)smem + OFF_CNT + 56);
#pragma unroll 1
        for (int i = 0; i < (1 << 20); ++i) {
          const unsigned m = ldd(pp + 3);
          unsigned sm_ = ldd(pp) + ldd(pp + 1) + ldd(pp + 2) + m + tg[0];
          sm_ += __builtin_amdgcn_update_dpp(0u, sm_, 0xB1, 0xf, 0xf, false);
          sm_ += __builtin_amdgcn_update_dpp(0u, sm_, 0x4E, 0xf, 0xf, false);
          sm_ += __builtin_amdgcn_update_dpp(0u, sm_, 0x141, 0xf, 0xf, false);
          if (__ballot(live && (sm_ > 1u || m >> 24 != tg[1])) == 0ull) break;
        }
        const unsigned m = ldd(pp + 3);
#pragma unroll 1
        for (int j = 0; j < 3; ++j) {
          const unsigned t = ldd(pp + j);
          put(live && t != ~0u && (j < 2 || (l2 & 7) != 7), t, (m >> (8 * j)) & 0xffu, cc);
        }
        ++gs;
      } else {
        const int c2 = gs - 64 + l2;
        const unsigned long long fm = __ballot(c2 < NCH && xf[min(c2, NCH - 1)] == 2);
        if (fm == 0ull) { gs = min(gs + 64, GEND); continue; }
        gs += __builtin_ctzll(fm);
        const int cc = gs - 64;
        const unsigned long long ds = ldq(FL + 512 + bu * NCH + cc);
        const int len = __builtin_amdgcn_readfirstlane((int)(ds >> 32));
        const unsigned long long* tp = FL + (TS + (unsigned)ds);
#pragma unroll 1
        for (; gp < len && n <= LCAP - 64; gp += 64) {
          const unsigned long long t = ldq(tp + min(gp + l2, len - 1));
          put(gp + l2 < len, (unsigned)t, (unsigned)(t >> 32), cc);
        }
        if (gp >= len) { gp = 0; ++gs; }
      }
    }
    __builtin_amdgcn_s_waitcnt(0x0F70);
    pads(n, l2);
    return n;
  };
  const bool quick = !slow && sl[0] >= 256u && __builtin_amdgcn_readlane(ef, gb) != 0u;
  auto gather1 = [&](unsigned skip) {
    u32x4 sx = ld128(AD[2] + 128);
    unsigned re = *(const LAS unsigned*)(size_t)(AD[3] + 128);
    asm volatile("s_cmp_lg_u32 %13, 0\n\t"
                 "s_cbranch_scc1 1f\n\t"
                 "s_add_i32 m0, %11, 4096\n\t"
                 "v_add_u32 %4, %4, %8\n\t"
                 "v_mad_u32_u16 %0, %0, %12, %6\n\t"
                 "global_load_lds_dwordx4 %4, %10\n\t"
                 "s_mov_b32 m0, %11\n\t"
                 "v_mad_u32_u16 %1, %1, %12, %7\n\t"
                 "global_load_lds_dwordx4 %0, %9\n\t"
                 "s_addk_i32 m0, 1024\n\t"
                 "v_mad_u32_u16 %2, %2, %12, %6\n\t"
                 "global_load_lds_dwordx4 %1, %9\n\t"
                 "s_addk_i32 m0, 1024\n\t"
                 "v_mad_u32_u16 %3, %3, %12, %7\n\t"
                 "global_load_lds_dwordx4 %2, %9\n\t"
                 "s_addk_i32 m0, 1024\n\t"
                 "s_nop 0\n\t"
                 "global_load_lds_dwordx4 %3, %9\n"
                 "1:"
                 : "+v"(sx[0]), "+v"(sx[1]), "+v"(sx[2]), "+v"(sx[3]), "+v"(re), "+{v[128:143]}"(XAf)
                 : "v"(cx0), "v"(cx1), "v"(ce), "s"(x), "s"(ES), "s"(ring0 + ringd), "s"(128u), "s"(skip)
                 : "memory", "m0", "scc");
  };
  unsigned mode;
  asm volatile("s_cmp_lg_u64 %1, 0\n\ts_cselect_b32 %0, %2, 0" : "=s"(mode) : "s"(__ballot(quick)), "s"(1 + wave / G1W) : "scc");
  gather1(mode ^ 2u);
  if (__builtin_expect(!slow, 1)) {
    const unsigned p0 = la + lane * 4;
    asm volatile(
        "ds_read2st64_b32 v[222:223], %1 offset0:2 offset1:12\n\t"
        "ds_read2st64_b32 v[224:225], %1 offset0:3 offset1:13\n\t"
        "ds_read2st64_b32 v[226:227], %1 offset0:4 offset1:14\n\t"
        "ds_read2st64_b32 v[228:229], %0 offset0:5 offset1:15\n\t"
        "ds_read2st64_b32 v[230:231], %0 offset0:6 offset1:16\n\t"
        "ds_read2st64_b32 v[232:233], %0 offset0:7 offset1:17\n\t"
        "ds_read2st64_b32 v[234:235], %1 offset0:7 offset1:17\n\t"
        "ds_read2st64_b32 v[236:237], %1 offset0:8 offset1:18\n\t"
        "ds_read2st64_b32 v[238:239], %1 offset0:9 offset1:19\n\t"
        "s_waitcnt lgkmcnt(8)\n\t"
        "ds_write2st64_b32 %2, v222, v223 offset0:0 offset1:10\n\t"
        "s_waitcnt lgkmcnt(8)\n\t"
        "ds_write2st64_b32 %2, v224, v225 offset0:1 offset1:11\n\t"
        "s_waitcnt lgkmcnt(8)\n\t"
        "ds_write2st64_b32 %2, v226, v227 offset0:2 offset1:12\n\t"
        "s_waitcnt lgkmcnt(8)\n\t"
        "ds_write2st64_b32 %3, v228, v229 offset0:0 offset1:10\n\t"
        "s_waitcnt lgkmcnt(8)\n\t"
        "ds_write2st64_b32 %3, v230, v231 offset0:1 offset1:11\n\t"
        "s_waitcnt lgkmcnt(8)\n\t"
        "ds_write2st64_b32 %3, v232, v233 offset0:2 offset1:12\n\t"
        "s_waitcnt lgkmcnt(8)\n\t"
        "ds_write2st64_b32 %4, v234, v235 offset0:0 offset1:10\n\t"
        "s_waitcnt lgkmcnt(8)\n\t"
        "ds_write2st64_b32 %4, v236, v237 offset0:1 offset1:11\n\t"
        "s_mov_b64 exec, 0xffffffff\n\t"
        "s_waitcnt lgkmcnt(8)\n\t"
        "ds_write2st64_b32 %4, v238, v239 offset0:2 offset1:12\n\t"
        "s_mov_b64 exec, -1"
        :: "v"(p0), "v"(p0 + 128u), "v"(p0 + sl[0]), "v"(p0 + o2), "v"(p0 + o3)
        : "memory", "v222", "v223", "v224", "v225", "v226", "v227", "v228", "v229", "v230", "v231", "v232", "v233", "v234",
          "v235", "v236", "v237", "v238", "v239");
  }
  pads(cnt, lane);
  AD[0] = aLS4 + gb * LIST_BYTES;
  AD[1] = aLS1 + gb * LIST_BYTES;
  AD[7] = cS - gb * 32 * TPITCH;
  AD[8] = cSt - gb * 32 * TPITCH;
#pragma unroll
  for (int t = 0; t < 4; ++t) AD[9 + t] = fo[t] + gb * RING_SLOT;
  AD[13] = foe + gb * RING_SLOT;
  AD[14] = lds0 + lane * 16;
  AD[15] = to + 18 * 1024 + h * 4096;
  if (__builtin_expect(mode == 0u, 0)) asm volatile("s_waitcnt vmcnt(0)\n\ts_barrier" ::: "memory");
  f32x16 X[2], U[2];
  if (__builtin_expect(mode == 0u, 0)) {
    issue_gather(0, 0);
    issue_gather(1, 1);
  }
  {
    int nt = __builtin_amdgcn_readfirstlane(max((cnt - 1) >> 5, 0));
    asm volatile(
        "s_movk_i32 s94, 128\n\t"
        "s_movk_i32 s95, 80\n\t"
        "s_mov_b32 s96, %[cu]\n\t"
        "ds_read_u16 v102, v161\n\t"
        "ds_read_b128 v[104:107], v162 offset:128\n\t"
        "ds_read_b32 v101, v163 offset:128\n\t"
        "ds_read_b128 v[96:99], v162 offset:256\n\t"
        "ds_read_b32 v100, v163 offset:256\n\t"
        "ds_read_b128 v[120:123], v160\n\t"
        "ds_read_u16 v124, v161 offset:128\n\t"
        "s_waitcnt lgkmcnt(6)\n\t"
        "v_mad_u32_u24 v102, v102, s95, v168\n\t"
        "ds_read_b128 v[184:187], v102\n\t"
        "ds_read_b128 v[188:191], v102 offset:16\n\t"
        "ds_read_b128 v[112:115], v174 offset:512\n\t"
        "ds_read_b128 v[116:119], v174 offset:2560\n\t"
        "ds_read_b128 v[108:111], v174 offset:8704\n\t"
        "ds_read_b128 v[196:199], v174 offset:16896\n\t"
        "ds_read_b128 v[200:203], v174 offset:1536\n\t"
        "ds_read_b128 v[204:207], v174 offset:3584\n\t"
        "s_waitcnt lgkmcnt(12)\n\t"
        "v_add_u32 v101, v101, v166\n\t"
        "v_mad_u32_u16 v104, v104, s94, v164\n\t"
        "v_mad_u32_u16 v105, v105, s94, v165\n\t"
        "v_mad_u32_u16 v106, v106, s94, v164\n\t"
        "v_mad_u32_u16 v107, v107, s94, v165\n\t"
        "s_waitcnt lgkmcnt(7)\n\t"
        "v_mfma_f32_32x32x16_f16 v[64:79], v[184:187], v[128:131], 0\n\t"
        "ds_read_b128 v[208:211], v174 offset:9728\n\t"
        "ds_read_b128 v[212:215], v174 offset:10752\n\t"
        "ds_read_b128 v[216:219], v174 offset:11776\n\t"
        "ds_read_b128 v[220:223], v174 offset:17920\n\t"
        "ds_read_b128 v[224:227], v174 offset:4608\n\t"
        "ds_read_b128 v[228:231], v174 offset:5632\n\t"
        "s_waitcnt lgkmcnt(12)\n\t"
        "v_mfma_f32_32x32x16_f16 v[64:79], v[188:191], v[136:139], v[64:79]\n\t"
        "ds_read_b128 v[232:235], v174 offset:6656\n\t"
        "ds_read_b128 v[236:239], v174 offset:7680\n\t"
        "ds_read_b128 v[240:243], v174 offset:12800\n\t"
        "ds_read_b128 v[244:247], v174 offset:13824\n\t"
        "ds_read_b128 v[248:251], v174 offset:14848\n\t"
        "ds_read_b128 v[252:255], v174 offset:15872\n\t"
        "s_add_i32 m0, %[ring], 45056\n\t"
        "s_waitcnt vmcnt(5)\n\t"
        "s_cmp_eq_u32 %[flag], 0\n\t"
        "s_cbranch_scc1 2f\n\t"
        "s_cmp_eq_u32 %[flag], 2\n\t"
        "s_cbranch_scc1 3f\n\t"
        "s_waitcnt vmcnt(0)\n\t"
        "3:\n\t"
        "s_barrier\n\t"
        "2:\n\t"
        "ds_read_b128 v[144:147], v169\n\t"
        "ds_read_b128 v[148:151], v170\n\t"
        "ds_read_b128 v[152:155], v171\n\t"
        "ds_read_b128 v[156:159], v172\n\t"
        "ds_read_b128 v[192:195], v173\n\t"
        "s_cbranch_scc1 4f\n\t"
        "global_load_lds_dwordx4 v101, %[es]\n\t"
        "s_add_i32 m0, %[ring], 40960\n\t"
        "s_nop 0\n\t"
        "global_load_lds_dwordx4 v104, %[x]\n\t"
        "s_add_i32 m0, %[ring], 41984\n\t"
        "s_nop 0\n\t"
        "global_load_lds_dwordx4 v105, %[x]\n\t"
        "s_add_i32 m0, %[ring], 43008\n\t"
        "s_nop 0\n\t"
        "global_load_lds_dwordx4 v106, %[x]\n\t"
        "s_add_i32 m0, %[ring], 44032\n\t"
        "s_nop 0\n\t"
        "global_load_lds_dwordx4 v107, %[x]\n\t"
        "4:\n\t"
        "s_waitcnt lgkmcnt(4)\n\t"
        "v_mfma_f32_32x32x16_bf16 v[64:79], v[144:147], v[112:115], v[64:79]\n\t"
        "s_waitcnt lgkmcnt(3)\n\t"
        "v_mfma_f32_32x32x16_bf16 v[64:79], v[148:151], v[200:203], v[64:79]\n\t"
        "s_waitcnt lgkmcnt(2)\n\t"
        "v_mfma_f32_32x32x16_bf16 v[64:79], v[152:155], v[116:119], v[64:79]\n\t"
        "s_waitcnt lgkmcnt(1)\n\t"
        "v_mfma_f32_32x32x16_bf16 v[64:79], v[156:159], v[204:207], v[64:79]\n\t"
        "s_waitcnt lgkmcnt(0)\n\t"
        "v_mfma_f32_32x32x16_bf16 v[64:79], v[192:195], v[196:199], v[64:79]\n\t"
        "s_add_i32 m0, %[ring], 4096\n\t"
        "v_add_u32 v100, v100, v166\n\t"
        "global_load_lds_dwordx4 v100, %[es]\n\t"
        "v_mfma_f32_32x32x16_bf16 v[80:95], v[144:147], v[108:111], 0\n\t"
        "s_mov_b32 m0, %[ring]\n\t"
        "v_mad_u32_u16 v96, v96, s94, v164\n\t"
        "global_load_lds_dwordx4 v96, %[x]\n\t"
        "s_add_i32 m0, %[ring], 1024\n\t"
        "v_mad_u32_u16 v97, v97, s94, v165\n\t"
        "global_load_lds_dwordx4 v97, %[x]\n\t"
        "v_mfma_f32_32x32x16_bf16 v[80:95], v[148:151], v[208:211], v[80:95]\n\t"
        "s_add_i32 m0, %[ring], 2048\n\t"
        "v_mad_u32_u16 v98, v98, s94, v164\n\t"
        "global_load_lds_dwordx4 v98, %[x]\n\t"
        "v_exp_f32 v64, v64\n\t"
        "v_exp_f32 v65, v65\n\t"
        "v_mfma_f32_32x32x16_bf16 v[80:95], v[152:155], v[212:215], v[80:95]\n\t"
        "s_add_i32 m0, %[ring], 3072\n\t"
        "v_mad_u32_u16 v99, v99, s94, v165\n\t"
        "global_load_lds_dwordx4 v99, %[x]\n\t"
        "v_exp_f32 v66, v66\n\t"
        "v_exp_f32 v67, v67\n\t"
        "v_mfma_f32_32x32x16_bf16 v[80:95], v[156:159], v[216:219], v[80:95]\n\t"
        "v_cvt_pk_f16_f32 v64, v64, v65\n\t"
        "v_exp_f32 v68, v68\n\t"
        "v_cvt_pk_f16_f32 v65, v66, v67\n\t"
        "v_pk_add_f16 v64, v64, s96\n\t"
        "v_exp_f32 v69, v69\n\t"
        "v_pk_add_f16 v65, v65, s96\n\t"
        "v_rcp_f16_sdwa v64, v64 dst_sel:WORD_0 dst_unused:UNUSED_PRESERVE src0_sel:WORD_0\n\t"
        "v_exp_f32 v70, v70\n\t"
        "v_cvt_pk_f16_f32 v66, v68, v69\n\t"
        "v_rcp_f16_sdwa v65, v65 dst_sel:WORD_0 dst_unused:UNUSED_PRESERVE src0_sel:WORD_0\n\t"
        "v_pk_add_f16 v66, v66, s96\n\t"
        "v_rcp_f16_sdwa v64, v64 dst_sel:WORD_1 dst_unused:UNUSED_PRESERVE src0_sel:WORD_1\n\t"
        "v_cvt_pk_f16_f32 v80, v80, v81\n\t"
        "v_rcp_f16_sdwa v65, v65 dst_sel:WORD_1 dst_unused:UNUSED_PRESERVE src0_sel:WORD_1\n\t"
        "v_pk_mul_f16 v80, v80, v64\n\t"
        "s_branch .Lr4_%=\n\t"
        ".Lrt_%=:\n\t"
        "v_mfma_f32_32x32x16_f16 v[16:31], v[96:99], v[176:179], v[16:31]\n\t"
        "s_cmp_lt_u32 %[wave], 4\n\t"
        "s_cbranch_scc1 1f\n\t"
        "s_setprio 1\n\t"
        "1:\n\t"
        "ds_read_b128 v[120:123], v160\n\t"
        "ds_read_u16 v124, v161 offset:128\n\t"
        "v_rcp_f16_sdwa v103, v103 dst_sel:WORD_1 dst_unused:UNUSED_PRESERVE src0_sel:WORD_1\n\t"
        "v_cvt_pk_f16_f32 v119, v126, v127\n\t"
        "v_pk_add_f16 v64, v64, s96\n\t"
        "v_exp_f32 v67, v67\n\t"
        "v_rcp_f16_sdwa v64, v64 dst_sel:WORD_0 dst_unused:UNUSED_PRESERVE src0_sel:WORD_0\n\t"
        "v_mfma_f32_32x32x16_f16 v[48:63], v[112:115], v[176:179], v[48:63]\n\t"
        "v_pk_mul_f16 v119, v119, v103\n\t"
        "v_cvt_pk_f16_f32 v65, v66, v67\n\t"
        "v_rcp_f16_sdwa v64, v64 dst_sel:WORD_1 dst_unused:UNUSED_PRESERVE src0_sel:WORD_1\n\t"
        "v_pk_add_f16 v65, v65, s96\n\t"
        "v_exp_f32 v68, v68\n\t"
        "v_mfma_f32_32x32x16_f16 v[16:31], v[100:103], v[180:183], v[16:31]\n\t"
        "v_cvt_pk_f16_f32 v80, v80, v81\n\t"
        "v_rcp_f16_sdwa v65, v65 dst_sel:WORD_0 dst_unused:UNUSED_PRESERVE src0_sel:WORD_0\n\t"
        "v_pk_mul_f16 v80, v80, v64\n\t"
        "v_exp_f32 v69, v69\n\t"
        "v_mfma_f32_32x32x16_f16 v[48:63], v[116:119], v[180:183], v[48:63]\n\t"
        "v_cvt_pk_f16_f32 v66, v68, v69\n\t"
        "v_rcp_f16_sdwa v65, v65 dst_sel:WORD_1 dst_unused:UNUSED_PRESERVE src0_sel:WORD_1\n\t"
        "v_pk_add_f16 v66, v66, s96\n\t"
        "v_exp_f32 v70, v70\n\t"
        ".Lr4_%=:\n\t"
        "v_mfma_f32_32x32x16_f16 v[96:111], v[184:187], v[132:135], 0\n\t"
        "s_waitcnt lgkmcnt(1)\n\t"
        "v_mad_u32_u16 v120, v120, s95, v167 op_sel:[1,0,0,0]\n\t"
        "ds_read_b64_tr_b16 v[176:177], v120\n\t"
        "v_mad_u32_u16 v121, v121, s95, v167 op_sel:[1,0,0,0]\n\t"
        "ds_read_b64_tr_b16 v[178:179], v121\n\t"
        "v_mad_u32_u16 v122, v122, s95, v167 op_sel:[1,0,0,0]\n\t"
        "ds_read_b64_tr_b16 v[180:181], v122\n\t"
        "v_mad_u32_u16 v123, v123, s95, v167 op_sel:[1,0,0,0]\n\t"
        "ds_read_b64_tr_b16 v[182:183], v123\n\t"
        "v_cvt_pk_f16_f32 v81, v82, v83\n\t"
        "v_rcp_f16_sdwa v66, v66 dst_sel:WORD_0 dst_unused:UNUSED_PRESERVE src0_sel:WORD_0\n\t"
        "v_pk_mul_f16 v81, v81, v65\n\t"
        "v_exp_f32 v71, v71\n\t"
        "v_mfma_f32_32x32x16_f16 v[96:111], v[188:191], v[140:143], v[96:111]\n\t"
        "s_waitcnt lgkmcnt(4)\n\t"
        "v_mad_u32_u24 v124, v124, s95, v168\n\t"
        "ds_read_b128 v[184:187], v124\n\t"
        "ds_read_b128 v[188:191], v124 offset:16\n\t"
        "v_cvt_pk_f16_f32 v67, v70, v71\n\t"
        "v_rcp_f16_sdwa v66, v66 dst_sel:WORD_1 dst_unused:UNUSED_PRESERVE src0_sel:WORD_1\n\t"
        "v_pk_add_f16 v67, v67, s96\n\t"
        "v_exp_f32 v72, v72\n\t"
        "v_mfma_f32_32x32x16_bf16 v[96:111], v[192:195], v[220:223], v[96:111]\n\t"
        "s_waitcnt vmcnt(9)\n\t"
        "ds_read_b128 v[192:195], v173 offset:40960\n\t"
        "ds_read_b32 v125, v163 offset:384\n\t"
        "v_cvt_pk_f16_f32 v82, v84, v85\n\t"
        "v_rcp_f16_sdwa v67, v67 dst_sel:WORD_0 dst_unused:UNUSED_PRESERVE src0_sel:WORD_0\n\t"
        "v_pk_mul_f16 v82, v82, v66\n\t"
        "v_exp_f32 v73, v73\n\t"
        "v_mfma_f32_32x32x16_bf16 v[96:111], v[144:147], v[224:227], v[96:111]\n\t"
        "v_cvt_pk_f16_f32 v68, v72, v73\n\t"
        "v_rcp_f16_sdwa v67, v67 dst_sel:WORD_1 dst_unused:UNUSED_PRESERVE src0_sel:WORD_1\n\t"
        "v_pk_add_f16 v68, v68, s96\n\t"
        "v_exp_f32 v74, v74\n\t"
        "v_mfma_f32_32x32x16_bf16 v[96:111], v[148:151], v[228:231], v[96:111]\n\t"
        "v_cvt_pk_f16_f32 v83, v86, v87\n\t"
        "v_rcp_f16_sdwa v68, v68 dst_sel:WORD_0 dst_unused:UNUSED_PRESERVE src0_sel:WORD_0\n\t"
        "v_pk_mul_f16 v83, v83, v67\n\t"
        "v_exp_f32 v75, v75\n\t"
        "v_mfma_f32_32x32x16_bf16 v[96:111], v[152:155], v[232:235], v[96:111]\n\t"
        "s_add_i32 m0, %[ring], 45056\n\t"
        "s_waitcnt lgkmcnt(0)\n\t"
        "v_add_u32 v125, v125, v166\n\t"
        "global_load_lds_dwordx4 v125, %[es]\n\t"
        "v_cvt_pk_f16_f32 v69, v74, v75\n\t"
        "v_rcp_f16_sdwa v68, v68 dst_sel:WORD_1 dst_unused:UNUSED_PRESERVE src0_sel:WORD_1\n\t"
        "v_pk_add_f16 v69, v69, s96\n\t"
        "v_exp_f32 v76, v76\n\t"
        "v_mfma_f32_32x32x16_bf16 v[96:111], v[156:159], v[236:239], v[96:111]\n\t"
        "v_cvt_pk_f16_f32 v84, v88, v89\n\t"
        "v_rcp_f16_sdwa v69, v69 dst_sel:WORD_0 dst_unused:UNUSED_PRESERVE src0_sel:WORD_0\n\t"
        "v_pk_mul_f16 v84, v84, v68\n\t"
        "v_exp_f32 v77, v77\n\t"
        "v_mfma_f32_32x32x16_bf16 v[112:127], v[144:147], v[240:243], 0\n\t"
        "s_waitcnt vmcnt(6)\n\t"
        "ds_read_b128 v[144:147], v169 offset:40960\n\t"
        "v_cvt_pk_f16_f32 v70, v76, v77\n\t"
        "v_rcp_f16_sdwa v69, v69 dst_sel:WORD_1 dst_unused:UNUSED_PRESERVE src0_sel:WORD_1\n\t"
        "v_pk_add_f16 v70, v70, s96\n\t"
        "v_exp_f32 v78, v78\n\t"
        "v_mfma_f32_32x32x16_bf16 v[112:127], v[148:151], v[244:247], v[112:127]\n\t"
        "ds_read_b128 v[148:151], v170 offset:40960\n\t"
        "s_sub_u32 %[nt], %[nt], 1\n\t"
        "v_cvt_pk_f16_f32 v85, v90, v91\n\t"
        "v_rcp_f16_sdwa v70, v70 dst_sel:WORD_0 dst_unused:UNUSED_PRESERVE src0_sel:WORD_0\n\t"
        "v_pk_mul_f16 v85, v85, v69\n\t"
        "v_exp_f32 v79, v79\n\t"
        "v_mfma_f32_32x32x16_bf16 v[112:127], v[152:155], v[248:251], v[112:127]\n\t"
        "ds_read_b128 v[152:155], v171 offset:40960\n\t"
        "ds_read_b128 v[88:91], v162 offset:384\n\t"
        "v_cvt_pk_f16_f32 v71, v78, v79\n\t"
        "v_rcp_f16_sdwa v70, v70 dst_sel:WORD_1 dst_unused:UNUSED_PRESERVE src0_sel:WORD_1\n\t"
        "v_pk_add_f16 v71, v71, s96\n\t"
        "v_exp_f32 v96, v96\n\t"
        "v_exp_f32 v97, v97\n\t"
        "v_mfma_f32_32x32x16_bf16 v[112:127], v[156:159], v[252:255], v[112:127]\n\t"
        "ds_read_b128 v[156:159], v172 offset:40960\n\t"
        "v_cvt_pk_f16_f32 v86, v92, v93\n\t"
        "v_rcp_f16_sdwa v71, v71 dst_sel:WORD_0 dst_unused:UNUSED_PRESERVE src0_sel:WORD_0\n\t"
        "v_pk_mul_f16 v86, v86, v70\n\t"
        "v_exp_f32 v98, v98\n\t"
        "v_cvt_pk_f16_f32 v96, v96, v97\n\t"
        "s_cbranch_scc1 .Lrl_%=\n\t"
        "v_mfma_f32_32x32x16_f16 v[0:15], v[64:67], v[176:179], v[0:15]\n\t"
        "v_rcp_f16_sdwa v71, v71 dst_sel:WORD_1 dst_unused:UNUSED_PRESERVE src0_sel:WORD_1\n\t"
        "v_cvt_pk_f16_f32 v87, v94, v95\n\t"
        "v_pk_add_f16 v96, v96, s96\n\t"
        "v_exp_f32 v99, v99\n\t"
        "v_rcp_f16_sdwa v96, v96 dst_sel:WORD_0 dst_unused:UNUSED_PRESERVE src0_sel:WORD_0\n\t"
        "v_mfma_f32_32x32x16_f16 v[32:47], v[80:83], v[176:179], v[32:47]\n\t"
        "s_add_i32 m0, %[ring], 40960\n\t"
        "s_waitcnt lgkmcnt(1)\n\t"
        "v_mad_u32_u16 v88, v88, s94, v164\n\t"
        "s_waitcnt lgkmcnt(0)\n\t"
        "global_load_lds_dwordx4 v88, %[x]\n\t"
        "v_pk_mul_f16 v87, v87, v71\n\t"
        "v_cvt_pk_f16_f32 v97, v98, v99\n\t"
        "v_rcp_f16_sdwa v96, v96 dst_sel:WORD_1 dst_unused:UNUSED_PRESERVE src0_sel:WORD_1\n\t"
        "v_pk_add_f16 v97, v97, s96\n\t"
        "v_exp_f32 v100, v100\n\t"
        "v_mfma_f32_32x32x16_f16 v[0:15], v[68:71], v[180:183], v[0:15]\n\t"
        "ds_read_b128 v[92:95], v174 offset:512\n\t"
        "s_add_i32 m0, %[ring], 41984\n\t"
        "v_mad_u32_u16 v89, v89, s94, v165\n\t"
        "global_load_lds_dwordx4 v89, %[x]\n\t"
        "v_cvt_pk_f16_f32 v112, v112, v113\n\t"
        "v_rcp_f16_sdwa v97, v97 dst_sel:WORD_0 dst_unused:UNUSED_PRESERVE src0_sel:WORD_0\n\t"
        "v_pk_mul_f16 v112, v112, v96\n\t"
        "v_exp_f32 v101, v101\n\t"
        "v_mfma_f32_32x32x16_f16 v[32:47], v[84:87], v[180:183], v[32:47]\n\t"
        "s_add_i32 m0, %[ring], 43008\n\t"
        "v_mad_u32_u16 v90, v90, s94, v164\n\t"
        "global_load_lds_dwordx4 v90, %[x]\n\t"
        "v_cvt_pk_f16_f32 v98, v100, v101\n\t"
        "v_rcp_f16_sdwa v97, v97 dst_sel:WORD_1 dst_unused:UNUSED_PRESERVE src0_sel:WORD_1\n\t"
        "v_pk_add_f16 v98, v98, s96\n\t"
        "v_exp_f32 v102, v102\n\t"
        "v_mfma_f32_32x32x16_f16 v[64:79], v[184:187], v[128:131], 0\n\t"
        "s_add_i32 m0, %[ring], 44032\n\t"
        "v_mad_u32_u16 v91, v91, s94, v165\n\t"
        "global_load_lds_dwordx4 v91, %[x]\n\t"
        "v_cvt_pk_f16_f32 v113, v114, v115\n\t"
        "v_rcp_f16_sdwa v98, v98 dst_sel:WORD_0 dst_unused:UNUSED_PRESERVE src0_sel:WORD_0\n\t"
        "v_pk_mul_f16 v113, v113, v97\n\t"
        "v_exp_f32 v103, v103\n\t"
        "v_mfma_f32_32x32x16_f16 v[64:79], v[188:191], v[136:139], v[64:79]\n\t"
        "ds_read_b128 v[80:83], v174 offset:2560\n\t"
        "v_cvt_pk_f16_f32 v99, v102, v103\n\t"
        "v_rcp_f16_sdwa v98, v98 dst_sel:WORD_1 dst_unused:UNUSED_PRESERVE src0_sel:WORD_1\n\t"
        "v_pk_add_f16 v99, v99, s96\n\t"
        "v_exp_f32 v104, v104\n\t"
        "v_mfma_f32_32x32x16_bf16 v[64:79], v[192:195], v[196:199], v[64:79]\n\t"
        "v_cvt_pk_f16_f32 v114, v116, v117\n\t"
        "v_rcp_f16_sdwa v99, v99 dst_sel:WORD_0 dst_unused:UNUSED_PRESERVE src0_sel:WORD_0\n\t"
        "v_pk_mul_f16 v114, v114, v98\n\t"
        "v_exp_f32 v105, v105\n\t"
        "s_waitcnt lgkmcnt(1)\n\t"
        "v_mfma_f32_32x32x16_bf16 v[64:79], v[144:147], v[92:95], v[64:79]\n\t"
        "ds_read_b128 v[84:87], v174 offset:8704\n\t"
        "v_cvt_pk_f16_f32 v100, v104, v105\n\t"
        "v_rcp_f16_sdwa v99, v99 dst_sel:WORD_1 dst_unused:UNUSED_PRESERVE src0_sel:WORD_1\n\t"
        "v_pk_add_f16 v100, v100, s96\n\t"
        "v_exp_f32 v106, v106\n\t"
        "v_mfma_f32_32x32x16_bf16 v[64:79], v[148:151], v[200:203], v[64:79]\n\t"
        "v_cvt_pk_f16_f32 v115, v118, v119\n\t"
        "v_rcp_f16_sdwa v100, v100 dst_sel:WORD_0 dst_unused:UNUSED_PRESERVE src0_sel:WORD_0\n\t"
        "v_pk_mul_f16 v115, v115, v99\n\t"
        "v_exp_f32 v107, v107\n\t"
        "s_waitcnt lgkmcnt(1)\n\t"
        "v_mfma_f32_32x32x16_bf16 v[64:79], v[152:155], v[80:83], v[64:79]\n\t"
        "v_cvt_pk_f16_f32 v101, v106, v107\n\t"
        "v_rcp_f16_sdwa v100, v100 dst_sel:WORD_1 dst_unused:UNUSED_PRESERVE src0_sel:WORD_1\n\t"
        "v_pk_add_f16 v101, v101, s96\n\t"
        "v_exp_f32 v108, v108\n\t"
        "v_mfma_f32_32x32x16_bf16 v[64:79], v[156:159], v[204:207], v[64:79]\n\t"
        "v_cvt_pk_f16_f32 v116, v120, v121\n\t"
        "v_rcp_f16_sdwa v101, v101 dst_sel:WORD_0 dst_unused:UNUSED_PRESERVE src0_sel:WORD_0\n\t"
        "v_pk_mul_f16 v116, v116, v100\n\t"
        "v_exp_f32 v109, v109\n\t"
        "s_waitcnt lgkmcnt(0)\n\t"
        "v_mfma_f32_32x32x16_bf16 v[80:95], v[144:147], v[84:87], 0\n\t"
        "v_cvt_pk_f16_f32 v102, v108, v109\n\t"
        "v_rcp_f16_sdwa v101, v101 dst_sel:WORD_1 dst_unused:UNUSED_PRESERVE src0_sel:WORD_1\n\t"
        "v_pk_add_f16 v102, v102, s96\n\t"
        "v_exp_f32 v110, v110\n\t"
        "v_mfma_f32_32x32x16_bf16 v[80:95], v[148:151], v[208:211], v[80:95]\n\t"
        "v_cvt_pk_f16_f32 v117, v122, v123\n\t"
        "v_rcp_f16_sdwa v102, v102 dst_sel:WORD_0 dst_unused:UNUSED_PRESERVE src0_sel:WORD_0\n\t"
        "v_pk_mul_f16 v117, v117, v101\n\t"
        "v_exp_f32 v111, v111\n\t"
        "v_mfma_f32_32x32x16_bf16 v[80:95], v[152:155], v[212:215], v[80:95]\n\t"
        "v_cvt_pk_f16_f32 v103, v110, v111\n\t"
        "v_rcp_f16_sdwa v102, v102 dst_sel:WORD_1 dst_unused:UNUSED_PRESERVE src0_sel:WORD_1\n\t"
        "v_pk_add_f16 v103, v103, s96\n\t"
        "v_exp_f32 v64, v64\n\t"
        "v_exp_f32 v65, v65\n\t"
        "v_mfma_f32_32x32x16_bf16 v[80:95], v[156:159], v[216:219], v[80:95]\n\t"
        "v_cvt_pk_f16_f32 v118, v124, v125\n\t"
        "v_rcp_f16_sdwa v103, v103 dst_sel:WORD_0 dst_unused:UNUSED_PRESERVE src0_sel:WORD_0\n\t"
        "v_pk_mul_f16 v118, v118, v102\n\t"
        "v_exp_f32 v66, v66\n\t"
        "v_cvt_pk_f16_f32 v64, v64, v65\n\t"
        "v_mfma_f32_32x32x16_f16 v[16:31], v[96:99], v[176:179], v[16:31]\n\t"
        "s_setprio 0\n\t"
        "ds_read_b128 v[120:123], v160 offset:128\n\t"
        "ds_read_u16 v124, v161 offset:256\n\t"
        "v_rcp_f16_sdwa v103, v103 dst_sel:WORD_1 dst_unused:UNUSED_PRESERVE src0_sel:WORD_1\n\t"
        "v_cvt_pk_f16_f32 v119, v126, v127\n\t"
        "v_pk_add_f16 v64, v64, s96\n\t"
        "v_exp_f32 v67, v67\n\t"
        "v_rcp_f16_sdwa v64, v64 dst_sel:WORD_0 dst_unused:UNUSED_PRESERVE src0_sel:WORD_0\n\t"
        "v_mfma_f32_32x32x16_f16 v[48:63], v[112:115], v[176:179], v[48:63]\n\t"
        "v_pk_mul_f16 v119, v119, v103\n\t"
        "v_cvt_pk_f16_f32 v65, v66, v67\n\t"
        "v_rcp_f16_sdwa v64, v64 dst_sel:WORD_1 dst_unused:UNUSED_PRESERVE src0_sel:WORD_1\n\t"
        "v_pk_add_f16 v65, v65, s96\n\t"
        "v_exp_f32 v68, v68\n\t"
        "v_mfma_f32_32x32x16_f16 v[16:31], v[100:103], v[180:183], v[16:31]\n\t"
        "v_cvt_pk_f16_f32 v80, v80, v81\n\t"
        "v_rcp_f16_sdwa v65, v65 dst_sel:WORD_0 dst_unused:UNUSED_PRESERVE src0_sel:WORD_0\n\t"
        "v_pk_mul_f16 v80, v80, v64\n\t"
        "v_exp_f32 v69, v69\n\t"
        "v_mfma_f32_32x32x16_f16 v[48:63], v[116:119], v[180:183], v[48:63]\n\t"
        "v_cvt_pk_f16_f32 v66, v68, v69\n\t"
        "v_rcp_f16_sdwa v65, v65 dst_sel:WORD_1 dst_unused:UNUSED_PRESERVE src0_sel:WORD_1\n\t"
        "v_pk_add_f16 v66, v66, s96\n\t"
        "v_exp_f32 v70, v70\n\t"
        "v_mfma_f32_32x32x16_f16 v[96:111], v[184:187], v[132:135], 0\n\t"
        "s_waitcnt lgkmcnt(1)\n\t"
        "v_mad_u32_u16 v120, v120, s95, v167 op_sel:[1,0,0,0]\n\t"
        "ds_read_b64_tr_b16 v[176:177], v120\n\t"
        "v_mad_u32_u16 v121, v121, s95, v167 op_sel:[1,0,0,0]\n\t"
        "ds_read_b64_tr_b16 v[178:179], v121\n\t"
        "v_mad_u32_u16 v122, v122, s95, v167 op_sel:[1,0,0,0]\n\t"
        "ds_read_b64_tr_b16 v[180:181], v122\n\t"
        "v_mad_u32_u16 v123, v123, s95, v167 op_sel:[1,0,0,0]\n\t"
        "ds_read_b64_tr_b16 v[182:183], v123\n\t"
        "v_cvt_pk_f16_f32 v81, v82, v83\n\t"
        "v_rcp_f16_sdwa v66, v66 dst_sel:WORD_0 dst_unused:UNUSED_PRESERVE src0_sel:WORD_0\n\t"
        "v_pk_mul_f16 v81, v81, v65\n\t"
        "v_exp_f32 v71, v71\n\t"
        "v_mfma_f32_32x32x16_f16 v[96:111], v[188:191], v[140:143], v[96:111]\n\t"
        "s_waitcnt lgkmcnt(4)\n\t"
        "v_mad_u32_u24 v124, v124, s95, v168\n\t"
        "ds_read_b128 v[184:187], v124\n\t"
        "ds_read_b128 v[188:191], v124 offset:16\n\t"
        "v_cvt_pk_f16_f32 v67, v70, v71\n\t"
        "v_rcp_f16_sdwa v66, v66 dst_sel:WORD_1 dst_unused:UNUSED_PRESERVE src0_sel:WORD_1\n\t"
        "v_pk_add_f16 v67, v67, s96\n\t"
        "v_exp_f32 v72, v72\n\t"
        "v_mfma_f32_32x32x16_bf16 v[96:111], v[192:195], v[220:223], v[96:111]\n\t"
        "s_waitcnt vmcnt(9)\n\t"
        "ds_read_b128 v[192:195], v173\n\t"
        "ds_read_b32 v125, v163 offset:512\n\t"
        "v_cvt_pk_f16_f32 v82, v84, v85\n\t"
        "v_rcp_f16_sdwa v67, v67 dst_sel:WORD_0 dst_unused:UNUSED_PRESERVE src0_sel:WORD_0\n\t"
        "v_pk_mul_f16 v82, v82, v66\n\t"
        "v_exp_f32 v73, v73\n\t"
        "v_mfma_f32_32x32x16_bf16 v[96:111], v[144:147], v[224:227], v[96:111]\n\t"
        "v_cvt_pk_f16_f32 v68, v72, v73\n\t"
        "v_rcp_f16_sdwa v67, v67 dst_sel:WORD_1 dst_unused:UNUSED_PRESERVE src0_sel:WORD_1\n\t"
        "v_pk_add_f16 v68, v68, s96\n\t"
        "v_exp_f32 v74, v74\n\t"
        "v_mfma_f32_32x32x16_bf16 v[96:111], v[148:151], v[228:231], v[96:111]\n\t"
        "v_cvt_pk_f16_f32 v83, v86, v87\n\t"
        "v_rcp_f16_sdwa v68, v68 dst_sel:WORD_0 dst_unused:UNUSED_PRESERVE src0_sel:WORD_0\n\t"
        "v_pk_mul_f16 v83, v83, v67\n\t"
        "v_exp_f32 v75, v75\n\t"
        "v_mfma_f32_32x32x16_bf16 v[96:111], v[152:155], v[232:235], v[96:111]\n\t"
        "s_add_i32 m0, %[ring], 4096\n\t"
        "s_waitcnt lgkmcnt(0)\n\t"
        "v_add_u32 v125, v125, v166\n\t"
        "global_load_lds_dwordx4 v125, %[es]\n\t"
        "v_cvt_pk_f16_f32 v69, v74, v75\n\t"
        "v_rcp_f16_sdwa v68, v68 dst_sel:WORD_1 dst_unused:UNUSED_PRESERVE src0_sel:WORD_1\n\t"
        "v_pk_add_f16 v69, v69, s96\n\t"
        "v_exp_f32 v76, v76\n\t"
        "v_mfma_f32_32x32x16_bf16 v[96:111], v[156:159], v[236:239], v[96:111]\n\t"
        "v_cvt_pk_f16_f32 v84, v88, v89\n\t"
        "v_rcp_f16_sdwa v69, v69 dst_sel:WORD_0 dst_unused:UNUSED_PRESERVE src0_sel:WORD_0\n\t"
        "v_pk_mul_f16 v84, v84, v68\n\t"
        "v_exp_f32 v77, v77\n\t"
        "v_mfma_f32_32x32x16_bf16 v[112:127], v[144:147], v[240:243], 0\n\t"
        "s_waitcnt vmcnt(6)\n\t"
        "ds_read_b128 v[144:147], v169\n\t"
        "v_cvt_pk_f16_f32 v70, v76, v77\n\t"
        "v_rcp_f16_sdwa v69, v69 dst_sel:WORD_1 dst_unused:UNUSED_PRESERVE src0_sel:WORD_1\n\t"
        "v_pk_add_f16 v70, v70, s96\n\t"
        "v_exp_f32 v78, v78\n\t"
        "v_mfma_f32_32x32x16_bf16 v[112:127], v[148:151], v[244:247], v[112:127]\n\t"
        "ds_read_b128 v[148:151], v170\n\t"
        "s_sub_u32 %[nt], %[nt], 1\n\t"
        "v_cvt_pk_f16_f32 v85, v90, v91\n\t"
        "v_rcp_f16_sdwa v70, v70 dst_sel:WORD_0 dst_unused:UNUSED_PRESERVE src0_sel:WORD_0\n\t"
        "v_pk_mul_f16 v85, v85, v69\n\t"
        "v_exp_f32 v79, v79\n\t"
        "v_mfma_f32_32x32x16_bf16 v[112:127], v[152:155], v[248:251], v[112:127]\n\t"
        "ds_read_b128 v[152:155], v171\n\t"
        "ds_read_b128 v[88:91], v162 offset:512\n\t"
        "v_cvt_pk_f16_f32 v71, v78, v79\n\t"
        "v_rcp_f16_sdwa v70, v70 dst_sel:WORD_1 dst_unused:UNUSED_PRESERVE src0_sel:WORD_1\n\t"
        "v_pk_add_f16 v71, v71, s96\n\t"
        "v_exp_f32 v96, v96\n\t"
        "v_exp_f32 v97, v97\n\t"
        "v_mfma_f32_32x32x16_bf16 v[112:127], v[156:159], v[252:255], v[112:127]\n\t"
        "ds_read_b128 v[156:159], v172\n\t"
        "v_cvt_pk_f16_f32 v86, v92, v93\n\t"
        "v_rcp_f16_sdwa v71, v71 dst_sel:WORD_0 dst_unused:UNUSED_PRESERVE src0_sel:WORD_0\n\t"
        "v_pk_mul_f16 v86, v86, v70\n\t"
        "v_exp_f32 v98, v98\n\t"
        "v_cvt_pk_f16_f32 v96, v96, v97\n\t"
        "s_cbranch_scc1 .Lrl_%=\n\t"
        "v_mfma_f32_32x32x16_f16 v[0:15], v[64:67], v[176:179], v[0:15]\n\t"
        "v_rcp_f16_sdwa v71, v71 dst_sel:WORD_1 dst_unused:UNUSED_PRESERVE src0_sel:WORD_1\n\t"
        "v_cvt_pk_f16_f32 v87, v94, v95\n\t"
        "v_pk_add_f16 v96, v96, s96\n\t"
        "v_exp_f32 v99, v99\n\t"
        "v_rcp_f16_sdwa v96, v96 dst_sel:WORD_0 dst_unused:UNUSED_PRESERVE src0_sel:WORD_0\n\t"
        "v_mfma_f32_32x32x16_f16 v[32:47], v[80:83], v[176:179], v[32:47]\n\t"
        "s_mov_b32 m0, %[ring]\n\t"
        "s_waitcnt lgkmcnt(1)\n\t"
        "v_mad_u32_u16 v88, v88, s94, v164\n\t"
        "s_waitcnt lgkmcnt(0)\n\t"
        "global_load_lds_dwordx4 v88, %[x]\n\t"
        "v_pk_mul_f16 v87, v87, v71\n\t"
        "v_cvt_pk_f16_f32 v97, v98, v99\n\t"
        "v_rcp_f16_sdwa v96, v96 dst_sel:WORD_1 dst_unused:UNUSED_PRESERVE src0_sel:WORD_1\n\t"
        "v_pk_add_f16 v97, v97, s96\n\t"
        "v_exp_f32 v100, v100\n\t"
        "v_mfma_f32_32x32x16_f16 v[0:15], v[68:71], v[180:183], v[0:15]\n\t"
        "ds_read_b128 v[92:95], v174 offset:512\n\t"
        "s_add_i32 m0, %[ring], 1024\n\t"
        "v_mad_u32_u16 v89, v89, s94, v165\n\t"
        "global_load_lds_dwordx4 v89, %[x]\n\t"
        "v_cvt_pk_f16_f32 v112, v112, v113\n\t"
        "v_rcp_f16_sdwa v97, v97 dst_sel:WORD_0 dst_unused:UNUSED_PRESERVE src0_sel:WORD_0\n\t"
        "v_pk_mul_f16 v112, v112, v96\n\t"
        "v_exp_f32 v101, v101\n\t"
        "v_mfma_f32_32x32x16_f16 v[32:47], v[84:87], v[180:183], v[32:47]\n\t"
        "s_add_i32 m0, %[ring], 2048\n\t"
        "v_mad_u32_u16 v90, v90, s94, v164\n\t"
        "global_load_lds_dwordx4 v90, %[x]\n\t"
        "v_cvt_pk_f16_f32 v98, v100, v101\n\t"
        "v_rcp_f16_sdwa v97, v97 dst_sel:WORD_1 dst_unused:UNUSED_PRESERVE src0_sel:WORD_1\n\t"
        "v_pk_add_f16 v98, v98, s96\n\t"
        "v_exp_f32 v102, v102\n\t"
        "v_mfma_f32_32x32x16_f16 v[64:79], v[184:187], v[128:131], 0\n\t"
        "s_add_i32 m0, %[ring], 3072\n\t"
        "v_mad_u32_u16 v91, v91, s94, v165\n\t"
        "global_load_lds_dwordx4 v91, %[x]\n\t"
        "v_cvt_pk_f16_f32 v113, v114, v115\n\t"
        "v_rcp_f16_sdwa v98, v98 dst_sel:WORD_0 dst_unused:UNUSED_PRESERVE src0_sel:WORD_0\n\t"
        "v_pk_mul_f16 v113, v113, v97\n\t"
        "v_exp_f32 v103, v103\n\t"
        "v_mfma_f32_32x32x16_f16 v[64:79], v[188:191], v[136:139], v[64:79]\n\t"
        "ds_read_b128 v[80:83], v174 offset:2560\n\t"
        "v_cvt_pk_f16_f32 v99, v102, v103\n\t"
        "v_rcp_f16_sdwa v98, v98 dst_sel:WORD_1 dst_unused:UNUSED_PRESERVE src0_sel:WORD_1\n\t"
        "v_pk_add_f16 v99, v99, s96\n\t"
        "v_exp_f32 v104, v104\n\t"
        "v_mfma_f32_32x32x16_bf16 v[64:79], v[192:195], v[196:199], v[64:79]\n\t"
        "v_cvt_pk_f16_f32 v114, v116, v117\n\t"
        "v_rcp_f16_sdwa v99, v99 dst_sel:WORD_0 dst_unused:UNUSED_PRESERVE src0_sel:WORD_0\n\t"
        "v_pk_mul_f16 v114, v114, v98\n\t"
        "v_exp_f32 v105, v105\n\t"
        "s_waitcnt lgkmcnt(1)\n\t"
        "v_mfma_f32_32x32x16_bf16 v[64:79], v[144:147], v[92:95], v[64:79]\n\t"
        "ds_read_b128 v[84:87], v174 offset:8704\n\t"
        "v_cvt_pk_f16_f32 v100, v104, v105\n\t"
        "v_rcp_f16_sdwa v99, v99 dst_sel:WORD_1 dst_unused:UNUSED_PRESERVE src0_sel:WORD_1\n\t"
        "v_pk_add_f16 v100, v100, s96\n\t"
        "v_exp_f32 v106, v106\n\t"
        "v_mfma_f32_32x32x16_bf16 v[64:79], v[148:151], v[200:203], v[64:79]\n\t"
        "v_cvt_pk_f16_f32 v115, v118, v119\n\t"
        "v_rcp_f16_sdwa v100, v100 dst_sel:WORD_0 dst_unused:UNUSED_PRESERVE src0_sel:WORD_0\n\t"
        "v_pk_mul_f16 v115, v115, v99\n\t"
        "v_exp_f32 v107, v107\n\t"
        "s_waitcnt lgkmcnt(1)\n\t"
        "v_mfma_f32_32x32x16_bf16 v[64:79], v[152:155], v[80:83], v[64:79]\n\t"
        "v_cvt_pk_f16_f32 v101, v106, v107\n\t"
        "v_rcp_f16_sdwa v100, v100 dst_sel:WORD_1 dst_unused:UNUSED_PRESERVE src0_sel:WORD_1\n\t"
        "v_pk_add_f16 v101, v101, s96\n\t"
        "v_exp_f32 v108, v108\n\t"
        "v_mfma_f32_32x32x16_bf16 v[64:79], v[156:159], v[204:207], v[64:79]\n\t"
        "v_cvt_pk_f16_f32 v116, v120, v121\n\t"
        "v_rcp_f16_sdwa v101, v101 dst_sel:WORD_0 dst_unused:UNUSED_PRESERVE src0_sel:WORD_0\n\t"
        "v_pk_mul_f16 v116, v116, v100\n\t"
        "v_exp_f32 v109, v109\n\t"
        "s_waitcnt lgkmcnt(0)\n\t"
        "v_mfma_f32_32x32x16_bf16 v[80:95], v[144:147], v[84:87], 0\n\t"
        "v_add_u32 v160, 0x100, v160\n\t"
        "v_cvt_pk_f16_f32 v102, v108, v109\n\t"
        "v_rcp_f16_sdwa v101, v101 dst_sel:WORD_1 dst_unused:UNUSED_PRESERVE src0_sel:WORD_1\n\t"
        "v_pk_add_f16 v102, v102, s96\n\t"
        "v_exp_f32 v110, v110\n\t"
        "v_mfma_f32_32x32x16_bf16 v[80:95], v[148:151], v[208:211], v[80:95]\n\t"
        "v_add_u32 v161, 0x100, v161\n\t"
        "v_cvt_pk_f16_f32 v117, v122, v123\n\t"
        "v_rcp_f16_sdwa v102, v102 dst_sel:WORD_0 dst_unused:UNUSED_PRESERVE src0_sel:WORD_0\n\t"
        "v_pk_mul_f16 v117, v117, v101\n\t"
        "v_exp_f32 v111, v111\n\t"
        "v_mfma_f32_32x32x16_bf16 v[80:95], v[152:155], v[212:215], v[80:95]\n\t"
        "v_add_u32 v162, 0x100, v162\n\t"
        "v_cvt_pk_f16_f32 v103, v110, v111\n\t"
        "v_rcp_f16_sdwa v102, v102 dst_sel:WORD_1 dst_unused:UNUSED_PRESERVE src0_sel:WORD_1\n\t"
        "v_pk_add_f16 v103, v103, s96\n\t"
        "v_exp_f32 v64, v64\n\t"
        "v_exp_f32 v65, v65\n\t"
        "v_mfma_f32_32x32x16_bf16 v[80:95], v[156:159], v[216:219], v[80:95]\n\t"
        "v_add_u32 v163, 0x100, v163\n\t"
        "v_cvt_pk_f16_f32 v118, v124, v125\n\t"
        "v_rcp_f16_sdwa v103, v103 dst_sel:WORD_0 dst_unused:UNUSED_PRESERVE src0_sel:WORD_0\n\t"
        "v_pk_mul_f16 v118, v118, v102\n\t"
        "v_exp_f32 v66, v66\n\t"
        "v_cvt_pk_f16_f32 v64, v64, v65\n\t"
        "s_branch .Lrt_%=\n\t"
        ".Lrl_%=:\n\t"
        "v_mfma_f32_32x32x16_f16 v[0:15], v[64:67], v[176:179], v[0:15]\n\t"
        "ds_read_b64_tr_b16 v[184:185], v175\n\t"
        "ds_read_b64_tr_b16 v[186:187], v175 offset:512\n\t"
        "ds_read_b64_tr_b16 v[188:189], v175 offset:1024\n\t"
        "ds_read_b64_tr_b16 v[190:191], v175 offset:1536\n\t"
        "v_rcp_f16_sdwa v71, v71 dst_sel:WORD_1 dst_unused:UNUSED_PRESERVE src0_sel:WORD_1\n\t"
        "v_cvt_pk_f16_f32 v87, v94, v95\n\t"
        "v_pk_add_f16 v96, v96, s96\n\t"
        "v_exp_f32 v99, v99\n\t"
        "v_rcp_f16_sdwa v96, v96 dst_sel:WORD_0 dst_unused:UNUSED_PRESERVE src0_sel:WORD_0\n\t"
        "v_mfma_f32_32x32x16_f16 v[32:47], v[80:83], v[176:179], v[32:47]\n\t"
        "ds_read_b64_tr_b16 v[192:193], v175 offset:2048\n\t"
        "ds_read_b64_tr_b16 v[194:195], v175 offset:2560\n\t"
        "ds_read_b64_tr_b16 v[196:197], v175 offset:3072\n\t"
        "ds_read_b64_tr_b16 v[198:199], v175 offset:3584\n\t"
        "v_pk_mul_f16 v87, v87, v71\n\t"
        "v_cvt_pk_f16_f32 v97, v98, v99\n\t"
        "v_rcp_f16_sdwa v96, v96 dst_sel:WORD_1 dst_unused:UNUSED_PRESERVE src0_sel:WORD_1\n\t"
        "v_pk_add_f16 v97, v97, s96\n\t"
        "v_exp_f32 v100, v100\n\t"
        "v_mfma_f32_32x32x16_f16 v[0:15], v[68:71], v[180:183], v[0:15]\n\t"
        "ds_read_b64_tr_b16 v[200:201], v175 offset:32\n\t"
        "ds_read_b64_tr_b16 v[202:203], v175 offset:544\n\t"
        "ds_read_b64_tr_b16 v[204:205], v175 offset:1056\n\t"
        "ds_read_b64_tr_b16 v[206:207], v175 offset:1568\n\t"
        "s_waitcnt lgkmcnt(14)\n\t"
        "v_lshlrev_b32 v216, 16, v144\n\t"
        "v_and_b32 v217, 0xffff0000, v144\n\t"
        "v_cvt_pk_f16_f32 v112, v112, v113\n\t"
        "v_rcp_f16_sdwa v97, v97 dst_sel:WORD_0 dst_unused:UNUSED_PRESERVE src0_sel:WORD_0\n\t"
        "v_pk_mul_f16 v112, v112, v96\n\t"
        "v_exp_f32 v101, v101\n\t"
        "v_mfma_f32_32x32x16_f16 v[32:47], v[84:87], v[180:183], v[32:47]\n\t"
        "ds_read_b64_tr_b16 v[208:209], v175 offset:2080\n\t"
        "ds_read_b64_tr_b16 v[210:211], v175 offset:2592\n\t"
        "ds_read_b64_tr_b16 v[212:213], v175 offset:3104\n\t"
        "ds_read_b64_tr_b16 v[214:215], v175 offset:3616\n\t"
        "v_lshlrev_b32 v218, 16, v145\n\t"
        "v_and_b32 v219, 0xffff0000, v145\n\t"
        "v_cvt_pk_f16_f32 v98, v100, v101\n\t"
        "v_rcp_f16_sdwa v97, v97 dst_sel:WORD_1 dst_unused:UNUSED_PRESERVE src0_sel:WORD_1\n\t"
        "v_pk_add_f16 v98, v98, s96\n\t"
        "v_exp_f32 v102, v102\n\t"
        "v_lshlrev_b32 v220, 16, v146\n\t"
        "v_and_b32 v221, 0xffff0000, v146\n\t"
        "v_lshlrev_b32 v222, 16, v147\n\t"
        "v_and_b32 v223, 0xffff0000, v147\n\t"
        "s_waitcnt lgkmcnt(14)\n\t"
        "v_lshlrev_b32 v224, 16, v148\n\t"
        "v_and_b32 v225, 0xffff0000, v148\n\t"
        "v_cvt_pk_f16_f32 v113, v114, v115\n\t"
        "v_rcp_f16_sdwa v98, v98 dst_sel:WORD_0 dst_unused:UNUSED_PRESERVE src0_sel:WORD_0\n\t"
        "v_pk_mul_f16 v113, v113, v97\n\t"
        "v_exp_f32 v103, v103\n\t"
        "v_lshlrev_b32 v226, 16, v149\n\t"
        "v_and_b32 v227, 0xffff0000, v149\n\t"
        "v_lshlrev_b32 v228, 16, v150\n\t"
        "v_and_b32 v229, 0xffff0000, v150\n\t"
        "v_lshlrev_b32 v230, 16, v151\n\t"
        "v_and_b32 v231, 0xffff0000, v151\n\t"
        "v_cvt_pk_f16_f32 v99, v102, v103\n\t"
        "v_rcp_f16_sdwa v98, v98 dst_sel:WORD_1 dst_unused:UNUSED_PRESERVE src0_sel:WORD_1\n\t"
        "v_pk_add_f16 v99, v99, s96\n\t"
        "v_exp_f32 v104, v104\n\t"
        "s_waitcnt lgkmcnt(14)\n\t"
        "v_mfma_f32_32x32x16_bf16 v[64:79], v[184:187], v[144:147], v[216:231]\n\t"
        "v_lshlrev_b32 v232, 16, v152\n\t"
        "v_and_b32 v233, 0xffff0000, v152\n\t"
        "v_cvt_pk_f16_f32 v114, v116, v117\n\t"
        "v_rcp_f16_sdwa v99, v99 dst_sel:WORD_0 dst_unused:UNUSED_PRESERVE src0_sel:WORD_0\n\t"
        "v_pk_mul_f16 v114, v114, v98\n\t"
        "v_exp_f32 v105, v105\n\t"
        "s_waitcnt lgkmcnt(12)\n\t"
        "v_mfma_f32_32x32x16_bf16 v[64:79], v[188:191], v[148:151], v[64:79]\n\t"
        "v_lshlrev_b32 v234, 16, v153\n\t"
        "v_and_b32 v235, 0xffff0000, v153\n\t"
        "v_cvt_pk_f16_f32 v100, v104, v105\n\t"
        "v_rcp_f16_sdwa v99, v99 dst_sel:WORD_1 dst_unused:UNUSED_PRESERVE src0_sel:WORD_1\n\t"
        "v_pk_add_f16 v100, v100, s96\n\t"
        "v_exp_f32 v106, v106\n\t"
        "s_waitcnt lgkmcnt(10)\n\t"
        "v_mfma_f32_32x32x16_bf16 v[64:79], v[192:195], v[152:155], v[64:79]\n\t"
        "v_lshlrev_b32 v236, 16, v154\n\t"
        "v_and_b32 v237, 0xffff0000, v154\n\t"
        "v_cvt_pk_f16_f32 v115, v118, v119\n\t"
        "v_rcp_f16_sdwa v100, v100 dst_sel:WORD_0 dst_unused:UNUSED_PRESERVE src0_sel:WORD_0\n\t"
        "v_pk_mul_f16 v115, v115, v99\n\t"
        "v_exp_f32 v107, v107\n\t"
        "s_waitcnt lgkmcnt(8)\n\t"
        "v_mfma_f32_32x32x16_bf16 v[64:79], v[196:199], v[156:159], v[64:79]\n\t"
        "v_lshlrev_b32 v238, 16, v155\n\t"
        "v_and_b32 v239, 0xffff0000, v155\n\t"
        "v_cvt_pk_f16_f32 v101, v106, v107\n\t"
        "v_rcp_f16_sdwa v100, v100 dst_sel:WORD_1 dst_unused:UNUSED_PRESERVE src0_sel:WORD_1\n\t"
        "v_pk_add_f16 v101, v101, s96\n\t"
        "v_exp_f32 v108, v108\n\t"
        "v_lshlrev_b32 v240, 16, v156\n\t"
        "v_and_b32 v241, 0xffff0000, v156\n\t"
        "v_lshlrev_b32 v242, 16, v157\n\t"
        "v_and_b32 v243, 0xffff0000, v157\n\t"
        "v_lshlrev_b32 v244, 16, v158\n\t"
        "v_and_b32 v245, 0xffff0000, v158\n\t"
        "v_lshlrev_b32 v246, 16, v159\n\t"
        "v_and_b32 v247, 0xffff0000, v159\n\t"
        "v_cvt_pk_f16_f32 v116, v120, v121\n\t"
        "v_rcp_f16_sdwa v101, v101 dst_sel:WORD_0 dst_unused:UNUSED_PRESERVE src0_sel:WORD_0\n\t"
        "v_pk_mul_f16 v116, v116, v100\n\t"
        "v_exp_f32 v109, v109\n\t"
        "s_waitcnt lgkmcnt(6)\n\t"
        "v_mfma_f32_32x32x16_bf16 v[80:95], v[200:203], v[144:147], v[232:247]\n\t"
        "v_cvt_pk_f16_f32 v102, v108, v109\n\t"
        "v_rcp_f16_sdwa v101, v101 dst_sel:WORD_1 dst_unused:UNUSED_PRESERVE src0_sel:WORD_1\n\t"
        "v_pk_add_f16 v102, v102, s96\n\t"
        "v_exp_f32 v110, v110\n\t"
        "s_waitcnt lgkmcnt(4)\n\t"
        "v_mfma_f32_32x32x16_bf16 v[80:95], v[204:207], v[148:151], v[80:95]\n\t"
        "v_cvt_pk_f16_f32 v117, v122, v123\n\t"
        "v_rcp_f16_sdwa v102, v102 dst_sel:WORD_0 dst_unused:UNUSED_PRESERVE src0_sel:WORD_0\n\t"
        "v_pk_mul_f16 v117, v117, v101\n\t"
        "v_exp_f32 v111, v111\n\t"
        "s_waitcnt lgkmcnt(2)\n\t"
        "v_mfma_f32_32x32x16_bf16 v[80:95], v[208:211], v[152:155], v[80:95]\n\t"
        "v_cvt_pk_f16_f32 v103, v110, v111\n\t"
        "v_rcp_f16_sdwa v102, v102 dst_sel:WORD_1 dst_unused:UNUSED_PRESERVE src0_sel:WORD_1\n\t"
        "v_pk_add_f16 v103, v103, s96\n\t"
        "s_waitcnt lgkmcnt(0)\n\t"
        "v_mfma_f32_32x32x16_bf16 v[80:95], v[212:215], v[156:159], v[80:95]\n\t"
        "v_cvt_pk_f16_f32 v118, v124, v125\n\t"
        "v_rcp_f16_sdwa v103, v103 dst_sel:WORD_0 dst_unused:UNUSED_PRESERVE src0_sel:WORD_0\n\t"
        "v_pk_mul_f16 v118, v118, v102\n\t"
        "v_mfma_f32_32x32x16_f16 v[16:31], v[96:99], v[176:179], v[16:31]\n\t"
        "v_rcp_f16_sdwa v103, v103 dst_sel:WORD_1 dst_unused:UNUSED_PRESERVE src0_sel:WORD_1\n\t"
        "v_cvt_pk_f16_f32 v119, v126, v127\n\t"
        "v_mfma_f32_32x32x16_f16 v[48:63], v[112:115], v[176:179], v[48:63]\n\t"
        "v_pk_mul_f16 v119, v119, v103\n\t"
        "v_mfma_f32_32x32x16_f16 v[16:31], v[100:103], v[180:183], v[16:31]\n\t"
        "s_nop 0\n\t"
        "v_mfma_f32_32x32x16_f16 v[48:63], v[116:119], v[180:183], v[48:63]\n\t"
        : "+{v[0:15]}"(Y[0]), "+{v[16:31]}"(Y[1]), "+{v[32:47]}"(Y[2]), "+{v[48:63]}"(Y[3]), "={v[64:79]}"(U[0]),
          "={v[80:95]}"(U[1]), "={v[216:231]}"(X[0]), "={v[232:247]}"(X[1]), "+{v[160:175]}"(AD), [nt] "+s"(nt)
        : "{v[128:143]}"(XAf), [x] "s"(x), [es] "s"(ES), [ring] "s"(ring0), [wave] "s"(wave), [flag] "s"(mode),
          [cu] "n"(cu)
        :
          "memory", "scc", "m0", "s94", "s95", "s96", "v96", "v97", "v98", "v99", "v100", "v101", "v102", "v103",
          "v104", "v105", "v106", "v107", "v108", "v109", "v110", "v111", "v112", "v113", "v114", "v115", "v116",
          "v117", "v118", "v119", "v120", "v121", "v122", "v123", "v124", "v125", "v126", "v127", "v144", "v145",
          "v146", "v147", "v148", "v149", "v150", "v151", "v152", "v153", "v154", "v155", "v156", "v157", "v158",
          "v159", "v176", "v177", "v178", "v179", "v180", "v181", "v182", "v183", "v184", "v185", "v186", "v187",
          "v188", "v189", "v190", "v191", "v192", "v193", "v194", "v195", "v196", "v197", "v198", "v199", "v200",
          "v201", "v202", "v203", "v204", "v205", "v206", "v207", "v208", "v209", "v210", "v211", "v212", "v213",
          "v214", "v215", "v248", "v249", "v250", "v251", "v252", "v253", "v254", "v255");
  }
  SB();
  auto wide = [&]() __attribute__((always_inline)) {
    int l2;
    asm volatile("v_mbcnt_lo_u32_b32 %0, -1, 0\n\tv_mbcnt_hi_u32_b32 %0, -1, %0\n\ts_waitcnt vmcnt(0)" : "=v"(l2) :: "memory");
    AD[0] = la + (4 * (l2 >> 5) + ((l2 >> 2) & 3)) * 16;
    AD[1] = la + (((l2 & 7) << 2) | ((l2 >> 3) & 3)) * 4 + 2;
    AD[2] = la + (l2 >> 3) * 16;
    AD[3] = la + LARR + ((((l2 >> 1) & 7) << 2) | (l2 >> 4)) * 4;
    auto rd = [&](unsigned a) { return *(const LAS bf16x8*)(size_t)a; };
    auto one = [&](bf16x8 v) {
      const u32x4 t = __builtin_bit_cast(u32x4, v);
      return __builtin_bit_cast(bf16x8, t | (t >> 3));
    };
    bf16x8 xq[4];
    {
      f32x16 T[2] = {zero16, zero16};
#pragma unroll
      for (int k = 0; k < 4; ++k) {
        u32x4 t;
#pragma unroll
        for (int j = 0; j < 4; ++j) t[j] = pack_bf16(X[k >> 1][8 * (k & 1) + 2 * j], X[k >> 1][8 * (k & 1) + 2 * j + 1]);
#pragma unroll
        for (int ct = 0; ct < 2; ++ct)
          T[ct] = MFMA32(__builtin_bit_cast(bf16x8, t), rawfrag(AD[15] + (NBLOB - 18 + k) * 1024 + ct * 32), T[ct]);
      }
#pragma unroll
      for (int q = 0; q < 4; ++q) {
        u32x4 t;
#pragma unroll
        for (int j = 0; j < 4; ++j)
          t[j] = pack_bf16(T[q & 1][8 * (q >> 1) + 2 * j] * NLOG2E, T[q & 1][8 * (q >> 1) + 2 * j + 1] * NLOG2E);
        xq[q] = __builtin_bit_cast(bf16x8, t);
      }
    }
#pragma unroll
    for (int i = 0; i < 16; ++i) { Y[0][i] = 1e-6f * (1 << RA); Y[1][i] = 1e-6f * (1 << RA); Y[2][i] = 0.f; Y[3][i] = 0.f; }
#pragma unroll 1
    do {
      const int n = slow ? gen() : cnt;
      issue_gather(0, 0);
#pragma unroll 1
      for (int t = 0; t * 32 < n; ++t) {
        const unsigned ro = (t & 1) * ringd;
        asm volatile("s_waitcnt lgkmcnt(0)" ::: "memory");
        issue_gather(t + 1, ~t & 1);
        const u32x4 ss = ld128(AD[0] + t * 128);
        const unsigned st = *(const LAS unsigned short*)(size_t)(AD[1] + t * 128) * TPITCH + AD[8];
        s16x4 a[4];
#pragma unroll
        for (int q = 0; q < 4; ++q)
          a[q] = __builtin_amdgcn_ds_read_tr16_b64_v4i16((LAS s16x4*)(size_t)((ss[q] >> 16) * TPITCH + AD[7]));
        const bf16x8 S0 = one(__builtin_shufflevector(a[0], a[1], 0, 1, 2, 3, 4, 5, 6, 7));
        const bf16x8 S1 = one(__builtin_shufflevector(a[2], a[3], 0, 1, 2, 3, 4, 5, 6, 7));
        const bf16x8 T0 = one(rd(st)), T1 = one(rd(st + 16));
        asm volatile("s_waitcnt vmcnt(5)" ::: "memory");
#pragma unroll
        for (int ct = 0; ct < 2; ++ct) {
          const unsigned w = AD[14] + OFF_W + ct * 4096;
          f32x16 G = MFMA32(T0, xq[ct], zero16), V = zero16;
          G = MFMA32(T1, xq[2 + ct], G);
          G = MFMA32(rd(AD[13] + ro), rd(AD[14] + OFF_W + (16 + ct) * 1024), G);
#pragma unroll
          for (int k = 0; k < 4; ++k) {
            const bf16x8 as = rd(AD[9 + k] + ro);
            G = MFMA32(as, rd(w + k * 1024), G);
            V = MFMA32(as, rd(w + (8 + k) * 1024), V);
          }
          u32x4 pd[2], pn[2];
#pragma unroll
          for (int i = 0; i < 16; i += 2) {
            constexpr float u = 1.0f / (1 << RA);
            const float r0 = __builtin_amdgcn_rcpf(__builtin_fmaf(__builtin_amdgcn_exp2f(G[i]), u, u));
            const float r1 = __builtin_amdgcn_rcpf(__builtin_fmaf(__builtin_amdgcn_exp2f(G[i + 1]), u, u));
            pd[i >> 3][(i >> 1) & 3] = pack_bf16(r0, r1);
            pn[i >> 3][(i >> 1) & 3] = pack_bf16(r0 * V[i], r1 * V[i + 1]);
          }
          Y[ct] = MFMA32(__builtin_bit_cast(bf16x8, pd[0]), S0, Y[ct]);
          Y[ct] = MFMA32(__builtin_bit_cast(bf16x8, pd[1]), S1, Y[ct]);
          Y[2 + ct] = MFMA32(__builtin_bit_cast(bf16x8, pn[0]), S0, Y[2 + ct]);
          Y[2 + ct] = MFMA32(__builtin_bit_cast(bf16x8, pn[1]), S1, Y[2 + ct]);
          SB();
        }
      }
      asm volatile("s_waitcnt vmcnt(0) lgkmcnt(0)" ::: "memory");
    } while (slow && gs < GEND);
  };

  const unsigned ot = ring0 + (max(cnt - 1, 0) & 32 ? 0u : ringd);
  auto finish = [&](const bool check) __attribute__((always_inline)) {
    auto inv = [&](f32x16& y) {
      y *= VSC;
#pragma unroll
      for (int i = 0; i < 16; ++i) y[i] = __builtin_amdgcn_rcpf(y[i]);
    };
    inv(Y[0]);
    SB();
    int lane;
    asm volatile("v_mbcnt_lo_u32_b32 %0, -1, 0\n\tv_mbcnt_hi_u32_b32 %0, -1, %0" : "=v"(lane));
    const int h = lane >> 5;
    const unsigned nd = nodeof(cg + (lane & 31));
    const bool live = (nd * Mw + Cw) >> 16 == cg + (lane & 31);
    const unsigned lm = (unsigned)__ballot(live);
    const int nodes = __builtin_popcount(lm);
    const unsigned n0 = __builtin_amdgcn_readlane(nd, __builtin_ctz(lm | 0x80000000u));
    const int r = live ? nd - n0 : 31;
    SB();
    inv(Y[1]);
    unsigned ov[16];
#pragma unroll
    for (int p = 0; p < 4; ++p) {
      const int ct = p >> 1;
#pragma unroll
      for (int j = 0; j < 4; ++j) {
        const int i = 8 * (p & 1) + 2 * j;
        const f32x2 z = __builtin_elementwise_fma(f32x2{Y[2 + ct][i], Y[2 + ct][i + 1]}, f32x2{Y[ct][i], Y[ct][i + 1]},
                                                  f32x2{U[ct][i], U[ct][i + 1]});
        ov[4 * p + j] = pack_bf16(MAXN(z[0], X[ct][i]), MAXN(z[1], X[ct][i + 1]));
      }
      *(LAS u32x4*)(size_t)(ot + r * 128 + (((4 * h + p) ^ (r & 7)) * 16)) =
          u32x4{ov[4 * p], ov[4 * p + 1], ov[4 * p + 2], ov[4 * p + 3]};
    }
    u32x4 ow[4];
#pragma unroll
    for (int q = 0; q < 4; ++q)
      ow[q] = *(const LAS u32x4*)(size_t)(ot + (8 * q + (lane >> 3)) * 128 + (((lane & 7) ^ (lane >> 3)) * 16));
    asm volatile("v_cmpx_lt_i32 vcc, 0, %6\n\t"
                 "global_store_dwordx4 %0, %1, %5 sc1\n\t"
                 "v_cmpx_lt_i32 vcc, 8, %6\n\t"
                 "global_store_dwordx4 %0, %2, %5 offset:1024 sc1\n\t"
                 "v_cmpx_lt_i32 vcc, 16, %6\n\t"
                 "global_store_dwordx4 %0, %3, %5 offset:2048 sc1\n\t"
                 "v_cmpx_lt_i32 vcc, 24, %6\n\t"
                 "global_store_dwordx4 %0, %4, %5 offset:3072 sc1\n\t"
                 "s_mov_b64 exec, -1"
                 :: "v"((unsigned)n0 * 128u + lane * 16u), "v"(ow[0]), "v"(ow[1]), "v"(ow[2]), "v"(ow[3]), "s"(out),
                    "v"(nodes - (lane >> 3))
                 : "memory", "vcc");
    SB();
    if (!check) return false;
    constexpr float WLIM = 1 << (22 - RA + VK);
    float wm = MAXN(Y[0][0], Y[1][0]);
#pragma unroll
    for (int i = 1; i < 16; ++i) wm = MAXN(MAXN(wm, Y[0][i]), Y[1][i]);
    f16x2 om = __builtin_bit_cast(f16x2, ov[0]);
#pragma unroll
    for (int i = 1; i < 16; ++i) om = MAXN(om, __builtin_bit_cast(f16x2, ov[i]));
    const _Float16 o1 = MAXN(om[0], om[1]);
    const unsigned long long lv = lm * 0x100000001ull;
    unsigned long long bad = __ballot(o1 != o1) | (__ballot(o1 < __builtin_bit_cast(_Float16, (unsigned short)((127 - 9 + VK) << 7))) & lv);
    if (__builtin_expect((__ballot(wm > WLIM / LCAP) & lv) != 0ull, 0)) {
      LAS unsigned* dg = (LAS unsigned*)(size_t)ot;
      dg[lane & 31] = 0u;
#pragma unroll 1
      for (int i = lane; i < cnt; i += 64)
        __hip_atomic_fetch_add(dg + (*(const LAS unsigned short*)(size_t)(la + i * 4 + 2) & 31), 1u, __ATOMIC_RELAXED,
                               __HIP_MEMORY_SCOPE_WORKGROUP);
      bad |= __ballot(wm * (float)dg[lane & 31] > WLIM) & lv;
    }
    return slow || bad != 0ull;
  };
  if (__builtin_expect(finish(true), 0)) {
    wide();
    finish(false);
  }
  if (bk == 0 && wave == 7) asm volatile("global_store_dwordx2 %0, %1, %2 sc1" :: "v"(0u), "v"(ep), "s"(FL) : "memory");
  asm volatile("s_waitcnt vmcnt(0)" ::: "memory");
}

extern "C" void kernel_launch(void* const* d_in, const int* in_sizes, int n_in,
                              void* d_out, int out_size, void* d_ws, size_t ws_size,
                              hipStream_t stream) {
  const unsigned short* x = (const unsigned short*)d_in[0];
  const u32x4* eattr = (const u32x4*)d_in[1];
  const int* src = (const int*)d_in[2];
  const int* dst = (const int*)d_in[3];
  const unsigned short* wa = (const unsigned short*)d_in[4];
  const unsigned short* wb = (const unsigned short*)d_in[5];
  const unsigned short* wc = (const unsigned short*)d_in[6];
  const unsigned short* wu = (const unsigned short*)d_in[7];
  const unsigned short* wv = (const unsigned short*)d_in[8];
  unsigned short* out = (unsigned short*)d_out;
  const int N = in_sizes[0] / 64;
  const int E = in_sizes[2];

  const bool skew = N > 4096 && N < 60000;
  const unsigned M = N > 256 ? (unsigned)(4294967295.0 / ((double)N * (skew ? 1.0 + SKEWF : 1.0))) : 0xffffffu;
  const unsigned N1 = (0x7fffffffu / M) + 1u;
  const unsigned M2 = skew ? (0xffffffffu - N1 * M) / ((unsigned)N - N1) : M;

  auto fb = [](double v) { return __builtin_bit_cast(unsigned, (float)v); };
  char* ws = (char*)d_ws;
  k_main<<<NBK, 512, 0, stream>>>(dst, src, (unsigned long long*)(ws + (1 << 20)), x, E, M, N1, M2 - M, N,
                                  fb(65536.0 / M), fb(65536.0 / M2),
                                  fb((double)N1 + (32768.0 - (double)N1 * M) / M2 + 0.5), wa, wb, wv, wc, wu, eattr, out
     );
}
```
